```python
import torch

def _softmax(x, axis=-1):
    return torch.softmax(x, dim=axis)
def _gelu(x):
    return torch.nn.functional.gelu(x, approximate="tanh")
def _silu(x):
    return torch.nn.functional.silu(x)
def _sigmoid(x):
    return torch.sigmoid(x)
def _layernorm(x, axis=-1, eps=1e-5):
    m = x.mean(dim=axis, keepdim=True)
    v = x.var(dim=axis, keepdim=True, unbiased=False)
    return (x - m) / torch.sqrt(v + eps)
def _scatter_add(tgt, idx, src, axis=0):
    return tgt.clone().index_add_(axis, idx, src)
def _segment_sum(src, seg, n):
    out = torch.zeros((n,) + src.shape[1:], dtype=src.dtype)
    return out.index_add_(0, seg, src)
def _pad(x, pw):
    flat = tuple(v for pair in reversed(pw) for v in pair)
    return torch.nn.functional.pad(x, flat)

kernel_name = 'standalone__window_attn_swin__vision'

def setup_inputs():

    _w = (lambda a: (lambda t: t.to(torch.bfloat16) if t.is_floating_point() else t)(torch.as_tensor(a)))
    H = W = 512
    C = 192
    ws = 8
    nh = 4
    shift = ws // 2
    coords = torch.stack(torch.meshgrid(torch.arange(ws), torch.arange(ws), indexing='ij'), 0).reshape(2, -1)
    rel = coords[:, :, None] - coords[:, None, :]
    rel[0] += ws - 1
    rel[1] += ws - 1
    rel[0] *= 2 * ws - 1
    rp_idx = rel.sum(0).to(torch.int64)
    img_mask = torch.zeros((1, H, W, 1), dtype=float)
    cnt = 0
    for hs in (slice(0, H - ws), slice(H - ws, H - shift), slice(H - shift, H)):
        for vs in (slice(0, W - ws), slice(W - ws, W - shift), slice(W - shift, W)):
            img_mask[:, hs, vs, :] = cnt
            cnt += 1
    mw = img_mask.reshape(1, H // ws, ws, W // ws, ws, 1).permute(0, 1, 3, 2, 4, 5).reshape(-1, ws * ws)
    am = mw[:, :, None] - mw[:, None, :]
    attn_mask = torch.where(am != 0, -100.0, 0.0).to(float)
    return {
        "x": _w((lambda *s: torch.randn(*s, dtype=torch.bfloat16) * 1.6)(1, H, W, C)),
        "wqkv": _w((lambda *s: torch.randn(*s, dtype=torch.bfloat16) * 1.6)(C, 3 * C) * 0.02),
        "wo": _w((lambda *s: torch.randn(*s, dtype=torch.bfloat16) * 0.05)(C, C) * 0.02),
        "rpb": _w((lambda *s: torch.randn(*s, dtype=torch.bfloat16) * 0.2)((2 * ws - 1) * (2 * ws - 1), nh)),
        "rp_idx": _w(rp_idx),
        "attn_mask": _w(attn_mask),
    }

def reference(x, wqkv, wo, rpb, rp_idx, attn_mask):
    B, H, W, C = x.shape
    N = rp_idx.shape[0]
    ws = int(round(N ** 0.5))
    nh = rpb.shape[-1]
    hd = C // nh
    shift = ws // 2
    xs = torch.roll(x, (-shift, -shift), (1, 2))
    nWh = H // ws
    nWw = W // ws
    xw = (lambda x, *d: x.permute(*d))(xs.reshape(B, nWh, ws, nWw, ws, C), 0, 1, 3, 2, 4, 5).reshape(B * nWh * nWw, N, C)
    qkv = (lambda x, *d: x.permute(*d))(torch.einsum('wnc,cd->wnd', xw, wqkv).reshape(-1, N, 3, nh, hd), 2, 0, 3, 1, 4)
    q, k, v = qkv[0], qkv[1], qkv[2]
    attn = torch.einsum('whid,whjd->whij', q, k) * (hd ** -0.5)
    bias = (lambda x, *d: x.permute(*d))(rpb[rp_idx.reshape(-1)].reshape(N, N, nh), 2, 0, 1)
    attn = attn + bias[None, :, :, :]
    attn = (attn.reshape(B, nWh * nWw, nh, N, N) + attn_mask[None, :, None, :, :]).reshape(-1, nh, N, N)
    attn = _softmax(attn, axis=-1)
    out = (lambda x, *d: x.permute(*d))(torch.einsum('whij,whjd->whid', attn, v), 0, 2, 1, 3).reshape(-1, N, C)
    out = torch.einsum('wnc,cd->wnd', out, wo)
    out = (lambda x, *d: x.permute(*d))(out.reshape(B, nWh, nWw, ws, ws, C), 0, 1, 3, 2, 4, 5).reshape(B, H, W, C)
    return torch.roll(out, (shift, shift), (1, 2))
```

```cpp
#include <hip/hip_runtime.h>
#include <hip/hip_bf16.h>

typedef unsigned int u32x4 __attribute__((ext_vector_type(4)));

__device__ __forceinline__ void guarded_pass(const char* x, const char* wqkv, const char* wo, const char* rpb, const char* rpi,
                                             const char* mask, char* out, unsigned ldsb, const unsigned lane, const unsigned w) {
    const unsigned wx = blockIdx.x & 63u, wy0 = blockIdx.x >> 6;
    unsigned dm[3], wsrc[3];
#pragma unroll
    for (int p = 0; p < 3; ++p) {
        unsigned gch = 64u * p + lane, t = gch / 24u, c1 = gch % 24u;
        dm[p] = ((8u * wx + 4u + t) & 511u) * 384u + ((c1 ^ (((t >> 1) & 1u) | (w << 1))) << 4);
        wsrc[p] = t * 1152u + ((c1 ^ (2u * (((t >> 1) & 1u) | ((w & 1u) << 1)))) << 4) - 1024u * p;
    }
    u32x4 srdx = {(unsigned)(size_t)x, (unsigned)((size_t)x >> 32) & 0xffffu, 100663296u, 0x20000u};
    u32x4 srdw = {(unsigned)(size_t)wqkv, (unsigned)((size_t)wqkv >> 32) & 0xffffu, 221184u, 0x20000u};
    unsigned ya = (8u * wy0 + 36u + w) * 196608u;
    unsigned wv = w;
    unsigned w3 = w * 3072u, w9 = w * 9216u;
    __builtin_amdgcn_sched_barrier(0);
    asm volatile(
        "s_nop 4\n"
        "s_add_u32 m0, s40, 0\n"
        "s_add_u32 s64, s65, 0\n"
        "buffer_load_dwordx4 v128, s[20:23], s64 offen lds\n"
        "buffer_load_dwordx4 v129, s[20:23], s64 offen offset:1024 lds\n"
        "buffer_load_dwordx4 v130, s[20:23], s64 offen offset:2048 lds\n"
        "s_add_u32 m0, s40, 12288\n"
        "s_add_u32 s64, s65, 36864\n"
        "buffer_load_dwordx4 v128, s[20:23], s64 offen lds\n"
        "buffer_load_dwordx4 v129, s[20:23], s64 offen offset:1024 lds\n"
        "buffer_load_dwordx4 v130, s[20:23], s64 offen offset:2048 lds\n"
        "s_add_u32 m0, s40, 24576\n"
        "s_add_u32 s64, s65, 73728\n"
        "buffer_load_dwordx4 v128, s[20:23], s64 offen lds\n"
        "buffer_load_dwordx4 v129, s[20:23], s64 offen offset:1024 lds\n"
        "buffer_load_dwordx4 v130, s[20:23], s64 offen offset:2048 lds\n"
        :: "{v242}"(dm[0]), "{v243}"(dm[1]), "{v244}"(dm[2]), "{v128}"(wsrc[0]), "{v129}"(wsrc[1]), "{v130}"(wsrc[2]), "{s[8:11]}"(srdx), "{s[20:23]}"(srdw), "{s47}"(ya), "{s40}"(w3), "{s65}"(w9), "s"(ldsb)
        : "s64", "scc", "memory");
    __builtin_amdgcn_sched_barrier(0);

    const unsigned rho = lane & 15u, g = lane >> 4;
    const unsigned cg = w >> 1, T = w & 1u;
    const unsigned ql = 8u * (rho >> 2) + (rho & 3u);
    unsigned xr[2], mr[2];
#pragma unroll
    for (int j = 0; j < 2; ++j) {
        xr[j] = 73728u + ql * 384u + (((4u * j + g) ^ (rho >> 1)) << 4);
        mr[j] = 147456u + 8192u + ql * 128u + (((4u * j + g) ^ (rho >> 1)) << 4);
    }
    unsigned ow = 122880u + (4u * w + g) * 1024u + rho * 16u;
    unsigned ow2 = 122880u + (16u + 2u * w + (g >> 1)) * 1024u + rho * 16u + (g & 1u) * 8u;
    unsigned ord = 122880u + g * 1024u + (32u * T + rho) * 16u;
    __builtin_amdgcn_sched_barrier(0);
    asm volatile(
        "s_nop 4\n"
        "s_add_u32 m0, s40, 36864\n"
        "s_add_u32 s64, s65, 110592\n"
        "buffer_load_dwordx4 v128, s[20:23], s64 offen lds\n"
        "buffer_load_dwordx4 v129, s[20:23], s64 offen offset:1024 lds\n"
        "buffer_load_dwordx4 v130, s[20:23], s64 offen offset:2048 lds\n"
        :: "{v242}"(dm[0]), "{v243}"(dm[1]), "{v244}"(dm[2]), "{v128}"(wsrc[0]), "{v129}"(wsrc[1]), "{v130}"(wsrc[2]), "{s[8:11]}"(srdx), "{s[20:23]}"(srdw), "{s47}"(ya), "{s40}"(w3), "{s65}"(w9), "s"(ldsb)
        : "s64", "scc", "memory");
    __builtin_amdgcn_sched_barrier(0);
    unsigned wor = 0u + cg * 18432u + g * 1536u + rho * 96u + (g & 1u) * 16u;
    unsigned wor2 = 0u + cg * 18432u + g * 1536u + rho * 96u - (g & 1u) * 16u;
    unsigned ml = 147456u + w * 2048u + lane * 16u;
    unsigned ms = (lane >> 3) * 128u + (((lane & 7u) ^ (((lane >> 4) & 1u) | ((w & 1u) << 2))) << 4);
    unsigned st = g * 196608u + cg * 192u + rho * 12u;
    unsigned l16 = lane * 16u;
    unsigned id0 = (rho == 4u * g ? 0x3f80u : 0u) | (rho == 4u * g + 1u ? 0x3f800000u : 0u);
    unsigned id1 = (rho == 4u * g + 2u ? 0x3f80u : 0u) | (rho == 4u * g + 3u ? 0x3f800000u : 0u);
    unsigned x0 = (8u * wx + 4u) * 384u, x1 = ((8u * wx + 8u) & 511u) * 384u;
    u32x4 srdm = {(unsigned)(size_t)mask, (unsigned)((size_t)mask >> 32) & 0xffffu, 33554432u, 0x20000u};
    u32x4 srdo = {(unsigned)(size_t)out, (unsigned)((size_t)out >> 32) & 0xffffu, 100663296u, 0x20000u};
    u32x4 srdwo = {(unsigned)(size_t)wo, (unsigned)((size_t)wo >> 32) & 0xffffu, 73728u, 0x20000u};
    u32x4 srdrp = {(unsigned)(size_t)rpi, (unsigned)((size_t)rpi >> 32) & 0xffffu, 16384u, 0x20000u};
    u32x4 srdrb = {(unsigned)(size_t)rpb, (unsigned)((size_t)rpb >> 32) & 0xffffu, 1800u, 0x20000u};
    __builtin_amdgcn_sched_barrier(0);
    asm volatile(
        "s_nop 4\n"
        "s_add_u32 m0, s40, 49152\n"
        "s_add_u32 s64, s65, 147456\n"
        "buffer_load_dwordx4 v128, s[20:23], s64 offen lds\n"
        "buffer_load_dwordx4 v129, s[20:23], s64 offen offset:1024 lds\n"
        "buffer_load_dwordx4 v130, s[20:23], s64 offen offset:2048 lds\n"
        :: "{v242}"(dm[0]), "{v243}"(dm[1]), "{v244}"(dm[2]), "{v128}"(wsrc[0]), "{v129}"(wsrc[1]), "{v130}"(wsrc[2]), "{s[8:11]}"(srdx), "{s[20:23]}"(srdw), "{s47}"(ya), "{s40}"(w3), "{s65}"(w9), "s"(ldsb)
        : "s64", "scc", "memory");
    __builtin_amdgcn_sched_barrier(0);
    const unsigned q4 = (lane >> 2) & 3u, p4 = lane & 3u;
    unsigned wrd[3];
#pragma unroll
    for (int p = 0; p < 3; ++p) {
        unsigned c = 6u * w + 2u * p + (p4 >> 1);
        wrd[p] = (8u * g + q4) * 384u + ((c ^ (2u * ((q4 >> 1) | ((g & 1u) << 1)))) << 4) + (p4 & 1u) * 8u;
    }
    unsigned od = cg * 18432u + g * 1536u + (((48u * T + rho) ^ (g & 1u)) << 4);
    unsigned rbs = lane * 8u + 2u * w, rbw = 151552u + w * 1024u + lane * 4u;
    const unsigned pfi = ((blockIdx.x >> 3) & 31u) * 4u + w;
    unsigned pf0 = pfi * 2048u + lane * 16u, pf1 = pfi * 1024u + lane * 16u;
    __builtin_amdgcn_sched_barrier(0);
    asm volatile(
        "s_nop 4\n"
        "s_add_u32 m0, s40, 61440\n"
        "s_add_u32 s64, s65, 184320\n"
        "buffer_load_dwordx4 v128, s[20:23], s64 offen lds\n"
        "buffer_load_dwordx4 v129, s[20:23], s64 offen offset:1024 lds\n"
        "buffer_load_dwordx4 v130, s[20:23], s64 offen offset:2048 lds\n"
        :: "{v242}"(dm[0]), "{v243}"(dm[1]), "{v244}"(dm[2]), "{v128}"(wsrc[0]), "{v129}"(wsrc[1]), "{v130}"(wsrc[2]), "{s[8:11]}"(srdx), "{s[20:23]}"(srdw), "{s47}"(ya), "{s40}"(w3), "{s65}"(w9), "s"(ldsb)
        : "s64", "scc", "memory");
    __builtin_amdgcn_sched_barrier(0);
    unsigned rs = g * 256u + ((rho ^ (g | ((w & 1u) << 3))) << 4);
    unsigned rr = 135168u + ql * 256u + (((2u * g) ^ rho) << 4);
    unsigned oa = (4u * g + q4) * 384u + 96u * w + p4 * 8u;
    unsigned ob = (48u * (g >> 1) + 8u * (g & 1u) + q4) * 384u + 96u * w + p4 * 8u;
    unsigned mk = (blockIdx.x + 256u) * 8192u + w * 2048u;
    unsigned raw = (8u * wy0 + 4u + 4u * T) * 196608u;
    asm volatile(
        "s_nop 4\n"
        "s_mul_i32 s40, s42, 3072\n"
        "s_mul_i32 s65, s42, 9216\n"
        "s_lshl_b32 s48, s42, 11\n"
        "s_add_u32 s48, s48, 147456\n"
        "s_mov_b32 s24, 0x3fb8aa3b\n"
        "s_mov_b32 s25, 0xbfb8aa3b\n"
        "s_mov_b32 s26, 0xff7fff7f\n"
        "s_mov_b32 s49, 0x3e13cd3a\n"
        "s_mov_b32 s50, 0x3e13cd3a\n"
        "s_mov_b32 s51, 0x3e13cd3a\n"
        "s_mov_b32 s27, 14\n"
        "s_mov_b32 s28, 73728\n"
        "s_mov_b32 s29, 122880\n"
        "s_mov_b32 s30, 98304\n"
        "s_mov_b32 s38, 0x80000000\n"
        "s_mov_b32 s39, 0x80000000\n"
        "s_lshl_b32 s67, s42, 10\n"
        "s_add_u32 s67, s67, 151552\n"
        "v_mov_b32 v188, v170\n"
        "v_mov_b32 v189, v171\n"
        "v_mov_b32 v210, v170\n"
        "v_mov_b32 v211, v171\n"
        "v_mov_b32 v228, v170\n"
        "v_mov_b32 v229, v171\n"
        "v_accvgpr_write_b32 a252, v140\n"
        "v_add_u32 v136, 98304, v131\n"
        "v_accvgpr_write_b32 a253, v141\n"
        "v_add_u32 v137, 98304, v132\n"
        "v_accvgpr_write_b32 a254, v147\n"
        "v_add_u32 v138, 98304, v133\n"
        "v_mov_b32 v150, v240\n"
        "v_mov_b32 v151, v241\n"
        "v_xor_b32 v253, 64, v139\n"
        "v_xor_b32 v232, 0, v144\n"
        "v_xor_b32 v233, 16, v144\n"
        "v_xor_b32 v234, 128, v144\n"
        "v_xor_b32 v235, 144, v144\n"
        "s_lshl_b32 s46, s42, 12\n"
        "buffer_load_dwordx4 v[64:67], v148, s[20:23], 0 offen\n"
        "buffer_load_dwordx4 v[68:71], v148, s[20:23], 0 offen offset:1024\n"
        "buffer_load_dwordx4 v[72:75], v149, s[52:55], 0 offen\n"
        "s_waitcnt vmcnt(12)\n"
        "s_barrier\n"
        "s_add_u32 m0, s40, 73728\n"
        "ds_read_b64_tr_b16 v[76:77], v131 offset:0\n"
        "ds_read_b64_tr_b16 v[78:79], v131 offset:1536\n"
        "ds_read_b64_tr_b16 v[80:81], v131 offset:12288\n"
        "ds_read_b64_tr_b16 v[82:83], v131 offset:13824\n"
        "ds_read_b64_tr_b16 v[84:85], v131 offset:24576\n"
        "ds_read_b64_tr_b16 v[86:87], v131 offset:26112\n"
        "ds_read_b64_tr_b16 v[88:89], v132 offset:0\n"
        "ds_read_b64_tr_b16 v[90:91], v132 offset:1536\n"
        "ds_read_b64_tr_b16 v[92:93], v132 offset:12288\n"
        "ds_read_b64_tr_b16 v[94:95], v132 offset:13824\n"
        "ds_read_b64_tr_b16 v[104:105], v132 offset:24576\n"
        "ds_read_b64_tr_b16 v[106:107], v132 offset:26112\n"
        "ds_read_b64_tr_b16 v[108:109], v133 offset:0\n"
        "ds_read_b64_tr_b16 v[110:111], v133 offset:1536\n"
        "ds_read_b64_tr_b16 v[116:117], v133 offset:12288\n"
        "ds_read_b64_tr_b16 v[118:119], v133 offset:13824\n"
        "ds_read_b64_tr_b16 v[120:121], v133 offset:24576\n"
        "ds_read_b64_tr_b16 v[122:123], v133 offset:26112\n"
        "s_sub_u32 s64, s47, 6291456\n"
        "buffer_load_dwordx4 v242, s[8:11], s64 offen lds\n"
        "s_add_u32 m0, s40, 74752\n"
        "s_sub_u32 s64, s47, 6291456\n"
        "buffer_load_dwordx4 v243, s[8:11], s64 offen lds\n"
        "s_add_u32 m0, s40, 75776\n"
        "s_sub_u32 s64, s47, 6291456\n"
        "buffer_load_dwordx4 v244, s[8:11], s64 offen lds\n"
        "s_add_u32 m0, s40, 86016\n"
        "s_sub_u32 s64, s47, 5505024\n"
        "buffer_load_dwordx4 v242, s[8:11], s64 offen lds\n"
        "s_add_u32 m0, s40, 87040\n"
        "s_sub_u32 s64, s47, 5505024\n"
        "buffer_load_dwordx4 v243, s[8:11], s64 offen lds\n"
        "s_add_u32 m0, s40, 88064\n"
        "s_sub_u32 s64, s47, 5505024\n"
        "buffer_load_dwordx4 v244, s[8:11], s64 offen lds\n"
        "s_waitcnt lgkmcnt(15)\n"
        "v_lshlrev_b32 v0, 16, v76\n"
        "v_lshlrev_b32 v2, 16, v77\n"
        "v_lshlrev_b32 v4, 16, v78\n"
        "v_lshlrev_b32 v6, 16, v79\n"
        "v_and_b32 v1, 0xffff0000, v76\n"
        "v_and_b32 v3, 0xffff0000, v77\n"
        "v_and_b32 v5, 0xffff0000, v78\n"
        "v_and_b32 v7, 0xffff0000, v79\n"
        "v_pk_mul_f32 v[0:1], v[0:1], s[50:51]\n"
        "v_pk_mul_f32 v[2:3], v[2:3], s[50:51]\n"
        "v_pk_mul_f32 v[4:5], v[4:5], s[50:51]\n"
        "v_pk_mul_f32 v[6:7], v[6:7], s[50:51]\n"
        "v_cvt_pk_bf16_f32 v0, v0, v1\n"
        "v_cvt_pk_bf16_f32 v2, v2, v3\n"
        "v_cvt_pk_bf16_f32 v4, v4, v5\n"
        "v_cvt_pk_bf16_f32 v6, v6, v7\n"
        "v_accvgpr_write_b32 a0, v0\n"
        "v_accvgpr_write_b32 a1, v2\n"
        "v_accvgpr_write_b32 a2, v4\n"
        "v_accvgpr_write_b32 a3, v6\n"
        "v_lshlrev_b32 v8, 16, v80\n"
        "v_lshlrev_b32 v10, 16, v81\n"
        "s_waitcnt lgkmcnt(14)\n"
        "v_lshlrev_b32 v12, 16, v82\n"
        "v_lshlrev_b32 v14, 16, v83\n"
        "v_and_b32 v9, 0xffff0000, v80\n"
        "v_and_b32 v11, 0xffff0000, v81\n"
        "v_and_b32 v13, 0xffff0000, v82\n"
        "v_and_b32 v15, 0xffff0000, v83\n"
        "v_pk_mul_f32 v[8:9], v[8:9], s[50:51]\n"
        "v_pk_mul_f32 v[10:11], v[10:11], s[50:51]\n"
        "v_pk_mul_f32 v[12:13], v[12:13], s[50:51]\n"
        "v_pk_mul_f32 v[14:15], v[14:15], s[50:51]\n"
        "v_cvt_pk_bf16_f32 v8, v8, v9\n"
        "v_cvt_pk_bf16_f32 v10, v10, v11\n"
        "v_cvt_pk_bf16_f32 v12, v12, v13\n"
        "v_cvt_pk_bf16_f32 v14, v14, v15\n"
        "v_accvgpr_write_b32 a4, v8\n"
        "v_accvgpr_write_b32 a5, v10\n"
        "v_accvgpr_write_b32 a6, v12\n"
        "v_accvgpr_write_b32 a7, v14\n"
        "s_waitcnt lgkmcnt(13)\n"
        "v_lshlrev_b32 v0, 16, v84\n"
        "v_lshlrev_b32 v2, 16, v85\n"
        "s_waitcnt lgkmcnt(12)\n"
        "v_lshlrev_b32 v4, 16, v86\n"
        "v_lshlrev_b32 v6, 16, v87\n"
        "v_and_b32 v1, 0xffff0000, v84\n"
        "v_and_b32 v3, 0xffff0000, v85\n"
        "v_and_b32 v5, 0xffff0000, v86\n"
        "v_and_b32 v7, 0xffff0000, v87\n"
        "v_pk_mul_f32 v[0:1], v[0:1], s[50:51]\n"
        "v_pk_mul_f32 v[2:3], v[2:3], s[50:51]\n"
        "v_pk_mul_f32 v[4:5], v[4:5], s[50:51]\n"
        "v_pk_mul_f32 v[6:7], v[6:7], s[50:51]\n"
        "v_cvt_pk_bf16_f32 v0, v0, v1\n"
        "v_cvt_pk_bf16_f32 v2, v2, v3\n"
        "v_cvt_pk_bf16_f32 v4, v4, v5\n"
        "v_cvt_pk_bf16_f32 v6, v6, v7\n"
        "v_accvgpr_write_b32 a8, v0\n"
        "v_accvgpr_write_b32 a9, v2\n"
        "v_accvgpr_write_b32 a10, v4\n"
        "v_accvgpr_write_b32 a11, v6\n"
        "s_waitcnt lgkmcnt(11)\n"
        "v_lshlrev_b32 v8, 16, v88\n"
        "v_lshlrev_b32 v10, 16, v89\n"
        "s_waitcnt lgkmcnt(10)\n"
        "v_lshlrev_b32 v12, 16, v90\n"
        "v_lshlrev_b32 v14, 16, v91\n"
        "buffer_load_ushort v236, v145, s[60:63], 0 offen offset:0\n"
        "buffer_load_ushort v237, v145, s[60:63], 0 offen offset:512\n"
        "buffer_load_ushort v238, v145, s[60:63], 0 offen offset:1024\n"
        "buffer_load_ushort v239, v145, s[60:63], 0 offen offset:1536\n"
        "v_and_b32 v9, 0xffff0000, v88\n"
        "v_and_b32 v11, 0xffff0000, v89\n"
        "v_and_b32 v13, 0xffff0000, v90\n"
        "v_and_b32 v15, 0xffff0000, v91\n"
        "v_pk_mul_f32 v[8:9], v[8:9], s[50:51]\n"
        "v_pk_mul_f32 v[10:11], v[10:11], s[50:51]\n"
        "v_pk_mul_f32 v[12:13], v[12:13], s[50:51]\n"
        "v_pk_mul_f32 v[14:15], v[14:15], s[50:51]\n"
        "s_add_u32 m0, s46, 135168\n"
        "v_cvt_pk_bf16_f32 v8, v8, v9\n"
        "v_cvt_pk_bf16_f32 v10, v10, v11\n"
        "v_cvt_pk_bf16_f32 v12, v12, v13\n"
        "v_cvt_pk_bf16_f32 v14, v14, v15\n"
        "s_mov_b32 s64, s46\n"
        "v_accvgpr_write_b32 a24, v8\n"
        "v_accvgpr_write_b32 a25, v10\n"
        "v_accvgpr_write_b32 a26, v12\n"
        "v_accvgpr_write_b32 a27, v14\n"
        "buffer_load_dwordx4 v139, s[56:59], s64 offen lds\n"
        "s_waitcnt lgkmcnt(9)\n"
        "v_lshlrev_b32 v0, 16, v92\n"
        "v_lshlrev_b32 v2, 16, v93\n"
        "s_waitcnt lgkmcnt(8)\n"
        "v_lshlrev_b32 v4, 16, v94\n"
        "v_lshlrev_b32 v6, 16, v95\n"
        "s_waitcnt lgkmcnt(0)\n"
        "s_waitcnt vmcnt(14)\n"
        "s_barrier\n"
        "v_and_b32 v1, 0xffff0000, v92\n"
        "v_and_b32 v3, 0xffff0000, v93\n"
        "v_and_b32 v5, 0xffff0000, v94\n"
        "v_and_b32 v7, 0xffff0000, v95\n"
        "ds_read_b64_tr_b16 v[152:153], v131 offset:36864\n"
        "ds_read_b64_tr_b16 v[154:155], v131 offset:38400\n"
        "ds_read_b64_tr_b16 v[172:173], v131 offset:49152\n"
        "ds_read_b64_tr_b16 v[174:175], v131 offset:50688\n"
        "ds_read_b64_tr_b16 v[192:193], v131 offset:61440\n"
        "ds_read_b64_tr_b16 v[194:195], v131 offset:62976\n"
        "ds_read_b64_tr_b16 v[212:213], v132 offset:36864\n"
        "ds_read_b64_tr_b16 v[214:215], v132 offset:38400\n"
        "ds_read_b64_tr_b16 v[156:157], v132 offset:49152\n"
        "ds_read_b64_tr_b16 v[158:159], v132 offset:50688\n"
        "ds_read_b64_tr_b16 v[176:177], v132 offset:61440\n"
        "ds_read_b64_tr_b16 v[178:179], v132 offset:62976\n"
        "ds_read_b64_tr_b16 v[196:197], v133 offset:36864\n"
        "ds_read_b64_tr_b16 v[198:199], v133 offset:38400\n"
        "ds_read_b64_tr_b16 v[216:217], v133 offset:49152\n"
        "ds_read_b64_tr_b16 v[218:219], v133 offset:50688\n"
        "ds_read_b64_tr_b16 v[124:125], v133 offset:61440\n"
        "ds_read_b64_tr_b16 v[126:127], v133 offset:62976\n"
        "v_pk_mul_f32 v[0:1], v[0:1], s[50:51]\n"
        "v_pk_mul_f32 v[2:3], v[2:3], s[50:51]\n"
        "v_pk_mul_f32 v[4:5], v[4:5], s[50:51]\n"
        "v_pk_mul_f32 v[6:7], v[6:7], s[50:51]\n"
        "v_cvt_pk_bf16_f32 v0, v0, v1\n"
        "v_cvt_pk_bf16_f32 v2, v2, v3\n"
        "v_cvt_pk_bf16_f32 v4, v4, v5\n"
        "v_cvt_pk_bf16_f32 v6, v6, v7\n"
        "buffer_load_dwordx4 v139, s[56:59], s64 offen offset:1024 lds\n"
        "v_accvgpr_write_b32 a28, v0\n"
        "v_accvgpr_write_b32 a29, v2\n"
        "v_accvgpr_write_b32 a30, v4\n"
        "v_accvgpr_write_b32 a31, v6\n"
        "v_lshlrev_b32 v8, 16, v104\n"
        "v_lshlrev_b32 v10, 16, v105\n"
        "v_lshlrev_b32 v12, 16, v106\n"
        "v_lshlrev_b32 v14, 16, v107\n"
        "v_and_b32 v9, 0xffff0000, v104\n"
        "v_and_b32 v11, 0xffff0000, v105\n"
        "v_and_b32 v13, 0xffff0000, v106\n"
        "v_and_b32 v15, 0xffff0000, v107\n"
        "v_pk_mul_f32 v[8:9], v[8:9], s[50:51]\n"
        "v_pk_mul_f32 v[10:11], v[10:11], s[50:51]\n"
        "v_pk_mul_f32 v[12:13], v[12:13], s[50:51]\n"
        "v_pk_mul_f32 v[14:15], v[14:15], s[50:51]\n"
        "buffer_load_dwordx4 v253, s[56:59], s64 offen offset:2048 lds\n"
        "v_cvt_pk_bf16_f32 v8, v8, v9\n"
        "v_cvt_pk_bf16_f32 v10, v10, v11\n"
        "v_cvt_pk_bf16_f32 v12, v12, v13\n"
        "v_cvt_pk_bf16_f32 v14, v14, v15\n"
        "v_accvgpr_write_b32 a32, v8\n"
        "v_accvgpr_write_b32 a33, v10\n"
        "v_accvgpr_write_b32 a34, v12\n"
        "v_accvgpr_write_b32 a35, v14\n"
        "v_lshlrev_b32 v0, 16, v108\n"
        "v_lshlrev_b32 v2, 16, v109\n"
        "v_lshlrev_b32 v4, 16, v110\n"
        "v_lshlrev_b32 v6, 16, v111\n"
        "v_and_b32 v1, 0xffff0000, v108\n"
        "v_and_b32 v3, 0xffff0000, v109\n"
        "v_and_b32 v5, 0xffff0000, v110\n"
        "v_and_b32 v7, 0xffff0000, v111\n"
        "buffer_load_dwordx4 v253, s[56:59], s64 offen offset:3072 lds\n"
        "v_pk_mul_f32 v[0:1], v[0:1], s[50:51]\n"
        "v_pk_mul_f32 v[2:3], v[2:3], s[50:51]\n"
        "v_pk_mul_f32 v[4:5], v[4:5], s[50:51]\n"
        "v_pk_mul_f32 v[6:7], v[6:7], s[50:51]\n"
        "v_cvt_pk_bf16_f32 v0, v0, v1\n"
        "v_cvt_pk_bf16_f32 v2, v2, v3\n"
        "v_cvt_pk_bf16_f32 v4, v4, v5\n"
        "v_cvt_pk_bf16_f32 v6, v6, v7\n"
        "v_accvgpr_write_b32 a48, v0\n"
        "v_accvgpr_write_b32 a49, v2\n"
        "v_accvgpr_write_b32 a50, v4\n"
        "v_accvgpr_write_b32 a51, v6\n"
        "v_lshlrev_b32 v8, 16, v116\n"
        "v_lshlrev_b32 v10, 16, v117\n"
        "v_lshlrev_b32 v12, 16, v118\n"
        "v_lshlrev_b32 v14, 16, v119\n"
        "v_and_b32 v9, 0xffff0000, v116\n"
        "v_and_b32 v11, 0xffff0000, v117\n"
        "v_and_b32 v13, 0xffff0000, v118\n"
        "v_and_b32 v15, 0xffff0000, v119\n"
        "v_pk_mul_f32 v[8:9], v[8:9], s[50:51]\n"
        "v_pk_mul_f32 v[10:11], v[10:11], s[50:51]\n"
        "v_pk_mul_f32 v[12:13], v[12:13], s[50:51]\n"
        "v_pk_mul_f32 v[14:15], v[14:15], s[50:51]\n"
        "v_cvt_pk_bf16_f32 v8, v8, v9\n"
        "v_cvt_pk_bf16_f32 v10, v10, v11\n"
        "v_cvt_pk_bf16_f32 v12, v12, v13\n"
        "v_cvt_pk_bf16_f32 v14, v14, v15\n"
        "v_accvgpr_write_b32 a52, v8\n"
        "v_accvgpr_write_b32 a53, v10\n"
        "v_accvgpr_write_b32 a54, v12\n"
        "v_accvgpr_write_b32 a55, v14\n"
        "v_lshlrev_b32 v0, 16, v120\n"
        "v_lshlrev_b32 v2, 16, v121\n"
        "v_lshlrev_b32 v4, 16, v122\n"
        "v_lshlrev_b32 v6, 16, v123\n"
        "v_and_b32 v1, 0xffff0000, v120\n"
        "v_and_b32 v3, 0xffff0000, v121\n"
        "v_and_b32 v5, 0xffff0000, v122\n"
        "v_and_b32 v7, 0xffff0000, v123\n"
        "v_pk_mul_f32 v[0:1], v[0:1], s[50:51]\n"
        "v_pk_mul_f32 v[2:3], v[2:3], s[50:51]\n"
        "v_pk_mul_f32 v[4:5], v[4:5], s[50:51]\n"
        "v_pk_mul_f32 v[6:7], v[6:7], s[50:51]\n"
        "v_cvt_pk_bf16_f32 v0, v0, v1\n"
        "v_cvt_pk_bf16_f32 v2, v2, v3\n"
        "v_cvt_pk_bf16_f32 v4, v4, v5\n"
        "v_cvt_pk_bf16_f32 v6, v6, v7\n"
        "v_accvgpr_write_b32 a56, v0\n"
        "v_accvgpr_write_b32 a57, v2\n"
        "v_accvgpr_write_b32 a58, v4\n"
        "v_accvgpr_write_b32 a59, v6\n"
        "s_waitcnt lgkmcnt(15)\n"
        "v_lshlrev_b32 v8, 16, v152\n"
        "v_lshlrev_b32 v10, 16, v153\n"
        "v_lshlrev_b32 v12, 16, v154\n"
        "v_lshlrev_b32 v14, 16, v155\n"
        "v_and_b32 v9, 0xffff0000, v152\n"
        "v_and_b32 v11, 0xffff0000, v153\n"
        "v_and_b32 v13, 0xffff0000, v154\n"
        "v_and_b32 v15, 0xffff0000, v155\n"
        "v_pk_mul_f32 v[8:9], v[8:9], s[50:51]\n"
        "v_pk_mul_f32 v[10:11], v[10:11], s[50:51]\n"
        "v_pk_mul_f32 v[12:13], v[12:13], s[50:51]\n"
        "v_pk_mul_f32 v[14:15], v[14:15], s[50:51]\n"
        "v_cvt_pk_bf16_f32 v8, v8, v9\n"
        "v_cvt_pk_bf16_f32 v10, v10, v11\n"
        "v_cvt_pk_bf16_f32 v12, v12, v13\n"
        "v_cvt_pk_bf16_f32 v14, v14, v15\n"
        "v_accvgpr_write_b32 a12, v8\n"
        "v_accvgpr_write_b32 a13, v10\n"
        "v_accvgpr_write_b32 a14, v12\n"
        "v_accvgpr_write_b32 a15, v14\n"
        "v_lshlrev_b32 v0, 16, v172\n"
        "v_lshlrev_b32 v2, 16, v173\n"
        "s_waitcnt lgkmcnt(14)\n"
        "v_lshlrev_b32 v4, 16, v174\n"
        "v_lshlrev_b32 v6, 16, v175\n"
        "v_and_b32 v1, 0xffff0000, v172\n"
        "v_and_b32 v3, 0xffff0000, v173\n"
        "v_and_b32 v5, 0xffff0000, v174\n"
        "v_and_b32 v7, 0xffff0000, v175\n"
        "v_pk_mul_f32 v[0:1], v[0:1], s[50:51]\n"
        "v_pk_mul_f32 v[2:3], v[2:3], s[50:51]\n"
        "v_pk_mul_f32 v[4:5], v[4:5], s[50:51]\n"
        "v_pk_mul_f32 v[6:7], v[6:7], s[50:51]\n"
        "v_cvt_pk_bf16_f32 v0, v0, v1\n"
        "v_cvt_pk_bf16_f32 v2, v2, v3\n"
        "v_cvt_pk_bf16_f32 v4, v4, v5\n"
        "v_cvt_pk_bf16_f32 v6, v6, v7\n"
        "v_accvgpr_write_b32 a16, v0\n"
        "v_accvgpr_write_b32 a17, v2\n"
        "v_accvgpr_write_b32 a18, v4\n"
        "v_accvgpr_write_b32 a19, v6\n"
        "s_waitcnt lgkmcnt(13)\n"
        "v_lshlrev_b32 v8, 16, v192\n"
        "v_lshlrev_b32 v10, 16, v193\n"
        "s_waitcnt lgkmcnt(12)\n"
        "v_lshlrev_b32 v12, 16, v194\n"
        "v_lshlrev_b32 v14, 16, v195\n"
        "v_and_b32 v9, 0xffff0000, v192\n"
        "v_and_b32 v11, 0xffff0000, v193\n"
        "v_and_b32 v13, 0xffff0000, v194\n"
        "v_and_b32 v15, 0xffff0000, v195\n"
        "v_pk_mul_f32 v[8:9], v[8:9], s[50:51]\n"
        "v_pk_mul_f32 v[10:11], v[10:11], s[50:51]\n"
        "v_pk_mul_f32 v[12:13], v[12:13], s[50:51]\n"
        "v_pk_mul_f32 v[14:15], v[14:15], s[50:51]\n"
        "v_cvt_pk_bf16_f32 v8, v8, v9\n"
        "v_cvt_pk_bf16_f32 v10, v10, v11\n"
        "v_cvt_pk_bf16_f32 v12, v12, v13\n"
        "v_cvt_pk_bf16_f32 v14, v14, v15\n"
        "v_accvgpr_write_b32 a20, v8\n"
        "v_accvgpr_write_b32 a21, v10\n"
        "v_accvgpr_write_b32 a22, v12\n"
        "v_accvgpr_write_b32 a23, v14\n"
        "s_waitcnt lgkmcnt(11)\n"
        "v_lshlrev_b32 v0, 16, v212\n"
        "v_lshlrev_b32 v2, 16, v213\n"
        "s_waitcnt lgkmcnt(10)\n"
        "v_lshlrev_b32 v4, 16, v214\n"
        "v_lshlrev_b32 v6, 16, v215\n"
        "v_and_b32 v1, 0xffff0000, v212\n"
        "v_and_b32 v3, 0xffff0000, v213\n"
        "v_and_b32 v5, 0xffff0000, v214\n"
        "v_and_b32 v7, 0xffff0000, v215\n"
        "v_pk_mul_f32 v[0:1], v[0:1], s[50:51]\n"
        "v_pk_mul_f32 v[2:3], v[2:3], s[50:51]\n"
        "v_pk_mul_f32 v[4:5], v[4:5], s[50:51]\n"
        "v_pk_mul_f32 v[6:7], v[6:7], s[50:51]\n"
        "v_cvt_pk_bf16_f32 v0, v0, v1\n"
        "v_cvt_pk_bf16_f32 v2, v2, v3\n"
        "v_cvt_pk_bf16_f32 v4, v4, v5\n"
        "v_cvt_pk_bf16_f32 v6, v6, v7\n"
        "s_add_u32 m0, s40, 98304\n"
        "v_accvgpr_write_b32 a36, v0\n"
        "v_accvgpr_write_b32 a37, v2\n"
        "v_accvgpr_write_b32 a38, v4\n"
        "v_accvgpr_write_b32 a39, v6\n"
        "s_add_u32 s64, s65, 384\n"
        "s_waitcnt lgkmcnt(9)\n"
        "v_lshlrev_b32 v8, 16, v156\n"
        "v_lshlrev_b32 v10, 16, v157\n"
        "s_waitcnt lgkmcnt(8)\n"
        "v_lshlrev_b32 v12, 16, v158\n"
        "v_lshlrev_b32 v14, 16, v159\n"
        "buffer_load_dwordx4 v128, s[20:23], s64 offen lds\n"
        "v_and_b32 v9, 0xffff0000, v156\n"
        "v_and_b32 v11, 0xffff0000, v157\n"
        "v_and_b32 v13, 0xffff0000, v158\n"
        "v_and_b32 v15, 0xffff0000, v159\n"
        "v_pk_mul_f32 v[8:9], v[8:9], s[50:51]\n"
        "v_pk_mul_f32 v[10:11], v[10:11], s[50:51]\n"
        "v_pk_mul_f32 v[12:13], v[12:13], s[50:51]\n"
        "v_pk_mul_f32 v[14:15], v[14:15], s[50:51]\n"
        "v_cvt_pk_bf16_f32 v8, v8, v9\n"
        "v_cvt_pk_bf16_f32 v10, v10, v11\n"
        "v_cvt_pk_bf16_f32 v12, v12, v13\n"
        "v_cvt_pk_bf16_f32 v14, v14, v15\n"
        "v_accvgpr_write_b32 a40, v8\n"
        "v_accvgpr_write_b32 a41, v10\n"
        "v_accvgpr_write_b32 a42, v12\n"
        "v_accvgpr_write_b32 a43, v14\n"
        "buffer_load_dwordx4 v129, s[20:23], s64 offen offset:1024 lds\n"
        "s_waitcnt lgkmcnt(7)\n"
        "v_lshlrev_b32 v0, 16, v176\n"
        "v_lshlrev_b32 v2, 16, v177\n"
        "s_waitcnt lgkmcnt(6)\n"
        "v_lshlrev_b32 v4, 16, v178\n"
        "v_lshlrev_b32 v6, 16, v179\n"
        "v_and_b32 v1, 0xffff0000, v176\n"
        "v_and_b32 v3, 0xffff0000, v177\n"
        "v_and_b32 v5, 0xffff0000, v178\n"
        "v_and_b32 v7, 0xffff0000, v179\n"
        "v_pk_mul_f32 v[0:1], v[0:1], s[50:51]\n"
        "v_pk_mul_f32 v[2:3], v[2:3], s[50:51]\n"
        "v_pk_mul_f32 v[4:5], v[4:5], s[50:51]\n"
        "v_pk_mul_f32 v[6:7], v[6:7], s[50:51]\n"
        "v_cvt_pk_bf16_f32 v0, v0, v1\n"
        "v_cvt_pk_bf16_f32 v2, v2, v3\n"
        "v_cvt_pk_bf16_f32 v4, v4, v5\n"
        "v_cvt_pk_bf16_f32 v6, v6, v7\n"
        "buffer_load_dwordx4 v130, s[20:23], s64 offen offset:2048 lds\n"
        "v_accvgpr_write_b32 a44, v0\n"
        "v_accvgpr_write_b32 a45, v2\n"
        "v_accvgpr_write_b32 a46, v4\n"
        "v_accvgpr_write_b32 a47, v6\n"
        "s_waitcnt lgkmcnt(5)\n"
        "v_lshlrev_b32 v8, 16, v196\n"
        "v_lshlrev_b32 v10, 16, v197\n"
        "s_waitcnt lgkmcnt(4)\n"
        "v_lshlrev_b32 v12, 16, v198\n"
        "v_lshlrev_b32 v14, 16, v199\n"
        "s_add_u32 m0, s40, 110592\n"
        "v_and_b32 v9, 0xffff0000, v196\n"
        "v_and_b32 v11, 0xffff0000, v197\n"
        "v_and_b32 v13, 0xffff0000, v198\n"
        "v_and_b32 v15, 0xffff0000, v199\n"
        "s_add_u32 s64, s65, 37248\n"
        "v_pk_mul_f32 v[8:9], v[8:9], s[50:51]\n"
        "v_pk_mul_f32 v[10:11], v[10:11], s[50:51]\n"
        "v_pk_mul_f32 v[12:13], v[12:13], s[50:51]\n"
        "v_pk_mul_f32 v[14:15], v[14:15], s[50:51]\n"
        "buffer_load_dwordx4 v128, s[20:23], s64 offen lds\n"
        "v_cvt_pk_bf16_f32 v8, v8, v9\n"
        "v_cvt_pk_bf16_f32 v10, v10, v11\n"
        "v_cvt_pk_bf16_f32 v12, v12, v13\n"
        "v_cvt_pk_bf16_f32 v14, v14, v15\n"
        "v_accvgpr_write_b32 a60, v8\n"
        "v_accvgpr_write_b32 a61, v10\n"
        "v_accvgpr_write_b32 a62, v12\n"
        "v_accvgpr_write_b32 a63, v14\n"
        "s_waitcnt lgkmcnt(3)\n"
        "v_lshlrev_b32 v0, 16, v216\n"
        "v_lshlrev_b32 v2, 16, v217\n"
        "s_waitcnt lgkmcnt(2)\n"
        "v_lshlrev_b32 v4, 16, v218\n"
        "v_lshlrev_b32 v6, 16, v219\n"
        "v_and_b32 v1, 0xffff0000, v216\n"
        "v_and_b32 v3, 0xffff0000, v217\n"
        "v_and_b32 v5, 0xffff0000, v218\n"
        "v_and_b32 v7, 0xffff0000, v219\n"
        "buffer_load_dwordx4 v129, s[20:23], s64 offen offset:1024 lds\n"
        "v_pk_mul_f32 v[0:1], v[0:1], s[50:51]\n"
        "v_pk_mul_f32 v[2:3], v[2:3], s[50:51]\n"
        "v_pk_mul_f32 v[4:5], v[4:5], s[50:51]\n"
        "v_pk_mul_f32 v[6:7], v[6:7], s[50:51]\n"
        "v_cvt_pk_bf16_f32 v0, v0, v1\n"
        "v_cvt_pk_bf16_f32 v2, v2, v3\n"
        "v_cvt_pk_bf16_f32 v4, v4, v5\n"
        "v_cvt_pk_bf16_f32 v6, v6, v7\n"
        "v_accvgpr_write_b32 a64, v0\n"
        "v_accvgpr_write_b32 a65, v2\n"
        "v_accvgpr_write_b32 a66, v4\n"
        "v_accvgpr_write_b32 a67, v6\n"
        "s_waitcnt lgkmcnt(1)\n"
        "v_lshlrev_b32 v8, 16, v124\n"
        "v_lshlrev_b32 v10, 16, v125\n"
        "s_waitcnt lgkmcnt(0)\n"
        "v_lshlrev_b32 v12, 16, v126\n"
        "v_lshlrev_b32 v14, 16, v127\n"
        "buffer_load_dwordx4 v130, s[20:23], s64 offen offset:2048 lds\n"
        "v_and_b32 v9, 0xffff0000, v124\n"
        "v_and_b32 v11, 0xffff0000, v125\n"
        "v_and_b32 v13, 0xffff0000, v126\n"
        "v_and_b32 v15, 0xffff0000, v127\n"
        "v_pk_mul_f32 v[8:9], v[8:9], s[50:51]\n"
        "v_pk_mul_f32 v[10:11], v[10:11], s[50:51]\n"
        "v_pk_mul_f32 v[12:13], v[12:13], s[50:51]\n"
        "v_pk_mul_f32 v[14:15], v[14:15], s[50:51]\n"
        "s_add_u32 m0, s40, 122880\n"
        "v_cvt_pk_bf16_f32 v8, v8, v9\n"
        "v_cvt_pk_bf16_f32 v10, v10, v11\n"
        "v_cvt_pk_bf16_f32 v12, v12, v13\n"
        "v_cvt_pk_bf16_f32 v14, v14, v15\n"
        "s_add_u32 s64, s65, 74112\n"
        "v_accvgpr_write_b32 a68, v8\n"
        "v_accvgpr_write_b32 a69, v10\n"
        "v_accvgpr_write_b32 a70, v12\n"
        "v_accvgpr_write_b32 a71, v14\n"
        "buffer_load_dwordx4 v128, s[20:23], s64 offen lds\n"
        "buffer_load_dwordx4 v129, s[20:23], s64 offen offset:1024 lds\n"
        "buffer_load_dwordx4 v130, s[20:23], s64 offen offset:2048 lds\n"
        "s_waitcnt vmcnt(20)\n"
        "s_barrier\n"
        "ds_read_b128 a[224:227], v150 offset:0\n"
        "s_waitcnt lgkmcnt(0)\n"
        "v_mfma_f32_16x16x32_bf16 v[96:99], a[0:3], a[224:227], 0\n"
        "ds_read_b128 a[228:231], v151 offset:0\n"
        "ds_read_b128 a[232:235], v150 offset:128\n"
        "s_add_u32 m0, s40, 0\n"
        "s_add_u32 s64, s65, 110976\n"
        "s_waitcnt lgkmcnt(1)\n"
        "v_mfma_f32_16x16x32_bf16 v[96:99], a[4:7], a[228:231], v[96:99]\n"
        "s_waitcnt lgkmcnt(0)\n"
        "ds_read_b128 a[236:239], v151 offset:128\n"
        "ds_read_b128 a[240:243], v150 offset:256\n"
        "v_mfma_f32_16x16x32_bf16 v[96:99], a[8:11], a[232:235], v[96:99]\n"
        "s_waitcnt lgkmcnt(1)\n"
        "ds_read_b128 a[244:247], v151 offset:256\n"
        "buffer_load_dwordx4 v128, s[20:23], s64 offen lds\n"
        "v_mfma_f32_16x16x32_bf16 v[96:99], a[12:15], a[236:239], v[96:99]\n"
        "s_waitcnt lgkmcnt(1)\n"
        "ds_read_b128 a[248:251], v150 offset:1536\n"
        "ds_read_b128 a[216:219], v151 offset:1536\n"
        "v_mfma_f32_16x16x32_bf16 v[96:99], a[16:19], a[240:243], v[96:99]\n"
        "s_waitcnt lgkmcnt(2)\n"
        "ds_read_b128 a[220:223], v150 offset:1664\n"
        "v_mfma_f32_16x16x32_bf16 v[96:99], a[20:23], a[244:247], v[96:99]\n"
        "buffer_load_dwordx4 v129, s[20:23], s64 offen offset:1024 lds\n"
        "v_mfma_f32_16x16x32_bf16 v[100:103], a[24:27], a[224:227], 0\n"
        "v_mfma_f32_16x16x32_bf16 v[100:103], a[28:31], a[228:231], v[100:103]\n"
        "v_mfma_f32_16x16x32_bf16 v[100:103], a[32:35], a[232:235], v[100:103]\n"
        "v_mfma_f32_16x16x32_bf16 v[100:103], a[36:39], a[236:239], v[100:103]\n"
        "buffer_load_dwordx4 v130, s[20:23], s64 offen offset:2048 lds\n"
        "v_mfma_f32_16x16x32_bf16 v[100:103], a[40:43], a[240:243], v[100:103]\n"
        "s_waitcnt vmcnt(19)\n"
        "v_lshlrev_b32 v236, 16, v236\n"
        "v_cvt_pk_bf16_f32 v156, v96, v97\n"
        "v_mfma_f32_16x16x32_bf16 v[100:103], a[44:47], a[244:247], v[100:103]\n"
        "s_add_u32 m0, s40, 12288\n"
        "ds_write_b32 v146, v236 offset:0\n"
        "v_mfma_f32_16x16x32_bf16 v[112:115], a[48:51], a[224:227], 0\n"
        "ds_read_b128 a[224:227], v151 offset:1664\n"
        "s_add_u32 s64, s65, 147840\n"
        "v_cvt_pk_bf16_f32 v157, v98, v99\n"
        "s_waitcnt vmcnt(18)\n"
        "v_lshlrev_b32 v237, 16, v237\n"
        "v_mfma_f32_16x16x32_bf16 v[112:115], a[52:55], a[228:231], v[112:115]\n"
        "ds_read_b128 a[228:231], v150 offset:1792\n"
        "buffer_load_dwordx4 v128, s[20:23], s64 offen lds\n"
        "v_cvt_pk_bf16_f32 v158, v100, v101\n"
        "v_mfma_f32_16x16x32_bf16 v[112:115], a[56:59], a[232:235], v[112:115]\n"
        "ds_read_b128 a[232:235], v151 offset:1792\n"
        "ds_write_b32 v146, v237 offset:256\n"
        "v_mfma_f32_16x16x32_bf16 v[112:115], a[60:63], a[236:239], v[112:115]\n"
        "v_cvt_pk_bf16_f32 v159, v102, v103\n"
        "s_waitcnt vmcnt(18)\n"
        "v_lshlrev_b32 v238, 16, v238\n"
        "s_waitcnt vmcnt(17)\n"
        "v_lshlrev_b32 v239, 16, v239\n"
        "v_mfma_f32_16x16x32_bf16 v[112:115], a[64:67], a[240:243], v[112:115]\n"
        "v_mfma_f32_16x16x32_bf16 v[112:115], a[68:71], a[244:247], v[112:115]\n"
        "s_waitcnt lgkmcnt(7)\n"
        "ds_write_b32 v146, v238 offset:512\n"
        "buffer_load_dwordx4 v129, s[20:23], s64 offen offset:1024 lds\n"
        "v_mfma_f32_16x16x32_bf16 v[96:99], a[0:3], a[248:251], 0\n"
        "s_waitcnt lgkmcnt(7)\n"
        "ds_write_b32 v146, v239 offset:768\n"
        "s_add_u32 s35, s47, 786432\n"
        "s_sub_u32 s46, s35, 100663296\n"
        "s_cmp_lt_u32 s46, 786432\n"
        "s_waitcnt vmcnt(5)\n"
        "s_barrier\n"
        "v_mfma_f32_16x16x32_bf16 v[96:99], a[4:7], a[216:219], v[96:99]\n"
        "s_waitcnt lgkmcnt(7)\n"
        "s_cselect_b32 s35, s46, s35\n"
        "s_add_u32 s41, s30, s40\n"
        "ds_read_b64_tr_b16 a[72:73], v136 offset:0\n"
        "ds_read_b64_tr_b16 a[74:75], v136 offset:1536\n"
        "v_mfma_f32_16x16x32_bf16 v[96:99], a[8:11], a[220:223], v[96:99]\n"
        "s_waitcnt lgkmcnt(7)\n"
        "ds_read_b64_tr_b16 a[76:77], v136 offset:12288\n"
        "ds_read_b64_tr_b16 a[78:79], v136 offset:13824\n"
        "v_cvt_pk_bf16_f32 v160, v112, v113\n"
        "v_mfma_f32_16x16x32_bf16 v[96:99], a[12:15], a[224:227], v[96:99]\n"
        "s_waitcnt lgkmcnt(8)\n"
        "buffer_load_dwordx4 v130, s[20:23], s64 offen offset:2048 lds\n"
        "ds_read_b64_tr_b16 a[80:81], v136 offset:24576\n"
        "v_cvt_pk_bf16_f32 v161, v114, v115\n"
        "v_mfma_f32_16x16x32_bf16 v[96:99], a[16:19], a[228:231], v[96:99]\n"
        "s_waitcnt lgkmcnt(8)\n"
        "ds_read_b64_tr_b16 a[82:83], v136 offset:26112\n"
        "ds_read_b64_tr_b16 a[96:97], v137 offset:0\n"
        "v_cvt_pk_bf16_f32 v166, v112, v113\n"
        "v_mfma_f32_16x16x32_bf16 v[96:99], a[20:23], a[232:235], v[96:99]\n"
        "s_add_u32 m0, s40, 24576\n"
        "ds_read_b64_tr_b16 a[98:99], v137 offset:1536\n"
        "ds_read_b128 a[236:239], v150 offset:12288\n"
        "v_cvt_pk_bf16_f32 v167, v114, v115\n"
        "v_mfma_f32_16x16x32_bf16 v[100:103], a[24:27], a[248:251], 0\n"
        "s_add_u32 s64, s65, 184704\n"
        "ds_read_b64_tr_b16 a[100:101], v137 offset:12288\n"
        "ds_read_b128 a[240:243], v151 offset:12288\n"
        "v_cvt_pk_bf16_f32 v176, v96, v97\n"
        "v_mfma_f32_16x16x32_bf16 v[100:103], a[28:31], a[216:219], v[100:103]\n"
        "buffer_load_dwordx4 v128, s[20:23], s64 offen lds\n"
        "ds_read_b64_tr_b16 a[102:103], v137 offset:13824\n"
        "v_cvt_pk_bf16_f32 v177, v98, v99\n"
        "v_mfma_f32_16x16x32_bf16 v[100:103], a[32:35], a[220:223], v[100:103]\n"
        "ds_read_b128 v[0:3], v232 offset:0\n"
        "ds_read_b128 a[244:247], v150 offset:12416\n"
        "v_mfma_f32_16x16x32_bf16 v[100:103], a[36:39], a[224:227], v[100:103]\n"
        "ds_read_b64_tr_b16 a[104:105], v137 offset:24576\n"
        "ds_read_b64_tr_b16 a[106:107], v137 offset:26112\n"
        "v_mfma_f32_16x16x32_bf16 v[100:103], a[40:43], a[228:231], v[100:103]\n"
        "ds_read_b64_tr_b16 a[120:121], v138 offset:0\n"
        "ds_read_b64_tr_b16 a[122:123], v138 offset:1536\n"
        "s_waitcnt lgkmcnt(5)\n"
        "v_lshl_add_u32 v0, v0, 2, s67\n"
        "v_mfma_f32_16x16x32_bf16 v[100:103], a[44:47], a[232:235], v[100:103]\n"
        "buffer_load_dwordx4 v129, s[20:23], s64 offen offset:1024 lds\n"
        "ds_read_b64_tr_b16 a[124:125], v138 offset:12288\n"
        "v_lshl_add_u32 v1, v1, 2, s67\n"
        "v_mfma_f32_16x16x32_bf16 v[112:115], a[48:51], a[248:251], 0\n"
        "ds_read_b128 a[248:251], v151 offset:12416\n"
        "ds_read_b128 v[16:19], v232 offset:1024\n"
        "v_lshl_add_u32 v2, v2, 2, s67\n"
        "v_mfma_f32_16x16x32_bf16 v[112:115], a[52:55], a[216:219], v[112:115]\n"
        "ds_read_b128 a[216:219], v150 offset:12544\n"
        "ds_read_b64_tr_b16 a[126:127], v138 offset:13824\n"
        "v_cvt_pk_bf16_f32 v178, v100, v101\n"
        "v_mfma_f32_16x16x32_bf16 v[112:115], a[56:59], a[220:223], v[112:115]\n"
        "ds_read_b128 a[220:223], v151 offset:12544\n"
        "ds_read_b64_tr_b16 a[128:129], v138 offset:24576\n"
        "v_cvt_pk_bf16_f32 v179, v102, v103\n"
        "v_mfma_f32_16x16x32_bf16 v[112:115], a[60:63], a[224:227], v[112:115]\n"
        "buffer_load_dwordx4 v130, s[20:23], s64 offen offset:2048 lds\n"
        "ds_read_b64_tr_b16 a[130:131], v138 offset:26112\n"
        "v_lshl_add_u32 v3, v3, 2, s67\n"
        "v_mfma_f32_16x16x32_bf16 v[112:115], a[64:67], a[228:231], v[112:115]\n"
        "ds_read_b128 v[32:35], v232 offset:8192\n"
        "ds_read_b128 v[48:51], v232 offset:9216\n"
        "s_waitcnt lgkmcnt(7)\n"
        "v_lshl_add_u32 v16, v16, 2, s67\n"
        "v_mfma_f32_16x16x32_bf16 v[112:115], a[68:71], a[232:235], v[112:115]\n"
        "ds_read_b128 a[224:227], v150 offset:13824\n"
        "ds_read_b128 a[228:231], v151 offset:13824\n"
        "v_xor_b32 v253, 32, v249\n"
        "v_mfma_f32_16x16x32_bf16 v[96:99], a[0:3], a[236:239], 0\n"
        "ds_read_b128 a[232:235], v150 offset:13952\n"
        "ds_read_b32 v0, v0\n"
        "v_lshl_add_u32 v17, v17, 2, s67\n"
        "v_mfma_f32_16x16x32_bf16 v[96:99], a[4:7], a[240:243], v[96:99]\n"
        "s_xor_b32 m0, s48, 8192\n"
        "ds_read_b32 v1, v1\n"
        "ds_read_b32 v2, v2\n"
        "v_cvt_pk_bf16_f32 v180, v112, v113\n"
        "v_mfma_f32_16x16x32_bf16 v[96:99], a[8:11], a[244:247], v[96:99]\n"
        "s_sub_u32 s64, s36, 2097152\n"
        "ds_read_b128 v[4:7], v233 offset:0\n"
        "ds_read_b32 v3, v3\n"
        "v_cvt_pk_bf16_f32 v181, v114, v115\n"
        "v_mfma_f32_16x16x32_bf16 v[96:99], a[12:15], a[248:251], v[96:99]\n"
        "s_waitcnt lgkmcnt(14)\n"
        "buffer_load_dwordx4 v249, s[12:15], s64 offen lds\n"
        "ds_read_b32 v16, v16\n"
        "v_cvt_pk_bf16_f32 v186, v112, v113\n"
        "v_mfma_f32_16x16x32_bf16 v[96:99], a[16:19], a[216:219], v[96:99]\n"
        "s_waitcnt lgkmcnt(13)\n"
        "ds_read_b32 v17, v17\n"
        "v_cvt_pk_bf16_f32 v187, v114, v115\n"
        "v_lshl_add_u32 v18, v18, 2, s67\n"
        "v_mfma_f32_16x16x32_bf16 v[96:99], a[20:23], a[220:223], v[96:99]\n"
        "ds_read_b32 v18, v18\n"
        "v_lshl_add_u32 v19, v19, 2, s67\n"
        "s_waitcnt lgkmcnt(12)\n"
        "v_lshl_add_u32 v32, v32, 2, s67\n"
        "v_mfma_f32_16x16x32_bf16 v[100:103], a[24:27], a[236:239], 0\n"
        "s_barrier\n"
        "ds_read_b32 v19, v19\n"
        "ds_read_b32 v32, v32\n"
        "v_lshl_add_u32 v33, v33, 2, s67\n"
        "v_mfma_f32_16x16x32_bf16 v[100:103], a[28:31], a[240:243], v[100:103]\n"
        "buffer_load_dwordx4 v253, s[12:15], s64 offen offset:1024 lds\n"
        "ds_read_b128 v[20:23], v233 offset:1024\n"
        "v_cvt_pk_bf16_f32 v196, v96, v97\n"
        "v_mfma_f32_16x16x32_bf16 v[100:103], a[32:35], a[244:247], v[100:103]\n"
        "ds_read_b32 v33, v33\n"
        "v_cvt_pk_bf16_f32 v197, v98, v99\n"
        "v_lshl_add_u32 v34, v34, 2, s67\n"
        "v_mfma_f32_16x16x32_bf16 v[100:103], a[36:39], a[248:251], v[100:103]\n"
        "ds_read_b32 v34, v34\n"
        "v_lshl_add_u32 v35, v35, 2, s67\n"
        "s_waitcnt lgkmcnt(15)\n"
        "v_lshl_add_u32 v48, v48, 2, s67\n"
        "v_mfma_f32_16x16x32_bf16 v[100:103], a[40:43], a[216:219], v[100:103]\n"
        "s_add_u32 m0, s41, 0\n"
        "ds_read_b32 v35, v35\n"
        "ds_read_b32 v48, v48\n"
        "v_lshl_add_u32 v49, v49, 2, s67\n"
        "v_mfma_f32_16x16x32_bf16 v[100:103], a[44:47], a[220:223], v[100:103]\n"
        "buffer_load_dwordx4 v242, s[8:11], s47 offen lds\n"
        "ds_read_b32 v49, v49\n"
        "v_mfma_f32_16x16x32_bf16 v[112:115], a[48:51], a[236:239], 0\n"
        "ds_read_b128 a[236:239], v151 offset:13952\n"
        "ds_read_b128 v[36:39], v233 offset:8192\n"
        "v_lshl_add_u32 v50, v50, 2, s67\n"
        "v_mfma_f32_16x16x32_bf16 v[112:115], a[52:55], a[240:243], v[112:115]\n"
        "ds_read_b128 a[240:243], v150 offset:14080\n"
        "ds_read_b32 v50, v50\n"
        "v_cvt_pk_bf16_f32 v198, v100, v101\n"
        "v_mfma_f32_16x16x32_bf16 v[112:115], a[56:59], a[244:247], v[112:115]\n"
        "ds_read_b128 a[244:247], v151 offset:14080\n"
        "v_cvt_pk_bf16_f32 v199, v102, v103\n"
        "v_lshl_add_u32 v51, v51, 2, s67\n"
        "v_mfma_f32_16x16x32_bf16 v[112:115], a[60:63], a[248:251], v[112:115]\n"
        "s_add_u32 m0, s41, 1024\n"
        "ds_read_b128 a[248:251], v150 offset:0\n"
        "ds_read_b32 v51, v51\n"
        "s_waitcnt lgkmcnt(15)\n"
        "v_lshl_add_u32 v4, v4, 2, s67\n"
        "v_mfma_f32_16x16x32_bf16 v[112:115], a[64:67], a[216:219], v[112:115]\n"
        "buffer_load_dwordx4 v243, s[8:11], s47 offen lds\n"
        "ds_read_b128 a[216:219], v151 offset:0\n"
        "v_mfma_f32_16x16x32_bf16 v[112:115], a[68:71], a[220:223], v[112:115]\n"
        "ds_read_b128 v[52:55], v233 offset:9216\n"
        "ds_read_b128 a[220:223], v150 offset:128\n"
        "v_lshl_add_u32 v5, v5, 2, s67\n"
        "v_mfma_f32_16x16x32_bf16 v[96:99], a[0:3], a[224:227], 0\n"
        "ds_read_b32 v4, v4\n"
        "ds_read_b32 v5, v5\n"
        "v_lshl_add_u32 v6, v6, 2, s67\n"
        "v_mfma_f32_16x16x32_bf16 v[96:99], a[4:7], a[228:231], v[96:99]\n"
        "ds_read_b32 v6, v6\n"
        "v_cvt_pk_bf16_f32 v200, v112, v113\n"
        "v_cvt_pk_bf16_f32 v201, v114, v115\n"
        "v_mfma_f32_16x16x32_bf16 v[96:99], a[8:11], a[232:235], v[96:99]\n"
        "s_waitcnt lgkmcnt(12)\n"
        "s_add_u32 m0, s41, 2048\n"
        "v_cvt_pk_bf16_f32 v206, v112, v113\n"
        "v_cvt_pk_bf16_f32 v207, v114, v115\n"
        "v_lshl_add_u32 v7, v7, 2, s67\n"
        "v_mfma_f32_16x16x32_bf16 v[96:99], a[12:15], a[236:239], v[96:99]\n"
        "s_waitcnt lgkmcnt(10)\n"
        "buffer_load_dwordx4 v244, s[8:11], s47 offen lds\n"
        "ds_read_b32 v7, v7\n"
        "v_mfma_f32_16x16x32_bf16 v[96:99], a[16:19], a[240:243], v[96:99]\n"
        "s_waitcnt lgkmcnt(9)\n"
        "ds_read_b128 v[8:11], v234 offset:0\n"
        "v_lshl_add_u32 v20, v20, 2, s67\n"
        "v_lshl_add_u32 v21, v21, 2, s67\n"
        "v_mfma_f32_16x16x32_bf16 v[96:99], a[20:23], a[244:247], v[96:99]\n"
        "ds_read_b32 v20, v20\n"
        "ds_read_b32 v21, v21\n"
        "v_lshl_add_u32 v22, v22, 2, s67\n"
        "v_mfma_f32_16x16x32_bf16 v[100:103], a[24:27], a[224:227], 0\n"
        "s_waitcnt vmcnt(5)\n"
        "s_barrier\n"
        "ds_read_b32 v22, v22\n"
        "v_lshl_add_u32 v23, v23, 2, s67\n"
        "v_lshl_add_u32 v36, v36, 2, s67\n"
        "v_mfma_f32_16x16x32_bf16 v[100:103], a[28:31], a[228:231], v[100:103]\n"
        "s_add_u32 m0, s41, 12288\n"
        "ds_read_b64_tr_b16 a[84:85], v131 offset:0\n"
        "ds_read_b64_tr_b16 a[86:87], v131 offset:1536\n"
        "ds_read_b64_tr_b16 a[88:89], v131 offset:12288\n"
        "v_mfma_f32_16x16x32_bf16 v[100:103], a[32:35], a[232:235], v[100:103]\n"
        "ds_read_b64_tr_b16 a[90:91], v131 offset:13824\n"
        "ds_read_b64_tr_b16 a[92:93], v131 offset:24576\n"
        "ds_read_b64_tr_b16 a[94:95], v131 offset:26112\n"
        "s_add_u32 s47, s47, 6291456\n"
        "v_mfma_f32_16x16x32_bf16 v[100:103], a[36:39], a[236:239], v[100:103]\n"
        "ds_read_b64_tr_b16 a[108:109], v132 offset:0\n"
        "ds_read_b64_tr_b16 a[110:111], v132 offset:1536\n"
        "ds_read_b64_tr_b16 a[112:113], v132 offset:12288\n"
        "s_sub_u32 s31, s30, s28\n"
        "v_mfma_f32_16x16x32_bf16 v[100:103], a[40:43], a[240:243], v[100:103]\n"
        "ds_read_b64_tr_b16 a[114:115], v132 offset:13824\n"
        "ds_read_b64_tr_b16 a[116:117], v132 offset:24576\n"
        "ds_read_b64_tr_b16 a[118:119], v132 offset:26112\n"
        "s_sub_u32 s45, s28, s29\n"
        "v_mfma_f32_16x16x32_bf16 v[100:103], a[44:47], a[244:247], v[100:103]\n"
        "ds_read_b64_tr_b16 a[132:133], v133 offset:0\n"
        "ds_read_b64_tr_b16 a[134:135], v133 offset:1536\n"
        "ds_read_b64_tr_b16 a[136:137], v133 offset:12288\n"
        "s_add_u32 s28, s28, s31\n"
        "v_mfma_f32_16x16x32_bf16 v[112:115], a[48:51], a[224:227], 0\n"
        "ds_read_b64_tr_b16 a[138:139], v133 offset:13824\n"
        "ds_read_b64_tr_b16 a[140:141], v133 offset:24576\n"
        "ds_read_b64_tr_b16 a[142:143], v133 offset:26112\n"
        "s_mov_b32 s30, s29\n"
        "v_mfma_f32_16x16x32_bf16 v[112:115], a[52:55], a[228:231], v[112:115]\n"
        "buffer_load_dwordx4 v242, s[8:11], s35 offen lds\n"
        "ds_read_b128 a[224:227], v151 offset:128\n"
        "s_add_u32 m0, s41, 13312\n"
        "v_mfma_f32_16x16x32_bf16 v[112:115], a[56:59], a[232:235], v[112:115]\n"
        "ds_read_b128 a[228:231], v150 offset:256\n"
        "buffer_load_dwordx4 v243, s[8:11], s35 offen lds\n"
        "s_add_u32 s29, s29, s45\n"
        "v_mfma_f32_16x16x32_bf16 v[112:115], a[60:63], a[236:239], v[112:115]\n"
        "ds_read_b128 a[232:235], v151 offset:256\n"
        "ds_read_b128 v[24:27], v234 offset:1024\n"
        "v_cvt_pk_bf16_f32 v216, v96, v97\n"
        "v_mfma_f32_16x16x32_bf16 v[112:115], a[64:67], a[240:243], v[112:115]\n"
        "ds_read_b32 v23, v23\n"
        "ds_read_b128 v[40:43], v234 offset:8192\n"
        "v_cvt_pk_bf16_f32 v217, v98, v99\n"
        "v_mfma_f32_16x16x32_bf16 v[112:115], a[68:71], a[244:247], v[112:115]\n"
        "s_waitcnt lgkmcnt(15)\n"
        "s_add_u32 m0, s41, 14336\n"
        "ds_read_b32 v36, v36\n"
        "ds_read_b128 a[236:239], v150 offset:1536\n"
        "v_cvt_pk_bf16_f32 v218, v100, v101\n"
        "v_mfma_f32_16x16x32_bf16 v[96:99], a[72:75], a[248:251], 0\n"
        "s_waitcnt lgkmcnt(15)\n"
        "buffer_load_dwordx4 v244, s[8:11], s35 offen lds\n"
        "ds_read_b128 a[240:243], v151 offset:1536\n"
        "v_mfma_f32_16x16x32_bf16 v[96:99], a[76:79], a[216:219], v[96:99]\n"
        "s_waitcnt lgkmcnt(15)\n"
        "ds_read_b128 a[244:247], v150 offset:1664\n"
        "ds_read_b128 v[56:59], v234 offset:9216\n"
        "v_cvt_pk_bf16_f32 v219, v102, v103\n"
        "v_mfma_f32_16x16x32_bf16 v[96:99], a[80:83], a[220:223], v[96:99]\n"
        "s_waitcnt lgkmcnt(10)\n"
        "v_cvt_pk_bf16_f32 v220, v112, v113\n"
        "v_cvt_pk_bf16_f32 v221, v114, v115\n"
        "v_cvt_pk_bf16_f32 v226, v112, v113\n"
        "v_mfma_f32_16x16x32_bf16 v[96:99], a[84:87], a[224:227], v[96:99]\n"
        "s_waitcnt lgkmcnt(9)\n"
        "s_add_u32 m0, s40, 36864\n"
        "v_cvt_pk_bf16_f32 v227, v114, v115\n"
        "v_lshl_add_u32 v37, v37, 2, s67\n"
        "v_lshl_add_u32 v38, v38, 2, s67\n"
        "v_mfma_f32_16x16x32_bf16 v[96:99], a[88:91], a[228:231], v[96:99]\n"
        "s_waitcnt lgkmcnt(8)\n"
        "s_add_u32 s64, s65, 768\n"
        "ds_read_b32 v37, v37\n"
        "ds_read_b32 v38, v38\n"
        "v_lshl_add_u32 v39, v39, 2, s67\n"
        "v_mfma_f32_16x16x32_bf16 v[96:99], a[92:95], a[232:235], v[96:99]\n"
        "buffer_load_dwordx4 v128, s[20:23], s64 offen lds\n"
        "ds_read_b32 v39, v39\n"
        "v_lshl_add_u32 v52, v52, 2, s67\n"
        "v_mfma_f32_16x16x32_bf16 v[100:103], a[96:99], a[248:251], 0\n"
        "ds_read_b32 v52, v52\n"
        "ds_read_b128 v[12:15], v235 offset:0\n"
        "v_lshl_add_u32 v53, v53, 2, s67\n"
        "v_mfma_f32_16x16x32_bf16 v[100:103], a[100:103], a[216:219], v[100:103]\n"
        "ds_read_b32 v53, v53\n"
        "v_cvt_pk_bf16_f32 v152, v96, v97\n"
        "v_cvt_pk_bf16_f32 v153, v98, v99\n"
        "v_mfma_f32_16x16x32_bf16 v[100:103], a[104:107], a[220:223], v[100:103]\n"
        "v_lshl_add_u32 v54, v54, 2, s67\n"
        "v_lshl_add_u32 v55, v55, 2, s67\n"
        "v_lshl_add_u32 v8, v8, 2, s67\n"
        "v_mfma_f32_16x16x32_bf16 v[100:103], a[108:111], a[224:227], v[100:103]\n"
        "ds_read_b32 v54, v54\n"
        "ds_read_b32 v55, v55\n"
        "v_lshl_add_u32 v9, v9, 2, s67\n"
        "v_mfma_f32_16x16x32_bf16 v[100:103], a[112:115], a[228:231], v[100:103]\n"
        "buffer_load_dwordx4 v129, s[20:23], s64 offen offset:1024 lds\n"
        "ds_read_b32 v8, v8\n"
        "v_lshl_add_u32 v10, v10, 2, s67\n"
        "v_mfma_f32_16x16x32_bf16 v[100:103], a[116:119], a[232:235], v[100:103]\n"
        "ds_read_b32 v9, v9\n"
        "ds_read_b32 v10, v10\n"
        "v_lshl_add_u32 v11, v11, 2, s67\n"
        "v_mfma_f32_16x16x32_bf16 v[112:115], a[120:123], a[248:251], 0\n"
        "ds_read_b128 a[248:251], v151 offset:1664\n"
        "ds_read_b32 v11, v11\n"
        "s_waitcnt lgkmcnt(15)\n"
        "v_lshl_add_u32 v24, v24, 2, s67\n"
        "v_mfma_f32_16x16x32_bf16 v[112:115], a[124:127], a[216:219], v[112:115]\n"
        "ds_read_b128 a[216:219], v150 offset:1792\n"
        "ds_read_b128 v[28:31], v235 offset:1024\n"
        "v_cvt_pk_bf16_f32 v154, v100, v101\n"
        "v_mfma_f32_16x16x32_bf16 v[112:115], a[128:131], a[220:223], v[112:115]\n"
        "ds_read_b128 a[220:223], v151 offset:1792\n"
        "ds_read_b32 v24, v24\n"
        "v_cvt_pk_bf16_f32 v155, v102, v103\n"
        "v_mfma_f32_16x16x32_bf16 v[112:115], a[132:135], a[224:227], v[112:115]\n"
        "buffer_load_dwordx4 v130, s[20:23], s64 offen offset:2048 lds\n"
        "ds_read_b128 a[224:227], v150 offset:12288\n"
        "v_lshl_add_u32 v25, v25, 2, s67\n"
        "v_mfma_f32_16x16x32_bf16 v[112:115], a[136:139], a[228:231], v[112:115]\n"
        "ds_read_b32 v25, v25\n"
        "ds_read_b128 v[44:47], v235 offset:8192\n"
        "v_lshl_add_u32 v26, v26, 2, s67\n"
        "v_mfma_f32_16x16x32_bf16 v[112:115], a[140:143], a[232:235], v[112:115]\n"
        "ds_read_b32 v26, v26\n"
        "ds_read_b128 a[228:231], v151 offset:12288\n"
        "v_lshl_add_u32 v27, v27, 2, s67\n"
        "v_mfma_f32_16x16x32_bf16 v[96:99], a[72:75], a[236:239], 0\n"
        "s_add_u32 m0, s40, 49152\n"
        "ds_read_b32 v27, v27\n"
        "ds_read_b128 a[232:235], v150 offset:12416\n"
        "v_lshl_add_u32 v40, v40, 2, s67\n"
        "v_mfma_f32_16x16x32_bf16 v[96:99], a[76:79], a[240:243], v[96:99]\n"
        "s_waitcnt lgkmcnt(15)\n"
        "s_add_u32 s64, s65, 37632\n"
        "ds_read_b32 v40, v40\n"
        "v_cvt_pk_bf16_f32 v168, v112, v113\n"
        "v_cvt_pk_bf16_f32 v169, v114, v115\n"
        "v_mfma_f32_16x16x32_bf16 v[96:99], a[80:83], a[244:247], v[96:99]\n"
        "s_waitcnt lgkmcnt(13)\n"
        "buffer_load_dwordx4 v128, s[20:23], s64 offen lds\n"
        "v_lshl_add_u32 v41, v41, 2, s67\n"
        "v_lshl_add_u32 v42, v42, 2, s67\n"
        "v_mfma_f32_16x16x32_bf16 v[96:99], a[84:87], a[248:251], v[96:99]\n"
        "s_waitcnt lgkmcnt(11)\n"
        "ds_read_b32 v41, v41\n"
        "ds_read_b32 v42, v42\n"
        "v_lshl_add_u32 v43, v43, 2, s67\n"
        "v_mfma_f32_16x16x32_bf16 v[96:99], a[88:91], a[216:219], v[96:99]\n"
        "s_waitcnt lgkmcnt(11)\n"
        "ds_read_b32 v43, v43\n"
        "ds_read_b128 v[60:63], v235 offset:9216\n"
        "v_lshl_add_u32 v56, v56, 2, s67\n"
        "v_mfma_f32_16x16x32_bf16 v[96:99], a[92:95], a[220:223], v[96:99]\n"
        "ds_read_b32 v56, v56\n"
        "v_lshl_add_u32 v57, v57, 2, s67\n"
        "v_lshl_add_u32 v58, v58, 2, s67\n"
        "v_mfma_f32_16x16x32_bf16 v[100:103], a[96:99], a[236:239], 0\n"
        "ds_read_b32 v57, v57\n"
        "ds_read_b32 v58, v58\n"
        "v_lshl_add_u32 v59, v59, 2, s67\n"
        "v_mfma_f32_16x16x32_bf16 v[100:103], a[100:103], a[240:243], v[100:103]\n"
        "buffer_load_dwordx4 v129, s[20:23], s64 offen offset:1024 lds\n"
        "ds_read_b32 v59, v59\n"
        "v_cvt_pk_bf16_f32 v172, v96, v97\n"
        "v_mfma_f32_16x16x32_bf16 v[100:103], a[104:107], a[244:247], v[100:103]\n"
        "v_cvt_pk_bf16_f32 v173, v98, v99\n"
        "v_lshl_add_u32 v12, v12, 2, s67\n"
        "v_lshl_add_u32 v13, v13, 2, s67\n"
        "v_mfma_f32_16x16x32_bf16 v[100:103], a[108:111], a[248:251], v[100:103]\n"
        "ds_read_b32 v12, v12\n"
        "ds_read_b32 v13, v13\n"
        "v_lshl_add_u32 v14, v14, 2, s67\n"
        "v_mfma_f32_16x16x32_bf16 v[100:103], a[112:115], a[216:219], v[100:103]\n"
        "ds_read_b32 v14, v14\n"
        "v_lshl_add_u32 v15, v15, 2, s67\n"
        "v_lshl_add_u32 v28, v28, 2, s67\n"
        "v_mfma_f32_16x16x32_bf16 v[100:103], a[116:119], a[220:223], v[100:103]\n"
        "ds_read_b32 v15, v15\n"
        "ds_read_b32 v28, v28\n"
        "v_lshl_add_u32 v29, v29, 2, s67\n"
        "v_mfma_f32_16x16x32_bf16 v[112:115], a[120:123], a[236:239], 0\n"
        "ds_read_b128 a[236:239], v151 offset:12416\n"
        "buffer_load_dwordx4 v130, s[20:23], s64 offen offset:2048 lds\n"
        "v_lshl_add_u32 v30, v30, 2, s67\n"
        "v_mfma_f32_16x16x32_bf16 v[112:115], a[124:127], a[240:243], v[112:115]\n"
        "ds_read_b128 a[240:243], v150 offset:12544\n"
        "ds_read_b32 v29, v29\n"
        "v_cvt_pk_bf16_f32 v174, v100, v101\n"
        "v_mfma_f32_16x16x32_bf16 v[112:115], a[128:131], a[244:247], v[112:115]\n"
        "ds_read_b128 a[244:247], v151 offset:12544\n"
        "ds_read_b32 v30, v30\n"
        "v_cvt_pk_bf16_f32 v175, v102, v103\n"
        "v_mfma_f32_16x16x32_bf16 v[112:115], a[132:135], a[248:251], v[112:115]\n"
        "s_add_u32 m0, s40, 61440\n"
        "ds_read_b128 a[248:251], v150 offset:13824\n"
        "v_lshl_add_u32 v31, v31, 2, s67\n"
        "s_waitcnt lgkmcnt(15)\n"
        "v_lshl_add_u32 v44, v44, 2, s67\n"
        "v_mfma_f32_16x16x32_bf16 v[112:115], a[136:139], a[216:219], v[112:115]\n"
        "s_add_u32 s64, s65, 74496\n"
        "ds_read_b32 v31, v31\n"
        "ds_read_b128 a[216:219], v151 offset:13824\n"
        "v_lshl_add_u32 v45, v45, 2, s67\n"
        "v_mfma_f32_16x16x32_bf16 v[112:115], a[140:143], a[220:223], v[112:115]\n"
        "buffer_load_dwordx4 v128, s[20:23], s64 offen lds\n"
        "ds_read_b128 a[220:223], v150 offset:13952\n"
        "v_lshl_add_u32 v46, v46, 2, s67\n"
        "v_mfma_f32_16x16x32_bf16 v[96:99], a[72:75], a[224:227], 0\n"
        "ds_read_b32 v44, v44\n"
        "ds_read_b32 v45, v45\n"
        "v_lshl_add_u32 v47, v47, 2, s67\n"
        "v_mfma_f32_16x16x32_bf16 v[96:99], a[76:79], a[228:231], v[96:99]\n"
        "ds_read_b32 v46, v46\n"
        "ds_read_b32 v47, v47\n"
        "v_cvt_pk_bf16_f32 v190, v112, v113\n"
        "v_mfma_f32_16x16x32_bf16 v[96:99], a[80:83], a[232:235], v[96:99]\n"
        "s_waitcnt lgkmcnt(12)\n"
        "v_cvt_pk_bf16_f32 v191, v114, v115\n"
        "v_lshl_add_u32 v60, v60, 2, s67\n"
        "v_lshl_add_u32 v61, v61, 2, s67\n"
        "v_mfma_f32_16x16x32_bf16 v[96:99], a[84:87], a[236:239], v[96:99]\n"
        "s_waitcnt lgkmcnt(11)\n"
        "ds_read_b32 v60, v60\n"
        "ds_read_b32 v61, v61\n"
        "v_lshl_add_u32 v62, v62, 2, s67\n"
        "v_mfma_f32_16x16x32_bf16 v[96:99], a[88:91], a[240:243], v[96:99]\n"
        "s_waitcnt lgkmcnt(11)\n"
        "buffer_load_dwordx4 v129, s[20:23], s64 offen offset:1024 lds\n"
        "ds_read_b32 v62, v62\n"
        "v_lshl_add_u32 v63, v63, 2, s67\n"
        "v_mfma_f32_16x16x32_bf16 v[96:99], a[92:95], a[244:247], v[96:99]\n"
        "ds_read_b32 v63, v63\n"
        "v_add_u32 v240, s31, v240\n"
        "v_add_u32 v241, s31, v241\n"
        "v_mfma_f32_16x16x32_bf16 v[100:103], a[96:99], a[224:227], 0\n"
        "v_add_u32 v245, s45, v245\n"
        "v_add_u32 v246, s45, v246\n"
        "v_add_u32 v247, s45, v247\n"
        "v_mfma_f32_16x16x32_bf16 v[100:103], a[100:103], a[228:231], v[100:103]\n"
        "v_cvt_pk_bf16_f32 v192, v96, v97\n"
        "v_cvt_pk_bf16_f32 v193, v98, v99\n"
        "v_mfma_f32_16x16x32_bf16 v[100:103], a[104:107], a[232:235], v[100:103]\n"
        "v_mfma_f32_16x16x32_bf16 v[100:103], a[108:111], a[236:239], v[100:103]\n"
        "buffer_load_dwordx4 v130, s[20:23], s64 offen offset:2048 lds\n"
        "v_mfma_f32_16x16x32_bf16 v[100:103], a[112:115], a[240:243], v[100:103]\n"
        "v_mfma_f32_16x16x32_bf16 v[100:103], a[116:119], a[244:247], v[100:103]\n"
        "v_mfma_f32_16x16x32_bf16 v[112:115], a[120:123], a[224:227], 0\n"
        "ds_read_b128 a[224:227], v151 offset:13952\n"
        "v_mfma_f32_16x16x32_bf16 v[112:115], a[124:127], a[228:231], v[112:115]\n"
        "ds_read_b128 a[228:231], v150 offset:14080\n"
        "v_mfma_f32_16x16x32_bf16 v[112:115], a[128:131], a[232:235], v[112:115]\n"
        "ds_read_b128 a[232:235], v151 offset:14080\n"
        "v_mfma_f32_16x16x32_bf16 v[112:115], a[132:135], a[236:239], v[112:115]\n"
        "v_mfma_f32_16x16x32_bf16 v[112:115], a[136:139], a[240:243], v[112:115]\n"
        "v_cvt_pk_bf16_f32 v194, v100, v101\n"
        "v_cvt_pk_bf16_f32 v195, v102, v103\n"
        "v_mfma_f32_16x16x32_bf16 v[112:115], a[140:143], a[244:247], v[112:115]\n"
        "s_waitcnt lgkmcnt(14)\n"
        "s_waitcnt vmcnt(15)\n"
        "s_barrier\n"
        "ds_read_b128 v[162:165], v251 offset:0\n"
        "ds_read_b128 v[182:185], v251 offset:512\n"
        "v_mfma_f32_16x16x32_bf16 v[96:99], a[72:75], a[248:251], 0\n"
        "s_waitcnt lgkmcnt(14)\n"
        "s_add_u32 m0, s40, 0\n"
        "v_mfma_f32_16x16x32_bf16 v[96:99], a[76:79], a[216:219], v[96:99]\n"
        "s_waitcnt lgkmcnt(13)\n"
        "s_add_u32 s64, s65, 111360\n"
        "v_mfma_f32_16x16x32_bf16 v[96:99], a[80:83], a[220:223], v[96:99]\n"
        "s_waitcnt lgkmcnt(4)\n"
        "buffer_load_dwordx4 v128, s[20:23], s64 offen lds\n"
        "ds_read_b128 v[202:205], v251 offset:4096\n"
        "v_cvt_pk_bf16_f32 v208, v112, v113\n"
        "v_mfma_f32_16x16x32_bf16 v[96:99], a[84:87], a[224:227], v[96:99]\n"
        "s_waitcnt lgkmcnt(4)\n"
        "v_cvt_pk_bf16_f32 v209, v114, v115\n"
        "v_mfma_f32_16x16x32_bf16 v[96:99], a[88:91], a[228:231], v[96:99]\n"
        "s_waitcnt lgkmcnt(3)\n"
        "v_mfma_f32_16x16x32_bf16 v[96:99], a[92:95], a[232:235], v[96:99]\n"
        "v_mfma_f32_16x16x32_bf16 v[100:103], a[96:99], a[248:251], 0\n"
        "ds_read_b128 v[222:225], v251 offset:4608\n"
        "v_mfma_f32_16x16x32_bf16 v[100:103], a[100:103], a[216:219], v[100:103]\n"
        "buffer_load_dwordx4 v129, s[20:23], s64 offen offset:1024 lds\n"
        "v_mfma_f32_16x16x32_bf16 v[100:103], a[104:107], a[220:223], v[100:103]\n"
        "s_waitcnt lgkmcnt(3)\n"
        "v_pk_min_u16 v162, v162, s26\n"
        "v_pk_min_u16 v163, v163, s26\n"
        "v_pk_min_u16 v164, v164, s26\n"
        "v_mfma_f32_16x16x32_bf16 v[100:103], a[108:111], a[224:227], v[100:103]\n"
        "v_cvt_pk_bf16_f32 v212, v96, v97\n"
        "v_cvt_pk_bf16_f32 v213, v98, v99\n"
        "v_pk_min_u16 v165, v165, s26\n"
        "v_mfma_f32_16x16x32_bf16 v[100:103], a[112:115], a[228:231], v[100:103]\n"
        "v_mfma_f32_16x16x32_bf16 v[100:103], a[116:119], a[232:235], v[100:103]\n"
        "v_mfma_f32_16x16x32_bf16 v[112:115], a[120:123], a[248:251], 0\n"
        "buffer_load_dwordx4 v130, s[20:23], s64 offen offset:2048 lds\n"
        "s_waitcnt lgkmcnt(2)\n"
        "v_pk_min_u16 v182, v182, s26\n"
        "v_pk_min_u16 v183, v183, s26\n"
        "v_mfma_f32_16x16x32_bf16 v[112:115], a[124:127], a[216:219], v[112:115]\n"
        "v_pk_min_u16 v184, v184, s26\n"
        "v_pk_min_u16 v185, v185, s26\n"
        "v_mfma_f32_16x16x32_bf16 v[112:115], a[128:131], a[220:223], v[112:115]\n"
        "v_cvt_pk_bf16_f32 v214, v100, v101\n"
        "v_cvt_pk_bf16_f32 v215, v102, v103\n"
        "v_mfma_f32_16x16x32_bf16 v[112:115], a[132:135], a[224:227], v[112:115]\n"
        "s_add_u32 m0, s40, 12288\n"
        "v_mfma_f32_16x16x32_bf16 v[112:115], a[136:139], a[228:231], v[112:115]\n"
        "s_add_u32 s64, s65, 148224\n"
        "s_waitcnt lgkmcnt(1)\n"
        "v_pk_min_u16 v202, v202, s26\n"
        "v_pk_min_u16 v203, v203, s26\n"
        "v_pk_min_u16 v204, v204, s26\n"
        "v_mfma_f32_16x16x32_bf16 v[112:115], a[140:143], a[232:235], v[112:115]\n"
        "buffer_load_dwordx4 v128, s[20:23], s64 offen lds\n"
        "v_pk_min_u16 v205, v205, s26\n"
        "v_mfma_f32_16x16x32_bf16 v[64:67], v[152:155], v[156:159], v[0:3]\n"
        "v_mfma_f32_16x16x32_bf16 v[64:67], v[168:171], v[160:163], v[64:67]\n"
        "v_mfma_f32_16x16x32_bf16 v[68:71], v[172:175], v[156:159], v[4:7]\n"
        "s_waitcnt lgkmcnt(0)\n"
        "v_pk_min_u16 v222, v222, s26\n"
        "v_pk_min_u16 v223, v223, s26\n"
        "v_pk_min_u16 v224, v224, s26\n"
        "v_mfma_f32_16x16x32_bf16 v[68:71], v[188:191], v[164:167], v[68:71]\n"
        "ds_read_b128 v[162:165], v252 offset:0\n"
        "v_cvt_pk_bf16_f32 v230, v112, v113\n"
        "v_cvt_pk_bf16_f32 v231, v114, v115\n"
        "v_mfma_f32_16x16x32_bf16 v[80:83], v[152:155], v[176:179], v[16:19]\n"
        "buffer_load_dwordx4 v129, s[20:23], s64 offen offset:1024 lds\n"
        "v_pk_min_u16 v225, v225, s26\n"
        "v_max3_f32 v232, v64, v65, v66\n"
        "v_mfma_f32_16x16x32_bf16 v[80:83], v[168:171], v[180:183], v[80:83]\n"
        "v_max3_f32 v232, v232, v67, v68\n"
        "v_max3_f32 v232, v232, v69, v70\n"
        "v_mfma_f32_16x16x32_bf16 v[84:87], v[172:175], v[176:179], v[20:23]\n"
        "v_mfma_f32_16x16x32_bf16 v[84:87], v[188:191], v[184:187], v[84:87]\n"
        "ds_read_b128 v[182:185], v252 offset:512\n"
        "v_mfma_f32_16x16x32_bf16 v[96:99], v[152:155], v[196:199], v[32:35]\n"
        "v_mfma_f32_16x16x32_bf16 v[96:99], v[168:171], v[200:203], v[96:99]\n"
        "buffer_load_dwordx4 v130, s[20:23], s64 offen offset:2048 lds\n"
        "v_max3_f32 v233, v80, v81, v82\n"
        "v_mfma_f32_16x16x32_bf16 v[100:103], v[172:175], v[196:199], v[36:39]\n"
        "v_mfma_f32_16x16x32_bf16 v[100:103], v[188:191], v[204:207], v[100:103]\n"
        "ds_read_b128 v[202:205], v252 offset:4096\n"
        "s_waitcnt vmcnt(15)\n"
        "s_barrier\n"
        "v_max3_f32 v233, v233, v83, v84\n"
        "v_max3_f32 v233, v233, v85, v86\n"
        "v_mfma_f32_16x16x32_bf16 v[112:115], v[152:155], v[216:219], v[48:51]\n"
        "s_add_u32 m0, s40, 24576\n"
        "ds_read_b128 a[236:239], v240 offset:0\n"
        "s_waitcnt lgkmcnt(3)\n"
        "v_pk_min_u16 v162, v162, s26\n"
        "v_pk_min_u16 v163, v163, s26\n"
        "v_mfma_f32_16x16x32_bf16 v[112:115], v[168:171], v[220:223], v[112:115]\n"
        "s_add_u32 s64, s65, 185088\n"
        "ds_read_b128 a[240:243], v241 offset:0\n"
        "v_pk_min_u16 v164, v164, s26\n"
        "v_pk_min_u16 v165, v165, s26\n"
        "v_mfma_f32_16x16x32_bf16 v[116:119], v[172:175], v[216:219], v[52:55]\n"
        "buffer_load_dwordx4 v128, s[20:23], s64 offen lds\n"
        "ds_read_b128 a[244:247], v240 offset:128\n"
        "v_max3_f32 v234, v96, v97, v98\n"
        "v_mfma_f32_16x16x32_bf16 v[116:119], v[188:191], v[224:227], v[116:119]\n"
        "ds_read_b128 v[222:225], v252 offset:4608\n"
        "ds_read_b128 a[248:251], v241 offset:128\n"
        "v_max3_f32 v234, v234, v99, v100\n"
        "v_mfma_f32_16x16x32_bf16 v[72:75], v[192:195], v[156:159], v[8:11]\n"
        "ds_read_b128 a[216:219], v240 offset:256\n"
        "s_waitcnt lgkmcnt(7)\n"
        "v_pk_min_u16 v182, v182, s26\n"
        "v_pk_min_u16 v183, v183, s26\n"
        "v_mfma_f32_16x16x32_bf16 v[72:75], v[208:211], v[160:163], v[72:75]\n"
        "ds_read_b128 a[220:223], v241 offset:256\n"
        "v_pk_min_u16 v184, v184, s26\n"
        "v_pk_min_u16 v185, v185, s26\n"
        "v_mfma_f32_16x16x32_bf16 v[76:79], v[212:215], v[156:159], v[12:15]\n"
        "v_max3_f32 v234, v234, v101, v102\n"
        "v_max3_f32 v235, v112, v113, v114\n"
        "v_max3_f32 v235, v235, v115, v116\n"
        "v_mfma_f32_16x16x32_bf16 v[76:79], v[228:231], v[164:167], v[76:79]\n"
        "buffer_load_dwordx4 v129, s[20:23], s64 offen offset:1024 lds\n"
        "v_max3_f32 v232, v232, v71, v72\n"
        "v_max3_f32 v232, v232, v73, v74\n"
        "v_mfma_f32_16x16x32_bf16 v[88:91], v[192:195], v[176:179], v[24:27]\n"
        "s_waitcnt lgkmcnt(7)\n"
        "v_pk_min_u16 v202, v202, s26\n"
        "v_pk_min_u16 v203, v203, s26\n"
        "v_pk_min_u16 v204, v204, s26\n"
        "v_mfma_f32_16x16x32_bf16 v[88:91], v[208:211], v[180:183], v[88:91]\n"
        "v_pk_min_u16 v205, v205, s26\n"
        "v_max3_f32 v232, v232, v75, v76\n"
        "v_max3_f32 v232, v232, v77, v78\n"
        "v_mfma_f32_16x16x32_bf16 v[92:95], v[212:215], v[176:179], v[28:31]\n"
        "v_max_f32 v232, v232, v79\n"
        "v_max3_f32 v235, v235, v117, v118\n"
        "v_xor_b32 v251, 0x2000, v251\n"
        "v_mfma_f32_16x16x32_bf16 v[92:95], v[228:231], v[184:187], v[92:95]\n"
        "v_max3_f32 v233, v233, v87, v88\n"
        "v_max3_f32 v233, v233, v89, v90\n"
        "v_xor_b32 v252, 0x2000, v252\n"
        "v_mfma_f32_16x16x32_bf16 v[104:107], v[192:195], v[196:199], v[40:43]\n"
        "buffer_load_dwordx4 v130, s[20:23], s64 offen offset:2048 lds\n"
        "s_waitcnt lgkmcnt(3)\n"
        "v_pk_min_u16 v222, v222, s26\n"
        "v_pk_min_u16 v223, v223, s26\n"
        "v_mfma_f32_16x16x32_bf16 v[104:107], v[208:211], v[200:203], v[104:107]\n"
        "v_pk_min_u16 v224, v224, s26\n"
        "v_pk_min_u16 v225, v225, s26\n"
        "v_max3_f32 v233, v233, v91, v92\n"
        "v_mfma_f32_16x16x32_bf16 v[108:111], v[212:215], v[196:199], v[44:47]\n"
        "s_waitcnt vmcnt(9)\n"
        "s_barrier\n"
        "v_max3_f32 v233, v233, v93, v94\n"
        "v_max_f32 v233, v233, v95\n"
        "v_mfma_f32_16x16x32_bf16 v[108:111], v[228:231], v[204:207], v[108:111]\n"
        "ds_read_b64_tr_b16 a[144:145], v131 offset:36864\n"
        "ds_read_b64_tr_b16 a[146:147], v131 offset:38400\n"
        "v_max3_f32 v234, v234, v103, v104\n"
        "v_mfma_f32_16x16x32_bf16 v[120:123], v[192:195], v[216:219], v[56:59]\n"
        "s_mov_b32 m0, s48\n"
        "s_xor_b32 s48, s48, 8192\n"
        "ds_read_b64_tr_b16 a[148:149], v131 offset:49152\n"
        "ds_read_b64_tr_b16 a[150:151], v131 offset:50688\n"
        "v_mfma_f32_16x16x32_bf16 v[120:123], v[208:211], v[220:223], v[120:123]\n"
        "buffer_load_dwordx4 v249, s[12:15], s36 offen offset:0 lds\n"
        "ds_read_b64_tr_b16 a[152:153], v131 offset:61440\n"
        "v_mfma_f32_16x16x32_bf16 v[124:127], v[212:215], v[216:219], v[60:63]\n"
        "ds_read_b64_tr_b16 a[154:155], v131 offset:62976\n"
        "ds_read_b64_tr_b16 a[168:169], v132 offset:36864\n"
        "v_xor_b32 v253, 32, v249\n"
        "v_mfma_f32_16x16x32_bf16 v[124:127], v[228:231], v[224:227], v[124:127]\n"
        "ds_read_b64_tr_b16 a[170:171], v132 offset:38400\n"
        "ds_read_b64_tr_b16 a[172:173], v132 offset:49152\n"
        "v_max3_f32 v234, v234, v105, v106\n"
        "v_mfma_f32_16x16x32_bf16 v[212:215], a[72:75], a[236:239], 0\n"
        "ds_read_b64_tr_b16 a[174:175], v132 offset:50688\n"
        "ds_read_b64_tr_b16 a[176:177], v132 offset:61440\n"
        "v_max3_f32 v234, v234, v107, v108\n"
        "v_mfma_f32_16x16x32_bf16 v[212:215], a[76:79], a[240:243], v[212:215]\n"
        "ds_read_b64_tr_b16 a[178:179], v132 offset:62976\n"
        "ds_read_b64_tr_b16 a[192:193], v133 offset:36864\n"
        "v_max3_f32 v234, v234, v109, v110\n"
        "v_mfma_f32_16x16x32_bf16 v[212:215], a[80:83], a[244:247], v[212:215]\n"
        "s_waitcnt lgkmcnt(15)\n"
        "buffer_load_dwordx4 v253, s[12:15], s36 offen offset:1024 lds\n"
        "s_add_u32 s36, s36, 2097152\n"
        "ds_read_b64_tr_b16 a[194:195], v133 offset:38400\n"
        "v_mfma_f32_16x16x32_bf16 v[212:215], a[84:87], a[248:251], v[212:215]\n"
        "s_waitcnt lgkmcnt(15)\n"
        "ds_read_b64_tr_b16 a[196:197], v133 offset:49152\n"
        "ds_read_b64_tr_b16 a[198:199], v133 offset:50688\n"
        "v_max_f32 v234, v234, v111\n"
        "v_mfma_f32_16x16x32_bf16 v[212:215], a[88:91], a[216:219], v[212:215]\n"
        "s_waitcnt lgkmcnt(15)\n"
        "ds_read_b64_tr_b16 a[200:201], v133 offset:61440\n"
        "ds_read_b64_tr_b16 a[202:203], v133 offset:62976\n"
        "v_max3_f32 v235, v235, v119, v120\n"
        "v_mfma_f32_16x16x32_bf16 v[212:215], a[92:95], a[220:223], v[212:215]\n"
        "v_permlane32_swap_b32 v232, v233\n"
        "v_max_f32 v232, v232, v233\n"
        "v_mfma_f32_16x16x32_bf16 v[216:219], a[96:99], a[236:239], 0\n"
        "v_mov_b32 v233, v232\n"
        "v_max3_f32 v235, v235, v121, v122\n"
        "v_max3_f32 v235, v235, v123, v124\n"
        "v_mfma_f32_16x16x32_bf16 v[216:219], a[100:103], a[240:243], v[216:219]\n"
        "v_permlane16_swap_b32 v232, v233\n"
        "v_max_f32 v232, v232, v233\n"
        "v_mfma_f32_16x16x32_bf16 v[216:219], a[104:107], a[244:247], v[216:219]\n"
        "v_mul_f32 v236, s25, v232\n"
        "v_mul_f32 v237, s25, v232\n"
        "v_max3_f32 v235, v235, v125, v126\n"
        "v_mfma_f32_16x16x32_bf16 v[216:219], a[108:111], a[248:251], v[216:219]\n"
        "v_permlane32_swap_b32 v236, v237\n"
        "v_max_f32 v235, v235, v127\n"
        "v_mfma_f32_16x16x32_bf16 v[216:219], a[112:115], a[216:219], v[216:219]\n"
        "v_cvt_pk_bf16_f32 v152, v212, v213\n"
        "v_permlane32_swap_b32 v234, v235\n"
        "v_mfma_f32_16x16x32_bf16 v[216:219], a[116:119], a[220:223], v[216:219]\n"
        "v_max_f32 v234, v234, v235\n"
        "v_mov_b32 v235, v234\n"
        "v_cvt_pk_bf16_f32 v153, v214, v215\n"
        "v_mfma_f32_16x16x32_bf16 v[162:165], a[120:123], a[236:239], 0\n"
        "v_permlane16_swap_b32 v234, v235\n"
        "v_max_f32 v234, v234, v235\n"
        "v_mfma_f32_16x16x32_bf16 v[162:165], a[124:127], a[240:243], v[162:165]\n"
        "v_mul_f32 v238, s25, v234\n"
        "v_mul_f32 v239, s25, v234\n"
        "v_cvt_pk_bf16_f32 v154, v216, v217\n"
        "v_mfma_f32_16x16x32_bf16 v[162:165], a[128:131], a[244:247], v[162:165]\n"
        "ds_read_b128 a[224:227], v240 offset:1536\n"
        "v_permlane32_swap_b32 v238, v239\n"
        "v_mfma_f32_16x16x32_bf16 v[162:165], a[132:135], a[248:251], v[162:165]\n"
        "ds_read_b128 a[228:231], v241 offset:1536\n"
        "v_cvt_pk_bf16_f32 v155, v218, v219\n"
        "v_fma_f32 v64, v64, s24, v236\n"
        "v_mfma_f32_16x16x32_bf16 v[162:165], a[136:139], a[216:219], v[162:165]\n"
        "ds_read_b128 a[232:235], v240 offset:1664\n"
        "v_exp_f32 v64, v64\n"
        "v_mfma_f32_16x16x32_bf16 v[162:165], a[140:143], a[220:223], v[162:165]\n"
        "v_fma_f32 v65, v65, s24, v236\n"
        "v_exp_f32 v65, v65\n"
        "v_mfma_f32_16x16x32_bf16 v[212:215], a[0:3], a[236:239], 0\n"
        "v_cvt_pk_bf16_f32 v64, v64, v65\n"
        "v_fma_f32 v66, v66, s24, v236\n"
        "v_fma_f32 v67, v67, s24, v236\n"
        "v_mfma_f32_16x16x32_bf16 v[212:215], a[4:7], a[240:243], v[212:215]\n"
        "v_exp_f32 v66, v66\n"
        "v_cvt_pk_bf16_f32 v168, v162, v163\n"
        "v_mfma_f32_16x16x32_bf16 v[212:215], a[8:11], a[244:247], v[212:215]\n"
        "v_exp_f32 v67, v67\n"
        "v_cvt_pk_bf16_f32 v169, v164, v165\n"
        "v_mfma_f32_16x16x32_bf16 v[212:215], a[12:15], a[248:251], v[212:215]\n"
        "v_cvt_pk_bf16_f32 v65, v66, v67\n"
        "v_fma_f32 v68, v68, s24, v236\n"
        "v_fma_f32 v69, v69, s24, v236\n"
        "v_mfma_f32_16x16x32_bf16 v[212:215], a[16:19], a[216:219], v[212:215]\n"
        "v_exp_f32 v68, v68\n"
        "v_fma_f32 v70, v70, s24, v236\n"
        "v_mfma_f32_16x16x32_bf16 v[212:215], a[20:23], a[220:223], v[212:215]\n"
        "v_exp_f32 v69, v69\n"
        "v_fma_f32 v71, v71, s24, v236\n"
        "v_mfma_f32_16x16x32_bf16 v[216:219], a[24:27], a[236:239], 0\n"
        "s_waitcnt lgkmcnt(3)\n"
        "s_waitcnt vmcnt(2)\n"
        "s_barrier\n"
        "v_exp_f32 v70, v70\n"
        "v_cvt_pk_bf16_f32 v66, v68, v69\n"
        "v_mfma_f32_16x16x32_bf16 v[216:219], a[28:31], a[240:243], v[216:219]\n"
        "ds_read_b64_tr_b16 a[156:157], v131 offset:0\n"
        "s_add_u32 m0, s65, 36864\n"
        "ds_read_b64_tr_b16 a[158:159], v131 offset:1536\n"
        "v_cvt_pk_bf16_f32 v156, v212, v213\n"
        "v_mfma_f32_16x16x32_bf16 v[216:219], a[32:35], a[244:247], v[216:219]\n"
        "s_add_u32 s64, s65, 36864\n"
        "ds_read_b64_tr_b16 a[160:161], v131 offset:12288\n"
        "ds_read_b64_tr_b16 a[162:163], v131 offset:13824\n"
        "v_cvt_pk_bf16_f32 v157, v214, v215\n"
        "v_mfma_f32_16x16x32_bf16 v[216:219], a[36:39], a[248:251], v[216:219]\n"
        "buffer_load_dwordx4 v142, s[52:55], s64 offen lds\n"
        "ds_read_b64_tr_b16 a[164:165], v131 offset:24576\n"
        "v_fma_f32 v72, v72, s24, v236\n"
        "v_mfma_f32_16x16x32_bf16 v[216:219], a[40:43], a[216:219], v[216:219]\n"
        "ds_read_b64_tr_b16 a[166:167], v131 offset:26112\n"
        "ds_read_b64_tr_b16 a[180:181], v132 offset:0\n"
        "v_fma_f32 v73, v73, s24, v236\n"
        "v_mfma_f32_16x16x32_bf16 v[216:219], a[44:47], a[220:223], v[216:219]\n"
        "ds_read_b64_tr_b16 a[182:183], v132 offset:1536\n"
        "ds_read_b64_tr_b16 a[184:185], v132 offset:12288\n"
        "v_fma_f32 v74, v74, s24, v236\n"
        "v_mfma_f32_16x16x32_bf16 v[162:165], a[48:51], a[236:239], 0\n"
        "ds_read_b128 a[236:239], v241 offset:1664\n"
        "ds_read_b64_tr_b16 a[186:187], v132 offset:13824\n"
        "v_fma_f32 v75, v75, s24, v236\n"
        "v_mfma_f32_16x16x32_bf16 v[162:165], a[52:55], a[240:243], v[162:165]\n"
        "ds_read_b128 a[240:243], v240 offset:1792\n"
        "ds_read_b64_tr_b16 a[188:189], v132 offset:24576\n"
        "v_cvt_pk_bf16_f32 v158, v216, v217\n"
        "v_mfma_f32_16x16x32_bf16 v[162:165], a[56:59], a[244:247], v[162:165]\n"
        "ds_read_b128 a[244:247], v241 offset:1792\n"
        "buffer_load_dwordx4 v142, s[52:55], s64 offen offset:1024 lds\n"
        "v_cvt_pk_bf16_f32 v159, v218, v219\n"
        "v_mfma_f32_16x16x32_bf16 v[162:165], a[60:63], a[248:251], v[162:165]\n"
        "ds_read_b64_tr_b16 a[190:191], v132 offset:26112\n"
        "ds_read_b64_tr_b16 a[204:205], v133 offset:0\n"
        "v_fma_f32 v76, v76, s24, v236\n"
        "v_mfma_f32_16x16x32_bf16 v[162:165], a[64:67], a[216:219], v[162:165]\n"
        "ds_read_b64_tr_b16 a[206:207], v133 offset:1536\n"
        "ds_read_b64_tr_b16 a[208:209], v133 offset:12288\n"
        "v_fma_f32 v77, v77, s24, v236\n"
        "v_mfma_f32_16x16x32_bf16 v[162:165], a[68:71], a[220:223], v[162:165]\n"
        "s_waitcnt lgkmcnt(15)\n"
        "ds_read_b64_tr_b16 a[210:211], v133 offset:13824\n"
        "ds_read_b64_tr_b16 a[212:213], v133 offset:24576\n"
        "v_fma_f32 v78, v78, s24, v236\n"
        "v_mfma_f32_16x16x32_bf16 v[212:215], a[72:75], a[224:227], 0\n"
        "s_waitcnt lgkmcnt(15)\n"
        "ds_read_b64_tr_b16 a[214:215], v133 offset:26112\n"
        "v_exp_f32 v71, v71\n"
        "v_mfma_f32_16x16x32_bf16 v[212:215], a[76:79], a[228:231], v[212:215]\n"
        "s_waitcnt lgkmcnt(15)\n"
        "buffer_load_dwordx4 v142, s[52:55], s64 offen offset:2048 lds\n"
        "v_exp_f32 v72, v72\n"
        "v_mfma_f32_16x16x32_bf16 v[212:215], a[80:83], a[232:235], v[212:215]\n"
        "s_waitcnt lgkmcnt(11)\n"
        "v_exp_f32 v73, v73\n"
        "v_cvt_pk_bf16_f32 v160, v162, v163\n"
        "v_mfma_f32_16x16x32_bf16 v[212:215], a[84:87], a[236:239], v[212:215]\n"
        "s_waitcnt lgkmcnt(9)\n"
        "v_exp_f32 v74, v74\n"
        "v_cvt_pk_bf16_f32 v161, v164, v165\n"
        "v_mfma_f32_16x16x32_bf16 v[212:215], a[88:91], a[240:243], v[212:215]\n"
        "s_waitcnt lgkmcnt(7)\n"
        "s_add_u32 m0, s65, 39936\n"
        "v_cvt_pk_bf16_f32 v166, v162, v163\n"
        "v_cvt_pk_bf16_f32 v167, v164, v165\n"
        "v_cvt_pk_bf16_f32 v67, v70, v71\n"
        "v_mfma_f32_16x16x32_bf16 v[212:215], a[92:95], a[244:247], v[212:215]\n"
        "s_add_u32 s64, s65, 39936\n"
        "v_exp_f32 v75, v75\n"
        "v_cvt_pk_bf16_f32 v68, v72, v73\n"
        "v_mfma_f32_16x16x32_bf16 v[216:219], a[96:99], a[224:227], 0\n"
        "buffer_load_dwordx4 v142, s[52:55], s64 offen lds\n"
        "v_exp_f32 v76, v76\n"
        "v_mfma_f32_16x16x32_bf16 v[216:219], a[100:103], a[228:231], v[216:219]\n"
        "v_exp_f32 v77, v77\n"
        "v_cvt_pk_bf16_f32 v172, v212, v213\n"
        "v_mfma_f32_16x16x32_bf16 v[216:219], a[104:107], a[232:235], v[216:219]\n"
        "v_exp_f32 v78, v78\n"
        "v_cvt_pk_bf16_f32 v173, v214, v215\n"
        "v_mfma_f32_16x16x32_bf16 v[216:219], a[108:111], a[236:239], v[216:219]\n"
        "v_cvt_pk_bf16_f32 v69, v74, v75\n"
        "v_cvt_pk_bf16_f32 v70, v76, v77\n"
        "v_fma_f32 v79, v79, s24, v236\n"
        "v_mfma_f32_16x16x32_bf16 v[216:219], a[112:115], a[240:243], v[216:219]\n"
        "s_waitcnt lgkmcnt(0)\n"
        "s_barrier\n"
        "v_exp_f32 v79, v79\n"
        "v_fma_f32 v80, v80, s24, v237\n"
        "v_mfma_f32_16x16x32_bf16 v[216:219], a[116:119], a[244:247], v[216:219]\n"
        "buffer_load_dwordx4 v142, s[52:55], s64 offen offset:1024 lds\n"
        "v_exp_f32 v80, v80\n"
        "v_mfma_f32_16x16x32_bf16 v[162:165], a[120:123], a[224:227], 0\n"
        "v_cvt_pk_bf16_f32 v71, v78, v79\n"
        "v_fma_f32 v81, v81, s24, v237\n"
        "v_fma_f32 v82, v82, s24, v237\n"
        "v_mfma_f32_16x16x32_bf16 v[162:165], a[124:127], a[228:231], v[162:165]\n"
        "v_exp_f32 v81, v81\n"
        "v_cvt_pk_bf16_f32 v174, v216, v217\n"
        "v_mfma_f32_16x16x32_bf16 v[162:165], a[128:131], a[232:235], v[162:165]\n"
        "ds_read_b128 a[248:251], v240 offset:12288\n"
        "v_exp_f32 v82, v82\n"
        "v_mfma_f32_16x16x32_bf16 v[162:165], a[132:135], a[236:239], v[162:165]\n"
        "ds_read_b128 a[216:219], v241 offset:12288\n"
        "v_cvt_pk_bf16_f32 v175, v218, v219\n"
        "v_cvt_pk_bf16_f32 v80, v80, v81\n"
        "v_mfma_f32_16x16x32_bf16 v[162:165], a[136:139], a[240:243], v[162:165]\n"
        "buffer_load_dwordx4 v142, s[52:55], s64 offen offset:2048 lds\n"
        "ds_read_b128 a[220:223], v240 offset:12416\n"
        "v_fma_f32 v83, v83, s24, v237\n"
        "v_mfma_f32_16x16x32_bf16 v[162:165], a[140:143], a[244:247], v[162:165]\n"
        "v_exp_f32 v83, v83\n"
        "v_fma_f32 v84, v84, s24, v237\n"
        "v_mfma_f32_16x16x32_bf16 v[212:215], a[0:3], a[224:227], 0\n"
        "v_exp_f32 v84, v84\n"
        "v_cvt_pk_bf16_f32 v81, v82, v83\n"
        "v_mfma_f32_16x16x32_bf16 v[212:215], a[4:7], a[228:231], v[212:215]\n"
        "s_add_u32 m0, s65, 43008\n"
        "v_fma_f32 v85, v85, s24, v237\n"
        "v_exp_f32 v85, v85\n"
        "v_mfma_f32_16x16x32_bf16 v[212:215], a[8:11], a[232:235], v[212:215]\n"
        "s_add_u32 s64, s65, 43008\n"
        "v_cvt_pk_bf16_f32 v190, v162, v163\n"
        "v_cvt_pk_bf16_f32 v191, v164, v165\n"
        "v_cvt_pk_bf16_f32 v82, v84, v85\n"
        "v_mfma_f32_16x16x32_bf16 v[212:215], a[12:15], a[236:239], v[212:215]\n"
        "buffer_load_dwordx4 v142, s[52:55], s64 offen lds\n"
        "v_fma_f32 v86, v86, s24, v237\n"
        "v_fma_f32 v87, v87, s24, v237\n"
        "v_mfma_f32_16x16x32_bf16 v[212:215], a[16:19], a[240:243], v[212:215]\n"
        "v_exp_f32 v86, v86\n"
        "v_fma_f32 v88, v88, s24, v237\n"
        "v_mfma_f32_16x16x32_bf16 v[212:215], a[20:23], a[244:247], v[212:215]\n"
        "v_exp_f32 v87, v87\n"
        "v_fma_f32 v89, v89, s24, v237\n"
        "v_mfma_f32_16x16x32_bf16 v[216:219], a[24:27], a[224:227], 0\n"
        "v_exp_f32 v88, v88\n"
        "v_cvt_pk_bf16_f32 v83, v86, v87\n"
        "v_mfma_f32_16x16x32_bf16 v[216:219], a[28:31], a[228:231], v[216:219]\n"
        "v_exp_f32 v89, v89\n"
        "v_fma_f32 v90, v90, s24, v237\n"
        "v_mfma_f32_16x16x32_bf16 v[216:219], a[32:35], a[232:235], v[216:219]\n"
        "buffer_load_dwordx4 v142, s[52:55], s64 offen offset:1024 lds\n"
        "v_exp_f32 v90, v90\n"
        "v_mfma_f32_16x16x32_bf16 v[216:219], a[36:39], a[236:239], v[216:219]\n"
        "v_cvt_pk_bf16_f32 v176, v212, v213\n"
        "v_cvt_pk_bf16_f32 v177, v214, v215\n"
        "v_cvt_pk_bf16_f32 v84, v88, v89\n"
        "v_mfma_f32_16x16x32_bf16 v[216:219], a[40:43], a[240:243], v[216:219]\n"
        "v_fma_f32 v91, v91, s24, v237\n"
        "v_exp_f32 v91, v91\n"
        "v_mfma_f32_16x16x32_bf16 v[216:219], a[44:47], a[244:247], v[216:219]\n"
        "v_cvt_pk_bf16_f32 v85, v90, v91\n"
        "v_fma_f32 v92, v92, s24, v237\n"
        "v_fma_f32 v93, v93, s24, v237\n"
        "v_mfma_f32_16x16x32_bf16 v[162:165], a[48:51], a[224:227], 0\n"
        "ds_read_b128 a[224:227], v241 offset:12416\n"
        "v_exp_f32 v92, v92\n"
        "v_mfma_f32_16x16x32_bf16 v[162:165], a[52:55], a[228:231], v[162:165]\n"
        "ds_read_b128 a[228:231], v240 offset:12544\n"
        "buffer_load_dwordx4 v142, s[52:55], s64 offen offset:2048 lds\n"
        "v_cvt_pk_bf16_f32 v178, v216, v217\n"
        "v_mfma_f32_16x16x32_bf16 v[162:165], a[56:59], a[232:235], v[162:165]\n"
        "ds_read_b128 a[232:235], v241 offset:12544\n"
        "v_exp_f32 v93, v93\n"
        "v_mfma_f32_16x16x32_bf16 v[162:165], a[60:63], a[236:239], v[162:165]\n"
        "v_cvt_pk_bf16_f32 v179, v218, v219\n"
        "v_cvt_pk_bf16_f32 v86, v92, v93\n"
        "v_fma_f32 v94, v94, s24, v237\n"
        "v_mfma_f32_16x16x32_bf16 v[162:165], a[64:67], a[240:243], v[162:165]\n"
        "s_add_u32 m0, s65, 0\n"
        "v_exp_f32 v94, v94\n"
        "v_fma_f32 v95, v95, s24, v237\n"
        "v_mfma_f32_16x16x32_bf16 v[162:165], a[68:71], a[244:247], v[162:165]\n"
        "s_waitcnt lgkmcnt(5)\n"
        "s_add_u32 s64, s65, 0\n"
        "v_exp_f32 v95, v95\n"
        "v_fma_f32 v96, v96, s24, v238\n"
        "v_mfma_f32_16x16x32_bf16 v[72:75], a[72:75], a[248:251], 0\n"
        "s_waitcnt lgkmcnt(4)\n"
        "buffer_load_dwordx4 v142, s[52:55], s64 offen lds\n"
        "v_exp_f32 v96, v96\n"
        "v_mfma_f32_16x16x32_bf16 v[72:75], a[76:79], a[216:219], v[72:75]\n"
        "s_waitcnt lgkmcnt(3)\n"
        "v_cvt_pk_bf16_f32 v87, v94, v95\n"
        "v_cvt_pk_bf16_f32 v180, v162, v163\n"
        "v_cvt_pk_bf16_f32 v181, v164, v165\n"
        "v_mfma_f32_16x16x32_bf16 v[72:75], a[80:83], a[220:223], v[72:75]\n"
        "s_waitcnt lgkmcnt(2)\n"
        "v_cvt_pk_bf16_f32 v186, v162, v163\n"
        "v_cvt_pk_bf16_f32 v187, v164, v165\n"
        "v_fma_f32 v97, v97, s24, v238\n"
        "v_mfma_f32_16x16x32_bf16 v[72:75], a[84:87], a[224:227], v[72:75]\n"
        "s_waitcnt lgkmcnt(1)\n"
        "v_exp_f32 v97, v97\n"
        "v_fma_f32 v98, v98, s24, v238\n"
        "v_mfma_f32_16x16x32_bf16 v[72:75], a[88:91], a[228:231], v[72:75]\n"
        "s_waitcnt lgkmcnt(0)\n"
        "v_exp_f32 v98, v98\n"
        "v_accvgpr_read_b32 v232, a252\n"
        "v_mfma_f32_16x16x32_bf16 v[72:75], a[92:95], a[232:235], v[72:75]\n"
        "buffer_load_dwordx4 v142, s[52:55], s64 offen offset:1024 lds\n"
        "v_accvgpr_read_b32 v233, a253\n"
        "v_accvgpr_read_b32 v234, a254\n"
        "v_mfma_f32_16x16x32_bf16 v[76:79], a[96:99], a[248:251], 0\n"
        "v_mov_b32 v235, 0x3f803f80\n"
        "v_accvgpr_write_b32 a252, v235\n"
        "v_accvgpr_write_b32 a253, v235\n"
        "v_mfma_f32_16x16x32_bf16 v[76:79], a[100:103], a[216:219], v[76:79]\n"
        "v_cvt_pk_bf16_f32 v192, v72, v73\n"
        "v_cvt_pk_bf16_f32 v193, v74, v75\n"
        "v_accvgpr_write_b32 a254, v235\n"
        "v_mfma_f32_16x16x32_bf16 v[76:79], a[104:107], a[220:223], v[76:79]\n"
        "v_accvgpr_write_b32 a255, v235\n"
        "v_cvt_pk_bf16_f32 v96, v96, v97\n"
        "v_fma_f32 v99, v99, s24, v238\n"
        "v_mfma_f32_16x16x32_bf16 v[76:79], a[108:111], a[224:227], v[76:79]\n"
        "v_exp_f32 v99, v99\n"
        "v_fma_f32 v100, v100, s24, v238\n"
        "v_mfma_f32_16x16x32_bf16 v[76:79], a[112:115], a[228:231], v[76:79]\n"
        "buffer_load_dwordx4 v142, s[52:55], s64 offen offset:2048 lds\n"
        "v_exp_f32 v100, v100\n"
        "v_mfma_f32_16x16x32_bf16 v[76:79], a[116:119], a[232:235], v[76:79]\n"
        "v_cvt_pk_bf16_f32 v97, v98, v99\n"
        "v_fma_f32 v101, v101, s24, v238\n"
        "v_fma_f32 v102, v102, s24, v238\n"
        "v_mfma_f32_16x16x32_bf16 v[162:165], a[120:123], a[248:251], 0\n"
        "ds_read_b128 a[236:239], v150 offset:0\n"
        "v_exp_f32 v101, v101\n"
        "v_mfma_f32_16x16x32_bf16 v[162:165], a[124:127], a[216:219], v[162:165]\n"
        "s_add_u32 m0, s65, 3072\n"
        "ds_read_b128 a[240:243], v151 offset:0\n"
        "v_exp_f32 v102, v102\n"
        "v_mfma_f32_16x16x32_bf16 v[162:165], a[128:131], a[220:223], v[162:165]\n"
        "s_add_u32 s64, s65, 3072\n"
        "s_waitcnt vmcnt(3)\n"
        "s_barrier\n"
        "ds_read_b64_tr_b16 v[212:213], v232 offset:36864\n"
        "ds_read_b128 a[244:247], v150 offset:128\n"
        "v_mfma_f32_16x16x32_bf16 v[162:165], a[132:135], a[224:227], v[162:165]\n"
        "buffer_load_dwordx4 v142, s[52:55], s64 offen lds\n"
        "v_cvt_pk_bf16_f32 v194, v76, v77\n"
        "v_cvt_pk_bf16_f32 v195, v78, v79\n"
        "v_mfma_f32_16x16x32_bf16 v[162:165], a[136:139], a[228:231], v[162:165]\n"
        "ds_read_b64_tr_b16 v[214:215], v232 offset:43008\n"
        "v_cvt_pk_bf16_f32 v98, v100, v101\n"
        "v_fma_f32 v103, v103, s24, v238\n"
        "v_mfma_f32_16x16x32_bf16 v[162:165], a[140:143], a[232:235], v[162:165]\n"
        "v_exp_f32 v103, v103\n"
        "v_fma_f32 v104, v104, s24, v238\n"
        "v_mfma_f32_16x16x32_bf16 v[72:75], a[0:3], a[248:251], 0\n"
        "ds_read_b64_tr_b16 v[216:217], v232 offset:55296\n"
        "v_exp_f32 v104, v104\n"
        "v_mfma_f32_16x16x32_bf16 v[72:75], a[4:7], a[216:219], v[72:75]\n"
        "v_cvt_pk_bf16_f32 v99, v102, v103\n"
        "v_fma_f32 v105, v105, s24, v238\n"
        "v_cvt_pk_bf16_f32 v208, v162, v163\n"
        "v_mfma_f32_16x16x32_bf16 v[72:75], a[8:11], a[220:223], v[72:75]\n"
        "buffer_load_dwordx4 v142, s[52:55], s64 offen offset:1024 lds\n"
        "ds_read_b64_tr_b16 v[218:219], v232 offset:61440\n"
        "v_cvt_pk_bf16_f32 v209, v164, v165\n"
        "v_mfma_f32_16x16x32_bf16 v[72:75], a[12:15], a[224:227], v[72:75]\n"
        "v_exp_f32 v105, v105\n"
        "v_fma_f32 v106, v106, s24, v238\n"
        "v_mfma_f32_16x16x32_bf16 v[72:75], a[16:19], a[228:231], v[72:75]\n"
        "ds_read_b64_tr_b16 v[220:221], v233 offset:49152\n"
        "v_exp_f32 v106, v106\n"
        "v_mfma_f32_16x16x32_bf16 v[72:75], a[20:23], a[232:235], v[72:75]\n"
        "v_cvt_pk_bf16_f32 v100, v104, v105\n"
        "v_fma_f32 v107, v107, s24, v238\n"
        "v_fma_f32 v108, v108, s24, v238\n"
        "v_mfma_f32_16x16x32_bf16 v[76:79], a[24:27], a[248:251], 0\n"
        "ds_read_b64_tr_b16 v[222:223], v233 offset:50688\n"
        "v_exp_f32 v107, v107\n"
        "v_mfma_f32_16x16x32_bf16 v[76:79], a[28:31], a[216:219], v[76:79]\n"
        "buffer_load_dwordx4 v142, s[52:55], s64 offen offset:2048 lds\n"
        "v_exp_f32 v108, v108\n"
        "v_mfma_f32_16x16x32_bf16 v[76:79], a[32:35], a[220:223], v[76:79]\n"
        "ds_read_b64_tr_b16 v[224:225], v232 offset:36896\n"
        "v_cvt_pk_bf16_f32 v196, v72, v73\n"
        "v_cvt_pk_bf16_f32 v197, v74, v75\n"
        "v_mfma_f32_16x16x32_bf16 v[76:79], a[36:39], a[224:227], v[76:79]\n"
        "v_cvt_pk_bf16_f32 v101, v106, v107\n"
        "v_fma_f32 v109, v109, s24, v238\n"
        "v_fma_f32 v110, v110, s24, v238\n"
        "v_mfma_f32_16x16x32_bf16 v[76:79], a[40:43], a[228:231], v[76:79]\n"
        "s_add_u32 m0, s65, 6144\n"
        "ds_read_b64_tr_b16 v[226:227], v232 offset:43040\n"
        "v_exp_f32 v109, v109\n"
        "v_mfma_f32_16x16x32_bf16 v[76:79], a[44:47], a[232:235], v[76:79]\n"
        "s_add_u32 s64, s65, 6144\n"
        "v_exp_f32 v110, v110\n"
        "v_cvt_pk_bf16_f32 v102, v108, v109\n"
        "v_mfma_f32_16x16x32_bf16 v[162:165], a[48:51], a[248:251], 0\n"
        "ds_read_b128 a[248:251], v151 offset:128\n"
        "buffer_load_dwordx4 v142, s[52:55], s64 offen lds\n"
        "v_fma_f32 v111, v111, s24, v238\n"
        "v_mfma_f32_16x16x32_bf16 v[162:165], a[52:55], a[216:219], v[162:165]\n"
        "ds_read_b128 a[216:219], v150 offset:256\n"
        "ds_read_b64_tr_b16 v[182:183], v232 offset:55328\n"
        "v_cvt_pk_bf16_f32 v198, v76, v77\n"
        "v_mfma_f32_16x16x32_bf16 v[162:165], a[56:59], a[220:223], v[162:165]\n"
        "ds_read_b128 a[220:223], v151 offset:256\n"
        "ds_read_b64_tr_b16 v[184:185], v232 offset:61472\n"
        "s_sub_u32 s31, s30, s28\n"
        "v_cvt_pk_bf16_f32 v199, v78, v79\n"
        "v_mfma_f32_16x16x32_bf16 v[162:165], a[60:63], a[224:227], v[162:165]\n"
        "s_sub_u32 s45, s28, s29\n"
        "v_exp_f32 v111, v111\n"
        "v_fma_f32 v112, v112, s24, v239\n"
        "v_mfma_f32_16x16x32_bf16 v[162:165], a[64:67], a[228:231], v[162:165]\n"
        "ds_read_b64_tr_b16 v[202:203], v233 offset:49184\n"
        "ds_read_b128 a[228:231], v150 offset:1536\n"
        "s_add_u32 s28, s28, s31\n"
        "v_cvt_pk_bf16_f32 v103, v110, v111\n"
        "v_mfma_f32_16x16x32_bf16 v[162:165], a[68:71], a[232:235], v[162:165]\n"
        "s_waitcnt lgkmcnt(15)\n"
        "buffer_load_dwordx4 v142, s[52:55], s64 offen offset:1024 lds\n"
        "ds_read_b128 a[232:235], v151 offset:1536\n"
        "v_fma_f32 v113, v113, s24, v239\n"
        "v_mfma_f32_16x16x32_bf16 v[72:75], a[236:239], a[144:147], 0\n"
        "s_waitcnt lgkmcnt(15)\n"
        "ds_read_b64_tr_b16 v[204:205], v233 offset:50720\n"
        "v_exp_f32 v112, v112\n"
        "v_mfma_f32_16x16x32_bf16 v[72:75], a[240:243], a[148:151], v[72:75]\n"
        "s_waitcnt lgkmcnt(15)\n"
        "v_exp_f32 v113, v113\n"
        "v_cvt_pk_bf16_f32 v200, v162, v163\n"
        "v_mfma_f32_16x16x32_bf16 v[72:75], a[244:247], a[152:155], v[72:75]\n"
        "s_waitcnt lgkmcnt(8)\n"
        "ds_read_b64_tr_b16 v[88:89], v232 offset:36928\n"
        "v_cvt_pk_bf16_f32 v201, v164, v165\n"
        "v_cvt_pk_bf16_f32 v206, v162, v163\n"
        "v_mfma_f32_16x16x32_bf16 v[72:75], a[248:251], a[156:159], v[72:75]\n"
        "s_waitcnt lgkmcnt(8)\n"
        "v_cvt_pk_bf16_f32 v207, v164, v165\n"
        "v_cvt_pk_bf16_f32 v112, v112, v113\n"
        "v_fma_f32 v114, v114, s24, v239\n"
        "v_mfma_f32_16x16x32_bf16 v[72:75], a[216:219], a[160:163], v[72:75]\n"
        "s_waitcnt lgkmcnt(6)\n"
        "buffer_load_dwordx4 v142, s[52:55], s64 offen offset:2048 lds\n"
        "s_add_u32 s41, s30, s40\n"
        "s_add_u32 s35, s47, 786432\n"
        "s_sub_u32 s46, s35, 100663296\n"
        "s_cmp_lt_u32 s46, 786432\n"
        "v_mfma_f32_16x16x32_bf16 v[72:75], a[220:223], a[164:167], v[72:75]\n"
        "s_cselect_b32 s35, s46, s35\n"
        "ds_read_b64_tr_b16 v[90:91], v232 offset:43072\n"
        "v_exp_f32 v114, v114\n"
        "v_mfma_f32_16x16x32_bf16 v[76:79], a[236:239], a[168:171], 0\n"
        "ds_read_b64_tr_b16 v[92:93], v232 offset:55360\n"
        "v_fma_f32 v115, v115, s24, v239\n"
        "v_fma_f32 v116, v116, s24, v239\n"
        "v_mfma_f32_16x16x32_bf16 v[76:79], a[240:243], a[172:175], v[76:79]\n"
        "v_exp_f32 v115, v115\n"
        "v_cvt_pk_bf16_f32 v128, v72, v73\n"
        "v_mfma_f32_16x16x32_bf16 v[76:79], a[244:247], a[176:179], v[76:79]\n"
        "ds_read_b64_tr_b16 v[94:95], v232 offset:61504\n"
        "v_exp_f32 v116, v116\n"
        "v_mfma_f32_16x16x32_bf16 v[76:79], a[248:251], a[180:183], v[76:79]\n"
        "s_mov_b32 m0, s48\n"
        "v_cvt_pk_bf16_f32 v129, v74, v75\n"
        "v_cvt_pk_bf16_f32 v113, v114, v115\n"
        "v_fma_f32 v117, v117, s24, v239\n"
        "v_mfma_f32_16x16x32_bf16 v[76:79], a[216:219], a[184:187], v[76:79]\n"
        "buffer_load_dwordx4 v249, s[12:15], s36 offen offset:0 lds\n"
        "ds_read_b64_tr_b16 v[104:105], v233 offset:49216\n"
        "v_mfma_f32_16x16x32_bf16 v[76:79], a[220:223], a[188:191], v[76:79]\n"
        "v_exp_f32 v117, v117\n"
        "v_fma_f32 v118, v118, s24, v239\n"
        "v_mfma_f32_16x16x32_bf16 v[162:165], a[236:239], a[192:195], 0\n"
        "ds_read_b128 a[236:239], v150 offset:1664\n"
        "ds_read_b64_tr_b16 v[106:107], v233 offset:50752\n"
        "v_cvt_pk_bf16_f32 v114, v116, v117\n"
        "v_mfma_f32_16x16x32_bf16 v[162:165], a[240:243], a[196:199], v[162:165]\n"
        "ds_read_b128 a[240:243], v151 offset:1664\n"
        "ds_read_b128 a[224:227], v151 offset:12288\n"
        "v_xor_b32 v253, 32, v249\n"
        "v_mfma_f32_16x16x32_bf16 v[162:165], a[244:247], a[200:203], v[162:165]\n"
        "ds_read_b128 a[244:247], v150 offset:1792\n"
        "v_exp_f32 v118, v118\n"
        "v_mfma_f32_16x16x32_bf16 v[162:165], a[248:251], a[204:207], v[162:165]\n"
        "ds_read_b128 a[248:251], v151 offset:1792\n"
        "buffer_load_dwordx4 v253, s[12:15], s36 offen offset:1024 lds\n"
        "s_mov_b32 s30, s29\n"
        "v_mfma_f32_16x16x32_bf16 v[162:165], a[216:219], a[208:211], v[162:165]\n"
        "s_waitcnt lgkmcnt(4)\n"
        "s_barrier\n"
        "ds_write_b128 v234, v[212:215] offset:36864\n"
        "v_mfma_f32_16x16x32_bf16 v[162:165], a[220:223], a[212:215], v[162:165]\n"
        "s_add_u32 s29, s29, s45\n"
        "ds_read_b128 a[220:223], v150 offset:12288\n"
        "v_cvt_pk_bf16_f32 v136, v76, v77\n"
        "v_mfma_f32_16x16x32_bf16 v[72:75], a[228:231], a[144:147], 0\n"
        "v_cvt_pk_bf16_f32 v137, v78, v79\n"
        "v_fma_f32 v119, v119, s24, v239\n"
        "v_mfma_f32_16x16x32_bf16 v[72:75], a[232:235], a[148:151], v[72:75]\n"
        "s_add_u32 m0, s41, 0\n"
        "v_exp_f32 v119, v119\n"
        "v_mfma_f32_16x16x32_bf16 v[72:75], a[236:239], a[152:155], v[72:75]\n"
        "s_waitcnt lgkmcnt(5)\n"
        "buffer_load_dwordx4 v242, s[8:11], s47 offen lds\n"
        "v_mfma_f32_16x16x32_bf16 v[72:75], a[240:243], a[156:159], v[72:75]\n"
        "s_waitcnt lgkmcnt(3)\n"
        "ds_write_b128 v234, v[216:219] offset:43008\n"
        "v_mfma_f32_16x16x32_bf16 v[72:75], a[244:247], a[160:163], v[72:75]\n"
        "s_waitcnt lgkmcnt(3)\n"
        "v_cvt_pk_bf16_f32 v144, v162, v163\n"
        "v_cvt_pk_bf16_f32 v145, v164, v165\n"
        "v_mfma_f32_16x16x32_bf16 v[72:75], a[248:251], a[164:167], v[72:75]\n"
        "v_cvt_pk_bf16_f32 v115, v118, v119\n"
        "v_fma_f32 v120, v120, s24, v239\n"
        "v_mfma_f32_16x16x32_bf16 v[76:79], a[228:231], a[168:171], 0\n"
        "ds_write_b128 v234, v[220:223] offset:49152\n"
        "v_mfma_f32_16x16x32_bf16 v[76:79], a[232:235], a[172:175], v[76:79]\n"
        "v_exp_f32 v120, v120\n"
        "v_mfma_f32_16x16x32_bf16 v[76:79], a[236:239], a[176:179], v[76:79]\n"
        "v_fma_f32 v121, v121, s24, v239\n"
        "v_cvt_pk_bf16_f32 v130, v72, v73\n"
        "v_mfma_f32_16x16x32_bf16 v[76:79], a[240:243], a[180:183], v[76:79]\n"
        "ds_write_b128 v234, v[224:227] offset:37120\n"
        "v_mfma_f32_16x16x32_bf16 v[76:79], a[244:247], a[184:187], v[76:79]\n"
        "v_cvt_pk_bf16_f32 v131, v74, v75\n"
        "v_fma_f32 v122, v122, s24, v239\n"
        "v_mfma_f32_16x16x32_bf16 v[76:79], a[248:251], a[188:191], v[76:79]\n"
        "v_exp_f32 v121, v121\n"
        "v_mfma_f32_16x16x32_bf16 v[162:165], a[228:231], a[192:195], 0\n"
        "ds_read_b128 a[228:231], v150 offset:12416\n"
        "v_cvt_pk_bf16_f32 v116, v120, v121\n"
        "v_mfma_f32_16x16x32_bf16 v[162:165], a[232:235], a[196:199], v[162:165]\n"
        "ds_read_b128 a[232:235], v151 offset:12416\n"
        "ds_read_b128 a[216:219], v151 offset:13824\n"
        "v_mfma_f32_16x16x32_bf16 v[162:165], a[236:239], a[200:203], v[162:165]\n"
        "ds_write_b128 v234, v[182:185] offset:43264\n"
        "v_mfma_f32_16x16x32_bf16 v[162:165], a[240:243], a[204:207], v[162:165]\n"
        "ds_read_b128 a[236:239], v150 offset:12544\n"
        "ds_read_b128 a[240:243], v151 offset:12544\n"
        "v_mfma_f32_16x16x32_bf16 v[162:165], a[244:247], a[208:211], v[162:165]\n"
        "v_cvt_pk_bf16_f32 v138, v76, v77\n"
        "v_cvt_pk_bf16_f32 v139, v78, v79\n"
        "v_mfma_f32_16x16x32_bf16 v[162:165], a[248:251], a[212:215], v[162:165]\n"
        "s_waitcnt lgkmcnt(9)\n"
        "ds_write_b128 v234, v[202:205] offset:49408\n"
        "v_mfma_f32_16x16x32_bf16 v[72:75], a[220:223], a[144:147], 0\n"
        "ds_read_b128 a[248:251], v150 offset:13824\n"
        "v_fma_f32 v123, v123, s24, v239\n"
        "v_mfma_f32_16x16x32_bf16 v[72:75], a[224:227], a[148:151], v[72:75]\n"
        "s_waitcnt lgkmcnt(7)\n"
        "v_exp_f32 v122, v122\n"
        "v_mfma_f32_16x16x32_bf16 v[72:75], a[228:231], a[152:155], v[72:75]\n"
        "s_waitcnt lgkmcnt(6)\n"
        "ds_write_b128 v234, v[88:91] offset:37376\n"
        "v_mfma_f32_16x16x32_bf16 v[72:75], a[232:235], a[156:159], v[72:75]\n"
        "s_waitcnt lgkmcnt(4)\n"
        "v_exp_f32 v123, v123\n"
        "v_mfma_f32_16x16x32_bf16 v[72:75], a[236:239], a[160:163], v[72:75]\n"
        "s_waitcnt lgkmcnt(3)\n"
        "v_cvt_pk_bf16_f32 v146, v162, v163\n"
        "v_cvt_pk_bf16_f32 v147, v164, v165\n"
        "v_mfma_f32_16x16x32_bf16 v[72:75], a[240:243], a[164:167], v[72:75]\n"
        "ds_write_b128 v234, v[92:95] offset:43520\n"
        "v_mfma_f32_16x16x32_bf16 v[76:79], a[220:223], a[168:171], 0\n"
        "v_cvt_pk_bf16_f32 v117, v122, v123\n"
        "v_fma_f32 v124, v124, s24, v239\n"
        "v_mfma_f32_16x16x32_bf16 v[76:79], a[224:227], a[172:175], v[76:79]\n"
        "v_fma_f32 v125, v125, s24, v239\n"
        "v_fma_f32 v126, v126, s24, v239\n"
        "v_mfma_f32_16x16x32_bf16 v[76:79], a[228:231], a[176:179], v[76:79]\n"
        "ds_write_b128 v234, v[104:107] offset:49664\n"
        "s_waitcnt vmcnt(3)\n"
        "s_barrier\n"
        "v_mfma_f32_16x16x32_bf16 v[76:79], a[232:235], a[180:183], v[76:79]\n"
        "ds_read_b64_tr_b16 v[108:109], v232 offset:0\n"
        "v_fma_f32 v127, v127, s24, v239\n"
        "v_mfma_f32_16x16x32_bf16 v[76:79], a[236:239], a[184:187], v[76:79]\n"
        "ds_read_b64_tr_b16 v[110:111], v232 offset:6144\n"
        "v_cvt_pk_bf16_f32 v132, v72, v73\n"
        "v_mfma_f32_16x16x32_bf16 v[76:79], a[240:243], a[188:191], v[76:79]\n"
        "v_exp_f32 v124, v124\n"
        "v_mfma_f32_16x16x32_bf16 v[162:165], a[220:223], a[192:195], 0\n"
        "ds_read_b128 a[220:223], v150 offset:13952\n"
        "ds_read_b64_tr_b16 v[120:121], v232 offset:18432\n"
        "v_mfma_f32_16x16x32_bf16 v[162:165], a[224:227], a[196:199], v[162:165]\n"
        "ds_read_b128 a[224:227], v151 offset:13952\n"
        "v_cvt_pk_bf16_f32 v133, v74, v75\n"
        "v_mfma_f32_16x16x32_bf16 v[162:165], a[228:231], a[200:203], v[162:165]\n"
        "ds_read_b128 a[228:231], v150 offset:14080\n"
        "ds_read_b64_tr_b16 v[122:123], v232 offset:24576\n"
        "v_mfma_f32_16x16x32_bf16 v[162:165], a[232:235], a[204:207], v[162:165]\n"
        "ds_read_b128 a[232:235], v151 offset:14080\n"
        "v_cvt_pk_bf16_f32 v140, v76, v77\n"
        "v_mfma_f32_16x16x32_bf16 v[162:165], a[236:239], a[208:211], v[162:165]\n"
        "ds_read_b128 a[236:239], v240 offset:13824\n"
        "ds_read_b128 a[244:247], v240 offset:13952\n"
        "v_mfma_f32_16x16x32_bf16 v[162:165], a[240:243], a[212:215], v[162:165]\n"
        "s_waitcnt lgkmcnt(13)\n"
        "ds_read_b128 a[240:243], v241 offset:13824\n"
        "v_cvt_pk_bf16_f32 v141, v78, v79\n"
        "v_mfma_f32_16x16x32_bf16 v[72:75], a[248:251], a[144:147], 0\n"
        "v_exp_f32 v125, v125\n"
        "v_mfma_f32_16x16x32_bf16 v[72:75], a[216:219], a[148:151], v[72:75]\n"
        "s_waitcnt lgkmcnt(8)\n"
        "v_cvt_pk_bf16_f32 v118, v124, v125\n"
        "v_mfma_f32_16x16x32_bf16 v[72:75], a[220:223], a[152:155], v[72:75]\n"
        "s_waitcnt lgkmcnt(6)\n"
        "ds_read_b64_tr_b16 v[124:125], v233 offset:12288\n"
        "ds_read_b64_tr_b16 v[222:223], v232 offset:32\n"
        "v_mfma_f32_16x16x32_bf16 v[72:75], a[224:227], a[156:159], v[72:75]\n"
        "s_waitcnt lgkmcnt(7)\n"
        "v_exp_f32 v126, v126\n"
        "v_mfma_f32_16x16x32_bf16 v[72:75], a[228:231], a[160:163], v[72:75]\n"
        "s_waitcnt lgkmcnt(5)\n"
        "ds_read_b64_tr_b16 v[224:225], v232 offset:6176\n"
        "v_cvt_pk_bf16_f32 v148, v162, v163\n"
        "v_mfma_f32_16x16x32_bf16 v[72:75], a[232:235], a[164:167], v[72:75]\n"
        "v_exp_f32 v127, v127\n"
        "v_mfma_f32_16x16x32_bf16 v[76:79], a[248:251], a[168:171], 0\n"
        "ds_read_b64_tr_b16 v[182:183], v232 offset:18464\n"
        "v_cvt_pk_bf16_f32 v119, v126, v127\n"
        "v_mfma_f32_16x16x32_bf16 v[76:79], a[216:219], a[172:175], v[76:79]\n"
        "ds_read_b64_tr_b16 v[126:127], v233 offset:13824\n"
        "v_cvt_pk_bf16_f32 v149, v164, v165\n"
        "v_mfma_f32_16x16x32_bf16 v[76:79], a[220:223], a[176:179], v[76:79]\n"
        "ds_read_b64_tr_b16 v[184:185], v232 offset:24608\n"
        "v_cvt_pk_bf16_f32 v134, v72, v73\n"
        "v_mfma_f32_16x16x32_bf16 v[76:79], a[224:227], a[180:183], v[76:79]\n"
        "v_cvt_pk_bf16_f32 v135, v74, v75\n"
        "v_mfma_f32_16x16x32_bf16 v[76:79], a[228:231], a[184:187], v[76:79]\n"
        "ds_read_b64_tr_b16 v[202:203], v233 offset:12320\n"
        "v_mfma_f32_16x16x32_bf16 v[76:79], a[232:235], a[188:191], v[76:79]\n"
        "v_mfma_f32_16x16x32_bf16 v[162:165], a[248:251], a[192:195], 0\n"
        "ds_read_b128 a[248:251], v241 offset:13952\n"
        "ds_read_b64_tr_b16 v[204:205], v233 offset:13856\n"
        "v_mfma_f32_16x16x32_bf16 v[162:165], a[216:219], a[196:199], v[162:165]\n"
        "ds_read_b128 a[216:219], v240 offset:14080\n"
        "v_mfma_f32_16x16x32_bf16 v[162:165], a[220:223], a[200:203], v[162:165]\n"
        "ds_read_b128 a[220:223], v241 offset:14080\n"
        "ds_read_b64_tr_b16 v[88:89], v232 offset:64\n"
        "v_mfma_f32_16x16x32_bf16 v[162:165], a[224:227], a[204:207], v[162:165]\n"
        "v_cvt_pk_bf16_f32 v142, v76, v77\n"
        "v_cvt_pk_bf16_f32 v143, v78, v79\n"
        "v_mfma_f32_16x16x32_bf16 v[162:165], a[228:231], a[208:211], v[162:165]\n"
        "ds_read_b64_tr_b16 v[90:91], v232 offset:6208\n"
        "v_mfma_f32_16x16x32_bf16 v[162:165], a[232:235], a[212:215], v[162:165]\n"
        "s_waitcnt lgkmcnt(15)\n"
        "v_mfma_f32_16x16x32_bf16 v[72:75], a[72:75], a[236:239], 0\n"
        "s_waitcnt lgkmcnt(13)\n"
        "ds_read_b64_tr_b16 v[92:93], v232 offset:18496\n"
        "v_mfma_f32_16x16x32_bf16 v[72:75], a[76:79], a[240:243], v[72:75]\n"
        "v_mfma_f32_16x16x32_bf16 v[72:75], a[80:83], a[244:247], v[72:75]\n"
        "s_waitcnt lgkmcnt(6)\n"
        "ds_read_b64_tr_b16 v[94:95], v232 offset:24640\n"
        "v_mfma_f32_16x16x32_bf16 v[72:75], a[84:87], a[248:251], v[72:75]\n"
        "s_waitcnt lgkmcnt(5)\n"
        "v_mfma_f32_16x16x32_bf16 v[72:75], a[88:91], a[216:219], v[72:75]\n"
        "s_waitcnt lgkmcnt(4)\n"
        "ds_read_b64_tr_b16 v[104:105], v233 offset:12352\n"
        "v_cvt_pk_bf16_f32 v150, v162, v163\n"
        "v_mfma_f32_16x16x32_bf16 v[72:75], a[92:95], a[220:223], v[72:75]\n"
        "v_cvt_pk_bf16_f32 v151, v164, v165\n"
        "v_mfma_f32_16x16x32_bf16 v[76:79], a[96:99], a[236:239], 0\n"
        "s_add_u32 m0, s41, 1024\n"
        "ds_read_b64_tr_b16 v[106:107], v233 offset:13888\n"
        "v_mfma_f32_16x16x32_bf16 v[76:79], a[100:103], a[240:243], v[76:79]\n"
        "v_mfma_f32_16x16x32_bf16 v[76:79], a[104:107], a[244:247], v[76:79]\n"
        "buffer_load_dwordx4 v243, s[8:11], s47 offen lds\n"
        "s_waitcnt lgkmcnt(0)\n"
        "s_barrier\n"
        "v_mfma_f32_16x16x32_bf16 v[76:79], a[108:111], a[248:251], v[76:79]\n"
        "ds_write_b128 v234, v[108:111] offset:0\n"
        "v_mfma_f32_16x16x32_bf16 v[76:79], a[112:115], a[216:219], v[76:79]\n"
        "v_cvt_pk_bf16_f32 v212, v72, v73\n"
        "v_cvt_pk_bf16_f32 v213, v74, v75\n"
        "v_mfma_f32_16x16x32_bf16 v[76:79], a[116:119], a[220:223], v[76:79]\n"
        "v_mfma_f32_16x16x32_bf16 v[162:165], a[120:123], a[236:239], 0\n"
        "ds_write_b128 v234, v[120:123] offset:6144\n"
        "v_mfma_f32_16x16x32_bf16 v[162:165], a[124:127], a[240:243], v[162:165]\n"
        "v_mfma_f32_16x16x32_bf16 v[162:165], a[128:131], a[244:247], v[162:165]\n"
        "v_mfma_f32_16x16x32_bf16 v[162:165], a[132:135], a[248:251], v[162:165]\n"
        "ds_write_b128 v234, v[124:127] offset:12288\n"
        "v_mfma_f32_16x16x32_bf16 v[162:165], a[136:139], a[216:219], v[162:165]\n"
        "v_mfma_f32_16x16x32_bf16 v[162:165], a[140:143], a[220:223], v[162:165]\n"
        "v_cvt_pk_bf16_f32 v214, v76, v77\n"
        "v_cvt_pk_bf16_f32 v215, v78, v79\n"
        "v_mfma_f32_16x16x32_bf16 v[72:75], a[0:3], a[236:239], 0\n"
        "ds_write_b128 v234, v[222:225] offset:256\n"
        "v_mfma_f32_16x16x32_bf16 v[72:75], a[4:7], a[240:243], v[72:75]\n"
        "v_mfma_f32_16x16x32_bf16 v[72:75], a[8:11], a[244:247], v[72:75]\n"
        "s_add_u32 m0, s41, 2048\n"
        "v_mfma_f32_16x16x32_bf16 v[72:75], a[12:15], a[248:251], v[72:75]\n"
        "ds_write_b128 v234, v[182:185] offset:6400\n"
        "v_mfma_f32_16x16x32_bf16 v[72:75], a[16:19], a[216:219], v[72:75]\n"
        "buffer_load_dwordx4 v244, s[8:11], s47 offen lds\n"
        "v_mfma_f32_16x16x32_bf16 v[72:75], a[20:23], a[220:223], v[72:75]\n"
        "v_cvt_pk_bf16_f32 v230, v162, v163\n"
        "v_cvt_pk_bf16_f32 v231, v164, v165\n"
        "v_mfma_f32_16x16x32_bf16 v[76:79], a[24:27], a[236:239], 0\n"
        "ds_write_b128 v234, v[202:205] offset:12544\n"
        "v_mfma_f32_16x16x32_bf16 v[76:79], a[28:31], a[240:243], v[76:79]\n"
        "v_mfma_f32_16x16x32_bf16 v[76:79], a[32:35], a[244:247], v[76:79]\n"
        "v_mfma_f32_16x16x32_bf16 v[76:79], a[36:39], a[248:251], v[76:79]\n"
        "ds_write_b128 v234, v[88:91] offset:512\n"
        "v_mfma_f32_16x16x32_bf16 v[76:79], a[40:43], a[216:219], v[76:79]\n"
        "v_cvt_pk_bf16_f32 v216, v72, v73\n"
        "v_cvt_pk_bf16_f32 v217, v74, v75\n"
        "v_mfma_f32_16x16x32_bf16 v[76:79], a[44:47], a[220:223], v[76:79]\n"
        "v_mfma_f32_16x16x32_bf16 v[162:165], a[48:51], a[236:239], 0\n"
        "ds_write_b128 v234, v[92:95] offset:6656\n"
        "v_mfma_f32_16x16x32_bf16 v[162:165], a[52:55], a[240:243], v[162:165]\n"
        "v_mfma_f32_16x16x32_bf16 v[162:165], a[56:59], a[244:247], v[162:165]\n"
        "v_mfma_f32_16x16x32_bf16 v[162:165], a[60:63], a[248:251], v[162:165]\n"
        "ds_write_b128 v234, v[104:107] offset:12800\n"
        "v_mfma_f32_16x16x32_bf16 v[162:165], a[64:67], a[216:219], v[162:165]\n"
        "s_add_u32 m0, s41, 12288\n"
        "v_cvt_pk_bf16_f32 v218, v76, v77\n"
        "v_cvt_pk_bf16_f32 v219, v78, v79\n"
        "v_mfma_f32_16x16x32_bf16 v[162:165], a[68:71], a[220:223], v[162:165]\n"
        "v_mfma_f32_16x16x32_bf16 v[88:91], a[252:255], v[64:67], 0\n"
        "buffer_load_dwordx4 v242, s[8:11], s35 offen lds\n"
        "v_mfma_f32_16x16x32_bf16 v[88:91], a[252:255], v[68:71], v[88:91]\n"
        "v_mfma_f32_16x16x32_bf16 v[92:95], v[132:135], v[68:71], 0\n"
        "v_mfma_f32_16x16x32_bf16 v[92:95], v[128:131], v[64:67], v[92:95]\n"
        "v_mfma_f32_16x16x32_bf16 v[182:185], v[136:139], v[64:67], 0\n"
        "v_mfma_f32_16x16x32_bf16 v[182:185], v[140:143], v[68:71], v[182:185]\n"
        "v_mfma_f32_16x16x32_bf16 v[202:205], v[148:151], v[68:71], 0\n"
        "s_cmp_eq_u32 s37, 100663296\n"
        "v_cvt_pk_bf16_f32 v220, v162, v163\n"
        "v_cvt_pk_bf16_f32 v221, v164, v165\n"
        "v_mfma_f32_16x16x32_bf16 v[202:205], v[144:147], v[64:67], v[202:205]\n"
        "s_cselect_b32 s46, 0, s37\n"
        "v_cvt_pk_bf16_f32 v226, v162, v163\n"
        "v_cvt_pk_bf16_f32 v227, v164, v165\n"
        "v_mfma_f32_16x16x32_bf16 v[72:75], a[252:255], v[80:83], 0\n"
        "s_add_u32 s38, s46, s43\n"
        "v_rcp_f32 v88, v88\n"
        "v_mfma_f32_16x16x32_bf16 v[72:75], a[252:255], v[84:87], v[72:75]\n"
        "s_add_u32 s39, s46, s44\n"
        "v_mul_f32 v92, v88, v92\n"
        "v_mul_f32 v93, v88, v93\n"
        "v_mfma_f32_16x16x32_bf16 v[76:79], v[132:135], v[84:87], 0\n"
        "s_add_u32 s37, s37, 6291456\n"
        "v_mul_f32 v94, v88, v94\n"
        "v_mul_f32 v95, v88, v95\n"
        "v_mfma_f32_16x16x32_bf16 v[76:79], v[128:131], v[80:83], v[76:79]\n"
        "v_rcp_f32 v72, v72\n"
        "v_mfma_f32_16x16x32_bf16 v[104:107], v[136:139], v[80:83], 0\n"
        "s_add_u32 m0, s41, 13312\n"
        "v_cvt_pk_bf16_f32 v92, v92, v93\n"
        "v_cvt_pk_bf16_f32 v93, v94, v95\n"
        "v_mfma_f32_16x16x32_bf16 v[104:107], v[140:143], v[84:87], v[104:107]\n"
        "v_mul_f32 v182, v88, v182\n"
        "v_mul_f32 v183, v88, v183\n"
        "v_mfma_f32_16x16x32_bf16 v[108:111], v[148:151], v[84:87], 0\n"
        "buffer_load_dwordx4 v243, s[8:11], s35 offen lds\n"
        "v_mfma_f32_16x16x32_bf16 v[108:111], v[144:147], v[80:83], v[108:111]\n"
        "v_mul_f32 v184, v88, v184\n"
        "v_mul_f32 v185, v88, v185\n"
        "v_mfma_f32_16x16x32_bf16 v[64:67], a[252:255], v[96:99], 0\n"
        "v_cvt_pk_bf16_f32 v94, v182, v183\n"
        "v_cvt_pk_bf16_f32 v95, v184, v185\n"
        "v_mfma_f32_16x16x32_bf16 v[64:67], a[252:255], v[100:103], v[64:67]\n"
        "ds_write_b128 v245, v[92:95] offset:0\n"
        "v_mfma_f32_16x16x32_bf16 v[68:71], v[132:135], v[100:103], 0\n"
        "v_mul_f32 v202, v88, v202\n"
        "v_mul_f32 v203, v88, v203\n"
        "v_mfma_f32_16x16x32_bf16 v[68:71], v[128:131], v[96:99], v[68:71]\n"
        "v_mul_f32 v204, v88, v204\n"
        "v_mul_f32 v205, v88, v205\n"
        "v_mfma_f32_16x16x32_bf16 v[120:123], v[136:139], v[96:99], 0\n"
        "v_cvt_pk_bf16_f32 v202, v202, v203\n"
        "v_cvt_pk_bf16_f32 v203, v204, v205\n"
        "v_mfma_f32_16x16x32_bf16 v[120:123], v[140:143], v[100:103], v[120:123]\n"
        "v_mul_f32 v76, v72, v76\n"
        "v_mul_f32 v77, v72, v77\n"
        "v_mfma_f32_16x16x32_bf16 v[124:127], v[148:151], v[100:103], 0\n"
        "ds_write_b64 v246, v[202:203] offset:0\n"
        "v_mfma_f32_16x16x32_bf16 v[124:127], v[144:147], v[96:99], v[124:127]\n"
        "v_rcp_f32 v64, v64\n"
        "v_mfma_f32_16x16x32_bf16 v[80:83], a[252:255], v[112:115], 0\n"
        "v_mul_f32 v78, v72, v78\n"
        "v_mul_f32 v79, v72, v79\n"
        "v_mfma_f32_16x16x32_bf16 v[80:83], a[252:255], v[116:119], v[80:83]\n"
        "v_cvt_pk_bf16_f32 v76, v76, v77\n"
        "v_cvt_pk_bf16_f32 v77, v78, v79\n"
        "v_mfma_f32_16x16x32_bf16 v[84:87], v[132:135], v[116:119], 0\n"
        "s_add_u32 m0, s41, 14336\n"
        "v_mul_f32 v104, v72, v104\n"
        "v_mul_f32 v105, v72, v105\n"
        "v_mfma_f32_16x16x32_bf16 v[84:87], v[128:131], v[112:115], v[84:87]\n"
        "v_mul_f32 v106, v72, v106\n"
        "v_mul_f32 v107, v72, v107\n"
        "v_mfma_f32_16x16x32_bf16 v[222:225], v[136:139], v[112:115], 0\n"
        "buffer_load_dwordx4 v244, s[8:11], s35 offen lds\n"
        "v_mfma_f32_16x16x32_bf16 v[222:225], v[140:143], v[116:119], v[222:225]\n"
        "ds_read_b128 a[224:227], v240 offset:0\n"
        "ds_read_b128 a[228:231], v241 offset:0\n"
        "v_mfma_f32_16x16x32_bf16 v[162:165], v[148:151], v[116:119], 0\n"
        "ds_read_b128 a[232:235], v240 offset:128\n"
        "v_cvt_pk_bf16_f32 v78, v104, v105\n"
        "v_mfma_f32_16x16x32_bf16 v[162:165], v[144:147], v[112:115], v[162:165]\n"
        "v_cvt_pk_bf16_f32 v79, v106, v107\n"
        "v_mul_f32 v108, v72, v108\n"
        "v_mfma_f32_16x16x32_bf16 v[96:99], a[236:239], a[144:147], 0\n"
        "ds_write_b128 v245, v[76:79] offset:256\n"
        "v_mfma_f32_16x16x32_bf16 v[96:99], a[240:243], a[148:151], v[96:99]\n"
        "v_mul_f32 v109, v72, v109\n"
        "v_mul_f32 v110, v72, v110\n"
        "v_mfma_f32_16x16x32_bf16 v[96:99], a[244:247], a[152:155], v[96:99]\n"
        "v_mul_f32 v111, v72, v111\n"
        "v_cvt_pk_bf16_f32 v108, v108, v109\n"
        "v_mfma_f32_16x16x32_bf16 v[96:99], a[248:251], a[156:159], v[96:99]\n"
        "v_cvt_pk_bf16_f32 v109, v110, v111\n"
        "v_mul_f32 v68, v64, v68\n"
        "v_mfma_f32_16x16x32_bf16 v[96:99], a[216:219], a[160:163], v[96:99]\n"
        "v_rcp_f32 v80, v80\n"
        "v_mfma_f32_16x16x32_bf16 v[96:99], a[220:223], a[164:167], v[96:99]\n"
        "ds_write_b64 v246, v[108:109] offset:256\n"
        "v_mfma_f32_16x16x32_bf16 v[100:103], a[236:239], a[168:171], 0\n"
        "v_mul_f32 v69, v64, v69\n"
        "v_mul_f32 v70, v64, v70\n"
        "v_mfma_f32_16x16x32_bf16 v[100:103], a[240:243], a[172:175], v[100:103]\n"
        "v_mul_f32 v71, v64, v71\n"
        "v_cvt_pk_bf16_f32 v68, v68, v69\n"
        "v_mfma_f32_16x16x32_bf16 v[100:103], a[244:247], a[176:179], v[100:103]\n"
        "v_cvt_pk_bf16_f32 v134, v96, v97\n"
        "v_cvt_pk_bf16_f32 v135, v98, v99\n"
        "v_mfma_f32_16x16x32_bf16 v[100:103], a[248:251], a[180:183], v[100:103]\n"
        "v_cvt_pk_bf16_f32 v69, v70, v71\n"
        "v_mul_f32 v120, v64, v120\n"
        "v_mfma_f32_16x16x32_bf16 v[100:103], a[216:219], a[184:187], v[100:103]\n"
        "v_mul_f32 v121, v64, v121\n"
        "v_mul_f32 v122, v64, v122\n"
        "v_mfma_f32_16x16x32_bf16 v[100:103], a[220:223], a[188:191], v[100:103]\n"
        "v_mul_f32 v123, v64, v123\n"
        "v_cvt_pk_bf16_f32 v70, v120, v121\n"
        "v_mfma_f32_16x16x32_bf16 v[112:115], a[236:239], a[192:195], 0\n"
        "ds_read_b128 a[236:239], v241 offset:128\n"
        "v_cvt_pk_bf16_f32 v71, v122, v123\n"
        "v_mfma_f32_16x16x32_bf16 v[112:115], a[240:243], a[196:199], v[112:115]\n"
        "ds_read_b128 a[240:243], v240 offset:256\n"
        "v_mul_f32 v124, v64, v124\n"
        "v_mfma_f32_16x16x32_bf16 v[112:115], a[244:247], a[200:203], v[112:115]\n"
        "ds_read_b128 a[244:247], v241 offset:256\n"
        "v_cvt_pk_bf16_f32 v142, v100, v101\n"
        "v_mfma_f32_16x16x32_bf16 v[112:115], a[248:251], a[204:207], v[112:115]\n"
        "ds_read_b128 a[248:251], v240 offset:1536\n"
        "v_cvt_pk_bf16_f32 v143, v102, v103\n"
        "v_mfma_f32_16x16x32_bf16 v[112:115], a[216:219], a[208:211], v[112:115]\n"
        "ds_read_b128 a[216:219], v241 offset:1536\n"
        "v_mul_f32 v125, v64, v125\n"
        "v_mfma_f32_16x16x32_bf16 v[112:115], a[220:223], a[212:215], v[112:115]\n"
        "s_waitcnt lgkmcnt(9)\n"
        "ds_read_b128 a[220:223], v240 offset:1664\n"
        "v_mul_f32 v126, v64, v126\n"
        "v_mfma_f32_16x16x32_bf16 v[96:99], a[224:227], a[144:147], 0\n"
        "s_waitcnt lgkmcnt(9)\n"
        "ds_write_b128 v245, v[68:71] offset:512\n"
        "v_mfma_f32_16x16x32_bf16 v[96:99], a[228:231], a[148:151], v[96:99]\n"
        "s_waitcnt lgkmcnt(9)\n"
        "v_mul_f32 v127, v64, v127\n"
        "v_cvt_pk_bf16_f32 v124, v124, v125\n"
        "v_mfma_f32_16x16x32_bf16 v[96:99], a[232:235], a[152:155], v[96:99]\n"
        "s_waitcnt lgkmcnt(6)\n"
        "v_cvt_pk_bf16_f32 v150, v112, v113\n"
        "v_cvt_pk_bf16_f32 v151, v114, v115\n"
        "v_mfma_f32_16x16x32_bf16 v[96:99], a[236:239], a[156:159], v[96:99]\n"
        "s_waitcnt lgkmcnt(5)\n"
        "v_cvt_pk_bf16_f32 v125, v126, v127\n"
        "v_mul_f32 v84, v80, v84\n"
        "v_mfma_f32_16x16x32_bf16 v[96:99], a[240:243], a[160:163], v[96:99]\n"
        "s_waitcnt lgkmcnt(4)\n"
        "v_mul_f32 v85, v80, v85\n"
        "v_mul_f32 v86, v80, v86\n"
        "v_mfma_f32_16x16x32_bf16 v[96:99], a[244:247], a[164:167], v[96:99]\n"
        "ds_write_b64 v246, v[124:125] offset:512\n"
        "v_mfma_f32_16x16x32_bf16 v[100:103], a[224:227], a[168:171], 0\n"
        "v_mul_f32 v87, v80, v87\n"
        "v_cvt_pk_bf16_f32 v84, v84, v85\n"
        "v_mfma_f32_16x16x32_bf16 v[100:103], a[228:231], a[172:175], v[100:103]\n"
        "v_cvt_pk_bf16_f32 v85, v86, v87\n"
        "v_mul_f32 v222, v80, v222\n"
        "v_mfma_f32_16x16x32_bf16 v[100:103], a[232:235], a[176:179], v[100:103]\n"
        "v_cvt_pk_bf16_f32 v128, v96, v97\n"
        "v_cvt_pk_bf16_f32 v129, v98, v99\n"
        "v_mfma_f32_16x16x32_bf16 v[100:103], a[236:239], a[180:183], v[100:103]\n"
        "v_mul_f32 v223, v80, v223\n"
        "v_mul_f32 v224, v80, v224\n"
        "v_mfma_f32_16x16x32_bf16 v[100:103], a[240:243], a[184:187], v[100:103]\n"
        "v_mul_f32 v225, v80, v225\n"
        "v_cvt_pk_bf16_f32 v86, v222, v223\n"
        "v_mfma_f32_16x16x32_bf16 v[100:103], a[244:247], a[188:191], v[100:103]\n"
        "v_cvt_pk_bf16_f32 v87, v224, v225\n"
        "v_mul_f32 v162, v80, v162\n"
        "v_mfma_f32_16x16x32_bf16 v[112:115], a[224:227], a[192:195], 0\n"
        "ds_read_b128 a[224:227], v241 offset:1664\n"
        "v_mul_f32 v163, v80, v163\n"
        "v_mfma_f32_16x16x32_bf16 v[112:115], a[228:231], a[196:199], v[112:115]\n"
        "ds_read_b128 a[228:231], v240 offset:1792\n"
        "v_mul_f32 v164, v80, v164\n"
        "v_mfma_f32_16x16x32_bf16 v[112:115], a[232:235], a[200:203], v[112:115]\n"
        "ds_read_b128 a[232:235], v241 offset:1792\n"
        "v_cvt_pk_bf16_f32 v136, v100, v101\n"
        "v_mfma_f32_16x16x32_bf16 v[112:115], a[236:239], a[204:207], v[112:115]\n"
        "ds_read_b128 a[236:239], v240 offset:12288\n"
        "v_cvt_pk_bf16_f32 v137, v102, v103\n"
        "v_mfma_f32_16x16x32_bf16 v[112:115], a[240:243], a[208:211], v[112:115]\n"
        "ds_read_b128 a[240:243], v241 offset:12288\n"
        "v_mul_f32 v165, v80, v165\n"
        "v_mfma_f32_16x16x32_bf16 v[112:115], a[244:247], a[212:215], v[112:115]\n"
        "s_waitcnt lgkmcnt(9)\n"
        "ds_read_b128 a[244:247], v240 offset:12416\n"
        "v_cvt_pk_bf16_f32 v162, v162, v163\n"
        "v_mfma_f32_16x16x32_bf16 v[96:99], a[248:251], a[144:147], 0\n"
        "s_waitcnt lgkmcnt(9)\n"
        "ds_write_b128 v245, v[84:87] offset:768\n"
        "v_mfma_f32_16x16x32_bf16 v[96:99], a[216:219], a[148:151], v[96:99]\n"
        "s_waitcnt lgkmcnt(9)\n"
        "v_cvt_pk_bf16_f32 v163, v164, v165\n"
        "v_mfma_f32_16x16x32_bf16 v[96:99], a[220:223], a[152:155], v[96:99]\n"
        "s_waitcnt lgkmcnt(6)\n"
        "v_mfma_f32_16x16x32_bf16 v[96:99], a[224:227], a[156:159], v[96:99]\n"
        "s_waitcnt lgkmcnt(5)\n"
        "v_cvt_pk_bf16_f32 v144, v112, v113\n"
        "v_cvt_pk_bf16_f32 v145, v114, v115\n"
        "v_mfma_f32_16x16x32_bf16 v[96:99], a[228:231], a[160:163], v[96:99]\n"
        "s_waitcnt lgkmcnt(4)\n"
        "v_mfma_f32_16x16x32_bf16 v[96:99], a[232:235], a[164:167], v[96:99]\n"
        "ds_write_b64 v246, v[162:163] offset:768\n"
        "v_mfma_f32_16x16x32_bf16 v[100:103], a[248:251], a[168:171], 0\n"
        "v_mfma_f32_16x16x32_bf16 v[100:103], a[216:219], a[172:175], v[100:103]\n"
        "v_mfma_f32_16x16x32_bf16 v[100:103], a[220:223], a[176:179], v[100:103]\n"
        "v_mfma_f32_16x16x32_bf16 v[100:103], a[224:227], a[180:183], v[100:103]\n"
        "v_mfma_f32_16x16x32_bf16 v[100:103], a[228:231], a[184:187], v[100:103]\n"
        "v_mfma_f32_16x16x32_bf16 v[100:103], a[232:235], a[188:191], v[100:103]\n"
        "v_mfma_f32_16x16x32_bf16 v[112:115], a[248:251], a[192:195], 0\n"
        "ds_read_b128 a[248:251], v241 offset:12416\n"
        "ds_read_b128 v[162:165], v251 offset:0\n"
        "v_mfma_f32_16x16x32_bf16 v[112:115], a[216:219], a[196:199], v[112:115]\n"
        "ds_read_b128 a[216:219], v240 offset:12544\n"
        "v_cvt_pk_bf16_f32 v130, v96, v97\n"
        "v_mfma_f32_16x16x32_bf16 v[112:115], a[220:223], a[200:203], v[112:115]\n"
        "ds_read_b128 a[220:223], v241 offset:12544\n"
        "v_cvt_pk_bf16_f32 v131, v98, v99\n"
        "v_mfma_f32_16x16x32_bf16 v[112:115], a[224:227], a[204:207], v[112:115]\n"
        "v_cvt_pk_bf16_f32 v138, v100, v101\n"
        "v_cvt_pk_bf16_f32 v139, v102, v103\n"
        "v_mfma_f32_16x16x32_bf16 v[112:115], a[228:231], a[208:211], v[112:115]\n"
        "ds_read_b128 v[182:185], v251 offset:512\n"
        "v_mfma_f32_16x16x32_bf16 v[112:115], a[232:235], a[212:215], v[112:115]\n"
        "s_waitcnt lgkmcnt(9)\n"
        "v_mfma_f32_16x16x32_bf16 v[96:99], a[236:239], a[144:147], 0\n"
        "s_waitcnt lgkmcnt(8)\n"
        "v_mfma_f32_16x16x32_bf16 v[96:99], a[240:243], a[148:151], v[96:99]\n"
        "s_waitcnt lgkmcnt(7)\n"
        "v_mfma_f32_16x16x32_bf16 v[96:99], a[244:247], a[152:155], v[96:99]\n"
        "s_waitcnt lgkmcnt(4)\n"
        "ds_read_b128 v[202:205], v251 offset:4096\n"
        "v_mfma_f32_16x16x32_bf16 v[96:99], a[248:251], a[156:159], v[96:99]\n"
        "s_waitcnt lgkmcnt(3)\n"
        "v_mfma_f32_16x16x32_bf16 v[96:99], a[216:219], a[160:163], v[96:99]\n"
        "s_waitcnt lgkmcnt(2)\n"
        "v_mfma_f32_16x16x32_bf16 v[96:99], a[220:223], a[164:167], v[96:99]\n"
        "s_add_u32 s47, s47, 6291456\n"
        "v_cvt_pk_bf16_f32 v146, v112, v113\n"
        "v_cvt_pk_bf16_f32 v147, v114, v115\n"
        "v_mfma_f32_16x16x32_bf16 v[100:103], a[236:239], a[168:171], 0\n"
        "ds_read_b128 v[222:225], v251 offset:4608\n"
        "s_add_u32 s36, s36, 2097152\n"
        "v_mfma_f32_16x16x32_bf16 v[100:103], a[240:243], a[172:175], v[100:103]\n"
        "s_xor_b32 s48, s48, 8192\n"
        "v_cvt_pk_bf16_f32 v132, v96, v97\n"
        "v_cvt_pk_bf16_f32 v133, v98, v99\n"
        "v_mfma_f32_16x16x32_bf16 v[100:103], a[244:247], a[176:179], v[100:103]\n"
        "v_pk_min_u16 v162, v162, s26\n"
        "v_pk_min_u16 v163, v163, s26\n"
        "v_mfma_f32_16x16x32_bf16 v[100:103], a[248:251], a[180:183], v[100:103]\n"
        "v_pk_min_u16 v164, v164, s26\n"
        "v_pk_min_u16 v165, v165, s26\n"
        "v_mfma_f32_16x16x32_bf16 v[100:103], a[216:219], a[184:187], v[100:103]\n"
        "v_add_u32 v240, s31, v240\n"
        "v_add_u32 v241, s31, v241\n"
        "v_mfma_f32_16x16x32_bf16 v[100:103], a[220:223], a[188:191], v[100:103]\n"
        "v_mfma_f32_16x16x32_bf16 v[112:115], a[236:239], a[192:195], 0\n"
        "s_waitcnt lgkmcnt(2)\n"
        "v_pk_min_u16 v182, v182, s26\n"
        "v_pk_min_u16 v183, v183, s26\n"
        "v_mfma_f32_16x16x32_bf16 v[112:115], a[240:243], a[196:199], v[112:115]\n"
        "v_pk_min_u16 v184, v184, s26\n"
        "v_pk_min_u16 v185, v185, s26\n"
        "v_mfma_f32_16x16x32_bf16 v[112:115], a[244:247], a[200:203], v[112:115]\n"
        "v_mfma_f32_16x16x32_bf16 v[112:115], a[248:251], a[204:207], v[112:115]\n"
        "v_cvt_pk_bf16_f32 v140, v100, v101\n"
        "v_cvt_pk_bf16_f32 v141, v102, v103\n"
        "v_mfma_f32_16x16x32_bf16 v[112:115], a[216:219], a[208:211], v[112:115]\n"
        "s_waitcnt lgkmcnt(1)\n"
        "v_pk_min_u16 v202, v202, s26\n"
        "v_pk_min_u16 v203, v203, s26\n"
        "v_mfma_f32_16x16x32_bf16 v[112:115], a[220:223], a[212:215], v[112:115]\n"
        "v_pk_min_u16 v204, v204, s26\n"
        "v_pk_min_u16 v205, v205, s26\n"
        "v_mfma_f32_16x16x32_bf16 v[64:67], v[152:155], v[156:159], v[0:3]\n"
        "v_mfma_f32_16x16x32_bf16 v[64:67], v[168:171], v[160:163], v[64:67]\n"
        "v_mfma_f32_16x16x32_bf16 v[68:71], v[172:175], v[156:159], v[4:7]\n"
        "s_waitcnt lgkmcnt(0)\n"
        "v_pk_min_u16 v222, v222, s26\n"
        "v_pk_min_u16 v223, v223, s26\n"
        "v_mfma_f32_16x16x32_bf16 v[68:71], v[188:191], v[164:167], v[68:71]\n"
        "ds_read_b128 v[162:165], v252 offset:0\n"
        "v_cvt_pk_bf16_f32 v148, v112, v113\n"
        "v_mfma_f32_16x16x32_bf16 v[80:83], v[152:155], v[176:179], v[16:19]\n"
        "v_cvt_pk_bf16_f32 v149, v114, v115\n"
        "v_pk_min_u16 v224, v224, s26\n"
        "v_mfma_f32_16x16x32_bf16 v[80:83], v[168:171], v[180:183], v[80:83]\n"
        "v_pk_min_u16 v225, v225, s26\n"
        "v_max3_f32 v232, v64, v65, v66\n"
        "v_mfma_f32_16x16x32_bf16 v[84:87], v[172:175], v[176:179], v[20:23]\n"
        "v_max3_f32 v232, v232, v67, v68\n"
        "v_max3_f32 v232, v232, v69, v70\n"
        "v_mfma_f32_16x16x32_bf16 v[84:87], v[188:191], v[184:187], v[84:87]\n"
        "ds_read_b128 v[182:185], v252 offset:512\n"
        "v_mfma_f32_16x16x32_bf16 v[96:99], v[152:155], v[196:199], v[32:35]\n"
        "v_max3_f32 v233, v80, v81, v82\n"
        "v_mfma_f32_16x16x32_bf16 v[96:99], v[168:171], v[200:203], v[96:99]\n"
        "v_mfma_f32_16x16x32_bf16 v[100:103], v[172:175], v[196:199], v[36:39]\n"
        "v_mfma_f32_16x16x32_bf16 v[100:103], v[188:191], v[204:207], v[100:103]\n"
        "ds_read_b128 v[202:205], v252 offset:4096\n"
        "v_mfma_f32_16x16x32_bf16 v[112:115], v[152:155], v[216:219], v[48:51]\n"
        "s_waitcnt lgkmcnt(2)\n"
        "v_pk_min_u16 v162, v162, s26\n"
        "v_pk_min_u16 v163, v163, s26\n"
        "v_mfma_f32_16x16x32_bf16 v[112:115], v[168:171], v[220:223], v[112:115]\n"
        "v_pk_min_u16 v164, v164, s26\n"
        "v_pk_min_u16 v165, v165, s26\n"
        "v_mfma_f32_16x16x32_bf16 v[116:119], v[172:175], v[216:219], v[52:55]\n"
        "v_max3_f32 v233, v233, v83, v84\n"
        "v_max3_f32 v233, v233, v85, v86\n"
        "v_mfma_f32_16x16x32_bf16 v[116:119], v[188:191], v[224:227], v[116:119]\n"
        "ds_read_b128 v[222:225], v252 offset:4608\n"
        "v_max3_f32 v234, v96, v97, v98\n"
        "v_mfma_f32_16x16x32_bf16 v[72:75], v[192:195], v[156:159], v[8:11]\n"
        "s_waitcnt lgkmcnt(2)\n"
        "v_pk_min_u16 v182, v182, s26\n"
        "v_pk_min_u16 v183, v183, s26\n"
        "v_mfma_f32_16x16x32_bf16 v[72:75], v[208:211], v[160:163], v[72:75]\n"
        "v_pk_min_u16 v184, v184, s26\n"
        "v_pk_min_u16 v185, v185, s26\n"
        "v_mfma_f32_16x16x32_bf16 v[76:79], v[212:215], v[156:159], v[12:15]\n"
        "v_max3_f32 v234, v234, v99, v100\n"
        "v_max3_f32 v234, v234, v101, v102\n"
        "v_mfma_f32_16x16x32_bf16 v[76:79], v[228:231], v[164:167], v[76:79]\n"
        "v_max3_f32 v235, v112, v113, v114\n"
        "v_max3_f32 v235, v235, v115, v116\n"
        "v_mfma_f32_16x16x32_bf16 v[88:91], v[192:195], v[176:179], v[24:27]\n"
        "s_barrier\n"
        "ds_read_b128 v[152:155], v247 offset:0\n"
        "ds_read_b128 v[160:163], v247 offset:4096\n"
        "v_mfma_f32_16x16x32_bf16 v[88:91], v[208:211], v[180:183], v[88:91]\n"
        "ds_read_b128 v[156:159], v247 offset:256\n"
        "ds_read_b128 v[164:167], v247 offset:4352\n"
        "v_mfma_f32_16x16x32_bf16 v[92:95], v[212:215], v[176:179], v[28:31]\n"
        "ds_read_b128 a[216:219], v248 offset:0\n"
        "ds_read_b128 a[220:223], v248 offset:6144\n"
        "v_mfma_f32_16x16x32_bf16 v[92:95], v[228:231], v[184:187], v[92:95]\n"
        "s_waitcnt lgkmcnt(7)\n"
        "v_pk_min_u16 v202, v202, s26\n"
        "v_pk_min_u16 v203, v203, s26\n"
        "v_mfma_f32_16x16x32_bf16 v[104:107], v[192:195], v[196:199], v[40:43]\n"
        "ds_read_b128 a[224:227], v254 offset:16\n"
        "ds_read_b128 a[228:231], v254 offset:6160\n"
        "v_mfma_f32_16x16x32_bf16 v[104:107], v[208:211], v[200:203], v[104:107]\n"
        "v_pk_min_u16 v204, v204, s26\n"
        "v_pk_min_u16 v205, v205, s26\n"
        "v_mfma_f32_16x16x32_bf16 v[108:111], v[212:215], v[196:199], v[44:47]\n"
        "s_waitcnt lgkmcnt(8)\n"
        "v_pk_min_u16 v222, v222, s26\n"
        "v_pk_min_u16 v223, v223, s26\n"
        "v_mfma_f32_16x16x32_bf16 v[108:111], v[228:231], v[204:207], v[108:111]\n"
        "v_pk_min_u16 v224, v224, s26\n"
        "v_pk_min_u16 v225, v225, s26\n"
        "v_mfma_f32_16x16x32_bf16 v[120:123], v[192:195], v[216:219], v[56:59]\n"
        "ds_read_b128 a[232:235], v248 offset:32\n"
        "v_max3_f32 v232, v232, v71, v72\n"
        "v_mfma_f32_16x16x32_bf16 v[120:123], v[208:211], v[220:223], v[120:123]\n"
        "v_max3_f32 v232, v232, v73, v74\n"
        "v_max3_f32 v232, v232, v75, v76\n"
        "v_mfma_f32_16x16x32_bf16 v[124:127], v[212:215], v[216:219], v[60:63]\n"
        "v_max3_f32 v232, v232, v77, v78\n"
        "v_max_f32 v232, v232, v79\n"
        "v_mfma_f32_16x16x32_bf16 v[124:127], v[228:231], v[224:227], v[124:127]\n"
        "s_waitcnt lgkmcnt(4)\n"
        "v_max3_f32 v233, v233, v87, v88\n"
        "v_max3_f32 v233, v233, v89, v90\n"
        "v_mfma_f32_16x16x32_bf16 v[172:175], v[152:155], a[216:219], 0\n"
        "s_waitcnt lgkmcnt(3)\n"
        "v_max3_f32 v233, v233, v91, v92\n"
        "v_max3_f32 v233, v233, v93, v94\n"
        "v_mfma_f32_16x16x32_bf16 v[172:175], v[160:163], a[220:223], v[172:175]\n"
        "v_max_f32 v233, v233, v95\n"
        "v_max3_f32 v234, v234, v103, v104\n"
        "v_mfma_f32_16x16x32_bf16 v[180:183], v[164:167], a[220:223], 0\n"
        "ds_read_b128 a[220:223], v248 offset:6176\n"
        "ds_read_b128 a[236:239], v247 offset:8192\n"
        "v_mfma_f32_16x16x32_bf16 v[180:183], v[156:159], a[216:219], v[180:183]\n"
        "s_waitcnt lgkmcnt(4)\n"
        "ds_read_b128 a[216:219], v254 offset:48\n"
        "ds_read_b128 a[244:247], v247 offset:12288\n"
        "v_mfma_f32_16x16x32_bf16 v[184:187], v[156:159], a[224:227], 0\n"
        "s_waitcnt lgkmcnt(5)\n"
        "ds_read_b128 a[240:243], v247 offset:8448\n"
        "ds_read_b128 a[248:251], v247 offset:12544\n"
        "v_mfma_f32_16x16x32_bf16 v[184:187], v[164:167], a[228:231], v[184:187]\n"
        "v_permlane32_swap_b32 v232, v233\n"
        "v_mfma_f32_16x16x32_bf16 v[176:179], v[160:163], a[228:231], 0\n"
        "ds_read_b128 a[228:231], v254 offset:6192\n"
        "v_max_f32 v232, v232, v233\n"
        "v_mfma_f32_16x16x32_bf16 v[176:179], v[152:155], a[224:227], v[176:179]\n"
        "s_waitcnt lgkmcnt(7)\n"
        "ds_read_b128 a[224:227], v248 offset:64\n"
        "v_mov_b32 v233, v232\n"
        "v_mfma_f32_16x16x32_bf16 v[212:215], v[152:155], a[232:235], 0\n"
        "s_waitcnt lgkmcnt(7)\n"
        "v_max3_f32 v234, v234, v105, v106\n"
        "v_max3_f32 v234, v234, v107, v108\n"
        "v_mfma_f32_16x16x32_bf16 v[212:215], v[160:163], a[220:223], v[212:215]\n"
        "v_permlane16_swap_b32 v232, v233\n"
        "v_mfma_f32_16x16x32_bf16 v[192:195], v[164:167], a[220:223], 0\n"
        "ds_read_b128 a[220:223], v248 offset:6208\n"
        "v_max_f32 v232, v232, v233\n"
        "v_mfma_f32_16x16x32_bf16 v[192:195], v[156:159], a[232:235], v[192:195]\n"
        "s_waitcnt lgkmcnt(6)\n"
        "ds_read_b128 a[232:235], v254 offset:80\n"
        "v_mul_f32 v236, s25, v232\n"
        "v_mfma_f32_16x16x32_bf16 v[196:199], v[156:159], a[216:219], 0\n"
        "s_waitcnt lgkmcnt(3)\n"
        "v_mul_f32 v237, s25, v232\n"
        "v_max3_f32 v234, v234, v109, v110\n"
        "v_mfma_f32_16x16x32_bf16 v[196:199], v[164:167], a[228:231], v[196:199]\n"
        "v_permlane32_swap_b32 v236, v237\n"
        "v_mfma_f32_16x16x32_bf16 v[216:219], v[160:163], a[228:231], 0\n"
        "ds_read_b128 a[228:231], v254 offset:6224\n"
        "v_max_f32 v234, v234, v111\n"
        "v_mfma_f32_16x16x32_bf16 v[216:219], v[152:155], a[216:219], v[216:219]\n"
        "s_waitcnt lgkmcnt(3)\n"
        "ds_read_b128 a[216:219], v248 offset:36864\n"
        "v_max3_f32 v235, v235, v117, v118\n"
        "v_mfma_f32_16x16x32_bf16 v[220:223], v[152:155], a[224:227], 0\n"
        "s_waitcnt lgkmcnt(3)\n"
        "v_max3_f32 v235, v235, v119, v120\n"
        "v_max3_f32 v235, v235, v121, v122\n"
        "v_mfma_f32_16x16x32_bf16 v[220:223], v[160:163], a[220:223], v[220:223]\n"
        "v_max3_f32 v235, v235, v123, v124\n"
        "v_max3_f32 v235, v235, v125, v126\n"
        "v_mfma_f32_16x16x32_bf16 v[200:203], v[164:167], a[220:223], 0\n"
        "ds_read_b128 a[220:223], v248 offset:43008\n"
        "v_max_f32 v235, v235, v127\n"
        "v_mfma_f32_16x16x32_bf16 v[200:203], v[156:159], a[224:227], v[200:203]\n"
        "s_waitcnt lgkmcnt(3)\n"
        "ds_read_b128 a[224:227], v254 offset:36880\n"
        "v_fma_f32 v64, v64, s24, v236\n"
        "v_mfma_f32_16x16x32_bf16 v[204:207], v[156:159], a[232:235], 0\n"
        "s_waitcnt lgkmcnt(3)\n"
        "v_permlane32_swap_b32 v234, v235\n"
        "v_mfma_f32_16x16x32_bf16 v[204:207], v[164:167], a[228:231], v[204:207]\n"
        "v_max_f32 v234, v234, v235\n"
        "v_mov_b32 v235, v234\n"
        "v_mfma_f32_16x16x32_bf16 v[224:227], v[160:163], a[228:231], 0\n"
        "ds_read_b128 a[228:231], v254 offset:43024\n"
        "v_fma_f32 v65, v65, s24, v236\n"
        "v_mfma_f32_16x16x32_bf16 v[224:227], v[152:155], a[232:235], v[224:227]\n"
        "s_waitcnt lgkmcnt(3)\n"
        "ds_read_b128 a[232:235], v248 offset:36896\n"
        "v_fma_f32 v66, v66, s24, v236\n"
        "v_mfma_f32_16x16x32_bf16 v[172:175], a[236:239], a[216:219], v[172:175]\n"
        "s_waitcnt lgkmcnt(3)\n"
        "v_permlane16_swap_b32 v234, v235\n"
        "v_mfma_f32_16x16x32_bf16 v[172:175], a[244:247], a[220:223], v[172:175]\n"
        "v_max_f32 v234, v234, v235\n"
        "v_mul_f32 v238, s25, v234\n"
        "v_mfma_f32_16x16x32_bf16 v[180:183], a[248:251], a[220:223], v[180:183]\n"
        "ds_read_b128 a[220:223], v248 offset:43040\n"
        "ds_read_b128 v[152:155], v247 offset:16384\n"
        "v_mfma_f32_16x16x32_bf16 v[180:183], a[240:243], a[216:219], v[180:183]\n"
        "s_waitcnt lgkmcnt(4)\n"
        "ds_read_b128 a[216:219], v254 offset:36912\n"
        "ds_read_b128 v[160:163], v247 offset:20480\n"
        "v_mfma_f32_16x16x32_bf16 v[184:187], a[240:243], a[224:227], v[184:187]\n"
        "s_waitcnt lgkmcnt(5)\n"
        "ds_read_b128 v[156:159], v247 offset:16640\n"
        "ds_read_b128 v[164:167], v247 offset:20736\n"
        "v_mfma_f32_16x16x32_bf16 v[184:187], a[248:251], a[228:231], v[184:187]\n"
        "v_mul_f32 v239, s25, v234\n"
        "v_fma_f32 v67, v67, s24, v236\n"
        "v_mfma_f32_16x16x32_bf16 v[176:179], a[244:247], a[228:231], v[176:179]\n"
        "ds_read_b128 a[228:231], v254 offset:43056\n"
        "v_fma_f32 v68, v68, s24, v236\n"
        "v_mfma_f32_16x16x32_bf16 v[176:179], a[236:239], a[224:227], v[176:179]\n"
        "s_waitcnt lgkmcnt(7)\n"
        "ds_read_b128 a[224:227], v248 offset:36928\n"
        "v_fma_f32 v69, v69, s24, v236\n"
        "v_mfma_f32_16x16x32_bf16 v[212:215], a[236:239], a[232:235], v[212:215]\n"
        "s_waitcnt lgkmcnt(7)\n"
        "v_permlane32_swap_b32 v238, v239\n"
        "v_mfma_f32_16x16x32_bf16 v[212:215], a[244:247], a[220:223], v[212:215]\n"
        "v_exp_f32 v64, v64\n"
        "v_mfma_f32_16x16x32_bf16 v[192:195], a[248:251], a[220:223], v[192:195]\n"
        "ds_read_b128 a[220:223], v248 offset:43072\n"
        "v_fma_f32 v70, v70, s24, v236\n"
        "v_mfma_f32_16x16x32_bf16 v[192:195], a[240:243], a[232:235], v[192:195]\n"
        "s_waitcnt lgkmcnt(6)\n"
        "ds_read_b128 a[232:235], v254 offset:36944\n"
        "v_fma_f32 v71, v71, s24, v236\n"
        "v_mfma_f32_16x16x32_bf16 v[196:199], a[240:243], a[216:219], v[196:199]\n"
        "s_waitcnt lgkmcnt(3)\n"
        "v_exp_f32 v65, v65\n"
        "v_mfma_f32_16x16x32_bf16 v[196:199], a[248:251], a[228:231], v[196:199]\n"
        "v_exp_f32 v66, v66\n"
        "v_mfma_f32_16x16x32_bf16 v[216:219], a[244:247], a[228:231], v[216:219]\n"
        "ds_read_b128 a[228:231], v254 offset:43088\n"
        "v_cvt_pk_bf16_f32 v64, v64, v65\n"
        "v_mfma_f32_16x16x32_bf16 v[216:219], a[236:239], a[216:219], v[216:219]\n"
        "s_waitcnt lgkmcnt(3)\n"
        "ds_read_b128 a[216:219], v248 offset:12288\n"
        "v_fma_f32 v72, v72, s24, v236\n"
        "v_mfma_f32_16x16x32_bf16 v[220:223], a[236:239], a[224:227], v[220:223]\n"
        "s_waitcnt lgkmcnt(3)\n"
        "v_exp_f32 v67, v67\n"
        "v_mfma_f32_16x16x32_bf16 v[220:223], a[244:247], a[220:223], v[220:223]\n"
        "v_exp_f32 v68, v68\n"
        "v_mfma_f32_16x16x32_bf16 v[200:203], a[248:251], a[220:223], v[200:203]\n"
        "ds_read_b128 a[220:223], v248 offset:49152\n"
        "v_cvt_pk_bf16_f32 v65, v66, v67\n"
        "v_mfma_f32_16x16x32_bf16 v[200:203], a[240:243], a[224:227], v[200:203]\n"
        "s_waitcnt lgkmcnt(3)\n"
        "ds_read_b128 a[224:227], v254 offset:12304\n"
        "v_fma_f32 v73, v73, s24, v236\n"
        "v_mfma_f32_16x16x32_bf16 v[204:207], a[240:243], a[232:235], v[204:207]\n"
        "s_waitcnt lgkmcnt(3)\n"
        "v_exp_f32 v69, v69\n"
        "v_mfma_f32_16x16x32_bf16 v[204:207], a[248:251], a[228:231], v[204:207]\n"
        "v_exp_f32 v70, v70\n"
        "v_mfma_f32_16x16x32_bf16 v[224:227], a[244:247], a[228:231], v[224:227]\n"
        "ds_read_b128 a[228:231], v254 offset:49168\n"
        "v_cvt_pk_bf16_f32 v66, v68, v69\n"
        "v_mfma_f32_16x16x32_bf16 v[224:227], a[236:239], a[232:235], v[224:227]\n"
        "s_waitcnt lgkmcnt(3)\n"
        "ds_read_b128 a[232:235], v248 offset:12320\n"
        "v_fma_f32 v74, v74, s24, v236\n"
        "v_mfma_f32_16x16x32_bf16 v[172:175], v[152:155], a[216:219], v[172:175]\n"
        "s_waitcnt lgkmcnt(3)\n"
        "v_exp_f32 v71, v71\n"
        "v_mfma_f32_16x16x32_bf16 v[172:175], v[160:163], a[220:223], v[172:175]\n"
        "v_exp_f32 v72, v72\n"
        "v_mfma_f32_16x16x32_bf16 v[180:183], v[164:167], a[220:223], v[180:183]\n"
        "ds_read_b128 a[220:223], v248 offset:49184\n"
        "v_cvt_pk_bf16_f32 v67, v70, v71\n"
        "v_mfma_f32_16x16x32_bf16 v[180:183], v[156:159], a[216:219], v[180:183]\n"
        "s_waitcnt lgkmcnt(3)\n"
        "ds_read_b128 a[216:219], v254 offset:12336\n"
        "v_fma_f32 v75, v75, s24, v236\n"
        "v_mfma_f32_16x16x32_bf16 v[184:187], v[156:159], a[224:227], v[184:187]\n"
        "s_waitcnt lgkmcnt(3)\n"
        "v_exp_f32 v73, v73\n"
        "v_mfma_f32_16x16x32_bf16 v[184:187], v[164:167], a[228:231], v[184:187]\n"
        "v_cvt_pk_bf16_f32 v68, v72, v73\n"
        "v_xor_b32 v251, 0x2000, v251\n"
        "v_mfma_f32_16x16x32_bf16 v[176:179], v[160:163], a[228:231], v[176:179]\n"
        "ds_read_b128 a[228:231], v254 offset:49200\n"
        "v_xor_b32 v252, 0x2000, v252\n"
        "v_mfma_f32_16x16x32_bf16 v[176:179], v[152:155], a[224:227], v[176:179]\n"
        "s_waitcnt lgkmcnt(3)\n"
        "ds_read_b128 a[224:227], v248 offset:12352\n"
        "v_add_u32 v245, s45, v245\n"
        "v_mfma_f32_16x16x32_bf16 v[212:215], v[152:155], a[232:235], v[212:215]\n"
        "s_waitcnt lgkmcnt(3)\n"
        "s_waitcnt vmcnt(0)\n"
        "s_barrier\n"
        "v_exp_f32 v74, v74\n"
        "v_mfma_f32_16x16x32_bf16 v[212:215], v[160:163], a[220:223], v[212:215]\n"
        "v_exp_f32 v75, v75\n"
        "v_mfma_f32_16x16x32_bf16 v[192:195], v[164:167], a[220:223], v[192:195]\n"
        "ds_read_b128 a[220:223], v248 offset:49216\n"
        "ds_read_b128 a[236:239], v240 offset:0\n"
        "v_mfma_f32_16x16x32_bf16 v[192:195], v[156:159], a[232:235], v[192:195]\n"
        "s_waitcnt lgkmcnt(4)\n"
        "ds_read_b128 a[232:235], v254 offset:12368\n"
        "ds_read_b128 a[240:243], v241 offset:0\n"
        "v_mfma_f32_16x16x32_bf16 v[196:199], v[156:159], a[216:219], v[196:199]\n"
        "s_waitcnt lgkmcnt(5)\n"
        "ds_read_b128 a[244:247], v240 offset:128\n"
        "v_cvt_pk_bf16_f32 v69, v74, v75\n"
        "v_mfma_f32_16x16x32_bf16 v[196:199], v[164:167], a[228:231], v[196:199]\n"
        "ds_read_b128 a[248:251], v241 offset:128\n"
        "v_add_u32 v246, s45, v246\n"
        "v_mfma_f32_16x16x32_bf16 v[216:219], v[160:163], a[228:231], v[216:219]\n"
        "ds_read_b128 a[228:231], v254 offset:49232\n"
        "v_add_u32 v247, s45, v247\n"
        "v_mfma_f32_16x16x32_bf16 v[216:219], v[152:155], a[216:219], v[216:219]\n"
        "s_waitcnt lgkmcnt(7)\n"
        "ds_read_b128 a[216:219], v240 offset:256\n"
        "v_mfma_f32_16x16x32_bf16 v[220:223], v[152:155], a[224:227], v[220:223]\n"
        "s_waitcnt lgkmcnt(7)\n"
        "v_mfma_f32_16x16x32_bf16 v[220:223], v[160:163], a[220:223], v[220:223]\n"
        "v_mfma_f32_16x16x32_bf16 v[200:203], v[164:167], a[220:223], v[200:203]\n"
        "ds_read_b128 a[220:223], v241 offset:256\n"
        "v_mfma_f32_16x16x32_bf16 v[200:203], v[156:159], a[224:227], v[200:203]\n"
        "s_waitcnt lgkmcnt(6)\n"
        "v_mfma_f32_16x16x32_bf16 v[204:207], v[156:159], a[232:235], v[204:207]\n"
        "s_waitcnt lgkmcnt(2)\n"
        "v_mfma_f32_16x16x32_bf16 v[204:207], v[164:167], a[228:231], v[204:207]\n"
        "v_mfma_f32_16x16x32_bf16 v[224:227], v[160:163], a[228:231], v[224:227]\n"
        "v_mfma_f32_16x16x32_bf16 v[224:227], v[152:155], a[232:235], v[224:227]\n"
        "s_waitcnt lgkmcnt(0)\n"
        ".p2align 6\n"
        "10:\n"
        "v_mfma_f32_16x16x32_bf16 v[162:165], a[72:75], a[236:239], 0\n"
        "v_fma_f32 v76, v76, s24, v236\n"
        "v_fma_f32 v77, v77, s24, v236\n"
        "v_mfma_f32_16x16x32_bf16 v[162:165], a[76:79], a[240:243], v[162:165]\n"
        "s_waitcnt lgkmcnt(3)\n"
        "s_add_u32 s41, s30, s40\n"
        "v_cvt_pk_bf16_f32 v232, v172, v176\n"
        "v_cvt_pk_bf16_f32 v233, v212, v216\n"
        "v_mfma_f32_16x16x32_bf16 v[162:165], a[80:83], a[244:247], v[162:165]\n"
        "s_waitcnt lgkmcnt(2)\n"
        "v_cvt_pk_bf16_f32 v234, v220, v224\n"
        "v_fma_f32 v78, v78, s24, v236\n"
        "v_mfma_f32_16x16x32_bf16 v[162:165], a[84:87], a[248:251], v[162:165]\n"
        "s_waitcnt lgkmcnt(1)\n"
        "buffer_store_dwordx3 v[232:234], v250, s[16:19], s38 offen offset:0 sc1\n"
        "v_mfma_f32_16x16x32_bf16 v[162:165], a[88:91], a[216:219], v[162:165]\n"
        "s_waitcnt lgkmcnt(0)\n"
        "v_exp_f32 v76, v76\n"
        "v_mfma_f32_16x16x32_bf16 v[162:165], a[92:95], a[220:223], v[162:165]\n"
        "v_exp_f32 v77, v77\n"
        "v_mfma_f32_16x16x32_bf16 v[72:75], a[116:119], a[220:223], 0\n"
        "v_fma_f32 v79, v79, s24, v236\n"
        "v_cvt_pk_bf16_f32 v70, v76, v77\n"
        "v_mfma_f32_16x16x32_bf16 v[72:75], a[112:115], a[216:219], v[72:75]\n"
        "v_exp_f32 v78, v78\n"
        "v_mfma_f32_16x16x32_bf16 v[72:75], a[108:111], a[248:251], v[72:75]\n"
        "v_exp_f32 v79, v79\n"
        "v_mfma_f32_16x16x32_bf16 v[72:75], a[104:107], a[244:247], v[72:75]\n"
        "v_cvt_pk_bf16_f32 v71, v78, v79\n"
        "v_cvt_pk_bf16_f32 v232, v173, v177\n"
        "v_mfma_f32_16x16x32_bf16 v[72:75], a[100:103], a[240:243], v[72:75]\n"
        "v_cvt_pk_bf16_f32 v233, v213, v217\n"
        "v_cvt_pk_bf16_f32 v234, v221, v225\n"
        "v_mfma_f32_16x16x32_bf16 v[72:75], a[96:99], a[236:239], v[72:75]\n"
        "buffer_store_dwordx3 v[232:234], v250, s[16:19], s38 offen offset:384 sc1\n"
        "v_mfma_f32_16x16x32_bf16 v[76:79], a[120:123], a[236:239], 0\n"
        "s_add_u32 s35, s47, 786432\n"
        "v_cvt_pk_bf16_f32 v152, v162, v163\n"
        "v_cvt_pk_bf16_f32 v153, v164, v165\n"
        "v_mfma_f32_16x16x32_bf16 v[76:79], a[124:127], a[240:243], v[76:79]\n"
        "s_sub_u32 s46, s35, 100663296\n"
        "v_fma_f32 v80, v80, s24, v237\n"
        "v_cvt_pk_bf16_f32 v154, v72, v73\n"
        "v_mfma_f32_16x16x32_bf16 v[76:79], a[128:131], a[244:247], v[76:79]\n"
        "s_cmp_lt_u32 s46, 786432\n"
        "v_exp_f32 v80, v80\n"
        "v_mfma_f32_16x16x32_bf16 v[76:79], a[132:135], a[248:251], v[76:79]\n"
        "s_cselect_b32 s35, s46, s35\n"
        "v_cvt_pk_bf16_f32 v155, v74, v75\n"
        "v_cvt_pk_bf16_f32 v232, v174, v178\n"
        "v_mfma_f32_16x16x32_bf16 v[76:79], a[136:139], a[216:219], v[76:79]\n"
        "v_cvt_pk_bf16_f32 v233, v214, v218\n"
        "v_cvt_pk_bf16_f32 v234, v222, v226\n"
        "v_mfma_f32_16x16x32_bf16 v[76:79], a[140:143], a[220:223], v[76:79]\n"
        "v_fma_f32 v81, v81, s24, v237\n"
        "v_fma_f32 v82, v82, s24, v237\n"
        "v_mfma_f32_16x16x32_bf16 v[162:165], a[20:23], a[220:223], 0\n"
        "v_exp_f32 v81, v81\n"
        "v_mfma_f32_16x16x32_bf16 v[162:165], a[16:19], a[216:219], v[162:165]\n"
        "v_exp_f32 v82, v82\n"
        "v_mfma_f32_16x16x32_bf16 v[162:165], a[12:15], a[248:251], v[162:165]\n"
        "buffer_store_dwordx3 v[232:234], v250, s[16:19], s38 offen offset:768 sc1\n"
        "v_mfma_f32_16x16x32_bf16 v[162:165], a[8:11], a[244:247], v[162:165]\n"
        "v_cvt_pk_bf16_f32 v168, v76, v77\n"
        "v_cvt_pk_bf16_f32 v169, v78, v79\n"
        "v_mfma_f32_16x16x32_bf16 v[162:165], a[4:7], a[240:243], v[162:165]\n"
        "v_cvt_pk_bf16_f32 v80, v80, v81\n"
        "v_fma_f32 v83, v83, s24, v237\n"
        "v_mfma_f32_16x16x32_bf16 v[162:165], a[0:3], a[236:239], v[162:165]\n"
        "v_exp_f32 v83, v83\n"
        "v_mfma_f32_16x16x32_bf16 v[72:75], a[24:27], a[236:239], 0\n"
        "v_cvt_pk_bf16_f32 v232, v175, v179\n"
        "v_cvt_pk_bf16_f32 v233, v215, v219\n"
        "v_mfma_f32_16x16x32_bf16 v[72:75], a[28:31], a[240:243], v[72:75]\n"
        "v_cvt_pk_bf16_f32 v234, v223, v227\n"
        "v_cvt_pk_bf16_f32 v81, v82, v83\n"
        "v_mfma_f32_16x16x32_bf16 v[72:75], a[32:35], a[244:247], v[72:75]\n"
        "v_cvt_pk_bf16_f32 v156, v162, v163\n"
        "v_cvt_pk_bf16_f32 v157, v164, v165\n"
        "v_mfma_f32_16x16x32_bf16 v[72:75], a[36:39], a[248:251], v[72:75]\n"
        "v_fma_f32 v84, v84, s24, v237\n"
        "v_fma_f32 v85, v85, s24, v237\n"
        "v_mfma_f32_16x16x32_bf16 v[72:75], a[40:43], a[216:219], v[72:75]\n"
        "v_exp_f32 v84, v84\n"
        "v_mfma_f32_16x16x32_bf16 v[72:75], a[44:47], a[220:223], v[72:75]\n"
        "buffer_store_dwordx3 v[232:234], v250, s[16:19], s38 offen offset:1152 sc1\n"
        "v_mfma_f32_16x16x32_bf16 v[76:79], a[68:71], a[220:223], 0\n"
        "v_exp_f32 v85, v85\n"
        "v_mfma_f32_16x16x32_bf16 v[76:79], a[64:67], a[216:219], v[76:79]\n"
        "v_cvt_pk_bf16_f32 v82, v84, v85\n"
        "v_fma_f32 v86, v86, s24, v237\n"
        "v_mfma_f32_16x16x32_bf16 v[76:79], a[60:63], a[248:251], v[76:79]\n"
        "ds_read_b128 a[224:227], v240 offset:1536\n"
        "v_cvt_pk_bf16_f32 v158, v72, v73\n"
        "v_mfma_f32_16x16x32_bf16 v[76:79], a[56:59], a[244:247], v[76:79]\n"
        "ds_read_b128 a[228:231], v241 offset:1536\n"
        "v_cvt_pk_bf16_f32 v159, v74, v75\n"
        "v_mfma_f32_16x16x32_bf16 v[76:79], a[52:55], a[240:243], v[76:79]\n"
        "ds_read_b128 a[232:235], v240 offset:1664\n"
        "v_cvt_pk_bf16_f32 v232, v180, v184\n"
        "v_mfma_f32_16x16x32_bf16 v[76:79], a[48:51], a[236:239], v[76:79]\n"
        "v_cvt_pk_bf16_f32 v233, v192, v196\n"
        "v_cvt_pk_bf16_f32 v234, v200, v204\n"
        "v_mfma_f32_16x16x32_bf16 v[162:165], a[236:239], a[144:147], 0\n"
        "v_exp_f32 v86, v86\n"
        "v_mfma_f32_16x16x32_bf16 v[162:165], a[240:243], a[148:151], v[162:165]\n"
        "v_fma_f32 v87, v87, s24, v237\n"
        "v_fma_f32 v88, v88, s24, v237\n"
        "v_mfma_f32_16x16x32_bf16 v[162:165], a[244:247], a[152:155], v[162:165]\n"
        "buffer_store_dwordx3 v[232:234], v250, s[16:19], s39 offen offset:0 sc1\n"
        "v_mfma_f32_16x16x32_bf16 v[162:165], a[248:251], a[156:159], v[162:165]\n"
        "v_exp_f32 v87, v87\n"
        "v_mfma_f32_16x16x32_bf16 v[162:165], a[216:219], a[160:163], v[162:165]\n"
        "v_cvt_pk_bf16_f32 v160, v76, v77\n"
        "v_cvt_pk_bf16_f32 v161, v78, v79\n"
        "v_mfma_f32_16x16x32_bf16 v[162:165], a[220:223], a[164:167], v[162:165]\n"
        "v_cvt_pk_bf16_f32 v166, v76, v77\n"
        "v_cvt_pk_bf16_f32 v167, v78, v79\n"
        "v_mfma_f32_16x16x32_bf16 v[72:75], a[220:223], a[188:191], 0\n"
        "v_exp_f32 v88, v88\n"
        "v_mfma_f32_16x16x32_bf16 v[72:75], a[216:219], a[184:187], v[72:75]\n"
        "v_cvt_pk_bf16_f32 v232, v181, v185\n"
        "v_cvt_pk_bf16_f32 v233, v193, v197\n"
        "v_mfma_f32_16x16x32_bf16 v[72:75], a[248:251], a[180:183], v[72:75]\n"
        "v_cvt_pk_bf16_f32 v234, v201, v205\n"
        "v_cvt_pk_bf16_f32 v212, v162, v163\n"
        "v_mfma_f32_16x16x32_bf16 v[72:75], a[244:247], a[176:179], v[72:75]\n"
        "v_cvt_pk_bf16_f32 v213, v164, v165\n"
        "v_cvt_pk_bf16_f32 v83, v86, v87\n"
        "v_mfma_f32_16x16x32_bf16 v[72:75], a[240:243], a[172:175], v[72:75]\n"
        "v_fma_f32 v89, v89, s24, v237\n"
        "v_fma_f32 v90, v90, s24, v237\n"
        "v_mfma_f32_16x16x32_bf16 v[72:75], a[236:239], a[168:171], v[72:75]\n"
        "buffer_store_dwordx3 v[232:234], v250, s[16:19], s39 offen offset:384 sc1\n"
        "v_mfma_f32_16x16x32_bf16 v[76:79], a[236:239], a[192:195], 0\n"
        "ds_read_b128 a[236:239], v241 offset:1664\n"
        "v_fma_f32 v91, v91, s24, v237\n"
        "v_mfma_f32_16x16x32_bf16 v[76:79], a[240:243], a[196:199], v[76:79]\n"
        "ds_read_b128 a[240:243], v240 offset:1792\n"
        "v_fma_f32 v92, v92, s24, v237\n"
        "v_mfma_f32_16x16x32_bf16 v[76:79], a[244:247], a[200:203], v[76:79]\n"
        "ds_read_b128 a[244:247], v241 offset:1792\n"
        "v_cvt_pk_bf16_f32 v214, v72, v73\n"
        "v_mfma_f32_16x16x32_bf16 v[76:79], a[248:251], a[204:207], v[76:79]\n"
        "v_exp_f32 v89, v89\n"
        "v_mfma_f32_16x16x32_bf16 v[76:79], a[216:219], a[208:211], v[76:79]\n"
        "v_cvt_pk_bf16_f32 v215, v74, v75\n"
        "v_cvt_pk_bf16_f32 v232, v182, v186\n"
        "v_mfma_f32_16x16x32_bf16 v[76:79], a[220:223], a[212:215], v[76:79]\n"
        "s_waitcnt lgkmcnt(5)\n"
        "v_cvt_pk_bf16_f32 v233, v194, v198\n"
        "v_cvt_pk_bf16_f32 v234, v202, v206\n"
        "v_mfma_f32_16x16x32_bf16 v[162:165], a[72:75], a[224:227], 0\n"
        "s_waitcnt lgkmcnt(4)\n"
        "v_exp_f32 v90, v90\n"
        "v_mfma_f32_16x16x32_bf16 v[162:165], a[76:79], a[228:231], v[162:165]\n"
        "s_waitcnt lgkmcnt(3)\n"
        "v_exp_f32 v91, v91\n"
        "v_mfma_f32_16x16x32_bf16 v[162:165], a[80:83], a[232:235], v[162:165]\n"
        "s_waitcnt lgkmcnt(2)\n"
        "buffer_store_dwordx3 v[232:234], v250, s[16:19], s39 offen offset:768 sc1\n"
        "v_mfma_f32_16x16x32_bf16 v[162:165], a[84:87], a[236:239], v[162:165]\n"
        "s_waitcnt lgkmcnt(1)\n"
        "v_exp_f32 v92, v92\n"
        "v_mfma_f32_16x16x32_bf16 v[162:165], a[88:91], a[240:243], v[162:165]\n"
        "s_waitcnt lgkmcnt(0)\n"
        "v_cvt_pk_bf16_f32 v216, v76, v77\n"
        "v_cvt_pk_bf16_f32 v217, v78, v79\n"
        "v_mfma_f32_16x16x32_bf16 v[162:165], a[92:95], a[244:247], v[162:165]\n"
        "v_cvt_pk_bf16_f32 v232, v183, v187\n"
        "v_cvt_pk_bf16_f32 v233, v195, v199\n"
        "v_mfma_f32_16x16x32_bf16 v[72:75], a[116:119], a[244:247], 0\n"
        "v_cvt_pk_bf16_f32 v234, v203, v207\n"
        "v_cvt_pk_bf16_f32 v84, v88, v89\n"
        "v_mfma_f32_16x16x32_bf16 v[72:75], a[112:115], a[240:243], v[72:75]\n"
        "v_cvt_pk_bf16_f32 v85, v90, v91\n"
        "v_fma_f32 v93, v93, s24, v237\n"
        "v_mfma_f32_16x16x32_bf16 v[72:75], a[108:111], a[236:239], v[72:75]\n"
        "v_exp_f32 v93, v93\n"
        "v_mfma_f32_16x16x32_bf16 v[72:75], a[104:107], a[232:235], v[72:75]\n"
        "v_cvt_pk_bf16_f32 v172, v162, v163\n"
        "v_cvt_pk_bf16_f32 v173, v164, v165\n"
        "v_mfma_f32_16x16x32_bf16 v[72:75], a[100:103], a[228:231], v[72:75]\n"
        "v_cvt_pk_bf16_f32 v86, v92, v93\n"
        "v_fma_f32 v94, v94, s24, v237\n"
        "v_mfma_f32_16x16x32_bf16 v[72:75], a[96:99], a[224:227], v[72:75]\n"
        "buffer_store_dwordx3 v[232:234], v250, s[16:19], s39 offen offset:1152 sc1\n"
        "v_mfma_f32_16x16x32_bf16 v[76:79], a[120:123], a[224:227], 0\n"
        "v_exp_f32 v94, v94\n"
        "v_mfma_f32_16x16x32_bf16 v[76:79], a[124:127], a[228:231], v[76:79]\n"
        "v_fma_f32 v95, v95, s24, v237\n"
        "v_fma_f32 v96, v96, s24, v238\n"
        "v_mfma_f32_16x16x32_bf16 v[76:79], a[128:131], a[232:235], v[76:79]\n"
        "v_exp_f32 v95, v95\n"
        "v_mfma_f32_16x16x32_bf16 v[76:79], a[132:135], a[236:239], v[76:79]\n"
        "v_exp_f32 v96, v96\n"
        "v_mfma_f32_16x16x32_bf16 v[76:79], a[136:139], a[240:243], v[76:79]\n"
        "v_cvt_pk_bf16_f32 v174, v72, v73\n"
        "v_cvt_pk_bf16_f32 v175, v74, v75\n"
        "v_mfma_f32_16x16x32_bf16 v[76:79], a[140:143], a[244:247], v[76:79]\n"
        "v_cvt_pk_bf16_f32 v87, v94, v95\n"
        "v_fma_f32 v97, v97, s24, v238\n"
        "v_mfma_f32_16x16x32_bf16 v[162:165], a[20:23], a[244:247], 0\n"
        "s_mov_b32 m0, s48\n"
        "v_exp_f32 v97, v97\n"
        "v_mfma_f32_16x16x32_bf16 v[162:165], a[16:19], a[240:243], v[162:165]\n"
        "v_cvt_pk_bf16_f32 v96, v96, v97\n"
        "v_fma_f32 v98, v98, s24, v238\n"
        "v_mfma_f32_16x16x32_bf16 v[162:165], a[12:15], a[236:239], v[162:165]\n"
        "buffer_load_dwordx4 v249, s[12:15], s36 offen offset:0 lds\n"
        "v_mfma_f32_16x16x32_bf16 v[162:165], a[8:11], a[232:235], v[162:165]\n"
        "v_exp_f32 v98, v98\n"
        "v_mfma_f32_16x16x32_bf16 v[162:165], a[4:7], a[228:231], v[162:165]\n"
        "v_cvt_pk_bf16_f32 v190, v76, v77\n"
        "v_cvt_pk_bf16_f32 v191, v78, v79\n"
        "v_mfma_f32_16x16x32_bf16 v[162:165], a[0:3], a[224:227], v[162:165]\n"
        "v_fma_f32 v99, v99, s24, v238\n"
        "v_fma_f32 v100, v100, s24, v238\n"
        "v_mfma_f32_16x16x32_bf16 v[72:75], a[24:27], a[224:227], 0\n"
        "v_exp_f32 v99, v99\n"
        "v_mfma_f32_16x16x32_bf16 v[72:75], a[28:31], a[228:231], v[72:75]\n"
        "v_exp_f32 v100, v100\n"
        "v_mfma_f32_16x16x32_bf16 v[72:75], a[32:35], a[232:235], v[72:75]\n"
        "v_cvt_pk_bf16_f32 v97, v98, v99\n"
        "v_cvt_pk_bf16_f32 v176, v162, v163\n"
        "v_mfma_f32_16x16x32_bf16 v[72:75], a[36:39], a[236:239], v[72:75]\n"
        "v_cvt_pk_bf16_f32 v177, v164, v165\n"
        "v_fma_f32 v101, v101, s24, v238\n"
        "v_mfma_f32_16x16x32_bf16 v[72:75], a[40:43], a[240:243], v[72:75]\n"
        "v_exp_f32 v101, v101\n"
        "v_mfma_f32_16x16x32_bf16 v[72:75], a[44:47], a[244:247], v[72:75]\n"
        "v_cvt_pk_bf16_f32 v98, v100, v101\n"
        "v_fma_f32 v102, v102, s24, v238\n"
        "v_mfma_f32_16x16x32_bf16 v[76:79], a[68:71], a[244:247], 0\n"
        "v_xor_b32 v253, 32, v249\n"
        "v_fma_f32 v103, v103, s24, v238\n"
        "v_mfma_f32_16x16x32_bf16 v[76:79], a[64:67], a[240:243], v[76:79]\n"
        "v_exp_f32 v102, v102\n"
        "v_mfma_f32_16x16x32_bf16 v[76:79], a[60:63], a[236:239], v[76:79]\n"
        "ds_read_b128 a[248:251], v240 offset:12288\n"
        "v_cvt_pk_bf16_f32 v178, v72, v73\n"
        "v_mfma_f32_16x16x32_bf16 v[76:79], a[56:59], a[232:235], v[76:79]\n"
        "ds_read_b128 a[216:219], v241 offset:12288\n"
        "v_cvt_pk_bf16_f32 v179, v74, v75\n"
        "v_mfma_f32_16x16x32_bf16 v[76:79], a[52:55], a[228:231], v[76:79]\n"
        "buffer_load_dwordx4 v253, s[12:15], s36 offen offset:1024 lds\n"
        "v_mfma_f32_16x16x32_bf16 v[76:79], a[48:51], a[224:227], v[76:79]\n"
        "ds_read_b128 a[220:223], v240 offset:12416\n"
        "v_fma_f32 v104, v104, s24, v238\n"
        "v_mfma_f32_16x16x32_bf16 v[162:165], a[224:227], a[144:147], 0\n"
        "v_exp_f32 v103, v103\n"
        "v_mfma_f32_16x16x32_bf16 v[162:165], a[228:231], a[148:151], v[162:165]\n"
        "v_exp_f32 v104, v104\n"
        "v_mfma_f32_16x16x32_bf16 v[162:165], a[232:235], a[152:155], v[162:165]\n"
        "v_cvt_pk_bf16_f32 v99, v102, v103\n"
        "v_cvt_pk_bf16_f32 v180, v76, v77\n"
        "v_mfma_f32_16x16x32_bf16 v[162:165], a[236:239], a[156:159], v[162:165]\n"
        "v_cvt_pk_bf16_f32 v181, v78, v79\n"
        "v_cvt_pk_bf16_f32 v186, v76, v77\n"
        "v_mfma_f32_16x16x32_bf16 v[162:165], a[240:243], a[160:163], v[162:165]\n"
        "v_cvt_pk_bf16_f32 v187, v78, v79\n"
        "v_fma_f32 v105, v105, s24, v238\n"
        "v_mfma_f32_16x16x32_bf16 v[162:165], a[244:247], a[164:167], v[162:165]\n"
        "v_exp_f32 v105, v105\n"
        "v_mfma_f32_16x16x32_bf16 v[72:75], a[244:247], a[188:191], 0\n"
        "v_cvt_pk_bf16_f32 v100, v104, v105\n"
        "v_fma_f32 v106, v106, s24, v238\n"
        "v_mfma_f32_16x16x32_bf16 v[72:75], a[240:243], a[184:187], v[72:75]\n"
        "v_exp_f32 v106, v106\n"
        "v_mfma_f32_16x16x32_bf16 v[72:75], a[236:239], a[180:183], v[72:75]\n"
        "v_fma_f32 v107, v107, s24, v238\n"
        "v_cvt_pk_bf16_f32 v218, v162, v163\n"
        "v_mfma_f32_16x16x32_bf16 v[72:75], a[232:235], a[176:179], v[72:75]\n"
        "v_exp_f32 v107, v107\n"
        "v_mfma_f32_16x16x32_bf16 v[72:75], a[228:231], a[172:175], v[72:75]\n"
        "s_add_u32 m0, s41, 0\n"
        "v_cvt_pk_bf16_f32 v219, v164, v165\n"
        "v_cvt_pk_bf16_f32 v101, v106, v107\n"
        "v_mfma_f32_16x16x32_bf16 v[72:75], a[224:227], a[168:171], v[72:75]\n"
        "v_fma_f32 v108, v108, s24, v238\n"
        "v_fma_f32 v109, v109, s24, v238\n"
        "v_mfma_f32_16x16x32_bf16 v[76:79], a[224:227], a[192:195], 0\n"
        "ds_read_b128 a[224:227], v241 offset:12416\n"
        "v_fma_f32 v110, v110, s24, v238\n"
        "v_mfma_f32_16x16x32_bf16 v[76:79], a[228:231], a[196:199], v[76:79]\n"
        "ds_read_b128 a[228:231], v240 offset:12544\n"
        "v_fma_f32 v111, v111, s24, v238\n"
        "v_mfma_f32_16x16x32_bf16 v[76:79], a[232:235], a[200:203], v[76:79]\n"
        "buffer_load_dwordx4 v242, s[8:11], s47 offen lds\n"
        "s_sub_u32 s31, s30, s28\n"
        "v_mfma_f32_16x16x32_bf16 v[76:79], a[236:239], a[204:207], v[76:79]\n"
        "ds_read_b128 a[232:235], v241 offset:12544\n"
        "s_sub_u32 s45, s28, s29\n"
        "v_cvt_pk_bf16_f32 v220, v72, v73\n"
        "v_mfma_f32_16x16x32_bf16 v[76:79], a[240:243], a[208:211], v[76:79]\n"
        "s_add_u32 s28, s28, s31\n"
        "v_cvt_pk_bf16_f32 v221, v74, v75\n"
        "v_fma_f32 v112, v112, s24, v239\n"
        "v_mfma_f32_16x16x32_bf16 v[76:79], a[244:247], a[212:215], v[76:79]\n"
        "s_waitcnt lgkmcnt(5)\n"
        "v_exp_f32 v108, v108\n"
        "v_mfma_f32_16x16x32_bf16 v[162:165], a[72:75], a[248:251], 0\n"
        "s_waitcnt lgkmcnt(4)\n"
        "v_exp_f32 v109, v109\n"
        "v_mfma_f32_16x16x32_bf16 v[162:165], a[76:79], a[216:219], v[162:165]\n"
        "s_waitcnt lgkmcnt(3)\n"
        "v_exp_f32 v110, v110\n"
        "v_mfma_f32_16x16x32_bf16 v[162:165], a[80:83], a[220:223], v[162:165]\n"
        "s_waitcnt lgkmcnt(2)\n"
        "v_exp_f32 v111, v111\n"
        "v_mfma_f32_16x16x32_bf16 v[162:165], a[84:87], a[224:227], v[162:165]\n"
        "s_waitcnt lgkmcnt(1)\n"
        "v_exp_f32 v112, v112\n"
        "v_mfma_f32_16x16x32_bf16 v[162:165], a[88:91], a[228:231], v[162:165]\n"
        "s_waitcnt lgkmcnt(0)\n"
        "v_cvt_pk_bf16_f32 v222, v76, v77\n"
        "v_cvt_pk_bf16_f32 v223, v78, v79\n"
        "v_mfma_f32_16x16x32_bf16 v[162:165], a[92:95], a[232:235], v[162:165]\n"
        "s_mov_b32 s30, s29\n"
        "v_cvt_pk_bf16_f32 v102, v108, v109\n"
        "v_cvt_pk_bf16_f32 v103, v110, v111\n"
        "v_mfma_f32_16x16x32_bf16 v[72:75], a[116:119], a[232:235], 0\n"
        "s_add_u32 m0, s41, 1024\n"
        "v_fma_f32 v113, v113, s24, v239\n"
        "v_fma_f32 v114, v114, s24, v239\n"
        "v_mfma_f32_16x16x32_bf16 v[72:75], a[112:115], a[228:231], v[72:75]\n"
        "s_add_u32 s29, s29, s45\n"
        "v_exp_f32 v113, v113\n"
        "v_mfma_f32_16x16x32_bf16 v[72:75], a[108:111], a[224:227], v[72:75]\n"
        "buffer_load_dwordx4 v243, s[8:11], s47 offen lds\n"
        "v_mfma_f32_16x16x32_bf16 v[72:75], a[104:107], a[220:223], v[72:75]\n"
        "v_exp_f32 v114, v114\n"
        "v_mfma_f32_16x16x32_bf16 v[72:75], a[100:103], a[216:219], v[72:75]\n"
        "v_cvt_pk_bf16_f32 v192, v162, v163\n"
        "v_cvt_pk_bf16_f32 v193, v164, v165\n"
        "v_mfma_f32_16x16x32_bf16 v[72:75], a[96:99], a[248:251], v[72:75]\n"
        "v_cvt_pk_bf16_f32 v112, v112, v113\n"
        "v_fma_f32 v115, v115, s24, v239\n"
        "v_mfma_f32_16x16x32_bf16 v[76:79], a[120:123], a[248:251], 0\n"
        "v_exp_f32 v115, v115\n"
        "v_mfma_f32_16x16x32_bf16 v[76:79], a[124:127], a[216:219], v[76:79]\n"
        "v_cvt_pk_bf16_f32 v113, v114, v115\n"
        "v_fma_f32 v116, v116, s24, v239\n"
        "v_mfma_f32_16x16x32_bf16 v[76:79], a[128:131], a[220:223], v[76:79]\n"
        "v_exp_f32 v116, v116\n"
        "v_mfma_f32_16x16x32_bf16 v[76:79], a[132:135], a[224:227], v[76:79]\n"
        "v_cvt_pk_bf16_f32 v194, v72, v73\n"
        "v_cvt_pk_bf16_f32 v195, v74, v75\n"
        "v_mfma_f32_16x16x32_bf16 v[76:79], a[136:139], a[228:231], v[76:79]\n"
        "v_fma_f32 v117, v117, s24, v239\n"
        "v_fma_f32 v118, v118, s24, v239\n"
        "v_mfma_f32_16x16x32_bf16 v[76:79], a[140:143], a[232:235], v[76:79]\n"
        "v_exp_f32 v117, v117\n"
        "v_mfma_f32_16x16x32_bf16 v[162:165], a[20:23], a[232:235], 0\n"
        "v_exp_f32 v118, v118\n"
        "v_mfma_f32_16x16x32_bf16 v[162:165], a[16:19], a[228:231], v[162:165]\n"
        "v_cvt_pk_bf16_f32 v114, v116, v117\n"
        "v_fma_f32 v119, v119, s24, v239\n"
        "v_mfma_f32_16x16x32_bf16 v[162:165], a[12:15], a[224:227], v[162:165]\n"
        "s_add_u32 m0, s41, 2048\n"
        "v_exp_f32 v119, v119\n"
        "v_mfma_f32_16x16x32_bf16 v[162:165], a[8:11], a[220:223], v[162:165]\n"
        "v_cvt_pk_bf16_f32 v208, v76, v77\n"
        "v_cvt_pk_bf16_f32 v209, v78, v79\n"
        "v_mfma_f32_16x16x32_bf16 v[162:165], a[4:7], a[216:219], v[162:165]\n"
        "buffer_load_dwordx4 v244, s[8:11], s47 offen lds\n"
        "v_mfma_f32_16x16x32_bf16 v[162:165], a[0:3], a[248:251], v[162:165]\n"
        "v_cvt_pk_bf16_f32 v115, v118, v119\n"
        "v_fma_f32 v120, v120, s24, v239\n"
        "v_mfma_f32_16x16x32_bf16 v[72:75], a[24:27], a[248:251], 0\n"
        "v_exp_f32 v120, v120\n"
        "v_mfma_f32_16x16x32_bf16 v[72:75], a[28:31], a[216:219], v[72:75]\n"
        "v_fma_f32 v121, v121, s24, v239\n"
        "v_fma_f32 v122, v122, s24, v239\n"
        "v_mfma_f32_16x16x32_bf16 v[72:75], a[32:35], a[220:223], v[72:75]\n"
        "v_exp_f32 v121, v121\n"
        "v_mfma_f32_16x16x32_bf16 v[72:75], a[36:39], a[224:227], v[72:75]\n"
        "v_exp_f32 v122, v122\n"
        "v_mfma_f32_16x16x32_bf16 v[72:75], a[40:43], a[228:231], v[72:75]\n"
        "v_cvt_pk_bf16_f32 v196, v162, v163\n"
        "v_cvt_pk_bf16_f32 v197, v164, v165\n"
        "v_mfma_f32_16x16x32_bf16 v[72:75], a[44:47], a[232:235], v[72:75]\n"
        "v_cvt_pk_bf16_f32 v116, v120, v121\n"
        "v_fma_f32 v123, v123, s24, v239\n"
        "v_mfma_f32_16x16x32_bf16 v[76:79], a[68:71], a[232:235], 0\n"
        "v_exp_f32 v123, v123\n"
        "v_mfma_f32_16x16x32_bf16 v[76:79], a[64:67], a[228:231], v[76:79]\n"
        "v_cvt_pk_bf16_f32 v117, v122, v123\n"
        "v_fma_f32 v124, v124, s24, v239\n"
        "v_mfma_f32_16x16x32_bf16 v[76:79], a[60:63], a[224:227], v[76:79]\n"
        "v_exp_f32 v124, v124\n"
        "v_mfma_f32_16x16x32_bf16 v[76:79], a[56:59], a[220:223], v[76:79]\n"
        "v_cvt_pk_bf16_f32 v198, v72, v73\n"
        "v_cvt_pk_bf16_f32 v199, v74, v75\n"
        "v_mfma_f32_16x16x32_bf16 v[76:79], a[52:55], a[216:219], v[76:79]\n"
        "s_add_u32 m0, s41, 12288\n"
        "v_fma_f32 v125, v125, s24, v239\n"
        "v_fma_f32 v126, v126, s24, v239\n"
        "v_mfma_f32_16x16x32_bf16 v[76:79], a[48:51], a[248:251], v[76:79]\n"
        "v_exp_f32 v125, v125\n"
        "v_mfma_f32_16x16x32_bf16 v[182:185], a[252:255], v[64:67], 0\n"
        "buffer_load_dwordx4 v242, s[8:11], s35 offen lds\n"
        "v_mfma_f32_16x16x32_bf16 v[182:185], a[252:255], v[68:71], v[182:185]\n"
        "v_exp_f32 v126, v126\n"
        "v_mfma_f32_16x16x32_bf16 v[202:205], v[132:135], v[68:71], 0\n"
        "v_cvt_pk_bf16_f32 v118, v124, v125\n"
        "v_fma_f32 v127, v127, s24, v239\n"
        "v_mfma_f32_16x16x32_bf16 v[202:205], v[128:131], v[64:67], v[202:205]\n"
        "v_exp_f32 v127, v127\n"
        "v_mfma_f32_16x16x32_bf16 v[88:91], v[136:139], v[64:67], 0\n"
        "v_cvt_pk_bf16_f32 v200, v76, v77\n"
        "v_cvt_pk_bf16_f32 v201, v78, v79\n"
        "v_mfma_f32_16x16x32_bf16 v[88:91], v[140:143], v[68:71], v[88:91]\n"
        "v_cvt_pk_bf16_f32 v206, v76, v77\n"
        "v_cvt_pk_bf16_f32 v207, v78, v79\n"
        "v_mfma_f32_16x16x32_bf16 v[92:95], v[148:151], v[68:71], 0\n"
        "s_cmp_eq_u32 s37, 100663296\n"
        "v_rcp_f32 v182, v182\n"
        "v_mfma_f32_16x16x32_bf16 v[92:95], v[144:147], v[64:67], v[92:95]\n"
        "s_cselect_b32 s46, 0, s37\n"
        "v_cvt_pk_bf16_f32 v119, v126, v127\n"
        "v_mul_f32 v202, v182, v202\n"
        "v_mfma_f32_16x16x32_bf16 v[64:67], a[252:255], v[80:83], 0\n"
        "s_add_u32 s38, s46, s43\n"
        "v_mul_f32 v203, v182, v203\n"
        "v_mul_f32 v204, v182, v204\n"
        "v_mfma_f32_16x16x32_bf16 v[64:67], a[252:255], v[84:87], v[64:67]\n"
        "s_add_u32 s39, s46, s44\n"
        "v_mul_f32 v205, v182, v205\n"
        "v_cvt_pk_bf16_f32 v202, v202, v203\n"
        "v_mfma_f32_16x16x32_bf16 v[68:71], v[132:135], v[84:87], 0\n"
        "s_add_u32 s37, s37, 6291456\n"
        "v_cvt_pk_bf16_f32 v203, v204, v205\n"
        "v_mul_f32 v88, v182, v88\n"
        "v_mfma_f32_16x16x32_bf16 v[68:71], v[128:131], v[80:83], v[68:71]\n"
        "v_rcp_f32 v64, v64\n"
        "v_mfma_f32_16x16x32_bf16 v[72:75], v[136:139], v[80:83], 0\n"
        "s_add_u32 m0, s41, 13312\n"
        "v_mul_f32 v89, v182, v89\n"
        "v_mul_f32 v90, v182, v90\n"
        "v_mfma_f32_16x16x32_bf16 v[72:75], v[140:143], v[84:87], v[72:75]\n"
        "v_mul_f32 v91, v182, v91\n"
        "v_cvt_pk_bf16_f32 v204, v88, v89\n"
        "v_mfma_f32_16x16x32_bf16 v[162:165], v[148:151], v[84:87], 0\n"
        "buffer_load_dwordx4 v243, s[8:11], s35 offen lds\n"
        "v_mfma_f32_16x16x32_bf16 v[162:165], v[144:147], v[80:83], v[162:165]\n"
        "v_cvt_pk_bf16_f32 v205, v90, v91\n"
        "v_mul_f32 v92, v182, v92\n"
        "v_mfma_f32_16x16x32_bf16 v[80:83], a[252:255], v[96:99], 0\n"
        "ds_write_b128 v245, v[202:205] offset:0\n"
        "v_mfma_f32_16x16x32_bf16 v[80:83], a[252:255], v[100:103], v[80:83]\n"
        "v_mul_f32 v93, v182, v93\n"
        "v_mul_f32 v94, v182, v94\n"
        "v_mfma_f32_16x16x32_bf16 v[84:87], v[132:135], v[100:103], 0\n"
        "v_mul_f32 v95, v182, v95\n"
        "v_cvt_pk_bf16_f32 v92, v92, v93\n"
        "v_mfma_f32_16x16x32_bf16 v[84:87], v[128:131], v[96:99], v[84:87]\n"
        "v_cvt_pk_bf16_f32 v93, v94, v95\n"
        "v_mul_f32 v68, v64, v68\n"
        "v_mfma_f32_16x16x32_bf16 v[76:79], v[136:139], v[96:99], 0\n"
        "v_rcp_f32 v80, v80\n"
        "v_mfma_f32_16x16x32_bf16 v[76:79], v[140:143], v[100:103], v[76:79]\n"
        "ds_write_b64 v246, v[92:93] offset:0\n"
        "v_mfma_f32_16x16x32_bf16 v[104:107], v[148:151], v[100:103], 0\n"
        "v_mul_f32 v69, v64, v69\n"
        "v_mul_f32 v70, v64, v70\n"
        "v_mfma_f32_16x16x32_bf16 v[104:107], v[144:147], v[96:99], v[104:107]\n"
        "v_mul_f32 v71, v64, v71\n"
        "v_cvt_pk_bf16_f32 v68, v68, v69\n"
        "v_mfma_f32_16x16x32_bf16 v[96:99], a[252:255], v[112:115], 0\n"
        "v_cvt_pk_bf16_f32 v69, v70, v71\n"
        "v_mul_f32 v72, v64, v72\n"
        "v_mfma_f32_16x16x32_bf16 v[96:99], a[252:255], v[116:119], v[96:99]\n"
        "v_mul_f32 v73, v64, v73\n"
        "v_mul_f32 v74, v64, v74\n"
        "v_mfma_f32_16x16x32_bf16 v[108:111], v[132:135], v[116:119], 0\n"
        "s_add_u32 m0, s41, 14336\n"
        "v_mul_f32 v75, v64, v75\n"
        "v_cvt_pk_bf16_f32 v70, v72, v73\n"
        "v_mfma_f32_16x16x32_bf16 v[108:111], v[128:131], v[112:115], v[108:111]\n"
        "v_mov_b32 v128, v212\n"
        "v_mov_b32 v129, v213\n"
        "v_mfma_f32_16x16x32_bf16 v[120:123], v[136:139], v[112:115], 0\n"
        "buffer_load_dwordx4 v244, s[8:11], s35 offen lds\n"
        "v_mfma_f32_16x16x32_bf16 v[120:123], v[140:143], v[116:119], v[120:123]\n"
        "ds_read_b128 a[236:239], v240 offset:13824\n"
        "v_mov_b32 v136, v214\n"
        "v_mfma_f32_16x16x32_bf16 v[124:127], v[148:151], v[116:119], 0\n"
        "ds_read_b128 a[240:243], v241 offset:13824\n"
        "v_mov_b32 v137, v215\n"
        "v_mfma_f32_16x16x32_bf16 v[124:127], v[144:147], v[112:115], v[124:127]\n"
        "ds_read_b128 a[244:247], v240 offset:13952\n"
        "v_mov_b32 v144, v216\n"
        "v_mfma_f32_16x16x32_bf16 v[100:103], a[248:251], a[144:147], 0\n"
        "v_mov_b32 v145, v217\n"
        "v_cvt_pk_bf16_f32 v71, v74, v75\n"
        "v_mfma_f32_16x16x32_bf16 v[100:103], a[216:219], a[148:151], v[100:103]\n"
        "ds_write_b128 v245, v[68:71] offset:256\n"
        "v_mfma_f32_16x16x32_bf16 v[100:103], a[220:223], a[152:155], v[100:103]\n"
        "v_mov_b32 v130, v218\n"
        "v_mov_b32 v131, v219\n"
        "v_mfma_f32_16x16x32_bf16 v[100:103], a[224:227], a[156:159], v[100:103]\n"
        "v_mov_b32 v138, v220\n"
        "v_mov_b32 v139, v221\n"
        "v_mfma_f32_16x16x32_bf16 v[100:103], a[228:231], a[160:163], v[100:103]\n"
        "v_mul_f32 v162, v64, v162\n"
        "v_mul_f32 v163, v64, v163\n"
        "v_mfma_f32_16x16x32_bf16 v[100:103], a[232:235], a[164:167], v[100:103]\n"
        "v_mul_f32 v164, v64, v164\n"
        "v_mul_f32 v165, v64, v165\n"
        "v_mfma_f32_16x16x32_bf16 v[112:115], a[232:235], a[188:191], 0\n"
        "v_mov_b32 v146, v222\n"
        "v_mov_b32 v147, v223\n"
        "v_mfma_f32_16x16x32_bf16 v[112:115], a[228:231], a[184:187], v[112:115]\n"
        "v_cvt_pk_bf16_f32 v162, v162, v163\n"
        "v_cvt_pk_bf16_f32 v163, v164, v165\n"
        "v_mfma_f32_16x16x32_bf16 v[112:115], a[224:227], a[180:183], v[112:115]\n"
        "ds_write_b64 v246, v[162:163] offset:256\n"
        "v_mfma_f32_16x16x32_bf16 v[112:115], a[220:223], a[176:179], v[112:115]\n"
        "v_cvt_pk_bf16_f32 v132, v100, v101\n"
        "v_cvt_pk_bf16_f32 v133, v102, v103\n"
        "v_mfma_f32_16x16x32_bf16 v[112:115], a[216:219], a[172:175], v[112:115]\n"
        "v_mul_f32 v84, v80, v84\n"
        "v_mul_f32 v85, v80, v85\n"
        "v_mfma_f32_16x16x32_bf16 v[112:115], a[248:251], a[168:171], v[112:115]\n"
        "v_rcp_f32 v96, v96\n"
        "v_mfma_f32_16x16x32_bf16 v[116:119], a[248:251], a[192:195], 0\n"
        "ds_read_b128 a[248:251], v241 offset:13952\n"
        "v_mul_f32 v86, v80, v86\n"
        "v_mfma_f32_16x16x32_bf16 v[116:119], a[216:219], a[196:199], v[116:119]\n"
        "ds_read_b128 a[216:219], v240 offset:14080\n"
        "v_mul_f32 v87, v80, v87\n"
        "v_mfma_f32_16x16x32_bf16 v[116:119], a[220:223], a[200:203], v[116:119]\n"
        "ds_read_b128 a[220:223], v241 offset:14080\n"
        "v_cvt_pk_bf16_f32 v140, v112, v113\n"
        "v_mfma_f32_16x16x32_bf16 v[116:119], a[224:227], a[204:207], v[116:119]\n"
        "v_cvt_pk_bf16_f32 v141, v114, v115\n"
        "v_cvt_pk_bf16_f32 v84, v84, v85\n"
        "v_mfma_f32_16x16x32_bf16 v[116:119], a[228:231], a[208:211], v[116:119]\n"
        "v_cvt_pk_bf16_f32 v85, v86, v87\n"
        "v_mul_f32 v76, v80, v76\n"
        "v_mfma_f32_16x16x32_bf16 v[116:119], a[232:235], a[212:215], v[116:119]\n"
        "s_waitcnt lgkmcnt(7)\n"
        "v_mul_f32 v77, v80, v77\n"
        "v_mul_f32 v78, v80, v78\n"
        "v_mfma_f32_16x16x32_bf16 v[100:103], a[236:239], a[144:147], 0\n"
        "s_waitcnt lgkmcnt(6)\n"
        "v_mul_f32 v79, v80, v79\n"
        "v_cvt_pk_bf16_f32 v86, v76, v77\n"
        "v_mfma_f32_16x16x32_bf16 v[100:103], a[240:243], a[148:151], v[100:103]\n"
        "s_waitcnt lgkmcnt(5)\n"
        "v_cvt_pk_bf16_f32 v87, v78, v79\n"
        "v_mul_f32 v104, v80, v104\n"
        "v_mfma_f32_16x16x32_bf16 v[100:103], a[244:247], a[152:155], v[100:103]\n"
        "s_waitcnt lgkmcnt(2)\n"
        "ds_write_b128 v245, v[84:87] offset:512\n"
        "v_mfma_f32_16x16x32_bf16 v[100:103], a[248:251], a[156:159], v[100:103]\n"
        "s_waitcnt lgkmcnt(2)\n"
        "v_cvt_pk_bf16_f32 v148, v116, v117\n"
        "v_cvt_pk_bf16_f32 v149, v118, v119\n"
        "v_mfma_f32_16x16x32_bf16 v[100:103], a[216:219], a[160:163], v[100:103]\n"
        "s_waitcnt lgkmcnt(1)\n"
        "v_mul_f32 v105, v80, v105\n"
        "v_mul_f32 v106, v80, v106\n"
        "v_mfma_f32_16x16x32_bf16 v[100:103], a[220:223], a[164:167], v[100:103]\n"
        "v_mul_f32 v107, v80, v107\n"
        "v_cvt_pk_bf16_f32 v104, v104, v105\n"
        "v_mfma_f32_16x16x32_bf16 v[112:115], a[220:223], a[188:191], 0\n"
        "v_cvt_pk_bf16_f32 v105, v106, v107\n"
        "v_mul_f32 v108, v96, v108\n"
        "v_mfma_f32_16x16x32_bf16 v[112:115], a[216:219], a[184:187], v[112:115]\n"
        "ds_write_b64 v246, v[104:105] offset:512\n"
        "v_mfma_f32_16x16x32_bf16 v[112:115], a[248:251], a[180:183], v[112:115]\n"
        "v_cvt_pk_bf16_f32 v134, v100, v101\n"
        "v_cvt_pk_bf16_f32 v135, v102, v103\n"
        "v_mfma_f32_16x16x32_bf16 v[112:115], a[244:247], a[176:179], v[112:115]\n"
        "v_mul_f32 v109, v96, v109\n"
        "v_mul_f32 v110, v96, v110\n"
        "v_mfma_f32_16x16x32_bf16 v[112:115], a[240:243], a[172:175], v[112:115]\n"
        "v_mul_f32 v111, v96, v111\n"
        "v_cvt_pk_bf16_f32 v108, v108, v109\n"
        "v_mfma_f32_16x16x32_bf16 v[112:115], a[236:239], a[168:171], v[112:115]\n"
        "v_cvt_pk_bf16_f32 v109, v110, v111\n"
        "v_mul_f32 v120, v96, v120\n"
        "v_mfma_f32_16x16x32_bf16 v[116:119], a[236:239], a[192:195], 0\n"
        "v_mul_f32 v121, v96, v121\n"
        "v_mul_f32 v122, v96, v122\n"
        "v_mfma_f32_16x16x32_bf16 v[116:119], a[240:243], a[196:199], v[116:119]\n"
        "v_mul_f32 v123, v96, v123\n"
        "v_cvt_pk_bf16_f32 v110, v120, v121\n"
        "v_mfma_f32_16x16x32_bf16 v[116:119], a[244:247], a[200:203], v[116:119]\n"
        "v_cvt_pk_bf16_f32 v142, v112, v113\n"
        "v_cvt_pk_bf16_f32 v143, v114, v115\n"
        "v_mfma_f32_16x16x32_bf16 v[116:119], a[248:251], a[204:207], v[116:119]\n"
        "v_cvt_pk_bf16_f32 v111, v122, v123\n"
        "v_mul_f32 v124, v96, v124\n"
        "v_mfma_f32_16x16x32_bf16 v[116:119], a[216:219], a[208:211], v[116:119]\n"
        "ds_write_b128 v245, v[108:111] offset:768\n"
        "v_mfma_f32_16x16x32_bf16 v[116:119], a[220:223], a[212:215], v[116:119]\n"
        "v_mul_f32 v125, v96, v125\n"
        "v_mul_f32 v126, v96, v126\n"
        "v_mfma_f32_16x16x32_bf16 v[100:103], a[20:23], a[220:223], 0\n"
        "v_mul_f32 v127, v96, v127\n"
        "v_cvt_pk_bf16_f32 v124, v124, v125\n"
        "v_mfma_f32_16x16x32_bf16 v[100:103], a[16:19], a[216:219], v[100:103]\n"
        "v_cvt_pk_bf16_f32 v125, v126, v127\n"
        "v_mfma_f32_16x16x32_bf16 v[100:103], a[12:15], a[248:251], v[100:103]\n"
        "v_cvt_pk_bf16_f32 v150, v116, v117\n"
        "v_cvt_pk_bf16_f32 v151, v118, v119\n"
        "v_mfma_f32_16x16x32_bf16 v[100:103], a[8:11], a[244:247], v[100:103]\n"
        "ds_write_b64 v246, v[124:125] offset:768\n"
        "v_mfma_f32_16x16x32_bf16 v[100:103], a[4:7], a[240:243], v[100:103]\n"
        "v_mfma_f32_16x16x32_bf16 v[100:103], a[0:3], a[236:239], v[100:103]\n"
        "v_mfma_f32_16x16x32_bf16 v[112:115], a[24:27], a[236:239], 0\n"
        "v_mfma_f32_16x16x32_bf16 v[112:115], a[28:31], a[240:243], v[112:115]\n"
        "v_mfma_f32_16x16x32_bf16 v[112:115], a[32:35], a[244:247], v[112:115]\n"
        "v_mfma_f32_16x16x32_bf16 v[112:115], a[36:39], a[248:251], v[112:115]\n"
        "v_mfma_f32_16x16x32_bf16 v[112:115], a[40:43], a[216:219], v[112:115]\n"
        "v_mfma_f32_16x16x32_bf16 v[112:115], a[44:47], a[220:223], v[112:115]\n"
        "v_mfma_f32_16x16x32_bf16 v[116:119], a[68:71], a[220:223], 0\n"
        "ds_read_b128 v[162:165], v251 offset:0\n"
        "v_cvt_pk_bf16_f32 v216, v100, v101\n"
        "v_mfma_f32_16x16x32_bf16 v[116:119], a[64:67], a[216:219], v[116:119]\n"
        "v_cvt_pk_bf16_f32 v217, v102, v103\n"
        "v_mfma_f32_16x16x32_bf16 v[116:119], a[60:63], a[248:251], v[116:119]\n"
        "v_mfma_f32_16x16x32_bf16 v[116:119], a[56:59], a[244:247], v[116:119]\n"
        "v_mfma_f32_16x16x32_bf16 v[116:119], a[52:55], a[240:243], v[116:119]\n"
        "ds_read_b128 v[182:185], v251 offset:512\n"
        "v_cvt_pk_bf16_f32 v218, v112, v113\n"
        "v_mfma_f32_16x16x32_bf16 v[116:119], a[48:51], a[236:239], v[116:119]\n"
        "v_cvt_pk_bf16_f32 v219, v114, v115\n"
        "v_mfma_f32_16x16x32_bf16 v[100:103], a[72:75], a[236:239], 0\n"
        "s_add_u32 s47, s47, 6291456\n"
        "v_mfma_f32_16x16x32_bf16 v[100:103], a[76:79], a[240:243], v[100:103]\n"
        "s_add_u32 s36, s36, 2097152\n"
        "v_mfma_f32_16x16x32_bf16 v[100:103], a[80:83], a[244:247], v[100:103]\n"
        "ds_read_b128 v[202:205], v251 offset:4096\n"
        "s_xor_b32 s48, s48, 8192\n"
        "v_cvt_pk_bf16_f32 v220, v116, v117\n"
        "v_mfma_f32_16x16x32_bf16 v[100:103], a[84:87], a[248:251], v[100:103]\n"
        "v_cvt_pk_bf16_f32 v221, v118, v119\n"
        "v_cvt_pk_bf16_f32 v226, v116, v117\n"
        "v_mfma_f32_16x16x32_bf16 v[100:103], a[88:91], a[216:219], v[100:103]\n"
        "v_cvt_pk_bf16_f32 v227, v118, v119\n"
        "v_mfma_f32_16x16x32_bf16 v[100:103], a[92:95], a[220:223], v[100:103]\n"
        "v_mfma_f32_16x16x32_bf16 v[112:115], a[116:119], a[220:223], 0\n"
        "ds_read_b128 v[222:225], v251 offset:4608\n"
        "v_mfma_f32_16x16x32_bf16 v[112:115], a[112:115], a[216:219], v[112:115]\n"
        "v_mfma_f32_16x16x32_bf16 v[112:115], a[108:111], a[248:251], v[112:115]\n"
        "s_waitcnt lgkmcnt(3)\n"
        "v_pk_min_u16 v162, v162, s26\n"
        "v_pk_min_u16 v163, v163, s26\n"
        "v_mfma_f32_16x16x32_bf16 v[112:115], a[104:107], a[244:247], v[112:115]\n"
        "v_pk_min_u16 v164, v164, s26\n"
        "v_cvt_pk_bf16_f32 v212, v100, v101\n"
        "v_mfma_f32_16x16x32_bf16 v[112:115], a[100:103], a[240:243], v[112:115]\n"
        "v_cvt_pk_bf16_f32 v213, v102, v103\n"
        "v_pk_min_u16 v165, v165, s26\n"
        "v_mfma_f32_16x16x32_bf16 v[112:115], a[96:99], a[236:239], v[112:115]\n"
        "v_add_u32 v240, s31, v240\n"
        "v_add_u32 v241, s31, v241\n"
        "v_mfma_f32_16x16x32_bf16 v[116:119], a[120:123], a[236:239], 0\n"
        "s_waitcnt lgkmcnt(2)\n"
        "v_pk_min_u16 v182, v182, s26\n"
        "v_pk_min_u16 v183, v183, s26\n"
        "v_mfma_f32_16x16x32_bf16 v[116:119], a[124:127], a[240:243], v[116:119]\n"
        "v_pk_min_u16 v184, v184, s26\n"
        "v_pk_min_u16 v185, v185, s26\n"
        "v_mfma_f32_16x16x32_bf16 v[116:119], a[128:131], a[244:247], v[116:119]\n"
        "v_cvt_pk_bf16_f32 v214, v112, v113\n"
        "v_cvt_pk_bf16_f32 v215, v114, v115\n"
        "v_mfma_f32_16x16x32_bf16 v[116:119], a[132:135], a[248:251], v[116:119]\n"
        "v_mfma_f32_16x16x32_bf16 v[116:119], a[136:139], a[216:219], v[116:119]\n"
        "s_waitcnt lgkmcnt(1)\n"
        "v_pk_min_u16 v202, v202, s26\n"
        "v_pk_min_u16 v203, v203, s26\n"
        "v_mfma_f32_16x16x32_bf16 v[116:119], a[140:143], a[220:223], v[116:119]\n"
        "v_pk_min_u16 v204, v204, s26\n"
        "v_pk_min_u16 v205, v205, s26\n"
        "v_mfma_f32_16x16x32_bf16 v[64:67], v[152:155], v[156:159], v[0:3]\n"
        "v_mfma_f32_16x16x32_bf16 v[64:67], v[168:171], v[160:163], v[64:67]\n"
        "v_mfma_f32_16x16x32_bf16 v[68:71], v[172:175], v[156:159], v[4:7]\n"
        "s_waitcnt lgkmcnt(0)\n"
        "v_pk_min_u16 v222, v222, s26\n"
        "v_pk_min_u16 v223, v223, s26\n"
        "v_mfma_f32_16x16x32_bf16 v[68:71], v[188:191], v[164:167], v[68:71]\n"
        "ds_read_b128 v[162:165], v252 offset:0\n"
        "v_cvt_pk_bf16_f32 v230, v116, v117\n"
        "v_mfma_f32_16x16x32_bf16 v[80:83], v[152:155], v[176:179], v[16:19]\n"
        "v_cvt_pk_bf16_f32 v231, v118, v119\n"
        "v_pk_min_u16 v224, v224, s26\n"
        "v_mfma_f32_16x16x32_bf16 v[80:83], v[168:171], v[180:183], v[80:83]\n"
        "v_pk_min_u16 v225, v225, s26\n"
        "v_max3_f32 v232, v64, v65, v66\n"
        "v_mfma_f32_16x16x32_bf16 v[84:87], v[172:175], v[176:179], v[20:23]\n"
        "v_max3_f32 v232, v232, v67, v68\n"
        "v_max3_f32 v232, v232, v69, v70\n"
        "v_mfma_f32_16x16x32_bf16 v[84:87], v[188:191], v[184:187], v[84:87]\n"
        "ds_read_b128 v[182:185], v252 offset:512\n"
        "v_mfma_f32_16x16x32_bf16 v[96:99], v[152:155], v[196:199], v[32:35]\n"
        "v_max3_f32 v233, v80, v81, v82\n"
        "v_mfma_f32_16x16x32_bf16 v[96:99], v[168:171], v[200:203], v[96:99]\n"
        "v_mfma_f32_16x16x32_bf16 v[100:103], v[172:175], v[196:199], v[36:39]\n"
        "v_mfma_f32_16x16x32_bf16 v[100:103], v[188:191], v[204:207], v[100:103]\n"
        "ds_read_b128 v[202:205], v252 offset:4096\n"
        "v_mfma_f32_16x16x32_bf16 v[112:115], v[152:155], v[216:219], v[48:51]\n"
        "s_waitcnt lgkmcnt(2)\n"
        "v_pk_min_u16 v162, v162, s26\n"
        "v_pk_min_u16 v163, v163, s26\n"
        "v_mfma_f32_16x16x32_bf16 v[112:115], v[168:171], v[220:223], v[112:115]\n"
        "v_pk_min_u16 v164, v164, s26\n"
        "v_pk_min_u16 v165, v165, s26\n"
        "v_mfma_f32_16x16x32_bf16 v[116:119], v[172:175], v[216:219], v[52:55]\n"
        "v_max3_f32 v233, v233, v83, v84\n"
        "v_max3_f32 v233, v233, v85, v86\n"
        "v_mfma_f32_16x16x32_bf16 v[116:119], v[188:191], v[224:227], v[116:119]\n"
        "ds_read_b128 v[222:225], v252 offset:4608\n"
        "v_max3_f32 v234, v96, v97, v98\n"
        "v_mfma_f32_16x16x32_bf16 v[72:75], v[192:195], v[156:159], v[8:11]\n"
        "s_waitcnt lgkmcnt(2)\n"
        "v_pk_min_u16 v182, v182, s26\n"
        "v_pk_min_u16 v183, v183, s26\n"
        "v_mfma_f32_16x16x32_bf16 v[72:75], v[208:211], v[160:163], v[72:75]\n"
        "v_pk_min_u16 v184, v184, s26\n"
        "v_pk_min_u16 v185, v185, s26\n"
        "v_mfma_f32_16x16x32_bf16 v[76:79], v[212:215], v[156:159], v[12:15]\n"
        "v_max3_f32 v234, v234, v99, v100\n"
        "v_max3_f32 v234, v234, v101, v102\n"
        "v_mfma_f32_16x16x32_bf16 v[76:79], v[228:231], v[164:167], v[76:79]\n"
        "v_max3_f32 v235, v112, v113, v114\n"
        "v_max3_f32 v235, v235, v115, v116\n"
        "v_mfma_f32_16x16x32_bf16 v[88:91], v[192:195], v[176:179], v[24:27]\n"
        "s_barrier\n"
        "ds_read_b128 v[152:155], v247 offset:0\n"
        "s_waitcnt lgkmcnt(2)\n"
        "v_pk_min_u16 v202, v202, s26\n"
        "v_mfma_f32_16x16x32_bf16 v[88:91], v[208:211], v[180:183], v[88:91]\n"
        "ds_read_b128 v[160:163], v247 offset:4096\n"
        "v_pk_min_u16 v203, v203, s26\n"
        "v_mfma_f32_16x16x32_bf16 v[92:95], v[212:215], v[176:179], v[28:31]\n"
        "ds_read_b128 v[156:159], v247 offset:256\n"
        "v_pk_min_u16 v204, v204, s26\n"
        "v_mfma_f32_16x16x32_bf16 v[92:95], v[228:231], v[184:187], v[92:95]\n"
        "ds_read_b128 v[164:167], v247 offset:4352\n"
        "v_pk_min_u16 v205, v205, s26\n"
        "v_mfma_f32_16x16x32_bf16 v[104:107], v[192:195], v[196:199], v[40:43]\n"
        "ds_read_b128 a[216:219], v248 offset:0\n"
        "s_waitcnt lgkmcnt(5)\n"
        "v_pk_min_u16 v222, v222, s26\n"
        "v_mfma_f32_16x16x32_bf16 v[104:107], v[208:211], v[200:203], v[104:107]\n"
        "ds_read_b128 a[220:223], v248 offset:6144\n"
        "v_pk_min_u16 v223, v223, s26\n"
        "v_mfma_f32_16x16x32_bf16 v[108:111], v[212:215], v[196:199], v[44:47]\n"
        "ds_read_b128 a[224:227], v254 offset:16\n"
        "v_pk_min_u16 v224, v224, s26\n"
        "v_mfma_f32_16x16x32_bf16 v[108:111], v[228:231], v[204:207], v[108:111]\n"
        "ds_read_b128 a[228:231], v254 offset:6160\n"
        "v_pk_min_u16 v225, v225, s26\n"
        "v_mfma_f32_16x16x32_bf16 v[120:123], v[192:195], v[216:219], v[56:59]\n"
        "ds_read_b128 a[232:235], v248 offset:32\n"
        "v_max3_f32 v232, v232, v71, v72\n"
        "v_mfma_f32_16x16x32_bf16 v[120:123], v[208:211], v[220:223], v[120:123]\n"
        "v_max3_f32 v232, v232, v73, v74\n"
        "v_max3_f32 v232, v232, v75, v76\n"
        "v_mfma_f32_16x16x32_bf16 v[124:127], v[212:215], v[216:219], v[60:63]\n"
        "v_max3_f32 v232, v232, v77, v78\n"
        "v_max_f32 v232, v232, v79\n"
        "v_mfma_f32_16x16x32_bf16 v[124:127], v[228:231], v[224:227], v[124:127]\n"
        "s_waitcnt lgkmcnt(4)\n"
        "v_max3_f32 v233, v233, v87, v88\n"
        "v_max3_f32 v233, v233, v89, v90\n"
        "v_mfma_f32_16x16x32_bf16 v[172:175], v[152:155], a[216:219], 0\n"
        "s_waitcnt lgkmcnt(3)\n"
        "v_max3_f32 v233, v233, v91, v92\n"
        "v_max3_f32 v233, v233, v93, v94\n"
        "v_mfma_f32_16x16x32_bf16 v[172:175], v[160:163], a[220:223], v[172:175]\n"
        "v_max_f32 v233, v233, v95\n"
        "v_max3_f32 v234, v234, v103, v104\n"
        "v_mfma_f32_16x16x32_bf16 v[180:183], v[164:167], a[220:223], 0\n"
        "ds_read_b128 a[220:223], v248 offset:6176\n"
        "v_max3_f32 v234, v234, v105, v106\n"
        "v_mfma_f32_16x16x32_bf16 v[180:183], v[156:159], a[216:219], v[180:183]\n"
        "s_waitcnt lgkmcnt(3)\n"
        "ds_read_b128 a[216:219], v254 offset:48\n"
        "v_max3_f32 v234, v234, v107, v108\n"
        "v_mfma_f32_16x16x32_bf16 v[184:187], v[156:159], a[224:227], 0\n"
        "s_waitcnt lgkmcnt(3)\n"
        "ds_read_b128 a[236:239], v247 offset:8192\n"
        "v_max3_f32 v234, v234, v109, v110\n"
        "v_mfma_f32_16x16x32_bf16 v[184:187], v[164:167], a[228:231], v[184:187]\n"
        "ds_read_b128 a[244:247], v247 offset:12288\n"
        "v_max_f32 v234, v234, v111\n"
        "v_mfma_f32_16x16x32_bf16 v[176:179], v[160:163], a[228:231], 0\n"
        "ds_read_b128 a[228:231], v254 offset:6192\n"
        "v_max3_f32 v235, v235, v117, v118\n"
        "v_mfma_f32_16x16x32_bf16 v[176:179], v[152:155], a[224:227], v[176:179]\n"
        "s_waitcnt lgkmcnt(5)\n"
        "ds_read_b128 a[224:227], v248 offset:64\n"
        "v_max3_f32 v235, v235, v119, v120\n"
        "v_mfma_f32_16x16x32_bf16 v[212:215], v[152:155], a[232:235], 0\n"
        "s_waitcnt lgkmcnt(5)\n"
        "ds_read_b128 a[240:243], v247 offset:8448\n"
        "v_max3_f32 v235, v235, v121, v122\n"
        "v_mfma_f32_16x16x32_bf16 v[212:215], v[160:163], a[220:223], v[212:215]\n"
        "ds_read_b128 a[248:251], v247 offset:12544\n"
        "v_max3_f32 v235, v235, v123, v124\n"
        "v_mfma_f32_16x16x32_bf16 v[192:195], v[164:167], a[220:223], 0\n"
        "ds_read_b128 a[220:223], v248 offset:6208\n"
        "v_max3_f32 v235, v235, v125, v126\n"
        "v_mfma_f32_16x16x32_bf16 v[192:195], v[156:159], a[232:235], v[192:195]\n"
        "s_waitcnt lgkmcnt(7)\n"
        "ds_read_b128 a[232:235], v254 offset:80\n"
        "v_max_f32 v235, v235, v127\n"
        "v_mfma_f32_16x16x32_bf16 v[196:199], v[156:159], a[216:219], 0\n"
        "s_waitcnt lgkmcnt(5)\n"
        "v_permlane32_swap_b32 v232, v233\n"
        "v_mfma_f32_16x16x32_bf16 v[196:199], v[164:167], a[228:231], v[196:199]\n"
        "v_max_f32 v232, v232, v233\n"
        "v_mov_b32 v233, v232\n"
        "v_mfma_f32_16x16x32_bf16 v[216:219], v[160:163], a[228:231], 0\n"
        "ds_read_b128 a[228:231], v254 offset:6224\n"
        "v_xor_b32 v251, 0x2000, v251\n"
        "v_mfma_f32_16x16x32_bf16 v[216:219], v[152:155], a[216:219], v[216:219]\n"
        "s_waitcnt lgkmcnt(5)\n"
        "ds_read_b128 a[216:219], v248 offset:36864\n"
        "v_xor_b32 v252, 0x2000, v252\n"
        "v_mfma_f32_16x16x32_bf16 v[220:223], v[152:155], a[224:227], 0\n"
        "s_waitcnt lgkmcnt(3)\n"
        "v_permlane16_swap_b32 v232, v233\n"
        "v_mfma_f32_16x16x32_bf16 v[220:223], v[160:163], a[220:223], v[220:223]\n"
        "v_max_f32 v232, v232, v233\n"
        "v_mul_f32 v236, s25, v232\n"
        "v_mfma_f32_16x16x32_bf16 v[200:203], v[164:167], a[220:223], 0\n"
        "ds_read_b128 a[220:223], v248 offset:43008\n"
        "v_mul_f32 v237, s25, v232\n"
        "v_mfma_f32_16x16x32_bf16 v[200:203], v[156:159], a[224:227], v[200:203]\n"
        "s_waitcnt lgkmcnt(3)\n"
        "ds_read_b128 a[224:227], v254 offset:36880\n"
        "v_add_u32 v245, s45, v245\n"
        "v_mfma_f32_16x16x32_bf16 v[204:207], v[156:159], a[232:235], 0\n"
        "s_waitcnt lgkmcnt(3)\n"
        "v_permlane32_swap_b32 v236, v237\n"
        "v_mfma_f32_16x16x32_bf16 v[204:207], v[164:167], a[228:231], v[204:207]\n"
        "v_permlane32_swap_b32 v234, v235\n"
        "v_mfma_f32_16x16x32_bf16 v[224:227], v[160:163], a[228:231], 0\n"
        "ds_read_b128 a[228:231], v254 offset:43024\n"
        "v_max_f32 v234, v234, v235\n"
        "v_mfma_f32_16x16x32_bf16 v[224:227], v[152:155], a[232:235], v[224:227]\n"
        "s_waitcnt lgkmcnt(3)\n"
        "ds_read_b128 a[232:235], v248 offset:36896\n"
        "v_mov_b32 v235, v234\n"
        "v_mfma_f32_16x16x32_bf16 v[172:175], a[236:239], a[216:219], v[172:175]\n"
        "s_waitcnt lgkmcnt(3)\n"
        "v_fma_f32 v64, v64, s24, v236\n"
        "v_fma_f32 v65, v65, s24, v236\n"
        "v_mfma_f32_16x16x32_bf16 v[172:175], a[244:247], a[220:223], v[172:175]\n"
        "v_permlane16_swap_b32 v234, v235\n"
        "v_mfma_f32_16x16x32_bf16 v[180:183], a[248:251], a[220:223], v[180:183]\n"
        "ds_read_b128 a[220:223], v248 offset:43040\n"
        "v_max_f32 v234, v234, v235\n"
        "v_mfma_f32_16x16x32_bf16 v[180:183], a[240:243], a[216:219], v[180:183]\n"
        "s_waitcnt lgkmcnt(3)\n"
        "ds_read_b128 a[216:219], v254 offset:36912\n"
        "v_mul_f32 v238, s25, v234\n"
        "v_mfma_f32_16x16x32_bf16 v[184:187], a[240:243], a[224:227], v[184:187]\n"
        "s_waitcnt lgkmcnt(3)\n"
        "ds_read_b128 v[152:155], v247 offset:16384\n"
        "v_mul_f32 v239, s25, v234\n"
        "v_mfma_f32_16x16x32_bf16 v[184:187], a[248:251], a[228:231], v[184:187]\n"
        "ds_read_b128 v[160:163], v247 offset:20480\n"
        "v_fma_f32 v66, v66, s24, v236\n"
        "v_mfma_f32_16x16x32_bf16 v[176:179], a[244:247], a[228:231], v[176:179]\n"
        "ds_read_b128 a[228:231], v254 offset:43056\n"
        "v_fma_f32 v67, v67, s24, v236\n"
        "v_mfma_f32_16x16x32_bf16 v[176:179], a[236:239], a[224:227], v[176:179]\n"
        "s_waitcnt lgkmcnt(5)\n"
        "ds_read_b128 a[224:227], v248 offset:36928\n"
        "v_fma_f32 v68, v68, s24, v236\n"
        "v_mfma_f32_16x16x32_bf16 v[212:215], a[236:239], a[232:235], v[212:215]\n"
        "s_waitcnt lgkmcnt(5)\n"
        "ds_read_b128 v[156:159], v247 offset:16640\n"
        "v_fma_f32 v69, v69, s24, v236\n"
        "v_mfma_f32_16x16x32_bf16 v[212:215], a[244:247], a[220:223], v[212:215]\n"
        "ds_read_b128 v[164:167], v247 offset:20736\n"
        "v_fma_f32 v70, v70, s24, v236\n"
        "v_mfma_f32_16x16x32_bf16 v[192:195], a[248:251], a[220:223], v[192:195]\n"
        "ds_read_b128 a[220:223], v248 offset:43072\n"
        "v_fma_f32 v71, v71, s24, v236\n"
        "v_mfma_f32_16x16x32_bf16 v[192:195], a[240:243], a[232:235], v[192:195]\n"
        "s_waitcnt lgkmcnt(7)\n"
        "ds_read_b128 a[232:235], v254 offset:36944\n"
        "v_fma_f32 v72, v72, s24, v236\n"
        "v_mfma_f32_16x16x32_bf16 v[196:199], a[240:243], a[216:219], v[196:199]\n"
        "s_waitcnt lgkmcnt(5)\n"
        "v_permlane32_swap_b32 v238, v239\n"
        "v_mfma_f32_16x16x32_bf16 v[196:199], a[248:251], a[228:231], v[196:199]\n"
        "v_exp_f32 v64, v64\n"
        "v_mfma_f32_16x16x32_bf16 v[216:219], a[244:247], a[228:231], v[216:219]\n"
        "ds_read_b128 a[228:231], v254 offset:43088\n"
        "v_fma_f32 v73, v73, s24, v236\n"
        "v_mfma_f32_16x16x32_bf16 v[216:219], a[236:239], a[216:219], v[216:219]\n"
        "s_waitcnt lgkmcnt(5)\n"
        "ds_read_b128 a[216:219], v248 offset:12288\n"
        "v_fma_f32 v74, v74, s24, v236\n"
        "v_mfma_f32_16x16x32_bf16 v[220:223], a[236:239], a[224:227], v[220:223]\n"
        "s_waitcnt lgkmcnt(3)\n"
        "v_exp_f32 v65, v65\n"
        "v_mfma_f32_16x16x32_bf16 v[220:223], a[244:247], a[220:223], v[220:223]\n"
        "v_exp_f32 v66, v66\n"
        "v_mfma_f32_16x16x32_bf16 v[200:203], a[248:251], a[220:223], v[200:203]\n"
        "ds_read_b128 a[220:223], v248 offset:49152\n"
        "v_cvt_pk_bf16_f32 v64, v64, v65\n"
        "v_mfma_f32_16x16x32_bf16 v[200:203], a[240:243], a[224:227], v[200:203]\n"
        "s_waitcnt lgkmcnt(3)\n"
        "ds_read_b128 a[224:227], v254 offset:12304\n"
        "v_fma_f32 v75, v75, s24, v236\n"
        "v_mfma_f32_16x16x32_bf16 v[204:207], a[240:243], a[232:235], v[204:207]\n"
        "s_waitcnt lgkmcnt(3)\n"
        "v_exp_f32 v67, v67\n"
        "v_mfma_f32_16x16x32_bf16 v[204:207], a[248:251], a[228:231], v[204:207]\n"
        "v_exp_f32 v68, v68\n"
        "v_mfma_f32_16x16x32_bf16 v[224:227], a[244:247], a[228:231], v[224:227]\n"
        "ds_read_b128 a[228:231], v254 offset:49168\n"
        "v_cvt_pk_bf16_f32 v65, v66, v67\n"
        "v_mfma_f32_16x16x32_bf16 v[224:227], a[236:239], a[232:235], v[224:227]\n"
        "s_waitcnt lgkmcnt(3)\n"
        "ds_read_b128 a[232:235], v248 offset:12320\n"
        "v_add_u32 v246, s45, v246\n"
        "v_mfma_f32_16x16x32_bf16 v[172:175], v[152:155], a[216:219], v[172:175]\n"
        "s_waitcnt lgkmcnt(3)\n"
        "v_exp_f32 v69, v69\n"
        "v_mfma_f32_16x16x32_bf16 v[172:175], v[160:163], a[220:223], v[172:175]\n"
        "v_cvt_pk_bf16_f32 v66, v68, v69\n"
        "v_add_u32 v247, s45, v247\n"
        "v_mfma_f32_16x16x32_bf16 v[180:183], v[164:167], a[220:223], v[180:183]\n"
        "ds_read_b128 a[220:223], v248 offset:49184\n"
        "v_mfma_f32_16x16x32_bf16 v[180:183], v[156:159], a[216:219], v[180:183]\n"
        "s_waitcnt lgkmcnt(3)\n"
        "ds_read_b128 a[216:219], v254 offset:12336\n"
        "v_mfma_f32_16x16x32_bf16 v[184:187], v[156:159], a[224:227], v[184:187]\n"
        "s_waitcnt lgkmcnt(3)\n"
        "v_exp_f32 v70, v70\n"
        "v_mfma_f32_16x16x32_bf16 v[184:187], v[164:167], a[228:231], v[184:187]\n"
        "v_exp_f32 v71, v71\n"
        "v_mfma_f32_16x16x32_bf16 v[176:179], v[160:163], a[228:231], v[176:179]\n"
        "ds_read_b128 a[228:231], v254 offset:49200\n"
        "v_cvt_pk_bf16_f32 v67, v70, v71\n"
        "v_mfma_f32_16x16x32_bf16 v[176:179], v[152:155], a[224:227], v[176:179]\n"
        "s_waitcnt lgkmcnt(3)\n"
        "ds_read_b128 a[224:227], v248 offset:12352\n"
        "v_mfma_f32_16x16x32_bf16 v[212:215], v[152:155], a[232:235], v[212:215]\n"
        "s_waitcnt lgkmcnt(3)\n"
        "s_waitcnt vmcnt(0)\n"
        "s_barrier\n"
        "v_exp_f32 v72, v72\n"
        "v_mfma_f32_16x16x32_bf16 v[212:215], v[160:163], a[220:223], v[212:215]\n"
        "v_exp_f32 v73, v73\n"
        "v_mfma_f32_16x16x32_bf16 v[192:195], v[164:167], a[220:223], v[192:195]\n"
        "ds_read_b128 a[220:223], v248 offset:49216\n"
        "v_cvt_pk_bf16_f32 v68, v72, v73\n"
        "v_mfma_f32_16x16x32_bf16 v[192:195], v[156:159], a[232:235], v[192:195]\n"
        "s_waitcnt lgkmcnt(3)\n"
        "ds_read_b128 a[232:235], v254 offset:12368\n"
        "v_mfma_f32_16x16x32_bf16 v[196:199], v[156:159], a[216:219], v[196:199]\n"
        "s_waitcnt lgkmcnt(3)\n"
        "ds_read_b128 a[236:239], v240 offset:0\n"
        "v_mfma_f32_16x16x32_bf16 v[196:199], v[164:167], a[228:231], v[196:199]\n"
        "ds_read_b128 a[240:243], v241 offset:0\n"
        "v_mfma_f32_16x16x32_bf16 v[216:219], v[160:163], a[228:231], v[216:219]\n"
        "ds_read_b128 a[228:231], v254 offset:49232\n"
        "v_mfma_f32_16x16x32_bf16 v[216:219], v[152:155], a[216:219], v[216:219]\n"
        "s_waitcnt lgkmcnt(5)\n"
        "ds_read_b128 a[244:247], v240 offset:128\n"
        "v_mfma_f32_16x16x32_bf16 v[220:223], v[152:155], a[224:227], v[220:223]\n"
        "s_waitcnt lgkmcnt(5)\n"
        "ds_read_b128 a[248:251], v241 offset:128\n"
        "v_mfma_f32_16x16x32_bf16 v[220:223], v[160:163], a[220:223], v[220:223]\n"
        "ds_read_b128 a[216:219], v240 offset:256\n"
        "v_mfma_f32_16x16x32_bf16 v[200:203], v[164:167], a[220:223], v[200:203]\n"
        "ds_read_b128 a[220:223], v241 offset:256\n"
        "v_mfma_f32_16x16x32_bf16 v[200:203], v[156:159], a[224:227], v[200:203]\n"
        "s_waitcnt lgkmcnt(7)\n"
        "v_exp_f32 v74, v74\n"
        "v_mfma_f32_16x16x32_bf16 v[204:207], v[156:159], a[232:235], v[204:207]\n"
        "s_waitcnt lgkmcnt(4)\n"
        "v_exp_f32 v75, v75\n"
        "v_mfma_f32_16x16x32_bf16 v[204:207], v[164:167], a[228:231], v[204:207]\n"
        "s_sub_u32 s27, s27, 1\n"
        "v_cvt_pk_bf16_f32 v69, v74, v75\n"
        "v_mfma_f32_16x16x32_bf16 v[224:227], v[160:163], a[228:231], v[224:227]\n"
        "s_cmp_lg_u32 s27, 0\n"
        "v_mfma_f32_16x16x32_bf16 v[224:227], v[152:155], a[232:235], v[224:227]\n"
        "s_cbranch_scc1 10b\n"
        "20:\n"
        "s_waitcnt lgkmcnt(0)\n"
        "s_nop 7\n"
        "v_mfma_f32_16x16x32_bf16 v[48:51], a[252:255], v[64:67], 0\n"
        "s_and_b32 s46, s42, 1\n"
        "s_lshl_b32 s31, s46, 8\n"
        "s_mul_i32 s45, s46, 786432\n"
        "s_sub_u32 s37, s37, s45\n"
        "s_cmp_eq_u32 s46, 0\n"
        "s_cselect_b32 s43, s43, s44\n"
        "s_add_u32 s28, s37, 786432\n"
        "s_cmp_eq_u32 s28, 100663296\n"
        "s_cselect_b32 s28, 0, s28\n"
        "v_fma_f32 v76, v76, s24, v236\n"
        "v_exp_f32 v76, v76\n"
        "v_fma_f32 v77, v77, s24, v236\n"
        "v_exp_f32 v77, v77\n"
        "v_fma_f32 v78, v78, s24, v236\n"
        "v_cvt_pk_bf16_f32 v70, v76, v77\n"
        "v_exp_f32 v78, v78\n"
        "v_fma_f32 v79, v79, s24, v236\n"
        "v_exp_f32 v79, v79\n"
        "v_subrev_u32 v247, s31, v247\n"
        "v_cvt_pk_bf16_f32 v71, v78, v79\n"
        "s_nop 1\n"
        "v_mfma_f32_16x16x32_bf16 v[48:51], a[252:255], v[68:71], v[48:51]\n"
        "v_cvt_pk_bf16_f32 v232, v172, v176\n"
        "v_cvt_pk_bf16_f32 v233, v212, v216\n"
        "v_cvt_pk_bf16_f32 v234, v220, v224\n"
        "v_fma_f32 v80, v80, s24, v237\n"
        "v_exp_f32 v80, v80\n"
        "v_mfma_f32_16x16x32_bf16 v[52:55], v[132:135], v[68:71], 0\n"
        "buffer_store_dwordx3 v[232:234], v250, s[16:19], s38 offen offset:0 sc1\n"
        "ds_read_b128 a[216:219], v248 offset:0\n"
        "v_mfma_f32_16x16x32_bf16 v[52:55], v[128:131], v[64:67], v[52:55]\n"
        "ds_read_b128 a[220:223], v248 offset:6144\n"
        "v_cvt_pk_bf16_f32 v232, v173, v177\n"
        "v_cvt_pk_bf16_f32 v233, v213, v217\n"
        "v_cvt_pk_bf16_f32 v234, v221, v225\n"
        "v_fma_f32 v81, v81, s24, v237\n"
        "v_fma_f32 v82, v82, s24, v237\n"
        "v_mfma_f32_16x16x32_bf16 v[56:59], v[136:139], v[64:67], 0\n"
        "buffer_store_dwordx3 v[232:234], v250, s[16:19], s38 offen offset:384 sc1\n"
        "ds_read_b128 a[224:227], v248 offset:36864\n"
        "v_mfma_f32_16x16x32_bf16 v[56:59], v[140:143], v[68:71], v[56:59]\n"
        "ds_read_b128 a[228:231], v248 offset:43008\n"
        "v_cvt_pk_bf16_f32 v232, v174, v178\n"
        "v_cvt_pk_bf16_f32 v233, v214, v218\n"
        "v_cvt_pk_bf16_f32 v234, v222, v226\n"
        "v_exp_f32 v81, v81\n"
        "v_mfma_f32_16x16x32_bf16 v[60:63], v[148:151], v[68:71], 0\n"
        "buffer_store_dwordx3 v[232:234], v250, s[16:19], s38 offen offset:768 sc1\n"
        "ds_read_b128 a[232:235], v248 offset:12288\n"
        "v_mfma_f32_16x16x32_bf16 v[60:63], v[144:147], v[64:67], v[60:63]\n"
        "ds_read_b128 a[236:239], v248 offset:49152\n"
        "v_cvt_pk_bf16_f32 v232, v175, v179\n"
        "v_cvt_pk_bf16_f32 v233, v215, v219\n"
        "v_cvt_pk_bf16_f32 v234, v223, v227\n"
        "v_cvt_pk_bf16_f32 v80, v80, v81\n"
        "v_fma_f32 v83, v83, s24, v237\n"
        "v_exp_f32 v82, v82\n"
        "v_exp_f32 v83, v83\n"
        "v_fma_f32 v84, v84, s24, v237\n"
        "v_cvt_pk_bf16_f32 v81, v82, v83\n"
        "v_exp_f32 v84, v84\n"
        "v_fma_f32 v85, v85, s24, v237\n"
        "v_exp_f32 v85, v85\n"
        "v_fma_f32 v86, v86, s24, v237\n"
        "v_cvt_pk_bf16_f32 v82, v84, v85\n"
        "v_exp_f32 v86, v86\n"
        "v_fma_f32 v87, v87, s24, v237\n"
        "v_exp_f32 v87, v87\n"
        "v_fma_f32 v88, v88, s24, v237\n"
        "v_cvt_pk_bf16_f32 v83, v86, v87\n"
        "s_nop 1\n"
        "v_mfma_f32_16x16x32_bf16 v[168:171], a[252:255], v[80:83], 0\n"
        "buffer_store_dwordx3 v[232:234], v250, s[16:19], s38 offen offset:1152 sc1\n"
        "ds_read_b128 a[240:243], v254 offset:16\n"
        "v_exp_f32 v88, v88\n"
        "v_fma_f32 v89, v89, s24, v237\n"
        "v_exp_f32 v89, v89\n"
        "v_fma_f32 v90, v90, s24, v237\n"
        "v_cvt_pk_bf16_f32 v84, v88, v89\n"
        "v_exp_f32 v90, v90\n"
        "v_fma_f32 v91, v91, s24, v237\n"
        "v_exp_f32 v91, v91\n"
        "v_fma_f32 v92, v92, s24, v237\n"
        "v_cvt_pk_bf16_f32 v85, v90, v91\n"
        "v_exp_f32 v92, v92\n"
        "v_fma_f32 v93, v93, s24, v237\n"
        "v_exp_f32 v93, v93\n"
        "v_fma_f32 v94, v94, s24, v237\n"
        "v_cvt_pk_bf16_f32 v86, v92, v93\n"
        "v_exp_f32 v94, v94\n"
        "v_fma_f32 v95, v95, s24, v237\n"
        "v_exp_f32 v95, v95\n"
        "v_cvt_pk_bf16_f32 v232, v180, v184\n"
        "v_cvt_pk_bf16_f32 v87, v94, v95\n"
        "s_nop 1\n"
        "v_mfma_f32_16x16x32_bf16 v[168:171], a[252:255], v[84:87], v[168:171]\n"
        "ds_read_b128 a[244:247], v254 offset:6160\n"
        "v_cvt_pk_bf16_f32 v233, v192, v196\n"
        "v_cvt_pk_bf16_f32 v234, v200, v204\n"
        "v_rcp_f32 v48, v48\n"
        "v_fma_f32 v96, v96, s24, v238\n"
        "v_mfma_f32_16x16x32_bf16 v[188:191], v[132:135], v[84:87], 0\n"
        "buffer_store_dwordx3 v[232:234], v250, s[16:19], s39 offen offset:0 sc1\n"
        "ds_read_b128 a[248:251], v254 offset:36880\n"
        "v_mfma_f32_16x16x32_bf16 v[188:191], v[128:131], v[80:83], v[188:191]\n"
        "v_cvt_pk_bf16_f32 v232, v181, v185\n"
        "v_cvt_pk_bf16_f32 v233, v193, v197\n"
        "v_cvt_pk_bf16_f32 v234, v201, v205\n"
        "v_mul_f32 v52, v48, v52\n"
        "v_mul_f32 v53, v48, v53\n"
        "v_mul_f32 v54, v48, v54\n"
        "v_mfma_f32_16x16x32_bf16 v[208:211], v[136:139], v[80:83], 0\n"
        "buffer_store_dwordx3 v[232:234], v250, s[16:19], s39 offen offset:384 sc1\n"
        "v_mul_f32 v55, v48, v55\n"
        "v_mfma_f32_16x16x32_bf16 v[208:211], v[140:143], v[84:87], v[208:211]\n"
        "v_cvt_pk_bf16_f32 v232, v182, v186\n"
        "v_cvt_pk_bf16_f32 v233, v194, v198\n"
        "v_cvt_pk_bf16_f32 v234, v202, v206\n"
        "v_cvt_pk_bf16_f32 v52, v52, v53\n"
        "v_cvt_pk_bf16_f32 v53, v54, v55\n"
        "v_mul_f32 v56, v48, v56\n"
        "v_mfma_f32_16x16x32_bf16 v[228:231], v[148:151], v[84:87], 0\n"
        "buffer_store_dwordx3 v[232:234], v250, s[16:19], s39 offen offset:768 sc1\n"
        "v_mul_f32 v57, v48, v57\n"
        "v_mfma_f32_16x16x32_bf16 v[228:231], v[144:147], v[80:83], v[228:231]\n"
        "v_cvt_pk_bf16_f32 v232, v183, v187\n"
        "v_cvt_pk_bf16_f32 v233, v195, v199\n"
        "v_cvt_pk_bf16_f32 v234, v203, v207\n"
        "v_mul_f32 v58, v48, v58\n"
        "v_mul_f32 v59, v48, v59\n"
        "v_cvt_pk_bf16_f32 v54, v56, v57\n"
        "v_cvt_pk_bf16_f32 v55, v58, v59\n"
        "v_mul_f32 v60, v48, v60\n"
        "v_mul_f32 v61, v48, v61\n"
        "v_mul_f32 v62, v48, v62\n"
        "v_mul_f32 v63, v48, v63\n"
        "v_cvt_pk_bf16_f32 v60, v60, v61\n"
        "v_cvt_pk_bf16_f32 v61, v62, v63\n"
        "ds_write_b128 v245, v[52:55] offset:0\n"
        "ds_write_b64 v246, v[60:61] offset:0\n"
        "v_rcp_f32 v168, v168\n"
        "v_exp_f32 v96, v96\n"
        "v_mul_f32 v188, v168, v188\n"
        "v_mul_f32 v189, v168, v189\n"
        "v_mul_f32 v190, v168, v190\n"
        "v_mul_f32 v191, v168, v191\n"
        "v_cvt_pk_bf16_f32 v188, v188, v189\n"
        "v_cvt_pk_bf16_f32 v189, v190, v191\n"
        "v_mul_f32 v208, v168, v208\n"
        "v_mul_f32 v209, v168, v209\n"
        "v_mul_f32 v210, v168, v210\n"
        "v_mul_f32 v211, v168, v211\n"
        "v_cvt_pk_bf16_f32 v190, v208, v209\n"
        "v_cvt_pk_bf16_f32 v191, v210, v211\n"
        "v_mul_f32 v228, v168, v228\n"
        "v_mul_f32 v229, v168, v229\n"
        "v_mul_f32 v230, v168, v230\n"
        "v_mul_f32 v231, v168, v231\n"
        "v_cvt_pk_bf16_f32 v228, v228, v229\n"
        "v_cvt_pk_bf16_f32 v229, v230, v231\n"
        "ds_write_b128 v245, v[188:191] offset:256\n"
        "ds_write_b64 v246, v[228:229] offset:256\n"
        "s_waitcnt lgkmcnt(0)\n"
        "s_barrier\n"
        "ds_read_b128 v[152:155], v247 offset:0\n"
        "s_waitcnt lgkmcnt(0)\n"
        "v_mfma_f32_16x16x32_bf16 v[0:3], v[152:155], a[216:219], 0\n"
        "buffer_store_dwordx3 v[232:234], v250, s[16:19], s39 offen offset:1152 sc1\n"
        "ds_read_b128 v[156:159], v247 offset:4096\n"
        "s_waitcnt lgkmcnt(0)\n"
        "v_mfma_f32_16x16x32_bf16 v[0:3], v[156:159], a[220:223], v[0:3]\n"
        "ds_read_b128 v[172:175], v247 offset:8192\n"
        "ds_read_b128 v[176:179], v247 offset:12288\n"
        "ds_read_b128 v[212:215], v247 offset:16384\n"
        "ds_read_b128 v[216:219], v247 offset:20480\n"
        "s_waitcnt lgkmcnt(3)\n"
        "v_mfma_f32_16x16x32_bf16 v[0:3], v[172:175], a[224:227], v[0:3]\n"
        "s_waitcnt lgkmcnt(2)\n"
        "ds_read_b128 a[216:219], v254 offset:43024\n"
        "ds_read_b128 a[220:223], v254 offset:12304\n"
        "v_fma_f32 v97, v97, s24, v238\n"
        "v_fma_f32 v98, v98, s24, v238\n"
        "v_mfma_f32_16x16x32_bf16 v[0:3], v[176:179], a[228:231], v[0:3]\n"
        "s_waitcnt lgkmcnt(3)\n"
        "ds_read_b128 a[224:227], v254 offset:49168\n"
        "ds_read_b128 a[228:231], v248 offset:32\n"
        "v_exp_f32 v97, v97\n"
        "v_mfma_f32_16x16x32_bf16 v[0:3], v[212:215], a[232:235], v[0:3]\n"
        "s_waitcnt lgkmcnt(4)\n"
        "ds_read_b128 a[232:235], v248 offset:6176\n"
        "v_exp_f32 v98, v98\n"
        "v_cvt_pk_bf16_f32 v96, v96, v97\n"
        "v_mfma_f32_16x16x32_bf16 v[0:3], v[216:219], a[236:239], v[0:3]\n"
        "ds_read_b128 a[236:239], v248 offset:36896\n"
        "v_fma_f32 v99, v99, s24, v238\n"
        "v_exp_f32 v99, v99\n"
        "v_mfma_f32_16x16x32_bf16 v[4:7], v[152:155], a[240:243], 0\n"
        "ds_read_b128 a[240:243], v248 offset:43040\n"
        "v_cvt_pk_bf16_f32 v97, v98, v99\n"
        "v_fma_f32 v100, v100, s24, v238\n"
        "v_fma_f32 v101, v101, s24, v238\n"
        "v_mfma_f32_16x16x32_bf16 v[4:7], v[156:159], a[244:247], v[4:7]\n"
        "ds_read_b128 a[244:247], v248 offset:12320\n"
        "v_exp_f32 v100, v100\n"
        "v_fma_f32 v102, v102, s24, v238\n"
        "v_mfma_f32_16x16x32_bf16 v[4:7], v[172:175], a[248:251], v[4:7]\n"
        "s_waitcnt lgkmcnt(7)\n"
        "ds_read_b128 a[248:251], v248 offset:49184\n"
        "v_exp_f32 v101, v101\n"
        "v_fma_f32 v103, v103, s24, v238\n"
        "v_mfma_f32_16x16x32_bf16 v[4:7], v[176:179], a[216:219], v[4:7]\n"
        "s_waitcnt lgkmcnt(7)\n"
        "v_cvt_pk_bf16_f32 v98, v100, v101\n"
        "v_exp_f32 v102, v102\n"
        "v_fma_f32 v104, v104, s24, v238\n"
        "v_mfma_f32_16x16x32_bf16 v[4:7], v[212:215], a[220:223], v[4:7]\n"
        "s_waitcnt lgkmcnt(6)\n"
        "v_exp_f32 v103, v103\n"
        "v_exp_f32 v104, v104\n"
        "v_mfma_f32_16x16x32_bf16 v[4:7], v[216:219], a[224:227], v[4:7]\n"
        "s_waitcnt lgkmcnt(5)\n"
        "v_cvt_pk_bf16_f32 v99, v102, v103\n"
        "v_fma_f32 v105, v105, s24, v238\n"
        "v_exp_f32 v105, v105\n"
        "v_mfma_f32_16x16x32_bf16 v[8:11], v[152:155], a[228:231], 0\n"
        "s_waitcnt lgkmcnt(4)\n"
        "ds_read_b128 a[216:219], v254 offset:48\n"
        "v_cvt_pk_bf16_f32 v100, v104, v105\n"
        "v_fma_f32 v106, v106, s24, v238\n"
        "v_fma_f32 v107, v107, s24, v238\n"
        "v_mfma_f32_16x16x32_bf16 v[8:11], v[156:159], a[232:235], v[8:11]\n"
        "s_waitcnt lgkmcnt(4)\n"
        "ds_read_b128 a[220:223], v254 offset:6192\n"
        "v_exp_f32 v106, v106\n"
        "v_fma_f32 v108, v108, s24, v238\n"
        "v_mfma_f32_16x16x32_bf16 v[8:11], v[172:175], a[236:239], v[8:11]\n"
        "s_waitcnt lgkmcnt(4)\n"
        "ds_read_b128 a[224:227], v254 offset:36912\n"
        "v_exp_f32 v107, v107\n"
        "v_fma_f32 v109, v109, s24, v238\n"
        "v_mfma_f32_16x16x32_bf16 v[8:11], v[176:179], a[240:243], v[8:11]\n"
        "s_waitcnt lgkmcnt(4)\n"
        "ds_read_b128 a[228:231], v254 offset:43056\n"
        "v_cvt_pk_bf16_f32 v101, v106, v107\n"
        "v_exp_f32 v108, v108\n"
        "v_mfma_f32_16x16x32_bf16 v[8:11], v[212:215], a[244:247], v[8:11]\n"
        "s_waitcnt lgkmcnt(4)\n"
        "ds_read_b128 a[232:235], v254 offset:12336\n"
        "v_exp_f32 v109, v109\n"
        "v_fma_f32 v110, v110, s24, v238\n"
        "v_mfma_f32_16x16x32_bf16 v[8:11], v[216:219], a[248:251], v[8:11]\n"
        "ds_read_b128 a[236:239], v254 offset:49200\n"
        "s_add_u32 s38, s37, s43\n"
        "v_cvt_pk_bf16_f32 v102, v108, v109\n"
        "v_exp_f32 v110, v110\n"
        "v_mfma_f32_16x16x32_bf16 v[64:67], a[252:255], v[96:99], 0\n"
        "ds_read_b128 a[240:243], v248 offset:64\n"
        "s_add_u32 s39, s28, s43\n"
        "v_fma_f32 v111, v111, s24, v238\n"
        "v_exp_f32 v111, v111\n"
        "v_fma_f32 v112, v112, s24, v239\n"
        "v_cvt_pk_bf16_f32 v103, v110, v111\n"
        "s_nop 1\n"
        "v_mfma_f32_16x16x32_bf16 v[64:67], a[252:255], v[100:103], v[64:67]\n"
        "ds_read_b128 a[244:247], v248 offset:6208\n"
        "v_exp_f32 v112, v112\n"
        "v_fma_f32 v113, v113, s24, v239\n"
        "v_mfma_f32_16x16x32_bf16 v[68:71], v[132:135], v[100:103], 0\n"
        "ds_read_b128 a[248:251], v248 offset:36928\n"
        "v_exp_f32 v113, v113\n"
        "v_fma_f32 v114, v114, s24, v239\n"
        "v_mfma_f32_16x16x32_bf16 v[68:71], v[128:131], v[96:99], v[68:71]\n"
        "v_rcp_f32 v64, v64\n"
        "v_exp_f32 v114, v114\n"
        "v_mfma_f32_16x16x32_bf16 v[72:75], v[136:139], v[96:99], 0\n"
        "v_cvt_pk_bf16_f32 v112, v112, v113\n"
        "v_fma_f32 v115, v115, s24, v239\n"
        "v_exp_f32 v115, v115\n"
        "v_mfma_f32_16x16x32_bf16 v[72:75], v[140:143], v[100:103], v[72:75]\n"
        "v_cvt_pk_bf16_f32 v113, v114, v115\n"
        "v_mul_f32 v68, v64, v68\n"
        "v_mul_f32 v69, v64, v69\n"
        "v_mul_f32 v70, v64, v70\n"
        "v_mfma_f32_16x16x32_bf16 v[76:79], v[148:151], v[100:103], 0\n"
        "v_mul_f32 v71, v64, v71\n"
        "v_cvt_pk_bf16_f32 v68, v68, v69\n"
        "v_cvt_pk_bf16_f32 v69, v70, v71\n"
        "v_mul_f32 v72, v64, v72\n"
        "v_mfma_f32_16x16x32_bf16 v[76:79], v[144:147], v[96:99], v[76:79]\n"
        "s_waitcnt lgkmcnt(8)\n"
        "v_mul_f32 v73, v64, v73\n"
        "v_mul_f32 v74, v64, v74\n"
        "v_mul_f32 v75, v64, v75\n"
        "v_cvt_pk_bf16_f32 v70, v72, v73\n"
        "v_mfma_f32_16x16x32_bf16 v[12:15], v[152:155], a[216:219], 0\n"
        "s_waitcnt lgkmcnt(7)\n"
        "ds_read_b128 a[216:219], v248 offset:43072\n"
        "v_cvt_pk_bf16_f32 v71, v74, v75\n"
        "v_fma_f32 v116, v116, s24, v239\n"
        "v_mul_f32 v76, v64, v76\n"
        "v_mfma_f32_16x16x32_bf16 v[12:15], v[156:159], a[220:223], v[12:15]\n"
        "s_waitcnt lgkmcnt(7)\n"
        "ds_read_b128 a[220:223], v248 offset:12352\n"
        "v_mul_f32 v77, v64, v77\n"
        "v_mul_f32 v78, v64, v78\n"
        "v_mul_f32 v79, v64, v79\n"
        "v_mfma_f32_16x16x32_bf16 v[12:15], v[172:175], a[224:227], v[12:15]\n"
        "s_waitcnt lgkmcnt(7)\n"
        "ds_write_b128 v245, v[68:71] offset:512\n"
        "v_mfma_f32_16x16x32_bf16 v[12:15], v[176:179], a[228:231], v[12:15]\n"
        "s_waitcnt lgkmcnt(7)\n"
        "ds_read_b128 a[224:227], v248 offset:49216\n"
        "ds_read_b128 a[228:231], v254 offset:80\n"
        "v_cvt_pk_bf16_f32 v76, v76, v77\n"
        "v_cvt_pk_bf16_f32 v77, v78, v79\n"
        "v_mfma_f32_16x16x32_bf16 v[12:15], v[212:215], a[232:235], v[12:15]\n"
        "s_waitcnt lgkmcnt(8)\n"
        "ds_read_b128 a[232:235], v254 offset:6224\n"
        "v_exp_f32 v116, v116\n"
        "v_fma_f32 v117, v117, s24, v239\n"
        "v_mfma_f32_16x16x32_bf16 v[12:15], v[216:219], a[236:239], v[12:15]\n"
        "s_waitcnt lgkmcnt(8)\n"
        "ds_write_b64 v246, v[76:77] offset:512\n"
        "ds_read_b128 a[236:239], v254 offset:36944\n"
        "v_mfma_f32_16x16x32_bf16 v[16:19], v[152:155], a[240:243], 0\n"
        "s_waitcnt lgkmcnt(9)\n"
        "ds_read_b128 a[240:243], v254 offset:43088\n"
        "v_exp_f32 v117, v117\n"
        "v_fma_f32 v118, v118, s24, v239\n"
        "v_mfma_f32_16x16x32_bf16 v[16:19], v[156:159], a[244:247], v[16:19]\n"
        "s_waitcnt lgkmcnt(9)\n"
        "ds_read_b128 a[244:247], v254 offset:12368\n"
        "v_exp_f32 v118, v118\n"
        "v_cvt_pk_bf16_f32 v114, v116, v117\n"
        "v_mfma_f32_16x16x32_bf16 v[16:19], v[172:175], a[248:251], v[16:19]\n"
        "s_waitcnt lgkmcnt(9)\n"
        "ds_read_b128 a[248:251], v254 offset:49232\n"
        "v_fma_f32 v119, v119, s24, v239\n"
        "v_exp_f32 v119, v119\n"
        "v_mfma_f32_16x16x32_bf16 v[16:19], v[176:179], a[216:219], v[16:19]\n"
        "s_waitcnt lgkmcnt(9)\n"
        "v_cvt_pk_bf16_f32 v115, v118, v119\n"
        "v_fma_f32 v120, v120, s24, v239\n"
        "v_exp_f32 v120, v120\n"
        "v_mfma_f32_16x16x32_bf16 v[16:19], v[212:215], a[220:223], v[16:19]\n"
        "s_waitcnt lgkmcnt(7)\n"
        "v_fma_f32 v121, v121, s24, v239\n"
        "v_exp_f32 v121, v121\n"
        "v_fma_f32 v122, v122, s24, v239\n"
        "v_mfma_f32_16x16x32_bf16 v[16:19], v[216:219], a[224:227], v[16:19]\n"
        "s_waitcnt lgkmcnt(6)\n"
        "v_cvt_pk_bf16_f32 v116, v120, v121\n"
        "v_exp_f32 v122, v122\n"
        "v_fma_f32 v123, v123, s24, v239\n"
        "v_mfma_f32_16x16x32_bf16 v[20:23], v[152:155], a[228:231], 0\n"
        "s_waitcnt lgkmcnt(5)\n"
        "ds_read_b128 a[216:219], v248 offset:0\n"
        "v_exp_f32 v123, v123\n"
        "v_fma_f32 v124, v124, s24, v239\n"
        "v_mfma_f32_16x16x32_bf16 v[20:23], v[156:159], a[232:235], v[20:23]\n"
        "s_waitcnt lgkmcnt(4)\n"
        "ds_read_b128 a[220:223], v248 offset:6144\n"
        "v_cvt_pk_bf16_f32 v117, v122, v123\n"
        "v_exp_f32 v124, v124\n"
        "v_mfma_f32_16x16x32_bf16 v[20:23], v[172:175], a[236:239], v[20:23]\n"
        "s_waitcnt lgkmcnt(4)\n"
        "ds_read_b128 a[224:227], v248 offset:36864\n"
        "v_fma_f32 v125, v125, s24, v239\n"
        "v_exp_f32 v125, v125\n"
        "v_mfma_f32_16x16x32_bf16 v[20:23], v[176:179], a[240:243], v[20:23]\n"
        "s_waitcnt lgkmcnt(4)\n"
        "ds_read_b128 a[228:231], v248 offset:43008\n"
        "v_cvt_pk_bf16_f32 v118, v124, v125\n"
        "v_fma_f32 v126, v126, s24, v239\n"
        "v_fma_f32 v127, v127, s24, v239\n"
        "v_mfma_f32_16x16x32_bf16 v[20:23], v[212:215], a[244:247], v[20:23]\n"
        "s_waitcnt lgkmcnt(4)\n"
        "ds_read_b128 a[232:235], v248 offset:12288\n"
        "v_exp_f32 v126, v126\n"
        "v_cvt_pk_bf16_f32 v192, v0, v4\n"
        "v_mfma_f32_16x16x32_bf16 v[20:23], v[216:219], a[248:251], v[20:23]\n"
        "ds_read_b128 a[236:239], v248 offset:49152\n"
        "v_exp_f32 v127, v127\n"
        "v_cvt_pk_bf16_f32 v193, v8, v12\n"
        "v_mfma_f32_16x16x32_bf16 v[80:83], a[252:255], v[112:115], 0\n"
        "ds_read_b128 a[240:243], v254 offset:16\n"
        "v_cvt_pk_bf16_f32 v119, v126, v127\n"
        "v_cvt_pk_bf16_f32 v196, v1, v5\n"
        "v_cvt_pk_bf16_f32 v197, v9, v13\n"
        "v_mfma_f32_16x16x32_bf16 v[80:83], a[252:255], v[116:119], v[80:83]\n"
        "ds_read_b128 a[244:247], v254 offset:6160\n"
        "v_cvt_pk_bf16_f32 v194, v16, v20\n"
        "v_cvt_pk_bf16_f32 v198, v17, v21\n"
        "v_cvt_pk_bf16_f32 v200, v2, v6\n"
        "v_mfma_f32_16x16x32_bf16 v[84:87], v[132:135], v[116:119], 0\n"
        "buffer_store_dwordx3 v[192:194], v250, s[16:19], s38 offen offset:0 sc1\n"
        "v_mfma_f32_16x16x32_bf16 v[84:87], v[128:131], v[112:115], v[84:87]\n"
        "ds_read_b128 a[248:251], v254 offset:36880\n"
        "v_rcp_f32 v80, v80\n"
        "v_cvt_pk_bf16_f32 v201, v10, v14\n"
        "v_mfma_f32_16x16x32_bf16 v[88:91], v[136:139], v[112:115], 0\n"
        "v_cvt_pk_bf16_f32 v202, v18, v22\n"
        "v_cvt_pk_bf16_f32 v204, v3, v7\n"
        "v_cvt_pk_bf16_f32 v205, v11, v15\n"
        "v_cvt_pk_bf16_f32 v206, v19, v23\n"
        "v_mfma_f32_16x16x32_bf16 v[88:91], v[140:143], v[116:119], v[88:91]\n"
        "buffer_store_dwordx3 v[196:198], v250, s[16:19], s38 offen offset:384 sc1\n"
        "v_mfma_f32_16x16x32_bf16 v[92:95], v[148:151], v[116:119], 0\n"
        "v_mul_f32 v84, v80, v84\n"
        "v_mul_f32 v85, v80, v85\n"
        "v_mul_f32 v86, v80, v86\n"
        "v_mul_f32 v87, v80, v87\n"
        "v_mfma_f32_16x16x32_bf16 v[92:95], v[144:147], v[112:115], v[92:95]\n"
        "v_cvt_pk_bf16_f32 v84, v84, v85\n"
        "v_cvt_pk_bf16_f32 v85, v86, v87\n"
        "v_mul_f32 v88, v80, v88\n"
        "v_mul_f32 v89, v80, v89\n"
        "v_mul_f32 v90, v80, v90\n"
        "v_mul_f32 v91, v80, v91\n"
        "v_cvt_pk_bf16_f32 v86, v88, v89\n"
        "v_cvt_pk_bf16_f32 v87, v90, v91\n"
        "v_mul_f32 v92, v80, v92\n"
        "v_mul_f32 v93, v80, v93\n"
        "v_mul_f32 v94, v80, v94\n"
        "v_mul_f32 v95, v80, v95\n"
        "v_cvt_pk_bf16_f32 v92, v92, v93\n"
        "v_cvt_pk_bf16_f32 v93, v94, v95\n"
        "ds_write_b128 v245, v[84:87] offset:768\n"
        "ds_write_b64 v246, v[92:93] offset:768\n"
        "s_waitcnt lgkmcnt(0)\n"
        "s_barrier\n"
        "ds_read_b128 v[128:131], v247 offset:512\n"
        "s_waitcnt lgkmcnt(0)\n"
        "v_mfma_f32_16x16x32_bf16 v[24:27], v[128:131], a[216:219], 0\n"
        "ds_read_b128 v[132:135], v247 offset:4608\n"
        "ds_read_b128 v[136:139], v247 offset:8704\n"
        "s_waitcnt lgkmcnt(1)\n"
        "v_mfma_f32_16x16x32_bf16 v[24:27], v[132:135], a[220:223], v[24:27]\n"
        "s_waitcnt lgkmcnt(0)\n"
        "ds_read_b128 v[140:143], v247 offset:12800\n"
        "ds_read_b128 v[144:147], v247 offset:16896\n"
        "v_mfma_f32_16x16x32_bf16 v[24:27], v[136:139], a[224:227], v[24:27]\n"
        "s_waitcnt lgkmcnt(1)\n"
        "ds_read_b128 v[148:151], v247 offset:20992\n"
        "ds_read_b128 a[216:219], v254 offset:43024\n"
        "v_mfma_f32_16x16x32_bf16 v[24:27], v[140:143], a[228:231], v[24:27]\n"
        "s_waitcnt lgkmcnt(2)\n"
        "buffer_store_dwordx3 v[200:202], v250, s[16:19], s38 offen offset:768 sc1\n"
        "v_mfma_f32_16x16x32_bf16 v[24:27], v[144:147], a[232:235], v[24:27]\n"
        "s_waitcnt lgkmcnt(1)\n"
        "ds_read_b128 a[220:223], v254 offset:12304\n"
        "ds_read_b128 a[224:227], v254 offset:49168\n"
        "v_mfma_f32_16x16x32_bf16 v[24:27], v[148:151], a[236:239], v[24:27]\n"
        "ds_read_b128 a[228:231], v248 offset:32\n"
        "ds_read_b128 a[232:235], v248 offset:6176\n"
        "v_mfma_f32_16x16x32_bf16 v[28:31], v[128:131], a[240:243], 0\n"
        "buffer_store_dwordx3 v[204:206], v250, s[16:19], s38 offen offset:1152 sc1\n"
        "v_mfma_f32_16x16x32_bf16 v[28:31], v[132:135], a[244:247], v[28:31]\n"
        "ds_read_b128 a[236:239], v248 offset:36896\n"
        "ds_read_b128 a[240:243], v248 offset:43040\n"
        "v_mfma_f32_16x16x32_bf16 v[28:31], v[136:139], a[248:251], v[28:31]\n"
        "s_waitcnt lgkmcnt(6)\n"
        "ds_read_b128 a[244:247], v248 offset:12320\n"
        "ds_read_b128 a[248:251], v248 offset:49184\n"
        "v_mfma_f32_16x16x32_bf16 v[28:31], v[140:143], a[216:219], v[28:31]\n"
        "s_waitcnt lgkmcnt(7)\n"
        "ds_read_b128 a[216:219], v254 offset:48\n"
        "v_mfma_f32_16x16x32_bf16 v[28:31], v[144:147], a[220:223], v[28:31]\n"
        "s_waitcnt lgkmcnt(7)\n"
        "ds_read_b128 a[220:223], v254 offset:6192\n"
        "v_mfma_f32_16x16x32_bf16 v[28:31], v[148:151], a[224:227], v[28:31]\n"
        "s_waitcnt lgkmcnt(7)\n"
        "ds_read_b128 a[224:227], v254 offset:36912\n"
        "v_mfma_f32_16x16x32_bf16 v[32:35], v[128:131], a[228:231], 0\n"
        "s_waitcnt lgkmcnt(7)\n"
        "ds_read_b128 a[228:231], v254 offset:43056\n"
        "v_mfma_f32_16x16x32_bf16 v[32:35], v[132:135], a[232:235], v[32:35]\n"
        "s_waitcnt lgkmcnt(7)\n"
        "ds_read_b128 a[232:235], v254 offset:12336\n"
        "v_mfma_f32_16x16x32_bf16 v[32:35], v[136:139], a[236:239], v[32:35]\n"
        "s_waitcnt lgkmcnt(7)\n"
        "ds_read_b128 a[236:239], v254 offset:49200\n"
        "v_mfma_f32_16x16x32_bf16 v[32:35], v[140:143], a[240:243], v[32:35]\n"
        "s_waitcnt lgkmcnt(7)\n"
        "ds_read_b128 a[240:243], v248 offset:64\n"
        "v_cvt_pk_bf16_f32 v180, v24, v28\n"
        "v_mfma_f32_16x16x32_bf16 v[32:35], v[144:147], a[244:247], v[32:35]\n"
        "s_waitcnt lgkmcnt(7)\n"
        "ds_read_b128 a[244:247], v248 offset:6208\n"
        "v_cvt_pk_bf16_f32 v184, v25, v29\n"
        "v_mfma_f32_16x16x32_bf16 v[32:35], v[148:151], a[248:251], v[32:35]\n"
        "s_waitcnt lgkmcnt(7)\n"
        "ds_read_b128 a[248:251], v248 offset:36928\n"
        "v_cvt_pk_bf16_f32 v220, v26, v30\n"
        "v_mfma_f32_16x16x32_bf16 v[36:39], v[128:131], a[216:219], 0\n"
        "s_waitcnt lgkmcnt(7)\n"
        "ds_read_b128 a[216:219], v248 offset:43072\n"
        "v_cvt_pk_bf16_f32 v224, v27, v31\n"
        "v_mfma_f32_16x16x32_bf16 v[36:39], v[132:135], a[220:223], v[36:39]\n"
        "s_waitcnt lgkmcnt(7)\n"
        "ds_read_b128 a[220:223], v248 offset:12352\n"
        "v_mfma_f32_16x16x32_bf16 v[36:39], v[136:139], a[224:227], v[36:39]\n"
        "s_waitcnt lgkmcnt(7)\n"
        "ds_read_b128 a[224:227], v248 offset:49216\n"
        "v_mfma_f32_16x16x32_bf16 v[36:39], v[140:143], a[228:231], v[36:39]\n"
        "s_waitcnt lgkmcnt(7)\n"
        "ds_read_b128 a[228:231], v254 offset:80\n"
        "v_mfma_f32_16x16x32_bf16 v[36:39], v[144:147], a[232:235], v[36:39]\n"
        "s_waitcnt lgkmcnt(7)\n"
        "ds_read_b128 a[232:235], v254 offset:6224\n"
        "v_mfma_f32_16x16x32_bf16 v[36:39], v[148:151], a[236:239], v[36:39]\n"
        "s_waitcnt lgkmcnt(7)\n"
        "ds_read_b128 a[236:239], v254 offset:36944\n"
        "v_mfma_f32_16x16x32_bf16 v[40:43], v[128:131], a[240:243], 0\n"
        "s_waitcnt lgkmcnt(7)\n"
        "ds_read_b128 a[240:243], v254 offset:43088\n"
        "v_mfma_f32_16x16x32_bf16 v[40:43], v[132:135], a[244:247], v[40:43]\n"
        "s_waitcnt lgkmcnt(7)\n"
        "ds_read_b128 a[244:247], v254 offset:12368\n"
        "v_mfma_f32_16x16x32_bf16 v[40:43], v[136:139], a[248:251], v[40:43]\n"
        "s_waitcnt lgkmcnt(7)\n"
        "ds_read_b128 a[248:251], v254 offset:49232\n"
        "v_mfma_f32_16x16x32_bf16 v[40:43], v[140:143], a[216:219], v[40:43]\n"
        "s_waitcnt lgkmcnt(7)\n"
        "v_cvt_pk_bf16_f32 v181, v32, v36\n"
        "v_cvt_pk_bf16_f32 v185, v33, v37\n"
        "v_mfma_f32_16x16x32_bf16 v[40:43], v[144:147], a[220:223], v[40:43]\n"
        "s_waitcnt lgkmcnt(6)\n"
        "v_cvt_pk_bf16_f32 v221, v34, v38\n"
        "v_cvt_pk_bf16_f32 v225, v35, v39\n"
        "v_mfma_f32_16x16x32_bf16 v[40:43], v[148:151], a[224:227], v[40:43]\n"
        "s_waitcnt lgkmcnt(5)\n"
        "v_mfma_f32_16x16x32_bf16 v[44:47], v[128:131], a[228:231], 0\n"
        "s_waitcnt lgkmcnt(4)\n"
        "v_mfma_f32_16x16x32_bf16 v[44:47], v[132:135], a[232:235], v[44:47]\n"
        "s_waitcnt lgkmcnt(3)\n"
        "v_mfma_f32_16x16x32_bf16 v[44:47], v[136:139], a[236:239], v[44:47]\n"
        "s_waitcnt lgkmcnt(2)\n"
        "v_mfma_f32_16x16x32_bf16 v[44:47], v[140:143], a[240:243], v[44:47]\n"
        "s_waitcnt lgkmcnt(1)\n"
        "v_mfma_f32_16x16x32_bf16 v[44:47], v[144:147], a[244:247], v[44:47]\n"
        "s_waitcnt lgkmcnt(0)\n"
        "v_mfma_f32_16x16x32_bf16 v[44:47], v[148:151], a[248:251], v[44:47]\n"
        "s_nop 7\n"
        "v_cvt_pk_bf16_f32 v182, v40, v44\n"
        "buffer_store_dwordx3 v[180:182], v250, s[16:19], s39 offen offset:0 sc1\n"
        "v_cvt_pk_bf16_f32 v186, v41, v45\n"
        "buffer_store_dwordx3 v[184:186], v250, s[16:19], s39 offen offset:384 sc1\n"
        "v_cvt_pk_bf16_f32 v222, v42, v46\n"
        "buffer_store_dwordx3 v[220:222], v250, s[16:19], s39 offen offset:768 sc1\n"
        "v_cvt_pk_bf16_f32 v226, v43, v47\n"
        "buffer_store_dwordx3 v[224:226], v250, s[16:19], s39 offen offset:1152 sc1\n"
        : "+{v240}"(xr[0]), "+{v241}"(xr[1]), "+{v251}"(mr[0]), "+{v252}"(mr[1]), "+{v242}"(dm[0]), "+{v243}"(dm[1]), "+{v244}"(dm[2]), "+{v245}"(ow), "+{v246}"(ow2), "+{v247}"(ord), "+{v248}"(wor), "+{v249}"(ms), "+{v250}"(st), "+{v142}"(l16), "+{v170}"(id0), "+{v171}"(id1), "+{v139}"(rs), "+{v144}"(rr), "+{v140}"(oa), "+{v141}"(ob), "+{v147}"(od), "+{v145}"(rbs), "+{v146}"(rbw), "+{v254}"(wor2), "+{v255}"(ml), "+{v148}"(pf0), "+{v149}"(pf1), "+{v128}"(wsrc[0]), "+{v129}"(wsrc[1]), "+{v130}"(wsrc[2]), "+{v131}"(wrd[0]), "+{v132}"(wrd[1]), "+{v133}"(wrd[2]), "+{s47}"(ya), "+{s36}"(mk), "+{s37}"(raw), "+{s43}"(x0), "+{s44}"(x1)
        : "{s[8:11]}"(srdx), "{s[12:15]}"(srdm), "{s[16:19]}"(srdo), "{s[20:23]}"(srdw), "{s[52:55]}"(srdwo), "{s[56:59]}"(srdrp), "{s[60:63]}"(srdrb), "{s42}"(wv), "s"(ldsb)
        : "v0", "v1", "v2", "v3", "v4", "v5", "v6", "v7", "v8", "v9", "v10", "v11", "v12", "v13", "v14", "v15", "v16", "v17", "v18", "v19", "v20", "v21", "v22", "v23", "v24", "v25", "v26", "v27", "v28", "v29", "v30", "v31", "v32", "v33", "v34", "v35", "v36", "v37", "v38", "v39", "v40", "v41", "v42", "v43", "v44", "v45", "v46", "v47", "v48", "v49", "v50", "v51", "v52", "v53", "v54", "v55", "v56", "v57", "v58", "v59", "v60", "v61", "v62", "v63", "v64", "v65", "v66", "v67", "v68", "v69", "v70", "v71", "v72", "v73", "v74", "v75", "v76", "v77", "v78", "v79", "v80", "v81", "v82", "v83", "v84", "v85", "v86", "v87", "v88", "v89", "v90", "v91", "v92", "v93", "v94", "v95", "v96", "v97", "v98", "v99", "v100", "v101", "v102", "v103", "v104", "v105", "v106", "v107", "v108", "v109", "v110", "v111", "v112", "v113", "v114", "v115", "v116", "v117", "v118", "v119", "v120", "v121", "v122", "v123", "v124", "v125", "v126", "v127", "v134", "v135", "v136", "v137", "v138", "v143", "v150", "v151", "v152", "v153", "v154", "v155", "v156", "v157", "v158", "v159", "v160", "v161", "v162", "v163", "v164", "v165", "v166", "v167", "v168", "v169", "v172", "v173", "v174", "v175", "v176", "v177", "v178", "v179", "v180", "v181", "v182", "v183", "v184", "v185", "v186", "v187", "v188", "v189", "v190", "v191", "v192", "v193", "v194", "v195", "v196", "v197", "v198", "v199", "v200", "v201", "v202", "v203", "v204", "v205", "v206", "v207", "v208", "v209", "v210", "v211", "v212", "v213", "v214", "v215", "v216", "v217", "v218", "v219", "v220", "v221", "v222", "v223", "v224", "v225", "v226", "v227", "v228", "v229", "v230", "v231", "v232", "v233", "v234", "v235", "v236", "v237", "v238", "v239", "v253", "a0", "a1", "a2", "a3", "a4", "a5", "a6", "a7", "a8", "a9", "a10", "a11", "a12", "a13", "a14", "a15", "a16", "a17", "a18", "a19", "a20", "a21", "a22", "a23", "a24", "a25", "a26", "a27", "a28", "a29", "a30", "a31", "a32", "a33", "a34", "a35", "a36", "a37", "a38", "a39", "a40", "a41", "a42", "a43", "a44", "a45", "a46", "a47", "a48", "a49", "a50", "a51", "a52", "a53", "a54", "a55", "a56", "a57", "a58", "a59", "a60", "a61", "a62", "a63", "a64", "a65", "a66", "a67", "a68", "a69", "a70", "a71", "a72", "a73", "a74", "a75", "a76", "a77", "a78", "a79", "a80", "a81", "a82", "a83", "a84", "a85", "a86", "a87", "a88", "a89", "a90", "a91", "a92", "a93", "a94", "a95", "a96", "a97", "a98", "a99", "a100", "a101", "a102", "a103", "a104", "a105", "a106", "a107", "a108", "a109", "a110", "a111", "a112", "a113", "a114", "a115", "a116", "a117", "a118", "a119", "a120", "a121", "a122", "a123", "a124", "a125", "a126", "a127", "a128", "a129", "a130", "a131", "a132", "a133", "a134", "a135", "a136", "a137", "a138", "a139", "a140", "a141", "a142", "a143", "a144", "a145", "a146", "a147", "a148", "a149", "a150", "a151", "a152", "a153", "a154", "a155", "a156", "a157", "a158", "a159", "a160", "a161", "a162", "a163", "a164", "a165", "a166", "a167", "a168", "a169", "a170", "a171", "a172", "a173", "a174", "a175", "a176", "a177", "a178", "a179", "a180", "a181", "a182", "a183", "a184", "a185", "a186", "a187", "a188", "a189", "a190", "a191", "a192", "a193", "a194", "a195", "a196", "a197", "a198", "a199", "a200", "a201", "a202", "a203", "a204", "a205", "a206", "a207", "a208", "a209", "a210", "a211", "a212", "a213", "a214", "a215", "a216", "a217", "a218", "a219", "a220", "a221", "a222", "a223", "a224", "a225", "a226", "a227", "a228", "a229", "a230", "a231", "a232", "a233", "a234", "a235", "a236", "a237", "a238", "a239", "a240", "a241", "a242", "a243", "a244", "a245", "a246", "a247", "a248", "a249", "a250", "a251", "a252", "a253", "a254", "a255", "s24", "s25", "s26", "s27", "s28", "s29", "s30", "s31", "s34", "s35", "s38", "s39", "s40", "s41", "s45", "s46", "s48", "s49", "s50", "s51", "s64", "s65", "s66", "s67", "vcc", "scc", "memory");
}


__global__ __launch_bounds__(256, 1) void win_attn_kernel(const char* __restrict__ x, const char* __restrict__ wqkv,
                                                          const char* __restrict__ wo, const char* __restrict__ rpb,
                                                          const char* __restrict__ rpi, const char* __restrict__ mask,
                                                          char* __restrict__ out) {
    __shared__ __attribute__((aligned(16))) char smem[163840];
    unsigned tid = threadIdx.x, w;
    const unsigned ldsb = (unsigned)(size_t)smem;
    asm volatile(
        "s_load_dwordx2 s[8:9], s[0:1], 0\n"
        "s_load_dwordx2 s[20:21], s[0:1], 8\n"
        "s_load_dwordx2 s[52:53], s[0:1], 16\n"
        "s_load_dwordx2 s[60:61], s[0:1], 24\n"
        "s_load_dwordx2 s[56:57], s[0:1], 32\n"
        "s_load_dwordx2 s[12:13], s[0:1], 40\n"
        "s_load_dwordx2 s[16:17], s[0:1], 48\n"
        "v_readfirstlane_b32 s42, v0\n"
        "v_and_b32 v1, 63, v0\n"
        "v_lshlrev_b32 v145, 3, v0\n"
        "s_lshr_b32 s42, s42, 6\n"
        "s_bfe_u32 s41, s2, 0x50003\n"
        "s_lshl2_add_u32 s41, s41, s42\n"
        "s_lshl_b32 s49, s41, 8\n"
        "v_and_b32 v10, 1, v0\n"
        "v_lshl_add_u32 v10, v10, 7, s49\n"
        "v_min_u32 v10, 24448, v10\n"
        "s_getpc_b64 s[38:39]\n"
        "global_load_dword v26, v10, s[38:39]\n"
        "v_bfe_u32 v2, v0, 4, 2\n"
        "s_and_b32 s51, s42, 1\n"
        "s_lshl_b32 s66, s51, 3\n"
        "v_xor_b32 v11, v1, v2\n"
        "v_xor_b32 v11, s66, v11\n"
        "v_lshlrev_b32 v139, 4, v11\n"
        "v_xor_b32 v144, 64, v139\n"
        "s_lshl_b32 s34, s42, 12\n"
        "s_mul_i32 s40, s42, 3072\n"
        "s_and_b32 s31, s2, 63\n"
        "s_lshl_b32 s31, s31, 3\n"
        "s_add_u32 s31, s31, 4\n"
        "s_lshr_b32 s45, s2, 6\n"
        "s_lshl_b32 s45, s45, 3\n"
        "s_add_u32 s47, s45, s42\n"
        "s_add_u32 s47, s47, 68\n"
        "s_mul_i32 s47, s47, 196608\n"
        "s_lshl_b32 s46, s42, 1\n"
        "s_mov_b32 s62, 1800\n"
        "s_mov_b32 s63, 0x20000\n"
        "s_mov_b32 s58, 16384\n"
        "s_mov_b32 s59, 0x20000\n"
        "s_mov_b32 s10, 100663296\n"
        "s_mov_b32 s11, 0x20000\n"
        "s_mov_b32 s14, 33554432\n"
        "s_mov_b32 s15, 0x20000\n"
        "s_mov_b32 s22, 221184\n"
        "s_mov_b32 s23, 0x20000\n"
        "s_mov_b32 s54, 73728\n"
        "s_mov_b32 s55, 0x20000\n"
        "s_mov_b32 s18, 100663296\n"
        "s_mov_b32 s19, 0x20000\n"
        "v_mul_u32_u24 v15, 171, v1\n"
        "v_lshrrev_b32 v15, 12, v15\n"
        "v_mul_u32_u24 v13, 24, v15\n"
        "v_sub_u32 v18, v1, v13\n"
        "v_bfe_u32 v21, v15, 1, 1\n"
        "v_add_u32 v13, s31, v15\n"
        "v_and_b32 v13, 0x1ff, v13\n"
        "v_mul_u32_u24 v13, 384, v13\n"
        "v_bitop3_b32 v14, v21, v18, s46 bitop3:0x36\n"
        "v_lshl_add_u32 v242, v14, 4, v13\n"
        "v_add_u32 v12, 64, v1\n"
        "v_mul_u32_u24 v16, 171, v12\n"
        "v_lshrrev_b32 v16, 12, v16\n"
        "v_mul_u32_u24 v13, 24, v16\n"
        "v_sub_u32 v19, v12, v13\n"
        "v_bfe_u32 v22, v16, 1, 1\n"
        "v_add_u32 v13, s31, v16\n"
        "v_and_b32 v13, 0x1ff, v13\n"
        "v_mul_u32_u24 v13, 384, v13\n"
        "v_bitop3_b32 v14, v22, v19, s46 bitop3:0x36\n"
        "v_lshl_add_u32 v243, v14, 4, v13\n"
        "v_add_u32 v12, 128, v1\n"
        "v_mul_u32_u24 v17, 171, v12\n"
        "v_lshrrev_b32 v17, 12, v17\n"
        "v_mul_u32_u24 v13, 24, v17\n"
        "v_sub_u32 v20, v12, v13\n"
        "v_bfe_u32 v23, v17, 1, 1\n"
        "v_add_u32 v13, s31, v17\n"
        "v_and_b32 v13, 0x1ff, v13\n"
        "v_mul_u32_u24 v13, 384, v13\n"
        "v_bitop3_b32 v14, v23, v20, s46 bitop3:0x36\n"
        "v_lshl_add_u32 v244, v14, 4, v13\n"
        "s_waitcnt lgkmcnt(0)\n"
        "buffer_load_dwordx2 v[28:29], v145, s[60:63], 0 offen\n"
        "s_add_u32 m0, s34, 122880\n"
        "s_mov_b32 s64, s34\n"
        "buffer_load_dwordx4 v139, s[56:59], s64 offen lds\n"
        "v_bfe_u32 v4, v0, 4, 1\n"
        "s_lshl_b32 s66, s51, 2\n"
        "v_bitop3_b32 v11, v4, v1, s66 bitop3:0x36\n"
        "v_lshlrev_b32 v249, 4, v11\n"
        "v_xor_b32 v149, 32, v249\n"
        "s_lshl_b32 s48, s42, 11\n"
        "s_lshl_b32 s36, s2, 13\n"
        "s_add_u32 s36, s36, s48\n"
        "s_add_u32 s36, s36, 4194304\n"
        "s_add_u32 s48, s48, 147456\n"
        "s_lshl_b32 s49, s41, 11\n"
        "v_lshl_add_u32 v148, v1, 7, s49\n"
        "buffer_load_dwordx4 v139, s[56:59], s64 offen offset:1024 lds\n"
        "v_lshl_or_b32 v148, v2, 28, v148\n"
        "s_mul_i32 s65, s42, 9216\n"
        "v_lshl_or_b32 v13, v21, 1, s66\n"
        "v_xor_b32 v13, v13, v18\n"
        "v_mul_u32_u24 v14, 1152, v15\n"
        "v_lshl_add_u32 v128, v13, 4, v14\n"
        "v_lshl_or_b32 v13, v22, 1, s66\n"
        "v_xor_b32 v13, v13, v19\n"
        "v_mul_u32_u24 v14, 1152, v16\n"
        "v_lshl_add_u32 v129, v13, 4, v14\n"
        "v_subrev_u32 v129, 1024, v129\n"
        "v_lshl_or_b32 v13, v23, 1, s66\n"
        "buffer_load_dwordx4 v144, s[56:59], s64 offen offset:2048 lds\n"
        "v_xor_b32 v13, v13, v20\n"
        "v_mul_u32_u24 v14, 1152, v17\n"
        "v_lshl_add_u32 v130, v13, 4, v14\n"
        "v_subrev_u32 v130, 2048, v130\n"
        "v_bfe_u32 v6, v0, 2, 2\n"
        "v_and_b32 v7, 3, v0\n"
        "v_lshlrev_b32 v150, 4, v1\n"
        "v_lshl_add_u32 v8, v6, 3, v7\n"
        "v_and_b32 v9, 0xf0, v150\n"
        "v_lshlrev_b32 v146, 2, v0\n"
        "v_add_u32 v146, 139264, v146\n"
        "v_lshlrev_b32 v10, 5, v2\n"
        "buffer_load_dwordx4 v144, s[56:59], s64 offen offset:3072 lds\n"
        "v_xor_b32 v10, v10, v9\n"
        "v_lshl_add_u32 v10, v8, 8, v10\n"
        "v_add_u32 v140, 122880, v10\n"
        "v_xor_b32 v141, 16, v140\n"
        "v_xor_b32 v142, 128, v140\n"
        "v_xor_b32 v143, 144, v140\n"
        "s_lshl_b32 s67, s42, 10\n"
        "s_add_u32 s67, s67, 139264\n"
        "v_bfe_u32 v10, v0, 1, 3\n"
        "v_xor_b32 v10, v10, v2\n"
        "v_lshlrev_b32 v10, 4, v10\n"
        "v_mul_u32_u24 v11, 384, v8\n"
        "s_add_u32 m0, s40, 73728\n"
        "s_sub_u32 s64, s47, 12582912\n"
        "buffer_load_dwordx4 v242, s[8:11], s64 offen nt lds\n"
        "v_add_u32 v11, 73728, v11\n"
        "v_add_u32 v240, v10, v11\n"
        "v_xor_b32 v241, 64, v240\n"
        "v_lshl_add_u32 v10, v8, 7, v10\n"
        "v_add_u32 v251, 147456, v10\n"
        "v_xor_b32 v252, 64, v251\n"
        "v_lshl_add_u32 v10, v2, 3, v6\n"
        "v_mul_u32_u24 v10, 384, v10\n"
        "v_and_b32 v11, 1, v0\n"
        "v_lshl_add_u32 v10, v11, 3, v10\n"
        "v_and_b32 v12, 2, v6\n"
        "v_lshl_or_b32 v12, v4, 2, v12\n"
        "s_add_u32 m0, s40, 74752\n"
        "s_sub_u32 s64, s47, 12582912\n"
        "buffer_load_dwordx4 v243, s[8:11], s64 offen nt lds\n"
        "s_mul_i32 s35, s42, 6\n"
        "v_bfe_u32 v13, v0, 1, 1\n"
        "v_add_u32 v13, s35, v13\n"
        "v_xor_b32 v14, v13, v12\n"
        "v_lshl_add_u32 v131, v14, 4, v10\n"
        "v_add_u32 v136, 98304, v131\n"
        "v_add_u32 v13, 2, v13\n"
        "v_xor_b32 v14, v13, v12\n"
        "v_lshl_add_u32 v132, v14, 4, v10\n"
        "v_add_u32 v137, 98304, v132\n"
        "v_add_u32 v13, 2, v13\n"
        "v_xor_b32 v14, v13, v12\n"
        "s_add_u32 m0, s40, 75776\n"
        "s_sub_u32 s64, s47, 12582912\n"
        "buffer_load_dwordx4 v244, s[8:11], s64 offen nt lds\n"
        "v_lshl_add_u32 v133, v14, 4, v10\n"
        "v_add_u32 v138, 98304, v133\n"
        "v_lshlrev_b32 v10, 4, v11\n"
        "v_mov_b32 v12, 0x3fb9\n"
        "v_cmp_eq_u32 vcc, v6, v2\n"
        "v_lshlrev_b32 v10, v10, v12\n"
        "v_bfe_i32 v13, v0, 0, 1\n"
        "v_cndmask_b32 v10, 0, v10, vcc\n"
        "v_cmp_gt_u32 vcc, 2, v7\n"
        "v_and_b32 v13, 0xaaaaaaaa, v13\n"
        "v_or_b32 v255, 0x44444444, v13\n"
        "v_cndmask_b32 v170, 0, v10, vcc\n"
        "s_add_u32 m0, s40, 86016\n"
        "s_sub_u32 s64, s47, 11796480\n"
        "buffer_load_dwordx4 v242, s[8:11], s64 offen nt lds\n"
        "v_xor_b32 v171, v170, v10\n"
        "v_mov_b64 v[188:189], v[170:171]\n"
        "v_mov_b64 v[210:211], v[170:171]\n"
        "v_mov_b64 v[228:229], v[170:171]\n"
        "v_bfe_u32 v5, v0, 5, 1\n"
        "v_and_b32 v3, 15, v0\n"
        "v_lshl_add_u32 v10, v2, 10, v9\n"
        "s_add_u32 s49, s34, 73728\n"
        "v_add_u32 v245, s49, v10\n"
        "s_lshl_b32 s49, s51, 9\n"
        "s_add_u32 s49, s49, 73728\n"
        "v_add_u32 v247, s49, v10\n"
        "s_add_u32 m0, s40, 87040\n"
        "s_sub_u32 s64, s47, 11796480\n"
        "buffer_load_dwordx4 v243, s[8:11], s64 offen nt lds\n"
        "v_lshl_add_u32 v10, v5, 10, v9\n"
        "v_lshl_add_u32 v10, v4, 3, v10\n"
        "s_lshl_b32 s49, s42, 11\n"
        "s_add_u32 s49, s49, 90112\n"
        "v_add_u32 v246, s49, v10\n"
        "s_lshr_b32 s35, s42, 1\n"
        "s_mul_i32 s49, s35, 18432\n"
        "v_mul_u32_u24 v12, 1536, v2\n"
        "v_add_u32 v12, s49, v12\n"
        "v_mad_u32_u24 v13, v9, 6, v12\n"
        "v_lshl_add_u32 v248, v4, 4, v13\n"
        "v_mad_i32_i24 v254, v4, -16, v13\n"
        "s_add_u32 m0, s40, 88064\n"
        "s_sub_u32 s64, s47, 11796480\n"
        "buffer_load_dwordx4 v244, s[8:11], s64 offen nt lds\n"
        "v_mul_u32_u24 v10, 196608, v2\n"
        "v_lshl_add_u32 v10, v3, 2, v10\n"
        "s_lshl_b32 s49, s35, 6\n"
        "s_add_u32 s49, s49, 128\n"
        "v_add_u32 v250, s49, v10\n"
        "s_mul_i32 s26, s35, 192\n"
        "s_sub_u32 s26, s26, 128\n"
        "s_mul_i32 s43, s31, 384\n"
        "s_add_u32 s44, s31, 4\n"
        "s_and_b32 s44, s44, 511\n"
        "s_mul_i32 s44, s44, 384\n"
        "s_add_u32 s37, s45, s66\n"
        "s_mov_b32 m0, s48\n"
        "s_sub_u32 s64, s36, 4194304\n"
        "buffer_load_dwordx4 v249, s[12:15], s64 offen nt lds\n"
        "s_add_u32 s37, s37, 4\n"
        "s_mul_i32 s37, s37, 196608\n"
        "v_mad_u32_u24 v10, v6, 6, v7\n"
        "v_bfe_u32 v13, v0, 1, 3\n"
        "v_mad_u32_u24 v13, v13, 6, v11\n"
        "v_add_u32 v13, 4, v13\n"
        "v_xor_b32 v10, v10, v4\n"
        "v_xor_b32 v13, v13, v4\n"
        "v_lshl_add_u32 v10, v10, 4, v12\n"
        "v_lshl_add_u32 v13, v13, 4, v12\n"
        "s_mul_i32 s35, s42, 3\n"
        "s_and_b32 s49, s35, 3\n"
        "buffer_load_dwordx4 v149, s[12:15], s64 offen offset:1024 nt lds\n"
        "s_mul_i32 s49, s49, 384\n"
        "s_and_b32 s46, s35, 1\n"
        "s_mul_i32 s46, s46, 768\n"
        "s_bitcmp1_b32 s35, 2\n"
        "s_cselect_b32 s49, s46, s49\n"
        "s_cselect_b64 vcc, -1, 0\n"
        "v_cndmask_b32 v237, v10, v13, vcc\n"
        "v_add_u32 v237, s49, v237\n"
        "s_add_u32 s35, s35, 1\n"
        "s_and_b32 s49, s35, 3\n"
        "s_mul_i32 s49, s49, 384\n"
        "s_and_b32 s46, s35, 1\n"
        "buffer_load_dword v24, v148, s[20:23], 0 offen\n"
        "buffer_load_dword v25, v148, s[52:55], 0 offen\n"
        "s_mul_i32 s46, s46, 768\n"
        "s_bitcmp1_b32 s35, 2\n"
        "s_cselect_b32 s49, s46, s49\n"
        "s_cselect_b64 vcc, -1, 0\n"
        "v_cndmask_b32 v238, v10, v13, vcc\n"
        "v_add_u32 v238, s49, v238\n"
        "s_add_u32 s35, s35, 1\n"
        "s_and_b32 s49, s35, 3\n"
        "s_mul_i32 s49, s49, 384\n"
        "s_and_b32 s46, s35, 1\n"
        "s_mul_i32 s46, s46, 768\n"
        "s_bitcmp1_b32 s35, 2\n"
        "s_cselect_b32 s49, s46, s49\n"
        "s_cselect_b64 vcc, -1, 0\n"
        "v_cndmask_b32 v239, v10, v13, vcc\n"
        "v_add_u32 v239, s49, v239\n"
        "s_add_u32 s35, s35, 1\n"
        "v_mul_u32_u24 v10, 1536, v2\n"
        "v_lshlrev_b32 v12, 6, v4\n"
        "v_mul_u32_u24 v13, 12288, v5\n"
        "v_sub_u32 v124, v131, v10\n"
        "v_xad_u32 v127, v131, v12, v13\n"
        "v_bfe_u32 v14, v15, 1, 2\n"
        "v_lshlrev_b32 v14, 5, v14\n"
        "v_xor_b32 v234, v14, v150\n"
        "v_sub_u32 v125, v132, v10\n"
        "v_xad_u32 v232, v132, v12, v13\n"
        "v_bfe_u32 v14, v16, 1, 2\n"
        "v_lshlrev_b32 v14, 5, v14\n"
        "v_xor_b32 v235, v14, v150\n"
        "v_sub_u32 v126, v133, v10\n"
        "v_xad_u32 v233, v133, v12, v13\n"
        "v_bfe_u32 v14, v17, 1, 2\n"
        "v_lshlrev_b32 v14, 5, v14\n"
        "v_xor_b32 v236, v14, v150\n"
        "s_mov_b32 s24, 0x3fb8aa3b\n"
        "s_mov_b32 s25, 0x4f800000\n"
        "s_mov_b32 s50, 0x3e553b95\n"
        "s_mov_b32 s27, 14\n"
        "s_mov_b32 s28, 98304\n"
        "s_mov_b32 s30, 73728\n"
        "s_mov_b32 s29, 122880\n"
        "v_mov_b32 v253, 0x3f803f80\n"
        "s_nop 0\n"
        "v_accvgpr_write_b32 a252, v253\n"
        "v_accvgpr_write_b32 a253, v253\n"
        "v_accvgpr_write_b32 a254, v253\n"
        "v_accvgpr_write_b32 a255, v253\n"
        "s_waitcnt vmcnt(14)\n"
        "v_lshlrev_b32 v32, 16, v28\n"
        "v_and_b32 v33, 0xffff0000, v28\n"
        "v_lshlrev_b32 v34, 16, v29\n"
        "v_and_b32 v35, 0xffff0000, v29\n"
        "v_mul_f32 v32, s24, v32\n"
        "v_mul_f32 v33, s24, v33\n"
        "v_mul_f32 v34, s24, v34\n"
        "v_mul_f32 v35, s24, v35\n"
        "ds_write_b32 v146, v32 offset:0\n"
        "ds_write_b32 v146, v33 offset:1024\n"
        "ds_write_b32 v146, v34 offset:2048\n"
        "ds_write_b32 v146, v35 offset:3072\n"
        "s_waitcnt lgkmcnt(0)\n"
        "s_waitcnt vmcnt(10)\n"
        "s_barrier\n"
        "ds_read_b128 v[0:3], v140 offset:0\n"
        "ds_read_b128 v[4:7], v141 offset:0\n"
        "ds_read_b128 v[8:11], v142 offset:0\n"
        "ds_read_b128 v[12:15], v143 offset:0\n"
        "ds_read_b128 v[16:19], v140 offset:1024\n"
        "ds_read_b128 v[20:23], v141 offset:1024\n"
        "ds_read_b128 v[28:31], v143 offset:1024\n"
        "ds_read_b128 v[32:35], v140 offset:8192\n"
        "ds_read_b128 v[36:39], v141 offset:8192\n"
        "ds_read_b128 v[40:43], v142 offset:8192\n"
        "ds_read_b128 v[44:47], v143 offset:8192\n"
        "ds_read_b128 v[48:51], v140 offset:9216\n"
        "ds_read_b128 v[52:55], v141 offset:9216\n"
        "ds_read_b128 v[56:59], v142 offset:9216\n"
        "ds_read_b128 v[60:63], v143 offset:9216\n"
        "s_add_u32 m0, s40, 0\n"
        "s_add_u32 s64, s65, 0\n"
        "buffer_load_dwordx4 v128, s[20:23], s64 offen lds\n"
        "s_waitcnt lgkmcnt(14)\n"
        "v_lshl_add_u32 v0, v0, 2, s67\n"
        "v_lshl_add_u32 v1, v1, 2, s67\n"
        "v_lshl_add_u32 v2, v2, 2, s67\n"
        "v_lshl_add_u32 v3, v3, 2, s67\n"
        "s_waitcnt lgkmcnt(13)\n"
        "v_lshl_add_u32 v4, v4, 2, s67\n"
        "v_lshl_add_u32 v5, v5, 2, s67\n"
        "v_lshl_add_u32 v6, v6, 2, s67\n"
        "v_lshl_add_u32 v7, v7, 2, s67\n"
        "ds_read_b32 v0, v0\n"
        "ds_read_b32 v1, v1\n"
        "ds_read_b32 v2, v2\n"
        "ds_read_b32 v3, v3\n"
        "ds_read_b32 v4, v4\n"
        "ds_read_b32 v5, v5\n"
        "ds_read_b32 v6, v6\n"
        "ds_read_b32 v7, v7\n"
        "buffer_load_dwordx4 v129, s[20:23], s64 offen offset:1024 lds\n"
        "s_waitcnt lgkmcnt(15)\n"
        "v_lshl_add_u32 v8, v8, 2, s67\n"
        "v_lshl_add_u32 v9, v9, 2, s67\n"
        "v_lshl_add_u32 v10, v10, 2, s67\n"
        "v_lshl_add_u32 v11, v11, 2, s67\n"
        "v_lshl_add_u32 v12, v12, 2, s67\n"
        "v_lshl_add_u32 v13, v13, 2, s67\n"
        "v_lshl_add_u32 v14, v14, 2, s67\n"
        "v_lshl_add_u32 v15, v15, 2, s67\n"
        "ds_read_b32 v8, v8\n"
        "ds_read_b32 v9, v9\n"
        "ds_read_b32 v10, v10\n"
        "ds_read_b32 v11, v11\n"
        "ds_read_b32 v12, v12\n"
        "ds_read_b32 v13, v13\n"
        "ds_read_b32 v14, v14\n"
        "ds_read_b32 v15, v15\n"
        "buffer_load_dwordx4 v130, s[20:23], s64 offen offset:2048 lds\n"
        "v_lshl_add_u32 v16, v16, 2, s67\n"
        "v_lshl_add_u32 v17, v17, 2, s67\n"
        "v_lshl_add_u32 v18, v18, 2, s67\n"
        "v_lshl_add_u32 v19, v19, 2, s67\n"
        "v_lshl_add_u32 v20, v20, 2, s67\n"
        "v_lshl_add_u32 v21, v21, 2, s67\n"
        "v_lshl_add_u32 v22, v22, 2, s67\n"
        "v_lshl_add_u32 v23, v23, 2, s67\n"
        "ds_read_b32 v16, v16\n"
        "ds_read_b32 v17, v17\n"
        "ds_read_b32 v18, v18\n"
        "ds_read_b32 v19, v19\n"
        "ds_read_b32 v20, v20\n"
        "ds_read_b32 v21, v21\n"
        "ds_read_b32 v22, v22\n"
        "ds_read_b32 v23, v23\n"
        "s_add_u32 m0, s40, 12288\n"
        "s_add_u32 s64, s65, 36864\n"
        "buffer_load_dwordx4 v128, s[20:23], s64 offen lds\n"
        "v_lshl_add_u32 v28, v28, 2, s67\n"
        "v_lshl_add_u32 v29, v29, 2, s67\n"
        "v_lshl_add_u32 v30, v30, 2, s67\n"
        "v_lshl_add_u32 v31, v31, 2, s67\n"
        "v_lshl_add_u32 v32, v32, 2, s67\n"
        "v_lshl_add_u32 v33, v33, 2, s67\n"
        "v_lshl_add_u32 v34, v34, 2, s67\n"
        "v_lshl_add_u32 v35, v35, 2, s67\n"
        "ds_read_b32 v28, v28\n"
        "ds_read_b32 v29, v29\n"
        "ds_read_b32 v30, v30\n"
        "ds_read_b32 v31, v31\n"
        "ds_read_b32 v32, v32\n"
        "ds_read_b32 v33, v33\n"
        "ds_read_b32 v34, v34\n"
        "ds_read_b32 v35, v35\n"
        "buffer_load_dwordx4 v129, s[20:23], s64 offen offset:1024 lds\n"
        "s_waitcnt lgkmcnt(15)\n"
        "v_lshl_add_u32 v36, v36, 2, s67\n"
        "v_lshl_add_u32 v37, v37, 2, s67\n"
        "v_lshl_add_u32 v38, v38, 2, s67\n"
        "v_lshl_add_u32 v39, v39, 2, s67\n"
        "v_lshl_add_u32 v40, v40, 2, s67\n"
        "v_lshl_add_u32 v41, v41, 2, s67\n"
        "v_lshl_add_u32 v42, v42, 2, s67\n"
        "v_lshl_add_u32 v43, v43, 2, s67\n"
        "ds_read_b32 v36, v36\n"
        "ds_read_b32 v37, v37\n"
        "ds_read_b32 v38, v38\n"
        "ds_read_b32 v39, v39\n"
        "ds_read_b32 v40, v40\n"
        "ds_read_b32 v41, v41\n"
        "ds_read_b32 v42, v42\n"
        "ds_read_b32 v43, v43\n"
        "buffer_load_dwordx4 v130, s[20:23], s64 offen offset:2048 lds\n"
        "v_lshl_add_u32 v44, v44, 2, s67\n"
        "v_lshl_add_u32 v45, v45, 2, s67\n"
        "v_lshl_add_u32 v46, v46, 2, s67\n"
        "v_lshl_add_u32 v47, v47, 2, s67\n"
        "v_lshl_add_u32 v48, v48, 2, s67\n"
        "v_lshl_add_u32 v49, v49, 2, s67\n"
        "v_lshl_add_u32 v50, v50, 2, s67\n"
        "v_lshl_add_u32 v51, v51, 2, s67\n"
        "ds_read_b32 v44, v44\n"
        "ds_read_b32 v45, v45\n"
        "ds_read_b32 v46, v46\n"
        "ds_read_b32 v47, v47\n"
        "ds_read_b32 v48, v48\n"
        "ds_read_b32 v49, v49\n"
        "ds_read_b32 v50, v50\n"
        "ds_read_b32 v51, v51\n"
        "v_lshl_add_u32 v52, v52, 2, s67\n"
        "v_lshl_add_u32 v53, v53, 2, s67\n"
        "v_lshl_add_u32 v54, v54, 2, s67\n"
        "v_lshl_add_u32 v55, v55, 2, s67\n"
        "v_lshl_add_u32 v56, v56, 2, s67\n"
        "v_lshl_add_u32 v57, v57, 2, s67\n"
        "v_lshl_add_u32 v58, v58, 2, s67\n"
        "v_lshl_add_u32 v59, v59, 2, s67\n"
        "ds_read_b32 v52, v52\n"
        "ds_read_b32 v53, v53\n"
        "ds_read_b32 v54, v54\n"
        "ds_read_b32 v55, v55\n"
        "ds_read_b32 v56, v56\n"
        "ds_read_b32 v57, v57\n"
        "ds_read_b32 v58, v58\n"
        "ds_read_b32 v59, v59\n"
        "v_lshl_add_u32 v60, v60, 2, s67\n"
        "v_lshl_add_u32 v61, v61, 2, s67\n"
        "v_lshl_add_u32 v62, v62, 2, s67\n"
        "v_lshl_add_u32 v63, v63, 2, s67\n"
        "ds_read_b32 v60, v60\n"
        "ds_read_b32 v61, v61\n"
        "ds_read_b32 v62, v62\n"
        "ds_read_b32 v63, v63\n"
        "s_add_u32 m0, s40, 24576\n"
        "s_add_u32 s64, s65, 73728\n"
        "buffer_load_dwordx4 v128, s[20:23], s64 offen lds\n"
        "buffer_load_dwordx4 v129, s[20:23], s64 offen offset:1024 lds\n"
        "buffer_load_dwordx4 v130, s[20:23], s64 offen offset:2048 lds\n"
        "s_add_u32 m0, s40, 36864\n"
        "s_add_u32 s64, s65, 110592\n"
        "buffer_load_dwordx4 v128, s[20:23], s64 offen lds\n"
        "buffer_load_dwordx4 v129, s[20:23], s64 offen offset:1024 lds\n"
        "buffer_load_dwordx4 v130, s[20:23], s64 offen offset:2048 lds\n"
        "s_add_u32 m0, s40, 49152\n"
        "s_add_u32 s64, s65, 147456\n"
        "buffer_load_dwordx4 v128, s[20:23], s64 offen lds\n"
        "buffer_load_dwordx4 v129, s[20:23], s64 offen offset:1024 lds\n"
        "buffer_load_dwordx4 v130, s[20:23], s64 offen offset:2048 lds\n"
        "s_add_u32 m0, s40, 61440\n"
        "s_add_u32 s64, s65, 184320\n"
        "buffer_load_dwordx4 v128, s[20:23], s64 offen lds\n"
        "buffer_load_dwordx4 v129, s[20:23], s64 offen offset:1024 lds\n"
        "buffer_load_dwordx4 v130, s[20:23], s64 offen offset:2048 lds\n"
        "s_waitcnt vmcnt(12)\n"
        "s_barrier\n"
        "ds_read_b64_tr_b16 a[0:1], v131 offset:0\n"
        "ds_read_b128 a[144:147], v240 offset:0\n"
        "ds_read_b64_tr_b16 a[2:3], v131 offset:1536\n"
        "ds_read_b64_tr_b16 a[4:5], v131 offset:12288\n"
        "ds_read_b128 a[148:151], v241 offset:0\n"
        "ds_read_b64_tr_b16 a[6:7], v131 offset:13824\n"
        "ds_read_b64_tr_b16 a[24:25], v132 offset:0\n"
        "ds_read_b64_tr_b16 a[26:27], v132 offset:1536\n"
        "ds_read_b64_tr_b16 a[28:29], v132 offset:12288\n"
        "ds_read_b64_tr_b16 a[30:31], v132 offset:13824\n"
        "ds_read_b64_tr_b16 a[48:49], v133 offset:0\n"
        "ds_read_b64_tr_b16 a[50:51], v133 offset:1536\n"
        "ds_read_b64_tr_b16 a[52:53], v133 offset:12288\n"
        "ds_read_b64_tr_b16 a[54:55], v133 offset:13824\n"
        "ds_read_b128 a[160:163], v240 offset:1536\n"
        "ds_read_b128 a[164:167], v241 offset:1536\n"
        "s_waitcnt lgkmcnt(13)\n"
        "v_mfma_f32_16x16x32_bf16 v[96:99], a[0:3], a[144:147], 0\n"
        "s_waitcnt lgkmcnt(10)\n"
        "s_add_u32 m0, s40, 98304\n"
        "s_add_u32 s64, s65, 384\n"
        "ds_read_b128 a[176:179], v240 offset:12288\n"
        "v_mfma_f32_16x16x32_bf16 v[96:99], a[4:7], a[148:151], v[96:99]\n"
        "s_waitcnt lgkmcnt(7)\n"
        "buffer_load_dwordx4 v128, s[20:23], s64 offen lds\n"
        "v_mfma_f32_16x16x32_bf16 v[100:103], a[28:31], a[148:151], 0\n"
        "ds_read_b128 a[180:183], v241 offset:12288\n"
        "v_mfma_f32_16x16x32_bf16 v[100:103], a[24:27], a[144:147], v[100:103]\n"
        "s_waitcnt lgkmcnt(6)\n"
        "ds_read_b128 a[192:195], v240 offset:13824\n"
        "v_mfma_f32_16x16x32_bf16 v[104:107], a[48:51], a[144:147], 0\n"
        "s_waitcnt lgkmcnt(5)\n"
        "ds_read_b128 a[196:199], v241 offset:13824\n"
        "v_mfma_f32_16x16x32_bf16 v[104:107], a[52:55], a[148:151], v[104:107]\n"
        "s_waitcnt lgkmcnt(4)\n"
        "buffer_load_dwordx4 v129, s[20:23], s64 offen offset:1024 lds\n"
        "v_mfma_f32_16x16x32_bf16 v[108:111], a[4:7], a[164:167], 0\n"
        "ds_read_b128 a[152:155], v240 offset:128\n"
        "v_mfma_f32_16x16x32_bf16 v[108:111], a[0:3], a[160:163], v[108:111]\n"
        "ds_read_b128 a[156:159], v241 offset:128\n"
        "v_mfma_f32_16x16x32_bf16 v[112:115], a[24:27], a[160:163], 0\n"
        "s_waitcnt vmcnt(8)\n"
        "s_barrier\n"
        "ds_read_b128 a[168:171], v240 offset:1664\n"
        "v_mfma_f32_16x16x32_bf16 v[112:115], a[28:31], a[164:167], v[112:115]\n"
        "buffer_load_dwordx4 v130, s[20:23], s64 offen offset:2048 lds\n"
        "v_mfma_f32_16x16x32_bf16 v[116:119], a[52:55], a[164:167], 0\n"
        "s_add_u32 m0, s40, 110592\n"
        "ds_read_b64_tr_b16 a[8:9], v131 offset:24576\n"
        "ds_read_b64_tr_b16 a[10:11], v131 offset:26112\n"
        "v_mfma_f32_16x16x32_bf16 v[116:119], a[48:51], a[160:163], v[116:119]\n"
        "s_waitcnt lgkmcnt(8)\n"
        "s_add_u32 s64, s65, 37248\n"
        "ds_read_b64_tr_b16 a[12:13], v131 offset:36864\n"
        "v_mfma_f32_16x16x32_bf16 v[120:123], a[0:3], a[176:179], 0\n"
        "s_waitcnt lgkmcnt(8)\n"
        "ds_read_b64_tr_b16 a[14:15], v131 offset:38400\n"
        "v_mfma_f32_16x16x32_bf16 v[120:123], a[4:7], a[180:183], v[120:123]\n"
        "buffer_load_dwordx4 v128, s[20:23], s64 offen lds\n"
        "v_mfma_f32_16x16x32_bf16 v[92:95], a[28:31], a[180:183], 0\n"
        "ds_read_b64_tr_b16 a[32:33], v132 offset:24576\n"
        "ds_read_b64_tr_b16 a[34:35], v132 offset:26112\n"
        "v_mfma_f32_16x16x32_bf16 v[92:95], a[24:27], a[176:179], v[92:95]\n"
        "ds_read_b64_tr_b16 a[36:37], v132 offset:36864\n"
        "v_mfma_f32_16x16x32_bf16 v[152:155], a[48:51], a[176:179], 0\n"
        "ds_read_b64_tr_b16 a[38:39], v132 offset:38400\n"
        "v_mfma_f32_16x16x32_bf16 v[152:155], a[52:55], a[180:183], v[152:155]\n"
        "s_waitcnt lgkmcnt(11)\n"
        "buffer_load_dwordx4 v129, s[20:23], s64 offen offset:1024 lds\n"
        "v_mfma_f32_16x16x32_bf16 v[172:175], a[4:7], a[196:199], 0\n"
        "ds_read_b64_tr_b16 a[56:57], v133 offset:24576\n"
        "ds_read_b64_tr_b16 a[58:59], v133 offset:26112\n"
        "v_mfma_f32_16x16x32_bf16 v[172:175], a[0:3], a[192:195], v[172:175]\n"
        "ds_read_b64_tr_b16 a[60:61], v133 offset:36864\n"
        "v_mfma_f32_16x16x32_bf16 v[192:195], a[24:27], a[192:195], 0\n"
        "ds_read_b64_tr_b16 a[62:63], v133 offset:38400\n"
        "v_mfma_f32_16x16x32_bf16 v[192:195], a[28:31], a[196:199], v[192:195]\n"
        "buffer_load_dwordx4 v130, s[20:23], s64 offen offset:2048 lds\n"
        "v_mfma_f32_16x16x32_bf16 v[212:215], a[52:55], a[196:199], 0\n"
        "s_add_u32 m0, s40, 0\n"
        "ds_read_b128 a[172:175], v241 offset:1664\n"
        "v_mfma_f32_16x16x32_bf16 v[212:215], a[48:51], a[192:195], v[212:215]\n"
        "s_waitcnt lgkmcnt(11)\n"
        "s_add_u32 s64, s65, 74112\n"
        "ds_read_b128 a[184:187], v240 offset:12416\n"
        "v_mfma_f32_16x16x32_bf16 v[96:99], a[8:11], a[152:155], v[96:99]\n"
        "s_waitcnt lgkmcnt(10)\n"
        "ds_read_b128 a[188:191], v241 offset:12416\n"
        "v_mfma_f32_16x16x32_bf16 v[96:99], a[12:15], a[156:159], v[96:99]\n"
        "s_waitcnt lgkmcnt(7)\n"
        "buffer_load_dwordx4 v128, s[20:23], s64 offen lds\n"
        "v_mfma_f32_16x16x32_bf16 v[100:103], a[36:39], a[156:159], v[100:103]\n"
        "ds_read_b128 a[200:203], v240 offset:13952\n"
        "v_mfma_f32_16x16x32_bf16 v[100:103], a[32:35], a[152:155], v[100:103]\n"
        "s_waitcnt lgkmcnt(6)\n"
        "ds_read_b128 a[204:207], v241 offset:13952\n"
        "v_mfma_f32_16x16x32_bf16 v[104:107], a[56:59], a[152:155], v[104:107]\n"
        "s_waitcnt lgkmcnt(5)\n"
        "ds_read_b128 a[216:219], v240 offset:256\n"
        "v_mfma_f32_16x16x32_bf16 v[104:107], a[60:63], a[156:159], v[104:107]\n"
        "s_waitcnt lgkmcnt(5)\n"
        "buffer_load_dwordx4 v129, s[20:23], s64 offen offset:1024 lds\n"
        "v_mfma_f32_16x16x32_bf16 v[108:111], a[12:15], a[172:175], v[108:111]\n"
        "ds_read_b128 a[220:223], v241 offset:256\n"
        "v_mfma_f32_16x16x32_bf16 v[108:111], a[8:11], a[168:171], v[108:111]\n"
        "ds_read_b128 a[224:227], v240 offset:1792\n"
        "v_mfma_f32_16x16x32_bf16 v[112:115], a[32:35], a[168:171], v[112:115]\n"
        "s_waitcnt vmcnt(8)\n"
        "s_barrier\n"
        "ds_read_b128 a[228:231], v241 offset:1792\n"
        "v_mfma_f32_16x16x32_bf16 v[112:115], a[36:39], a[172:175], v[112:115]\n"
        "buffer_load_dwordx4 v130, s[20:23], s64 offen offset:2048 lds\n"
        "v_mfma_f32_16x16x32_bf16 v[116:119], a[60:63], a[172:175], v[116:119]\n"
        "s_add_u32 m0, s40, 12288\n"
        "ds_read_b64_tr_b16 a[16:17], v131 offset:49152\n"
        "ds_read_b64_tr_b16 a[18:19], v131 offset:50688\n"
        "v_mfma_f32_16x16x32_bf16 v[116:119], a[56:59], a[168:171], v[116:119]\n"
        "s_waitcnt lgkmcnt(9)\n"
        "s_add_u32 s64, s65, 110976\n"
        "ds_read_b64_tr_b16 a[20:21], v131 offset:61440\n"
        "v_mfma_f32_16x16x32_bf16 v[120:123], a[8:11], a[184:187], v[120:123]\n"
        "s_waitcnt lgkmcnt(9)\n"
        "ds_read_b64_tr_b16 a[22:23], v131 offset:62976\n"
        "v_mfma_f32_16x16x32_bf16 v[120:123], a[12:15], a[188:191], v[120:123]\n"
        "buffer_load_dwordx4 v128, s[20:23], s64 offen lds\n"
        "v_mfma_f32_16x16x32_bf16 v[92:95], a[36:39], a[188:191], v[92:95]\n"
        "ds_read_b64_tr_b16 a[40:41], v132 offset:49152\n"
        "ds_read_b64_tr_b16 a[42:43], v132 offset:50688\n"
        "v_mfma_f32_16x16x32_bf16 v[92:95], a[32:35], a[184:187], v[92:95]\n"
        "ds_read_b64_tr_b16 a[44:45], v132 offset:61440\n"
        "v_mfma_f32_16x16x32_bf16 v[152:155], a[56:59], a[184:187], v[152:155]\n"
        "ds_read_b64_tr_b16 a[46:47], v132 offset:62976\n"
        "v_mfma_f32_16x16x32_bf16 v[152:155], a[60:63], a[188:191], v[152:155]\n"
        "s_waitcnt lgkmcnt(12)\n"
        "buffer_load_dwordx4 v129, s[20:23], s64 offen offset:1024 lds\n"
        "v_mfma_f32_16x16x32_bf16 v[172:175], a[12:15], a[204:207], v[172:175]\n"
        "ds_read_b64_tr_b16 a[64:65], v133 offset:49152\n"
        "ds_read_b64_tr_b16 a[66:67], v133 offset:50688\n"
        "v_mfma_f32_16x16x32_bf16 v[172:175], a[8:11], a[200:203], v[172:175]\n"
        "ds_read_b64_tr_b16 a[68:69], v133 offset:61440\n"
        "v_mfma_f32_16x16x32_bf16 v[192:195], a[32:35], a[200:203], v[192:195]\n"
        "ds_read_b64_tr_b16 a[70:71], v133 offset:62976\n"
        "v_mfma_f32_16x16x32_bf16 v[192:195], a[36:39], a[204:207], v[192:195]\n"
        "buffer_load_dwordx4 v130, s[20:23], s64 offen offset:2048 lds\n"
        "v_mfma_f32_16x16x32_bf16 v[212:215], a[60:63], a[204:207], v[212:215]\n"
        "s_add_u32 m0, s40, 24576\n"
        "ds_read_b128 a[232:235], v240 offset:12544\n"
        "v_mfma_f32_16x16x32_bf16 v[212:215], a[56:59], a[200:203], v[212:215]\n"
        "s_waitcnt lgkmcnt(11)\n"
        "s_add_u32 s64, s65, 147840\n"
        "ds_read_b128 a[236:239], v241 offset:12544\n"
        "v_mfma_f32_16x16x32_bf16 v[96:99], a[16:19], a[216:219], v[96:99]\n"
        "s_waitcnt lgkmcnt(10)\n"
        "ds_read_b128 a[240:243], v240 offset:14080\n"
        "v_mfma_f32_16x16x32_bf16 v[96:99], a[20:23], a[220:223], v[96:99]\n"
        "s_waitcnt lgkmcnt(7)\n"
        "buffer_load_dwordx4 v128, s[20:23], s64 offen lds\n"
        "v_mfma_f32_16x16x32_bf16 v[100:103], a[44:47], a[220:223], v[100:103]\n"
        "ds_read_b128 a[244:247], v241 offset:14080\n"
        "v_mfma_f32_16x16x32_bf16 v[100:103], a[40:43], a[216:219], v[100:103]\n"
        "s_waitcnt lgkmcnt(6)\n"
        "v_mfma_f32_16x16x32_bf16 v[104:107], a[64:67], a[216:219], v[104:107]\n"
        "s_waitcnt lgkmcnt(4)\n"
        "v_mfma_f32_16x16x32_bf16 v[104:107], a[68:71], a[220:223], v[104:107]\n"
        "buffer_load_dwordx4 v129, s[20:23], s64 offen offset:1024 lds\n"
        "v_mfma_f32_16x16x32_bf16 v[108:111], a[20:23], a[228:231], v[108:111]\n"
        "v_mul_f32 v96, s50, v96\n"
        "v_mul_f32 v97, s50, v97\n"
        "v_mfma_f32_16x16x32_bf16 v[108:111], a[16:19], a[224:227], v[108:111]\n"
        "v_mul_f32 v98, s50, v98\n"
        "v_mul_f32 v99, s50, v99\n"
        "v_mfma_f32_16x16x32_bf16 v[112:115], a[40:43], a[224:227], v[112:115]\n"
        "s_waitcnt vmcnt(8)\n"
        "s_barrier\n"
        "v_cvt_pk_bf16_f32 v156, v96, v97\n"
        "v_cvt_pk_bf16_f32 v157, v98, v99\n"
        "v_mfma_f32_16x16x32_bf16 v[112:115], a[44:47], a[228:231], v[112:115]\n"
        "buffer_load_dwordx4 v130, s[20:23], s64 offen offset:2048 lds\n"
        "v_mfma_f32_16x16x32_bf16 v[116:119], a[68:71], a[228:231], v[116:119]\n"
        "s_add_u32 m0, s40, 36864\n"
        "ds_read_b64_tr_b16 a[72:73], v136 offset:0\n"
        "ds_read_b64_tr_b16 a[74:75], v136 offset:1536\n"
        "v_mfma_f32_16x16x32_bf16 v[116:119], a[64:67], a[224:227], v[116:119]\n"
        "s_waitcnt lgkmcnt(5)\n"
        "s_add_u32 s64, s65, 184704\n"
        "ds_read_b64_tr_b16 a[76:77], v136 offset:12288\n"
        "v_mul_f32 v100, s50, v100\n"
        "v_mfma_f32_16x16x32_bf16 v[120:123], a[16:19], a[232:235], v[120:123]\n"
        "s_waitcnt lgkmcnt(5)\n"
        "ds_read_b64_tr_b16 a[78:79], v136 offset:13824\n"
        "v_mul_f32 v101, s50, v101\n"
        "v_mfma_f32_16x16x32_bf16 v[120:123], a[20:23], a[236:239], v[120:123]\n"
        "buffer_load_dwordx4 v128, s[20:23], s64 offen lds\n"
        "v_mfma_f32_16x16x32_bf16 v[92:95], a[44:47], a[236:239], v[92:95]\n"
        "ds_read_b64_tr_b16 a[96:97], v137 offset:0\n"
        "ds_read_b64_tr_b16 a[98:99], v137 offset:1536\n"
        "v_mfma_f32_16x16x32_bf16 v[92:95], a[40:43], a[232:235], v[92:95]\n"
        "ds_read_b64_tr_b16 a[100:101], v137 offset:12288\n"
        "v_mul_f32 v102, s50, v102\n"
        "v_mfma_f32_16x16x32_bf16 v[152:155], a[64:67], a[232:235], v[152:155]\n"
        "ds_read_b64_tr_b16 a[102:103], v137 offset:13824\n"
        "v_mul_f32 v103, s50, v103\n"
        "v_mfma_f32_16x16x32_bf16 v[152:155], a[68:71], a[236:239], v[152:155]\n"
        "s_waitcnt lgkmcnt(8)\n"
        "buffer_load_dwordx4 v129, s[20:23], s64 offen offset:1024 lds\n"
        "v_mfma_f32_16x16x32_bf16 v[172:175], a[20:23], a[244:247], v[172:175]\n"
        "ds_read_b64_tr_b16 a[120:121], v138 offset:0\n"
        "ds_read_b64_tr_b16 a[122:123], v138 offset:1536\n"
        "v_mfma_f32_16x16x32_bf16 v[172:175], a[16:19], a[240:243], v[172:175]\n"
        "ds_read_b64_tr_b16 a[124:125], v138 offset:12288\n"
        "v_cvt_pk_bf16_f32 v158, v100, v101\n"
        "v_mfma_f32_16x16x32_bf16 v[192:195], a[40:43], a[240:243], v[192:195]\n"
        "ds_read_b64_tr_b16 a[126:127], v138 offset:13824\n"
        "v_cvt_pk_bf16_f32 v159, v102, v103\n"
        "v_mfma_f32_16x16x32_bf16 v[192:195], a[44:47], a[244:247], v[192:195]\n"
        "buffer_load_dwordx4 v130, s[20:23], s64 offen offset:2048 lds\n"
        "v_mfma_f32_16x16x32_bf16 v[212:215], a[68:71], a[244:247], v[212:215]\n"
        "s_add_u32 m0, s40, 49152\n"
        "ds_read_b128 v[24:27], v142 offset:1024\n"
        "v_mul_f32 v104, s50, v104\n"
        "v_mfma_f32_16x16x32_bf16 v[212:215], a[64:67], a[240:243], v[212:215]\n"
        "s_waitcnt lgkmcnt(11)\n"
        "s_add_u32 s64, s65, 768\n"
        "v_mul_f32 v105, s50, v105\n"
        "v_mul_f32 v106, s50, v106\n"
        "v_mfma_f32_16x16x32_bf16 v[64:67], a[72:75], a[144:147], 0\n"
        "s_waitcnt lgkmcnt(9)\n"
        "v_mul_f32 v107, s50, v107\n"
        "v_cvt_pk_bf16_f32 v160, v104, v105\n"
        "v_mfma_f32_16x16x32_bf16 v[64:67], a[76:79], a[148:151], v[64:67]\n"
        "s_waitcnt lgkmcnt(5)\n"
        "buffer_load_dwordx4 v128, s[20:23], s64 offen lds\n"
        "v_mfma_f32_16x16x32_bf16 v[68:71], a[100:103], a[148:151], 0\n"
        "v_cvt_pk_bf16_f32 v161, v106, v107\n"
        "v_mul_f32 v108, s50, v108\n"
        "v_mfma_f32_16x16x32_bf16 v[68:71], a[96:99], a[144:147], v[68:71]\n"
        "s_waitcnt lgkmcnt(3)\n"
        "v_mul_f32 v109, s50, v109\n"
        "v_mov_b64 v[166:167], v[160:161]\n"
        "v_mfma_f32_16x16x32_bf16 v[72:75], a[120:123], a[144:147], 0\n"
        "s_waitcnt lgkmcnt(1)\n"
        "v_mul_f32 v110, s50, v110\n"
        "v_mul_f32 v111, s50, v111\n"
        "v_mfma_f32_16x16x32_bf16 v[72:75], a[124:127], a[148:151], v[72:75]\n"
        "buffer_load_dwordx4 v129, s[20:23], s64 offen offset:1024 lds\n"
        "v_mfma_f32_16x16x32_bf16 v[76:79], a[76:79], a[164:167], 0\n"
        "v_cvt_pk_bf16_f32 v176, v108, v109\n"
        "v_cvt_pk_bf16_f32 v177, v110, v111\n"
        "v_mfma_f32_16x16x32_bf16 v[76:79], a[72:75], a[160:163], v[76:79]\n"
        "v_mul_f32 v112, s50, v112\n"
        "v_mul_f32 v113, s50, v113\n"
        "v_mfma_f32_16x16x32_bf16 v[80:83], a[96:99], a[160:163], 0\n"
        "s_waitcnt vmcnt(8)\n"
        "s_barrier\n"
        "v_mul_f32 v114, s50, v114\n"
        "v_mul_f32 v115, s50, v115\n"
        "v_mfma_f32_16x16x32_bf16 v[80:83], a[100:103], a[164:167], v[80:83]\n"
        "buffer_load_dwordx4 v130, s[20:23], s64 offen offset:2048 lds\n"
        "v_mfma_f32_16x16x32_bf16 v[84:87], a[124:127], a[164:167], 0\n"
        "s_add_u32 m0, s40, 61440\n"
        "ds_read_b64_tr_b16 a[80:81], v131 offset:0\n"
        "ds_read_b64_tr_b16 a[82:83], v131 offset:1536\n"
        "v_mfma_f32_16x16x32_bf16 v[84:87], a[120:123], a[160:163], v[84:87]\n"
        "s_add_u32 s64, s65, 37632\n"
        "ds_read_b64_tr_b16 a[84:85], v131 offset:12288\n"
        "v_cvt_pk_bf16_f32 v178, v112, v113\n"
        "v_mfma_f32_16x16x32_bf16 v[88:91], a[72:75], a[176:179], 0\n"
        "ds_read_b64_tr_b16 a[86:87], v131 offset:13824\n"
        "v_cvt_pk_bf16_f32 v179, v114, v115\n"
        "v_mfma_f32_16x16x32_bf16 v[88:91], a[76:79], a[180:183], v[88:91]\n"
        "buffer_load_dwordx4 v128, s[20:23], s64 offen lds\n"
        "v_mfma_f32_16x16x32_bf16 v[108:111], a[100:103], a[180:183], 0\n"
        "ds_read_b64_tr_b16 a[104:105], v132 offset:0\n"
        "ds_read_b64_tr_b16 a[106:107], v132 offset:1536\n"
        "v_mfma_f32_16x16x32_bf16 v[108:111], a[96:99], a[176:179], v[108:111]\n"
        "ds_read_b64_tr_b16 a[108:109], v132 offset:12288\n"
        "v_mul_f32 v116, s50, v116\n"
        "v_mfma_f32_16x16x32_bf16 v[112:115], a[120:123], a[176:179], 0\n"
        "ds_read_b64_tr_b16 a[110:111], v132 offset:13824\n"
        "v_mul_f32 v117, s50, v117\n"
        "v_mfma_f32_16x16x32_bf16 v[112:115], a[124:127], a[180:183], v[112:115]\n"
        "buffer_load_dwordx4 v129, s[20:23], s64 offen offset:1024 lds\n"
        "v_mfma_f32_16x16x32_bf16 v[96:99], a[76:79], a[196:199], 0\n"
        "ds_read_b64_tr_b16 a[128:129], v133 offset:0\n"
        "ds_read_b64_tr_b16 a[130:131], v133 offset:1536\n"
        "v_mfma_f32_16x16x32_bf16 v[96:99], a[72:75], a[192:195], v[96:99]\n"
        "ds_read_b64_tr_b16 a[132:133], v133 offset:12288\n"
        "v_mul_f32 v118, s50, v118\n"
        "v_mfma_f32_16x16x32_bf16 v[100:103], a[96:99], a[192:195], 0\n"
        "ds_read_b64_tr_b16 a[134:135], v133 offset:13824\n"
        "v_mul_f32 v119, s50, v119\n"
        "v_mfma_f32_16x16x32_bf16 v[100:103], a[100:103], a[196:199], v[100:103]\n"
        "buffer_load_dwordx4 v130, s[20:23], s64 offen offset:2048 lds\n"
        "v_mfma_f32_16x16x32_bf16 v[104:107], a[124:127], a[196:199], 0\n"
        "v_cvt_pk_bf16_f32 v180, v116, v117\n"
        "v_cvt_pk_bf16_f32 v181, v118, v119\n"
        "v_mfma_f32_16x16x32_bf16 v[104:107], a[120:123], a[192:195], v[104:107]\n"
        "s_waitcnt lgkmcnt(10)\n"
        "v_mul_f32 v120, s50, v120\n"
        "v_mul_f32 v121, s50, v121\n"
        "v_mfma_f32_16x16x32_bf16 v[64:67], a[80:83], a[152:155], v[64:67]\n"
        "s_waitcnt lgkmcnt(8)\n"
        "v_mov_b64 v[186:187], v[180:181]\n"
        "v_mul_f32 v122, s50, v122\n"
        "v_mfma_f32_16x16x32_bf16 v[64:67], a[84:87], a[156:159], v[64:67]\n"
        "s_waitcnt lgkmcnt(4)\n"
        "v_mul_f32 v123, s50, v123\n"
        "v_cvt_pk_bf16_f32 v196, v120, v121\n"
        "v_mfma_f32_16x16x32_bf16 v[68:71], a[108:111], a[156:159], v[68:71]\n"
        "v_cvt_pk_bf16_f32 v197, v122, v123\n"
        "v_mul_f32 v92, s50, v92\n"
        "v_mfma_f32_16x16x32_bf16 v[68:71], a[104:107], a[152:155], v[68:71]\n"
        "s_waitcnt lgkmcnt(2)\n"
        "v_mul_f32 v93, s50, v93\n"
        "v_mul_f32 v94, s50, v94\n"
        "v_mfma_f32_16x16x32_bf16 v[72:75], a[128:131], a[152:155], v[72:75]\n"
        "s_waitcnt lgkmcnt(0)\n"
        "v_mul_f32 v95, s50, v95\n"
        "v_cvt_pk_bf16_f32 v198, v92, v93\n"
        "v_mfma_f32_16x16x32_bf16 v[72:75], a[132:135], a[156:159], v[72:75]\n"
        "v_cvt_pk_bf16_f32 v199, v94, v95\n"
        "v_mul_f32 v152, s50, v152\n"
        "v_mfma_f32_16x16x32_bf16 v[76:79], a[84:87], a[172:175], v[76:79]\n"
        "v_mul_f32 v153, s50, v153\n"
        "v_mul_f32 v154, s50, v154\n"
        "v_mfma_f32_16x16x32_bf16 v[76:79], a[80:83], a[168:171], v[76:79]\n"
        "v_mul_f32 v155, s50, v155\n"
        "v_cvt_pk_bf16_f32 v200, v152, v153\n"
        "v_mfma_f32_16x16x32_bf16 v[80:83], a[104:107], a[168:171], v[80:83]\n"
        "s_waitcnt vmcnt(6)\n"
        "s_barrier\n"
        "s_add_u32 m0, s40, 0\n"
        "v_cvt_pk_bf16_f32 v201, v154, v155\n"
        "v_mfma_f32_16x16x32_bf16 v[80:83], a[108:111], a[172:175], v[80:83]\n"
        "s_add_u32 s64, s65, 74496\n"
        "ds_read_b64_tr_b16 a[88:89], v131 offset:24576\n"
        "v_mul_f32 v172, s50, v172\n"
        "v_mfma_f32_16x16x32_bf16 v[84:87], a[132:135], a[172:175], v[84:87]\n"
        "ds_read_b64_tr_b16 a[90:91], v131 offset:26112\n"
        "v_mul_f32 v173, s50, v173\n"
        "v_mfma_f32_16x16x32_bf16 v[84:87], a[128:131], a[168:171], v[84:87]\n"
        "buffer_load_dwordx4 v128, s[20:23], s64 offen lds\n"
        "v_mfma_f32_16x16x32_bf16 v[88:91], a[80:83], a[184:187], v[88:91]\n"
        "ds_read_b64_tr_b16 a[92:93], v131 offset:36864\n"
        "ds_read_b64_tr_b16 a[94:95], v131 offset:38400\n"
        "v_mfma_f32_16x16x32_bf16 v[88:91], a[84:87], a[188:191], v[88:91]\n"
        "ds_read_b64_tr_b16 a[112:113], v132 offset:24576\n"
        "v_mov_b64 v[206:207], v[200:201]\n"
        "v_mfma_f32_16x16x32_bf16 v[108:111], a[108:111], a[188:191], v[108:111]\n"
        "ds_read_b64_tr_b16 a[114:115], v132 offset:26112\n"
        "v_mul_f32 v174, s50, v174\n"
        "v_mfma_f32_16x16x32_bf16 v[108:111], a[104:107], a[184:187], v[108:111]\n"
        "buffer_load_dwordx4 v129, s[20:23], s64 offen offset:1024 lds\n"
        "v_mfma_f32_16x16x32_bf16 v[112:115], a[128:131], a[184:187], v[112:115]\n"
        "ds_read_b64_tr_b16 a[116:117], v132 offset:36864\n"
        "ds_read_b64_tr_b16 a[118:119], v132 offset:38400\n"
        "v_mfma_f32_16x16x32_bf16 v[112:115], a[132:135], a[188:191], v[112:115]\n"
        "ds_read_b64_tr_b16 a[136:137], v133 offset:24576\n"
        "v_mul_f32 v175, s50, v175\n"
        "v_mfma_f32_16x16x32_bf16 v[96:99], a[84:87], a[204:207], v[96:99]\n"
        "ds_read_b64_tr_b16 a[138:139], v133 offset:26112\n"
        "v_cvt_pk_bf16_f32 v216, v172, v173\n"
        "v_mfma_f32_16x16x32_bf16 v[96:99], a[80:83], a[200:203], v[96:99]\n"
        "buffer_load_dwordx4 v130, s[20:23], s64 offen offset:2048 lds\n"
        "v_mfma_f32_16x16x32_bf16 v[100:103], a[104:107], a[200:203], v[100:103]\n"
        "s_add_u32 m0, s40, 12288\n"
        "ds_read_b64_tr_b16 a[140:141], v133 offset:36864\n"
        "ds_read_b64_tr_b16 a[142:143], v133 offset:38400\n"
        "v_mfma_f32_16x16x32_bf16 v[100:103], a[108:111], a[204:207], v[100:103]\n"
        "s_add_u32 s64, s65, 111360\n"
        "v_cvt_pk_bf16_f32 v217, v174, v175\n"
        "v_mul_f32 v192, s50, v192\n"
        "v_mfma_f32_16x16x32_bf16 v[104:107], a[132:135], a[204:207], v[104:107]\n"
        "buffer_load_dwordx4 v128, s[20:23], s64 offen lds\n"
        "v_mfma_f32_16x16x32_bf16 v[104:107], a[128:131], a[200:203], v[104:107]\n"
        "s_waitcnt lgkmcnt(10)\n"
        "v_mul_f32 v193, s50, v193\n"
        "v_mul_f32 v194, s50, v194\n"
        "v_mfma_f32_16x16x32_bf16 v[64:67], a[88:91], a[216:219], v[64:67]\n"
        "s_waitcnt lgkmcnt(8)\n"
        "v_mul_f32 v195, s50, v195\n"
        "v_cvt_pk_bf16_f32 v218, v192, v193\n"
        "v_mfma_f32_16x16x32_bf16 v[64:67], a[92:95], a[220:223], v[64:67]\n"
        "s_waitcnt lgkmcnt(4)\n"
        "v_cvt_pk_bf16_f32 v219, v194, v195\n"
        "v_mul_f32 v212, s50, v212\n"
        "v_mfma_f32_16x16x32_bf16 v[68:71], a[116:119], a[220:223], v[68:71]\n"
        "buffer_load_dwordx4 v129, s[20:23], s64 offen offset:1024 lds\n"
        "v_mfma_f32_16x16x32_bf16 v[68:71], a[112:115], a[216:219], v[68:71]\n"
        "s_waitcnt lgkmcnt(2)\n"
        "v_mul_f32 v213, s50, v213\n"
        "v_mul_f32 v214, s50, v214\n"
        "v_mfma_f32_16x16x32_bf16 v[72:75], a[136:139], a[216:219], v[72:75]\n"
        "s_waitcnt lgkmcnt(0)\n"
        "v_mul_f32 v215, s50, v215\n"
        "v_cvt_pk_bf16_f32 v220, v212, v213\n"
        "v_mfma_f32_16x16x32_bf16 v[72:75], a[140:143], a[220:223], v[72:75]\n"
        "v_cvt_pk_bf16_f32 v221, v214, v215\n"
        "v_mov_b64 v[226:227], v[220:221]\n"
        "v_mfma_f32_16x16x32_bf16 v[76:79], a[92:95], a[228:231], v[76:79]\n"
        "buffer_load_dwordx4 v130, s[20:23], s64 offen offset:2048 lds\n"
        "v_mfma_f32_16x16x32_bf16 v[76:79], a[88:91], a[224:227], v[76:79]\n"
        "v_lshl_add_u32 v24, v24, 2, s67\n"
        "v_lshl_add_u32 v25, v25, 2, s67\n"
        "v_mfma_f32_16x16x32_bf16 v[80:83], a[112:115], a[224:227], v[80:83]\n"
        "ds_read_b32 v24, v24\n"
        "ds_read_b32 v25, v25\n"
        "v_mfma_f32_16x16x32_bf16 v[80:83], a[116:119], a[228:231], v[80:83]\n"
        "v_lshl_add_u32 v26, v26, 2, s67\n"
        "v_lshl_add_u32 v27, v27, 2, s67\n"
        "v_mfma_f32_16x16x32_bf16 v[84:87], a[140:143], a[228:231], v[84:87]\n"
        "ds_read_b32 v26, v26\n"
        "ds_read_b32 v27, v27\n"
        "v_mfma_f32_16x16x32_bf16 v[84:87], a[136:139], a[224:227], v[84:87]\n"
        "v_cvt_pk_bf16_f32 v152, v64, v65\n"
        "v_cvt_pk_bf16_f32 v153, v66, v67\n"
        "v_mfma_f32_16x16x32_bf16 v[88:91], a[88:91], a[232:235], v[88:91]\n"
        "s_waitcnt vmcnt(6)\n"
        "s_barrier\n"
        "s_add_u32 m0, s40, 24576\n"
        "v_cvt_pk_bf16_f32 v154, v68, v69\n"
        "v_mfma_f32_16x16x32_bf16 v[88:91], a[92:95], a[236:239], v[88:91]\n"
        "s_add_u32 s64, s65, 148224\n"
        "ds_read_b64_tr_b16 a[144:145], v131 offset:49152\n"
        "v_cvt_pk_bf16_f32 v155, v70, v71\n"
        "v_mfma_f32_16x16x32_bf16 v[108:111], a[116:119], a[236:239], v[108:111]\n"
        "ds_read_b64_tr_b16 a[146:147], v131 offset:50688\n"
        "ds_read_b128 v[162:165], v251 offset:0\n"
        "v_mfma_f32_16x16x32_bf16 v[108:111], a[112:115], a[232:235], v[108:111]\n"
        "buffer_load_dwordx4 v128, s[20:23], s64 offen lds\n"
        "v_mfma_f32_16x16x32_bf16 v[112:115], a[136:139], a[232:235], v[112:115]\n"
        "ds_read_b64_tr_b16 a[148:149], v131 offset:61440\n"
        "ds_read_b64_tr_b16 a[150:151], v131 offset:62976\n"
        "v_mfma_f32_16x16x32_bf16 v[112:115], a[140:143], a[236:239], v[112:115]\n"
        "ds_read_b64_tr_b16 a[168:169], v132 offset:49152\n"
        "v_cvt_pk_bf16_f32 v168, v72, v73\n"
        "v_mfma_f32_16x16x32_bf16 v[96:99], a[92:95], a[244:247], v[96:99]\n"
        "ds_read_b64_tr_b16 a[170:171], v132 offset:50688\n"
        "ds_read_b128 v[182:185], v251 offset:512\n"
        "v_mfma_f32_16x16x32_bf16 v[96:99], a[88:91], a[240:243], v[96:99]\n"
        "buffer_load_dwordx4 v129, s[20:23], s64 offen offset:1024 lds\n"
        "v_mfma_f32_16x16x32_bf16 v[100:103], a[112:115], a[240:243], v[100:103]\n"
        "ds_read_b64_tr_b16 a[172:173], v132 offset:61440\n"
        "ds_read_b64_tr_b16 a[174:175], v132 offset:62976\n"
        "v_mfma_f32_16x16x32_bf16 v[100:103], a[116:119], a[244:247], v[100:103]\n"
        "ds_read_b64_tr_b16 a[192:193], v133 offset:49152\n"
        "v_cvt_pk_bf16_f32 v169, v74, v75\n"
        "v_mfma_f32_16x16x32_bf16 v[104:107], a[140:143], a[244:247], v[104:107]\n"
        "ds_read_b64_tr_b16 a[194:195], v133 offset:50688\n"
        "ds_read_b128 v[202:205], v251 offset:4096\n"
        "v_mfma_f32_16x16x32_bf16 v[104:107], a[136:139], a[240:243], v[104:107]\n"
        "buffer_load_dwordx4 v130, s[20:23], s64 offen offset:2048 lds\n"
        "v_mfma_f32_16x16x32_bf16 v[64:67], v[152:155], v[156:159], v[0:3]\n"
        "s_waitcnt lgkmcnt(10)\n"
        "s_add_u32 m0, s40, 36864\n"
        "ds_read_b64_tr_b16 a[196:197], v133 offset:61440\n"
        "ds_read_b64_tr_b16 a[198:199], v133 offset:62976\n"
        "v_mfma_f32_16x16x32_bf16 v[64:67], v[168:171], v[160:163], v[64:67]\n"
        "s_add_u32 s64, s65, 185088\n"
        "v_cvt_pk_bf16_f32 v172, v76, v77\n"
        "v_cvt_pk_bf16_f32 v173, v78, v79\n"
        "v_cvt_pk_bf16_f32 v174, v80, v81\n"
        "v_cvt_pk_bf16_f32 v175, v82, v83\n"
        "s_nop 1\n"
        "v_mfma_f32_16x16x32_bf16 v[68:71], v[172:175], v[156:159], v[4:7]\n"
        "buffer_load_dwordx4 v128, s[20:23], s64 offen lds\n"
        "v_cvt_pk_bf16_f32 v190, v84, v85\n"
        "v_cvt_pk_bf16_f32 v191, v86, v87\n"
        "s_nop 1\n"
        "v_mfma_f32_16x16x32_bf16 v[68:71], v[188:191], v[164:167], v[68:71]\n"
        "ds_read_b128 v[222:225], v251 offset:4608\n"
        "v_cvt_pk_bf16_f32 v192, v88, v89\n"
        "v_mfma_f32_16x16x32_bf16 v[72:75], v[152:155], v[176:179], v[16:19]\n"
        "s_waitcnt lgkmcnt(8)\n"
        "v_cvt_pk_bf16_f32 v193, v90, v91\n"
        "v_cvt_pk_bf16_f32 v194, v108, v109\n"
        "v_mfma_f32_16x16x32_bf16 v[72:75], v[168:171], v[180:183], v[72:75]\n"
        "v_cvt_pk_bf16_f32 v195, v110, v111\n"
        "v_cvt_pk_bf16_f32 v208, v112, v113\n"
        "v_mfma_f32_16x16x32_bf16 v[76:79], v[172:175], v[176:179], v[20:23]\n"
        "buffer_load_dwordx4 v129, s[20:23], s64 offen offset:1024 lds\n"
        "v_mfma_f32_16x16x32_bf16 v[76:79], v[188:191], v[184:187], v[76:79]\n"
        "ds_read_b128 v[162:165], v252 offset:0\n"
        "v_cvt_pk_bf16_f32 v209, v114, v115\n"
        "v_mfma_f32_16x16x32_bf16 v[80:83], v[152:155], v[196:199], v[32:35]\n"
        "s_waitcnt lgkmcnt(4)\n"
        "ds_read_b128 v[182:185], v252 offset:512\n"
        "v_cvt_pk_bf16_f32 v212, v96, v97\n"
        "v_mfma_f32_16x16x32_bf16 v[80:83], v[168:171], v[200:203], v[80:83]\n"
        "v_cvt_pk_bf16_f32 v213, v98, v99\n"
        "v_cvt_pk_bf16_f32 v214, v100, v101\n"
        "v_mfma_f32_16x16x32_bf16 v[84:87], v[172:175], v[196:199], v[36:39]\n"
        "buffer_load_dwordx4 v130, s[20:23], s64 offen offset:2048 lds\n"
        "v_mfma_f32_16x16x32_bf16 v[84:87], v[188:191], v[204:207], v[84:87]\n"
        "s_add_u32 m0, s40, 98304\n"
        "ds_read_b128 v[202:205], v252 offset:4096\n"
        "v_cvt_pk_bf16_f32 v215, v102, v103\n"
        "v_mfma_f32_16x16x32_bf16 v[88:91], v[152:155], v[216:219], v[48:51]\n"
        "s_waitcnt lgkmcnt(3)\n"
        "s_waitcnt lgkmcnt(4)\n"
        "s_waitcnt vmcnt(6)\n"
        "s_barrier\n"
        "ds_read_b128 a[248:251], v240 offset:0\n"
        "v_cvt_pk_bf16_f32 v230, v104, v105\n"
        "v_mfma_f32_16x16x32_bf16 v[88:91], v[168:171], v[220:223], v[88:91]\n"
        "s_sub_u32 s64, s47, 6291456\n"
        "ds_read_b64_tr_b16 a[152:153], v131 offset:0\n"
        "ds_read_b128 a[216:219], v241 offset:0\n"
        "v_mfma_f32_16x16x32_bf16 v[92:95], v[172:175], v[216:219], v[52:55]\n"
        "ds_read_b64_tr_b16 a[154:155], v131 offset:1536\n"
        "ds_read_b128 a[220:223], v240 offset:128\n"
        "v_mfma_f32_16x16x32_bf16 v[92:95], v[188:191], v[224:227], v[92:95]\n"
        "buffer_load_dwordx4 v242, s[8:11], s64 offen lds\n"
        "v_mfma_f32_16x16x32_bf16 v[108:111], v[212:215], v[156:159], v[12:15]\n"
        "s_waitcnt lgkmcnt(7)\n"
        "s_add_u32 m0, s40, 99328\n"
        "ds_read_b64_tr_b16 a[156:157], v131 offset:12288\n"
        "ds_read_b128 a[224:227], v241 offset:128\n"
        "v_cvt_pk_bf16_f32 v231, v106, v107\n"
        "s_nop 1\n"
        "v_mfma_f32_16x16x32_bf16 v[108:111], v[228:231], v[164:167], v[108:111]\n"
        "s_sub_u32 s64, s47, 6291456\n"
        "ds_read_b64_tr_b16 a[158:159], v131 offset:13824\n"
        "ds_read_b64_tr_b16 a[176:177], v132 offset:0\n"
        "v_mfma_f32_16x16x32_bf16 v[112:115], v[212:215], v[176:179], v[28:31]\n"
        "s_waitcnt lgkmcnt(10)\n"
        "ds_read_b64_tr_b16 a[178:179], v132 offset:1536\n"
        "v_mfma_f32_16x16x32_bf16 v[112:115], v[228:231], v[184:187], v[112:115]\n"
        "buffer_load_dwordx4 v243, s[8:11], s64 offen lds\n"
        "v_mfma_f32_16x16x32_bf16 v[116:119], v[212:215], v[196:199], v[44:47]\n"
        "s_waitcnt lgkmcnt(10)\n"
        "s_add_u32 m0, s40, 100352\n"
        "ds_read_b64_tr_b16 a[180:181], v132 offset:12288\n"
        "ds_read_b64_tr_b16 a[182:183], v132 offset:13824\n"
        "v_mfma_f32_16x16x32_bf16 v[116:119], v[228:231], v[204:207], v[116:119]\n"
        "s_waitcnt lgkmcnt(11)\n"
        "s_sub_u32 s64, s47, 6291456\n"
        "ds_read_b64_tr_b16 a[200:201], v133 offset:0\n"
        "v_mfma_f32_16x16x32_bf16 v[120:123], a[248:251], a[144:147], 0\n"
        "s_waitcnt lgkmcnt(10)\n"
        "ds_read_b64_tr_b16 a[202:203], v133 offset:1536\n"
        "v_mfma_f32_16x16x32_bf16 v[120:123], a[216:219], a[148:151], v[120:123]\n"
        "s_waitcnt lgkmcnt(9)\n"
        "buffer_load_dwordx4 v244, s[8:11], s64 offen lds\n"
        "v_mfma_f32_16x16x32_bf16 v[120:123], a[220:223], a[152:155], v[120:123]\n"
        "s_waitcnt lgkmcnt(6)\n"
        "s_add_u32 m0, s40, 110592\n"
        "ds_read_b64_tr_b16 a[204:205], v133 offset:12288\n"
        "ds_read_b64_tr_b16 a[206:207], v133 offset:13824\n"
        "v_mfma_f32_16x16x32_bf16 v[120:123], a[224:227], a[156:159], v[120:123]\n"
        "s_waitcnt lgkmcnt(4)\n"
        "s_sub_u32 s64, s47, 5505024\n"
        "v_exp_f32 v64, v64\n"
        "v_mfma_f32_16x16x32_bf16 v[96:99], a[224:227], a[180:183], 0\n"
        "buffer_load_dwordx4 v242, s[8:11], s64 offen lds\n"
        "v_mfma_f32_16x16x32_bf16 v[96:99], a[220:223], a[176:179], v[96:99]\n"
        "s_add_u32 m0, s40, 111616\n"
        "ds_read_b128 a[228:231], v240 offset:1536\n"
        "ds_read_b128 a[232:235], v241 offset:1536\n"
        "v_mfma_f32_16x16x32_bf16 v[96:99], a[216:219], a[172:175], v[96:99]\n"
        "s_sub_u32 s64, s47, 5505024\n"
        "ds_read_b128 a[236:239], v240 offset:1664\n"
        "v_mfma_f32_16x16x32_bf16 v[96:99], a[248:251], a[168:171], v[96:99]\n"
        "ds_read_b128 a[240:243], v241 offset:1664\n"
        "v_mfma_f32_16x16x32_bf16 v[100:103], a[248:251], a[192:195], 0\n"
        "buffer_load_dwordx4 v243, s[8:11], s64 offen lds\n"
        "v_mfma_f32_16x16x32_bf16 v[100:103], a[216:219], a[196:199], v[100:103]\n"
        "s_waitcnt lgkmcnt(6)\n"
        "s_add_u32 m0, s40, 112640\n"
        "v_exp_f32 v65, v65\n"
        "v_mfma_f32_16x16x32_bf16 v[100:103], a[220:223], a[200:203], v[100:103]\n"
        "s_waitcnt lgkmcnt(4)\n"
        "s_sub_u32 s64, s47, 5505024\n"
        "v_exp_f32 v66, v66\n"
        "v_mfma_f32_16x16x32_bf16 v[100:103], a[224:227], a[204:207], v[100:103]\n"
        "ds_read_b128 a[244:247], v240 offset:12288\n"
        "v_cvt_pk_bf16_f32 v64, v64, v65\n"
        "v_exp_f32 v68, v68\n"
        "v_exp_f32 v67, v67\n"
        "v_exp_f32 v69, v69\n"
        "v_cvt_pk_bf16_f32 v65, v66, v67\n"
        "v_exp_f32 v70, v70\n"
        "v_cvt_pk_bf16_f32 v66, v68, v69\n"
        "v_exp_f32 v71, v71\n"
        "v_exp_f32 v72, v72\n"
        "v_cvt_pk_bf16_f32 v67, v70, v71\n"
        "v_mfma_f32_16x16x32_bf16 v[68:71], v[192:195], v[156:159], v[8:11]\n"
        "buffer_load_dwordx4 v244, s[8:11], s64 offen lds\n"
        "v_mfma_f32_16x16x32_bf16 v[68:71], v[208:211], v[160:163], v[68:71]\n"
        "s_waitcnt lgkmcnt(1)\n"
        "s_xor_b32 m0, s48, 8192\n"
        "v_exp_f32 v73, v73\n"
        "v_mfma_f32_16x16x32_bf16 v[104:107], a[240:243], a[156:159], 0\n"
        "s_sub_u32 s64, s36, 2097152\n"
        "v_exp_f32 v74, v74\n"
        "v_mfma_f32_16x16x32_bf16 v[104:107], a[236:239], a[152:155], v[104:107]\n"
        "ds_read_b128 a[248:251], v241 offset:12288\n"
        "v_cvt_pk_bf16_f32 v72, v72, v73\n"
        "v_mfma_f32_16x16x32_bf16 v[104:107], a[232:235], a[148:151], v[104:107]\n"
        "buffer_load_dwordx4 v249, s[12:15], s64 offen lds\n"
        "v_mfma_f32_16x16x32_bf16 v[104:107], a[228:231], a[144:147], v[104:107]\n"
        "v_exp_f32 v75, v75\n"
        "v_mfma_f32_16x16x32_bf16 v[156:159], a[228:231], a[168:171], 0\n"
        "s_waitcnt vmcnt(7)\n"
        "s_barrier\n"
        "ds_read_b128 a[216:219], v240 offset:12416\n"
        "v_cvt_pk_bf16_f32 v73, v74, v75\n"
        "v_mfma_f32_16x16x32_bf16 v[156:159], a[232:235], a[172:175], v[156:159]\n"
        "ds_read_b64_tr_b16 a[160:161], v131 offset:24576\n"
        "v_mfma_f32_16x16x32_bf16 v[156:159], a[236:239], a[176:179], v[156:159]\n"
        "ds_read_b64_tr_b16 a[162:163], v131 offset:26112\n"
        "v_mfma_f32_16x16x32_bf16 v[156:159], a[240:243], a[180:183], v[156:159]\n"
        "buffer_load_dwordx4 v149, s[12:15], s64 offen offset:1024 lds\n"
        "v_mfma_f32_16x16x32_bf16 v[160:163], a[240:243], a[204:207], 0\n"
        "ds_read_b64_tr_b16 a[164:165], v131 offset:36864\n"
        "ds_read_b128 a[220:223], v241 offset:12416\n"
        "v_mfma_f32_16x16x32_bf16 v[160:163], a[236:239], a[200:203], v[160:163]\n"
        "ds_read_b64_tr_b16 a[166:167], v131 offset:38400\n"
        "ds_read_b64_tr_b16 a[184:185], v132 offset:24576\n"
        "v_mfma_f32_16x16x32_bf16 v[160:163], a[232:235], a[196:199], v[160:163]\n"
        "ds_read_b64_tr_b16 a[186:187], v132 offset:26112\n"
        "v_mfma_f32_16x16x32_bf16 v[160:163], a[228:231], a[192:195], v[160:163]\n"
        "ds_read_b64_tr_b16 a[188:189], v132 offset:36864\n"
        "v_exp_f32 v76, v76\n"
        "v_exp_f32 v77, v77\n"
        "v_exp_f32 v78, v78\n"
        "v_cvt_pk_bf16_f32 v74, v76, v77\n"
        "v_exp_f32 v79, v79\n"
        "v_exp_f32 v80, v80\n"
        "v_cvt_pk_bf16_f32 v75, v78, v79\n"
        "v_mfma_f32_16x16x32_bf16 v[76:79], v[192:195], v[176:179], v[24:27]\n"
        "ds_read_b64_tr_b16 a[190:191], v132 offset:38400\n"
        "v_mfma_f32_16x16x32_bf16 v[76:79], v[208:211], v[180:183], v[76:79]\n"
        "s_waitcnt lgkmcnt(11)\n"
        "ds_read_b64_tr_b16 a[208:209], v133 offset:24576\n"
        "v_mfma_f32_16x16x32_bf16 v[164:167], a[244:247], a[144:147], 0\n"
        "s_waitcnt lgkmcnt(11)\n"
        "ds_read_b64_tr_b16 a[210:211], v133 offset:26112\n"
        "v_mfma_f32_16x16x32_bf16 v[164:167], a[248:251], a[148:151], v[164:167]\n"
        "s_waitcnt lgkmcnt(11)\n"
        "ds_read_b64_tr_b16 a[212:213], v133 offset:36864\n"
        "v_mfma_f32_16x16x32_bf16 v[164:167], a[216:219], a[152:155], v[164:167]\n"
        "s_waitcnt lgkmcnt(8)\n"
        "ds_read_b64_tr_b16 a[214:215], v133 offset:38400\n"
        "v_mfma_f32_16x16x32_bf16 v[164:167], a[220:223], a[156:159], v[164:167]\n"
        "v_exp_f32 v81, v81\n"
        "v_mfma_f32_16x16x32_bf16 v[176:179], a[220:223], a[180:183], 0\n"
        "ds_read_b128 a[224:227], v240 offset:13824\n"
        "v_cvt_pk_bf16_f32 v80, v80, v81\n"
        "v_mfma_f32_16x16x32_bf16 v[176:179], a[216:219], a[176:179], v[176:179]\n"
        "ds_read_b128 a[228:231], v241 offset:13824\n"
        "v_mfma_f32_16x16x32_bf16 v[176:179], a[248:251], a[172:175], v[176:179]\n"
        "ds_read_b128 a[232:235], v240 offset:13952\n"
        "v_mfma_f32_16x16x32_bf16 v[176:179], a[244:247], a[168:171], v[176:179]\n"
        "ds_read_b128 a[236:239], v241 offset:13952\n"
        "v_mfma_f32_16x16x32_bf16 v[180:183], a[244:247], a[192:195], 0\n"
        "v_exp_f32 v82, v82\n"
        "v_mfma_f32_16x16x32_bf16 v[180:183], a[248:251], a[196:199], v[180:183]\n"
        "v_exp_f32 v83, v83\n"
        "v_mfma_f32_16x16x32_bf16 v[180:183], a[216:219], a[200:203], v[180:183]\n"
        "ds_read_b128 v[222:225], v252 offset:4608\n"
        "v_cvt_pk_bf16_f32 v81, v82, v83\n"
        "v_mfma_f32_16x16x32_bf16 v[180:183], a[220:223], a[204:207], v[180:183]\n"
        "v_exp_f32 v84, v84\n"
        "v_exp_f32 v85, v85\n"
        "v_exp_f32 v86, v86\n"
        "v_cvt_pk_bf16_f32 v82, v84, v85\n"
        "v_exp_f32 v87, v87\n"
        "v_exp_f32 v88, v88\n"
        "v_cvt_pk_bf16_f32 v83, v86, v87\n"
        "v_mfma_f32_16x16x32_bf16 v[84:87], v[192:195], v[196:199], v[40:43]\n"
        "v_exp_f32 v89, v89\n"
        "v_mfma_f32_16x16x32_bf16 v[84:87], v[208:211], v[200:203], v[84:87]\n"
        "s_waitcnt lgkmcnt(1)\n"
        "v_exp_f32 v90, v90\n"
        "v_mfma_f32_16x16x32_bf16 v[184:187], a[236:239], a[156:159], 0\n"
        "ds_read_b128 a[240:243], v240 offset:256\n"
        "v_cvt_pk_bf16_f32 v88, v88, v89\n"
        "v_mfma_f32_16x16x32_bf16 v[184:187], a[232:235], a[152:155], v[184:187]\n"
        "v_exp_f32 v91, v91\n"
        "v_mfma_f32_16x16x32_bf16 v[184:187], a[228:231], a[148:151], v[184:187]\n"
        "ds_read_b128 a[244:247], v241 offset:256\n"
        "v_cvt_pk_bf16_f32 v89, v90, v91\n"
        "v_mfma_f32_16x16x32_bf16 v[184:187], a[224:227], a[144:147], v[184:187]\n"
        "v_exp_f32 v92, v92\n"
        "v_mfma_f32_16x16x32_bf16 v[196:199], a[224:227], a[168:171], 0\n"
        "v_exp_f32 v93, v93\n"
        "v_mfma_f32_16x16x32_bf16 v[196:199], a[228:231], a[172:175], v[196:199]\n"
        "v_exp_f32 v94, v94\n"
        "v_mfma_f32_16x16x32_bf16 v[196:199], a[232:235], a[176:179], v[196:199]\n"
        "ds_read_b128 a[248:251], v240 offset:1792\n"
        "v_cvt_pk_bf16_f32 v90, v92, v93\n"
        "v_mfma_f32_16x16x32_bf16 v[196:199], a[236:239], a[180:183], v[196:199]\n"
        "v_exp_f32 v95, v95\n"
        "v_mfma_f32_16x16x32_bf16 v[200:203], a[236:239], a[204:207], 0\n"
        "ds_read_b128 a[216:219], v241 offset:1792\n"
        "v_cvt_pk_bf16_f32 v91, v94, v95\n"
        "v_mfma_f32_16x16x32_bf16 v[200:203], a[232:235], a[200:203], v[200:203]\n"
        "v_exp_f32 v108, v108\n"
        "v_mfma_f32_16x16x32_bf16 v[200:203], a[228:231], a[196:199], v[200:203]\n"
        "v_exp_f32 v109, v109\n"
        "v_mfma_f32_16x16x32_bf16 v[200:203], a[224:227], a[192:195], v[200:203]\n"
        "v_exp_f32 v110, v110\n"
        "v_exp_f32 v111, v111\n"
        "v_exp_f32 v70, v70\n"
        "v_exp_f32 v69, v69\n"
        "v_exp_f32 v68, v68\n"
        "v_exp_f32 v71, v71\n"
        "v_cvt_pk_bf16_f32 v68, v68, v69\n"
        "v_cvt_pk_bf16_f32 v69, v70, v71\n"
        "v_cvt_pk_bf16_f32 v70, v108, v109\n"
        "v_cvt_pk_bf16_f32 v71, v110, v111\n"
        "v_mfma_f32_16x16x32_bf16 v[108:111], v[192:195], v[216:219], v[56:59]\n"
        "s_waitcnt lgkmcnt(4)\n"
        "v_exp_f32 v112, v112\n"
        "v_mfma_f32_16x16x32_bf16 v[108:111], v[208:211], v[220:223], v[108:111]\n"
        "v_exp_f32 v113, v113\n"
        "v_exp_f32 v114, v114\n"
        "v_exp_f32 v115, v115\n"
        "v_exp_f32 v78, v78\n"
        "v_exp_f32 v77, v77\n"
        "v_exp_f32 v76, v76\n"
        "v_exp_f32 v79, v79\n"
        "v_cvt_pk_bf16_f32 v76, v76, v77\n"
        "v_cvt_pk_bf16_f32 v77, v78, v79\n"
        "v_cvt_pk_bf16_f32 v78, v112, v113\n"
        "v_cvt_pk_bf16_f32 v79, v114, v115\n"
        "v_mfma_f32_16x16x32_bf16 v[112:115], v[212:215], v[216:219], v[60:63]\n"
        "v_exp_f32 v116, v116\n"
        "v_mfma_f32_16x16x32_bf16 v[112:115], v[228:231], v[224:227], v[112:115]\n"
        "s_waitcnt lgkmcnt(3)\n"
        "v_exp_f32 v117, v117\n"
        "v_mfma_f32_16x16x32_bf16 v[120:123], a[240:243], a[160:163], v[120:123]\n"
        "s_waitcnt lgkmcnt(2)\n"
        "v_exp_f32 v118, v118\n"
        "v_mfma_f32_16x16x32_bf16 v[120:123], a[244:247], a[164:167], v[120:123]\n"
        "v_exp_f32 v119, v119\n"
        "v_mfma_f32_16x16x32_bf16 v[96:99], a[244:247], a[188:191], v[96:99]\n"
        "ds_read_b128 a[220:223], v240 offset:12544\n"
        "v_mfma_f32_16x16x32_bf16 v[96:99], a[240:243], a[184:187], v[96:99]\n"
        "ds_read_b128 a[224:227], v241 offset:12544\n"
        "v_mfma_f32_16x16x32_bf16 v[100:103], a[240:243], a[208:211], v[100:103]\n"
        "v_exp_f32 v84, v84\n"
        "v_mfma_f32_16x16x32_bf16 v[100:103], a[244:247], a[212:215], v[100:103]\n"
        "s_waitcnt lgkmcnt(2)\n"
        "v_exp_f32 v85, v85\n"
        "v_mfma_f32_16x16x32_bf16 v[104:107], a[216:219], a[164:167], v[104:107]\n"
        "v_exp_f32 v86, v86\n"
        "v_mfma_f32_16x16x32_bf16 v[104:107], a[248:251], a[160:163], v[104:107]\n"
        "v_cvt_pk_bf16_f32 v84, v84, v85\n"
        "v_cvt_pk_bf16_f32 v128, v120, v121\n"
        "v_mfma_f32_16x16x32_bf16 v[156:159], a[248:251], a[184:187], v[156:159]\n"
        "ds_read_b128 a[228:231], v240 offset:14080\n"
        "v_cvt_pk_bf16_f32 v129, v122, v123\n"
        "v_mfma_f32_16x16x32_bf16 v[156:159], a[216:219], a[188:191], v[156:159]\n"
        "ds_read_b128 a[232:235], v241 offset:14080\n"
        "v_cvt_pk_bf16_f32 v136, v96, v97\n"
        "v_mfma_f32_16x16x32_bf16 v[160:163], a[216:219], a[212:215], v[160:163]\n"
        "v_exp_f32 v87, v87\n"
        "v_mfma_f32_16x16x32_bf16 v[160:163], a[248:251], a[208:211], v[160:163]\n"
        "s_waitcnt lgkmcnt(3)\n"
        "v_cvt_pk_bf16_f32 v85, v86, v87\n"
        "v_cvt_pk_bf16_f32 v86, v116, v117\n"
        "v_mfma_f32_16x16x32_bf16 v[164:167], a[220:223], a[160:163], v[164:167]\n"
        "s_waitcnt lgkmcnt(2)\n"
        "v_cvt_pk_bf16_f32 v87, v118, v119\n"
        "v_cvt_pk_bf16_f32 v137, v98, v99\n"
        "v_mfma_f32_16x16x32_bf16 v[164:167], a[224:227], a[164:167], v[164:167]\n"
        "v_exp_f32 v108, v108\n"
        "v_mfma_f32_16x16x32_bf16 v[176:179], a[224:227], a[188:191], v[176:179]\n"
        "v_exp_f32 v109, v109\n"
        "v_mfma_f32_16x16x32_bf16 v[176:179], a[220:223], a[184:187], v[176:179]\n"
        "v_exp_f32 v110, v110\n"
        "v_mfma_f32_16x16x32_bf16 v[180:183], a[220:223], a[208:211], v[180:183]\n"
        "s_waitcnt lgkmcnt(0)\n"
        "s_barrier\n"
        "v_cvt_pk_bf16_f32 v92, v108, v109\n"
        "v_cvt_pk_bf16_f32 v144, v100, v101\n"
        "v_mfma_f32_16x16x32_bf16 v[180:183], a[224:227], a[212:215], v[180:183]\n"
        "v_exp_f32 v111, v111\n"
        "v_mfma_f32_16x16x32_bf16 v[184:187], a[232:235], a[164:167], v[184:187]\n"
        "v_cvt_pk_bf16_f32 v93, v110, v111\n"
        "v_cvt_pk_bf16_f32 v145, v102, v103\n"
        "v_mfma_f32_16x16x32_bf16 v[184:187], a[228:231], a[160:163], v[184:187]\n"
        "v_exp_f32 v112, v112\n"
        "v_mfma_f32_16x16x32_bf16 v[196:199], a[228:231], a[184:187], v[196:199]\n"
        "v_exp_f32 v113, v113\n"
        "v_mfma_f32_16x16x32_bf16 v[196:199], a[232:235], a[188:191], v[196:199]\n"
        "v_exp_f32 v114, v114\n"
        "v_mfma_f32_16x16x32_bf16 v[200:203], a[232:235], a[212:215], v[200:203]\n"
        "v_cvt_pk_bf16_f32 v94, v112, v113\n"
        "v_cvt_pk_bf16_f32 v130, v104, v105\n"
        "v_mfma_f32_16x16x32_bf16 v[200:203], a[228:231], a[208:211], v[200:203]\n"
        "v_exp_f32 v115, v115\n"
        "v_mov_b64 v[120:121], 0\n"
        "s_nop 1\n"
        "v_smfmac_f32_16x16x64_bf16 v[120:123], a[252:255], v[64:71], v255\n"
        "v_cvt_pk_bf16_f32 v95, v114, v115\n"
        "v_cvt_pk_bf16_f32 v131, v106, v107\n"
        "s_nop 1\n"
        "v_mfma_f32_16x16x32_bf16 v[116:119], v[128:131], v[64:67], 0\n"
        "v_cvt_pk_bf16_f32 v138, v156, v157\n"
        "v_cvt_pk_bf16_f32 v139, v158, v159\n"
        "v_cvt_pk_bf16_f32 v132, v164, v165\n"
        "v_cvt_pk_bf16_f32 v133, v166, v167\n"
        "v_cvt_pk_bf16_f32 v134, v184, v185\n"
        "v_cvt_pk_bf16_f32 v135, v186, v187\n"
        "s_nop 1\n"
        "v_mfma_f32_16x16x32_bf16 v[116:119], v[132:135], v[68:71], v[116:119]\n"
        "v_cvt_pk_bf16_f32 v146, v160, v161\n"
        "v_cvt_pk_bf16_f32 v147, v162, v163\n"
        "v_cvt_pk_bf16_f32 v140, v176, v177\n"
        "v_cvt_pk_bf16_f32 v141, v178, v179\n"
        "v_cvt_pk_bf16_f32 v142, v196, v197\n"
        "v_cvt_pk_bf16_f32 v143, v198, v199\n"
        "s_nop 1\n"
        "v_mfma_f32_16x16x32_bf16 v[152:155], v[140:143], v[68:71], 0\n"
        "v_mov_b64 v[172:173], 0\n"
        "v_cvt_pk_bf16_f32 v148, v180, v181\n"
        "v_mfma_f32_16x16x32_bf16 v[152:155], v[136:139], v[64:67], v[152:155]\n"
        "v_cvt_pk_bf16_f32 v149, v182, v183\n"
        "v_cvt_pk_bf16_f32 v150, v200, v201\n"
        "v_mfma_f32_16x16x32_bf16 v[160:163], v[144:147], v[64:67], 0\n"
        "v_cvt_pk_bf16_f32 v151, v202, v203\n"
        "v_mov_b64 v[192:193], 0\n"
        "s_nop 0\n"
        "v_mfma_f32_16x16x32_bf16 v[160:163], v[148:151], v[68:71], v[160:163]\n"
        "v_add_f32 v120, v120, v121\n"
        "v_add_u32 v240, 24576, v240\n"
        "v_smfmac_f32_16x16x64_bf16 v[172:175], a[252:255], v[72:79], v255\n"
        "v_add_u32 v241, 24576, v241\n"
        "v_mfma_f32_16x16x32_bf16 v[176:179], v[128:131], v[72:75], 0\n"
        "v_rcp_f32 v121, v120\n"
        "v_mfma_f32_16x16x32_bf16 v[176:179], v[132:135], v[76:79], v[176:179]\n"
        "v_mov_b64 v[212:213], 0\n"
        "v_mul_f32 v116, v121, v116\n"
        "v_mfma_f32_16x16x32_bf16 v[180:183], v[140:143], v[76:79], 0\n"
        "v_mul_f32 v117, v121, v117\n"
        "v_mul_f32 v118, v121, v118\n"
        "v_mfma_f32_16x16x32_bf16 v[180:183], v[136:139], v[72:75], v[180:183]\n"
        "v_mul_f32 v119, v121, v119\n"
        "v_cvt_pk_bf16_f32 v116, v116, v117\n"
        "v_mfma_f32_16x16x32_bf16 v[184:187], v[144:147], v[72:75], 0\n"
        "v_cvt_pk_bf16_f32 v117, v118, v119\n"
        "v_mul_f32 v152, v121, v152\n"
        "v_mfma_f32_16x16x32_bf16 v[184:187], v[148:151], v[76:79], v[184:187]\n"
        "v_mul_f32 v153, v121, v153\n"
        "v_maximum3_f32 v253, v253, v120, v121\n"
        "v_smfmac_f32_16x16x64_bf16 v[192:195], a[252:255], v[80:87], v255\n"
        "s_waitcnt vmcnt(5)\n"
        "s_barrier\n"
        "ds_read_b128 a[236:239], v240 offset:0\n"
        "v_mul_f32 v154, v121, v154\n"
        "v_mfma_f32_16x16x32_bf16 v[196:199], v[128:131], v[80:83], 0\n"
        "ds_read_b128 a[240:243], v241 offset:0\n"
        "v_mul_f32 v155, v121, v155\n"
        "v_mfma_f32_16x16x32_bf16 v[196:199], v[132:135], v[84:87], v[196:199]\n"
        "v_cvt_pk_bf16_f32 v118, v152, v153\n"
        "v_cvt_pk_bf16_f32 v119, v154, v155\n"
        "v_mfma_f32_16x16x32_bf16 v[200:203], v[140:143], v[84:87], 0\n"
        "ds_read_b128 a[244:247], v240 offset:128\n"
        "v_mul_f32 v160, v121, v160\n"
        "v_mfma_f32_16x16x32_bf16 v[200:203], v[136:139], v[80:83], v[200:203]\n"
        "ds_write_b128 v245, v[116:119] offset:0\n"
        "v_mfma_f32_16x16x32_bf16 v[204:207], v[144:147], v[80:83], 0\n"
        "ds_read_b128 a[248:251], v241 offset:128\n"
        "v_mul_f32 v161, v121, v161\n"
        "v_mfma_f32_16x16x32_bf16 v[204:207], v[148:151], v[84:87], v[204:207]\n"
        "ds_read_b128 a[216:219], v240 offset:256\n"
        "v_mul_f32 v162, v121, v162\n"
        "v_smfmac_f32_16x16x64_bf16 v[212:215], a[252:255], v[88:95], v255\n"
        "ds_read_b128 a[220:223], v241 offset:256\n"
        "v_mul_f32 v163, v121, v163\n"
        "v_mfma_f32_16x16x32_bf16 v[216:219], v[128:131], v[88:91], 0\n"
        "v_cvt_pk_bf16_f32 v160, v160, v161\n"
        "v_cvt_pk_bf16_f32 v161, v162, v163\n"
        "v_mfma_f32_16x16x32_bf16 v[216:219], v[132:135], v[92:95], v[216:219]\n"
        "ds_write_b64 v246, v[160:161] offset:0\n"
        "v_mfma_f32_16x16x32_bf16 v[220:223], v[140:143], v[92:95], 0\n"
        "v_add_f32 v172, v172, v173\n"
        "v_add_f32 v192, v192, v193\n"
        "v_mfma_f32_16x16x32_bf16 v[220:223], v[136:139], v[88:91], v[220:223]\n"
        "v_rcp_f32 v173, v172\n"
        "v_mfma_f32_16x16x32_bf16 v[224:227], v[144:147], v[88:91], 0\n"
        "v_rcp_f32 v193, v192\n"
        "v_mfma_f32_16x16x32_bf16 v[224:227], v[148:151], v[92:95], v[224:227]\n"
        "s_waitcnt lgkmcnt(7)\n"
        "v_mul_f32 v176, v173, v176\n"
        "v_mul_f32 v177, v173, v177\n"
        "v_mfma_f32_16x16x32_bf16 v[96:99], a[72:75], a[236:239], 0\n"
        "s_waitcnt lgkmcnt(6)\n"
        "v_mul_f32 v178, v173, v178\n"
        "v_mul_f32 v179, v173, v179\n"
        "v_mfma_f32_16x16x32_bf16 v[96:99], a[76:79], a[240:243], v[96:99]\n"
        "s_waitcnt lgkmcnt(5)\n"
        "v_cvt_pk_bf16_f32 v176, v176, v177\n"
        "v_cvt_pk_bf16_f32 v177, v178, v179\n"
        "v_mfma_f32_16x16x32_bf16 v[96:99], a[80:83], a[244:247], v[96:99]\n"
        "s_waitcnt lgkmcnt(3)\n"
        "v_mul_f32 v180, v173, v180\n"
        "v_mul_f32 v181, v173, v181\n"
        "v_mfma_f32_16x16x32_bf16 v[96:99], a[84:87], a[248:251], v[96:99]\n"
        "s_waitcnt lgkmcnt(2)\n"
        "v_maximum3_f32 v253, v253, v172, v173\n"
        "v_mul_f32 v182, v173, v182\n"
        "v_mfma_f32_16x16x32_bf16 v[96:99], a[88:91], a[216:219], v[96:99]\n"
        "s_waitcnt lgkmcnt(1)\n"
        "v_mul_f32 v183, v173, v183\n"
        "v_cvt_pk_bf16_f32 v178, v180, v181\n"
        "v_mfma_f32_16x16x32_bf16 v[96:99], a[92:95], a[220:223], v[96:99]\n"
        "v_cvt_pk_bf16_f32 v179, v182, v183\n"
        "v_mul_f32 v184, v173, v184\n"
        "v_mfma_f32_16x16x32_bf16 v[100:103], a[116:119], a[220:223], 0\n"
        "ds_write_b128 v245, v[176:179] offset:256\n"
        "v_mfma_f32_16x16x32_bf16 v[100:103], a[112:115], a[216:219], v[100:103]\n"
        "v_mul_f32 v185, v173, v185\n"
        "v_mul_f32 v186, v173, v186\n"
        "v_mfma_f32_16x16x32_bf16 v[100:103], a[108:111], a[248:251], v[100:103]\n"
        "s_sub_u32 s31, s28, s30\n"
        "v_cvt_pk_bf16_f32 v152, v96, v97\n"
        "v_cvt_pk_bf16_f32 v153, v98, v99\n"
        "v_mfma_f32_16x16x32_bf16 v[100:103], a[104:107], a[244:247], v[100:103]\n"
        "s_sub_u32 s45, s29, s28\n"
        "v_mul_f32 v187, v173, v187\n"
        "v_cvt_pk_bf16_f32 v184, v184, v185\n"
        "v_mfma_f32_16x16x32_bf16 v[100:103], a[100:103], a[240:243], v[100:103]\n"
        "v_cvt_pk_bf16_f32 v185, v186, v187\n"
        "v_mul_f32 v196, v193, v196\n"
        "v_mfma_f32_16x16x32_bf16 v[100:103], a[96:99], a[236:239], v[100:103]\n"
        "ds_write_b64 v246, v[184:185] offset:256\n"
        "v_mfma_f32_16x16x32_bf16 v[104:107], a[120:123], a[236:239], 0\n"
        "v_mul_f32 v197, v193, v197\n"
        "v_mul_f32 v198, v193, v198\n"
        "v_mfma_f32_16x16x32_bf16 v[104:107], a[124:127], a[240:243], v[104:107]\n"
        "v_mul_f32 v199, v193, v199\n"
        "v_cvt_pk_bf16_f32 v196, v196, v197\n"
        "v_mfma_f32_16x16x32_bf16 v[104:107], a[128:131], a[244:247], v[104:107]\n"
        "v_cvt_pk_bf16_f32 v154, v100, v101\n"
        "v_cvt_pk_bf16_f32 v155, v102, v103\n"
        "v_mfma_f32_16x16x32_bf16 v[104:107], a[132:135], a[248:251], v[104:107]\n"
        "v_cvt_pk_bf16_f32 v197, v198, v199\n"
        "v_mul_f32 v200, v193, v200\n"
        "v_mfma_f32_16x16x32_bf16 v[104:107], a[136:139], a[216:219], v[104:107]\n"
        "v_mul_f32 v201, v193, v201\n"
        "v_maximum3_f32 v253, v253, v192, v193\n"
        "v_mfma_f32_16x16x32_bf16 v[104:107], a[140:143], a[220:223], v[104:107]\n"
        "v_mul_f32 v202, v193, v202\n"
        "v_mul_f32 v203, v193, v203\n"
        "v_mfma_f32_16x16x32_bf16 v[96:99], a[20:23], a[220:223], 0\n"
        "v_cvt_pk_bf16_f32 v198, v200, v201\n"
        "v_cvt_pk_bf16_f32 v199, v202, v203\n"
        "v_mfma_f32_16x16x32_bf16 v[96:99], a[16:19], a[216:219], v[96:99]\n"
        "ds_write_b128 v245, v[196:199] offset:512\n"
        "v_mfma_f32_16x16x32_bf16 v[96:99], a[12:15], a[248:251], v[96:99]\n"
        "v_cvt_pk_bf16_f32 v168, v104, v105\n"
        "v_cvt_pk_bf16_f32 v169, v106, v107\n"
        "v_mfma_f32_16x16x32_bf16 v[96:99], a[8:11], a[244:247], v[96:99]\n"
        "v_mul_f32 v204, v193, v204\n"
        "v_mul_f32 v205, v193, v205\n"
        "v_mfma_f32_16x16x32_bf16 v[96:99], a[4:7], a[240:243], v[96:99]\n"
        "v_mul_f32 v206, v193, v206\n"
        "v_mul_f32 v207, v193, v207\n"
        "v_mfma_f32_16x16x32_bf16 v[96:99], a[0:3], a[236:239], v[96:99]\n"
        "v_cvt_pk_bf16_f32 v204, v204, v205\n"
        "v_cvt_pk_bf16_f32 v205, v206, v207\n"
        "v_mfma_f32_16x16x32_bf16 v[100:103], a[24:27], a[236:239], 0\n"
        "ds_write_b64 v246, v[204:205] offset:512\n"
        "v_mfma_f32_16x16x32_bf16 v[100:103], a[28:31], a[240:243], v[100:103]\n"
        "v_add_f32 v212, v212, v213\n"
        "v_xor_b32 v251, 0x2000, v251\n"
        "v_mfma_f32_16x16x32_bf16 v[100:103], a[32:35], a[244:247], v[100:103]\n"
        "v_mul_f32 v96, s50, v96\n"
        "v_mul_f32 v97, s50, v97\n"
        "v_mfma_f32_16x16x32_bf16 v[100:103], a[36:39], a[248:251], v[100:103]\n"
        "s_add_u32 m0, s65, 36864\n"
        "v_mul_f32 v98, s50, v98\n"
        "v_mul_f32 v99, s50, v99\n"
        "v_mfma_f32_16x16x32_bf16 v[100:103], a[40:43], a[216:219], v[100:103]\n"
        "s_add_u32 s64, s65, 36864\n"
        "v_cvt_pk_bf16_f32 v156, v96, v97\n"
        "v_cvt_pk_bf16_f32 v157, v98, v99\n"
        "v_mfma_f32_16x16x32_bf16 v[100:103], a[44:47], a[220:223], v[100:103]\n"
        "v_rcp_f32 v213, v212\n"
        "v_mfma_f32_16x16x32_bf16 v[104:107], a[68:71], a[220:223], 0\n"
        "buffer_load_dwordx4 v234, s[52:55], s64 offen lds\n"
        "v_mfma_f32_16x16x32_bf16 v[104:107], a[64:67], a[216:219], v[104:107]\n"
        "v_mul_f32 v216, v213, v216\n"
        "v_mul_f32 v217, v213, v217\n"
        "v_mfma_f32_16x16x32_bf16 v[104:107], a[60:63], a[248:251], v[104:107]\n"
        "ds_read_b128 a[224:227], v240 offset:1536\n"
        "v_mul_f32 v100, s50, v100\n"
        "v_mfma_f32_16x16x32_bf16 v[104:107], a[56:59], a[244:247], v[104:107]\n"
        "v_mul_f32 v101, s50, v101\n"
        "v_mul_f32 v102, s50, v102\n"
        "v_mfma_f32_16x16x32_bf16 v[104:107], a[52:55], a[240:243], v[104:107]\n"
        "buffer_load_dwordx4 v235, s[52:55], s64 offen offset:1024 lds\n"
        "v_mfma_f32_16x16x32_bf16 v[104:107], a[48:51], a[236:239], v[104:107]\n"
        "v_mul_f32 v103, s50, v103\n"
        "v_cvt_pk_bf16_f32 v158, v100, v101\n"
        "v_mfma_f32_16x16x32_bf16 v[96:99], a[236:239], a[144:147], 0\n"
        "ds_read_b128 a[228:231], v241 offset:1536\n"
        "v_cvt_pk_bf16_f32 v159, v102, v103\n"
        "v_mfma_f32_16x16x32_bf16 v[96:99], a[240:243], a[148:151], v[96:99]\n"
        "ds_read_b128 a[232:235], v240 offset:1664\n"
        "v_mul_f32 v218, v213, v218\n"
        "v_mfma_f32_16x16x32_bf16 v[96:99], a[244:247], a[152:155], v[96:99]\n"
        "buffer_load_dwordx4 v236, s[52:55], s64 offen offset:2048 lds\n"
        "v_mfma_f32_16x16x32_bf16 v[96:99], a[248:251], a[156:159], v[96:99]\n"
        "s_add_u32 m0, s65, 39936\n"
        "v_mul_f32 v104, s50, v104\n"
        "v_mul_f32 v105, s50, v105\n"
        "v_mfma_f32_16x16x32_bf16 v[96:99], a[216:219], a[160:163], v[96:99]\n"
        "s_add_u32 s64, s65, 39936\n"
        "v_mul_f32 v106, s50, v106\n"
        "v_mul_f32 v107, s50, v107\n"
        "v_mfma_f32_16x16x32_bf16 v[96:99], a[220:223], a[164:167], v[96:99]\n"
        "v_cvt_pk_bf16_f32 v160, v104, v105\n"
        "v_cvt_pk_bf16_f32 v161, v106, v107\n"
        "v_mfma_f32_16x16x32_bf16 v[100:103], a[220:223], a[188:191], 0\n"
        "buffer_load_dwordx4 v234, s[52:55], s64 offen lds\n"
        "v_mfma_f32_16x16x32_bf16 v[100:103], a[216:219], a[184:187], v[100:103]\n"
        "v_mov_b64 v[166:167], v[160:161]\n"
        "v_mul_f32 v219, v213, v219\n"
        "v_mfma_f32_16x16x32_bf16 v[100:103], a[248:251], a[180:183], v[100:103]\n"
        "v_cvt_pk_bf16_f32 v216, v216, v217\n"
        "v_cvt_pk_bf16_f32 v217, v218, v219\n"
        "v_mfma_f32_16x16x32_bf16 v[100:103], a[244:247], a[176:179], v[100:103]\n"
        "v_cvt_pk_bf16_f32 v128, v96, v97\n"
        "v_cvt_pk_bf16_f32 v129, v98, v99\n"
        "v_mfma_f32_16x16x32_bf16 v[100:103], a[240:243], a[172:175], v[100:103]\n"
        "buffer_load_dwordx4 v235, s[52:55], s64 offen offset:1024 lds\n"
        "v_mfma_f32_16x16x32_bf16 v[100:103], a[236:239], a[168:171], v[100:103]\n"
        "v_mul_f32 v220, v213, v220\n"
        "v_mul_f32 v221, v213, v221\n"
        "v_mfma_f32_16x16x32_bf16 v[104:107], a[236:239], a[192:195], 0\n"
        "ds_read_b128 a[236:239], v241 offset:1664\n"
        "v_maximum3_f32 v253, v253, v212, v213\n"
        "v_mfma_f32_16x16x32_bf16 v[104:107], a[240:243], a[196:199], v[104:107]\n"
        "ds_read_b128 a[240:243], v240 offset:1792\n"
        "v_mul_f32 v222, v213, v222\n"
        "v_mfma_f32_16x16x32_bf16 v[104:107], a[244:247], a[200:203], v[104:107]\n"
        "ds_read_b128 a[244:247], v241 offset:1792\n"
        "v_mul_f32 v223, v213, v223\n"
        "v_mfma_f32_16x16x32_bf16 v[104:107], a[248:251], a[204:207], v[104:107]\n"
        "buffer_load_dwordx4 v236, s[52:55], s64 offen offset:2048 lds\n"
        "v_mfma_f32_16x16x32_bf16 v[104:107], a[216:219], a[208:211], v[104:107]\n"
        "s_add_u32 m0, s65, 43008\n"
        "v_cvt_pk_bf16_f32 v218, v220, v221\n"
        "v_cvt_pk_bf16_f32 v219, v222, v223\n"
        "v_mfma_f32_16x16x32_bf16 v[104:107], a[220:223], a[212:215], v[104:107]\n"
        "s_waitcnt lgkmcnt(5)\n"
        "s_add_u32 s64, s65, 43008\n"
        "v_xor_b32 v252, 0x2000, v252\n"
        "v_mul_f32 v224, v213, v224\n"
        "v_mfma_f32_16x16x32_bf16 v[96:99], a[72:75], a[224:227], 0\n"
        "s_waitcnt lgkmcnt(4)\n"
        "buffer_load_dwordx4 v234, s[52:55], s64 offen lds\n"
        "v_mfma_f32_16x16x32_bf16 v[96:99], a[76:79], a[228:231], v[96:99]\n"
        "s_waitcnt lgkmcnt(3)\n"
        "ds_write_b128 v245, v[216:219] offset:768\n"
        "v_mfma_f32_16x16x32_bf16 v[96:99], a[80:83], a[232:235], v[96:99]\n"
        "s_waitcnt lgkmcnt(3)\n"
        "v_mul_f32 v225, v213, v225\n"
        "v_cvt_pk_bf16_f32 v136, v100, v101\n"
        "v_mfma_f32_16x16x32_bf16 v[96:99], a[84:87], a[236:239], v[96:99]\n"
        "s_waitcnt lgkmcnt(2)\n"
        "v_cvt_pk_bf16_f32 v137, v102, v103\n"
        "v_mul_f32 v226, v213, v226\n"
        "v_mfma_f32_16x16x32_bf16 v[96:99], a[88:91], a[240:243], v[96:99]\n"
        "s_waitcnt lgkmcnt(1)\n"
        "buffer_load_dwordx4 v235, s[52:55], s64 offen offset:1024 lds\n"
        "v_mfma_f32_16x16x32_bf16 v[96:99], a[92:95], a[244:247], v[96:99]\n"
        "v_mul_f32 v227, v213, v227\n"
        "v_cvt_pk_bf16_f32 v224, v224, v225\n"
        "v_mfma_f32_16x16x32_bf16 v[100:103], a[116:119], a[244:247], 0\n"
        "s_waitcnt vmcnt(8)\n"
        "s_barrier\n"
        "v_cvt_pk_bf16_f32 v225, v226, v227\n"
        "v_cvt_pk_bf16_f32 v144, v104, v105\n"
        "v_mfma_f32_16x16x32_bf16 v[100:103], a[112:115], a[240:243], v[100:103]\n"
        "ds_write_b64 v246, v[224:225] offset:768\n"
        "v_mfma_f32_16x16x32_bf16 v[100:103], a[108:111], a[236:239], v[100:103]\n"
        "buffer_load_dwordx4 v236, s[52:55], s64 offen offset:2048 lds\n"
        "v_mfma_f32_16x16x32_bf16 v[100:103], a[104:107], a[232:235], v[100:103]\n"
        "s_add_u32 m0, s65, 0\n"
        "v_cvt_pk_bf16_f32 v145, v106, v107\n"
        "v_cvt_pk_bf16_f32 v172, v96, v97\n"
        "v_mfma_f32_16x16x32_bf16 v[100:103], a[100:103], a[228:231], v[100:103]\n"
        "s_add_u32 s64, s65, 0\n"
        "ds_read_b128 v[162:165], v251 offset:0\n"
        "v_cvt_pk_bf16_f32 v173, v98, v99\n"
        "v_mfma_f32_16x16x32_bf16 v[100:103], a[96:99], a[224:227], v[100:103]\n"
        "v_add_u32 v245, s31, v245\n"
        "v_add_u32 v246, s31, v246\n"
        "v_mfma_f32_16x16x32_bf16 v[104:107], a[120:123], a[224:227], 0\n"
        "buffer_load_dwordx4 v234, s[52:55], s64 offen lds\n"
        "v_mfma_f32_16x16x32_bf16 v[104:107], a[124:127], a[228:231], v[104:107]\n"
        "v_mfma_f32_16x16x32_bf16 v[104:107], a[128:131], a[232:235], v[104:107]\n"
        "v_mfma_f32_16x16x32_bf16 v[104:107], a[132:135], a[236:239], v[104:107]\n"
        "v_mfma_f32_16x16x32_bf16 v[104:107], a[136:139], a[240:243], v[104:107]\n"
        "buffer_load_dwordx4 v235, s[52:55], s64 offen offset:1024 lds\n"
        "v_mfma_f32_16x16x32_bf16 v[104:107], a[140:143], a[244:247], v[104:107]\n"
        "v_cvt_pk_bf16_f32 v174, v100, v101\n"
        "v_cvt_pk_bf16_f32 v175, v102, v103\n"
        "v_mfma_f32_16x16x32_bf16 v[96:99], a[20:23], a[244:247], 0\n"
        "v_mfma_f32_16x16x32_bf16 v[96:99], a[16:19], a[240:243], v[96:99]\n"
        "v_mfma_f32_16x16x32_bf16 v[96:99], a[12:15], a[236:239], v[96:99]\n"
        "buffer_load_dwordx4 v236, s[52:55], s64 offen offset:2048 lds\n"
        "v_mfma_f32_16x16x32_bf16 v[96:99], a[8:11], a[232:235], v[96:99]\n"
        "s_add_u32 m0, s65, 3072\n"
        "v_cvt_pk_bf16_f32 v190, v104, v105\n"
        "v_cvt_pk_bf16_f32 v191, v106, v107\n"
        "v_mfma_f32_16x16x32_bf16 v[96:99], a[4:7], a[228:231], v[96:99]\n"
        "s_add_u32 s64, s65, 3072\n"
        "ds_read_b128 v[182:185], v251 offset:512\n"
        "v_mfma_f32_16x16x32_bf16 v[96:99], a[0:3], a[224:227], v[96:99]\n"
        "v_mfma_f32_16x16x32_bf16 v[64:67], v[152:155], v[156:159], v[0:3]\n"
        "s_waitcnt lgkmcnt(1)\n"
        "buffer_load_dwordx4 v234, s[52:55], s64 offen lds\n"
        "v_mfma_f32_16x16x32_bf16 v[64:67], v[168:171], v[160:163], v[64:67]\n"
        "v_mfma_f32_16x16x32_bf16 v[68:71], v[172:175], v[156:159], v[4:7]\n"
        "v_mfma_f32_16x16x32_bf16 v[68:71], v[188:191], v[164:167], v[68:71]\n"
        "v_mfma_f32_16x16x32_bf16 v[100:103], a[24:27], a[224:227], 0\n"
        "buffer_load_dwordx4 v235, s[52:55], s64 offen offset:1024 lds\n"
        "v_mfma_f32_16x16x32_bf16 v[100:103], a[28:31], a[228:231], v[100:103]\n"
        "v_mul_f32 v96, s50, v96\n"
        "v_mul_f32 v97, s50, v97\n"
        "v_mfma_f32_16x16x32_bf16 v[100:103], a[32:35], a[232:235], v[100:103]\n"
        "v_mul_f32 v98, s50, v98\n"
        "v_mul_f32 v99, s50, v99\n"
        "v_mfma_f32_16x16x32_bf16 v[100:103], a[36:39], a[236:239], v[100:103]\n"
        "v_cvt_pk_bf16_f32 v176, v96, v97\n"
        "v_cvt_pk_bf16_f32 v177, v98, v99\n"
        "v_mfma_f32_16x16x32_bf16 v[100:103], a[40:43], a[240:243], v[100:103]\n"
        "buffer_load_dwordx4 v236, s[52:55], s64 offen offset:2048 lds\n"
        "v_mfma_f32_16x16x32_bf16 v[100:103], a[44:47], a[244:247], v[100:103]\n"
        "s_add_u32 m0, s65, 6144\n"
        "v_exp_f32 v64, v64\n"
        "v_mfma_f32_16x16x32_bf16 v[104:107], a[68:71], a[244:247], 0\n"
        "s_add_u32 s64, s65, 6144\n"
        "v_exp_f32 v65, v65\n"
        "v_mfma_f32_16x16x32_bf16 v[104:107], a[64:67], a[240:243], v[104:107]\n"
        "v_exp_f32 v66, v66\n"
        "v_mfma_f32_16x16x32_bf16 v[104:107], a[60:63], a[236:239], v[104:107]\n"
        "buffer_load_dwordx4 v234, s[52:55], s64 offen lds\n"
        "v_mfma_f32_16x16x32_bf16 v[104:107], a[56:59], a[232:235], v[104:107]\n"
        "v_mul_f32 v100, s50, v100\n"
        "v_mul_f32 v101, s50, v101\n"
        "v_mfma_f32_16x16x32_bf16 v[104:107], a[52:55], a[228:231], v[104:107]\n"
        "s_waitcnt vmcnt(7)\n"
        "s_barrier\n"
        "v_mul_f32 v102, s50, v102\n"
        "v_mul_f32 v103, s50, v103\n"
        "v_mfma_f32_16x16x32_bf16 v[104:107], a[48:51], a[224:227], v[104:107]\n"
        "ds_read_b64_tr_b16 v[88:89], v124 offset:36864\n"
        "v_cvt_pk_bf16_f32 v178, v100, v101\n"
        "v_mfma_f32_16x16x32_bf16 v[96:99], a[224:227], a[144:147], 0\n"
        "buffer_load_dwordx4 v235, s[52:55], s64 offen offset:1024 lds\n"
        "v_mfma_f32_16x16x32_bf16 v[96:99], a[228:231], a[148:151], v[96:99]\n"
        "ds_read_b64_tr_b16 v[92:93], v124 offset:55296\n"
        "ds_read_b64_tr_b16 v[108:109], v127 offset:49152\n"
        "v_mfma_f32_16x16x32_bf16 v[96:99], a[232:235], a[152:155], v[96:99]\n"
        "ds_read_b64_tr_b16 v[112:113], v125 offset:36864\n"
        "v_cvt_pk_bf16_f32 v179, v102, v103\n"
        "v_mfma_f32_16x16x32_bf16 v[96:99], a[236:239], a[156:159], v[96:99]\n"
        "ds_read_b64_tr_b16 v[120:121], v125 offset:55296\n"
        "v_xor_b32 v127, 64, v127\n"
        "v_mfma_f32_16x16x32_bf16 v[96:99], a[240:243], a[160:163], v[96:99]\n"
        "buffer_load_dwordx4 v236, s[52:55], s64 offen offset:2048 lds\n"
        "v_mfma_f32_16x16x32_bf16 v[96:99], a[244:247], a[164:167], v[96:99]\n"
        "s_add_u32 s41, s29, s40\n"
        "s_add_u32 s35, s47, 786432\n"
        "s_sub_u32 s46, s35, 100663296\n"
        "s_cmp_lt_u32 s46, 786432\n"
        "s_mov_b32 s29, s30\n"
        "s_mov_b32 s30, s28\n"
        "v_mfma_f32_16x16x32_bf16 v[72:75], v[152:155], v[176:179], v[16:19]\n"
        "s_waitcnt lgkmcnt(5)\n"
        "s_cselect_b32 s35, s46, s35\n"
        "s_add_u32 s28, s28, s45\n"
        "ds_read_b64_tr_b16 v[212:213], v232 offset:49152\n"
        "v_mul_f32 v104, s50, v104\n"
        "v_mul_f32 v105, s50, v105\n"
        "v_mul_f32 v106, s50, v106\n"
        "v_mul_f32 v107, s50, v107\n"
        "v_cvt_pk_bf16_f32 v180, v104, v105\n"
        "v_cvt_pk_bf16_f32 v181, v106, v107\n"
        "s_nop 1\n"
        "v_mfma_f32_16x16x32_bf16 v[72:75], v[168:171], v[180:183], v[72:75]\n"
        "s_add_u32 m0, s41, 0\n"
        "ds_read_b64_tr_b16 v[216:217], v126 offset:36864\n"
        "ds_read_b64_tr_b16 v[220:221], v126 offset:55296\n"
        "v_mfma_f32_16x16x32_bf16 v[76:79], v[172:175], v[176:179], v[20:23]\n"
        "ds_read_b64_tr_b16 v[224:225], v233 offset:49152\n"
        "ds_read_b64_tr_b16 v[90:91], v124 offset:43008\n"
        "v_mov_b64 v[186:187], v[180:181]\n"
        "s_nop 1\n"
        "v_mfma_f32_16x16x32_bf16 v[76:79], v[188:191], v[184:187], v[76:79]\n"
        "buffer_load_dwordx4 v242, s[8:11], s47 offen lds\n"
        "v_mfma_f32_16x16x32_bf16 v[100:103], a[244:247], a[188:191], 0\n"
        "ds_read_b64_tr_b16 v[94:95], v124 offset:61440\n"
        "ds_read_b64_tr_b16 v[110:111], v127 offset:50688\n"
        "v_mfma_f32_16x16x32_bf16 v[100:103], a[240:243], a[184:187], v[100:103]\n"
        "s_add_u32 m0, s41, 1024\n"
        "ds_read_b64_tr_b16 v[114:115], v125 offset:43008\n"
        "v_xor_b32 v232, 64, v232\n"
        "v_mfma_f32_16x16x32_bf16 v[100:103], a[236:239], a[180:183], v[100:103]\n"
        "ds_read_b64_tr_b16 v[122:123], v125 offset:61440\n"
        "ds_read_b128 a[248:251], v240 offset:12288\n"
        "v_mfma_f32_16x16x32_bf16 v[100:103], a[232:235], a[176:179], v[100:103]\n"
        "ds_read_b64_tr_b16 v[214:215], v232 offset:50688\n"
        "ds_read_b128 a[216:219], v241 offset:12288\n"
        "v_mfma_f32_16x16x32_bf16 v[100:103], a[228:231], a[172:175], v[100:103]\n"
        "buffer_load_dwordx4 v243, s[8:11], s47 offen lds\n"
        "v_mfma_f32_16x16x32_bf16 v[100:103], a[224:227], a[168:171], v[100:103]\n"
        "ds_read_b64_tr_b16 v[218:219], v126 offset:43008\n"
        "ds_read_b128 a[220:223], v240 offset:12416\n"
        "v_mfma_f32_16x16x32_bf16 v[104:107], a[224:227], a[192:195], 0\n"
        "s_add_u32 m0, s41, 2048\n"
        "ds_read_b64_tr_b16 v[222:223], v126 offset:61440\n"
        "ds_read_b128 a[224:227], v241 offset:12416\n"
        "v_mfma_f32_16x16x32_bf16 v[104:107], a[228:231], a[196:199], v[104:107]\n"
        "ds_read_b128 a[228:231], v240 offset:12544\n"
        "v_xor_b32 v233, 64, v233\n"
        "v_mfma_f32_16x16x32_bf16 v[104:107], a[232:235], a[200:203], v[104:107]\n"
        "ds_read_b64_tr_b16 v[226:227], v233 offset:50688\n"
        "ds_read_b128 a[232:235], v241 offset:12544\n"
        "v_mfma_f32_16x16x32_bf16 v[104:107], a[236:239], a[204:207], v[104:107]\n"
        "buffer_load_dwordx4 v244, s[8:11], s47 offen lds\n"
        "v_mfma_f32_16x16x32_bf16 v[104:107], a[240:243], a[208:211], v[104:107]\n"
        "v_cvt_pk_bf16_f32 v130, v96, v97\n"
        "v_cvt_pk_bf16_f32 v131, v98, v99\n"
        "v_mfma_f32_16x16x32_bf16 v[104:107], a[244:247], a[212:215], v[104:107]\n"
        "s_waitcnt lgkmcnt(9)\n"
        "s_add_u32 m0, s41, 12288\n"
        "v_cvt_pk_bf16_f32 v138, v100, v101\n"
        "v_cvt_pk_bf16_f32 v139, v102, v103\n"
        "v_mfma_f32_16x16x32_bf16 v[96:99], a[72:75], a[248:251], 0\n"
        "s_waitcnt lgkmcnt(7)\n"
        "ds_read_b128 v[162:165], v252 offset:0\n"
        "v_cvt_pk_bf16_f32 v64, v64, v65\n"
        "v_mfma_f32_16x16x32_bf16 v[96:99], a[76:79], a[216:219], v[96:99]\n"
        "s_waitcnt lgkmcnt(6)\n"
        "buffer_load_dwordx4 v242, s[8:11], s35 offen lds\n"
        "v_mfma_f32_16x16x32_bf16 v[96:99], a[80:83], a[220:223], v[96:99]\n"
        "s_waitcnt lgkmcnt(2)\n"
        "s_barrier\n"
        "ds_write_b128 v237, v[88:91] offset:36864\n"
        "v_mfma_f32_16x16x32_bf16 v[96:99], a[84:87], a[224:227], v[96:99]\n"
        "v_cvt_pk_bf16_f32 v146, v104, v105\n"
        "v_cvt_pk_bf16_f32 v147, v106, v107\n"
        "v_mfma_f32_16x16x32_bf16 v[96:99], a[88:91], a[228:231], v[96:99]\n"
        "s_waitcnt lgkmcnt(2)\n"
        "s_add_u32 m0, s41, 13312\n"
        "v_exp_f32 v67, v67\n"
        "v_mfma_f32_16x16x32_bf16 v[96:99], a[92:95], a[232:235], v[96:99]\n"
        "ds_write_b128 v237, v[92:95] offset:43008\n"
        "v_mfma_f32_16x16x32_bf16 v[100:103], a[116:119], a[232:235], 0\n"
        "buffer_load_dwordx4 v243, s[8:11], s35 offen lds\n"
        "v_mfma_f32_16x16x32_bf16 v[100:103], a[112:115], a[228:231], v[100:103]\n"
        "ds_read_b128 v[182:185], v252 offset:512\n"
        "v_cvt_pk_bf16_f32 v65, v66, v67\n"
        "v_mfma_f32_16x16x32_bf16 v[100:103], a[108:111], a[224:227], v[100:103]\n"
        "ds_write_b128 v237, v[108:111] offset:49152\n"
        "v_mfma_f32_16x16x32_bf16 v[100:103], a[104:107], a[220:223], v[100:103]\n"
        "s_add_u32 m0, s41, 14336\n"
        "v_exp_f32 v68, v68\n"
        "v_mfma_f32_16x16x32_bf16 v[100:103], a[100:103], a[216:219], v[100:103]\n"
        "v_cvt_pk_bf16_f32 v192, v96, v97\n"
        "v_cvt_pk_bf16_f32 v193, v98, v99\n"
        "v_mfma_f32_16x16x32_bf16 v[100:103], a[96:99], a[248:251], v[100:103]\n"
        "buffer_load_dwordx4 v244, s[8:11], s35 offen lds\n"
        "v_mfma_f32_16x16x32_bf16 v[104:107], a[120:123], a[248:251], 0\n"
        "ds_write_b128 v238, v[112:115] offset:36864\n"
        "v_mfma_f32_16x16x32_bf16 v[104:107], a[124:127], a[216:219], v[104:107]\n"
        "v_exp_f32 v69, v69\n"
        "v_mfma_f32_16x16x32_bf16 v[104:107], a[128:131], a[220:223], v[104:107]\n"
        "s_mov_b32 m0, s48\n"
        "v_exp_f32 v70, v70\n"
        "v_mfma_f32_16x16x32_bf16 v[104:107], a[132:135], a[224:227], v[104:107]\n"
        "ds_write_b128 v238, v[120:123] offset:43008\n"
        "v_mfma_f32_16x16x32_bf16 v[104:107], a[136:139], a[228:231], v[104:107]\n"
        "buffer_load_dwordx4 v249, s[12:15], s36 offen offset:0 lds\n"
        "v_mfma_f32_16x16x32_bf16 v[104:107], a[140:143], a[232:235], v[104:107]\n"
        "v_cvt_pk_bf16_f32 v66, v68, v69\n"
        "v_cvt_pk_bf16_f32 v194, v100, v101\n"
        "v_mfma_f32_16x16x32_bf16 v[96:99], a[20:23], a[232:235], 0\n"
        "ds_write_b128 v238, v[212:215] offset:49152\n"
        "v_mfma_f32_16x16x32_bf16 v[96:99], a[16:19], a[228:231], v[96:99]\n"
        "v_exp_f32 v71, v71\n"
        "v_mfma_f32_16x16x32_bf16 v[96:99], a[12:15], a[224:227], v[96:99]\n"
        "v_cvt_pk_bf16_f32 v67, v70, v71\n"
        "v_cvt_pk_bf16_f32 v195, v102, v103\n"
        "v_mfma_f32_16x16x32_bf16 v[96:99], a[8:11], a[220:223], v[96:99]\n"
        "ds_write_b128 v239, v[216:219] offset:36864\n"
        "v_mfma_f32_16x16x32_bf16 v[96:99], a[4:7], a[216:219], v[96:99]\n"
        "v_exp_f32 v72, v72\n"
        "v_mfma_f32_16x16x32_bf16 v[96:99], a[0:3], a[248:251], v[96:99]\n"
        "v_exp_f32 v73, v73\n"
        "v_mfma_f32_16x16x32_bf16 v[68:71], v[192:195], v[156:159], v[8:11]\n"
        "s_waitcnt lgkmcnt(8)\n"
        "s_waitcnt vmcnt(7)\n"
        "s_barrier\n"
        "ds_write_b128 v239, v[220:223] offset:43008\n"
        "v_cvt_pk_bf16_f32 v208, v104, v105\n"
        "v_cvt_pk_bf16_f32 v209, v106, v107\n"
        "s_nop 1\n"
        "v_mfma_f32_16x16x32_bf16 v[68:71], v[208:211], v[160:163], v[68:71]\n"
        "ds_read_b64_tr_b16 v[90:91], v124 offset:6144\n"
        "v_cvt_pk_bf16_f32 v72, v72, v73\n"
        "v_exp_f32 v76, v76\n"
        "v_exp_f32 v74, v74\n"
        "v_exp_f32 v75, v75\n"
        "v_exp_f32 v77, v77\n"
        "v_cvt_pk_bf16_f32 v73, v74, v75\n"
        "v_exp_f32 v78, v78\n"
        "v_cvt_pk_bf16_f32 v74, v76, v77\n"
        "v_exp_f32 v79, v79\n"
        "v_xor_b32 v116, 32, v249\n"
        "v_cvt_pk_bf16_f32 v75, v78, v79\n"
        "v_mfma_f32_16x16x32_bf16 v[76:79], v[192:195], v[176:179], v[24:27]\n"
        "s_waitcnt lgkmcnt(7)\n"
        "buffer_load_dwordx4 v116, s[12:15], s36 offen offset:1024 lds\n"
        "v_mfma_f32_16x16x32_bf16 v[76:79], v[208:211], v[180:183], v[76:79]\n"
        "ds_read_b64_tr_b16 v[94:95], v124 offset:24576\n"
        "ds_read_b64_tr_b16 v[110:111], v127 offset:13824\n"
        "v_mfma_f32_16x16x32_bf16 v[100:103], a[24:27], a[248:251], 0\n"
        "ds_read_b64_tr_b16 v[114:115], v125 offset:6144\n"
        "v_mul_f32 v96, s50, v96\n"
        "v_mfma_f32_16x16x32_bf16 v[100:103], a[28:31], a[216:219], v[100:103]\n"
        "ds_read_b64_tr_b16 v[122:123], v125 offset:24576\n"
        "v_mul_f32 v97, s50, v97\n"
        "v_mfma_f32_16x16x32_bf16 v[100:103], a[32:35], a[220:223], v[100:103]\n"
        "ds_read_b64_tr_b16 v[214:215], v232 offset:13824\n"
        "v_xor_b32 v127, 64, v127\n"
        "v_mfma_f32_16x16x32_bf16 v[100:103], a[36:39], a[224:227], v[100:103]\n"
        "ds_read_b64_tr_b16 v[218:219], v126 offset:6144\n"
        "v_mul_f32 v98, s50, v98\n"
        "v_mfma_f32_16x16x32_bf16 v[100:103], a[40:43], a[228:231], v[100:103]\n"
        "ds_read_b64_tr_b16 v[222:223], v126 offset:24576\n"
        "v_mul_f32 v99, s50, v99\n"
        "v_mfma_f32_16x16x32_bf16 v[100:103], a[44:47], a[232:235], v[100:103]\n"
        "ds_write_b128 v239, v[224:227] offset:49152\n"
        "v_mfma_f32_16x16x32_bf16 v[104:107], a[68:71], a[232:235], 0\n"
        "ds_read_b64_tr_b16 v[88:89], v124 offset:0\n"
        "ds_read_b64_tr_b16 v[226:227], v233 offset:13824\n"
        "v_mfma_f32_16x16x32_bf16 v[104:107], a[64:67], a[228:231], v[104:107]\n"
        "ds_read_b64_tr_b16 v[92:93], v124 offset:18432\n"
        "v_cvt_pk_bf16_f32 v196, v96, v97\n"
        "v_mfma_f32_16x16x32_bf16 v[104:107], a[60:63], a[224:227], v[104:107]\n"
        "ds_read_b64_tr_b16 v[108:109], v127 offset:12288\n"
        "ds_read_b128 v[202:205], v251 offset:4096\n"
        "v_mfma_f32_16x16x32_bf16 v[104:107], a[56:59], a[220:223], v[104:107]\n"
        "ds_read_b64_tr_b16 v[112:113], v125 offset:0\n"
        "v_cvt_pk_bf16_f32 v197, v98, v99\n"
        "v_mfma_f32_16x16x32_bf16 v[104:107], a[52:55], a[216:219], v[104:107]\n"
        "ds_read_b64_tr_b16 v[120:121], v125 offset:18432\n"
        "v_xor_b32 v232, 64, v232\n"
        "v_mfma_f32_16x16x32_bf16 v[104:107], a[48:51], a[248:251], v[104:107]\n"
        "ds_read_b64_tr_b16 v[212:213], v232 offset:12288\n"
        "v_xor_b32 v233, 64, v233\n"
        "v_mfma_f32_16x16x32_bf16 v[96:99], a[248:251], a[144:147], 0\n"
        "ds_read_b64_tr_b16 v[216:217], v126 offset:0\n"
        "v_mul_f32 v100, s50, v100\n"
        "v_mfma_f32_16x16x32_bf16 v[96:99], a[216:219], a[148:151], v[96:99]\n"
        "ds_read_b64_tr_b16 v[220:221], v126 offset:18432\n"
        "v_mul_f32 v101, s50, v101\n"
        "v_mfma_f32_16x16x32_bf16 v[96:99], a[220:223], a[152:155], v[96:99]\n"
        "ds_read_b64_tr_b16 v[224:225], v233 offset:12288\n"
        "v_mul_f32 v102, s50, v102\n"
        "v_mfma_f32_16x16x32_bf16 v[96:99], a[224:227], a[156:159], v[96:99]\n"
        "v_mul_f32 v103, s50, v103\n"
        "v_cvt_pk_bf16_f32 v198, v100, v101\n"
        "v_mfma_f32_16x16x32_bf16 v[96:99], a[228:231], a[160:163], v[96:99]\n"
        "v_cvt_pk_bf16_f32 v199, v102, v103\n"
        "v_mul_f32 v104, s50, v104\n"
        "v_mfma_f32_16x16x32_bf16 v[96:99], a[232:235], a[164:167], v[96:99]\n"
        "v_mul_f32 v105, s50, v105\n"
        "v_mul_f32 v106, s50, v106\n"
        "v_mfma_f32_16x16x32_bf16 v[80:83], v[152:155], v[196:199], v[32:35]\n"
        "s_waitcnt lgkmcnt(6)\n"
        "v_mul_f32 v107, s50, v107\n"
        "v_cvt_pk_bf16_f32 v200, v104, v105\n"
        "v_cvt_pk_bf16_f32 v201, v106, v107\n"
        "s_nop 1\n"
        "v_mfma_f32_16x16x32_bf16 v[80:83], v[168:171], v[200:203], v[80:83]\n"
        "ds_read_b128 a[236:239], v240 offset:13824\n"
        "v_mov_b64 v[206:207], v[200:201]\n"
        "v_mfma_f32_16x16x32_bf16 v[84:87], v[172:175], v[196:199], v[36:39]\n"
        "v_exp_f32 v68, v68\n"
        "v_mfma_f32_16x16x32_bf16 v[84:87], v[188:191], v[204:207], v[84:87]\n"
        "v_exp_f32 v69, v69\n"
        "v_mfma_f32_16x16x32_bf16 v[100:103], a[232:235], a[188:191], 0\n"
        "s_waitcnt lgkmcnt(1)\n"
        "s_barrier\n"
        "v_exp_f32 v70, v70\n"
        "v_mfma_f32_16x16x32_bf16 v[100:103], a[228:231], a[184:187], v[100:103]\n"
        "v_cvt_pk_bf16_f32 v68, v68, v69\n"
        "v_cvt_pk_bf16_f32 v132, v96, v97\n"
        "v_mfma_f32_16x16x32_bf16 v[100:103], a[224:227], a[180:183], v[100:103]\n"
        "v_exp_f32 v71, v71\n"
        "v_mfma_f32_16x16x32_bf16 v[100:103], a[220:223], a[176:179], v[100:103]\n"
        "ds_read_b128 a[240:243], v241 offset:13824\n"
        "v_cvt_pk_bf16_f32 v133, v98, v99\n"
        "v_mfma_f32_16x16x32_bf16 v[100:103], a[216:219], a[172:175], v[100:103]\n"
        "ds_read_b128 a[244:247], v240 offset:13952\n"
        "v_cvt_pk_bf16_f32 v69, v70, v71\n"
        "v_mfma_f32_16x16x32_bf16 v[100:103], a[248:251], a[168:171], v[100:103]\n"
        "ds_write_b128 v237, v[88:91] offset:0\n"
        "v_mfma_f32_16x16x32_bf16 v[104:107], a[248:251], a[192:195], 0\n"
        "ds_read_b128 a[248:251], v241 offset:13952\n"
        "v_mfma_f32_16x16x32_bf16 v[104:107], a[216:219], a[196:199], v[104:107]\n"
        "ds_read_b128 a[216:219], v240 offset:14080\n"
        "v_mfma_f32_16x16x32_bf16 v[104:107], a[220:223], a[200:203], v[104:107]\n"
        "ds_read_b128 a[220:223], v241 offset:14080\n"
        "ds_read_b128 v[202:205], v252 offset:4096\n"
        "v_mfma_f32_16x16x32_bf16 v[104:107], a[224:227], a[204:207], v[104:107]\n"
        "v_exp_f32 v76, v76\n"
        "v_mfma_f32_16x16x32_bf16 v[104:107], a[228:231], a[208:211], v[104:107]\n"
        "ds_write_b128 v237, v[92:95] offset:6144\n"
        "v_mfma_f32_16x16x32_bf16 v[104:107], a[232:235], a[212:215], v[104:107]\n"
        "s_waitcnt lgkmcnt(8)\n"
        "v_exp_f32 v77, v77\n"
        "v_mfma_f32_16x16x32_bf16 v[96:99], a[72:75], a[236:239], 0\n"
        "s_waitcnt lgkmcnt(7)\n"
        "v_exp_f32 v78, v78\n"
        "v_mfma_f32_16x16x32_bf16 v[96:99], a[76:79], a[240:243], v[96:99]\n"
        "s_waitcnt lgkmcnt(6)\n"
        "v_cvt_pk_bf16_f32 v76, v76, v77\n"
        "v_cvt_pk_bf16_f32 v140, v100, v101\n"
        "v_mfma_f32_16x16x32_bf16 v[96:99], a[80:83], a[244:247], v[96:99]\n"
        "s_waitcnt lgkmcnt(4)\n"
        "ds_write_b128 v237, v[108:111] offset:12288\n"
        "v_mfma_f32_16x16x32_bf16 v[96:99], a[84:87], a[248:251], v[96:99]\n"
        "s_waitcnt lgkmcnt(4)\n"
        "v_exp_f32 v79, v79\n"
        "v_mfma_f32_16x16x32_bf16 v[96:99], a[88:91], a[216:219], v[96:99]\n"
        "s_waitcnt lgkmcnt(3)\n"
        "v_cvt_pk_bf16_f32 v77, v78, v79\n"
        "v_cvt_pk_bf16_f32 v141, v102, v103\n"
        "v_mfma_f32_16x16x32_bf16 v[96:99], a[92:95], a[220:223], v[96:99]\n"
        "ds_write_b128 v238, v[112:115] offset:0\n"
        "v_exp_f32 v84, v84\n"
        "v_exp_f32 v82, v82\n"
        "v_exp_f32 v81, v81\n"
        "v_exp_f32 v80, v80\n"
        "v_exp_f32 v83, v83\n"
        "v_cvt_pk_bf16_f32 v80, v80, v81\n"
        "v_cvt_pk_bf16_f32 v81, v82, v83\n"
        "v_exp_f32 v85, v85\n"
        "v_exp_f32 v86, v86\n"
        "v_cvt_pk_bf16_f32 v82, v84, v85\n"
        "v_exp_f32 v87, v87\n"
        "v_cvt_pk_bf16_f32 v148, v104, v105\n"
        "v_cvt_pk_bf16_f32 v83, v86, v87\n"
        "v_mfma_f32_16x16x32_bf16 v[84:87], v[192:195], v[196:199], v[40:43]\n"
        "s_waitcnt lgkmcnt(3)\n"
        "ds_write_b128 v238, v[120:123] offset:6144\n"
        "v_mfma_f32_16x16x32_bf16 v[84:87], v[208:211], v[200:203], v[84:87]\n"
        "ds_write_b128 v238, v[212:215] offset:12288\n"
        "v_mfma_f32_16x16x32_bf16 v[100:103], a[116:119], a[220:223], 0\n"
        "ds_write_b128 v239, v[216:219] offset:0\n"
        "v_mfma_f32_16x16x32_bf16 v[100:103], a[112:115], a[216:219], v[100:103]\n"
        "ds_write_b128 v239, v[220:223] offset:6144\n"
        "v_mfma_f32_16x16x32_bf16 v[100:103], a[108:111], a[248:251], v[100:103]\n"
        "v_cvt_pk_bf16_f32 v149, v106, v107\n"
        "v_cvt_pk_bf16_f32 v212, v96, v97\n"
        "v_mfma_f32_16x16x32_bf16 v[100:103], a[104:107], a[244:247], v[100:103]\n"
        "ds_write_b128 v239, v[224:227] offset:12288\n"
        "v_mfma_f32_16x16x32_bf16 v[100:103], a[100:103], a[240:243], v[100:103]\n"
        "v_cvt_pk_bf16_f32 v213, v98, v99\n"
        "v_add_u32 v240, s45, v240\n"
        "v_mfma_f32_16x16x32_bf16 v[100:103], a[96:99], a[236:239], v[100:103]\n"
        "v_exp_f32 v84, v84\n"
        "v_mfma_f32_16x16x32_bf16 v[104:107], a[120:123], a[236:239], 0\n"
        "v_exp_f32 v85, v85\n"
        "v_mfma_f32_16x16x32_bf16 v[104:107], a[124:127], a[240:243], v[104:107]\n"
        "v_exp_f32 v86, v86\n"
        "v_mfma_f32_16x16x32_bf16 v[104:107], a[128:131], a[244:247], v[104:107]\n"
        "v_exp_f32 v87, v87\n"
        "v_mfma_f32_16x16x32_bf16 v[104:107], a[132:135], a[248:251], v[104:107]\n"
        "v_cvt_pk_bf16_f32 v214, v100, v101\n"
        "v_cvt_pk_bf16_f32 v215, v102, v103\n"
        "v_mfma_f32_16x16x32_bf16 v[104:107], a[136:139], a[216:219], v[104:107]\n"
        "s_waitcnt lgkmcnt(0)\n"
        "s_barrier\n"
        "v_add_u32 v241, s45, v241\n"
        "v_cvt_pk_bf16_f32 v84, v84, v85\n"
        "v_mfma_f32_16x16x32_bf16 v[104:107], a[140:143], a[220:223], v[104:107]\n"
        "v_cvt_pk_bf16_f32 v85, v86, v87\n"
        "v_mfma_f32_16x16x32_bf16 v[96:99], a[20:23], a[220:223], 0\n"
        "v_mfma_f32_16x16x32_bf16 v[96:99], a[16:19], a[216:219], v[96:99]\n"
        "v_mfma_f32_16x16x32_bf16 v[96:99], a[12:15], a[248:251], v[96:99]\n"
        "s_cmp_eq_u32 s37, 100663296\n"
        "v_mfma_f32_16x16x32_bf16 v[96:99], a[8:11], a[244:247], v[96:99]\n"
        "s_cselect_b32 s46, 0, s37\n"
        "v_mfma_f32_16x16x32_bf16 v[96:99], a[4:7], a[240:243], v[96:99]\n"
        "s_add_u32 s38, s46, s43\n"
        "v_cvt_pk_bf16_f32 v230, v104, v105\n"
        "v_cvt_pk_bf16_f32 v231, v106, v107\n"
        "v_mfma_f32_16x16x32_bf16 v[96:99], a[0:3], a[236:239], v[96:99]\n"
        "s_add_u32 s39, s46, s44\n"
        "v_mfma_f32_16x16x32_bf16 v[108:111], v[212:215], v[156:159], v[12:15]\n"
        "s_add_u32 s37, s37, 6291456\n"
        "ds_read_b128 v[222:225], v251 offset:4608\n"
        "v_mfma_f32_16x16x32_bf16 v[108:111], v[228:231], v[164:167], v[108:111]\n"
        "v_mfma_f32_16x16x32_bf16 v[112:115], v[212:215], v[176:179], v[28:31]\n"
        "v_mfma_f32_16x16x32_bf16 v[112:115], v[228:231], v[184:187], v[112:115]\n"
        "v_mfma_f32_16x16x32_bf16 v[116:119], v[212:215], v[196:199], v[44:47]\n"
        "v_mul_f32 v96, s50, v96\n"
        "v_mul_f32 v97, s50, v97\n"
        "v_mfma_f32_16x16x32_bf16 v[116:119], v[228:231], v[204:207], v[116:119]\n"
        "v_mul_f32 v98, s50, v98\n"
        "v_mul_f32 v99, s50, v99\n"
        "v_mfma_f32_16x16x32_bf16 v[100:103], a[24:27], a[236:239], 0\n"
        "v_cvt_pk_bf16_f32 v216, v96, v97\n"
        "v_cvt_pk_bf16_f32 v217, v98, v99\n"
        "v_mfma_f32_16x16x32_bf16 v[100:103], a[28:31], a[240:243], v[100:103]\n"
        "v_exp_f32 v108, v108\n"
        "v_mfma_f32_16x16x32_bf16 v[100:103], a[32:35], a[244:247], v[100:103]\n"
        "v_exp_f32 v109, v109\n"
        "v_mfma_f32_16x16x32_bf16 v[100:103], a[36:39], a[248:251], v[100:103]\n"
        "v_exp_f32 v110, v110\n"
        "v_mfma_f32_16x16x32_bf16 v[100:103], a[40:43], a[216:219], v[100:103]\n"
        "v_exp_f32 v111, v111\n"
        "v_mfma_f32_16x16x32_bf16 v[100:103], a[44:47], a[220:223], v[100:103]\n"
        "v_exp_f32 v112, v112\n"
        "v_mfma_f32_16x16x32_bf16 v[104:107], a[68:71], a[220:223], 0\n"
        "v_exp_f32 v113, v113\n"
        "v_mfma_f32_16x16x32_bf16 v[104:107], a[64:67], a[216:219], v[104:107]\n"
        "v_exp_f32 v114, v114\n"
        "v_mfma_f32_16x16x32_bf16 v[104:107], a[60:63], a[248:251], v[104:107]\n"
        "v_exp_f32 v115, v115\n"
        "v_mfma_f32_16x16x32_bf16 v[104:107], a[56:59], a[244:247], v[104:107]\n"
        "v_mul_f32 v100, s50, v100\n"
        "v_mul_f32 v101, s50, v101\n"
        "v_mfma_f32_16x16x32_bf16 v[104:107], a[52:55], a[240:243], v[104:107]\n"
        "v_mul_f32 v102, s50, v102\n"
        "v_mul_f32 v103, s50, v103\n"
        "v_mfma_f32_16x16x32_bf16 v[104:107], a[48:51], a[236:239], v[104:107]\n"
        "v_cvt_pk_bf16_f32 v218, v100, v101\n"
        "v_cvt_pk_bf16_f32 v219, v102, v103\n"
        "v_mfma_f32_16x16x32_bf16 v[96:99], a[236:239], a[144:147], 0\n"
        "v_exp_f32 v116, v116\n"
        "v_mfma_f32_16x16x32_bf16 v[96:99], a[240:243], a[148:151], v[96:99]\n"
        "v_exp_f32 v117, v117\n"
        "v_mfma_f32_16x16x32_bf16 v[96:99], a[244:247], a[152:155], v[96:99]\n"
        "v_exp_f32 v118, v118\n"
        "v_mfma_f32_16x16x32_bf16 v[96:99], a[248:251], a[156:159], v[96:99]\n"
        "v_mul_f32 v104, s50, v104\n"
        "v_mul_f32 v105, s50, v105\n"
        "v_mfma_f32_16x16x32_bf16 v[96:99], a[216:219], a[160:163], v[96:99]\n"
        "v_mul_f32 v106, s50, v106\n"
        "v_mul_f32 v107, s50, v107\n"
        "v_mfma_f32_16x16x32_bf16 v[96:99], a[220:223], a[164:167], v[96:99]\n"
        "v_cvt_pk_bf16_f32 v220, v104, v105\n"
        "v_cvt_pk_bf16_f32 v221, v106, v107\n"
        "v_mfma_f32_16x16x32_bf16 v[88:91], v[152:155], v[216:219], v[48:51]\n"
        "s_waitcnt lgkmcnt(0)\n"
        "ds_read_b128 v[156:159], v247 offset:0\n"
        "v_mov_b64 v[226:227], v[220:221]\n"
        "v_mfma_f32_16x16x32_bf16 v[88:91], v[168:171], v[220:223], v[88:91]\n"
        "ds_read_b128 v[164:167], v247 offset:4096\n"
        "v_cvt_pk_bf16_f32 v70, v108, v109\n"
        "v_mfma_f32_16x16x32_bf16 v[92:95], v[172:175], v[216:219], v[52:55]\n"
        "s_add_u32 s47, s47, 6291456\n"
        "ds_read_b128 v[160:163], v247 offset:256\n"
        "v_cvt_pk_bf16_f32 v134, v96, v97\n"
        "v_mfma_f32_16x16x32_bf16 v[92:95], v[188:191], v[224:227], v[92:95]\n"
        "s_add_u32 s36, s36, 2097152\n"
        "ds_read_b128 v[222:225], v252 offset:4608\n"
        "v_cvt_pk_bf16_f32 v135, v98, v99\n"
        "v_mfma_f32_16x16x32_bf16 v[100:103], a[220:223], a[188:191], 0\n"
        "s_xor_b32 s48, s48, 8192\n"
        "ds_read_b128 v[152:155], v247 offset:4352\n"
        "v_cvt_pk_bf16_f32 v71, v110, v111\n"
        "v_mfma_f32_16x16x32_bf16 v[100:103], a[216:219], a[184:187], v[100:103]\n"
        "v_exp_f32 v119, v119\n"
        "v_mfma_f32_16x16x32_bf16 v[100:103], a[248:251], a[180:183], v[100:103]\n"
        "v_exp_f32 v88, v88\n"
        "v_mfma_f32_16x16x32_bf16 v[100:103], a[244:247], a[176:179], v[100:103]\n"
        "v_exp_f32 v89, v89\n"
        "v_mfma_f32_16x16x32_bf16 v[100:103], a[240:243], a[172:175], v[100:103]\n"
        "v_exp_f32 v90, v90\n"
        "v_mfma_f32_16x16x32_bf16 v[100:103], a[236:239], a[168:171], v[100:103]\n"
        "v_exp_f32 v91, v91\n"
        "v_mfma_f32_16x16x32_bf16 v[104:107], a[236:239], a[192:195], 0\n"
        "ds_read_b128 a[236:239], v248 offset:0\n"
        "v_cvt_pk_bf16_f32 v78, v112, v113\n"
        "v_mfma_f32_16x16x32_bf16 v[104:107], a[240:243], a[196:199], v[104:107]\n"
        "ds_read_b128 a[240:243], v248 offset:6144\n"
        "v_cvt_pk_bf16_f32 v79, v114, v115\n"
        "v_mfma_f32_16x16x32_bf16 v[104:107], a[244:247], a[200:203], v[104:107]\n"
        "v_cvt_pk_bf16_f32 v142, v100, v101\n"
        "v_cvt_pk_bf16_f32 v143, v102, v103\n"
        "v_mfma_f32_16x16x32_bf16 v[104:107], a[248:251], a[204:207], v[104:107]\n"
        "v_exp_f32 v92, v92\n"
        "v_mfma_f32_16x16x32_bf16 v[104:107], a[216:219], a[208:211], v[104:107]\n"
        "v_exp_f32 v93, v93\n"
        "v_mfma_f32_16x16x32_bf16 v[104:107], a[220:223], a[212:215], v[104:107]\n"
        "ds_read_b128 a[244:247], v254 offset:16\n"
        "v_cvt_pk_bf16_f32 v86, v116, v117\n"
        "v_mfma_f32_16x16x32_bf16 v[108:111], v[192:195], v[216:219], v[56:59]\n"
        "s_waitcnt lgkmcnt(4)\n"
        "ds_read_b128 a[248:251], v254 offset:6160\n"
        "v_mov_b64 v[116:117], 0\n"
        "v_mfma_f32_16x16x32_bf16 v[108:111], v[208:211], v[220:223], v[108:111]\n"
        "v_exp_f32 v94, v94\n"
        "v_mfma_f32_16x16x32_bf16 v[112:115], v[212:215], v[216:219], v[60:63]\n"
        "v_cvt_pk_bf16_f32 v150, v104, v105\n"
        "v_cvt_pk_bf16_f32 v151, v106, v107\n"
        "v_mfma_f32_16x16x32_bf16 v[112:115], v[228:231], v[224:227], v[112:115]\n"
        "s_waitcnt lgkmcnt(3)\n"
        "ds_read_b128 a[216:219], v248 offset:32\n"
        "v_cvt_pk_bf16_f32 v87, v118, v119\n"
        "v_mfma_f32_16x16x32_bf16 v[172:175], v[156:159], a[236:239], 0\n"
        "s_waitcnt lgkmcnt(3)\n"
        "ds_read_b128 a[220:223], v248 offset:6176\n"
        "v_cvt_pk_bf16_f32 v88, v88, v89\n"
        "v_mfma_f32_16x16x32_bf16 v[172:175], v[164:167], a[240:243], v[172:175]\n"
        "v_exp_f32 v95, v95\n"
        "v_mfma_f32_16x16x32_bf16 v[200:203], v[152:155], a[240:243], 0\n"
        "v_exp_f32 v108, v108\n"
        "v_mfma_f32_16x16x32_bf16 v[200:203], v[160:163], a[236:239], v[200:203]\n"
        "ds_read_b128 a[224:227], v254 offset:48\n"
        "v_cvt_pk_bf16_f32 v89, v90, v91\n"
        "v_smfmac_f32_16x16x64_bf16 v[116:119], a[252:255], v[64:71], v255\n"
        "ds_read_b128 a[228:231], v254 offset:6192\n"
        "v_cvt_pk_bf16_f32 v90, v92, v93\n"
        "v_mfma_f32_16x16x32_bf16 v[120:123], v[128:131], v[64:67], 0\n"
        "v_exp_f32 v109, v109\n"
        "v_mfma_f32_16x16x32_bf16 v[120:123], v[132:135], v[68:71], v[120:123]\n"
        "v_exp_f32 v110, v110\n"
        "v_mfma_f32_16x16x32_bf16 v[124:127], v[140:143], v[68:71], 0\n"
        "ds_read_b128 a[232:235], v248 offset:64\n"
        "v_add_f32 v116, v116, v117\n"
        "v_mfma_f32_16x16x32_bf16 v[124:127], v[136:139], v[64:67], v[124:127]\n"
        "ds_read_b128 a[236:239], v248 offset:6208\n"
        "v_cvt_pk_bf16_f32 v91, v94, v95\n"
        "v_mfma_f32_16x16x32_bf16 v[96:99], v[144:147], v[64:67], 0\n"
        "v_rcp_f32 v117, v116\n"
        "v_mfma_f32_16x16x32_bf16 v[96:99], v[148:151], v[68:71], v[96:99]\n"
        "s_waitcnt lgkmcnt(7)\n"
        "v_exp_f32 v111, v111\n"
        "v_mfma_f32_16x16x32_bf16 v[204:207], v[160:163], a[244:247], 0\n"
        "s_waitcnt lgkmcnt(6)\n"
        "v_exp_f32 v112, v112\n"
        "v_mfma_f32_16x16x32_bf16 v[204:207], v[152:155], a[248:251], v[204:207]\n"
        "v_exp_f32 v113, v113\n"
        "v_mfma_f32_16x16x32_bf16 v[176:179], v[164:167], a[248:251], 0\n"
        "v_exp_f32 v114, v114\n"
        "v_mfma_f32_16x16x32_bf16 v[176:179], v[156:159], a[244:247], v[176:179]\n"
        "s_waitcnt lgkmcnt(5)\n"
        "v_mul_f32 v120, v117, v120\n"
        "v_mul_f32 v121, v117, v121\n"
        "v_mfma_f32_16x16x32_bf16 v[180:183], v[156:159], a[216:219], 0\n"
        "s_waitcnt lgkmcnt(4)\n"
        "v_mov_b64 v[100:101], 0\n"
        "v_mul_f32 v122, v117, v122\n"
        "v_mfma_f32_16x16x32_bf16 v[180:183], v[164:167], a[220:223], v[180:183]\n"
        "ds_read_b128 v[232:235], v247 offset:8192\n"
        "v_mul_f32 v123, v117, v123\n"
        "v_mfma_f32_16x16x32_bf16 v[212:215], v[152:155], a[220:223], 0\n"
        "ds_read_b128 v[64:67], v247 offset:12288\n"
        "v_cvt_pk_bf16_f32 v92, v108, v109\n"
        "v_mfma_f32_16x16x32_bf16 v[212:215], v[160:163], a[216:219], v[212:215]\n"
        "s_waitcnt lgkmcnt(5)\n"
        "ds_read_b128 a[240:243], v254 offset:80\n"
        "v_cvt_pk_bf16_f32 v120, v120, v121\n"
        "v_mfma_f32_16x16x32_bf16 v[216:219], v[160:163], a[224:227], 0\n"
        "s_waitcnt lgkmcnt(5)\n"
        "ds_read_b128 a[244:247], v254 offset:6224\n"
        "v_cvt_pk_bf16_f32 v121, v122, v123\n"
        "v_mfma_f32_16x16x32_bf16 v[216:219], v[152:155], a[228:231], v[216:219]\n"
        "ds_write_b32 v245, v120 offset:0\n"
        "v_mfma_f32_16x16x32_bf16 v[184:187], v[164:167], a[228:231], 0\n"
        "v_cvt_pk_bf16_f32 v93, v110, v111\n"
        "v_mul_f32 v124, v117, v124\n"
        "v_mfma_f32_16x16x32_bf16 v[184:187], v[156:159], a[224:227], v[184:187]\n"
        "s_waitcnt lgkmcnt(6)\n"
        "ds_write_b32 v245, v121 offset:4\n"
        "v_mfma_f32_16x16x32_bf16 v[192:195], v[156:159], a[232:235], 0\n"
        "s_waitcnt lgkmcnt(6)\n"
        "v_mul_f32 v125, v117, v125\n"
        "v_maximum3_f32 v253, v253, v116, v117\n"
        "v_mfma_f32_16x16x32_bf16 v[192:195], v[164:167], a[236:239], v[192:195]\n"
        "v_mul_f32 v126, v117, v126\n"
        "v_mul_f32 v127, v117, v127\n"
        "v_mfma_f32_16x16x32_bf16 v[220:223], v[152:155], a[236:239], 0\n"
        "v_cvt_pk_bf16_f32 v94, v112, v113\n"
        "v_cvt_pk_bf16_f32 v124, v124, v125\n"
        "v_mfma_f32_16x16x32_bf16 v[220:223], v[160:163], a[232:235], v[220:223]\n"
        "v_exp_f32 v115, v115\n"
        "v_smfmac_f32_16x16x64_bf16 v[100:103], a[252:255], v[72:79], v255\n"
        "v_cvt_pk_bf16_f32 v125, v126, v127\n"
        "v_cvt_pk_bf16_f32 v95, v114, v115\n"
        "v_mfma_f32_16x16x32_bf16 v[104:107], v[128:131], v[72:75], 0\n"
        "ds_read_b128 v[236:239], v247 offset:8448\n"
        "ds_read_b128 a[248:251], v248 offset:36864\n"
        "v_mfma_f32_16x16x32_bf16 v[104:107], v[132:135], v[76:79], v[104:107]\n"
        "ds_read_b128 v[68:71], v247 offset:12544\n"
        "ds_read_b128 a[216:219], v248 offset:43008\n"
        "v_mfma_f32_16x16x32_bf16 v[108:111], v[140:143], v[76:79], 0\n"
        "ds_write_b32 v245, v124 offset:8\n"
        "v_mfma_f32_16x16x32_bf16 v[108:111], v[136:139], v[72:75], v[108:111]\n"
        "ds_write_b32 v245, v125 offset:12\n"
        "v_mfma_f32_16x16x32_bf16 v[112:115], v[144:147], v[72:75], 0\n"
        "ds_read_b128 a[220:223], v254 offset:36880\n"
        "v_mul_f32 v96, v117, v96\n"
        "v_mfma_f32_16x16x32_bf16 v[112:115], v[148:151], v[76:79], v[112:115]\n"
        "s_waitcnt lgkmcnt(10)\n"
        "ds_read_b128 a[224:227], v254 offset:43024\n"
        "v_mul_f32 v97, v117, v97\n"
        "v_mfma_f32_16x16x32_bf16 v[224:227], v[160:163], a[240:243], 0\n"
        "s_waitcnt lgkmcnt(10)\n"
        "v_mul_f32 v98, v117, v98\n"
        "v_mul_f32 v99, v117, v99\n"
        "v_mfma_f32_16x16x32_bf16 v[224:227], v[152:155], a[244:247], v[224:227]\n"
        "ds_read_b128 a[228:231], v248 offset:36896\n"
        "v_cvt_pk_bf16_f32 v96, v96, v97\n"
        "v_mfma_f32_16x16x32_bf16 v[196:199], v[164:167], a[244:247], 0\n"
        "ds_read_b128 a[232:235], v248 offset:43040\n"
        "v_cvt_pk_bf16_f32 v97, v98, v99\n"
        "v_mfma_f32_16x16x32_bf16 v[196:199], v[156:159], a[240:243], v[196:199]\n"
        "s_waitcnt lgkmcnt(8)\n"
        "ds_write_b32 v246, v96 offset:0\n"
        "v_mfma_f32_16x16x32_bf16 v[172:175], v[232:235], a[248:251], v[172:175]\n"
        "s_waitcnt lgkmcnt(7)\n"
        "ds_write_b32 v246, v97 offset:4\n"
        "v_mfma_f32_16x16x32_bf16 v[172:175], v[64:67], a[216:219], v[172:175]\n"
        "ds_read_b128 a[236:239], v254 offset:36912\n"
        "v_add_f32 v100, v100, v101\n"
        "v_mfma_f32_16x16x32_bf16 v[200:203], v[68:71], a[216:219], v[200:203]\n"
        "v_rcp_f32 v101, v100\n"
        "v_mfma_f32_16x16x32_bf16 v[200:203], v[236:239], a[248:251], v[200:203]\n"
        "s_waitcnt lgkmcnt(6)\n"
        "ds_read_b128 a[240:243], v254 offset:43056\n"
        "v_mul_f32 v104, v101, v104\n"
        "v_mfma_f32_16x16x32_bf16 v[204:207], v[236:239], a[220:223], v[204:207]\n"
        "s_waitcnt lgkmcnt(6)\n"
        "v_mul_f32 v105, v101, v105\n"
        "v_mul_f32 v106, v101, v106\n"
        "v_mfma_f32_16x16x32_bf16 v[204:207], v[68:71], a[224:227], v[204:207]\n"
        "v_mul_f32 v107, v101, v107\n"
        "v_cvt_pk_bf16_f32 v104, v104, v105\n"
        "v_mfma_f32_16x16x32_bf16 v[176:179], v[64:67], a[224:227], v[176:179]\n"
        "v_cvt_pk_bf16_f32 v105, v106, v107\n"
        "v_mov_b64 v[116:117], 0\n"
        "v_mfma_f32_16x16x32_bf16 v[176:179], v[232:235], a[220:223], v[176:179]\n"
        "s_waitcnt lgkmcnt(5)\n"
        "ds_write_b32 v245, v104 offset:256\n"
        "v_mfma_f32_16x16x32_bf16 v[180:183], v[232:235], a[228:231], v[180:183]\n"
        "s_waitcnt lgkmcnt(5)\n"
        "ds_write_b32 v245, v105 offset:260\n"
        "v_mfma_f32_16x16x32_bf16 v[180:183], v[64:67], a[232:235], v[180:183]\n"
        "v_mul_f32 v108, v101, v108\n"
        "v_mul_f32 v109, v101, v109\n"
        "v_mfma_f32_16x16x32_bf16 v[212:215], v[68:71], a[232:235], v[212:215]\n"
        "v_maximum3_f32 v253, v253, v100, v101\n"
        "v_mul_f32 v110, v101, v110\n"
        "v_mfma_f32_16x16x32_bf16 v[212:215], v[236:239], a[228:231], v[212:215]\n"
        "s_waitcnt lgkmcnt(3)\n"
        "v_mul_f32 v111, v101, v111\n"
        "v_cvt_pk_bf16_f32 v108, v108, v109\n"
        "v_mfma_f32_16x16x32_bf16 v[216:219], v[236:239], a[236:239], v[216:219]\n"
        "s_waitcnt lgkmcnt(2)\n"
        "v_cvt_pk_bf16_f32 v109, v110, v111\n"
        "v_mul_f32 v112, v101, v112\n"
        "v_mfma_f32_16x16x32_bf16 v[216:219], v[68:71], a[240:243], v[216:219]\n"
        "ds_read_b128 a[244:247], v248 offset:36928\n"
        "v_mul_f32 v113, v101, v113\n"
        "v_mfma_f32_16x16x32_bf16 v[184:187], v[64:67], a[240:243], v[184:187]\n"
        "ds_read_b128 a[248:251], v248 offset:43072\n"
        "v_mul_f32 v114, v101, v114\n"
        "v_mfma_f32_16x16x32_bf16 v[184:187], v[232:235], a[236:239], v[184:187]\n"
        "ds_write_b32 v245, v108 offset:264\n"
        "v_smfmac_f32_16x16x64_bf16 v[116:119], a[252:255], v[80:87], v255\n"
        "ds_write_b32 v245, v109 offset:268\n"
        "v_mfma_f32_16x16x32_bf16 v[120:123], v[128:131], v[80:83], 0\n"
        "ds_read_b128 a[216:219], v254 offset:36944\n"
        "v_mul_f32 v115, v101, v115\n"
        "v_mfma_f32_16x16x32_bf16 v[120:123], v[132:135], v[84:87], v[120:123]\n"
        "ds_read_b128 a[220:223], v254 offset:43088\n"
        "v_cvt_pk_bf16_f32 v112, v112, v113\n"
        "v_mfma_f32_16x16x32_bf16 v[124:127], v[140:143], v[84:87], 0\n"
        "ds_read_b128 v[156:159], v247 offset:16384\n"
        "v_cvt_pk_bf16_f32 v113, v114, v115\n"
        "v_mfma_f32_16x16x32_bf16 v[124:127], v[136:139], v[80:83], v[124:127]\n"
        "ds_read_b128 v[164:167], v247 offset:20480\n"
        "v_add_f32 v116, v116, v117\n"
        "v_mfma_f32_16x16x32_bf16 v[96:99], v[144:147], v[80:83], 0\n"
        "ds_read_b128 v[160:163], v247 offset:16640\n"
        "ds_read_b128 a[224:227], v248 offset:12288\n"
        "v_mfma_f32_16x16x32_bf16 v[96:99], v[148:151], v[84:87], v[96:99]\n"
        "s_waitcnt lgkmcnt(9)\n"
        "ds_read_b128 v[152:155], v247 offset:20736\n"
        "ds_read_b128 a[228:231], v248 offset:49152\n"
        "v_mfma_f32_16x16x32_bf16 v[192:195], v[232:235], a[244:247], v[192:195]\n"
        "s_waitcnt lgkmcnt(10)\n"
        "ds_write_b32 v246, v112 offset:256\n"
        "v_mfma_f32_16x16x32_bf16 v[192:195], v[64:67], a[248:251], v[192:195]\n"
        "ds_write_b32 v246, v113 offset:260\n"
        "v_mfma_f32_16x16x32_bf16 v[220:223], v[68:71], a[248:251], v[220:223]\n"
        "ds_read_b128 a[232:235], v254 offset:12304\n"
        "v_add_u32 v247, s31, v247\n"
        "v_mfma_f32_16x16x32_bf16 v[220:223], v[236:239], a[244:247], v[220:223]\n"
        "s_waitcnt lgkmcnt(10)\n"
        "ds_read_b128 a[236:239], v254 offset:49168\n"
        "v_mfma_f32_16x16x32_bf16 v[224:227], v[236:239], a[216:219], v[224:227]\n"
        "s_waitcnt lgkmcnt(10)\n"
        "v_rcp_f32 v117, v116\n"
        "v_mfma_f32_16x16x32_bf16 v[224:227], v[68:71], a[220:223], v[224:227]\n"
        "v_mul_f32 v120, v117, v120\n"
        "v_mul_f32 v121, v117, v121\n"
        "v_mfma_f32_16x16x32_bf16 v[196:199], v[64:67], a[220:223], v[196:199]\n"
        "ds_read_b128 a[240:243], v248 offset:12320\n"
        "v_mul_f32 v122, v117, v122\n"
        "v_mfma_f32_16x16x32_bf16 v[196:199], v[232:235], a[216:219], v[196:199]\n"
        "s_waitcnt lgkmcnt(7)\n"
        "ds_read_b128 a[244:247], v248 offset:49184\n"
        "v_mul_f32 v123, v117, v123\n"
        "v_mfma_f32_16x16x32_bf16 v[172:175], v[156:159], a[224:227], v[172:175]\n"
        "s_waitcnt lgkmcnt(6)\n"
        "ds_read_b128 a[248:251], v254 offset:12336\n"
        "v_cvt_pk_bf16_f32 v120, v120, v121\n"
        "v_mfma_f32_16x16x32_bf16 v[172:175], v[164:167], a[228:231], v[172:175]\n"
        "ds_read_b128 a[216:219], v254 offset:49200\n"
        "v_cvt_pk_bf16_f32 v121, v122, v123\n"
        "v_mfma_f32_16x16x32_bf16 v[200:203], v[152:155], a[228:231], v[200:203]\n"
        "ds_write_b32 v245, v120 offset:512\n"
        "v_mfma_f32_16x16x32_bf16 v[200:203], v[160:163], a[224:227], v[200:203]\n"
        "s_waitcnt lgkmcnt(6)\n"
        "ds_write_b32 v245, v121 offset:516\n"
        "v_mfma_f32_16x16x32_bf16 v[204:207], v[160:163], a[232:235], v[204:207]\n"
        "s_waitcnt lgkmcnt(6)\n"
        "v_mul_f32 v124, v117, v124\n"
        "v_mul_f32 v125, v117, v125\n"
        "v_mfma_f32_16x16x32_bf16 v[204:207], v[152:155], a[236:239], v[204:207]\n"
        "v_maximum3_f32 v253, v253, v116, v117\n"
        "v_mul_f32 v126, v117, v126\n"
        "v_mfma_f32_16x16x32_bf16 v[176:179], v[164:167], a[236:239], v[176:179]\n"
        "ds_read_b128 a[220:223], v248 offset:12352\n"
        "v_mul_f32 v127, v117, v127\n"
        "v_mfma_f32_16x16x32_bf16 v[176:179], v[156:159], a[232:235], v[176:179]\n"
        "s_waitcnt lgkmcnt(6)\n"
        "ds_read_b128 a[224:227], v248 offset:49216\n"
        "v_cvt_pk_bf16_f32 v124, v124, v125\n"
        "v_mfma_f32_16x16x32_bf16 v[180:183], v[156:159], a[240:243], v[180:183]\n"
        "s_waitcnt lgkmcnt(6)\n"
        "v_cvt_pk_bf16_f32 v125, v126, v127\n"
        "v_mul_f32 v96, v117, v96\n"
        "v_mfma_f32_16x16x32_bf16 v[180:183], v[164:167], a[244:247], v[180:183]\n"
        "ds_write_b32 v245, v124 offset:520\n"
        "v_mfma_f32_16x16x32_bf16 v[212:215], v[152:155], a[244:247], v[212:215]\n"
        "ds_read_b128 a[228:231], v254 offset:12368\n"
        "v_mul_f32 v97, v117, v97\n"
        "v_mfma_f32_16x16x32_bf16 v[212:215], v[160:163], a[240:243], v[212:215]\n"
        "s_waitcnt lgkmcnt(7)\n"
        "ds_read_b128 a[232:235], v254 offset:49232\n"
        "v_mul_f32 v98, v117, v98\n"
        "v_mfma_f32_16x16x32_bf16 v[216:219], v[160:163], a[248:251], v[216:219]\n"
        "s_waitcnt lgkmcnt(7)\n"
        "ds_write_b32 v245, v125 offset:524\n"
        "v_mfma_f32_16x16x32_bf16 v[216:219], v[152:155], a[216:219], v[216:219]\n"
        "v_mul_f32 v99, v117, v99\n"
        "v_cvt_pk_bf16_f32 v96, v96, v97\n"
        "v_mfma_f32_16x16x32_bf16 v[184:187], v[164:167], a[216:219], v[184:187]\n"
        "v_cvt_pk_bf16_f32 v97, v98, v99\n"
        "v_mov_b64 v[80:81], 0\n"
        "v_mfma_f32_16x16x32_bf16 v[184:187], v[156:159], a[248:251], v[184:187]\n"
        "s_waitcnt lgkmcnt(5)\n"
        "s_waitcnt vmcnt(0)\n"
        "s_barrier\n"
        "ds_write_b32 v246, v96 offset:512\n"
        "v_mfma_f32_16x16x32_bf16 v[192:195], v[156:159], a[220:223], v[192:195]\n"
        "s_waitcnt lgkmcnt(5)\n"
        "ds_write_b32 v246, v97 offset:516\n"
        "v_mfma_f32_16x16x32_bf16 v[192:195], v[164:167], a[224:227], v[192:195]\n"
        "ds_read_b128 a[236:239], v240 offset:0\n"
        "v_mfma_f32_16x16x32_bf16 v[220:223], v[152:155], a[224:227], v[220:223]\n"
        "ds_read_b128 a[240:243], v241 offset:0\n"
        "v_mfma_f32_16x16x32_bf16 v[220:223], v[160:163], a[220:223], v[220:223]\n"
        "s_waitcnt lgkmcnt(6)\n"
        "ds_read_b128 a[244:247], v240 offset:128\n"
        "v_mfma_f32_16x16x32_bf16 v[224:227], v[160:163], a[228:231], v[224:227]\n"
        "s_waitcnt lgkmcnt(6)\n"
        "ds_read_b128 a[248:251], v241 offset:128\n"
        "v_mfma_f32_16x16x32_bf16 v[224:227], v[152:155], a[232:235], v[224:227]\n"
        "ds_read_b128 a[216:219], v240 offset:256\n"
        "v_mfma_f32_16x16x32_bf16 v[196:199], v[164:167], a[232:235], v[196:199]\n"
        "ds_read_b128 a[220:223], v241 offset:256\n"
        "v_mfma_f32_16x16x32_bf16 v[196:199], v[156:159], a[228:231], v[196:199]\n"
        "v_smfmac_f32_16x16x64_bf16 v[80:83], a[252:255], v[88:95], v255\n"
        "v_mfma_f32_16x16x32_bf16 v[84:87], v[128:131], v[88:91], 0\n"
        "v_mfma_f32_16x16x32_bf16 v[84:87], v[132:135], v[92:95], v[84:87]\n"
        "v_mfma_f32_16x16x32_bf16 v[232:235], v[140:143], v[92:95], 0\n"
        "v_mfma_f32_16x16x32_bf16 v[232:235], v[136:139], v[88:91], v[232:235]\n"
        "v_mfma_f32_16x16x32_bf16 v[236:239], v[144:147], v[88:91], 0\n"
        "v_mfma_f32_16x16x32_bf16 v[236:239], v[148:151], v[92:95], v[236:239]\n"
        "s_waitcnt lgkmcnt(0)\n"
        ".p2align 6\n"
        "10:\n"
        "s_waitcnt lgkmcnt(5)\n"
        "v_mfma_f32_16x16x32_bf16 v[96:99], a[72:75], a[236:239], 0\n"
        "s_waitcnt lgkmcnt(4)\n"
        "v_and_b32 v117, 60, v250\n"
        "v_add3_u32 v117, v250, v117, s26\n"
        "v_mfma_f32_16x16x32_bf16 v[96:99], a[76:79], a[240:243], v[96:99]\n"
        "s_waitcnt lgkmcnt(3)\n"
        "v_cvt_pk_bf16_f32 v108, v172, v176\n"
        "v_cvt_pk_bf16_f32 v109, v180, v184\n"
        "v_mfma_f32_16x16x32_bf16 v[96:99], a[80:83], a[244:247], v[96:99]\n"
        "s_waitcnt lgkmcnt(2)\n"
        "v_cvt_pk_bf16_f32 v110, v192, v196\n"
        "v_add_f32 v80, v80, v81\n"
        "v_mfma_f32_16x16x32_bf16 v[96:99], a[84:87], a[248:251], v[96:99]\n"
        "s_waitcnt lgkmcnt(1)\n"
        "buffer_store_dwordx2 v[108:109], v117, s[16:19], s38 offen offset:0 sc1\n"
        "v_mfma_f32_16x16x32_bf16 v[96:99], a[88:91], a[216:219], v[96:99]\n"
        "s_waitcnt lgkmcnt(0)\n"
        "v_rcp_f32 v81, v80\n"
        "v_mfma_f32_16x16x32_bf16 v[96:99], a[92:95], a[220:223], v[96:99]\n"
        "s_add_u32 s41, s29, s40\n"
        "v_cvt_pk_bf16_f32 v112, v173, v177\n"
        "v_cvt_pk_bf16_f32 v113, v181, v185\n"
        "v_mfma_f32_16x16x32_bf16 v[100:103], a[116:119], a[220:223], 0\n"
        "s_add_u32 s35, s47, 786432\n"
        "v_mul_f32 v84, v81, v84\n"
        "v_mul_f32 v85, v81, v85\n"
        "v_mfma_f32_16x16x32_bf16 v[100:103], a[112:115], a[216:219], v[100:103]\n"
        "s_sub_u32 s46, s35, 100663296\n"
        "v_cvt_pk_bf16_f32 v152, v96, v97\n"
        "v_cvt_pk_bf16_f32 v153, v98, v99\n"
        "v_mfma_f32_16x16x32_bf16 v[100:103], a[108:111], a[248:251], v[100:103]\n"
        "s_cmp_lt_u32 s46, 786432\n"
        "buffer_store_dword v110, v250, s[16:19], s38 offen offset:0 sc1\n"
        "v_mfma_f32_16x16x32_bf16 v[100:103], a[104:107], a[244:247], v[100:103]\n"
        "s_cselect_b32 s35, s46, s35\n"
        "v_cvt_pk_bf16_f32 v114, v193, v197\n"
        "v_mul_f32 v86, v81, v86\n"
        "v_mfma_f32_16x16x32_bf16 v[100:103], a[100:103], a[240:243], v[100:103]\n"
        "s_sub_u32 s31, s28, s30\n"
        "v_mul_f32 v87, v81, v87\n"
        "v_cvt_pk_bf16_f32 v84, v84, v85\n"
        "v_mfma_f32_16x16x32_bf16 v[100:103], a[96:99], a[236:239], v[100:103]\n"
        "s_sub_u32 s45, s29, s28\n"
        "ds_write_b32 v245, v84 offset:768\n"
        "v_mfma_f32_16x16x32_bf16 v[104:107], a[120:123], a[236:239], 0\n"
        "s_mov_b32 s29, s30\n"
        "v_cvt_pk_bf16_f32 v85, v86, v87\n"
        "v_mul_f32 v232, v81, v232\n"
        "v_mfma_f32_16x16x32_bf16 v[104:107], a[124:127], a[240:243], v[104:107]\n"
        "s_mov_b32 s30, s28\n"
        "buffer_store_dwordx2 v[112:113], v117, s[16:19], s38 offen offset:384 sc1\n"
        "v_mfma_f32_16x16x32_bf16 v[104:107], a[128:131], a[244:247], v[104:107]\n"
        "s_add_u32 s28, s28, s45\n"
        "v_cvt_pk_bf16_f32 v154, v100, v101\n"
        "v_cvt_pk_bf16_f32 v155, v102, v103\n"
        "v_mfma_f32_16x16x32_bf16 v[104:107], a[132:135], a[248:251], v[104:107]\n"
        "ds_write_b32 v245, v85 offset:772\n"
        "v_mfma_f32_16x16x32_bf16 v[104:107], a[136:139], a[216:219], v[104:107]\n"
        "v_cvt_pk_bf16_f32 v108, v174, v178\n"
        "v_cvt_pk_bf16_f32 v109, v182, v186\n"
        "v_mfma_f32_16x16x32_bf16 v[104:107], a[140:143], a[220:223], v[104:107]\n"
        "v_mul_f32 v233, v81, v233\n"
        "v_maximum3_f32 v253, v253, v80, v81\n"
        "v_mfma_f32_16x16x32_bf16 v[96:99], a[20:23], a[220:223], 0\n"
        "buffer_store_dword v114, v250, s[16:19], s38 offen offset:384 sc1\n"
        "v_mfma_f32_16x16x32_bf16 v[96:99], a[16:19], a[216:219], v[96:99]\n"
        "v_cvt_pk_bf16_f32 v110, v194, v198\n"
        "v_mul_f32 v234, v81, v234\n"
        "v_mfma_f32_16x16x32_bf16 v[96:99], a[12:15], a[248:251], v[96:99]\n"
        "v_cvt_pk_bf16_f32 v168, v104, v105\n"
        "v_cvt_pk_bf16_f32 v169, v106, v107\n"
        "v_mfma_f32_16x16x32_bf16 v[96:99], a[8:11], a[244:247], v[96:99]\n"
        "v_mul_f32 v235, v81, v235\n"
        "v_cvt_pk_bf16_f32 v232, v232, v233\n"
        "v_mfma_f32_16x16x32_bf16 v[96:99], a[4:7], a[240:243], v[96:99]\n"
        "ds_write_b32 v245, v232 offset:776\n"
        "v_mfma_f32_16x16x32_bf16 v[96:99], a[0:3], a[236:239], v[96:99]\n"
        "buffer_store_dwordx2 v[108:109], v117, s[16:19], s38 offen offset:768 sc1\n"
        "v_mfma_f32_16x16x32_bf16 v[100:103], a[24:27], a[236:239], 0\n"
        "v_cvt_pk_bf16_f32 v112, v175, v179\n"
        "v_cvt_pk_bf16_f32 v113, v183, v187\n"
        "v_mfma_f32_16x16x32_bf16 v[100:103], a[28:31], a[240:243], v[100:103]\n"
        "v_cvt_pk_bf16_f32 v233, v234, v235\n"
        "v_xor_b32 v251, 0x2000, v251\n"
        "v_mfma_f32_16x16x32_bf16 v[100:103], a[32:35], a[244:247], v[100:103]\n"
        "v_mul_f32 v96, s50, v96\n"
        "v_mul_f32 v97, s50, v97\n"
        "v_mfma_f32_16x16x32_bf16 v[100:103], a[36:39], a[248:251], v[100:103]\n"
        "v_mul_f32 v98, s50, v98\n"
        "v_mul_f32 v99, s50, v99\n"
        "v_mfma_f32_16x16x32_bf16 v[100:103], a[40:43], a[216:219], v[100:103]\n"
        "v_cvt_pk_bf16_f32 v156, v96, v97\n"
        "v_cvt_pk_bf16_f32 v157, v98, v99\n"
        "v_mfma_f32_16x16x32_bf16 v[100:103], a[44:47], a[220:223], v[100:103]\n"
        "buffer_store_dword v110, v250, s[16:19], s38 offen offset:768 sc1\n"
        "v_mfma_f32_16x16x32_bf16 v[104:107], a[68:71], a[220:223], 0\n"
        "ds_write_b32 v245, v233 offset:780\n"
        "v_mfma_f32_16x16x32_bf16 v[104:107], a[64:67], a[216:219], v[104:107]\n"
        "v_cvt_pk_bf16_f32 v114, v195, v199\n"
        "v_xor_b32 v252, 0x2000, v252\n"
        "v_mfma_f32_16x16x32_bf16 v[104:107], a[60:63], a[248:251], v[104:107]\n"
        "ds_read_b128 a[224:227], v240 offset:1536\n"
        "v_mul_f32 v100, s50, v100\n"
        "v_mfma_f32_16x16x32_bf16 v[104:107], a[56:59], a[244:247], v[104:107]\n"
        "v_mul_f32 v101, s50, v101\n"
        "v_mul_f32 v102, s50, v102\n"
        "v_mfma_f32_16x16x32_bf16 v[104:107], a[52:55], a[240:243], v[104:107]\n"
        "v_mul_f32 v103, s50, v103\n"
        "v_cvt_pk_bf16_f32 v158, v100, v101\n"
        "v_mfma_f32_16x16x32_bf16 v[104:107], a[48:51], a[236:239], v[104:107]\n"
        "buffer_store_dwordx2 v[112:113], v117, s[16:19], s38 offen offset:1152 sc1\n"
        "v_mfma_f32_16x16x32_bf16 v[96:99], a[236:239], a[144:147], 0\n"
        "ds_read_b128 a[228:231], v241 offset:1536\n"
        "v_cvt_pk_bf16_f32 v159, v102, v103\n"
        "v_mfma_f32_16x16x32_bf16 v[96:99], a[240:243], a[148:151], v[96:99]\n"
        "ds_read_b128 a[232:235], v240 offset:1664\n"
        "v_cvt_pk_bf16_f32 v108, v200, v204\n"
        "v_mfma_f32_16x16x32_bf16 v[96:99], a[244:247], a[152:155], v[96:99]\n"
        "v_mul_f32 v104, s50, v104\n"
        "v_mul_f32 v105, s50, v105\n"
        "v_mfma_f32_16x16x32_bf16 v[96:99], a[248:251], a[156:159], v[96:99]\n"
        "buffer_store_dword v114, v250, s[16:19], s38 offen offset:1152 sc1\n"
        "v_mfma_f32_16x16x32_bf16 v[96:99], a[216:219], a[160:163], v[96:99]\n"
        "v_mul_f32 v106, s50, v106\n"
        "v_mul_f32 v107, s50, v107\n"
        "v_mfma_f32_16x16x32_bf16 v[96:99], a[220:223], a[164:167], v[96:99]\n"
        "v_cvt_pk_bf16_f32 v160, v104, v105\n"
        "v_cvt_pk_bf16_f32 v161, v106, v107\n"
        "v_mfma_f32_16x16x32_bf16 v[100:103], a[220:223], a[188:191], 0\n"
        "v_mov_b64 v[166:167], v[160:161]\n"
        "v_cvt_pk_bf16_f32 v109, v212, v216\n"
        "v_mfma_f32_16x16x32_bf16 v[100:103], a[216:219], a[184:187], v[100:103]\n"
        "buffer_store_dwordx2 v[108:109], v117, s[16:19], s39 offen offset:0 sc1\n"
        "v_mfma_f32_16x16x32_bf16 v[100:103], a[248:251], a[180:183], v[100:103]\n"
        "v_cvt_pk_bf16_f32 v128, v96, v97\n"
        "v_cvt_pk_bf16_f32 v129, v98, v99\n"
        "v_mfma_f32_16x16x32_bf16 v[100:103], a[244:247], a[176:179], v[100:103]\n"
        "v_cvt_pk_bf16_f32 v110, v220, v224\n"
        "v_cvt_pk_bf16_f32 v112, v201, v205\n"
        "v_mfma_f32_16x16x32_bf16 v[100:103], a[240:243], a[172:175], v[100:103]\n"
        "v_cvt_pk_bf16_f32 v113, v213, v217\n"
        "v_mul_f32 v236, v81, v236\n"
        "v_mfma_f32_16x16x32_bf16 v[100:103], a[236:239], a[168:171], v[100:103]\n"
        "v_mul_f32 v237, v81, v237\n"
        "v_mul_f32 v238, v81, v238\n"
        "v_mfma_f32_16x16x32_bf16 v[104:107], a[236:239], a[192:195], 0\n"
        "ds_read_b128 a[236:239], v241 offset:1664\n"
        "v_mul_f32 v239, v81, v239\n"
        "v_mfma_f32_16x16x32_bf16 v[104:107], a[240:243], a[196:199], v[104:107]\n"
        "ds_read_b128 a[240:243], v240 offset:1792\n"
        "v_cvt_pk_bf16_f32 v236, v236, v237\n"
        "v_mfma_f32_16x16x32_bf16 v[104:107], a[244:247], a[200:203], v[104:107]\n"
        "ds_read_b128 a[244:247], v241 offset:1792\n"
        "v_cvt_pk_bf16_f32 v136, v100, v101\n"
        "v_mfma_f32_16x16x32_bf16 v[104:107], a[248:251], a[204:207], v[104:107]\n"
        "buffer_store_dword v110, v250, s[16:19], s39 offen offset:0 sc1\n"
        "v_mfma_f32_16x16x32_bf16 v[104:107], a[216:219], a[208:211], v[104:107]\n"
        "v_cvt_pk_bf16_f32 v137, v102, v103\n"
        "v_cvt_pk_bf16_f32 v237, v238, v239\n"
        "v_mfma_f32_16x16x32_bf16 v[104:107], a[220:223], a[212:215], v[104:107]\n"
        "s_waitcnt lgkmcnt(5)\n"
        "buffer_store_dwordx2 v[112:113], v117, s[16:19], s39 offen offset:384 sc1\n"
        "v_mfma_f32_16x16x32_bf16 v[96:99], a[72:75], a[224:227], 0\n"
        "s_waitcnt lgkmcnt(4)\n"
        "ds_write_b32 v246, v236 offset:768\n"
        "v_mfma_f32_16x16x32_bf16 v[96:99], a[76:79], a[228:231], v[96:99]\n"
        "s_waitcnt lgkmcnt(4)\n"
        "ds_write_b32 v246, v237 offset:772\n"
        "v_mfma_f32_16x16x32_bf16 v[96:99], a[80:83], a[232:235], v[96:99]\n"
        "s_waitcnt lgkmcnt(4)\n"
        "ds_read_b128 v[162:165], v251 offset:0\n"
        "v_cvt_pk_bf16_f32 v114, v221, v225\n"
        "v_mfma_f32_16x16x32_bf16 v[96:99], a[84:87], a[236:239], v[96:99]\n"
        "s_waitcnt lgkmcnt(4)\n"
        "v_cvt_pk_bf16_f32 v144, v104, v105\n"
        "v_cvt_pk_bf16_f32 v145, v106, v107\n"
        "v_mfma_f32_16x16x32_bf16 v[96:99], a[88:91], a[240:243], v[96:99]\n"
        "s_waitcnt lgkmcnt(3)\n"
        "buffer_store_dword v114, v250, s[16:19], s39 offen offset:384 sc1\n"
        "v_mfma_f32_16x16x32_bf16 v[96:99], a[92:95], a[244:247], v[96:99]\n"
        "v_cvt_pk_bf16_f32 v108, v202, v206\n"
        "v_cvt_pk_bf16_f32 v109, v214, v218\n"
        "v_mfma_f32_16x16x32_bf16 v[100:103], a[116:119], a[244:247], 0\n"
        "v_cvt_pk_bf16_f32 v110, v222, v226\n"
        "v_add_u32 v245, s31, v245\n"
        "v_mfma_f32_16x16x32_bf16 v[100:103], a[112:115], a[240:243], v[100:103]\n"
        "v_add_u32 v246, s31, v246\n"
        "v_mfma_f32_16x16x32_bf16 v[100:103], a[108:111], a[236:239], v[100:103]\n"
        "v_cvt_pk_bf16_f32 v172, v96, v97\n"
        "v_cvt_pk_bf16_f32 v173, v98, v99\n"
        "v_mfma_f32_16x16x32_bf16 v[100:103], a[104:107], a[232:235], v[100:103]\n"
        "buffer_store_dwordx2 v[108:109], v117, s[16:19], s39 offen offset:768 sc1\n"
        "v_mfma_f32_16x16x32_bf16 v[100:103], a[100:103], a[228:231], v[100:103]\n"
        "v_cvt_pk_bf16_f32 v112, v203, v207\n"
        "v_cvt_pk_bf16_f32 v113, v215, v219\n"
        "v_mfma_f32_16x16x32_bf16 v[100:103], a[96:99], a[224:227], v[100:103]\n"
        "v_mfma_f32_16x16x32_bf16 v[104:107], a[120:123], a[224:227], 0\n"
        "v_mfma_f32_16x16x32_bf16 v[104:107], a[124:127], a[228:231], v[104:107]\n"
        "v_mfma_f32_16x16x32_bf16 v[104:107], a[128:131], a[232:235], v[104:107]\n"
        "buffer_store_dword v110, v250, s[16:19], s39 offen offset:768 sc1\n"
        "v_mfma_f32_16x16x32_bf16 v[104:107], a[132:135], a[236:239], v[104:107]\n"
        "v_cvt_pk_bf16_f32 v114, v223, v227\n"
        "v_mfma_f32_16x16x32_bf16 v[104:107], a[136:139], a[240:243], v[104:107]\n"
        "v_mfma_f32_16x16x32_bf16 v[104:107], a[140:143], a[244:247], v[104:107]\n"
        "v_cvt_pk_bf16_f32 v174, v100, v101\n"
        "v_cvt_pk_bf16_f32 v175, v102, v103\n"
        "v_mfma_f32_16x16x32_bf16 v[96:99], a[20:23], a[244:247], 0\n"
        "v_mfma_f32_16x16x32_bf16 v[96:99], a[16:19], a[240:243], v[96:99]\n"
        "buffer_store_dwordx2 v[112:113], v117, s[16:19], s39 offen offset:1152 sc1\n"
        "v_mfma_f32_16x16x32_bf16 v[96:99], a[12:15], a[236:239], v[96:99]\n"
        "v_mfma_f32_16x16x32_bf16 v[96:99], a[8:11], a[232:235], v[96:99]\n"
        "v_mfma_f32_16x16x32_bf16 v[96:99], a[4:7], a[228:231], v[96:99]\n"
        "v_cvt_pk_bf16_f32 v190, v104, v105\n"
        "v_cvt_pk_bf16_f32 v191, v106, v107\n"
        "v_mfma_f32_16x16x32_bf16 v[96:99], a[0:3], a[224:227], v[96:99]\n"
        "ds_read_b128 v[182:185], v251 offset:512\n"
        "v_mfma_f32_16x16x32_bf16 v[64:67], v[152:155], v[156:159], v[0:3]\n"
        "s_waitcnt lgkmcnt(1)\n"
        "buffer_store_dword v114, v250, s[16:19], s39 offen offset:1152 sc1\n"
        "v_mfma_f32_16x16x32_bf16 v[64:67], v[168:171], v[160:163], v[64:67]\n"
        "v_mfma_f32_16x16x32_bf16 v[68:71], v[172:175], v[156:159], v[4:7]\n"
        "v_mfma_f32_16x16x32_bf16 v[68:71], v[188:191], v[164:167], v[68:71]\n"
        "v_mfma_f32_16x16x32_bf16 v[100:103], a[24:27], a[224:227], 0\n"
        "v_mfma_f32_16x16x32_bf16 v[100:103], a[28:31], a[228:231], v[100:103]\n"
        "v_mul_f32 v96, s50, v96\n"
        "v_mul_f32 v97, s50, v97\n"
        "v_mfma_f32_16x16x32_bf16 v[100:103], a[32:35], a[232:235], v[100:103]\n"
        "s_add_u32 m0, s41, 0\n"
        "v_mul_f32 v98, s50, v98\n"
        "v_mul_f32 v99, s50, v99\n"
        "v_mfma_f32_16x16x32_bf16 v[100:103], a[36:39], a[236:239], v[100:103]\n"
        "v_cvt_pk_bf16_f32 v176, v96, v97\n"
        "v_cvt_pk_bf16_f32 v177, v98, v99\n"
        "v_mfma_f32_16x16x32_bf16 v[100:103], a[40:43], a[240:243], v[100:103]\n"
        "buffer_load_dwordx4 v242, s[8:11], s47 offen lds\n"
        "v_mfma_f32_16x16x32_bf16 v[100:103], a[44:47], a[244:247], v[100:103]\n"
        "v_exp_f32 v64, v64\n"
        "v_mfma_f32_16x16x32_bf16 v[104:107], a[68:71], a[244:247], 0\n"
        "v_exp_f32 v65, v65\n"
        "v_mfma_f32_16x16x32_bf16 v[104:107], a[64:67], a[240:243], v[104:107]\n"
        "s_add_u32 m0, s41, 1024\n"
        "v_exp_f32 v66, v66\n"
        "v_mfma_f32_16x16x32_bf16 v[104:107], a[60:63], a[236:239], v[104:107]\n"
        "v_exp_f32 v67, v67\n"
        "v_mfma_f32_16x16x32_bf16 v[104:107], a[56:59], a[232:235], v[104:107]\n"
        "buffer_load_dwordx4 v243, s[8:11], s47 offen lds\n"
        "v_mfma_f32_16x16x32_bf16 v[104:107], a[52:55], a[228:231], v[104:107]\n"
        "v_mul_f32 v100, s50, v100\n"
        "v_mul_f32 v101, s50, v101\n"
        "v_mfma_f32_16x16x32_bf16 v[104:107], a[48:51], a[224:227], v[104:107]\n"
        "v_mul_f32 v102, s50, v102\n"
        "v_mul_f32 v103, s50, v103\n"
        "v_mfma_f32_16x16x32_bf16 v[96:99], a[224:227], a[144:147], 0\n"
        "s_add_u32 m0, s41, 2048\n"
        "v_cvt_pk_bf16_f32 v178, v100, v101\n"
        "v_cvt_pk_bf16_f32 v179, v102, v103\n"
        "v_mfma_f32_16x16x32_bf16 v[96:99], a[228:231], a[148:151], v[96:99]\n"
        "ds_read_b128 a[248:251], v240 offset:12288\n"
        "v_mul_f32 v104, s50, v104\n"
        "v_mfma_f32_16x16x32_bf16 v[96:99], a[232:235], a[152:155], v[96:99]\n"
        "buffer_load_dwordx4 v244, s[8:11], s47 offen lds\n"
        "v_mfma_f32_16x16x32_bf16 v[96:99], a[236:239], a[156:159], v[96:99]\n"
        "v_mul_f32 v105, s50, v105\n"
        "v_mul_f32 v106, s50, v106\n"
        "v_mfma_f32_16x16x32_bf16 v[96:99], a[240:243], a[160:163], v[96:99]\n"
        "v_mul_f32 v107, s50, v107\n"
        "v_cvt_pk_bf16_f32 v180, v104, v105\n"
        "v_mfma_f32_16x16x32_bf16 v[96:99], a[244:247], a[164:167], v[96:99]\n"
        "s_add_u32 m0, s41, 12288\n"
        "ds_read_b128 a[216:219], v241 offset:12288\n"
        "v_cvt_pk_bf16_f32 v181, v106, v107\n"
        "v_mfma_f32_16x16x32_bf16 v[72:75], v[152:155], v[176:179], v[16:19]\n"
        "s_waitcnt lgkmcnt(2)\n"
        "ds_read_b128 a[220:223], v240 offset:12416\n"
        "v_mov_b64 v[186:187], v[180:181]\n"
        "v_mfma_f32_16x16x32_bf16 v[72:75], v[168:171], v[180:183], v[72:75]\n"
        "buffer_load_dwordx4 v242, s[8:11], s35 offen lds\n"
        "v_mfma_f32_16x16x32_bf16 v[76:79], v[172:175], v[176:179], v[20:23]\n"
        "v_cvt_pk_bf16_f32 v130, v96, v97\n"
        "v_cvt_pk_bf16_f32 v131, v98, v99\n"
        "v_mfma_f32_16x16x32_bf16 v[76:79], v[188:191], v[184:187], v[76:79]\n"
        "v_exp_f32 v68, v68\n"
        "v_mfma_f32_16x16x32_bf16 v[100:103], a[244:247], a[188:191], 0\n"
        "s_add_u32 m0, s41, 13312\n"
        "v_exp_f32 v69, v69\n"
        "v_mfma_f32_16x16x32_bf16 v[100:103], a[240:243], a[184:187], v[100:103]\n"
        "v_exp_f32 v70, v70\n"
        "v_mfma_f32_16x16x32_bf16 v[100:103], a[236:239], a[180:183], v[100:103]\n"
        "buffer_load_dwordx4 v243, s[8:11], s35 offen lds\n"
        "v_mfma_f32_16x16x32_bf16 v[100:103], a[232:235], a[176:179], v[100:103]\n"
        "v_exp_f32 v71, v71\n"
        "v_mfma_f32_16x16x32_bf16 v[100:103], a[228:231], a[172:175], v[100:103]\n"
        "v_exp_f32 v72, v72\n"
        "v_mfma_f32_16x16x32_bf16 v[100:103], a[224:227], a[168:171], v[100:103]\n"
        "s_add_u32 m0, s41, 14336\n"
        "v_exp_f32 v73, v73\n"
        "v_mfma_f32_16x16x32_bf16 v[104:107], a[224:227], a[192:195], 0\n"
        "ds_read_b128 a[224:227], v241 offset:12416\n"
        "v_cvt_pk_bf16_f32 v64, v64, v65\n"
        "v_mfma_f32_16x16x32_bf16 v[104:107], a[228:231], a[196:199], v[104:107]\n"
        "ds_read_b128 a[228:231], v240 offset:12544\n"
        "v_cvt_pk_bf16_f32 v65, v66, v67\n"
        "v_mfma_f32_16x16x32_bf16 v[104:107], a[232:235], a[200:203], v[104:107]\n"
        "ds_read_b128 a[232:235], v241 offset:12544\n"
        "v_cvt_pk_bf16_f32 v138, v100, v101\n"
        "v_mfma_f32_16x16x32_bf16 v[104:107], a[236:239], a[204:207], v[104:107]\n"
        "buffer_load_dwordx4 v244, s[8:11], s35 offen lds\n"
        "v_mfma_f32_16x16x32_bf16 v[104:107], a[240:243], a[208:211], v[104:107]\n"
        "s_mov_b32 m0, s48\n"
        "v_cvt_pk_bf16_f32 v139, v102, v103\n"
        "v_cvt_pk_bf16_f32 v66, v68, v69\n"
        "v_mfma_f32_16x16x32_bf16 v[104:107], a[244:247], a[212:215], v[104:107]\n"
        "s_waitcnt lgkmcnt(5)\n"
        "v_exp_f32 v74, v74\n"
        "v_mfma_f32_16x16x32_bf16 v[96:99], a[72:75], a[248:251], 0\n"
        "s_waitcnt lgkmcnt(4)\n"
        "buffer_load_dwordx4 v249, s[12:15], s36 offen offset:0 lds\n"
        "v_mfma_f32_16x16x32_bf16 v[96:99], a[76:79], a[216:219], v[96:99]\n"
        "s_waitcnt lgkmcnt(3)\n"
        "ds_read_b128 v[162:165], v252 offset:0\n"
        "v_xor_b32 v116, 32, v249\n"
        "v_mfma_f32_16x16x32_bf16 v[96:99], a[80:83], a[220:223], v[96:99]\n"
        "s_waitcnt lgkmcnt(3)\n"
        "ds_read_b128 v[182:185], v252 offset:512\n"
        "v_cvt_pk_bf16_f32 v146, v104, v105\n"
        "v_mfma_f32_16x16x32_bf16 v[96:99], a[84:87], a[224:227], v[96:99]\n"
        "s_waitcnt lgkmcnt(3)\n"
        "v_cvt_pk_bf16_f32 v147, v106, v107\n"
        "v_cvt_pk_bf16_f32 v67, v70, v71\n"
        "v_mfma_f32_16x16x32_bf16 v[96:99], a[88:91], a[228:231], v[96:99]\n"
        "s_waitcnt lgkmcnt(2)\n"
        "v_exp_f32 v75, v75\n"
        "v_mfma_f32_16x16x32_bf16 v[96:99], a[92:95], a[232:235], v[96:99]\n"
        "buffer_load_dwordx4 v116, s[12:15], s36 offen offset:1024 lds\n"
        "v_mfma_f32_16x16x32_bf16 v[100:103], a[116:119], a[232:235], 0\n"
        "v_exp_f32 v76, v76\n"
        "v_mfma_f32_16x16x32_bf16 v[100:103], a[112:115], a[228:231], v[100:103]\n"
        "v_exp_f32 v77, v77\n"
        "v_mfma_f32_16x16x32_bf16 v[100:103], a[108:111], a[224:227], v[100:103]\n"
        "v_exp_f32 v78, v78\n"
        "v_mfma_f32_16x16x32_bf16 v[100:103], a[104:107], a[220:223], v[100:103]\n"
        "v_cvt_pk_bf16_f32 v192, v96, v97\n"
        "v_cvt_pk_bf16_f32 v193, v98, v99\n"
        "v_mfma_f32_16x16x32_bf16 v[100:103], a[100:103], a[216:219], v[100:103]\n"
        "v_exp_f32 v79, v79\n"
        "v_mfma_f32_16x16x32_bf16 v[100:103], a[96:99], a[248:251], v[100:103]\n"
        "v_cvt_pk_bf16_f32 v72, v72, v73\n"
        "v_cvt_pk_bf16_f32 v73, v74, v75\n"
        "v_mfma_f32_16x16x32_bf16 v[104:107], a[120:123], a[248:251], 0\n"
        "v_cvt_pk_bf16_f32 v74, v76, v77\n"
        "v_cvt_pk_bf16_f32 v75, v78, v79\n"
        "v_mfma_f32_16x16x32_bf16 v[104:107], a[124:127], a[216:219], v[104:107]\n"
        "v_mfma_f32_16x16x32_bf16 v[104:107], a[128:131], a[220:223], v[104:107]\n"
        "v_mfma_f32_16x16x32_bf16 v[104:107], a[132:135], a[224:227], v[104:107]\n"
        "v_cvt_pk_bf16_f32 v194, v100, v101\n"
        "v_cvt_pk_bf16_f32 v195, v102, v103\n"
        "v_mfma_f32_16x16x32_bf16 v[104:107], a[136:139], a[228:231], v[104:107]\n"
        "v_mfma_f32_16x16x32_bf16 v[104:107], a[140:143], a[232:235], v[104:107]\n"
        "v_mfma_f32_16x16x32_bf16 v[96:99], a[20:23], a[232:235], 0\n"
        "v_mfma_f32_16x16x32_bf16 v[96:99], a[16:19], a[228:231], v[96:99]\n"
        "v_mfma_f32_16x16x32_bf16 v[96:99], a[12:15], a[224:227], v[96:99]\n"
        "v_mfma_f32_16x16x32_bf16 v[96:99], a[8:11], a[220:223], v[96:99]\n"
        "v_mfma_f32_16x16x32_bf16 v[96:99], a[4:7], a[216:219], v[96:99]\n"
        "ds_read_b128 v[202:205], v251 offset:4096\n"
        "v_mfma_f32_16x16x32_bf16 v[96:99], a[0:3], a[248:251], v[96:99]\n"
        "v_mfma_f32_16x16x32_bf16 v[68:71], v[192:195], v[156:159], v[8:11]\n"
        "s_waitcnt lgkmcnt(2)\n"
        "v_cvt_pk_bf16_f32 v208, v104, v105\n"
        "v_cvt_pk_bf16_f32 v209, v106, v107\n"
        "s_nop 1\n"
        "v_mfma_f32_16x16x32_bf16 v[68:71], v[208:211], v[160:163], v[68:71]\n"
        "v_mfma_f32_16x16x32_bf16 v[76:79], v[192:195], v[176:179], v[24:27]\n"
        "s_waitcnt lgkmcnt(1)\n"
        "v_mfma_f32_16x16x32_bf16 v[76:79], v[208:211], v[180:183], v[76:79]\n"
        "v_mul_f32 v96, s50, v96\n"
        "v_mul_f32 v97, s50, v97\n"
        "v_mfma_f32_16x16x32_bf16 v[100:103], a[24:27], a[248:251], 0\n"
        "v_mul_f32 v98, s50, v98\n"
        "v_mul_f32 v99, s50, v99\n"
        "v_mfma_f32_16x16x32_bf16 v[100:103], a[28:31], a[216:219], v[100:103]\n"
        "v_cvt_pk_bf16_f32 v196, v96, v97\n"
        "v_cvt_pk_bf16_f32 v197, v98, v99\n"
        "v_mfma_f32_16x16x32_bf16 v[100:103], a[32:35], a[220:223], v[100:103]\n"
        "v_exp_f32 v68, v68\n"
        "v_mfma_f32_16x16x32_bf16 v[100:103], a[36:39], a[224:227], v[100:103]\n"
        "v_exp_f32 v69, v69\n"
        "v_mfma_f32_16x16x32_bf16 v[100:103], a[40:43], a[228:231], v[100:103]\n"
        "v_exp_f32 v70, v70\n"
        "v_mfma_f32_16x16x32_bf16 v[100:103], a[44:47], a[232:235], v[100:103]\n"
        "v_exp_f32 v71, v71\n"
        "v_mfma_f32_16x16x32_bf16 v[104:107], a[68:71], a[232:235], 0\n"
        "v_exp_f32 v76, v76\n"
        "v_mfma_f32_16x16x32_bf16 v[104:107], a[64:67], a[228:231], v[104:107]\n"
        "v_exp_f32 v77, v77\n"
        "v_mfma_f32_16x16x32_bf16 v[104:107], a[60:63], a[224:227], v[104:107]\n"
        "v_exp_f32 v78, v78\n"
        "v_mfma_f32_16x16x32_bf16 v[104:107], a[56:59], a[220:223], v[104:107]\n"
        "v_mul_f32 v100, s50, v100\n"
        "v_mul_f32 v101, s50, v101\n"
        "v_mfma_f32_16x16x32_bf16 v[104:107], a[52:55], a[216:219], v[104:107]\n"
        "v_mul_f32 v102, s50, v102\n"
        "v_mul_f32 v103, s50, v103\n"
        "v_mfma_f32_16x16x32_bf16 v[104:107], a[48:51], a[248:251], v[104:107]\n"
        "v_cvt_pk_bf16_f32 v198, v100, v101\n"
        "v_cvt_pk_bf16_f32 v199, v102, v103\n"
        "v_mfma_f32_16x16x32_bf16 v[96:99], a[248:251], a[144:147], 0\n"
        "ds_read_b128 a[236:239], v240 offset:13824\n"
        "v_cvt_pk_bf16_f32 v68, v68, v69\n"
        "v_mfma_f32_16x16x32_bf16 v[96:99], a[216:219], a[148:151], v[96:99]\n"
        "ds_read_b128 a[240:243], v241 offset:13824\n"
        "v_cvt_pk_bf16_f32 v69, v70, v71\n"
        "v_mfma_f32_16x16x32_bf16 v[96:99], a[220:223], a[152:155], v[96:99]\n"
        "v_mul_f32 v104, s50, v104\n"
        "v_mul_f32 v105, s50, v105\n"
        "v_mfma_f32_16x16x32_bf16 v[96:99], a[224:227], a[156:159], v[96:99]\n"
        "v_mul_f32 v106, s50, v106\n"
        "v_mul_f32 v107, s50, v107\n"
        "v_mfma_f32_16x16x32_bf16 v[96:99], a[228:231], a[160:163], v[96:99]\n"
        "v_cvt_pk_bf16_f32 v200, v104, v105\n"
        "v_cvt_pk_bf16_f32 v201, v106, v107\n"
        "v_mfma_f32_16x16x32_bf16 v[96:99], a[232:235], a[164:167], v[96:99]\n"
        "ds_read_b128 a[244:247], v240 offset:13952\n"
        "v_mov_b64 v[206:207], v[200:201]\n"
        "v_mfma_f32_16x16x32_bf16 v[80:83], v[152:155], v[196:199], v[32:35]\n"
        "s_waitcnt lgkmcnt(3)\n"
        "v_exp_f32 v79, v79\n"
        "v_mfma_f32_16x16x32_bf16 v[80:83], v[168:171], v[200:203], v[80:83]\n"
        "v_cvt_pk_bf16_f32 v76, v76, v77\n"
        "v_cvt_pk_bf16_f32 v77, v78, v79\n"
        "v_mfma_f32_16x16x32_bf16 v[84:87], v[172:175], v[196:199], v[36:39]\n"
        "v_cvt_pk_bf16_f32 v132, v96, v97\n"
        "v_cvt_pk_bf16_f32 v133, v98, v99\n"
        "v_mfma_f32_16x16x32_bf16 v[84:87], v[188:191], v[204:207], v[84:87]\n"
        "ds_read_b128 v[202:205], v252 offset:4096\n"
        "v_mfma_f32_16x16x32_bf16 v[100:103], a[232:235], a[188:191], 0\n"
        "v_exp_f32 v80, v80\n"
        "v_mfma_f32_16x16x32_bf16 v[100:103], a[228:231], a[184:187], v[100:103]\n"
        "v_exp_f32 v81, v81\n"
        "v_mfma_f32_16x16x32_bf16 v[100:103], a[224:227], a[180:183], v[100:103]\n"
        "v_exp_f32 v82, v82\n"
        "v_mfma_f32_16x16x32_bf16 v[100:103], a[220:223], a[176:179], v[100:103]\n"
        "v_exp_f32 v83, v83\n"
        "v_mfma_f32_16x16x32_bf16 v[100:103], a[216:219], a[172:175], v[100:103]\n"
        "v_exp_f32 v84, v84\n"
        "v_mfma_f32_16x16x32_bf16 v[100:103], a[248:251], a[168:171], v[100:103]\n"
        "v_exp_f32 v85, v85\n"
        "v_mfma_f32_16x16x32_bf16 v[104:107], a[248:251], a[192:195], 0\n"
        "ds_read_b128 a[248:251], v241 offset:13952\n"
        "v_cvt_pk_bf16_f32 v80, v80, v81\n"
        "v_mfma_f32_16x16x32_bf16 v[104:107], a[216:219], a[196:199], v[104:107]\n"
        "ds_read_b128 a[216:219], v240 offset:14080\n"
        "v_cvt_pk_bf16_f32 v81, v82, v83\n"
        "v_mfma_f32_16x16x32_bf16 v[104:107], a[220:223], a[200:203], v[104:107]\n"
        "ds_read_b128 a[220:223], v241 offset:14080\n"
        "v_cvt_pk_bf16_f32 v140, v100, v101\n"
        "v_mfma_f32_16x16x32_bf16 v[104:107], a[224:227], a[204:207], v[104:107]\n"
        "v_cvt_pk_bf16_f32 v141, v102, v103\n"
        "v_cvt_pk_bf16_f32 v82, v84, v85\n"
        "v_mfma_f32_16x16x32_bf16 v[104:107], a[228:231], a[208:211], v[104:107]\n"
        "v_exp_f32 v86, v86\n"
        "v_mfma_f32_16x16x32_bf16 v[104:107], a[232:235], a[212:215], v[104:107]\n"
        "s_waitcnt lgkmcnt(6)\n"
        "v_exp_f32 v87, v87\n"
        "v_mfma_f32_16x16x32_bf16 v[96:99], a[72:75], a[236:239], 0\n"
        "s_waitcnt lgkmcnt(5)\n"
        "v_cvt_pk_bf16_f32 v83, v86, v87\n"
        "v_mfma_f32_16x16x32_bf16 v[96:99], a[76:79], a[240:243], v[96:99]\n"
        "s_waitcnt lgkmcnt(4)\n"
        "v_mfma_f32_16x16x32_bf16 v[96:99], a[80:83], a[244:247], v[96:99]\n"
        "s_waitcnt lgkmcnt(2)\n"
        "v_mfma_f32_16x16x32_bf16 v[96:99], a[84:87], a[248:251], v[96:99]\n"
        "s_waitcnt lgkmcnt(1)\n"
        "v_mfma_f32_16x16x32_bf16 v[96:99], a[88:91], a[216:219], v[96:99]\n"
        "s_waitcnt lgkmcnt(0)\n"
        "v_mfma_f32_16x16x32_bf16 v[96:99], a[92:95], a[220:223], v[96:99]\n"
        "v_cvt_pk_bf16_f32 v148, v104, v105\n"
        "v_cvt_pk_bf16_f32 v149, v106, v107\n"
        "v_mfma_f32_16x16x32_bf16 v[84:87], v[192:195], v[196:199], v[40:43]\n"
        "v_mfma_f32_16x16x32_bf16 v[84:87], v[208:211], v[200:203], v[84:87]\n"
        "v_mfma_f32_16x16x32_bf16 v[100:103], a[116:119], a[220:223], 0\n"
        "v_add_u32 v240, s45, v240\n"
        "v_add_u32 v241, s45, v241\n"
        "v_mfma_f32_16x16x32_bf16 v[100:103], a[112:115], a[216:219], v[100:103]\n"
        "v_cvt_pk_bf16_f32 v212, v96, v97\n"
        "v_cvt_pk_bf16_f32 v213, v98, v99\n"
        "v_mfma_f32_16x16x32_bf16 v[100:103], a[108:111], a[248:251], v[100:103]\n"
        "v_mfma_f32_16x16x32_bf16 v[100:103], a[104:107], a[244:247], v[100:103]\n"
        "v_exp_f32 v84, v84\n"
        "v_mfma_f32_16x16x32_bf16 v[100:103], a[100:103], a[240:243], v[100:103]\n"
        "v_exp_f32 v85, v85\n"
        "v_mfma_f32_16x16x32_bf16 v[100:103], a[96:99], a[236:239], v[100:103]\n"
        "v_exp_f32 v86, v86\n"
        "v_mfma_f32_16x16x32_bf16 v[104:107], a[120:123], a[236:239], 0\n"
        "v_exp_f32 v87, v87\n"
        "v_mfma_f32_16x16x32_bf16 v[104:107], a[124:127], a[240:243], v[104:107]\n"
        "v_cvt_pk_bf16_f32 v84, v84, v85\n"
        "v_cvt_pk_bf16_f32 v85, v86, v87\n"
        "v_mfma_f32_16x16x32_bf16 v[104:107], a[128:131], a[244:247], v[104:107]\n"
        "v_mfma_f32_16x16x32_bf16 v[104:107], a[132:135], a[248:251], v[104:107]\n"
        "v_cvt_pk_bf16_f32 v214, v100, v101\n"
        "v_cvt_pk_bf16_f32 v215, v102, v103\n"
        "v_mfma_f32_16x16x32_bf16 v[104:107], a[136:139], a[216:219], v[104:107]\n"
        "s_barrier\n"
        "v_mfma_f32_16x16x32_bf16 v[104:107], a[140:143], a[220:223], v[104:107]\n"
        "v_mfma_f32_16x16x32_bf16 v[96:99], a[20:23], a[220:223], 0\n"
        "v_mfma_f32_16x16x32_bf16 v[96:99], a[16:19], a[216:219], v[96:99]\n"
        "v_mfma_f32_16x16x32_bf16 v[96:99], a[12:15], a[248:251], v[96:99]\n"
        "s_cmp_eq_u32 s37, 100663296\n"
        "v_mfma_f32_16x16x32_bf16 v[96:99], a[8:11], a[244:247], v[96:99]\n"
        "s_cselect_b32 s46, 0, s37\n"
        "v_mfma_f32_16x16x32_bf16 v[96:99], a[4:7], a[240:243], v[96:99]\n"
        "s_add_u32 s38, s46, s43\n"
        "v_cvt_pk_bf16_f32 v230, v104, v105\n"
        "v_cvt_pk_bf16_f32 v231, v106, v107\n"
        "v_mfma_f32_16x16x32_bf16 v[96:99], a[0:3], a[236:239], v[96:99]\n"
        "s_add_u32 s39, s46, s44\n"
        "v_mfma_f32_16x16x32_bf16 v[108:111], v[212:215], v[156:159], v[12:15]\n"
        "s_add_u32 s37, s37, 6291456\n"
        "ds_read_b128 v[222:225], v251 offset:4608\n"
        "v_mfma_f32_16x16x32_bf16 v[108:111], v[228:231], v[164:167], v[108:111]\n"
        "v_mfma_f32_16x16x32_bf16 v[112:115], v[212:215], v[176:179], v[28:31]\n"
        "v_mfma_f32_16x16x32_bf16 v[112:115], v[228:231], v[184:187], v[112:115]\n"
        "v_mfma_f32_16x16x32_bf16 v[116:119], v[212:215], v[196:199], v[44:47]\n"
        "v_mul_f32 v96, s50, v96\n"
        "v_mul_f32 v97, s50, v97\n"
        "v_mfma_f32_16x16x32_bf16 v[116:119], v[228:231], v[204:207], v[116:119]\n"
        "v_mul_f32 v98, s50, v98\n"
        "v_mul_f32 v99, s50, v99\n"
        "v_mfma_f32_16x16x32_bf16 v[100:103], a[24:27], a[236:239], 0\n"
        "v_cvt_pk_bf16_f32 v216, v96, v97\n"
        "v_cvt_pk_bf16_f32 v217, v98, v99\n"
        "v_mfma_f32_16x16x32_bf16 v[100:103], a[28:31], a[240:243], v[100:103]\n"
        "v_exp_f32 v108, v108\n"
        "v_mfma_f32_16x16x32_bf16 v[100:103], a[32:35], a[244:247], v[100:103]\n"
        "v_exp_f32 v109, v109\n"
        "v_mfma_f32_16x16x32_bf16 v[100:103], a[36:39], a[248:251], v[100:103]\n"
        "v_exp_f32 v110, v110\n"
        "v_mfma_f32_16x16x32_bf16 v[100:103], a[40:43], a[216:219], v[100:103]\n"
        "v_exp_f32 v111, v111\n"
        "v_mfma_f32_16x16x32_bf16 v[100:103], a[44:47], a[220:223], v[100:103]\n"
        "v_exp_f32 v112, v112\n"
        "v_mfma_f32_16x16x32_bf16 v[104:107], a[68:71], a[220:223], 0\n"
        "v_exp_f32 v113, v113\n"
        "v_mfma_f32_16x16x32_bf16 v[104:107], a[64:67], a[216:219], v[104:107]\n"
        "v_exp_f32 v114, v114\n"
        "v_mfma_f32_16x16x32_bf16 v[104:107], a[60:63], a[248:251], v[104:107]\n"
        "v_exp_f32 v115, v115\n"
        "v_mfma_f32_16x16x32_bf16 v[104:107], a[56:59], a[244:247], v[104:107]\n"
        "v_mul_f32 v100, s50, v100\n"
        "v_mul_f32 v101, s50, v101\n"
        "v_mfma_f32_16x16x32_bf16 v[104:107], a[52:55], a[240:243], v[104:107]\n"
        "v_mul_f32 v102, s50, v102\n"
        "v_mul_f32 v103, s50, v103\n"
        "v_mfma_f32_16x16x32_bf16 v[104:107], a[48:51], a[236:239], v[104:107]\n"
        "v_cvt_pk_bf16_f32 v218, v100, v101\n"
        "v_cvt_pk_bf16_f32 v219, v102, v103\n"
        "v_mfma_f32_16x16x32_bf16 v[96:99], a[236:239], a[144:147], 0\n"
        "v_exp_f32 v116, v116\n"
        "v_mfma_f32_16x16x32_bf16 v[96:99], a[240:243], a[148:151], v[96:99]\n"
        "v_exp_f32 v117, v117\n"
        "v_mfma_f32_16x16x32_bf16 v[96:99], a[244:247], a[152:155], v[96:99]\n"
        "v_exp_f32 v118, v118\n"
        "v_mfma_f32_16x16x32_bf16 v[96:99], a[248:251], a[156:159], v[96:99]\n"
        "v_mul_f32 v104, s50, v104\n"
        "v_mul_f32 v105, s50, v105\n"
        "v_mfma_f32_16x16x32_bf16 v[96:99], a[216:219], a[160:163], v[96:99]\n"
        "v_mul_f32 v106, s50, v106\n"
        "v_mul_f32 v107, s50, v107\n"
        "v_mfma_f32_16x16x32_bf16 v[96:99], a[220:223], a[164:167], v[96:99]\n"
        "v_cvt_pk_bf16_f32 v220, v104, v105\n"
        "v_cvt_pk_bf16_f32 v221, v106, v107\n"
        "v_mfma_f32_16x16x32_bf16 v[88:91], v[152:155], v[216:219], v[48:51]\n"
        "s_waitcnt lgkmcnt(0)\n"
        "ds_read_b128 v[156:159], v247 offset:0\n"
        "v_mov_b64 v[226:227], v[220:221]\n"
        "v_mfma_f32_16x16x32_bf16 v[88:91], v[168:171], v[220:223], v[88:91]\n"
        "ds_read_b128 v[164:167], v247 offset:4096\n"
        "v_cvt_pk_bf16_f32 v70, v108, v109\n"
        "v_mfma_f32_16x16x32_bf16 v[92:95], v[172:175], v[216:219], v[52:55]\n"
        "s_add_u32 s47, s47, 6291456\n"
        "ds_read_b128 v[160:163], v247 offset:256\n"
        "v_cvt_pk_bf16_f32 v134, v96, v97\n"
        "v_mfma_f32_16x16x32_bf16 v[92:95], v[188:191], v[224:227], v[92:95]\n"
        "s_add_u32 s36, s36, 2097152\n"
        "ds_read_b128 v[222:225], v252 offset:4608\n"
        "v_cvt_pk_bf16_f32 v135, v98, v99\n"
        "v_mfma_f32_16x16x32_bf16 v[100:103], a[220:223], a[188:191], 0\n"
        "s_xor_b32 s48, s48, 8192\n"
        "ds_read_b128 v[152:155], v247 offset:4352\n"
        "v_cvt_pk_bf16_f32 v71, v110, v111\n"
        "v_mfma_f32_16x16x32_bf16 v[100:103], a[216:219], a[184:187], v[100:103]\n"
        "v_exp_f32 v119, v119\n"
        "v_mfma_f32_16x16x32_bf16 v[100:103], a[248:251], a[180:183], v[100:103]\n"
        "v_exp_f32 v88, v88\n"
        "v_mfma_f32_16x16x32_bf16 v[100:103], a[244:247], a[176:179], v[100:103]\n"
        "v_exp_f32 v89, v89\n"
        "v_mfma_f32_16x16x32_bf16 v[100:103], a[240:243], a[172:175], v[100:103]\n"
        "v_exp_f32 v90, v90\n"
        "v_mfma_f32_16x16x32_bf16 v[100:103], a[236:239], a[168:171], v[100:103]\n"
        "v_exp_f32 v91, v91\n"
        "v_mfma_f32_16x16x32_bf16 v[104:107], a[236:239], a[192:195], 0\n"
        "ds_read_b128 a[236:239], v248 offset:0\n"
        "v_cvt_pk_bf16_f32 v78, v112, v113\n"
        "v_mfma_f32_16x16x32_bf16 v[104:107], a[240:243], a[196:199], v[104:107]\n"
        "ds_read_b128 a[240:243], v248 offset:6144\n"
        "v_cvt_pk_bf16_f32 v79, v114, v115\n"
        "v_mfma_f32_16x16x32_bf16 v[104:107], a[244:247], a[200:203], v[104:107]\n"
        "v_cvt_pk_bf16_f32 v142, v100, v101\n"
        "v_cvt_pk_bf16_f32 v143, v102, v103\n"
        "v_mfma_f32_16x16x32_bf16 v[104:107], a[248:251], a[204:207], v[104:107]\n"
        "v_exp_f32 v92, v92\n"
        "v_mfma_f32_16x16x32_bf16 v[104:107], a[216:219], a[208:211], v[104:107]\n"
        "v_exp_f32 v93, v93\n"
        "v_mfma_f32_16x16x32_bf16 v[104:107], a[220:223], a[212:215], v[104:107]\n"
        "ds_read_b128 a[244:247], v254 offset:16\n"
        "v_cvt_pk_bf16_f32 v86, v116, v117\n"
        "v_mfma_f32_16x16x32_bf16 v[108:111], v[192:195], v[216:219], v[56:59]\n"
        "s_waitcnt lgkmcnt(4)\n"
        "ds_read_b128 a[248:251], v254 offset:6160\n"
        "v_mov_b64 v[116:117], 0\n"
        "v_mfma_f32_16x16x32_bf16 v[108:111], v[208:211], v[220:223], v[108:111]\n"
        "v_exp_f32 v94, v94\n"
        "v_mfma_f32_16x16x32_bf16 v[112:115], v[212:215], v[216:219], v[60:63]\n"
        "v_cvt_pk_bf16_f32 v150, v104, v105\n"
        "v_cvt_pk_bf16_f32 v151, v106, v107\n"
        "v_mfma_f32_16x16x32_bf16 v[112:115], v[228:231], v[224:227], v[112:115]\n"
        "s_waitcnt lgkmcnt(3)\n"
        "ds_read_b128 a[216:219], v248 offset:32\n"
        "v_cvt_pk_bf16_f32 v87, v118, v119\n"
        "v_mfma_f32_16x16x32_bf16 v[172:175], v[156:159], a[236:239], 0\n"
        "s_waitcnt lgkmcnt(3)\n"
        "ds_read_b128 a[220:223], v248 offset:6176\n"
        "v_cvt_pk_bf16_f32 v88, v88, v89\n"
        "v_mfma_f32_16x16x32_bf16 v[172:175], v[164:167], a[240:243], v[172:175]\n"
        "v_exp_f32 v95, v95\n"
        "v_mfma_f32_16x16x32_bf16 v[200:203], v[152:155], a[240:243], 0\n"
        "v_exp_f32 v108, v108\n"
        "v_mfma_f32_16x16x32_bf16 v[200:203], v[160:163], a[236:239], v[200:203]\n"
        "ds_read_b128 a[224:227], v254 offset:48\n"
        "v_cvt_pk_bf16_f32 v89, v90, v91\n"
        "v_smfmac_f32_16x16x64_bf16 v[116:119], a[252:255], v[64:71], v255\n"
        "ds_read_b128 a[228:231], v254 offset:6192\n"
        "v_cvt_pk_bf16_f32 v90, v92, v93\n"
        "v_mfma_f32_16x16x32_bf16 v[120:123], v[128:131], v[64:67], 0\n"
        "v_exp_f32 v109, v109\n"
        "v_mfma_f32_16x16x32_bf16 v[120:123], v[132:135], v[68:71], v[120:123]\n"
        "v_exp_f32 v110, v110\n"
        "v_mfma_f32_16x16x32_bf16 v[124:127], v[140:143], v[68:71], 0\n"
        "ds_read_b128 a[232:235], v248 offset:64\n"
        "v_add_f32 v116, v116, v117\n"
        "v_mfma_f32_16x16x32_bf16 v[124:127], v[136:139], v[64:67], v[124:127]\n"
        "ds_read_b128 a[236:239], v248 offset:6208\n"
        "v_cvt_pk_bf16_f32 v91, v94, v95\n"
        "v_mfma_f32_16x16x32_bf16 v[96:99], v[144:147], v[64:67], 0\n"
        "v_rcp_f32 v117, v116\n"
        "v_mfma_f32_16x16x32_bf16 v[96:99], v[148:151], v[68:71], v[96:99]\n"
        "s_waitcnt lgkmcnt(7)\n"
        "v_exp_f32 v111, v111\n"
        "v_mfma_f32_16x16x32_bf16 v[204:207], v[160:163], a[244:247], 0\n"
        "s_waitcnt lgkmcnt(6)\n"
        "v_exp_f32 v112, v112\n"
        "v_mfma_f32_16x16x32_bf16 v[204:207], v[152:155], a[248:251], v[204:207]\n"
        "v_exp_f32 v113, v113\n"
        "v_mfma_f32_16x16x32_bf16 v[176:179], v[164:167], a[248:251], 0\n"
        "v_exp_f32 v114, v114\n"
        "v_mfma_f32_16x16x32_bf16 v[176:179], v[156:159], a[244:247], v[176:179]\n"
        "s_waitcnt lgkmcnt(5)\n"
        "v_mul_f32 v120, v117, v120\n"
        "v_mul_f32 v121, v117, v121\n"
        "v_mfma_f32_16x16x32_bf16 v[180:183], v[156:159], a[216:219], 0\n"
        "s_waitcnt lgkmcnt(4)\n"
        "v_mov_b64 v[100:101], 0\n"
        "v_mul_f32 v122, v117, v122\n"
        "v_mfma_f32_16x16x32_bf16 v[180:183], v[164:167], a[220:223], v[180:183]\n"
        "ds_read_b128 v[232:235], v247 offset:8192\n"
        "v_mul_f32 v123, v117, v123\n"
        "v_mfma_f32_16x16x32_bf16 v[212:215], v[152:155], a[220:223], 0\n"
        "ds_read_b128 v[64:67], v247 offset:12288\n"
        "v_cvt_pk_bf16_f32 v92, v108, v109\n"
        "v_mfma_f32_16x16x32_bf16 v[212:215], v[160:163], a[216:219], v[212:215]\n"
        "s_waitcnt lgkmcnt(5)\n"
        "ds_read_b128 a[240:243], v254 offset:80\n"
        "v_cvt_pk_bf16_f32 v120, v120, v121\n"
        "v_mfma_f32_16x16x32_bf16 v[216:219], v[160:163], a[224:227], 0\n"
        "s_waitcnt lgkmcnt(5)\n"
        "ds_read_b128 a[244:247], v254 offset:6224\n"
        "v_cvt_pk_bf16_f32 v121, v122, v123\n"
        "v_mfma_f32_16x16x32_bf16 v[216:219], v[152:155], a[228:231], v[216:219]\n"
        "ds_write_b32 v245, v120 offset:0\n"
        "v_mfma_f32_16x16x32_bf16 v[184:187], v[164:167], a[228:231], 0\n"
        "v_cvt_pk_bf16_f32 v93, v110, v111\n"
        "v_mul_f32 v124, v117, v124\n"
        "v_mfma_f32_16x16x32_bf16 v[184:187], v[156:159], a[224:227], v[184:187]\n"
        "s_waitcnt lgkmcnt(6)\n"
        "ds_write_b32 v245, v121 offset:4\n"
        "v_mfma_f32_16x16x32_bf16 v[192:195], v[156:159], a[232:235], 0\n"
        "s_waitcnt lgkmcnt(6)\n"
        "v_mul_f32 v125, v117, v125\n"
        "v_maximum3_f32 v253, v253, v116, v117\n"
        "v_mfma_f32_16x16x32_bf16 v[192:195], v[164:167], a[236:239], v[192:195]\n"
        "v_mul_f32 v126, v117, v126\n"
        "v_mul_f32 v127, v117, v127\n"
        "v_mfma_f32_16x16x32_bf16 v[220:223], v[152:155], a[236:239], 0\n"
        "v_cvt_pk_bf16_f32 v94, v112, v113\n"
        "v_cvt_pk_bf16_f32 v124, v124, v125\n"
        "v_mfma_f32_16x16x32_bf16 v[220:223], v[160:163], a[232:235], v[220:223]\n"
        "v_exp_f32 v115, v115\n"
        "v_smfmac_f32_16x16x64_bf16 v[100:103], a[252:255], v[72:79], v255\n"
        "v_cvt_pk_bf16_f32 v125, v126, v127\n"
        "v_cvt_pk_bf16_f32 v95, v114, v115\n"
        "v_mfma_f32_16x16x32_bf16 v[104:107], v[128:131], v[72:75], 0\n"
        "ds_read_b128 v[236:239], v247 offset:8448\n"
        "ds_read_b128 a[248:251], v248 offset:36864\n"
        "v_mfma_f32_16x16x32_bf16 v[104:107], v[132:135], v[76:79], v[104:107]\n"
        "ds_read_b128 v[68:71], v247 offset:12544\n"
        "ds_read_b128 a[216:219], v248 offset:43008\n"
        "v_mfma_f32_16x16x32_bf16 v[108:111], v[140:143], v[76:79], 0\n"
        "ds_write_b32 v245, v124 offset:8\n"
        "v_mfma_f32_16x16x32_bf16 v[108:111], v[136:139], v[72:75], v[108:111]\n"
        "ds_write_b32 v245, v125 offset:12\n"
        "v_mfma_f32_16x16x32_bf16 v[112:115], v[144:147], v[72:75], 0\n"
        "ds_read_b128 a[220:223], v254 offset:36880\n"
        "v_mul_f32 v96, v117, v96\n"
        "v_mfma_f32_16x16x32_bf16 v[112:115], v[148:151], v[76:79], v[112:115]\n"
        "s_waitcnt lgkmcnt(10)\n"
        "ds_read_b128 a[224:227], v254 offset:43024\n"
        "v_mul_f32 v97, v117, v97\n"
        "v_mfma_f32_16x16x32_bf16 v[224:227], v[160:163], a[240:243], 0\n"
        "s_waitcnt lgkmcnt(10)\n"
        "v_mul_f32 v98, v117, v98\n"
        "v_mul_f32 v99, v117, v99\n"
        "v_mfma_f32_16x16x32_bf16 v[224:227], v[152:155], a[244:247], v[224:227]\n"
        "ds_read_b128 a[228:231], v248 offset:36896\n"
        "v_cvt_pk_bf16_f32 v96, v96, v97\n"
        "v_mfma_f32_16x16x32_bf16 v[196:199], v[164:167], a[244:247], 0\n"
        "ds_read_b128 a[232:235], v248 offset:43040\n"
        "v_cvt_pk_bf16_f32 v97, v98, v99\n"
        "v_mfma_f32_16x16x32_bf16 v[196:199], v[156:159], a[240:243], v[196:199]\n"
        "s_waitcnt lgkmcnt(8)\n"
        "ds_write_b32 v246, v96 offset:0\n"
        "v_mfma_f32_16x16x32_bf16 v[172:175], v[232:235], a[248:251], v[172:175]\n"
        "s_waitcnt lgkmcnt(7)\n"
        "ds_write_b32 v246, v97 offset:4\n"
        "v_mfma_f32_16x16x32_bf16 v[172:175], v[64:67], a[216:219], v[172:175]\n"
        "ds_read_b128 a[236:239], v254 offset:36912\n"
        "v_add_f32 v100, v100, v101\n"
        "v_mfma_f32_16x16x32_bf16 v[200:203], v[68:71], a[216:219], v[200:203]\n"
        "v_rcp_f32 v101, v100\n"
        "v_mfma_f32_16x16x32_bf16 v[200:203], v[236:239], a[248:251], v[200:203]\n"
        "s_waitcnt lgkmcnt(6)\n"
        "ds_read_b128 a[240:243], v254 offset:43056\n"
        "v_mul_f32 v104, v101, v104\n"
        "v_mfma_f32_16x16x32_bf16 v[204:207], v[236:239], a[220:223], v[204:207]\n"
        "s_waitcnt lgkmcnt(6)\n"
        "v_mul_f32 v105, v101, v105\n"
        "v_mul_f32 v106, v101, v106\n"
        "v_mfma_f32_16x16x32_bf16 v[204:207], v[68:71], a[224:227], v[204:207]\n"
        "v_mul_f32 v107, v101, v107\n"
        "v_cvt_pk_bf16_f32 v104, v104, v105\n"
        "v_mfma_f32_16x16x32_bf16 v[176:179], v[64:67], a[224:227], v[176:179]\n"
        "v_cvt_pk_bf16_f32 v105, v106, v107\n"
        "v_mov_b64 v[116:117], 0\n"
        "v_mfma_f32_16x16x32_bf16 v[176:179], v[232:235], a[220:223], v[176:179]\n"
        "s_waitcnt lgkmcnt(5)\n"
        "ds_write_b32 v245, v104 offset:256\n"
        "v_mfma_f32_16x16x32_bf16 v[180:183], v[232:235], a[228:231], v[180:183]\n"
        "s_waitcnt lgkmcnt(5)\n"
        "ds_write_b32 v245, v105 offset:260\n"
        "v_mfma_f32_16x16x32_bf16 v[180:183], v[64:67], a[232:235], v[180:183]\n"
        "v_mul_f32 v108, v101, v108\n"
        "v_mul_f32 v109, v101, v109\n"
        "v_mfma_f32_16x16x32_bf16 v[212:215], v[68:71], a[232:235], v[212:215]\n"
        "v_maximum3_f32 v253, v253, v100, v101\n"
        "v_mul_f32 v110, v101, v110\n"
        "v_mfma_f32_16x16x32_bf16 v[212:215], v[236:239], a[228:231], v[212:215]\n"
        "s_waitcnt lgkmcnt(3)\n"
        "v_mul_f32 v111, v101, v111\n"
        "v_cvt_pk_bf16_f32 v108, v108, v109\n"
        "v_mfma_f32_16x16x32_bf16 v[216:219], v[236:239], a[236:239], v[216:219]\n"
        "s_waitcnt lgkmcnt(2)\n"
        "v_cvt_pk_bf16_f32 v109, v110, v111\n"
        "v_mul_f32 v112, v101, v112\n"
        "v_mfma_f32_16x16x32_bf16 v[216:219], v[68:71], a[240:243], v[216:219]\n"
        "ds_read_b128 a[244:247], v248 offset:36928\n"
        "v_mul_f32 v113, v101, v113\n"
        "v_mfma_f32_16x16x32_bf16 v[184:187], v[64:67], a[240:243], v[184:187]\n"
        "ds_read_b128 a[248:251], v248 offset:43072\n"
        "v_mul_f32 v114, v101, v114\n"
        "v_mfma_f32_16x16x32_bf16 v[184:187], v[232:235], a[236:239], v[184:187]\n"
        "ds_write_b32 v245, v108 offset:264\n"
        "v_smfmac_f32_16x16x64_bf16 v[116:119], a[252:255], v[80:87], v255\n"
        "ds_write_b32 v245, v109 offset:268\n"
        "v_mfma_f32_16x16x32_bf16 v[120:123], v[128:131], v[80:83], 0\n"
        "ds_read_b128 a[216:219], v254 offset:36944\n"
        "v_mul_f32 v115, v101, v115\n"
        "v_mfma_f32_16x16x32_bf16 v[120:123], v[132:135], v[84:87], v[120:123]\n"
        "ds_read_b128 a[220:223], v254 offset:43088\n"
        "v_cvt_pk_bf16_f32 v112, v112, v113\n"
        "v_mfma_f32_16x16x32_bf16 v[124:127], v[140:143], v[84:87], 0\n"
        "ds_read_b128 v[156:159], v247 offset:16384\n"
        "v_cvt_pk_bf16_f32 v113, v114, v115\n"
        "v_mfma_f32_16x16x32_bf16 v[124:127], v[136:139], v[80:83], v[124:127]\n"
        "ds_read_b128 v[164:167], v247 offset:20480\n"
        "v_add_f32 v116, v116, v117\n"
        "v_mfma_f32_16x16x32_bf16 v[96:99], v[144:147], v[80:83], 0\n"
        "ds_read_b128 v[160:163], v247 offset:16640\n"
        "ds_read_b128 a[224:227], v248 offset:12288\n"
        "v_mfma_f32_16x16x32_bf16 v[96:99], v[148:151], v[84:87], v[96:99]\n"
        "s_waitcnt lgkmcnt(9)\n"
        "ds_read_b128 v[152:155], v247 offset:20736\n"
        "ds_read_b128 a[228:231], v248 offset:49152\n"
        "v_mfma_f32_16x16x32_bf16 v[192:195], v[232:235], a[244:247], v[192:195]\n"
        "s_waitcnt lgkmcnt(10)\n"
        "ds_write_b32 v246, v112 offset:256\n"
        "v_mfma_f32_16x16x32_bf16 v[192:195], v[64:67], a[248:251], v[192:195]\n"
        "ds_write_b32 v246, v113 offset:260\n"
        "v_mfma_f32_16x16x32_bf16 v[220:223], v[68:71], a[248:251], v[220:223]\n"
        "ds_read_b128 a[232:235], v254 offset:12304\n"
        "v_add_u32 v247, s31, v247\n"
        "v_mfma_f32_16x16x32_bf16 v[220:223], v[236:239], a[244:247], v[220:223]\n"
        "s_waitcnt lgkmcnt(10)\n"
        "ds_read_b128 a[236:239], v254 offset:49168\n"
        "v_mfma_f32_16x16x32_bf16 v[224:227], v[236:239], a[216:219], v[224:227]\n"
        "s_waitcnt lgkmcnt(10)\n"
        "v_rcp_f32 v117, v116\n"
        "v_mfma_f32_16x16x32_bf16 v[224:227], v[68:71], a[220:223], v[224:227]\n"
        "v_mul_f32 v120, v117, v120\n"
        "v_mul_f32 v121, v117, v121\n"
        "v_mfma_f32_16x16x32_bf16 v[196:199], v[64:67], a[220:223], v[196:199]\n"
        "ds_read_b128 a[240:243], v248 offset:12320\n"
        "v_mul_f32 v122, v117, v122\n"
        "v_mfma_f32_16x16x32_bf16 v[196:199], v[232:235], a[216:219], v[196:199]\n"
        "s_waitcnt lgkmcnt(7)\n"
        "ds_read_b128 a[244:247], v248 offset:49184\n"
        "v_mul_f32 v123, v117, v123\n"
        "v_mfma_f32_16x16x32_bf16 v[172:175], v[156:159], a[224:227], v[172:175]\n"
        "s_waitcnt lgkmcnt(6)\n"
        "ds_read_b128 a[248:251], v254 offset:12336\n"
        "v_cvt_pk_bf16_f32 v120, v120, v121\n"
        "v_mfma_f32_16x16x32_bf16 v[172:175], v[164:167], a[228:231], v[172:175]\n"
        "ds_read_b128 a[216:219], v254 offset:49200\n"
        "v_cvt_pk_bf16_f32 v121, v122, v123\n"
        "v_mfma_f32_16x16x32_bf16 v[200:203], v[152:155], a[228:231], v[200:203]\n"
        "ds_write_b32 v245, v120 offset:512\n"
        "v_mfma_f32_16x16x32_bf16 v[200:203], v[160:163], a[224:227], v[200:203]\n"
        "s_waitcnt lgkmcnt(6)\n"
        "ds_write_b32 v245, v121 offset:516\n"
        "v_mfma_f32_16x16x32_bf16 v[204:207], v[160:163], a[232:235], v[204:207]\n"
        "s_waitcnt lgkmcnt(6)\n"
        "v_mul_f32 v124, v117, v124\n"
        "v_mul_f32 v125, v117, v125\n"
        "v_mfma_f32_16x16x32_bf16 v[204:207], v[152:155], a[236:239], v[204:207]\n"
        "v_maximum3_f32 v253, v253, v116, v117\n"
        "v_mul_f32 v126, v117, v126\n"
        "v_mfma_f32_16x16x32_bf16 v[176:179], v[164:167], a[236:239], v[176:179]\n"
        "ds_read_b128 a[220:223], v248 offset:12352\n"
        "v_mul_f32 v127, v117, v127\n"
        "v_mfma_f32_16x16x32_bf16 v[176:179], v[156:159], a[232:235], v[176:179]\n"
        "s_waitcnt lgkmcnt(6)\n"
        "ds_read_b128 a[224:227], v248 offset:49216\n"
        "v_cvt_pk_bf16_f32 v124, v124, v125\n"
        "v_mfma_f32_16x16x32_bf16 v[180:183], v[156:159], a[240:243], v[180:183]\n"
        "s_waitcnt lgkmcnt(6)\n"
        "v_cvt_pk_bf16_f32 v125, v126, v127\n"
        "v_mul_f32 v96, v117, v96\n"
        "v_mfma_f32_16x16x32_bf16 v[180:183], v[164:167], a[244:247], v[180:183]\n"
        "ds_write_b32 v245, v124 offset:520\n"
        "v_mfma_f32_16x16x32_bf16 v[212:215], v[152:155], a[244:247], v[212:215]\n"
        "ds_read_b128 a[228:231], v254 offset:12368\n"
        "v_mul_f32 v97, v117, v97\n"
        "v_mfma_f32_16x16x32_bf16 v[212:215], v[160:163], a[240:243], v[212:215]\n"
        "s_waitcnt lgkmcnt(7)\n"
        "ds_read_b128 a[232:235], v254 offset:49232\n"
        "v_mul_f32 v98, v117, v98\n"
        "v_mfma_f32_16x16x32_bf16 v[216:219], v[160:163], a[248:251], v[216:219]\n"
        "s_waitcnt lgkmcnt(7)\n"
        "ds_write_b32 v245, v125 offset:524\n"
        "v_mfma_f32_16x16x32_bf16 v[216:219], v[152:155], a[216:219], v[216:219]\n"
        "v_mul_f32 v99, v117, v99\n"
        "v_cvt_pk_bf16_f32 v96, v96, v97\n"
        "v_mfma_f32_16x16x32_bf16 v[184:187], v[164:167], a[216:219], v[184:187]\n"
        "v_cvt_pk_bf16_f32 v97, v98, v99\n"
        "v_mov_b64 v[80:81], 0\n"
        "v_mfma_f32_16x16x32_bf16 v[184:187], v[156:159], a[248:251], v[184:187]\n"
        "s_waitcnt lgkmcnt(5)\n"
        "s_waitcnt vmcnt(0)\n"
        "s_barrier\n"
        "ds_write_b32 v246, v96 offset:512\n"
        "v_mfma_f32_16x16x32_bf16 v[192:195], v[156:159], a[220:223], v[192:195]\n"
        "s_waitcnt lgkmcnt(5)\n"
        "ds_write_b32 v246, v97 offset:516\n"
        "v_mfma_f32_16x16x32_bf16 v[192:195], v[164:167], a[224:227], v[192:195]\n"
        "ds_read_b128 a[236:239], v240 offset:0\n"
        "v_mfma_f32_16x16x32_bf16 v[220:223], v[152:155], a[224:227], v[220:223]\n"
        "ds_read_b128 a[240:243], v241 offset:0\n"
        "v_mfma_f32_16x16x32_bf16 v[220:223], v[160:163], a[220:223], v[220:223]\n"
        "s_waitcnt lgkmcnt(6)\n"
        "ds_read_b128 a[244:247], v240 offset:128\n"
        "v_mfma_f32_16x16x32_bf16 v[224:227], v[160:163], a[228:231], v[224:227]\n"
        "s_waitcnt lgkmcnt(6)\n"
        "ds_read_b128 a[248:251], v241 offset:128\n"
        "v_mfma_f32_16x16x32_bf16 v[224:227], v[152:155], a[232:235], v[224:227]\n"
        "ds_read_b128 a[216:219], v240 offset:256\n"
        "v_mfma_f32_16x16x32_bf16 v[196:199], v[164:167], a[232:235], v[196:199]\n"
        "ds_read_b128 a[220:223], v241 offset:256\n"
        "v_mfma_f32_16x16x32_bf16 v[196:199], v[156:159], a[228:231], v[196:199]\n"
        "v_smfmac_f32_16x16x64_bf16 v[80:83], a[252:255], v[88:95], v255\n"
        "v_mfma_f32_16x16x32_bf16 v[84:87], v[128:131], v[88:91], 0\n"
        "v_mfma_f32_16x16x32_bf16 v[84:87], v[132:135], v[92:95], v[84:87]\n"
        "v_mfma_f32_16x16x32_bf16 v[232:235], v[140:143], v[92:95], 0\n"
        "v_mfma_f32_16x16x32_bf16 v[232:235], v[136:139], v[88:91], v[232:235]\n"
        "s_sub_u32 s27, s27, 1\n"
        "v_mfma_f32_16x16x32_bf16 v[236:239], v[144:147], v[88:91], 0\n"
        "s_cmp_lg_u32 s27, 0\n"
        "v_mfma_f32_16x16x32_bf16 v[236:239], v[148:151], v[92:95], v[236:239]\n"
        "s_cbranch_scc1 10b\n"
        "20:\n"
        "s_waitcnt lgkmcnt(0)\n"
        "ds_read_b128 a[0:3], v248 offset:0\n"
        "ds_read_b128 a[4:7], v248 offset:6144\n"
        "ds_read_b128 a[8:11], v248 offset:36864\n"
        "s_and_b32 s46, s42, 1\n"
        "s_lshl_b32 s31, s46, 8\n"
        "s_mul_i32 s45, s46, 786432\n"
        "s_sub_u32 s37, s37, s45\n"
        "s_cmp_eq_u32 s46, 0\n"
        "s_cselect_b32 s43, s43, s44\n"
        "s_add_u32 s28, s37, 786432\n"
        "s_cmp_eq_u32 s28, 100663296\n"
        "s_cselect_b32 s28, 0, s28\n"
        "s_lshl_b32 s29, s42, 8\n"
        "s_add_u32 s29, s29, 147456\n"
        "s_mov_b32 s30, 147456\n"
        "v_subrev_u32 v247, s31, v247\n"
        "ds_read_b128 v[128:131], v247 offset:0\n"
        "ds_read_b128 v[132:135], v247 offset:4096\n"
        "ds_read_b128 v[136:139], v247 offset:8192\n"
        "ds_read_b128 v[140:143], v247 offset:12288\n"
        "ds_read_b128 a[12:15], v248 offset:43008\n"
        "ds_read_b128 v[144:147], v247 offset:16384\n"
        "ds_read_b128 a[16:19], v248 offset:12288\n"
        "ds_read_b128 v[148:151], v247 offset:20480\n"
        "ds_read_b128 a[20:23], v248 offset:49152\n"
        "ds_read_b128 a[44:47], v254 offset:49168\n"
        "ds_read_b128 a[40:43], v254 offset:12304\n"
        "ds_read_b128 a[36:39], v254 offset:43024\n"
        "v_add_f32 v80, v80, v81\n"
        "v_rcp_f32 v81, v80\n"
        "v_mbcnt_lo_u32_b32 v240, -1, 0\n"
        "v_mul_f32 v84, v81, v84\n"
        "v_mul_f32 v85, v81, v85\n"
        "v_mul_f32 v86, v81, v86\n"
        "v_mul_f32 v87, v81, v87\n"
        "v_cvt_pk_bf16_f32 v84, v84, v85\n"
        "v_cvt_pk_bf16_f32 v85, v86, v87\n"
        "v_mul_f32 v232, v81, v232\n"
        "v_mul_f32 v233, v81, v233\n"
        "v_maximum3_f32 v253, v253, v80, v81\n"
        "v_mul_f32 v234, v81, v234\n"
        "v_mul_f32 v235, v81, v235\n"
        "v_cvt_pk_bf16_f32 v86, v232, v233\n"
        "v_cvt_pk_bf16_f32 v87, v234, v235\n"
        "ds_write_b128 v245, v[84:87] offset:768\n"
        "v_mul_f32 v236, v81, v236\n"
        "v_mul_f32 v237, v81, v237\n"
        "v_mul_f32 v238, v81, v238\n"
        "v_mul_f32 v239, v81, v239\n"
        "v_cvt_pk_bf16_f32 v236, v236, v237\n"
        "v_cvt_pk_bf16_f32 v237, v238, v239\n"
        "ds_write_b64 v246, v[236:237] offset:768\n"
        "v_mbcnt_hi_u32_b32 v240, -1, v240\n"
        "v_lshl_add_u32 v241, v240, 4, s30\n"
        "v_lshl_add_u32 v240, v240, 2, s29\n"
        "v_and_b32 v117, 60, v250\n"
        "ds_write_b32 v240, v253\n"
        "s_waitcnt lgkmcnt(14)\n"
        "v_mfma_f32_16x16x32_bf16 v[0:3], v[128:131], a[0:3], 0\n"
        "s_waitcnt lgkmcnt(13)\n"
        "ds_read_b128 a[32:35], v254 offset:36880\n"
        "v_add3_u32 v117, v250, v117, s26\n"
        "v_mfma_f32_16x16x32_bf16 v[0:3], v[132:135], a[4:7], v[0:3]\n"
        "s_waitcnt lgkmcnt(13)\n"
        "ds_read_b128 a[28:31], v254 offset:6160\n"
        "v_cvt_pk_bf16_f32 v108, v172, v176\n"
        "v_mfma_f32_16x16x32_bf16 v[0:3], v[136:139], a[8:11], v[0:3]\n"
        "s_waitcnt lgkmcnt(12)\n"
        "ds_read_b128 a[24:27], v254 offset:16\n"
        "v_cvt_pk_bf16_f32 v109, v180, v184\n"
        "v_mfma_f32_16x16x32_bf16 v[0:3], v[140:143], a[12:15], v[0:3]\n"
        "s_waitcnt lgkmcnt(11)\n"
        "ds_read_b128 a[48:51], v248 offset:32\n"
        "v_cvt_pk_bf16_f32 v110, v192, v196\n"
        "v_mfma_f32_16x16x32_bf16 v[0:3], v[144:147], a[16:19], v[0:3]\n"
        "s_waitcnt lgkmcnt(10)\n"
        "ds_read_b128 a[52:55], v248 offset:6176\n"
        "v_cvt_pk_bf16_f32 v112, v173, v177\n"
        "v_mfma_f32_16x16x32_bf16 v[0:3], v[148:151], a[20:23], v[0:3]\n"
        "s_waitcnt lgkmcnt(10)\n"
        "ds_read_b128 a[56:59], v248 offset:36896\n"
        "v_cvt_pk_bf16_f32 v113, v181, v185\n"
        "v_mfma_f32_16x16x32_bf16 v[4:7], v[148:151], a[44:47], 0\n"
        "s_waitcnt lgkmcnt(10)\n"
        "ds_read_b128 a[60:63], v248 offset:43040\n"
        "v_cvt_pk_bf16_f32 v114, v193, v197\n"
        "v_mfma_f32_16x16x32_bf16 v[4:7], v[144:147], a[40:43], v[4:7]\n"
        "s_waitcnt lgkmcnt(10)\n"
        "ds_read_b128 a[64:67], v248 offset:12320\n"
        "v_mfma_f32_16x16x32_bf16 v[4:7], v[140:143], a[36:39], v[4:7]\n"
        "s_waitcnt lgkmcnt(7)\n"
        "s_waitcnt lgkmcnt(8)\n"
        "s_barrier\n"
        "ds_read_b128 a[68:71], v248 offset:49184\n"
        "v_mfma_f32_16x16x32_bf16 v[4:7], v[136:139], a[32:35], v[4:7]\n"
        "s_waitcnt lgkmcnt(7)\n"
        "ds_read_b128 a[92:95], v254 offset:49200\n"
        "v_mfma_f32_16x16x32_bf16 v[4:7], v[132:135], a[28:31], v[4:7]\n"
        "s_waitcnt lgkmcnt(7)\n"
        "ds_read_b128 a[88:91], v254 offset:12336\n"
        "v_mfma_f32_16x16x32_bf16 v[4:7], v[128:131], a[24:27], v[4:7]\n"
        "s_waitcnt lgkmcnt(7)\n"
        "ds_read_b128 a[84:87], v254 offset:43056\n"
        "v_mfma_f32_16x16x32_bf16 v[8:11], v[128:131], a[48:51], 0\n"
        "s_waitcnt lgkmcnt(7)\n"
        "ds_read_b128 a[80:83], v254 offset:36912\n"
        "v_mfma_f32_16x16x32_bf16 v[8:11], v[132:135], a[52:55], v[8:11]\n"
        "s_waitcnt lgkmcnt(7)\n"
        "ds_read_b128 a[76:79], v254 offset:6192\n"
        "v_mfma_f32_16x16x32_bf16 v[8:11], v[136:139], a[56:59], v[8:11]\n"
        "s_waitcnt lgkmcnt(7)\n"
        "ds_read_b128 a[72:75], v254 offset:48\n"
        "v_mfma_f32_16x16x32_bf16 v[8:11], v[140:143], a[60:63], v[8:11]\n"
        "s_waitcnt lgkmcnt(7)\n"
        "ds_read_b128 a[96:99], v248 offset:64\n"
        "v_cvt_pk_bf16_f32 v192, v0, v4\n"
        "v_mfma_f32_16x16x32_bf16 v[8:11], v[144:147], a[64:67], v[8:11]\n"
        "s_waitcnt lgkmcnt(7)\n"
        "ds_read_b128 a[100:103], v248 offset:6208\n"
        "v_cvt_pk_bf16_f32 v196, v1, v5\n"
        "v_mfma_f32_16x16x32_bf16 v[8:11], v[148:151], a[68:71], v[8:11]\n"
        "s_waitcnt lgkmcnt(7)\n"
        "ds_read_b128 a[104:107], v248 offset:36928\n"
        "v_cvt_pk_bf16_f32 v180, v2, v6\n"
        "v_mfma_f32_16x16x32_bf16 v[12:15], v[148:151], a[92:95], 0\n"
        "s_waitcnt lgkmcnt(7)\n"
        "ds_read_b128 a[108:111], v248 offset:43072\n"
        "v_cvt_pk_bf16_f32 v184, v3, v7\n"
        "v_mfma_f32_16x16x32_bf16 v[12:15], v[144:147], a[88:91], v[12:15]\n"
        "s_waitcnt lgkmcnt(7)\n"
        "ds_read_b128 a[112:115], v248 offset:12352\n"
        "v_mfma_f32_16x16x32_bf16 v[12:15], v[140:143], a[84:87], v[12:15]\n"
        "s_waitcnt lgkmcnt(7)\n"
        "ds_read_b128 a[116:119], v248 offset:49216\n"
        "v_mfma_f32_16x16x32_bf16 v[12:15], v[136:139], a[80:83], v[12:15]\n"
        "s_waitcnt lgkmcnt(7)\n"
        "ds_read_b128 a[140:143], v254 offset:49232\n"
        "v_mfma_f32_16x16x32_bf16 v[12:15], v[132:135], a[76:79], v[12:15]\n"
        "s_waitcnt lgkmcnt(7)\n"
        "ds_read_b128 a[136:139], v254 offset:12368\n"
        "v_mfma_f32_16x16x32_bf16 v[12:15], v[128:131], a[72:75], v[12:15]\n"
        "s_waitcnt lgkmcnt(7)\n"
        "ds_read_b128 a[132:135], v254 offset:43088\n"
        "v_mfma_f32_16x16x32_bf16 v[16:19], v[128:131], a[96:99], 0\n"
        "s_waitcnt lgkmcnt(7)\n"
        "ds_read_b128 a[128:131], v254 offset:36944\n"
        "v_mfma_f32_16x16x32_bf16 v[16:19], v[132:135], a[100:103], v[16:19]\n"
        "s_waitcnt lgkmcnt(7)\n"
        "ds_read_b128 a[124:127], v254 offset:6224\n"
        "v_mfma_f32_16x16x32_bf16 v[16:19], v[136:139], a[104:107], v[16:19]\n"
        "s_waitcnt lgkmcnt(7)\n"
        "ds_read_b128 a[120:123], v254 offset:80\n"
        "ds_read_b128 v[64:67], v247 offset:512\n"
        "v_mfma_f32_16x16x32_bf16 v[16:19], v[140:143], a[108:111], v[16:19]\n"
        "s_waitcnt lgkmcnt(8)\n"
        "ds_read_b128 v[68:71], v247 offset:4608\n"
        "v_cvt_pk_bf16_f32 v193, v8, v12\n"
        "v_mfma_f32_16x16x32_bf16 v[16:19], v[144:147], a[112:115], v[16:19]\n"
        "s_waitcnt lgkmcnt(8)\n"
        "ds_read_b128 v[72:75], v247 offset:8704\n"
        "v_cvt_pk_bf16_f32 v197, v9, v13\n"
        "v_mfma_f32_16x16x32_bf16 v[16:19], v[148:151], a[116:119], v[16:19]\n"
        "s_waitcnt lgkmcnt(8)\n"
        "ds_read_b128 v[76:79], v247 offset:12800\n"
        "v_cvt_pk_bf16_f32 v181, v10, v14\n"
        "v_mfma_f32_16x16x32_bf16 v[20:23], v[148:151], a[140:143], 0\n"
        "s_waitcnt lgkmcnt(8)\n"
        "ds_read_b128 v[80:83], v247 offset:16896\n"
        "v_cvt_pk_bf16_f32 v185, v11, v15\n"
        "v_mfma_f32_16x16x32_bf16 v[20:23], v[144:147], a[136:139], v[20:23]\n"
        "s_waitcnt lgkmcnt(8)\n"
        "ds_read_b128 v[84:87], v247 offset:20992\n"
        "v_mfma_f32_16x16x32_bf16 v[20:23], v[140:143], a[132:135], v[20:23]\n"
        "s_waitcnt lgkmcnt(8)\n"
        "buffer_store_dwordx2 v[108:109], v117, s[16:19], s38 offen offset:0 sc1\n"
        "v_mfma_f32_16x16x32_bf16 v[20:23], v[136:139], a[128:131], v[20:23]\n"
        "s_waitcnt lgkmcnt(7)\n"
        "buffer_store_dword v110, v250, s[16:19], s38 offen offset:0 sc1\n"
        "v_mfma_f32_16x16x32_bf16 v[20:23], v[132:135], a[124:127], v[20:23]\n"
        "s_waitcnt lgkmcnt(6)\n"
        "buffer_store_dwordx2 v[112:113], v117, s[16:19], s38 offen offset:384 sc1\n"
        "v_mfma_f32_16x16x32_bf16 v[20:23], v[128:131], a[120:123], v[20:23]\n"
        "s_waitcnt lgkmcnt(5)\n"
        "buffer_store_dword v114, v250, s[16:19], s38 offen offset:384 sc1\n"
        "v_mfma_f32_16x16x32_bf16 v[24:27], v[64:67], a[0:3], 0\n"
        "s_waitcnt lgkmcnt(4)\n"
        "v_cvt_pk_bf16_f32 v108, v174, v178\n"
        "v_cvt_pk_bf16_f32 v109, v182, v186\n"
        "v_mfma_f32_16x16x32_bf16 v[24:27], v[68:71], a[4:7], v[24:27]\n"
        "s_waitcnt lgkmcnt(3)\n"
        "v_cvt_pk_bf16_f32 v110, v194, v198\n"
        "v_cvt_pk_bf16_f32 v112, v175, v179\n"
        "v_mfma_f32_16x16x32_bf16 v[24:27], v[72:75], a[8:11], v[24:27]\n"
        "s_waitcnt lgkmcnt(2)\n"
        "buffer_store_dwordx2 v[108:109], v117, s[16:19], s38 offen offset:768 sc1\n"
        "v_mfma_f32_16x16x32_bf16 v[24:27], v[76:79], a[12:15], v[24:27]\n"
        "s_waitcnt lgkmcnt(1)\n"
        "buffer_store_dword v110, v250, s[16:19], s38 offen offset:768 sc1\n"
        "v_mfma_f32_16x16x32_bf16 v[24:27], v[80:83], a[16:19], v[24:27]\n"
        "s_waitcnt lgkmcnt(0)\n"
        "v_cvt_pk_bf16_f32 v113, v183, v187\n"
        "v_cvt_pk_bf16_f32 v114, v195, v199\n"
        "v_mfma_f32_16x16x32_bf16 v[24:27], v[84:87], a[20:23], v[24:27]\n"
        "buffer_store_dwordx2 v[112:113], v117, s[16:19], s38 offen offset:1152 sc1\n"
        "v_mfma_f32_16x16x32_bf16 v[28:31], v[84:87], a[44:47], 0\n"
        "buffer_store_dword v114, v250, s[16:19], s38 offen offset:1152 sc1\n"
        "v_mfma_f32_16x16x32_bf16 v[28:31], v[80:83], a[40:43], v[28:31]\n"
        "v_cvt_pk_bf16_f32 v108, v200, v204\n"
        "v_cvt_pk_bf16_f32 v109, v212, v216\n"
        "v_mfma_f32_16x16x32_bf16 v[28:31], v[76:79], a[36:39], v[28:31]\n"
        "s_add_u32 s38, s37, s43\n"
        "ds_read_b128 v[96:99], v241\n"
        "v_cvt_pk_bf16_f32 v110, v220, v224\n"
        "v_mfma_f32_16x16x32_bf16 v[28:31], v[72:75], a[32:35], v[28:31]\n"
        "buffer_store_dwordx2 v[108:109], v117, s[16:19], s39 offen offset:0 sc1\n"
        "v_mfma_f32_16x16x32_bf16 v[28:31], v[68:71], a[28:31], v[28:31]\n"
        "buffer_store_dword v110, v250, s[16:19], s39 offen offset:0 sc1\n"
        "v_mfma_f32_16x16x32_bf16 v[28:31], v[64:67], a[24:27], v[28:31]\n"
        "v_cvt_pk_bf16_f32 v112, v201, v205\n"
        "v_cvt_pk_bf16_f32 v113, v213, v217\n"
        "v_mfma_f32_16x16x32_bf16 v[32:35], v[64:67], a[48:51], 0\n"
        "v_cvt_pk_bf16_f32 v114, v221, v225\n"
        "v_cvt_pk_bf16_f32 v108, v202, v206\n"
        "v_mfma_f32_16x16x32_bf16 v[32:35], v[68:71], a[52:55], v[32:35]\n"
        "buffer_store_dwordx2 v[112:113], v117, s[16:19], s39 offen offset:384 sc1\n"
        "v_mfma_f32_16x16x32_bf16 v[32:35], v[72:75], a[56:59], v[32:35]\n"
        "buffer_store_dword v114, v250, s[16:19], s39 offen offset:384 sc1\n"
        "v_mfma_f32_16x16x32_bf16 v[32:35], v[76:79], a[60:63], v[32:35]\n"
        "v_cvt_pk_bf16_f32 v109, v214, v218\n"
        "v_cvt_pk_bf16_f32 v110, v222, v226\n"
        "v_mfma_f32_16x16x32_bf16 v[32:35], v[80:83], a[64:67], v[32:35]\n"
        "buffer_store_dwordx2 v[108:109], v117, s[16:19], s39 offen offset:768 sc1\n"
        "v_mfma_f32_16x16x32_bf16 v[32:35], v[84:87], a[68:71], v[32:35]\n"
        "buffer_store_dword v110, v250, s[16:19], s39 offen offset:768 sc1\n"
        "v_mfma_f32_16x16x32_bf16 v[36:39], v[84:87], a[92:95], 0\n"
        "v_cvt_pk_bf16_f32 v112, v203, v207\n"
        "v_cvt_pk_bf16_f32 v113, v215, v219\n"
        "v_mfma_f32_16x16x32_bf16 v[36:39], v[80:83], a[88:91], v[36:39]\n"
        "v_cvt_pk_bf16_f32 v114, v223, v227\n"
        "v_cvt_pk_bf16_f32 v194, v16, v20\n"
        "v_mfma_f32_16x16x32_bf16 v[36:39], v[76:79], a[84:87], v[36:39]\n"
        "buffer_store_dwordx2 v[112:113], v117, s[16:19], s39 offen offset:1152 sc1\n"
        "v_mfma_f32_16x16x32_bf16 v[36:39], v[72:75], a[80:83], v[36:39]\n"
        "buffer_store_dword v114, v250, s[16:19], s39 offen offset:1152 sc1\n"
        "v_mfma_f32_16x16x32_bf16 v[36:39], v[68:71], a[76:79], v[36:39]\n"
        "buffer_store_dwordx2 v[192:193], v117, s[16:19], s38 offen offset:0 sc1\n"
        "v_mfma_f32_16x16x32_bf16 v[36:39], v[64:67], a[72:75], v[36:39]\n"
        "s_add_u32 s39, s28, s43\n"
        "buffer_store_dword v194, v250, s[16:19], s38 offen offset:0 sc1\n"
        "v_mfma_f32_16x16x32_bf16 v[40:43], v[64:67], a[96:99], 0\n"
        "v_cvt_pk_bf16_f32 v198, v17, v21\n"
        "v_cvt_pk_bf16_f32 v182, v18, v22\n"
        "v_mfma_f32_16x16x32_bf16 v[40:43], v[68:71], a[100:103], v[40:43]\n"
        "buffer_store_dwordx2 v[196:197], v117, s[16:19], s38 offen offset:384 sc1\n"
        "v_mfma_f32_16x16x32_bf16 v[40:43], v[72:75], a[104:107], v[40:43]\n"
        "buffer_store_dword v198, v250, s[16:19], s38 offen offset:384 sc1\n"
        "v_mfma_f32_16x16x32_bf16 v[40:43], v[76:79], a[108:111], v[40:43]\n"
        "buffer_store_dwordx2 v[180:181], v117, s[16:19], s38 offen offset:768 sc1\n"
        "v_mfma_f32_16x16x32_bf16 v[40:43], v[80:83], a[112:115], v[40:43]\n"
        "buffer_store_dword v182, v250, s[16:19], s38 offen offset:768 sc1\n"
        "v_mfma_f32_16x16x32_bf16 v[40:43], v[84:87], a[116:119], v[40:43]\n"
        "v_cvt_pk_bf16_f32 v186, v19, v23\n"
        "s_waitcnt lgkmcnt(0)\n"
        "v_maximum3_f32 v96, v96, v97, v98\n"
        "v_mfma_f32_16x16x32_bf16 v[44:47], v[84:87], a[140:143], 0\n"
        "buffer_store_dwordx2 v[184:185], v117, s[16:19], s38 offen offset:1152 sc1\n"
        "v_mfma_f32_16x16x32_bf16 v[44:47], v[80:83], a[136:139], v[44:47]\n"
        "buffer_store_dword v186, v250, s[16:19], s38 offen offset:1152 sc1\n"
        "v_mfma_f32_16x16x32_bf16 v[44:47], v[76:79], a[132:135], v[44:47]\n"
        "v_maximum3_f32 v96, v96, v99, v99\n"
        "v_cmp_nge_f32 vcc, s25, v96\n"
        "v_mfma_f32_16x16x32_bf16 v[44:47], v[72:75], a[128:131], v[44:47]\n"
        "v_cvt_pk_bf16_f32 v172, v24, v28\n"
        "v_cvt_pk_bf16_f32 v173, v32, v36\n"
        "v_mfma_f32_16x16x32_bf16 v[44:47], v[68:71], a[124:127], v[44:47]\n"
        "v_cvt_pk_bf16_f32 v176, v25, v29\n"
        "v_cvt_pk_bf16_f32 v177, v33, v37\n"
        "v_mfma_f32_16x16x32_bf16 v[44:47], v[64:67], a[120:123], v[44:47]\n"
        "buffer_store_dwordx2 v[172:173], v117, s[16:19], s39 offen offset:0 sc1\n"
        "v_cvt_pk_bf16_f32 v152, v26, v30\n"
        "v_cvt_pk_bf16_f32 v153, v34, v38\n"
        "buffer_store_dwordx2 v[176:177], v117, s[16:19], s39 offen offset:384 sc1\n"
        "v_cvt_pk_bf16_f32 v156, v27, v31\n"
        "v_cvt_pk_bf16_f32 v157, v35, v39\n"
        "buffer_store_dwordx2 v[152:153], v117, s[16:19], s39 offen offset:768 sc1\n"
        "buffer_store_dwordx2 v[156:157], v117, s[16:19], s39 offen offset:1152 sc1\n"
        "v_cvt_pk_bf16_f32 v174, v40, v44\n"
        "buffer_store_dword v174, v250, s[16:19], s39 offen offset:0 sc1\n"
        "v_cvt_pk_bf16_f32 v178, v41, v45\n"
        "buffer_store_dword v178, v250, s[16:19], s39 offen offset:384 sc1\n"
        "v_cvt_pk_bf16_f32 v154, v42, v46\n"
        "buffer_store_dword v154, v250, s[16:19], s39 offen offset:768 sc1\n"
        "v_cvt_pk_bf16_f32 v158, v43, v47\n"
        "buffer_store_dword v158, v250, s[16:19], s39 offen offset:1152 sc1\n"
        "s_cmp_lg_u64 vcc, 0\n"
        "s_cbranch_scc1 30f\n"
        "s_endpgm\n"
        "30:\n"
        : "+{v0}"(tid), "=&{s42}"(w)
        : "{s[0:1]}"(__builtin_amdgcn_kernarg_segment_ptr()), "{s2}"(blockIdx.x), "s"(ldsb)
        : "v1", "v2", "v3", "v4", "v5", "v6", "v7", "v8", "v9", "v10", "v11", "v12", "v13", "v14", "v15", "v16", "v17", "v18", "v19", "v20", "v21", "v22", "v23", "v24", "v25", "v26", "v27", "v28", "v29", "v30", "v31", "v32", "v33", "v34", "v35", "v36", "v37", "v38", "v39", "v40", "v41", "v42", "v43", "v44", "v45", "v46", "v47", "v48", "v49", "v50", "v51", "v52", "v53", "v54", "v55", "v56", "v57", "v58", "v59", "v60", "v61", "v62", "v63", "v64", "v65", "v66", "v67", "v68", "v69", "v70", "v71", "v72", "v73", "v74", "v75", "v76", "v77", "v78", "v79", "v80", "v81", "v82", "v83", "v84", "v85", "v86", "v87", "v88", "v89", "v90", "v91", "v92", "v93", "v94", "v95", "v96", "v97", "v98", "v99", "v100", "v101", "v102", "v103", "v104", "v105", "v106", "v107", "v108", "v109", "v110", "v111", "v112", "v113", "v114", "v115", "v116", "v117", "v118", "v119", "v120", "v121", "v122", "v123", "v124", "v125", "v126", "v127", "v128", "v129", "v130", "v131", "v132", "v133", "v134", "v135", "v136", "v137", "v138", "v139", "v140", "v141", "v142", "v143", "v144", "v145", "v146", "v147", "v148", "v149", "v150", "v151", "v152", "v153", "v154", "v155", "v156", "v157", "v158", "v159", "v160", "v161", "v162", "v163", "v164", "v165", "v166", "v167", "v168", "v169", "v170", "v171", "v172", "v173", "v174", "v175", "v176", "v177", "v178", "v179", "v180", "v181", "v182", "v183", "v184", "v185", "v186", "v187", "v188", "v189", "v190", "v191", "v192", "v193", "v194", "v195", "v196", "v197", "v198", "v199", "v200", "v201", "v202", "v203", "v204", "v205", "v206", "v207", "v208", "v209", "v210", "v211", "v212", "v213", "v214", "v215", "v216", "v217", "v218", "v219", "v220", "v221", "v222", "v223", "v224", "v225", "v226", "v227", "v228", "v229", "v230", "v231", "v232", "v233", "v234", "v235", "v236", "v237", "v238", "v239", "v240", "v241", "v242", "v243", "v244", "v245", "v246", "v247", "v248", "v249", "v250", "v251", "v252", "v253", "v254", "v255", "a0", "a1", "a2", "a3", "a4", "a5", "a6", "a7", "a8", "a9", "a10", "a11", "a12", "a13", "a14", "a15", "a16", "a17", "a18", "a19", "a20", "a21", "a22", "a23", "a24", "a25", "a26", "a27", "a28", "a29", "a30", "a31", "a32", "a33", "a34", "a35", "a36", "a37", "a38", "a39", "a40", "a41", "a42", "a43", "a44", "a45", "a46", "a47", "a48", "a49", "a50", "a51", "a52", "a53", "a54", "a55", "a56", "a57", "a58", "a59", "a60", "a61", "a62", "a63", "a64", "a65", "a66", "a67", "a68", "a69", "a70", "a71", "a72", "a73", "a74", "a75", "a76", "a77", "a78", "a79", "a80", "a81", "a82", "a83", "a84", "a85", "a86", "a87", "a88", "a89", "a90", "a91", "a92", "a93", "a94", "a95", "a96", "a97", "a98", "a99", "a100", "a101", "a102", "a103", "a104", "a105", "a106", "a107", "a108", "a109", "a110", "a111", "a112", "a113", "a114", "a115", "a116", "a117", "a118", "a119", "a120", "a121", "a122", "a123", "a124", "a125", "a126", "a127", "a128", "a129", "a130", "a131", "a132", "a133", "a134", "a135", "a136", "a137", "a138", "a139", "a140", "a141", "a142", "a143", "a144", "a145", "a146", "a147", "a148", "a149", "a150", "a151", "a152", "a153", "a154", "a155", "a156", "a157", "a158", "a159", "a160", "a161", "a162", "a163", "a164", "a165", "a166", "a167", "a168", "a169", "a170", "a171", "a172", "a173", "a174", "a175", "a176", "a177", "a178", "a179", "a180", "a181", "a182", "a183", "a184", "a185", "a186", "a187", "a188", "a189", "a190", "a191", "a192", "a193", "a194", "a195", "a196", "a197", "a198", "a199", "a200", "a201", "a202", "a203", "a204", "a205", "a206", "a207", "a208", "a209", "a210", "a211", "a212", "a213", "a214", "a215", "a216", "a217", "a218", "a219", "a220", "a221", "a222", "a223", "a224", "a225", "a226", "a227", "a228", "a229", "a230", "a231", "a232", "a233", "a234", "a235", "a236", "a237", "a238", "a239", "a240", "a241", "a242", "a243", "a244", "a245", "a246", "a247", "a248", "a249", "a250", "a251", "a252", "a253", "a254", "a255", "s8", "s9", "s10", "s11", "s12", "s13", "s14", "s15", "s16", "s17", "s18", "s19", "s20", "s21", "s22", "s23", "s24", "s25", "s26", "s27", "s28", "s29", "s30", "s31", "s34", "s35", "s36", "s37", "s38", "s39", "s40", "s41", "s43", "s44", "s45", "s46", "s47", "s48", "s49", "s50", "s51", "s52", "s53", "s54", "s55", "s56", "s57", "s58", "s59", "s60", "s61", "s62", "s63", "s64", "s65", "s66", "s67", "vcc", "scc", "memory");
    {
        unsigned l2;
        asm volatile("s_waitcnt vmcnt(0) lgkmcnt(0)\n s_barrier\n v_mbcnt_lo_u32_b32 %0, -1, 0\n v_mbcnt_hi_u32_b32 %0, -1, %0\n s_nop 1"
                     : "=v"(l2) :: "memory");
        guarded_pass(x, wqkv, wo, rpb, rpi, mask, out, ldsb, l2, w);
    }
}


extern "C" void kernel_launch(void* const* d_in, const int* in_sizes, int n_in, void* d_out, int out_size, void* d_ws,
                              size_t ws_size, hipStream_t stream) {
    win_attn_kernel<<<256, 256, 0, stream>>>((const char*)d_in[0], (const char*)d_in[1], (const char*)d_in[2],
                                             (const char*)d_in[3], (const char*)d_in[4], (const char*)d_in[5],
                                             (char*)d_out);

}
```

```cpp
#include <hip/hip_runtime.h>
#include <hip/hip_bf16.h>

typedef unsigned int u32x4 __attribute__((ext_vector_type(4)));

__device__ __forceinline__ void guarded_pass(const char* x, const char* wqkv, const char* wo, const char* rpb, const char* rpi,
                                             const char* mask, char* out, unsigned ldsb, const unsigned lane, const unsigned w) {
    const unsigned wx = blockIdx.x & 63u, wy0 = blockIdx.x >> 6;
    unsigned dm[3], wsrc[3];
#pragma unroll
    for (int p = 0; p < 3; ++p) {
        unsigned gch = 64u * p + lane, t = gch / 24u, c1 = gch % 24u;
        dm[p] = ((8u * wx + 4u + t) & 511u) * 384u + ((c1 ^ (((t >> 1) & 1u) | (w << 1))) << 4);
        wsrc[p] = t * 1152u + ((c1 ^ (2u * (((t >> 1) & 1u) | ((w & 1u) << 1)))) << 4) - 1024u * p;
    }
    u32x4 srdx = {(unsigned)(size_t)x, (unsigned)((size_t)x >> 32) & 0xffffu, 100663296u, 0x20000u};
    u32x4 srdw = {(unsigned)(size_t)wqkv, (unsigned)((size_t)wqkv >> 32) & 0xffffu, 221184u, 0x20000u};
    unsigned ya = (8u * wy0 + 36u + w) * 196608u;
    unsigned wv = w;
    unsigned w3 = w * 3072u, w9 = w * 9216u;
    __builtin_amdgcn_sched_barrier(0);
    asm volatile(
        "s_nop 4\n"
        "s_add_u32 m0, s40, 0\n"
        "s_add_u32 s64, s65, 0\n"
        "buffer_load_dwordx4 v128, s[20:23], s64 offen lds\n"
        "buffer_load_dwordx4 v129, s[20:23], s64 offen offset:1024 lds\n"
        "buffer_load_dwordx4 v130, s[20:23], s64 offen offset:2048 lds\n"
        "s_add_u32 m0, s40, 12288\n"
        "s_add_u32 s64, s65, 36864\n"
        "buffer_load_dwordx4 v128, s[20:23], s64 offen lds\n"
        "buffer_load_dwordx4 v129, s[20:23], s64 offen offset:1024 lds\n"
        "buffer_load_dwordx4 v130, s[20:23], s64 offen offset:2048 lds\n"
        "s_add_u32 m0, s40, 24576\n"
        "s_add_u32 s64, s65, 73728\n"
        "buffer_load_dwordx4 v128, s[20:23], s64 offen lds\n"
        "buffer_load_dwordx4 v129, s[20:23], s64 offen offset:1024 lds\n"
        "buffer_load_dwordx4 v130, s[20:23], s64 offen offset:2048 lds\n"
        :: "{v242}"(dm[0]), "{v243}"(dm[1]), "{v244}"(dm[2]), "{v128}"(wsrc[0]), "{v129}"(wsrc[1]), "{v130}"(wsrc[2]), "{s[8:11]}"(srdx), "{s[20:23]}"(srdw), "{s47}"(ya), "{s40}"(w3), "{s65}"(w9), "s"(ldsb)
        : "s64", "scc", "memory");
    __builtin_amdgcn_sched_barrier(0);

    const unsigned rho = lane & 15u, g = lane >> 4;
    const unsigned cg = w >> 1, T = w & 1u;
    const unsigned ql = 8u * (rho >> 2) + (rho & 3u);
    unsigned xr[2], mr[2];
#pragma unroll
    for (int j = 0; j < 2; ++j) {
        xr[j] = 73728u + ql * 384u + (((4u * j + g) ^ (rho >> 1)) << 4);
        mr[j] = 147456u + 8192u + ql * 128u + (((4u * j + g) ^ (rho >> 1)) << 4);
    }
    unsigned ow = 122880u + (4u * w + g) * 1024u + rho * 16u;
    unsigned ow2 = 122880u + (16u + 2u * w + (g >> 1)) * 1024u + rho * 16u + (g & 1u) * 8u;
    unsigned ord = 122880u + g * 1024u + (32u * T + rho) * 16u;
    __builtin_amdgcn_sched_barrier(0);
    asm volatile(
        "s_nop 4\n"
        "s_add_u32 m0, s40, 36864\n"
        "s_add_u32 s64, s65, 110592\n"
        "buffer_load_dwordx4 v128, s[20:23], s64 offen lds\n"
        "buffer_load_dwordx4 v129, s[20:23], s64 offen offset:1024 lds\n"
        "buffer_load_dwordx4 v130, s[20:23], s64 offen offset:2048 lds\n"
        :: "{v242}"(dm[0]), "{v243}"(dm[1]), "{v244}"(dm[2]), "{v128}"(wsrc[0]), "{v129}"(wsrc[1]), "{v130}"(wsrc[2]), "{s[8:11]}"(srdx), "{s[20:23]}"(srdw), "{s47}"(ya), "{s40}"(w3), "{s65}"(w9), "s"(ldsb)
        : "s64", "scc", "memory");
    __builtin_amdgcn_sched_barrier(0);
    unsigned wor = 0u + cg * 18432u + g * 1536u + rho * 96u + (g & 1u) * 16u;
    unsigned wor2 = 0u + cg * 18432u + g * 1536u + rho * 96u - (g & 1u) * 16u;
    unsigned ml = 147456u + w * 2048u + lane * 16u;
    unsigned ms = (lane >> 3) * 128u + (((lane & 7u) ^ (((lane >> 4) & 1u) | ((w & 1u) << 2))) << 4);
    unsigned st = g * 196608u + cg * 192u + rho * 12u;
    unsigned l16 = lane * 16u;
    unsigned id0 = (rho == 4u * g ? 0x3f80u : 0u) | (rho == 4u * g + 1u ? 0x3f800000u : 0u);
    unsigned id1 = (rho == 4u * g + 2u ? 0x3f80u : 0u) | (rho == 4u * g + 3u ? 0x3f800000u : 0u);
    unsigned x0 = (8u * wx + 4u) * 384u, x1 = ((8u * wx + 8u) & 511u) * 384u;
    u32x4 srdm = {(unsigned)(size_t)mask, (unsigned)((size_t)mask >> 32) & 0xffffu, 33554432u, 0x20000u};
    u32x4 srdo = {(unsigned)(size_t)out, (unsigned)((size_t)out >> 32) & 0xffffu, 100663296u, 0x20000u};
    u32x4 srdwo = {(unsigned)(size_t)wo, (unsigned)((size_t)wo >> 32) & 0xffffu, 73728u, 0x20000u};
    u32x4 srdrp = {(unsigned)(size_t)rpi, (unsigned)((size_t)rpi >> 32) & 0xffffu, 16384u, 0x20000u};
    u32x4 srdrb = {(unsigned)(size_t)rpb, (unsigned)((size_t)rpb >> 32) & 0xffffu, 1800u, 0x20000u};
    __builtin_amdgcn_sched_barrier(0);
    asm volatile(
        "s_nop 4\n"
        "s_add_u32 m0, s40, 49152\n"
        "s_add_u32 s64, s65, 147456\n"
        "buffer_load_dwordx4 v128, s[20:23], s64 offen lds\n"
        "buffer_load_dwordx4 v129, s[20:23], s64 offen offset:1024 lds\n"
        "buffer_load_dwordx4 v130, s[20:23], s64 offen offset:2048 lds\n"
        :: "{v242}"(dm[0]), "{v243}"(dm[1]), "{v244}"(dm[2]), "{v128}"(wsrc[0]), "{v129}"(wsrc[1]), "{v130}"(wsrc[2]), "{s[8:11]}"(srdx), "{s[20:23]}"(srdw), "{s47}"(ya), "{s40}"(w3), "{s65}"(w9), "s"(ldsb)
        : "s64", "scc", "memory");
    __builtin_amdgcn_sched_barrier(0);
    const unsigned q4 = (lane >> 2) & 3u, p4 = lane & 3u;
    unsigned wrd[3];
#pragma unroll
    for (int p = 0; p < 3; ++p) {
        unsigned c = 6u * w + 2u * p + (p4 >> 1);
        wrd[p] = (8u * g + q4) * 384u + ((c ^ (2u * ((q4 >> 1) | ((g & 1u) << 1)))) << 4) + (p4 & 1u) * 8u;
    }
    unsigned od = cg * 18432u + g * 1536u + (((48u * T + rho) ^ (g & 1u)) << 4);
    unsigned rbs = lane * 8u + 2u * w, rbw = 151552u + w * 1024u + lane * 4u;
    const unsigned pfi = ((blockIdx.x >> 3) & 31u) * 4u + w;
    unsigned pf0 = pfi * 2048u + lane * 16u, pf1 = pfi * 1024u + lane * 16u;
    __builtin_amdgcn_sched_barrier(0);
    asm volatile(
        "s_nop 4\n"
        "s_add_u32 m0, s40, 61440\n"
        "s_add_u32 s64, s65, 184320\n"
        "buffer_load_dwordx4 v128, s[20:23], s64 offen lds\n"
        "buffer_load_dwordx4 v129, s[20:23], s64 offen offset:1024 lds\n"
        "buffer_load_dwordx4 v130, s[20:23], s64 offen offset:2048 lds\n"
        :: "{v242}"(dm[0]), "{v243}"(dm[1]), "{v244}"(dm[2]), "{v128}"(wsrc[0]), "{v129}"(wsrc[1]), "{v130}"(wsrc[2]), "{s[8:11]}"(srdx), "{s[20:23]}"(srdw), "{s47}"(ya), "{s40}"(w3), "{s65}"(w9), "s"(ldsb)
        : "s64", "scc", "memory");
    __builtin_amdgcn_sched_barrier(0);
    unsigned rs = g * 256u + ((rho ^ (g | ((w & 1u) << 3))) << 4);
    unsigned rr = 135168u + ql * 256u + (((2u * g) ^ rho) << 4);
    unsigned oa = (4u * g + q4) * 384u + 96u * w + p4 * 8u;
    unsigned ob = (48u * (g >> 1) + 8u * (g & 1u) + q4) * 384u + 96u * w + p4 * 8u;
    unsigned mk = (blockIdx.x + 256u) * 8192u + w * 2048u;
    unsigned raw = (8u * wy0 + 4u + 4u * T) * 196608u;
    asm volatile(
        "s_nop 4\n"
        "s_mul_i32 s40, s42, 3072\n"
        "s_mul_i32 s65, s42, 9216\n"
        "s_lshl_b32 s48, s42, 11\n"
        "s_add_u32 s48, s48, 147456\n"
        "s_mov_b32 s24, 0x3fb8aa3b\n"
        "s_mov_b32 s25, 0xbfb8aa3b\n"
        "s_mov_b32 s26, 0xff7fff7f\n"
        "s_mov_b32 s49, 0x3e13cd3a\n"
        "s_mov_b32 s50, 0x3e13cd3a\n"
        "s_mov_b32 s51, 0x3e13cd3a\n"
        "s_mov_b32 s27, 14\n"
        "s_mov_b32 s28, 73728\n"
        "s_mov_b32 s29, 122880\n"
        "s_mov_b32 s30, 98304\n"
        "s_mov_b32 s38, 0x80000000\n"
        "s_mov_b32 s39, 0x80000000\n"
        "s_lshl_b32 s67, s42, 10\n"
        "s_add_u32 s67, s67, 151552\n"
        "v_mov_b32 v188, v170\n"
        "v_mov_b32 v189, v171\n"
        "v_mov_b32 v210, v170\n"
        "v_mov_b32 v211, v171\n"
        "v_mov_b32 v228, v170\n"
        "v_mov_b32 v229, v171\n"
        "v_accvgpr_write_b32 a252, v140\n"
        "v_add_u32 v136, 98304, v131\n"
        "v_accvgpr_write_b32 a253, v141\n"
        "v_add_u32 v137, 98304, v132\n"
        "v_accvgpr_write_b32 a254, v147\n"
        "v_add_u32 v138, 98304, v133\n"
        "v_mov_b32 v150, v240\n"
        "v_mov_b32 v151, v241\n"
        "v_xor_b32 v253, 64, v139\n"
        "v_xor_b32 v232, 0, v144\n"
        "v_xor_b32 v233, 16, v144\n"
        "v_xor_b32 v234, 128, v144\n"
        "v_xor_b32 v235, 144, v144\n"
        "s_lshl_b32 s46, s42, 12\n"
        "buffer_load_dwordx4 v[64:67], v148, s[20:23], 0 offen\n"
        "buffer_load_dwordx4 v[68:71], v148, s[20:23], 0 offen offset:1024\n"
        "buffer_load_dwordx4 v[72:75], v149, s[52:55], 0 offen\n"
        "s_waitcnt vmcnt(12)\n"
        "s_barrier\n"
        "s_add_u32 m0, s40, 73728\n"
        "ds_read_b64_tr_b16 v[76:77], v131 offset:0\n"
        "ds_read_b64_tr_b16 v[78:79], v131 offset:1536\n"
        "ds_read_b64_tr_b16 v[80:81], v131 offset:12288\n"
        "ds_read_b64_tr_b16 v[82:83], v131 offset:13824\n"
        "ds_read_b64_tr_b16 v[84:85], v131 offset:24576\n"
        "ds_read_b64_tr_b16 v[86:87], v131 offset:26112\n"
        "ds_read_b64_tr_b16 v[88:89], v132 offset:0\n"
        "ds_read_b64_tr_b16 v[90:91], v132 offset:1536\n"
        "ds_read_b64_tr_b16 v[92:93], v132 offset:12288\n"
        "ds_read_b64_tr_b16 v[94:95], v132 offset:13824\n"
        "ds_read_b64_tr_b16 v[104:105], v132 offset:24576\n"
        "ds_read_b64_tr_b16 v[106:107], v132 offset:26112\n"
        "ds_read_b64_tr_b16 v[108:109], v133 offset:0\n"
        "ds_read_b64_tr_b16 v[110:111], v133 offset:1536\n"
        "ds_read_b64_tr_b16 v[116:117], v133 offset:12288\n"
        "ds_read_b64_tr_b16 v[118:119], v133 offset:13824\n"
        "ds_read_b64_tr_b16 v[120:121], v133 offset:24576\n"
        "ds_read_b64_tr_b16 v[122:123], v133 offset:26112\n"
        "s_sub_u32 s64, s47, 6291456\n"
        "buffer_load_dwordx4 v242, s[8:11], s64 offen lds\n"
        "s_add_u32 m0, s40, 74752\n"
        "s_sub_u32 s64, s47, 6291456\n"
        "buffer_load_dwordx4 v243, s[8:11], s64 offen lds\n"
        "s_add_u32 m0, s40, 75776\n"
        "s_sub_u32 s64, s47, 6291456\n"
        "buffer_load_dwordx4 v244, s[8:11], s64 offen lds\n"
        "s_add_u32 m0, s40, 86016\n"
        "s_sub_u32 s64, s47, 5505024\n"
        "buffer_load_dwordx4 v242, s[8:11], s64 offen lds\n"
        "s_add_u32 m0, s40, 87040\n"
        "s_sub_u32 s64, s47, 5505024\n"
        "buffer_load_dwordx4 v243, s[8:11], s64 offen lds\n"
        "s_add_u32 m0, s40, 88064\n"
        "s_sub_u32 s64, s47, 5505024\n"
        "buffer_load_dwordx4 v244, s[8:11], s64 offen lds\n"
        "s_waitcnt lgkmcnt(15)\n"
        "v_lshlrev_b32 v0, 16, v76\n"
        "v_lshlrev_b32 v2, 16, v77\n"
        "v_lshlrev_b32 v4, 16, v78\n"
        "v_lshlrev_b32 v6, 16, v79\n"
        "v_and_b32 v1, 0xffff0000, v76\n"
        "v_and_b32 v3, 0xffff0000, v77\n"
        "v_and_b32 v5, 0xffff0000, v78\n"
        "v_and_b32 v7, 0xffff0000, v79\n"
        "v_pk_mul_f32 v[0:1], v[0:1], s[50:51]\n"
        "v_pk_mul_f32 v[2:3], v[2:3], s[50:51]\n"
        "v_pk_mul_f32 v[4:5], v[4:5], s[50:51]\n"
        "v_pk_mul_f32 v[6:7], v[6:7], s[50:51]\n"
        "v_cvt_pk_bf16_f32 v0, v0, v1\n"
        "v_cvt_pk_bf16_f32 v2, v2, v3\n"
        "v_cvt_pk_bf16_f32 v4, v4, v5\n"
        "v_cvt_pk_bf16_f32 v6, v6, v7\n"
        "v_accvgpr_write_b32 a0, v0\n"
        "v_accvgpr_write_b32 a1, v2\n"
        "v_accvgpr_write_b32 a2, v4\n"
        "v_accvgpr_write_b32 a3, v6\n"
        "v_lshlrev_b32 v8, 16, v80\n"
        "v_lshlrev_b32 v10, 16, v81\n"
        "s_waitcnt lgkmcnt(14)\n"
        "v_lshlrev_b32 v12, 16, v82\n"
        "v_lshlrev_b32 v14, 16, v83\n"
        "v_and_b32 v9, 0xffff0000, v80\n"
        "v_and_b32 v11, 0xffff0000, v81\n"
        "v_and_b32 v13, 0xffff0000, v82\n"
        "v_and_b32 v15, 0xffff0000, v83\n"
        "v_pk_mul_f32 v[8:9], v[8:9], s[50:51]\n"
        "v_pk_mul_f32 v[10:11], v[10:11], s[50:51]\n"
        "v_pk_mul_f32 v[12:13], v[12:13], s[50:51]\n"
        "v_pk_mul_f32 v[14:15], v[14:15], s[50:51]\n"
        "v_cvt_pk_bf16_f32 v8, v8, v9\n"
        "v_cvt_pk_bf16_f32 v10, v10, v11\n"
        "v_cvt_pk_bf16_f32 v12, v12, v13\n"
        "v_cvt_pk_bf16_f32 v14, v14, v15\n"
        "v_accvgpr_write_b32 a4, v8\n"
        "v_accvgpr_write_b32 a5, v10\n"
        "v_accvgpr_write_b32 a6, v12\n"
        "v_accvgpr_write_b32 a7, v14\n"
        "s_waitcnt lgkmcnt(13)\n"
        "v_lshlrev_b32 v0, 16, v84\n"
        "v_lshlrev_b32 v2, 16, v85\n"
        "s_waitcnt lgkmcnt(12)\n"
        "v_lshlrev_b32 v4, 16, v86\n"
        "v_lshlrev_b32 v6, 16, v87\n"
        "v_and_b32 v1, 0xffff0000, v84\n"
        "v_and_b32 v3, 0xffff0000, v85\n"
        "v_and_b32 v5, 0xffff0000, v86\n"
        "v_and_b32 v7, 0xffff0000, v87\n"
        "v_pk_mul_f32 v[0:1], v[0:1], s[50:51]\n"
        "v_pk_mul_f32 v[2:3], v[2:3], s[50:51]\n"
        "v_pk_mul_f32 v[4:5], v[4:5], s[50:51]\n"
        "v_pk_mul_f32 v[6:7], v[6:7], s[50:51]\n"
        "v_cvt_pk_bf16_f32 v0, v0, v1\n"
        "v_cvt_pk_bf16_f32 v2, v2, v3\n"
        "v_cvt_pk_bf16_f32 v4, v4, v5\n"
        "v_cvt_pk_bf16_f32 v6, v6, v7\n"
        "v_accvgpr_write_b32 a8, v0\n"
        "v_accvgpr_write_b32 a9, v2\n"
        "v_accvgpr_write_b32 a10, v4\n"
        "v_accvgpr_write_b32 a11, v6\n"
        "s_waitcnt lgkmcnt(11)\n"
        "v_lshlrev_b32 v8, 16, v88\n"
        "v_lshlrev_b32 v10, 16, v89\n"
        "s_waitcnt lgkmcnt(10)\n"
        "v_lshlrev_b32 v12, 16, v90\n"
        "v_lshlrev_b32 v14, 16, v91\n"
        "buffer_load_ushort v236, v145, s[60:63], 0 offen offset:0\n"
        "buffer_load_ushort v237, v145, s[60:63], 0 offen offset:512\n"
        "buffer_load_ushort v238, v145, s[60:63], 0 offen offset:1024\n"
        "buffer_load_ushort v239, v145, s[60:63], 0 offen offset:1536\n"
        "v_and_b32 v9, 0xffff0000, v88\n"
        "v_and_b32 v11, 0xffff0000, v89\n"
        "v_and_b32 v13, 0xffff0000, v90\n"
        "v_and_b32 v15, 0xffff0000, v91\n"
        "v_pk_mul_f32 v[8:9], v[8:9], s[50:51]\n"
        "v_pk_mul_f32 v[10:11], v[10:11], s[50:51]\n"
        "v_pk_mul_f32 v[12:13], v[12:13], s[50:51]\n"
        "v_pk_mul_f32 v[14:15], v[14:15], s[50:51]\n"
        "s_add_u32 m0, s46, 135168\n"
        "v_cvt_pk_bf16_f32 v8, v8, v9\n"
        "v_cvt_pk_bf16_f32 v10, v10, v11\n"
        "v_cvt_pk_bf16_f32 v12, v12, v13\n"
        "v_cvt_pk_bf16_f32 v14, v14, v15\n"
        "s_mov_b32 s64, s46\n"
        "v_accvgpr_write_b32 a24, v8\n"
        "v_accvgpr_write_b32 a25, v10\n"
        "v_accvgpr_write_b32 a26, v12\n"
        "v_accvgpr_write_b32 a27, v14\n"
        "buffer_load_dwordx4 v139, s[56:59], s64 offen lds\n"
        "s_waitcnt lgkmcnt(9)\n"
        "v_lshlrev_b32 v0, 16, v92\n"
        "v_lshlrev_b32 v2, 16, v93\n"
        "s_waitcnt lgkmcnt(8)\n"
        "v_lshlrev_b32 v4, 16, v94\n"
        "v_lshlrev_b32 v6, 16, v95\n"
        "s_waitcnt lgkmcnt(0)\n"
        "s_waitcnt vmcnt(14)\n"
        "s_barrier\n"
        "v_and_b32 v1, 0xffff0000, v92\n"
        "v_and_b32 v3, 0xffff0000, v93\n"
        "v_and_b32 v5, 0xffff0000, v94\n"
        "v_and_b32 v7, 0xffff0000, v95\n"
        "ds_read_b64_tr_b16 v[152:153], v131 offset:36864\n"
        "ds_read_b64_tr_b16 v[154:155], v131 offset:38400\n"
        "ds_read_b64_tr_b16 v[172:173], v131 offset:49152\n"
        "ds_read_b64_tr_b16 v[174:175], v131 offset:50688\n"
        "ds_read_b64_tr_b16 v[192:193], v131 offset:61440\n"
        "ds_read_b64_tr_b16 v[194:195], v131 offset:62976\n"
        "ds_read_b64_tr_b16 v[212:213], v132 offset:36864\n"
        "ds_read_b64_tr_b16 v[214:215], v132 offset:38400\n"
        "ds_read_b64_tr_b16 v[156:157], v132 offset:49152\n"
        "ds_read_b64_tr_b16 v[158:159], v132 offset:50688\n"
        "ds_read_b64_tr_b16 v[176:177], v132 offset:61440\n"
        "ds_read_b64_tr_b16 v[178:179], v132 offset:62976\n"
        "ds_read_b64_tr_b16 v[196:197], v133 offset:36864\n"
        "ds_read_b64_tr_b16 v[198:199], v133 offset:38400\n"
        "ds_read_b64_tr_b16 v[216:217], v133 offset:49152\n"
        "ds_read_b64_tr_b16 v[218:219], v133 offset:50688\n"
        "ds_read_b64_tr_b16 v[124:125], v133 offset:61440\n"
        "ds_read_b64_tr_b16 v[126:127], v133 offset:62976\n"
        "v_pk_mul_f32 v[0:1], v[0:1], s[50:51]\n"
        "v_pk_mul_f32 v[2:3], v[2:3], s[50:51]\n"
        "v_pk_mul_f32 v[4:5], v[4:5], s[50:51]\n"
        "v_pk_mul_f32 v[6:7], v[6:7], s[50:51]\n"
        "v_cvt_pk_bf16_f32 v0, v0, v1\n"
        "v_cvt_pk_bf16_f32 v2, v2, v3\n"
        "v_cvt_pk_bf16_f32 v4, v4, v5\n"
        "v_cvt_pk_bf16_f32 v6, v6, v7\n"
        "buffer_load_dwordx4 v139, s[56:59], s64 offen offset:1024 lds\n"
        "v_accvgpr_write_b32 a28, v0\n"
        "v_accvgpr_write_b32 a29, v2\n"
        "v_accvgpr_write_b32 a30, v4\n"
        "v_accvgpr_write_b32 a31, v6\n"
        "v_lshlrev_b32 v8, 16, v104\n"
        "v_lshlrev_b32 v10, 16, v105\n"
        "v_lshlrev_b32 v12, 16, v106\n"
        "v_lshlrev_b32 v14, 16, v107\n"
        "v_and_b32 v9, 0xffff0000, v104\n"
        "v_and_b32 v11, 0xffff0000, v105\n"
        "v_and_b32 v13, 0xffff0000, v106\n"
        "v_and_b32 v15, 0xffff0000, v107\n"
        "v_pk_mul_f32 v[8:9], v[8:9], s[50:51]\n"
        "v_pk_mul_f32 v[10:11], v[10:11], s[50:51]\n"
        "v_pk_mul_f32 v[12:13], v[12:13], s[50:51]\n"
        "v_pk_mul_f32 v[14:15], v[14:15], s[50:51]\n"
        "buffer_load_dwordx4 v253, s[56:59], s64 offen offset:2048 lds\n"
        "v_cvt_pk_bf16_f32 v8, v8, v9\n"
        "v_cvt_pk_bf16_f32 v10, v10, v11\n"
        "v_cvt_pk_bf16_f32 v12, v12, v13\n"
        "v_cvt_pk_bf16_f32 v14, v14, v15\n"
        "v_accvgpr_write_b32 a32, v8\n"
        "v_accvgpr_write_b32 a33, v10\n"
        "v_accvgpr_write_b32 a34, v12\n"
        "v_accvgpr_write_b32 a35, v14\n"
        "v_lshlrev_b32 v0, 16, v108\n"
        "v_lshlrev_b32 v2, 16, v109\n"
        "v_lshlrev_b32 v4, 16, v110\n"
        "v_lshlrev_b32 v6, 16, v111\n"
        "v_and_b32 v1, 0xffff0000, v108\n"
        "v_and_b32 v3, 0xffff0000, v109\n"
        "v_and_b32 v5, 0xffff0000, v110\n"
        "v_and_b32 v7, 0xffff0000, v111\n"
        "buffer_load_dwordx4 v253, s[56:59], s64 offen offset:3072 lds\n"
        "v_pk_mul_f32 v[0:1], v[0:1], s[50:51]\n"
        "v_pk_mul_f32 v[2:3], v[2:3], s[50:51]\n"
        "v_pk_mul_f32 v[4:5], v[4:5], s[50:51]\n"
        "v_pk_mul_f32 v[6:7], v[6:7], s[50:51]\n"
        "v_cvt_pk_bf16_f32 v0, v0, v1\n"
        "v_cvt_pk_bf16_f32 v2, v2, v3\n"
        "v_cvt_pk_bf16_f32 v4, v4, v5\n"
        "v_cvt_pk_bf16_f32 v6, v6, v7\n"
        "v_accvgpr_write_b32 a48, v0\n"
        "v_accvgpr_write_b32 a49, v2\n"
        "v_accvgpr_write_b32 a50, v4\n"
        "v_accvgpr_write_b32 a51, v6\n"
        "v_lshlrev_b32 v8, 16, v116\n"
        "v_lshlrev_b32 v10, 16, v117\n"
        "v_lshlrev_b32 v12, 16, v118\n"
        "v_lshlrev_b32 v14, 16, v119\n"
        "v_and_b32 v9, 0xffff0000, v116\n"
        "v_and_b32 v11, 0xffff0000, v117\n"
        "v_and_b32 v13, 0xffff0000, v118\n"
        "v_and_b32 v15, 0xffff0000, v119\n"
        "v_pk_mul_f32 v[8:9], v[8:9], s[50:51]\n"
        "v_pk_mul_f32 v[10:11], v[10:11], s[50:51]\n"
        "v_pk_mul_f32 v[12:13], v[12:13], s[50:51]\n"
        "v_pk_mul_f32 v[14:15], v[14:15], s[50:51]\n"
        "v_cvt_pk_bf16_f32 v8, v8, v9\n"
        "v_cvt_pk_bf16_f32 v10, v10, v11\n"
        "v_cvt_pk_bf16_f32 v12, v12, v13\n"
        "v_cvt_pk_bf16_f32 v14, v14, v15\n"
        "v_accvgpr_write_b32 a52, v8\n"
        "v_accvgpr_write_b32 a53, v10\n"
        "v_accvgpr_write_b32 a54, v12\n"
        "v_accvgpr_write_b32 a55, v14\n"
        "v_lshlrev_b32 v0, 16, v120\n"
        "v_lshlrev_b32 v2, 16, v121\n"
        "v_lshlrev_b32 v4, 16, v122\n"
        "v_lshlrev_b32 v6, 16, v123\n"
        "v_and_b32 v1, 0xffff0000, v120\n"
        "v_and_b32 v3, 0xffff0000, v121\n"
        "v_and_b32 v5, 0xffff0000, v122\n"
        "v_and_b32 v7, 0xffff0000, v123\n"
        "v_pk_mul_f32 v[0:1], v[0:1], s[50:51]\n"
        "v_pk_mul_f32 v[2:3], v[2:3], s[50:51]\n"
        "v_pk_mul_f32 v[4:5], v[4:5], s[50:51]\n"
        "v_pk_mul_f32 v[6:7], v[6:7], s[50:51]\n"
        "v_cvt_pk_bf16_f32 v0, v0, v1\n"
        "v_cvt_pk_bf16_f32 v2, v2, v3\n"
        "v_cvt_pk_bf16_f32 v4, v4, v5\n"
        "v_cvt_pk_bf16_f32 v6, v6, v7\n"
        "v_accvgpr_write_b32 a56, v0\n"
        "v_accvgpr_write_b32 a57, v2\n"
        "v_accvgpr_write_b32 a58, v4\n"
        "v_accvgpr_write_b32 a59, v6\n"
        "s_waitcnt lgkmcnt(15)\n"
        "v_lshlrev_b32 v8, 16, v152\n"
        "v_lshlrev_b32 v10, 16, v153\n"
        "v_lshlrev_b32 v12, 16, v154\n"
        "v_lshlrev_b32 v14, 16, v155\n"
        "v_and_b32 v9, 0xffff0000, v152\n"
        "v_and_b32 v11, 0xffff0000, v153\n"
        "v_and_b32 v13, 0xffff0000, v154\n"
        "v_and_b32 v15, 0xffff0000, v155\n"
        "v_pk_mul_f32 v[8:9], v[8:9], s[50:51]\n"
        "v_pk_mul_f32 v[10:11], v[10:11], s[50:51]\n"
        "v_pk_mul_f32 v[12:13], v[12:13], s[50:51]\n"
        "v_pk_mul_f32 v[14:15], v[14:15], s[50:51]\n"
        "v_cvt_pk_bf16_f32 v8, v8, v9\n"
        "v_cvt_pk_bf16_f32 v10, v10, v11\n"
        "v_cvt_pk_bf16_f32 v12, v12, v13\n"
        "v_cvt_pk_bf16_f32 v14, v14, v15\n"
        "v_accvgpr_write_b32 a12, v8\n"
        "v_accvgpr_write_b32 a13, v10\n"
        "v_accvgpr_write_b32 a14, v12\n"
        "v_accvgpr_write_b32 a15, v14\n"
        "v_lshlrev_b32 v0, 16, v172\n"
        "v_lshlrev_b32 v2, 16, v173\n"
        "s_waitcnt lgkmcnt(14)\n"
        "v_lshlrev_b32 v4, 16, v174\n"
        "v_lshlrev_b32 v6, 16, v175\n"
        "v_and_b32 v1, 0xffff0000, v172\n"
        "v_and_b32 v3, 0xffff0000, v173\n"
        "v_and_b32 v5, 0xffff0000, v174\n"
        "v_and_b32 v7, 0xffff0000, v175\n"
        "v_pk_mul_f32 v[0:1], v[0:1], s[50:51]\n"
        "v_pk_mul_f32 v[2:3], v[2:3], s[50:51]\n"
        "v_pk_mul_f32 v[4:5], v[4:5], s[50:51]\n"
        "v_pk_mul_f32 v[6:7], v[6:7], s[50:51]\n"
        "v_cvt_pk_bf16_f32 v0, v0, v1\n"
        "v_cvt_pk_bf16_f32 v2, v2, v3\n"
        "v_cvt_pk_bf16_f32 v4, v4, v5\n"
        "v_cvt_pk_bf16_f32 v6, v6, v7\n"
        "v_accvgpr_write_b32 a16, v0\n"
        "v_accvgpr_write_b32 a17, v2\n"
        "v_accvgpr_write_b32 a18, v4\n"
        "v_accvgpr_write_b32 a19, v6\n"
        "s_waitcnt lgkmcnt(13)\n"
        "v_lshlrev_b32 v8, 16, v192\n"
        "v_lshlrev_b32 v10, 16, v193\n"
        "s_waitcnt lgkmcnt(12)\n"
        "v_lshlrev_b32 v12, 16, v194\n"
        "v_lshlrev_b32 v14, 16, v195\n"
        "v_and_b32 v9, 0xffff0000, v192\n"
        "v_and_b32 v11, 0xffff0000, v193\n"
        "v_and_b32 v13, 0xffff0000, v194\n"
        "v_and_b32 v15, 0xffff0000, v195\n"
        "v_pk_mul_f32 v[8:9], v[8:9], s[50:51]\n"
        "v_pk_mul_f32 v[10:11], v[10:11], s[50:51]\n"
        "v_pk_mul_f32 v[12:13], v[12:13], s[50:51]\n"
        "v_pk_mul_f32 v[14:15], v[14:15], s[50:51]\n"
        "v_cvt_pk_bf16_f32 v8, v8, v9\n"
        "v_cvt_pk_bf16_f32 v10, v10, v11\n"
        "v_cvt_pk_bf16_f32 v12, v12, v13\n"
        "v_cvt_pk_bf16_f32 v14, v14, v15\n"
        "v_accvgpr_write_b32 a20, v8\n"
        "v_accvgpr_write_b32 a21, v10\n"
        "v_accvgpr_write_b32 a22, v12\n"
        "v_accvgpr_write_b32 a23, v14\n"
        "s_waitcnt lgkmcnt(11)\n"
        "v_lshlrev_b32 v0, 16, v212\n"
        "v_lshlrev_b32 v2, 16, v213\n"
        "s_waitcnt lgkmcnt(10)\n"
        "v_lshlrev_b32 v4, 16, v214\n"
        "v_lshlrev_b32 v6, 16, v215\n"
        "v_and_b32 v1, 0xffff0000, v212\n"
        "v_and_b32 v3, 0xffff0000, v213\n"
        "v_and_b32 v5, 0xffff0000, v214\n"
        "v_and_b32 v7, 0xffff0000, v215\n"
        "v_pk_mul_f32 v[0:1], v[0:1], s[50:51]\n"
        "v_pk_mul_f32 v[2:3], v[2:3], s[50:51]\n"
        "v_pk_mul_f32 v[4:5], v[4:5], s[50:51]\n"
        "v_pk_mul_f32 v[6:7], v[6:7], s[50:51]\n"
        "v_cvt_pk_bf16_f32 v0, v0, v1\n"
        "v_cvt_pk_bf16_f32 v2, v2, v3\n"
        "v_cvt_pk_bf16_f32 v4, v4, v5\n"
        "v_cvt_pk_bf16_f32 v6, v6, v7\n"
        "s_add_u32 m0, s40, 98304\n"
        "v_accvgpr_write_b32 a36, v0\n"
        "v_accvgpr_write_b32 a37, v2\n"
        "v_accvgpr_write_b32 a38, v4\n"
        "v_accvgpr_write_b32 a39, v6\n"
        "s_add_u32 s64, s65, 384\n"
        "s_waitcnt lgkmcnt(9)\n"
        "v_lshlrev_b32 v8, 16, v156\n"
        "v_lshlrev_b32 v10, 16, v157\n"
        "s_waitcnt lgkmcnt(8)\n"
        "v_lshlrev_b32 v12, 16, v158\n"
        "v_lshlrev_b32 v14, 16, v159\n"
        "buffer_load_dwordx4 v128, s[20:23], s64 offen lds\n"
        "v_and_b32 v9, 0xffff0000, v156\n"
        "v_and_b32 v11, 0xffff0000, v157\n"
        "v_and_b32 v13, 0xffff0000, v158\n"
        "v_and_b32 v15, 0xffff0000, v159\n"
        "v_pk_mul_f32 v[8:9], v[8:9], s[50:51]\n"
        "v_pk_mul_f32 v[10:11], v[10:11], s[50:51]\n"
        "v_pk_mul_f32 v[12:13], v[12:13], s[50:51]\n"
        "v_pk_mul_f32 v[14:15], v[14:15], s[50:51]\n"
        "v_cvt_pk_bf16_f32 v8, v8, v9\n"
        "v_cvt_pk_bf16_f32 v10, v10, v11\n"
        "v_cvt_pk_bf16_f32 v12, v12, v13\n"
        "v_cvt_pk_bf16_f32 v14, v14, v15\n"
        "v_accvgpr_write_b32 a40, v8\n"
        "v_accvgpr_write_b32 a41, v10\n"
        "v_accvgpr_write_b32 a42, v12\n"
        "v_accvgpr_write_b32 a43, v14\n"
        "buffer_load_dwordx4 v129, s[20:23], s64 offen offset:1024 lds\n"
        "s_waitcnt lgkmcnt(7)\n"
        "v_lshlrev_b32 v0, 16, v176\n"
        "v_lshlrev_b32 v2, 16, v177\n"
        "s_waitcnt lgkmcnt(6)\n"
        "v_lshlrev_b32 v4, 16, v178\n"
        "v_lshlrev_b32 v6, 16, v179\n"
        "v_and_b32 v1, 0xffff0000, v176\n"
        "v_and_b32 v3, 0xffff0000, v177\n"
        "v_and_b32 v5, 0xffff0000, v178\n"
        "v_and_b32 v7, 0xffff0000, v179\n"
        "v_pk_mul_f32 v[0:1], v[0:1], s[50:51]\n"
        "v_pk_mul_f32 v[2:3], v[2:3], s[50:51]\n"
        "v_pk_mul_f32 v[4:5], v[4:5], s[50:51]\n"
        "v_pk_mul_f32 v[6:7], v[6:7], s[50:51]\n"
        "v_cvt_pk_bf16_f32 v0, v0, v1\n"
        "v_cvt_pk_bf16_f32 v2, v2, v3\n"
        "v_cvt_pk_bf16_f32 v4, v4, v5\n"
        "v_cvt_pk_bf16_f32 v6, v6, v7\n"
        "buffer_load_dwordx4 v130, s[20:23], s64 offen offset:2048 lds\n"
        "v_accvgpr_write_b32 a44, v0\n"
        "v_accvgpr_write_b32 a45, v2\n"
        "v_accvgpr_write_b32 a46, v4\n"
        "v_accvgpr_write_b32 a47, v6\n"
        "s_waitcnt lgkmcnt(5)\n"
        "v_lshlrev_b32 v8, 16, v196\n"
        "v_lshlrev_b32 v10, 16, v197\n"
        "s_waitcnt lgkmcnt(4)\n"
        "v_lshlrev_b32 v12, 16, v198\n"
        "v_lshlrev_b32 v14, 16, v199\n"
        "s_add_u32 m0, s40, 110592\n"
        "v_and_b32 v9, 0xffff0000, v196\n"
        "v_and_b32 v11, 0xffff0000, v197\n"
        "v_and_b32 v13, 0xffff0000, v198\n"
        "v_and_b32 v15, 0xffff0000, v199\n"
        "s_add_u32 s64, s65, 37248\n"
        "v_pk_mul_f32 v[8:9], v[8:9], s[50:51]\n"
        "v_pk_mul_f32 v[10:11], v[10:11], s[50:51]\n"
        "v_pk_mul_f32 v[12:13], v[12:13], s[50:51]\n"
        "v_pk_mul_f32 v[14:15], v[14:15], s[50:51]\n"
        "buffer_load_dwordx4 v128, s[20:23], s64 offen lds\n"
        "v_cvt_pk_bf16_f32 v8, v8, v9\n"
        "v_cvt_pk_bf16_f32 v10, v10, v11\n"
        "v_cvt_pk_bf16_f32 v12, v12, v13\n"
        "v_cvt_pk_bf16_f32 v14, v14, v15\n"
        "v_accvgpr_write_b32 a60, v8\n"
        "v_accvgpr_write_b32 a61, v10\n"
        "v_accvgpr_write_b32 a62, v12\n"
        "v_accvgpr_write_b32 a63, v14\n"
        "s_waitcnt lgkmcnt(3)\n"
        "v_lshlrev_b32 v0, 16, v216\n"
        "v_lshlrev_b32 v2, 16, v217\n"
        "s_waitcnt lgkmcnt(2)\n"
        "v_lshlrev_b32 v4, 16, v218\n"
        "v_lshlrev_b32 v6, 16, v219\n"
        "v_and_b32 v1, 0xffff0000, v216\n"
        "v_and_b32 v3, 0xffff0000, v217\n"
        "v_and_b32 v5, 0xffff0000, v218\n"
        "v_and_b32 v7, 0xffff0000, v219\n"
        "buffer_load_dwordx4 v129, s[20:23], s64 offen offset:1024 lds\n"
        "v_pk_mul_f32 v[0:1], v[0:1], s[50:51]\n"
        "v_pk_mul_f32 v[2:3], v[2:3], s[50:51]\n"
        "v_pk_mul_f32 v[4:5], v[4:5], s[50:51]\n"
        "v_pk_mul_f32 v[6:7], v[6:7], s[50:51]\n"
        "v_cvt_pk_bf16_f32 v0, v0, v1\n"
        "v_cvt_pk_bf16_f32 v2, v2, v3\n"
        "v_cvt_pk_bf16_f32 v4, v4, v5\n"
        "v_cvt_pk_bf16_f32 v6, v6, v7\n"
        "v_accvgpr_write_b32 a64, v0\n"
        "v_accvgpr_write_b32 a65, v2\n"
        "v_accvgpr_write_b32 a66, v4\n"
        "v_accvgpr_write_b32 a67, v6\n"
        "s_waitcnt lgkmcnt(1)\n"
        "v_lshlrev_b32 v8, 16, v124\n"
        "v_lshlrev_b32 v10, 16, v125\n"
        "s_waitcnt lgkmcnt(0)\n"
        "v_lshlrev_b32 v12, 16, v126\n"
        "v_lshlrev_b32 v14, 16, v127\n"
        "buffer_load_dwordx4 v130, s[20:23], s64 offen offset:2048 lds\n"
        "v_and_b32 v9, 0xffff0000, v124\n"
        "v_and_b32 v11, 0xffff0000, v125\n"
        "v_and_b32 v13, 0xffff0000, v126\n"
        "v_and_b32 v15, 0xffff0000, v127\n"
        "v_pk_mul_f32 v[8:9], v[8:9], s[50:51]\n"
        "v_pk_mul_f32 v[10:11], v[10:11], s[50:51]\n"
        "v_pk_mul_f32 v[12:13], v[12:13], s[50:51]\n"
        "v_pk_mul_f32 v[14:15], v[14:15], s[50:51]\n"
        "s_add_u32 m0, s40, 122880\n"
        "v_cvt_pk_bf16_f32 v8, v8, v9\n"
        "v_cvt_pk_bf16_f32 v10, v10, v11\n"
        "v_cvt_pk_bf16_f32 v12, v12, v13\n"
        "v_cvt_pk_bf16_f32 v14, v14, v15\n"
        "s_add_u32 s64, s65, 74112\n"
        "v_accvgpr_write_b32 a68, v8\n"
        "v_accvgpr_write_b32 a69, v10\n"
        "v_accvgpr_write_b32 a70, v12\n"
        "v_accvgpr_write_b32 a71, v14\n"
        "buffer_load_dwordx4 v128, s[20:23], s64 offen lds\n"
        "buffer_load_dwordx4 v129, s[20:23], s64 offen offset:1024 lds\n"
        "buffer_load_dwordx4 v130, s[20:23], s64 offen offset:2048 lds\n"
        "s_waitcnt vmcnt(20)\n"
        "s_barrier\n"
        "ds_read_b128 a[224:227], v150 offset:0\n"
        "s_waitcnt lgkmcnt(0)\n"
        "v_mfma_f32_16x16x32_bf16 v[96:99], a[0:3], a[224:227], 0\n"
        "ds_read_b128 a[228:231], v151 offset:0\n"
        "ds_read_b128 a[232:235], v150 offset:128\n"
        "s_add_u32 m0, s40, 0\n"
        "s_add_u32 s64, s65, 110976\n"
        "s_waitcnt lgkmcnt(1)\n"
        "v_mfma_f32_16x16x32_bf16 v[96:99], a[4:7], a[228:231], v[96:99]\n"
        "s_waitcnt lgkmcnt(0)\n"
        "ds_read_b128 a[236:239], v151 offset:128\n"
        "ds_read_b128 a[240:243], v150 offset:256\n"
        "v_mfma_f32_16x16x32_bf16 v[96:99], a[8:11], a[232:235], v[96:99]\n"
        "s_waitcnt lgkmcnt(1)\n"
        "ds_read_b128 a[244:247], v151 offset:256\n"
        "buffer_load_dwordx4 v128, s[20:23], s64 offen lds\n"
        "v_mfma_f32_16x16x32_bf16 v[96:99], a[12:15], a[236:239], v[96:99]\n"
        "s_waitcnt lgkmcnt(1)\n"
        "ds_read_b128 a[248:251], v150 offset:1536\n"
        "ds_read_b128 a[216:219], v151 offset:1536\n"
        "v_mfma_f32_16x16x32_bf16 v[96:99], a[16:19], a[240:243], v[96:99]\n"
        "s_waitcnt lgkmcnt(2)\n"
        "ds_read_b128 a[220:223], v150 offset:1664\n"
        "v_mfma_f32_16x16x32_bf16 v[96:99], a[20:23], a[244:247], v[96:99]\n"
        "buffer_load_dwordx4 v129, s[20:23], s64 offen offset:1024 lds\n"
        "v_mfma_f32_16x16x32_bf16 v[100:103], a[24:27], a[224:227], 0\n"
        "v_mfma_f32_16x16x32_bf16 v[100:103], a[28:31], a[228:231], v[100:103]\n"
        "v_mfma_f32_16x16x32_bf16 v[100:103], a[32:35], a[232:235], v[100:103]\n"
        "v_mfma_f32_16x16x32_bf16 v[100:103], a[36:39], a[236:239], v[100:103]\n"
        "buffer_load_dwordx4 v130, s[20:23], s64 offen offset:2048 lds\n"
        "v_mfma_f32_16x16x32_bf16 v[100:103], a[40:43], a[240:243], v[100:103]\n"
        "s_waitcnt vmcnt(19)\n"
        "v_lshlrev_b32 v236, 16, v236\n"
        "v_cvt_pk_bf16_f32 v156, v96, v97\n"
        "v_mfma_f32_16x16x32_bf16 v[100:103], a[44:47], a[244:247], v[100:103]\n"
        "s_add_u32 m0, s40, 12288\n"
        "ds_write_b32 v146, v236 offset:0\n"
        "v_mfma_f32_16x16x32_bf16 v[112:115], a[48:51], a[224:227], 0\n"
        "ds_read_b128 a[224:227], v151 offset:1664\n"
        "s_add_u32 s64, s65, 147840\n"
        "v_cvt_pk_bf16_f32 v157, v98, v99\n"
        "s_waitcnt vmcnt(18)\n"
        "v_lshlrev_b32 v237, 16, v237\n"
        "v_mfma_f32_16x16x32_bf16 v[112:115], a[52:55], a[228:231], v[112:115]\n"
        "ds_read_b128 a[228:231], v150 offset:1792\n"
        "buffer_load_dwordx4 v128, s[20:23], s64 offen lds\n"
        "v_cvt_pk_bf16_f32 v158, v100, v101\n"
        "v_mfma_f32_16x16x32_bf16 v[112:115], a[56:59], a[232:235], v[112:115]\n"
        "ds_read_b128 a[232:235], v151 offset:1792\n"
        "ds_write_b32 v146, v237 offset:256\n"
        "v_mfma_f32_16x16x32_bf16 v[112:115], a[60:63], a[236:239], v[112:115]\n"
        "v_cvt_pk_bf16_f32 v159, v102, v103\n"
        "s_waitcnt vmcnt(18)\n"
        "v_lshlrev_b32 v238, 16, v238\n"
        "s_waitcnt vmcnt(17)\n"
        "v_lshlrev_b32 v239, 16, v239\n"
        "v_mfma_f32_16x16x32_bf16 v[112:115], a[64:67], a[240:243], v[112:115]\n"
        "v_mfma_f32_16x16x32_bf16 v[112:115], a[68:71], a[244:247], v[112:115]\n"
        "s_waitcnt lgkmcnt(7)\n"
        "ds_write_b32 v146, v238 offset:512\n"
        "buffer_load_dwordx4 v129, s[20:23], s64 offen offset:1024 lds\n"
        "v_mfma_f32_16x16x32_bf16 v[96:99], a[0:3], a[248:251], 0\n"
        "s_waitcnt lgkmcnt(7)\n"
        "ds_write_b32 v146, v239 offset:768\n"
        "s_add_u32 s35, s47, 786432\n"
        "s_sub_u32 s46, s35, 100663296\n"
        "s_cmp_lt_u32 s46, 786432\n"
        "s_waitcnt vmcnt(5)\n"
        "s_barrier\n"
        "v_mfma_f32_16x16x32_bf16 v[96:99], a[4:7], a[216:219], v[96:99]\n"
        "s_waitcnt lgkmcnt(7)\n"
        "s_cselect_b32 s35, s46, s35\n"
        "s_add_u32 s41, s30, s40\n"
        "ds_read_b64_tr_b16 a[72:73], v136 offset:0\n"
        "ds_read_b64_tr_b16 a[74:75], v136 offset:1536\n"
        "v_mfma_f32_16x16x32_bf16 v[96:99], a[8:11], a[220:223], v[96:99]\n"
        "s_waitcnt lgkmcnt(7)\n"
        "ds_read_b64_tr_b16 a[76:77], v136 offset:12288\n"
        "ds_read_b64_tr_b16 a[78:79], v136 offset:13824\n"
        "v_cvt_pk_bf16_f32 v160, v112, v113\n"
        "v_mfma_f32_16x16x32_bf16 v[96:99], a[12:15], a[224:227], v[96:99]\n"
        "s_waitcnt lgkmcnt(8)\n"
        "buffer_load_dwordx4 v130, s[20:23], s64 offen offset:2048 lds\n"
        "ds_read_b64_tr_b16 a[80:81], v136 offset:24576\n"
        "v_cvt_pk_bf16_f32 v161, v114, v115\n"
        "v_mfma_f32_16x16x32_bf16 v[96:99], a[16:19], a[228:231], v[96:99]\n"
        "s_waitcnt lgkmcnt(8)\n"
        "ds_read_b64_tr_b16 a[82:83], v136 offset:26112\n"
        "ds_read_b64_tr_b16 a[96:97], v137 offset:0\n"
        "v_cvt_pk_bf16_f32 v166, v112, v113\n"
        "v_mfma_f32_16x16x32_bf16 v[96:99], a[20:23], a[232:235], v[96:99]\n"
        "s_add_u32 m0, s40, 24576\n"
        "ds_read_b64_tr_b16 a[98:99], v137 offset:1536\n"
        "ds_read_b128 a[236:239], v150 offset:12288\n"
        "v_cvt_pk_bf16_f32 v167, v114, v115\n"
        "v_mfma_f32_16x16x32_bf16 v[100:103], a[24:27], a[248:251], 0\n"
        "s_add_u32 s64, s65, 184704\n"
        "ds_read_b64_tr_b16 a[100:101], v137 offset:12288\n"
        "ds_read_b128 a[240:243], v151 offset:12288\n"
        "v_cvt_pk_bf16_f32 v176, v96, v97\n"
        "v_mfma_f32_16x16x32_bf16 v[100:103], a[28:31], a[216:219], v[100:103]\n"
        "buffer_load_dwordx4 v128, s[20:23], s64 offen lds\n"
        "ds_read_b64_tr_b16 a[102:103], v137 offset:13824\n"
        "v_cvt_pk_bf16_f32 v177, v98, v99\n"
        "v_mfma_f32_16x16x32_bf16 v[100:103], a[32:35], a[220:223], v[100:103]\n"
        "ds_read_b128 v[0:3], v232 offset:0\n"
        "ds_read_b128 a[244:247], v150 offset:12416\n"
        "v_mfma_f32_16x16x32_bf16 v[100:103], a[36:39], a[224:227], v[100:103]\n"
        "ds_read_b64_tr_b16 a[104:105], v137 offset:24576\n"
        "ds_read_b64_tr_b16 a[106:107], v137 offset:26112\n"
        "v_mfma_f32_16x16x32_bf16 v[100:103], a[40:43], a[228:231], v[100:103]\n"
        "ds_read_b64_tr_b16 a[120:121], v138 offset:0\n"
        "ds_read_b64_tr_b16 a[122:123], v138 offset:1536\n"
        "s_waitcnt lgkmcnt(5)\n"
        "v_lshl_add_u32 v0, v0, 2, s67\n"
        "v_mfma_f32_16x16x32_bf16 v[100:103], a[44:47], a[232:235], v[100:103]\n"
        "buffer_load_dwordx4 v129, s[20:23], s64 offen offset:1024 lds\n"
        "ds_read_b64_tr_b16 a[124:125], v138 offset:12288\n"
        "v_lshl_add_u32 v1, v1, 2, s67\n"
        "v_mfma_f32_16x16x32_bf16 v[112:115], a[48:51], a[248:251], 0\n"
        "ds_read_b128 a[248:251], v151 offset:12416\n"
        "ds_read_b128 v[16:19], v232 offset:1024\n"
        "v_lshl_add_u32 v2, v2, 2, s67\n"
        "v_mfma_f32_16x16x32_bf16 v[112:115], a[52:55], a[216:219], v[112:115]\n"
        "ds_read_b128 a[216:219], v150 offset:12544\n"
        "ds_read_b64_tr_b16 a[126:127], v138 offset:13824\n"
        "v_cvt_pk_bf16_f32 v178, v100, v101\n"
        "v_mfma_f32_16x16x32_bf16 v[112:115], a[56:59], a[220:223], v[112:115]\n"
        "ds_read_b128 a[220:223], v151 offset:12544\n"
        "ds_read_b64_tr_b16 a[128:129], v138 offset:24576\n"
        "v_cvt_pk_bf16_f32 v179, v102, v103\n"
        "v_mfma_f32_16x16x32_bf16 v[112:115], a[60:63], a[224:227], v[112:115]\n"
        "buffer_load_dwordx4 v130, s[20:23], s64 offen offset:2048 lds\n"
        "ds_read_b64_tr_b16 a[130:131], v138 offset:26112\n"
        "v_lshl_add_u32 v3, v3, 2, s67\n"
        "v_mfma_f32_16x16x32_bf16 v[112:115], a[64:67], a[228:231], v[112:115]\n"
        "ds_read_b128 v[32:35], v232 offset:8192\n"
        "ds_read_b128 v[48:51], v232 offset:9216\n"
        "s_waitcnt lgkmcnt(7)\n"
        "v_lshl_add_u32 v16, v16, 2, s67\n"
        "v_mfma_f32_16x16x32_bf16 v[112:115], a[68:71], a[232:235], v[112:115]\n"
        "ds_read_b128 a[224:227], v150 offset:13824\n"
        "ds_read_b128 a[228:231], v151 offset:13824\n"
        "v_xor_b32 v253, 32, v249\n"
        "v_mfma_f32_16x16x32_bf16 v[96:99], a[0:3], a[236:239], 0\n"
        "ds_read_b128 a[232:235], v150 offset:13952\n"
        "ds_read_b32 v0, v0\n"
        "v_lshl_add_u32 v17, v17, 2, s67\n"
        "v_mfma_f32_16x16x32_bf16 v[96:99], a[4:7], a[240:243], v[96:99]\n"
        "s_xor_b32 m0, s48, 8192\n"
        "ds_read_b32 v1, v1\n"
        "ds_read_b32 v2, v2\n"
        "v_cvt_pk_bf16_f32 v180, v112, v113\n"
        "v_mfma_f32_16x16x32_bf16 v[96:99], a[8:11], a[244:247], v[96:99]\n"
        "s_sub_u32 s64, s36, 2097152\n"
        "ds_read_b128 v[4:7], v233 offset:0\n"
        "ds_read_b32 v3, v3\n"
        "v_cvt_pk_bf16_f32 v181, v114, v115\n"
        "v_mfma_f32_16x16x32_bf16 v[96:99], a[12:15], a[248:251], v[96:99]\n"
        "s_waitcnt lgkmcnt(14)\n"
        "buffer_load_dwordx4 v249, s[12:15], s64 offen lds\n"
        "ds_read_b32 v16, v16\n"
        "v_cvt_pk_bf16_f32 v186, v112, v113\n"
        "v_mfma_f32_16x16x32_bf16 v[96:99], a[16:19], a[216:219], v[96:99]\n"
        "s_waitcnt lgkmcnt(13)\n"
        "ds_read_b32 v17, v17\n"
        "v_cvt_pk_bf16_f32 v187, v114, v115\n"
        "v_lshl_add_u32 v18, v18, 2, s67\n"
        "v_mfma_f32_16x16x32_bf16 v[96:99], a[20:23], a[220:223], v[96:99]\n"
        "ds_read_b32 v18, v18\n"
        "v_lshl_add_u32 v19, v19, 2, s67\n"
        "s_waitcnt lgkmcnt(12)\n"
        "v_lshl_add_u32 v32, v32, 2, s67\n"
        "v_mfma_f32_16x16x32_bf16 v[100:103], a[24:27], a[236:239], 0\n"
        "s_barrier\n"
        "ds_read_b32 v19, v19\n"
        "ds_read_b32 v32, v32\n"
        "v_lshl_add_u32 v33, v33, 2, s67\n"
        "v_mfma_f32_16x16x32_bf16 v[100:103], a[28:31], a[240:243], v[100:103]\n"
        "buffer_load_dwordx4 v253, s[12:15], s64 offen offset:1024 lds\n"
        "ds_read_b128 v[20:23], v233 offset:1024\n"
        "v_cvt_pk_bf16_f32 v196, v96, v97\n"
        "v_mfma_f32_16x16x32_bf16 v[100:103], a[32:35], a[244:247], v[100:103]\n"
        "ds_read_b32 v33, v33\n"
        "v_cvt_pk_bf16_f32 v197, v98, v99\n"
        "v_lshl_add_u32 v34, v34, 2, s67\n"
        "v_mfma_f32_16x16x32_bf16 v[100:103], a[36:39], a[248:251], v[100:103]\n"
        "ds_read_b32 v34, v34\n"
        "v_lshl_add_u32 v35, v35, 2, s67\n"
        "s_waitcnt lgkmcnt(15)\n"
        "v_lshl_add_u32 v48, v48, 2, s67\n"
        "v_mfma_f32_16x16x32_bf16 v[100:103], a[40:43], a[216:219], v[100:103]\n"
        "s_add_u32 m0, s41, 0\n"
        "ds_read_b32 v35, v35\n"
        "ds_read_b32 v48, v48\n"
        "v_lshl_add_u32 v49, v49, 2, s67\n"
        "v_mfma_f32_16x16x32_bf16 v[100:103], a[44:47], a[220:223], v[100:103]\n"
        "buffer_load_dwordx4 v242, s[8:11], s47 offen lds\n"
        "ds_read_b32 v49, v49\n"
        "v_mfma_f32_16x16x32_bf16 v[112:115], a[48:51], a[236:239], 0\n"
        "ds_read_b128 a[236:239], v151 offset:13952\n"
        "ds_read_b128 v[36:39], v233 offset:8192\n"
        "v_lshl_add_u32 v50, v50, 2, s67\n"
        "v_mfma_f32_16x16x32_bf16 v[112:115], a[52:55], a[240:243], v[112:115]\n"
        "ds_read_b128 a[240:243], v150 offset:14080\n"
        "ds_read_b32 v50, v50\n"
        "v_cvt_pk_bf16_f32 v198, v100, v101\n"
        "v_mfma_f32_16x16x32_bf16 v[112:115], a[56:59], a[244:247], v[112:115]\n"
        "ds_read_b128 a[244:247], v151 offset:14080\n"
        "v_cvt_pk_bf16_f32 v199, v102, v103\n"
        "v_lshl_add_u32 v51, v51, 2, s67\n"
        "v_mfma_f32_16x16x32_bf16 v[112:115], a[60:63], a[248:251], v[112:115]\n"
        "s_add_u32 m0, s41, 1024\n"
        "ds_read_b128 a[248:251], v150 offset:0\n"
        "ds_read_b32 v51, v51\n"
        "s_waitcnt lgkmcnt(15)\n"
        "v_lshl_add_u32 v4, v4, 2, s67\n"
        "v_mfma_f32_16x16x32_bf16 v[112:115], a[64:67], a[216:219], v[112:115]\n"
        "buffer_load_dwordx4 v243, s[8:11], s47 offen lds\n"
        "ds_read_b128 a[216:219], v151 offset:0\n"
        "v_mfma_f32_16x16x32_bf16 v[112:115], a[68:71], a[220:223], v[112:115]\n"
        "ds_read_b128 v[52:55], v233 offset:9216\n"
        "ds_read_b128 a[220:223], v150 offset:128\n"
        "v_lshl_add_u32 v5, v5, 2, s67\n"
        "v_mfma_f32_16x16x32_bf16 v[96:99], a[0:3], a[224:227], 0\n"
        "ds_read_b32 v4, v4\n"
        "ds_read_b32 v5, v5\n"
        "v_lshl_add_u32 v6, v6, 2, s67\n"
        "v_mfma_f32_16x16x32_bf16 v[96:99], a[4:7], a[228:231], v[96:99]\n"
        "ds_read_b32 v6, v6\n"
        "v_cvt_pk_bf16_f32 v200, v112, v113\n"
        "v_cvt_pk_bf16_f32 v201, v114, v115\n"
        "v_mfma_f32_16x16x32_bf16 v[96:99], a[8:11], a[232:235], v[96:99]\n"
        "s_waitcnt lgkmcnt(12)\n"
        "s_add_u32 m0, s41, 2048\n"
        "v_cvt_pk_bf16_f32 v206, v112, v113\n"
        "v_cvt_pk_bf16_f32 v207, v114, v115\n"
        "v_lshl_add_u32 v7, v7, 2, s67\n"
        "v_mfma_f32_16x16x32_bf16 v[96:99], a[12:15], a[236:239], v[96:99]\n"
        "s_waitcnt lgkmcnt(10)\n"
        "buffer_load_dwordx4 v244, s[8:11], s47 offen lds\n"
        "ds_read_b32 v7, v7\n"
        "v_mfma_f32_16x16x32_bf16 v[96:99], a[16:19], a[240:243], v[96:99]\n"
        "s_waitcnt lgkmcnt(9)\n"
        "ds_read_b128 v[8:11], v234 offset:0\n"
        "v_lshl_add_u32 v20, v20, 2, s67\n"
        "v_lshl_add_u32 v21, v21, 2, s67\n"
        "v_mfma_f32_16x16x32_bf16 v[96:99], a[20:23], a[244:247], v[96:99]\n"
        "ds_read_b32 v20, v20\n"
        "ds_read_b32 v21, v21\n"
        "v_lshl_add_u32 v22, v22, 2, s67\n"
        "v_mfma_f32_16x16x32_bf16 v[100:103], a[24:27], a[224:227], 0\n"
        "s_waitcnt vmcnt(5)\n"
        "s_barrier\n"
        "ds_read_b32 v22, v22\n"
        "v_lshl_add_u32 v23, v23, 2, s67\n"
        "v_lshl_add_u32 v36, v36, 2, s67\n"
        "v_mfma_f32_16x16x32_bf16 v[100:103], a[28:31], a[228:231], v[100:103]\n"
        "s_add_u32 m0, s41, 12288\n"
        "ds_read_b64_tr_b16 a[84:85], v131 offset:0\n"
        "ds_read_b64_tr_b16 a[86:87], v131 offset:1536\n"
        "ds_read_b64_tr_b16 a[88:89], v131 offset:12288\n"
        "v_mfma_f32_16x16x32_bf16 v[100:103], a[32:35], a[232:235], v[100:103]\n"
        "ds_read_b64_tr_b16 a[90:91], v131 offset:13824\n"
        "ds_read_b64_tr_b16 a[92:93], v131 offset:24576\n"
        "ds_read_b64_tr_b16 a[94:95], v131 offset:26112\n"
        "s_add_u32 s47, s47, 6291456\n"
        "v_mfma_f32_16x16x32_bf16 v[100:103], a[36:39], a[236:239], v[100:103]\n"
        "ds_read_b64_tr_b16 a[108:109], v132 offset:0\n"
        "ds_read_b64_tr_b16 a[110:111], v132 offset:1536\n"
        "ds_read_b64_tr_b16 a[112:113], v132 offset:12288\n"
        "s_sub_u32 s31, s30, s28\n"
        "v_mfma_f32_16x16x32_bf16 v[100:103], a[40:43], a[240:243], v[100:103]\n"
        "ds_read_b64_tr_b16 a[114:115], v132 offset:13824\n"
        "ds_read_b64_tr_b16 a[116:117], v132 offset:24576\n"
        "ds_read_b64_tr_b16 a[118:119], v132 offset:26112\n"
        "s_sub_u32 s45, s28, s29\n"
        "v_mfma_f32_16x16x32_bf16 v[100:103], a[44:47], a[244:247], v[100:103]\n"
        "ds_read_b64_tr_b16 a[132:133], v133 offset:0\n"
        "ds_read_b64_tr_b16 a[134:135], v133 offset:1536\n"
        "ds_read_b64_tr_b16 a[136:137], v133 offset:12288\n"
        "s_add_u32 s28, s28, s31\n"
        "v_mfma_f32_16x16x32_bf16 v[112:115], a[48:51], a[224:227], 0\n"
        "ds_read_b64_tr_b16 a[138:139], v133 offset:13824\n"
        "ds_read_b64_tr_b16 a[140:141], v133 offset:24576\n"
        "ds_read_b64_tr_b16 a[142:143], v133 offset:26112\n"
        "s_mov_b32 s30, s29\n"
        "v_mfma_f32_16x16x32_bf16 v[112:115], a[52:55], a[228:231], v[112:115]\n"
        "buffer_load_dwordx4 v242, s[8:11], s35 offen lds\n"
        "ds_read_b128 a[224:227], v151 offset:128\n"
        "s_add_u32 m0, s41, 13312\n"
        "v_mfma_f32_16x16x32_bf16 v[112:115], a[56:59], a[232:235], v[112:115]\n"
        "ds_read_b128 a[228:231], v150 offset:256\n"
        "buffer_load_dwordx4 v243, s[8:11], s35 offen lds\n"
        "s_add_u32 s29, s29, s45\n"
        "v_mfma_f32_16x16x32_bf16 v[112:115], a[60:63], a[236:239], v[112:115]\n"
        "ds_read_b128 a[232:235], v151 offset:256\n"
        "ds_read_b128 v[24:27], v234 offset:1024\n"
        "v_cvt_pk_bf16_f32 v216, v96, v97\n"
        "v_mfma_f32_16x16x32_bf16 v[112:115], a[64:67], a[240:243], v[112:115]\n"
        "ds_read_b32 v23, v23\n"
        "ds_read_b128 v[40:43], v234 offset:8192\n"
        "v_cvt_pk_bf16_f32 v217, v98, v99\n"
        "v_mfma_f32_16x16x32_bf16 v[112:115], a[68:71], a[244:247], v[112:115]\n"
        "s_waitcnt lgkmcnt(15)\n"
        "s_add_u32 m0, s41, 14336\n"
        "ds_read_b32 v36, v36\n"
        "ds_read_b128 a[236:239], v150 offset:1536\n"
        "v_cvt_pk_bf16_f32 v218, v100, v101\n"
        "v_mfma_f32_16x16x32_bf16 v[96:99], a[72:75], a[248:251], 0\n"
        "s_waitcnt lgkmcnt(15)\n"
        "buffer_load_dwordx4 v244, s[8:11], s35 offen lds\n"
        "ds_read_b128 a[240:243], v151 offset:1536\n"
        "v_mfma_f32_16x16x32_bf16 v[96:99], a[76:79], a[216:219], v[96:99]\n"
        "s_waitcnt lgkmcnt(15)\n"
        "ds_read_b128 a[244:247], v150 offset:1664\n"
        "ds_read_b128 v[56:59], v234 offset:9216\n"
        "v_cvt_pk_bf16_f32 v219, v102, v103\n"
        "v_mfma_f32_16x16x32_bf16 v[96:99], a[80:83], a[220:223], v[96:99]\n"
        "s_waitcnt lgkmcnt(10)\n"
        "v_cvt_pk_bf16_f32 v220, v112, v113\n"
        "v_cvt_pk_bf16_f32 v221, v114, v115\n"
        "v_cvt_pk_bf16_f32 v226, v112, v113\n"
        "v_mfma_f32_16x16x32_bf16 v[96:99], a[84:87], a[224:227], v[96:99]\n"
        "s_waitcnt lgkmcnt(9)\n"
        "s_add_u32 m0, s40, 36864\n"
        "v_cvt_pk_bf16_f32 v227, v114, v115\n"
        "v_lshl_add_u32 v37, v37, 2, s67\n"
        "v_lshl_add_u32 v38, v38, 2, s67\n"
        "v_mfma_f32_16x16x32_bf16 v[96:99], a[88:91], a[228:231], v[96:99]\n"
        "s_waitcnt lgkmcnt(8)\n"
        "s_add_u32 s64, s65, 768\n"
        "ds_read_b32 v37, v37\n"
        "ds_read_b32 v38, v38\n"
        "v_lshl_add_u32 v39, v39, 2, s67\n"
        "v_mfma_f32_16x16x32_bf16 v[96:99], a[92:95], a[232:235], v[96:99]\n"
        "buffer_load_dwordx4 v128, s[20:23], s64 offen lds\n"
        "ds_read_b32 v39, v39\n"
        "v_lshl_add_u32 v52, v52, 2, s67\n"
        "v_mfma_f32_16x16x32_bf16 v[100:103], a[96:99], a[248:251], 0\n"
        "ds_read_b32 v52, v52\n"
        "ds_read_b128 v[12:15], v235 offset:0\n"
        "v_lshl_add_u32 v53, v53, 2, s67\n"
        "v_mfma_f32_16x16x32_bf16 v[100:103], a[100:103], a[216:219], v[100:103]\n"
        "ds_read_b32 v53, v53\n"
        "v_cvt_pk_bf16_f32 v152, v96, v97\n"
        "v_cvt_pk_bf16_f32 v153, v98, v99\n"
        "v_mfma_f32_16x16x32_bf16 v[100:103], a[104:107], a[220:223], v[100:103]\n"
        "v_lshl_add_u32 v54, v54, 2, s67\n"
        "v_lshl_add_u32 v55, v55, 2, s67\n"
        "v_lshl_add_u32 v8, v8, 2, s67\n"
        "v_mfma_f32_16x16x32_bf16 v[100:103], a[108:111], a[224:227], v[100:103]\n"
        "ds_read_b32 v54, v54\n"
        "ds_read_b32 v55, v55\n"
        "v_lshl_add_u32 v9, v9, 2, s67\n"
        "v_mfma_f32_16x16x32_bf16 v[100:103], a[112:115], a[228:231], v[100:103]\n"
        "buffer_load_dwordx4 v129, s[20:23], s64 offen offset:1024 lds\n"
        "ds_read_b32 v8, v8\n"
        "v_lshl_add_u32 v10, v10, 2, s67\n"
        "v_mfma_f32_16x16x32_bf16 v[100:103], a[116:119], a[232:235], v[100:103]\n"
        "ds_read_b32 v9, v9\n"
        "ds_read_b32 v10, v10\n"
        "v_lshl_add_u32 v11, v11, 2, s67\n"
        "v_mfma_f32_16x16x32_bf16 v[112:115], a[120:123], a[248:251], 0\n"
        "ds_read_b128 a[248:251], v151 offset:1664\n"
        "ds_read_b32 v11, v11\n"
        "s_waitcnt lgkmcnt(15)\n"
        "v_lshl_add_u32 v24, v24, 2, s67\n"
        "v_mfma_f32_16x16x32_bf16 v[112:115], a[124:127], a[216:219], v[112:115]\n"
        "ds_read_b128 a[216:219], v150 offset:1792\n"
        "ds_read_b128 v[28:31], v235 offset:1024\n"
        "v_cvt_pk_bf16_f32 v154, v100, v101\n"
        "v_mfma_f32_16x16x32_bf16 v[112:115], a[128:131], a[220:223], v[112:115]\n"
        "ds_read_b128 a[220:223], v151 offset:1792\n"
        "ds_read_b32 v24, v24\n"
        "v_cvt_pk_bf16_f32 v155, v102, v103\n"
        "v_mfma_f32_16x16x32_bf16 v[112:115], a[132:135], a[224:227], v[112:115]\n"
        "buffer_load_dwordx4 v130, s[20:23], s64 offen offset:2048 lds\n"
        "ds_read_b128 a[224:227], v150 offset:12288\n"
        "v_lshl_add_u32 v25, v25, 2, s67\n"
        "v_mfma_f32_16x16x32_bf16 v[112:115], a[136:139], a[228:231], v[112:115]\n"
        "ds_read_b32 v25, v25\n"
        "ds_read_b128 v[44:47], v235 offset:8192\n"
        "v_lshl_add_u32 v26, v26, 2, s67\n"
        "v_mfma_f32_16x16x32_bf16 v[112:115], a[140:143], a[232:235], v[112:115]\n"
        "ds_read_b32 v26, v26\n"
        "ds_read_b128 a[228:231], v151 offset:12288\n"
        "v_lshl_add_u32 v27, v27, 2, s67\n"
        "v_mfma_f32_16x16x32_bf16 v[96:99], a[72:75], a[236:239], 0\n"
        "s_add_u32 m0, s40, 49152\n"
        "ds_read_b32 v27, v27\n"
        "ds_read_b128 a[232:235], v150 offset:12416\n"
        "v_lshl_add_u32 v40, v40, 2, s67\n"
        "v_mfma_f32_16x16x32_bf16 v[96:99], a[76:79], a[240:243], v[96:99]\n"
        "s_waitcnt lgkmcnt(15)\n"
        "s_add_u32 s64, s65, 37632\n"
        "ds_read_b32 v40, v40\n"
        "v_cvt_pk_bf16_f32 v168, v112, v113\n"
        "v_cvt_pk_bf16_f32 v169, v114, v115\n"
        "v_mfma_f32_16x16x32_bf16 v[96:99], a[80:83], a[244:247], v[96:99]\n"
        "s_waitcnt lgkmcnt(13)\n"
        "buffer_load_dwordx4 v128, s[20:23], s64 offen lds\n"
        "v_lshl_add_u32 v41, v41, 2, s67\n"
        "v_lshl_add_u32 v42, v42, 2, s67\n"
        "v_mfma_f32_16x16x32_bf16 v[96:99], a[84:87], a[248:251], v[96:99]\n"
        "s_waitcnt lgkmcnt(11)\n"
        "ds_read_b32 v41, v41\n"
        "ds_read_b32 v42, v42\n"
        "v_lshl_add_u32 v43, v43, 2, s67\n"
        "v_mfma_f32_16x16x32_bf16 v[96:99], a[88:91], a[216:219], v[96:99]\n"
        "s_waitcnt lgkmcnt(11)\n"
        "ds_read_b32 v43, v43\n"
        "ds_read_b128 v[60:63], v235 offset:9216\n"
        "v_lshl_add_u32 v56, v56, 2, s67\n"
        "v_mfma_f32_16x16x32_bf16 v[96:99], a[92:95], a[220:223], v[96:99]\n"
        "ds_read_b32 v56, v56\n"
        "v_lshl_add_u32 v57, v57, 2, s67\n"
        "v_lshl_add_u32 v58, v58, 2, s67\n"
        "v_mfma_f32_16x16x32_bf16 v[100:103], a[96:99], a[236:239], 0\n"
        "ds_read_b32 v57, v57\n"
        "ds_read_b32 v58, v58\n"
        "v_lshl_add_u32 v59, v59, 2, s67\n"
        "v_mfma_f32_16x16x32_bf16 v[100:103], a[100:103], a[240:243], v[100:103]\n"
        "buffer_load_dwordx4 v129, s[20:23], s64 offen offset:1024 lds\n"
        "ds_read_b32 v59, v59\n"
        "v_cvt_pk_bf16_f32 v172, v96, v97\n"
        "v_mfma_f32_16x16x32_bf16 v[100:103], a[104:107], a[244:247], v[100:103]\n"
        "v_cvt_pk_bf16_f32 v173, v98, v99\n"
        "v_lshl_add_u32 v12, v12, 2, s67\n"
        "v_lshl_add_u32 v13, v13, 2, s67\n"
        "v_mfma_f32_16x16x32_bf16 v[100:103], a[108:111], a[248:251], v[100:103]\n"
        "ds_read_b32 v12, v12\n"
        "ds_read_b32 v13, v13\n"
        "v_lshl_add_u32 v14, v14, 2, s67\n"
        "v_mfma_f32_16x16x32_bf16 v[100:103], a[112:115], a[216:219], v[100:103]\n"
        "ds_read_b32 v14, v14\n"
        "v_lshl_add_u32 v15, v15, 2, s67\n"
        "v_lshl_add_u32 v28, v28, 2, s67\n"
        "v_mfma_f32_16x16x32_bf16 v[100:103], a[116:119], a[220:223], v[100:103]\n"
        "ds_read_b32 v15, v15\n"
        "ds_read_b32 v28, v28\n"
        "v_lshl_add_u32 v29, v29, 2, s67\n"
        "v_mfma_f32_16x16x32_bf16 v[112:115], a[120:123], a[236:239], 0\n"
        "ds_read_b128 a[236:239], v151 offset:12416\n"
        "buffer_load_dwordx4 v130, s[20:23], s64 offen offset:2048 lds\n"
        "v_lshl_add_u32 v30, v30, 2, s67\n"
        "v_mfma_f32_16x16x32_bf16 v[112:115], a[124:127], a[240:243], v[112:115]\n"
        "ds_read_b128 a[240:243], v150 offset:12544\n"
        "ds_read_b32 v29, v29\n"
        "v_cvt_pk_bf16_f32 v174, v100, v101\n"
        "v_mfma_f32_16x16x32_bf16 v[112:115], a[128:131], a[244:247], v[112:115]\n"
        "ds_read_b128 a[244:247], v151 offset:12544\n"
        "ds_read_b32 v30, v30\n"
        "v_cvt_pk_bf16_f32 v175, v102, v103\n"
        "v_mfma_f32_16x16x32_bf16 v[112:115], a[132:135], a[248:251], v[112:115]\n"
        "s_add_u32 m0, s40, 61440\n"
        "ds_read_b128 a[248:251], v150 offset:13824\n"
        "v_lshl_add_u32 v31, v31, 2, s67\n"
        "s_waitcnt lgkmcnt(15)\n"
        "v_lshl_add_u32 v44, v44, 2, s67\n"
        "v_mfma_f32_16x16x32_bf16 v[112:115], a[136:139], a[216:219], v[112:115]\n"
        "s_add_u32 s64, s65, 74496\n"
        "ds_read_b32 v31, v31\n"
        "ds_read_b128 a[216:219], v151 offset:13824\n"
        "v_lshl_add_u32 v45, v45, 2, s67\n"
        "v_mfma_f32_16x16x32_bf16 v[112:115], a[140:143], a[220:223], v[112:115]\n"
        "buffer_load_dwordx4 v128, s[20:23], s64 offen lds\n"
        "ds_read_b128 a[220:223], v150 offset:13952\n"
        "v_lshl_add_u32 v46, v46, 2, s67\n"
        "v_mfma_f32_16x16x32_bf16 v[96:99], a[72:75], a[224:227], 0\n"
        "ds_read_b32 v44, v44\n"
        "ds_read_b32 v45, v45\n"
        "v_lshl_add_u32 v47, v47, 2, s67\n"
        "v_mfma_f32_16x16x32_bf16 v[96:99], a[76:79], a[228:231], v[96:99]\n"
        "ds_read_b32 v46, v46\n"
        "ds_read_b32 v47, v47\n"
        "v_cvt_pk_bf16_f32 v190, v112, v113\n"
        "v_mfma_f32_16x16x32_bf16 v[96:99], a[80:83], a[232:235], v[96:99]\n"
        "s_waitcnt lgkmcnt(12)\n"
        "v_cvt_pk_bf16_f32 v191, v114, v115\n"
        "v_lshl_add_u32 v60, v60, 2, s67\n"
        "v_lshl_add_u32 v61, v61, 2, s67\n"
        "v_mfma_f32_16x16x32_bf16 v[96:99], a[84:87], a[236:239], v[96:99]\n"
        "s_waitcnt lgkmcnt(11)\n"
        "ds_read_b32 v60, v60\n"
        "ds_read_b32 v61, v61\n"
        "v_lshl_add_u32 v62, v62, 2, s67\n"
        "v_mfma_f32_16x16x32_bf16 v[96:99], a[88:91], a[240:243], v[96:99]\n"
        "s_waitcnt lgkmcnt(11)\n"
        "buffer_load_dwordx4 v129, s[20:23], s64 offen offset:1024 lds\n"
        "ds_read_b32 v62, v62\n"
        "v_lshl_add_u32 v63, v63, 2, s67\n"
        "v_mfma_f32_16x16x32_bf16 v[96:99], a[92:95], a[244:247], v[96:99]\n"
        "ds_read_b32 v63, v63\n"
        "v_add_u32 v240, s31, v240\n"
        "v_add_u32 v241, s31, v241\n"
        "v_mfma_f32_16x16x32_bf16 v[100:103], a[96:99], a[224:227], 0\n"
        "v_add_u32 v245, s45, v245\n"
        "v_add_u32 v246, s45, v246\n"
        "v_add_u32 v247, s45, v247\n"
        "v_mfma_f32_16x16x32_bf16 v[100:103], a[100:103], a[228:231], v[100:103]\n"
        "v_cvt_pk_bf16_f32 v192, v96, v97\n"
        "v_cvt_pk_bf16_f32 v193, v98, v99\n"
        "v_mfma_f32_16x16x32_bf16 v[100:103], a[104:107], a[232:235], v[100:103]\n"
        "v_mfma_f32_16x16x32_bf16 v[100:103], a[108:111], a[236:239], v[100:103]\n"
        "buffer_load_dwordx4 v130, s[20:23], s64 offen offset:2048 lds\n"
        "v_mfma_f32_16x16x32_bf16 v[100:103], a[112:115], a[240:243], v[100:103]\n"
        "v_mfma_f32_16x16x32_bf16 v[100:103], a[116:119], a[244:247], v[100:103]\n"
        "v_mfma_f32_16x16x32_bf16 v[112:115], a[120:123], a[224:227], 0\n"
        "ds_read_b128 a[224:227], v151 offset:13952\n"
        "v_mfma_f32_16x16x32_bf16 v[112:115], a[124:127], a[228:231], v[112:115]\n"
        "ds_read_b128 a[228:231], v150 offset:14080\n"
        "v_mfma_f32_16x16x32_bf16 v[112:115], a[128:131], a[232:235], v[112:115]\n"
        "ds_read_b128 a[232:235], v151 offset:14080\n"
        "v_mfma_f32_16x16x32_bf16 v[112:115], a[132:135], a[236:239], v[112:115]\n"
        "v_mfma_f32_16x16x32_bf16 v[112:115], a[136:139], a[240:243], v[112:115]\n"
        "v_cvt_pk_bf16_f32 v194, v100, v101\n"
        "v_cvt_pk_bf16_f32 v195, v102, v103\n"
        "v_mfma_f32_16x16x32_bf16 v[112:115], a[140:143], a[244:247], v[112:115]\n"
        "s_waitcnt lgkmcnt(14)\n"
        "s_waitcnt vmcnt(15)\n"
        "s_barrier\n"
        "ds_read_b128 v[162:165], v251 offset:0\n"
        "ds_read_b128 v[182:185], v251 offset:512\n"
        "v_mfma_f32_16x16x32_bf16 v[96:99], a[72:75], a[248:251], 0\n"
        "s_waitcnt lgkmcnt(14)\n"
        "s_add_u32 m0, s40, 0\n"
        "v_mfma_f32_16x16x32_bf16 v[96:99], a[76:79], a[216:219], v[96:99]\n"
        "s_waitcnt lgkmcnt(13)\n"
        "s_add_u32 s64, s65, 111360\n"
        "v_mfma_f32_16x16x32_bf16 v[96:99], a[80:83], a[220:223], v[96:99]\n"
        "s_waitcnt lgkmcnt(4)\n"
        "buffer_load_dwordx4 v128, s[20:23], s64 offen lds\n"
        "ds_read_b128 v[202:205], v251 offset:4096\n"
        "v_cvt_pk_bf16_f32 v208, v112, v113\n"
        "v_mfma_f32_16x16x32_bf16 v[96:99], a[84:87], a[224:227], v[96:99]\n"
        "s_waitcnt lgkmcnt(4)\n"
        "v_cvt_pk_bf16_f32 v209, v114, v115\n"
        "v_mfma_f32_16x16x32_bf16 v[96:99], a[88:91], a[228:231], v[96:99]\n"
        "s_waitcnt lgkmcnt(3)\n"
        "v_mfma_f32_16x16x32_bf16 v[96:99], a[92:95], a[232:235], v[96:99]\n"
        "v_mfma_f32_16x16x32_bf16 v[100:103], a[96:99], a[248:251], 0\n"
        "ds_read_b128 v[222:225], v251 offset:4608\n"
        "v_mfma_f32_16x16x32_bf16 v[100:103], a[100:103], a[216:219], v[100:103]\n"
        "buffer_load_dwordx4 v129, s[20:23], s64 offen offset:1024 lds\n"
        "v_mfma_f32_16x16x32_bf16 v[100:103], a[104:107], a[220:223], v[100:103]\n"
        "s_waitcnt lgkmcnt(3)\n"
        "v_pk_min_u16 v162, v162, s26\n"
        "v_pk_min_u16 v163, v163, s26\n"
        "v_pk_min_u16 v164, v164, s26\n"
        "v_mfma_f32_16x16x32_bf16 v[100:103], a[108:111], a[224:227], v[100:103]\n"
        "v_cvt_pk_bf16_f32 v212, v96, v97\n"
        "v_cvt_pk_bf16_f32 v213, v98, v99\n"
        "v_pk_min_u16 v165, v165, s26\n"
        "v_mfma_f32_16x16x32_bf16 v[100:103], a[112:115], a[228:231], v[100:103]\n"
        "v_mfma_f32_16x16x32_bf16 v[100:103], a[116:119], a[232:235], v[100:103]\n"
        "v_mfma_f32_16x16x32_bf16 v[112:115], a[120:123], a[248:251], 0\n"
        "buffer_load_dwordx4 v130, s[20:23], s64 offen offset:2048 lds\n"
        "s_waitcnt lgkmcnt(2)\n"
        "v_pk_min_u16 v182, v182, s26\n"
        "v_pk_min_u16 v183, v183, s26\n"
        "v_mfma_f32_16x16x32_bf16 v[112:115], a[124:127], a[216:219], v[112:115]\n"
        "v_pk_min_u16 v184, v184, s26\n"
        "v_pk_min_u16 v185, v185, s26\n"
        "v_mfma_f32_16x16x32_bf16 v[112:115], a[128:131], a[220:223], v[112:115]\n"
        "v_cvt_pk_bf16_f32 v214, v100, v101\n"
        "v_cvt_pk_bf16_f32 v215, v102, v103\n"
        "v_mfma_f32_16x16x32_bf16 v[112:115], a[132:135], a[224:227], v[112:115]\n"
        "s_add_u32 m0, s40, 12288\n"
        "v_mfma_f32_16x16x32_bf16 v[112:115], a[136:139], a[228:231], v[112:115]\n"
        "s_add_u32 s64, s65, 148224\n"
        "s_waitcnt lgkmcnt(1)\n"
        "v_pk_min_u16 v202, v202, s26\n"
        "v_pk_min_u16 v203, v203, s26\n"
        "v_pk_min_u16 v204, v204, s26\n"
        "v_mfma_f32_16x16x32_bf16 v[112:115], a[140:143], a[232:235], v[112:115]\n"
        "buffer_load_dwordx4 v128, s[20:23], s64 offen lds\n"
        "v_pk_min_u16 v205, v205, s26\n"
        "v_mfma_f32_16x16x32_bf16 v[64:67], v[152:155], v[156:159], v[0:3]\n"
        "v_mfma_f32_16x16x32_bf16 v[64:67], v[168:171], v[160:163], v[64:67]\n"
        "v_mfma_f32_16x16x32_bf16 v[68:71], v[172:175], v[156:159], v[4:7]\n"
        "s_waitcnt lgkmcnt(0)\n"
        "v_pk_min_u16 v222, v222, s26\n"
        "v_pk_min_u16 v223, v223, s26\n"
        "v_pk_min_u16 v224, v224, s26\n"
        "v_mfma_f32_16x16x32_bf16 v[68:71], v[188:191], v[164:167], v[68:71]\n"
        "ds_read_b128 v[162:165], v252 offset:0\n"
        "v_cvt_pk_bf16_f32 v230, v112, v113\n"
        "v_cvt_pk_bf16_f32 v231, v114, v115\n"
        "v_mfma_f32_16x16x32_bf16 v[80:83], v[152:155], v[176:179], v[16:19]\n"
        "buffer_load_dwordx4 v129, s[20:23], s64 offen offset:1024 lds\n"
        "v_pk_min_u16 v225, v225, s26\n"
        "v_max3_f32 v232, v64, v65, v66\n"
        "v_mfma_f32_16x16x32_bf16 v[80:83], v[168:171], v[180:183], v[80:83]\n"
        "v_max3_f32 v232, v232, v67, v68\n"
        "v_max3_f32 v232, v232, v69, v70\n"
        "v_mfma_f32_16x16x32_bf16 v[84:87], v[172:175], v[176:179], v[20:23]\n"
        "v_mfma_f32_16x16x32_bf16 v[84:87], v[188:191], v[184:187], v[84:87]\n"
        "ds_read_b128 v[182:185], v252 offset:512\n"
        "v_mfma_f32_16x16x32_bf16 v[96:99], v[152:155], v[196:199], v[32:35]\n"
        "v_mfma_f32_16x16x32_bf16 v[96:99], v[168:171], v[200:203], v[96:99]\n"
        "buffer_load_dwordx4 v130, s[20:23], s64 offen offset:2048 lds\n"
        "v_max3_f32 v233, v80, v81, v82\n"
        "v_mfma_f32_16x16x32_bf16 v[100:103], v[172:175], v[196:199], v[36:39]\n"
        "v_mfma_f32_16x16x32_bf16 v[100:103], v[188:191], v[204:207], v[100:103]\n"
        "ds_read_b128 v[202:205], v252 offset:4096\n"
        "s_waitcnt vmcnt(15)\n"
        "s_barrier\n"
        "v_max3_f32 v233, v233, v83, v84\n"
        "v_max3_f32 v233, v233, v85, v86\n"
        "v_mfma_f32_16x16x32_bf16 v[112:115], v[152:155], v[216:219], v[48:51]\n"
        "s_add_u32 m0, s40, 24576\n"
        "ds_read_b128 a[236:239], v240 offset:0\n"
        "s_waitcnt lgkmcnt(3)\n"
        "v_pk_min_u16 v162, v162, s26\n"
        "v_pk_min_u16 v163, v163, s26\n"
        "v_mfma_f32_16x16x32_bf16 v[112:115], v[168:171], v[220:223], v[112:115]\n"
        "s_add_u32 s64, s65, 185088\n"
        "ds_read_b128 a[240:243], v241 offset:0\n"
        "v_pk_min_u16 v164, v164, s26\n"
        "v_pk_min_u16 v165, v165, s26\n"
        "v_mfma_f32_16x16x32_bf16 v[116:119], v[172:175], v[216:219], v[52:55]\n"
        "buffer_load_dwordx4 v128, s[20:23], s64 offen lds\n"
        "ds_read_b128 a[244:247], v240 offset:128\n"
        "v_max3_f32 v234, v96, v97, v98\n"
        "v_mfma_f32_16x16x32_bf16 v[116:119], v[188:191], v[224:227], v[116:119]\n"
        "ds_read_b128 v[222:225], v252 offset:4608\n"
        "ds_read_b128 a[248:251], v241 offset:128\n"
        "v_max3_f32 v234, v234, v99, v100\n"
        "v_mfma_f32_16x16x32_bf16 v[72:75], v[192:195], v[156:159], v[8:11]\n"
        "ds_read_b128 a[216:219], v240 offset:256\n"
        "s_waitcnt lgkmcnt(7)\n"
        "v_pk_min_u16 v182, v182, s26\n"
        "v_pk_min_u16 v183, v183, s26\n"
        "v_mfma_f32_16x16x32_bf16 v[72:75], v[208:211], v[160:163], v[72:75]\n"
        "ds_read_b128 a[220:223], v241 offset:256\n"
        "v_pk_min_u16 v184, v184, s26\n"
        "v_pk_min_u16 v185, v185, s26\n"
        "v_mfma_f32_16x16x32_bf16 v[76:79], v[212:215], v[156:159], v[12:15]\n"
        "v_max3_f32 v234, v234, v101, v102\n"
        "v_max3_f32 v235, v112, v113, v114\n"
        "v_max3_f32 v235, v235, v115, v116\n"
        "v_mfma_f32_16x16x32_bf16 v[76:79], v[228:231], v[164:167], v[76:79]\n"
        "buffer_load_dwordx4 v129, s[20:23], s64 offen offset:1024 lds\n"
        "v_max3_f32 v232, v232, v71, v72\n"
        "v_max3_f32 v232, v232, v73, v74\n"
        "v_mfma_f32_16x16x32_bf16 v[88:91], v[192:195], v[176:179], v[24:27]\n"
        "s_waitcnt lgkmcnt(7)\n"
        "v_pk_min_u16 v202, v202, s26\n"
        "v_pk_min_u16 v203, v203, s26\n"
        "v_pk_min_u16 v204, v204, s26\n"
        "v_mfma_f32_16x16x32_bf16 v[88:91], v[208:211], v[180:183], v[88:91]\n"
        "v_pk_min_u16 v205, v205, s26\n"
        "v_max3_f32 v232, v232, v75, v76\n"
        "v_max3_f32 v232, v232, v77, v78\n"
        "v_mfma_f32_16x16x32_bf16 v[92:95], v[212:215], v[176:179], v[28:31]\n"
        "v_max_f32 v232, v232, v79\n"
        "v_max3_f32 v235, v235, v117, v118\n"
        "v_xor_b32 v251, 0x2000, v251\n"
        "v_mfma_f32_16x16x32_bf16 v[92:95], v[228:231], v[184:187], v[92:95]\n"
        "v_max3_f32 v233, v233, v87, v88\n"
        "v_max3_f32 v233, v233, v89, v90\n"
        "v_xor_b32 v252, 0x2000, v252\n"
        "v_mfma_f32_16x16x32_bf16 v[104:107], v[192:195], v[196:199], v[40:43]\n"
        "buffer_load_dwordx4 v130, s[20:23], s64 offen offset:2048 lds\n"
        "s_waitcnt lgkmcnt(3)\n"
        "v_pk_min_u16 v222, v222, s26\n"
        "v_pk_min_u16 v223, v223, s26\n"
        "v_mfma_f32_16x16x32_bf16 v[104:107], v[208:211], v[200:203], v[104:107]\n"
        "v_pk_min_u16 v224, v224, s26\n"
        "v_pk_min_u16 v225, v225, s26\n"
        "v_max3_f32 v233, v233, v91, v92\n"
        "v_mfma_f32_16x16x32_bf16 v[108:111], v[212:215], v[196:199], v[44:47]\n"
        "s_waitcnt vmcnt(9)\n"
        "s_barrier\n"
        "v_max3_f32 v233, v233, v93, v94\n"
        "v_max_f32 v233, v233, v95\n"
        "v_mfma_f32_16x16x32_bf16 v[108:111], v[228:231], v[204:207], v[108:111]\n"
        "ds_read_b64_tr_b16 a[144:145], v131 offset:36864\n"
        "ds_read_b64_tr_b16 a[146:147], v131 offset:38400\n"
        "v_max3_f32 v234, v234, v103, v104\n"
        "v_mfma_f32_16x16x32_bf16 v[120:123], v[192:195], v[216:219], v[56:59]\n"
        "s_mov_b32 m0, s48\n"
        "s_xor_b32 s48, s48, 8192\n"
        "ds_read_b64_tr_b16 a[148:149], v131 offset:49152\n"
        "ds_read_b64_tr_b16 a[150:151], v131 offset:50688\n"
        "v_mfma_f32_16x16x32_bf16 v[120:123], v[208:211], v[220:223], v[120:123]\n"
        "buffer_load_dwordx4 v249, s[12:15], s36 offen offset:0 lds\n"
        "ds_read_b64_tr_b16 a[152:153], v131 offset:61440\n"
        "v_mfma_f32_16x16x32_bf16 v[124:127], v[212:215], v[216:219], v[60:63]\n"
        "ds_read_b64_tr_b16 a[154:155], v131 offset:62976\n"
        "ds_read_b64_tr_b16 a[168:169], v132 offset:36864\n"
        "v_xor_b32 v253, 32, v249\n"
        "v_mfma_f32_16x16x32_bf16 v[124:127], v[228:231], v[224:227], v[124:127]\n"
        "ds_read_b64_tr_b16 a[170:171], v132 offset:38400\n"
        "ds_read_b64_tr_b16 a[172:173], v132 offset:49152\n"
        "v_max3_f32 v234, v234, v105, v106\n"
        "v_mfma_f32_16x16x32_bf16 v[212:215], a[72:75], a[236:239], 0\n"
        "ds_read_b64_tr_b16 a[174:175], v132 offset:50688\n"
        "ds_read_b64_tr_b16 a[176:177], v132 offset:61440\n"
        "v_max3_f32 v234, v234, v107, v108\n"
        "v_mfma_f32_16x16x32_bf16 v[212:215], a[76:79], a[240:243], v[212:215]\n"
        "ds_read_b64_tr_b16 a[178:179], v132 offset:62976\n"
        "ds_read_b64_tr_b16 a[192:193], v133 offset:36864\n"
        "v_max3_f32 v234, v234, v109, v110\n"
        "v_mfma_f32_16x16x32_bf16 v[212:215], a[80:83], a[244:247], v[212:215]\n"
        "s_waitcnt lgkmcnt(15)\n"
        "buffer_load_dwordx4 v253, s[12:15], s36 offen offset:1024 lds\n"
        "s_add_u32 s36, s36, 2097152\n"
        "ds_read_b64_tr_b16 a[194:195], v133 offset:38400\n"
        "v_mfma_f32_16x16x32_bf16 v[212:215], a[84:87], a[248:251], v[212:215]\n"
        "s_waitcnt lgkmcnt(15)\n"
        "ds_read_b64_tr_b16 a[196:197], v133 offset:49152\n"
        "ds_read_b64_tr_b16 a[198:199], v133 offset:50688\n"
        "v_max_f32 v234, v234, v111\n"
        "v_mfma_f32_16x16x32_bf16 v[212:215], a[88:91], a[216:219], v[212:215]\n"
        "s_waitcnt lgkmcnt(15)\n"
        "ds_read_b64_tr_b16 a[200:201], v133 offset:61440\n"
        "ds_read_b64_tr_b16 a[202:203], v133 offset:62976\n"
        "v_max3_f32 v235, v235, v119, v120\n"
        "v_mfma_f32_16x16x32_bf16 v[212:215], a[92:95], a[220:223], v[212:215]\n"
        "v_permlane32_swap_b32 v232, v233\n"
        "v_max_f32 v232, v232, v233\n"
        "v_mfma_f32_16x16x32_bf16 v[216:219], a[96:99], a[236:239], 0\n"
        "v_mov_b32 v233, v232\n"
        "v_max3_f32 v235, v235, v121, v122\n"
        "v_max3_f32 v235, v235, v123, v124\n"
        "v_mfma_f32_16x16x32_bf16 v[216:219], a[100:103], a[240:243], v[216:219]\n"
        "v_permlane16_swap_b32 v232, v233\n"
        "v_max_f32 v232, v232, v233\n"
        "v_mfma_f32_16x16x32_bf16 v[216:219], a[104:107], a[244:247], v[216:219]\n"
        "v_mul_f32 v236, s25, v232\n"
        "v_mul_f32 v237, s25, v232\n"
        "v_max3_f32 v235, v235, v125, v126\n"
        "v_mfma_f32_16x16x32_bf16 v[216:219], a[108:111], a[248:251], v[216:219]\n"
        "v_permlane32_swap_b32 v236, v237\n"
        "v_max_f32 v235, v235, v127\n"
        "v_mfma_f32_16x16x32_bf16 v[216:219], a[112:115], a[216:219], v[216:219]\n"
        "v_cvt_pk_bf16_f32 v152, v212, v213\n"
        "v_permlane32_swap_b32 v234, v235\n"
        "v_mfma_f32_16x16x32_bf16 v[216:219], a[116:119], a[220:223], v[216:219]\n"
        "v_max_f32 v234, v234, v235\n"
        "v_mov_b32 v235, v234\n"
        "v_cvt_pk_bf16_f32 v153, v214, v215\n"
        "v_mfma_f32_16x16x32_bf16 v[162:165], a[120:123], a[236:239], 0\n"
        "v_permlane16_swap_b32 v234, v235\n"
        "v_max_f32 v234, v234, v235\n"
        "v_mfma_f32_16x16x32_bf16 v[162:165], a[124:127], a[240:243], v[162:165]\n"
        "v_mul_f32 v238, s25, v234\n"
        "v_mul_f32 v239, s25, v234\n"
        "v_cvt_pk_bf16_f32 v154, v216, v217\n"
        "v_mfma_f32_16x16x32_bf16 v[162:165], a[128:131], a[244:247], v[162:165]\n"
        "ds_read_b128 a[224:227], v240 offset:1536\n"
        "v_permlane32_swap_b32 v238, v239\n"
        "v_mfma_f32_16x16x32_bf16 v[162:165], a[132:135], a[248:251], v[162:165]\n"
        "ds_read_b128 a[228:231], v241 offset:1536\n"
        "v_cvt_pk_bf16_f32 v155, v218, v219\n"
        "v_fma_f32 v64, v64, s24, v236\n"
        "v_mfma_f32_16x16x32_bf16 v[162:165], a[136:139], a[216:219], v[162:165]\n"
        "ds_read_b128 a[232:235], v240 offset:1664\n"
        "v_exp_f32 v64, v64\n"
        "v_mfma_f32_16x16x32_bf16 v[162:165], a[140:143], a[220:223], v[162:165]\n"
        "v_fma_f32 v65, v65, s24, v236\n"
        "v_exp_f32 v65, v65\n"
        "v_mfma_f32_16x16x32_bf16 v[212:215], a[0:3], a[236:239], 0\n"
        "v_cvt_pk_bf16_f32 v64, v64, v65\n"
        "v_fma_f32 v66, v66, s24, v236\n"
        "v_fma_f32 v67, v67, s24, v236\n"
        "v_mfma_f32_16x16x32_bf16 v[212:215], a[4:7], a[240:243], v[212:215]\n"
        "v_exp_f32 v66, v66\n"
        "v_cvt_pk_bf16_f32 v168, v162, v163\n"
        "v_mfma_f32_16x16x32_bf16 v[212:215], a[8:11], a[244:247], v[212:215]\n"
        "v_exp_f32 v67, v67\n"
        "v_cvt_pk_bf16_f32 v169, v164, v165\n"
        "v_mfma_f32_16x16x32_bf16 v[212:215], a[12:15], a[248:251], v[212:215]\n"
        "v_cvt_pk_bf16_f32 v65, v66, v67\n"
        "v_fma_f32 v68, v68, s24, v236\n"
        "v_fma_f32 v69, v69, s24, v236\n"
        "v_mfma_f32_16x16x32_bf16 v[212:215], a[16:19], a[216:219], v[212:215]\n"
        "v_exp_f32 v68, v68\n"
        "v_fma_f32 v70, v70, s24, v236\n"
        "v_mfma_f32_16x16x32_bf16 v[212:215], a[20:23], a[220:223], v[212:215]\n"
        "v_exp_f32 v69, v69\n"
        "v_fma_f32 v71, v71, s24, v236\n"
        "v_mfma_f32_16x16x32_bf16 v[216:219], a[24:27], a[236:239], 0\n"
        "s_waitcnt lgkmcnt(3)\n"
        "s_waitcnt vmcnt(2)\n"
        "s_barrier\n"
        "v_exp_f32 v70, v70\n"
        "v_cvt_pk_bf16_f32 v66, v68, v69\n"
        "v_mfma_f32_16x16x32_bf16 v[216:219], a[28:31], a[240:243], v[216:219]\n"
        "ds_read_b64_tr_b16 a[156:157], v131 offset:0\n"
        "s_add_u32 m0, s65, 36864\n"
        "ds_read_b64_tr_b16 a[158:159], v131 offset:1536\n"
        "v_cvt_pk_bf16_f32 v156, v212, v213\n"
        "v_mfma_f32_16x16x32_bf16 v[216:219], a[32:35], a[244:247], v[216:219]\n"
        "s_add_u32 s64, s65, 36864\n"
        "ds_read_b64_tr_b16 a[160:161], v131 offset:12288\n"
        "ds_read_b64_tr_b16 a[162:163], v131 offset:13824\n"
        "v_cvt_pk_bf16_f32 v157, v214, v215\n"
        "v_mfma_f32_16x16x32_bf16 v[216:219], a[36:39], a[248:251], v[216:219]\n"
        "buffer_load_dwordx4 v142, s[52:55], s64 offen lds\n"
        "ds_read_b64_tr_b16 a[164:165], v131 offset:24576\n"
        "v_fma_f32 v72, v72, s24, v236\n"
        "v_mfma_f32_16x16x32_bf16 v[216:219], a[40:43], a[216:219], v[216:219]\n"
        "ds_read_b64_tr_b16 a[166:167], v131 offset:26112\n"
        "ds_read_b64_tr_b16 a[180:181], v132 offset:0\n"
        "v_fma_f32 v73, v73, s24, v236\n"
        "v_mfma_f32_16x16x32_bf16 v[216:219], a[44:47], a[220:223], v[216:219]\n"
        "ds_read_b64_tr_b16 a[182:183], v132 offset:1536\n"
        "ds_read_b64_tr_b16 a[184:185], v132 offset:12288\n"
        "v_fma_f32 v74, v74, s24, v236\n"
        "v_mfma_f32_16x16x32_bf16 v[162:165], a[48:51], a[236:239], 0\n"
        "ds_read_b128 a[236:239], v241 offset:1664\n"
        "ds_read_b64_tr_b16 a[186:187], v132 offset:13824\n"
        "v_fma_f32 v75, v75, s24, v236\n"
        "v_mfma_f32_16x16x32_bf16 v[162:165], a[52:55], a[240:243], v[162:165]\n"
        "ds_read_b128 a[240:243], v240 offset:1792\n"
        "ds_read_b64_tr_b16 a[188:189], v132 offset:24576\n"
        "v_cvt_pk_bf16_f32 v158, v216, v217\n"
        "v_mfma_f32_16x16x32_bf16 v[162:165], a[56:59], a[244:247], v[162:165]\n"
        "ds_read_b128 a[244:247], v241 offset:1792\n"
        "buffer_load_dwordx4 v142, s[52:55], s64 offen offset:1024 lds\n"
        "v_cvt_pk_bf16_f32 v159, v218, v219\n"
        "v_mfma_f32_16x16x32_bf16 v[162:165], a[60:63], a[248:251], v[162:165]\n"
        "ds_read_b64_tr_b16 a[190:191], v132 offset:26112\n"
        "ds_read_b64_tr_b16 a[204:205], v133 offset:0\n"
        "v_fma_f32 v76, v76, s24, v236\n"
        "v_mfma_f32_16x16x32_bf16 v[162:165], a[64:67], a[216:219], v[162:165]\n"
        "ds_read_b64_tr_b16 a[206:207], v133 offset:1536\n"
        "ds_read_b64_tr_b16 a[208:209], v133 offset:12288\n"
        "v_fma_f32 v77, v77, s24, v236\n"
        "v_mfma_f32_16x16x32_bf16 v[162:165], a[68:71], a[220:223], v[162:165]\n"
        "s_waitcnt lgkmcnt(15)\n"
        "ds_read_b64_tr_b16 a[210:211], v133 offset:13824\n"
        "ds_read_b64_tr_b16 a[212:213], v133 offset:24576\n"
        "v_fma_f32 v78, v78, s24, v236\n"
        "v_mfma_f32_16x16x32_bf16 v[212:215], a[72:75], a[224:227], 0\n"
        "s_waitcnt lgkmcnt(15)\n"
        "ds_read_b64_tr_b16 a[214:215], v133 offset:26112\n"
        "v_exp_f32 v71, v71\n"
        "v_mfma_f32_16x16x32_bf16 v[212:215], a[76:79], a[228:231], v[212:215]\n"
        "s_waitcnt lgkmcnt(15)\n"
        "buffer_load_dwordx4 v142, s[52:55], s64 offen offset:2048 lds\n"
        "v_exp_f32 v72, v72\n"
        "v_mfma_f32_16x16x32_bf16 v[212:215], a[80:83], a[232:235], v[212:215]\n"
        "s_waitcnt lgkmcnt(11)\n"
        "v_exp_f32 v73, v73\n"
        "v_cvt_pk_bf16_f32 v160, v162, v163\n"
        "v_mfma_f32_16x16x32_bf16 v[212:215], a[84:87], a[236:239], v[212:215]\n"
        "s_waitcnt lgkmcnt(9)\n"
        "v_exp_f32 v74, v74\n"
        "v_cvt_pk_bf16_f32 v161, v164, v165\n"
        "v_mfma_f32_16x16x32_bf16 v[212:215], a[88:91], a[240:243], v[212:215]\n"
        "s_waitcnt lgkmcnt(7)\n"
        "s_add_u32 m0, s65, 39936\n"
        "v_cvt_pk_bf16_f32 v166, v162, v163\n"
        "v_cvt_pk_bf16_f32 v167, v164, v165\n"
        "v_cvt_pk_bf16_f32 v67, v70, v71\n"
        "v_mfma_f32_16x16x32_bf16 v[212:215], a[92:95], a[244:247], v[212:215]\n"
        "s_add_u32 s64, s65, 39936\n"
        "v_exp_f32 v75, v75\n"
        "v_cvt_pk_bf16_f32 v68, v72, v73\n"
        "v_mfma_f32_16x16x32_bf16 v[216:219], a[96:99], a[224:227], 0\n"
        "buffer_load_dwordx4 v142, s[52:55], s64 offen lds\n"
        "v_exp_f32 v76, v76\n"
        "v_mfma_f32_16x16x32_bf16 v[216:219], a[100:103], a[228:231], v[216:219]\n"
        "v_exp_f32 v77, v77\n"
        "v_cvt_pk_bf16_f32 v172, v212, v213\n"
        "v_mfma_f32_16x16x32_bf16 v[216:219], a[104:107], a[232:235], v[216:219]\n"
        "v_exp_f32 v78, v78\n"
        "v_cvt_pk_bf16_f32 v173, v214, v215\n"
        "v_mfma_f32_16x16x32_bf16 v[216:219], a[108:111], a[236:239], v[216:219]\n"
        "v_cvt_pk_bf16_f32 v69, v74, v75\n"
        "v_cvt_pk_bf16_f32 v70, v76, v77\n"
        "v_fma_f32 v79, v79, s24, v236\n"
        "v_mfma_f32_16x16x32_bf16 v[216:219], a[112:115], a[240:243], v[216:219]\n"
        "s_waitcnt lgkmcnt(0)\n"
        "s_barrier\n"
        "v_exp_f32 v79, v79\n"
        "v_fma_f32 v80, v80, s24, v237\n"
        "v_mfma_f32_16x16x32_bf16 v[216:219], a[116:119], a[244:247], v[216:219]\n"
        "buffer_load_dwordx4 v142, s[52:55], s64 offen offset:1024 lds\n"
        "v_exp_f32 v80, v80\n"
        "v_mfma_f32_16x16x32_bf16 v[162:165], a[120:123], a[224:227], 0\n"
        "v_cvt_pk_bf16_f32 v71, v78, v79\n"
        "v_fma_f32 v81, v81, s24, v237\n"
        "v_fma_f32 v82, v82, s24, v237\n"
        "v_mfma_f32_16x16x32_bf16 v[162:165], a[124:127], a[228:231], v[162:165]\n"
        "v_exp_f32 v81, v81\n"
        "v_cvt_pk_bf16_f32 v174, v216, v217\n"
        "v_mfma_f32_16x16x32_bf16 v[162:165], a[128:131], a[232:235], v[162:165]\n"
        "ds_read_b128 a[248:251], v240 offset:12288\n"
        "v_exp_f32 v82, v82\n"
        "v_mfma_f32_16x16x32_bf16 v[162:165], a[132:135], a[236:239], v[162:165]\n"
        "ds_read_b128 a[216:219], v241 offset:12288\n"
        "v_cvt_pk_bf16_f32 v175, v218, v219\n"
        "v_cvt_pk_bf16_f32 v80, v80, v81\n"
        "v_mfma_f32_16x16x32_bf16 v[162:165], a[136:139], a[240:243], v[162:165]\n"
        "buffer_load_dwordx4 v142, s[52:55], s64 offen offset:2048 lds\n"
        "ds_read_b128 a[220:223], v240 offset:12416\n"
        "v_fma_f32 v83, v83, s24, v237\n"
        "v_mfma_f32_16x16x32_bf16 v[162:165], a[140:143], a[244:247], v[162:165]\n"
        "v_exp_f32 v83, v83\n"
        "v_fma_f32 v84, v84, s24, v237\n"
        "v_mfma_f32_16x16x32_bf16 v[212:215], a[0:3], a[224:227], 0\n"
        "v_exp_f32 v84, v84\n"
        "v_cvt_pk_bf16_f32 v81, v82, v83\n"
        "v_mfma_f32_16x16x32_bf16 v[212:215], a[4:7], a[228:231], v[212:215]\n"
        "s_add_u32 m0, s65, 43008\n"
        "v_fma_f32 v85, v85, s24, v237\n"
        "v_exp_f32 v85, v85\n"
        "v_mfma_f32_16x16x32_bf16 v[212:215], a[8:11], a[232:235], v[212:215]\n"
        "s_add_u32 s64, s65, 43008\n"
        "v_cvt_pk_bf16_f32 v190, v162, v163\n"
        "v_cvt_pk_bf16_f32 v191, v164, v165\n"
        "v_cvt_pk_bf16_f32 v82, v84, v85\n"
        "v_mfma_f32_16x16x32_bf16 v[212:215], a[12:15], a[236:239], v[212:215]\n"
        "buffer_load_dwordx4 v142, s[52:55], s64 offen lds\n"
        "v_fma_f32 v86, v86, s24, v237\n"
        "v_fma_f32 v87, v87, s24, v237\n"
        "v_mfma_f32_16x16x32_bf16 v[212:215], a[16:19], a[240:243], v[212:215]\n"
        "v_exp_f32 v86, v86\n"
        "v_fma_f32 v88, v88, s24, v237\n"
        "v_mfma_f32_16x16x32_bf16 v[212:215], a[20:23], a[244:247], v[212:215]\n"
        "v_exp_f32 v87, v87\n"
        "v_fma_f32 v89, v89, s24, v237\n"
        "v_mfma_f32_16x16x32_bf16 v[216:219], a[24:27], a[224:227], 0\n"
        "v_exp_f32 v88, v88\n"
        "v_cvt_pk_bf16_f32 v83, v86, v87\n"
        "v_mfma_f32_16x16x32_bf16 v[216:219], a[28:31], a[228:231], v[216:219]\n"
        "v_exp_f32 v89, v89\n"
        "v_fma_f32 v90, v90, s24, v237\n"
        "v_mfma_f32_16x16x32_bf16 v[216:219], a[32:35], a[232:235], v[216:219]\n"
        "buffer_load_dwordx4 v142, s[52:55], s64 offen offset:1024 lds\n"
        "v_exp_f32 v90, v90\n"
        "v_mfma_f32_16x16x32_bf16 v[216:219], a[36:39], a[236:239], v[216:219]\n"
        "v_cvt_pk_bf16_f32 v176, v212, v213\n"
        "v_cvt_pk_bf16_f32 v177, v214, v215\n"
        "v_cvt_pk_bf16_f32 v84, v88, v89\n"
        "v_mfma_f32_16x16x32_bf16 v[216:219], a[40:43], a[240:243], v[216:219]\n"
        "v_fma_f32 v91, v91, s24, v237\n"
        "v_exp_f32 v91, v91\n"
        "v_mfma_f32_16x16x32_bf16 v[216:219], a[44:47], a[244:247], v[216:219]\n"
        "v_cvt_pk_bf16_f32 v85, v90, v91\n"
        "v_fma_f32 v92, v92, s24, v237\n"
        "v_fma_f32 v93, v93, s24, v237\n"
        "v_mfma_f32_16x16x32_bf16 v[162:165], a[48:51], a[224:227], 0\n"
        "ds_read_b128 a[224:227], v241 offset:12416\n"
        "v_exp_f32 v92, v92\n"
        "v_mfma_f32_16x16x32_bf16 v[162:165], a[52:55], a[228:231], v[162:165]\n"
        "ds_read_b128 a[228:231], v240 offset:12544\n"
        "buffer_load_dwordx4 v142, s[52:55], s64 offen offset:2048 lds\n"
        "v_cvt_pk_bf16_f32 v178, v216, v217\n"
        "v_mfma_f32_16x16x32_bf16 v[162:165], a[56:59], a[232:235], v[162:165]\n"
        "ds_read_b128 a[232:235], v241 offset:12544\n"
        "v_exp_f32 v93, v93\n"
        "v_mfma_f32_16x16x32_bf16 v[162:165], a[60:63], a[236:239], v[162:165]\n"
        "v_cvt_pk_bf16_f32 v179, v218, v219\n"
        "v_cvt_pk_bf16_f32 v86, v92, v93\n"
        "v_fma_f32 v94, v94, s24, v237\n"
        "v_mfma_f32_16x16x32_bf16 v[162:165], a[64:67], a[240:243], v[162:165]\n"
        "s_add_u32 m0, s65, 0\n"
        "v_exp_f32 v94, v94\n"
        "v_fma_f32 v95, v95, s24, v237\n"
        "v_mfma_f32_16x16x32_bf16 v[162:165], a[68:71], a[244:247], v[162:165]\n"
        "s_waitcnt lgkmcnt(5)\n"
        "s_add_u32 s64, s65, 0\n"
        "v_exp_f32 v95, v95\n"
        "v_fma_f32 v96, v96, s24, v238\n"
        "v_mfma_f32_16x16x32_bf16 v[72:75], a[72:75], a[248:251], 0\n"
        "s_waitcnt lgkmcnt(4)\n"
        "buffer_load_dwordx4 v142, s[52:55], s64 offen lds\n"
        "v_exp_f32 v96, v96\n"
        "v_mfma_f32_16x16x32_bf16 v[72:75], a[76:79], a[216:219], v[72:75]\n"
        "s_waitcnt lgkmcnt(3)\n"
        "v_cvt_pk_bf16_f32 v87, v94, v95\n"
        "v_cvt_pk_bf16_f32 v180, v162, v163\n"
        "v_cvt_pk_bf16_f32 v181, v164, v165\n"
        "v_mfma_f32_16x16x32_bf16 v[72:75], a[80:83], a[220:223], v[72:75]\n"
        "s_waitcnt lgkmcnt(2)\n"
        "v_cvt_pk_bf16_f32 v186, v162, v163\n"
        "v_cvt_pk_bf16_f32 v187, v164, v165\n"
        "v_fma_f32 v97, v97, s24, v238\n"
        "v_mfma_f32_16x16x32_bf16 v[72:75], a[84:87], a[224:227], v[72:75]\n"
        "s_waitcnt lgkmcnt(1)\n"
        "v_exp_f32 v97, v97\n"
        "v_fma_f32 v98, v98, s24, v238\n"
        "v_mfma_f32_16x16x32_bf16 v[72:75], a[88:91], a[228:231], v[72:75]\n"
        "s_waitcnt lgkmcnt(0)\n"
        "v_exp_f32 v98, v98\n"
        "v_accvgpr_read_b32 v232, a252\n"
        "v_mfma_f32_16x16x32_bf16 v[72:75], a[92:95], a[232:235], v[72:75]\n"
        "buffer_load_dwordx4 v142, s[52:55], s64 offen offset:1024 lds\n"
        "v_accvgpr_read_b32 v233, a253\n"
        "v_accvgpr_read_b32 v234, a254\n"
        "v_mfma_f32_16x16x32_bf16 v[76:79], a[96:99], a[248:251], 0\n"
        "v_mov_b32 v235, 0x3f803f80\n"
        "v_accvgpr_write_b32 a252, v235\n"
        "v_accvgpr_write_b32 a253, v235\n"
        "v_mfma_f32_16x16x32_bf16 v[76:79], a[100:103], a[216:219], v[76:79]\n"
        "v_cvt_pk_bf16_f32 v192, v72, v73\n"
        "v_cvt_pk_bf16_f32 v193, v74, v75\n"
        "v_accvgpr_write_b32 a254, v235\n"
        "v_mfma_f32_16x16x32_bf16 v[76:79], a[104:107], a[220:223], v[76:79]\n"
        "v_accvgpr_write_b32 a255, v235\n"
        "v_cvt_pk_bf16_f32 v96, v96, v97\n"
        "v_fma_f32 v99, v99, s24, v238\n"
        "v_mfma_f32_16x16x32_bf16 v[76:79], a[108:111], a[224:227], v[76:79]\n"
        "v_exp_f32 v99, v99\n"
        "v_fma_f32 v100, v100, s24, v238\n"
        "v_mfma_f32_16x16x32_bf16 v[76:79], a[112:115], a[228:231], v[76:79]\n"
        "buffer_load_dwordx4 v142, s[52:55], s64 offen offset:2048 lds\n"
        "v_exp_f32 v100, v100\n"
        "v_mfma_f32_16x16x32_bf16 v[76:79], a[116:119], a[232:235], v[76:79]\n"
        "v_cvt_pk_bf16_f32 v97, v98, v99\n"
        "v_fma_f32 v101, v101, s24, v238\n"
        "v_fma_f32 v102, v102, s24, v238\n"
        "v_mfma_f32_16x16x32_bf16 v[162:165], a[120:123], a[248:251], 0\n"
        "ds_read_b128 a[236:239], v150 offset:0\n"
        "v_exp_f32 v101, v101\n"
        "v_mfma_f32_16x16x32_bf16 v[162:165], a[124:127], a[216:219], v[162:165]\n"
        "s_add_u32 m0, s65, 3072\n"
        "ds_read_b128 a[240:243], v151 offset:0\n"
        "v_exp_f32 v102, v102\n"
        "v_mfma_f32_16x16x32_bf16 v[162:165], a[128:131], a[220:223], v[162:165]\n"
        "s_add_u32 s64, s65, 3072\n"
        "s_waitcnt vmcnt(3)\n"
        "s_barrier\n"
        "ds_read_b64_tr_b16 v[212:213], v232 offset:36864\n"
        "ds_read_b128 a[244:247], v150 offset:128\n"
        "v_mfma_f32_16x16x32_bf16 v[162:165], a[132:135], a[224:227], v[162:165]\n"
        "buffer_load_dwordx4 v142, s[52:55], s64 offen lds\n"
        "v_cvt_pk_bf16_f32 v194, v76, v77\n"
        "v_cvt_pk_bf16_f32 v195, v78, v79\n"
        "v_mfma_f32_16x16x32_bf16 v[162:165], a[136:139], a[228:231], v[162:165]\n"
        "ds_read_b64_tr_b16 v[214:215], v232 offset:43008\n"
        "v_cvt_pk_bf16_f32 v98, v100, v101\n"
        "v_fma_f32 v103, v103, s24, v238\n"
        "v_mfma_f32_16x16x32_bf16 v[162:165], a[140:143], a[232:235], v[162:165]\n"
        "v_exp_f32 v103, v103\n"
        "v_fma_f32 v104, v104, s24, v238\n"
        "v_mfma_f32_16x16x32_bf16 v[72:75], a[0:3], a[248:251], 0\n"
        "ds_read_b64_tr_b16 v[216:217], v232 offset:55296\n"
        "v_exp_f32 v104, v104\n"
        "v_mfma_f32_16x16x32_bf16 v[72:75], a[4:7], a[216:219], v[72:75]\n"
        "v_cvt_pk_bf16_f32 v99, v102, v103\n"
        "v_fma_f32 v105, v105, s24, v238\n"
        "v_cvt_pk_bf16_f32 v208, v162, v163\n"
        "v_mfma_f32_16x16x32_bf16 v[72:75], a[8:11], a[220:223], v[72:75]\n"
        "buffer_load_dwordx4 v142, s[52:55], s64 offen offset:1024 lds\n"
        "ds_read_b64_tr_b16 v[218:219], v232 offset:61440\n"
        "v_cvt_pk_bf16_f32 v209, v164, v165\n"
        "v_mfma_f32_16x16x32_bf16 v[72:75], a[12:15], a[224:227], v[72:75]\n"
        "v_exp_f32 v105, v105\n"
        "v_fma_f32 v106, v106, s24, v238\n"
        "v_mfma_f32_16x16x32_bf16 v[72:75], a[16:19], a[228:231], v[72:75]\n"
        "ds_read_b64_tr_b16 v[220:221], v233 offset:49152\n"
        "v_exp_f32 v106, v106\n"
        "v_mfma_f32_16x16x32_bf16 v[72:75], a[20:23], a[232:235], v[72:75]\n"
        "v_cvt_pk_bf16_f32 v100, v104, v105\n"
        "v_fma_f32 v107, v107, s24, v238\n"
        "v_fma_f32 v108, v108, s24, v238\n"
        "v_mfma_f32_16x16x32_bf16 v[76:79], a[24:27], a[248:251], 0\n"
        "ds_read_b64_tr_b16 v[222:223], v233 offset:50688\n"
        "v_exp_f32 v107, v107\n"
        "v_mfma_f32_16x16x32_bf16 v[76:79], a[28:31], a[216:219], v[76:79]\n"
        "buffer_load_dwordx4 v142, s[52:55], s64 offen offset:2048 lds\n"
        "v_exp_f32 v108, v108\n"
        "v_mfma_f32_16x16x32_bf16 v[76:79], a[32:35], a[220:223], v[76:79]\n"
        "ds_read_b64_tr_b16 v[224:225], v232 offset:36896\n"
        "v_cvt_pk_bf16_f32 v196, v72, v73\n"
        "v_cvt_pk_bf16_f32 v197, v74, v75\n"
        "v_mfma_f32_16x16x32_bf16 v[76:79], a[36:39], a[224:227], v[76:79]\n"
        "v_cvt_pk_bf16_f32 v101, v106, v107\n"
        "v_fma_f32 v109, v109, s24, v238\n"
        "v_fma_f32 v110, v110, s24, v238\n"
        "v_mfma_f32_16x16x32_bf16 v[76:79], a[40:43], a[228:231], v[76:79]\n"
        "s_add_u32 m0, s65, 6144\n"
        "ds_read_b64_tr_b16 v[226:227], v232 offset:43040\n"
        "v_exp_f32 v109, v109\n"
        "v_mfma_f32_16x16x32_bf16 v[76:79], a[44:47], a[232:235], v[76:79]\n"
        "s_add_u32 s64, s65, 6144\n"
        "v_exp_f32 v110, v110\n"
        "v_cvt_pk_bf16_f32 v102, v108, v109\n"
        "v_mfma_f32_16x16x32_bf16 v[162:165], a[48:51], a[248:251], 0\n"
        "ds_read_b128 a[248:251], v151 offset:128\n"
        "buffer_load_dwordx4 v142, s[52:55], s64 offen lds\n"
        "v_fma_f32 v111, v111, s24, v238\n"
        "v_mfma_f32_16x16x32_bf16 v[162:165], a[52:55], a[216:219], v[162:165]\n"
        "ds_read_b128 a[216:219], v150 offset:256\n"
        "ds_read_b64_tr_b16 v[182:183], v232 offset:55328\n"
        "v_cvt_pk_bf16_f32 v198, v76, v77\n"
        "v_mfma_f32_16x16x32_bf16 v[162:165], a[56:59], a[220:223], v[162:165]\n"
        "ds_read_b128 a[220:223], v151 offset:256\n"
        "ds_read_b64_tr_b16 v[184:185], v232 offset:61472\n"
        "s_sub_u32 s31, s30, s28\n"
        "v_cvt_pk_bf16_f32 v199, v78, v79\n"
        "v_mfma_f32_16x16x32_bf16 v[162:165], a[60:63], a[224:227], v[162:165]\n"
        "s_sub_u32 s45, s28, s29\n"
        "v_exp_f32 v111, v111\n"
        "v_fma_f32 v112, v112, s24, v239\n"
        "v_mfma_f32_16x16x32_bf16 v[162:165], a[64:67], a[228:231], v[162:165]\n"
        "ds_read_b64_tr_b16 v[202:203], v233 offset:49184\n"
        "ds_read_b128 a[228:231], v150 offset:1536\n"
        "s_add_u32 s28, s28, s31\n"
        "v_cvt_pk_bf16_f32 v103, v110, v111\n"
        "v_mfma_f32_16x16x32_bf16 v[162:165], a[68:71], a[232:235], v[162:165]\n"
        "s_waitcnt lgkmcnt(15)\n"
        "buffer_load_dwordx4 v142, s[52:55], s64 offen offset:1024 lds\n"
        "ds_read_b128 a[232:235], v151 offset:1536\n"
        "v_fma_f32 v113, v113, s24, v239\n"
        "v_mfma_f32_16x16x32_bf16 v[72:75], a[236:239], a[144:147], 0\n"
        "s_waitcnt lgkmcnt(15)\n"
        "ds_read_b64_tr_b16 v[204:205], v233 offset:50720\n"
        "v_exp_f32 v112, v112\n"
        "v_mfma_f32_16x16x32_bf16 v[72:75], a[240:243], a[148:151], v[72:75]\n"
        "s_waitcnt lgkmcnt(15)\n"
        "v_exp_f32 v113, v113\n"
        "v_cvt_pk_bf16_f32 v200, v162, v163\n"
        "v_mfma_f32_16x16x32_bf16 v[72:75], a[244:247], a[152:155], v[72:75]\n"
        "s_waitcnt lgkmcnt(8)\n"
        "ds_read_b64_tr_b16 v[88:89], v232 offset:36928\n"
        "v_cvt_pk_bf16_f32 v201, v164, v165\n"
        "v_cvt_pk_bf16_f32 v206, v162, v163\n"
        "v_mfma_f32_16x16x32_bf16 v[72:75], a[248:251], a[156:159], v[72:75]\n"
        "s_waitcnt lgkmcnt(8)\n"
        "v_cvt_pk_bf16_f32 v207, v164, v165\n"
        "v_cvt_pk_bf16_f32 v112, v112, v113\n"
        "v_fma_f32 v114, v114, s24, v239\n"
        "v_mfma_f32_16x16x32_bf16 v[72:75], a[216:219], a[160:163], v[72:75]\n"
        "s_waitcnt lgkmcnt(6)\n"
        "buffer_load_dwordx4 v142, s[52:55], s64 offen offset:2048 lds\n"
        "s_add_u32 s41, s30, s40\n"
        "s_add_u32 s35, s47, 786432\n"
        "s_sub_u32 s46, s35, 100663296\n"
        "s_cmp_lt_u32 s46, 786432\n"
        "v_mfma_f32_16x16x32_bf16 v[72:75], a[220:223], a[164:167], v[72:75]\n"
        "s_cselect_b32 s35, s46, s35\n"
        "ds_read_b64_tr_b16 v[90:91], v232 offset:43072\n"
        "v_exp_f32 v114, v114\n"
        "v_mfma_f32_16x16x32_bf16 v[76:79], a[236:239], a[168:171], 0\n"
        "ds_read_b64_tr_b16 v[92:93], v232 offset:55360\n"
        "v_fma_f32 v115, v115, s24, v239\n"
        "v_fma_f32 v116, v116, s24, v239\n"
        "v_mfma_f32_16x16x32_bf16 v[76:79], a[240:243], a[172:175], v[76:79]\n"
        "v_exp_f32 v115, v115\n"
        "v_cvt_pk_bf16_f32 v128, v72, v73\n"
        "v_mfma_f32_16x16x32_bf16 v[76:79], a[244:247], a[176:179], v[76:79]\n"
        "ds_read_b64_tr_b16 v[94:95], v232 offset:61504\n"
        "v_exp_f32 v116, v116\n"
        "v_mfma_f32_16x16x32_bf16 v[76:79], a[248:251], a[180:183], v[76:79]\n"
        "s_mov_b32 m0, s48\n"
        "v_cvt_pk_bf16_f32 v129, v74, v75\n"
        "v_cvt_pk_bf16_f32 v113, v114, v115\n"
        "v_fma_f32 v117, v117, s24, v239\n"
        "v_mfma_f32_16x16x32_bf16 v[76:79], a[216:219], a[184:187], v[76:79]\n"
        "buffer_load_dwordx4 v249, s[12:15], s36 offen offset:0 lds\n"
        "ds_read_b64_tr_b16 v[104:105], v233 offset:49216\n"
        "v_mfma_f32_16x16x32_bf16 v[76:79], a[220:223], a[188:191], v[76:79]\n"
        "v_exp_f32 v117, v117\n"
        "v_fma_f32 v118, v118, s24, v239\n"
        "v_mfma_f32_16x16x32_bf16 v[162:165], a[236:239], a[192:195], 0\n"
        "ds_read_b128 a[236:239], v150 offset:1664\n"
        "ds_read_b64_tr_b16 v[106:107], v233 offset:50752\n"
        "v_cvt_pk_bf16_f32 v114, v116, v117\n"
        "v_mfma_f32_16x16x32_bf16 v[162:165], a[240:243], a[196:199], v[162:165]\n"
        "ds_read_b128 a[240:243], v151 offset:1664\n"
        "ds_read_b128 a[224:227], v151 offset:12288\n"
        "v_xor_b32 v253, 32, v249\n"
        "v_mfma_f32_16x16x32_bf16 v[162:165], a[244:247], a[200:203], v[162:165]\n"
        "ds_read_b128 a[244:247], v150 offset:1792\n"
        "v_exp_f32 v118, v118\n"
        "v_mfma_f32_16x16x32_bf16 v[162:165], a[248:251], a[204:207], v[162:165]\n"
        "ds_read_b128 a[248:251], v151 offset:1792\n"
        "buffer_load_dwordx4 v253, s[12:15], s36 offen offset:1024 lds\n"
        "s_mov_b32 s30, s29\n"
        "v_mfma_f32_16x16x32_bf16 v[162:165], a[216:219], a[208:211], v[162:165]\n"
        "s_waitcnt lgkmcnt(4)\n"
        "s_barrier\n"
        "ds_write_b128 v234, v[212:215] offset:36864\n"
        "v_mfma_f32_16x16x32_bf16 v[162:165], a[220:223], a[212:215], v[162:165]\n"
        "s_add_u32 s29, s29, s45\n"
        "ds_read_b128 a[220:223], v150 offset:12288\n"
        "v_cvt_pk_bf16_f32 v136, v76, v77\n"
        "v_mfma_f32_16x16x32_bf16 v[72:75], a[228:231], a[144:147], 0\n"
        "v_cvt_pk_bf16_f32 v137, v78, v79\n"
        "v_fma_f32 v119, v119, s24, v239\n"
        "v_mfma_f32_16x16x32_bf16 v[72:75], a[232:235], a[148:151], v[72:75]\n"
        "s_add_u32 m0, s41, 0\n"
        "v_exp_f32 v119, v119\n"
        "v_mfma_f32_16x16x32_bf16 v[72:75], a[236:239], a[152:155], v[72:75]\n"
        "s_waitcnt lgkmcnt(5)\n"
        "buffer_load_dwordx4 v242, s[8:11], s47 offen lds\n"
        "v_mfma_f32_16x16x32_bf16 v[72:75], a[240:243], a[156:159], v[72:75]\n"
        "s_waitcnt lgkmcnt(3)\n"
        "ds_write_b128 v234, v[216:219] offset:43008\n"
        "v_mfma_f32_16x16x32_bf16 v[72:75], a[244:247], a[160:163], v[72:75]\n"
        "s_waitcnt lgkmcnt(3)\n"
        "v_cvt_pk_bf16_f32 v144, v162, v163\n"
        "v_cvt_pk_bf16_f32 v145, v164, v165\n"
        "v_mfma_f32_16x16x32_bf16 v[72:75], a[248:251], a[164:167], v[72:75]\n"
        "v_cvt_pk_bf16_f32 v115, v118, v119\n"
        "v_fma_f32 v120, v120, s24, v239\n"
        "v_mfma_f32_16x16x32_bf16 v[76:79], a[228:231], a[168:171], 0\n"
        "ds_write_b128 v234, v[220:223] offset:49152\n"
        "v_mfma_f32_16x16x32_bf16 v[76:79], a[232:235], a[172:175], v[76:79]\n"
        "v_exp_f32 v120, v120\n"
        "v_mfma_f32_16x16x32_bf16 v[76:79], a[236:239], a[176:179], v[76:79]\n"
        "v_fma_f32 v121, v121, s24, v239\n"
        "v_cvt_pk_bf16_f32 v130, v72, v73\n"
        "v_mfma_f32_16x16x32_bf16 v[76:79], a[240:243], a[180:183], v[76:79]\n"
        "ds_write_b128 v234, v[224:227] offset:37120\n"
        "v_mfma_f32_16x16x32_bf16 v[76:79], a[244:247], a[184:187], v[76:79]\n"
        "v_cvt_pk_bf16_f32 v131, v74, v75\n"
        "v_fma_f32 v122, v122, s24, v239\n"
        "v_mfma_f32_16x16x32_bf16 v[76:79], a[248:251], a[188:191], v[76:79]\n"
        "v_exp_f32 v121, v121\n"
        "v_mfma_f32_16x16x32_bf16 v[162:165], a[228:231], a[192:195], 0\n"
        "ds_read_b128 a[228:231], v150 offset:12416\n"
        "v_cvt_pk_bf16_f32 v116, v120, v121\n"
        "v_mfma_f32_16x16x32_bf16 v[162:165], a[232:235], a[196:199], v[162:165]\n"
        "ds_read_b128 a[232:235], v151 offset:12416\n"
        "ds_read_b128 a[216:219], v151 offset:13824\n"
        "v_mfma_f32_16x16x32_bf16 v[162:165], a[236:239], a[200:203], v[162:165]\n"
        "ds_write_b128 v234, v[182:185] offset:43264\n"
        "v_mfma_f32_16x16x32_bf16 v[162:165], a[240:243], a[204:207], v[162:165]\n"
        "ds_read_b128 a[236:239], v150 offset:12544\n"
        "ds_read_b128 a[240:243], v151 offset:12544\n"
        "v_mfma_f32_16x16x32_bf16 v[162:165], a[244:247], a[208:211], v[162:165]\n"
        "v_cvt_pk_bf16_f32 v138, v76, v77\n"
        "v_cvt_pk_bf16_f32 v139, v78, v79\n"
        "v_mfma_f32_16x16x32_bf16 v[162:165], a[248:251], a[212:215], v[162:165]\n"
        "s_waitcnt lgkmcnt(9)\n"
        "ds_write_b128 v234, v[202:205] offset:49408\n"
        "v_mfma_f32_16x16x32_bf16 v[72:75], a[220:223], a[144:147], 0\n"
        "ds_read_b128 a[248:251], v150 offset:13824\n"
        "v_fma_f32 v123, v123, s24, v239\n"
        "v_mfma_f32_16x16x32_bf16 v[72:75], a[224:227], a[148:151], v[72:75]\n"
        "s_waitcnt lgkmcnt(7)\n"
        "v_exp_f32 v122, v122\n"
        "v_mfma_f32_16x16x32_bf16 v[72:75], a[228:231], a[152:155], v[72:75]\n"
        "s_waitcnt lgkmcnt(6)\n"
        "ds_write_b128 v234, v[88:91] offset:37376\n"
        "v_mfma_f32_16x16x32_bf16 v[72:75], a[232:235], a[156:159], v[72:75]\n"
        "s_waitcnt lgkmcnt(4)\n"
        "v_exp_f32 v123, v123\n"
        "v_mfma_f32_16x16x32_bf16 v[72:75], a[236:239], a[160:163], v[72:75]\n"
        "s_waitcnt lgkmcnt(3)\n"
        "v_cvt_pk_bf16_f32 v146, v162, v163\n"
        "v_cvt_pk_bf16_f32 v147, v164, v165\n"
        "v_mfma_f32_16x16x32_bf16 v[72:75], a[240:243], a[164:167], v[72:75]\n"
        "ds_write_b128 v234, v[92:95] offset:43520\n"
        "v_mfma_f32_16x16x32_bf16 v[76:79], a[220:223], a[168:171], 0\n"
        "v_cvt_pk_bf16_f32 v117, v122, v123\n"
        "v_fma_f32 v124, v124, s24, v239\n"
        "v_mfma_f32_16x16x32_bf16 v[76:79], a[224:227], a[172:175], v[76:79]\n"
        "v_fma_f32 v125, v125, s24, v239\n"
        "v_fma_f32 v126, v126, s24, v239\n"
        "v_mfma_f32_16x16x32_bf16 v[76:79], a[228:231], a[176:179], v[76:79]\n"
        "ds_write_b128 v234, v[104:107] offset:49664\n"
        "s_waitcnt vmcnt(3)\n"
        "s_barrier\n"
        "v_mfma_f32_16x16x32_bf16 v[76:79], a[232:235], a[180:183], v[76:79]\n"
        "ds_read_b64_tr_b16 v[108:109], v232 offset:0\n"
        "v_fma_f32 v127, v127, s24, v239\n"
        "v_mfma_f32_16x16x32_bf16 v[76:79], a[236:239], a[184:187], v[76:79]\n"
        "ds_read_b64_tr_b16 v[110:111], v232 offset:6144\n"
        "v_cvt_pk_bf16_f32 v132, v72, v73\n"
        "v_mfma_f32_16x16x32_bf16 v[76:79], a[240:243], a[188:191], v[76:79]\n"
        "v_exp_f32 v124, v124\n"
        "v_mfma_f32_16x16x32_bf16 v[162:165], a[220:223], a[192:195], 0\n"
        "ds_read_b128 a[220:223], v150 offset:13952\n"
        "ds_read_b64_tr_b16 v[120:121], v232 offset:18432\n"
        "v_mfma_f32_16x16x32_bf16 v[162:165], a[224:227], a[196:199], v[162:165]\n"
        "ds_read_b128 a[224:227], v151 offset:13952\n"
        "v_cvt_pk_bf16_f32 v133, v74, v75\n"
        "v_mfma_f32_16x16x32_bf16 v[162:165], a[228:231], a[200:203], v[162:165]\n"
        "ds_read_b128 a[228:231], v150 offset:14080\n"
        "ds_read_b64_tr_b16 v[122:123], v232 offset:24576\n"
        "v_mfma_f32_16x16x32_bf16 v[162:165], a[232:235], a[204:207], v[162:165]\n"
        "ds_read_b128 a[232:235], v151 offset:14080\n"
        "v_cvt_pk_bf16_f32 v140, v76, v77\n"
        "v_mfma_f32_16x16x32_bf16 v[162:165], a[236:239], a[208:211], v[162:165]\n"
        "ds_read_b128 a[236:239], v240 offset:13824\n"
        "ds_read_b128 a[244:247], v240 offset:13952\n"
        "v_mfma_f32_16x16x32_bf16 v[162:165], a[240:243], a[212:215], v[162:165]\n"
        "s_waitcnt lgkmcnt(13)\n"
        "ds_read_b128 a[240:243], v241 offset:13824\n"
        "v_cvt_pk_bf16_f32 v141, v78, v79\n"
        "v_mfma_f32_16x16x32_bf16 v[72:75], a[248:251], a[144:147], 0\n"
        "v_exp_f32 v125, v125\n"
        "v_mfma_f32_16x16x32_bf16 v[72:75], a[216:219], a[148:151], v[72:75]\n"
        "s_waitcnt lgkmcnt(8)\n"
        "v_cvt_pk_bf16_f32 v118, v124, v125\n"
        "v_mfma_f32_16x16x32_bf16 v[72:75], a[220:223], a[152:155], v[72:75]\n"
        "s_waitcnt lgkmcnt(6)\n"
        "ds_read_b64_tr_b16 v[124:125], v233 offset:12288\n"
        "ds_read_b64_tr_b16 v[222:223], v232 offset:32\n"
        "v_mfma_f32_16x16x32_bf16 v[72:75], a[224:227], a[156:159], v[72:75]\n"
        "s_waitcnt lgkmcnt(7)\n"
        "v_exp_f32 v126, v126\n"
        "v_mfma_f32_16x16x32_bf16 v[72:75], a[228:231], a[160:163], v[72:75]\n"
        "s_waitcnt lgkmcnt(5)\n"
        "ds_read_b64_tr_b16 v[224:225], v232 offset:6176\n"
        "v_cvt_pk_bf16_f32 v148, v162, v163\n"
        "v_mfma_f32_16x16x32_bf16 v[72:75], a[232:235], a[164:167], v[72:75]\n"
        "v_exp_f32 v127, v127\n"
        "v_mfma_f32_16x16x32_bf16 v[76:79], a[248:251], a[168:171], 0\n"
        "ds_read_b64_tr_b16 v[182:183], v232 offset:18464\n"
        "v_cvt_pk_bf16_f32 v119, v126, v127\n"
        "v_mfma_f32_16x16x32_bf16 v[76:79], a[216:219], a[172:175], v[76:79]\n"
        "ds_read_b64_tr_b16 v[126:127], v233 offset:13824\n"
        "v_cvt_pk_bf16_f32 v149, v164, v165\n"
        "v_mfma_f32_16x16x32_bf16 v[76:79], a[220:223], a[176:179], v[76:79]\n"
        "ds_read_b64_tr_b16 v[184:185], v232 offset:24608\n"
        "v_cvt_pk_bf16_f32 v134, v72, v73\n"
        "v_mfma_f32_16x16x32_bf16 v[76:79], a[224:227], a[180:183], v[76:79]\n"
        "v_cvt_pk_bf16_f32 v135, v74, v75\n"
        "v_mfma_f32_16x16x32_bf16 v[76:79], a[228:231], a[184:187], v[76:79]\n"
        "ds_read_b64_tr_b16 v[202:203], v233 offset:12320\n"
        "v_mfma_f32_16x16x32_bf16 v[76:79], a[232:235], a[188:191], v[76:79]\n"
        "v_mfma_f32_16x16x32_bf16 v[162:165], a[248:251], a[192:195], 0\n"
        "ds_read_b128 a[248:251], v241 offset:13952\n"
        "ds_read_b64_tr_b16 v[204:205], v233 offset:13856\n"
        "v_mfma_f32_16x16x32_bf16 v[162:165], a[216:219], a[196:199], v[162:165]\n"
        "ds_read_b128 a[216:219], v240 offset:14080\n"
        "v_mfma_f32_16x16x32_bf16 v[162:165], a[220:223], a[200:203], v[162:165]\n"
        "ds_read_b128 a[220:223], v241 offset:14080\n"
        "ds_read_b64_tr_b16 v[88:89], v232 offset:64\n"
        "v_mfma_f32_16x16x32_bf16 v[162:165], a[224:227], a[204:207], v[162:165]\n"
        "v_cvt_pk_bf16_f32 v142, v76, v77\n"
        "v_cvt_pk_bf16_f32 v143, v78, v79\n"
        "v_mfma_f32_16x16x32_bf16 v[162:165], a[228:231], a[208:211], v[162:165]\n"
        "ds_read_b64_tr_b16 v[90:91], v232 offset:6208\n"
        "v_mfma_f32_16x16x32_bf16 v[162:165], a[232:235], a[212:215], v[162:165]\n"
        "s_waitcnt lgkmcnt(15)\n"
        "v_mfma_f32_16x16x32_bf16 v[72:75], a[72:75], a[236:239], 0\n"
        "s_waitcnt lgkmcnt(13)\n"
        "ds_read_b64_tr_b16 v[92:93], v232 offset:18496\n"
        "v_mfma_f32_16x16x32_bf16 v[72:75], a[76:79], a[240:243], v[72:75]\n"
        "v_mfma_f32_16x16x32_bf16 v[72:75], a[80:83], a[244:247], v[72:75]\n"
        "s_waitcnt lgkmcnt(6)\n"
        "ds_read_b64_tr_b16 v[94:95], v232 offset:24640\n"
        "v_mfma_f32_16x16x32_bf16 v[72:75], a[84:87], a[248:251], v[72:75]\n"
        "s_waitcnt lgkmcnt(5)\n"
        "v_mfma_f32_16x16x32_bf16 v[72:75], a[88:91], a[216:219], v[72:75]\n"
        "s_waitcnt lgkmcnt(4)\n"
        "ds_read_b64_tr_b16 v[104:105], v233 offset:12352\n"
        "v_cvt_pk_bf16_f32 v150, v162, v163\n"
        "v_mfma_f32_16x16x32_bf16 v[72:75], a[92:95], a[220:223], v[72:75]\n"
        "v_cvt_pk_bf16_f32 v151, v164, v165\n"
        "v_mfma_f32_16x16x32_bf16 v[76:79], a[96:99], a[236:239], 0\n"
        "s_add_u32 m0, s41, 1024\n"
        "ds_read_b64_tr_b16 v[106:107], v233 offset:13888\n"
        "v_mfma_f32_16x16x32_bf16 v[76:79], a[100:103], a[240:243], v[76:79]\n"
        "v_mfma_f32_16x16x32_bf16 v[76:79], a[104:107], a[244:247], v[76:79]\n"
        "buffer_load_dwordx4 v243, s[8:11], s47 offen lds\n"
        "s_waitcnt lgkmcnt(0)\n"
        "s_barrier\n"
        "v_mfma_f32_16x16x32_bf16 v[76:79], a[108:111], a[248:251], v[76:79]\n"
        "ds_write_b128 v234, v[108:111] offset:0\n"
        "v_mfma_f32_16x16x32_bf16 v[76:79], a[112:115], a[216:219], v[76:79]\n"
        "v_cvt_pk_bf16_f32 v212, v72, v73\n"
        "v_cvt_pk_bf16_f32 v213, v74, v75\n"
        "v_mfma_f32_16x16x32_bf16 v[76:79], a[116:119], a[220:223], v[76:79]\n"
        "v_mfma_f32_16x16x32_bf16 v[162:165], a[120:123], a[236:239], 0\n"
        "ds_write_b128 v234, v[120:123] offset:6144\n"
        "v_mfma_f32_16x16x32_bf16 v[162:165], a[124:127], a[240:243], v[162:165]\n"
        "v_mfma_f32_16x16x32_bf16 v[162:165], a[128:131], a[244:247], v[162:165]\n"
        "v_mfma_f32_16x16x32_bf16 v[162:165], a[132:135], a[248:251], v[162:165]\n"
        "ds_write_b128 v234, v[124:127] offset:12288\n"
        "v_mfma_f32_16x16x32_bf16 v[162:165], a[136:139], a[216:219], v[162:165]\n"
        "v_mfma_f32_16x16x32_bf16 v[162:165], a[140:143], a[220:223], v[162:165]\n"
        "v_cvt_pk_bf16_f32 v214, v76, v77\n"
        "v_cvt_pk_bf16_f32 v215, v78, v79\n"
        "v_mfma_f32_16x16x32_bf16 v[72:75], a[0:3], a[236:239], 0\n"
        "ds_write_b128 v234, v[222:225] offset:256\n"
        "v_mfma_f32_16x16x32_bf16 v[72:75], a[4:7], a[240:243], v[72:75]\n"
        "v_mfma_f32_16x16x32_bf16 v[72:75], a[8:11], a[244:247], v[72:75]\n"
        "s_add_u32 m0, s41, 2048\n"
        "v_mfma_f32_16x16x32_bf16 v[72:75], a[12:15], a[248:251], v[72:75]\n"
        "ds_write_b128 v234, v[182:185] offset:6400\n"
        "v_mfma_f32_16x16x32_bf16 v[72:75], a[16:19], a[216:219], v[72:75]\n"
        "buffer_load_dwordx4 v244, s[8:11], s47 offen lds\n"
        "v_mfma_f32_16x16x32_bf16 v[72:75], a[20:23], a[220:223], v[72:75]\n"
        "v_cvt_pk_bf16_f32 v230, v162, v163\n"
        "v_cvt_pk_bf16_f32 v231, v164, v165\n"
        "v_mfma_f32_16x16x32_bf16 v[76:79], a[24:27], a[236:239], 0\n"
        "ds_write_b128 v234, v[202:205] offset:12544\n"
        "v_mfma_f32_16x16x32_bf16 v[76:79], a[28:31], a[240:243], v[76:79]\n"
        "v_mfma_f32_16x16x32_bf16 v[76:79], a[32:35], a[244:247], v[76:79]\n"
        "v_mfma_f32_16x16x32_bf16 v[76:79], a[36:39], a[248:251], v[76:79]\n"
        "ds_write_b128 v234, v[88:91] offset:512\n"
        "v_mfma_f32_16x16x32_bf16 v[76:79], a[40:43], a[216:219], v[76:79]\n"
        "v_cvt_pk_bf16_f32 v216, v72, v73\n"
        "v_cvt_pk_bf16_f32 v217, v74, v75\n"
        "v_mfma_f32_16x16x32_bf16 v[76:79], a[44:47], a[220:223], v[76:79]\n"
        "v_mfma_f32_16x16x32_bf16 v[162:165], a[48:51], a[236:239], 0\n"
        "ds_write_b128 v234, v[92:95] offset:6656\n"
        "v_mfma_f32_16x16x32_bf16 v[162:165], a[52:55], a[240:243], v[162:165]\n"
        "v_mfma_f32_16x16x32_bf16 v[162:165], a[56:59], a[244:247], v[162:165]\n"
        "v_mfma_f32_16x16x32_bf16 v[162:165], a[60:63], a[248:251], v[162:165]\n"
        "ds_write_b128 v234, v[104:107] offset:12800\n"
        "v_mfma_f32_16x16x32_bf16 v[162:165], a[64:67], a[216:219], v[162:165]\n"
        "s_add_u32 m0, s41, 12288\n"
        "v_cvt_pk_bf16_f32 v218, v76, v77\n"
        "v_cvt_pk_bf16_f32 v219, v78, v79\n"
        "v_mfma_f32_16x16x32_bf16 v[162:165], a[68:71], a[220:223], v[162:165]\n"
        "v_mfma_f32_16x16x32_bf16 v[88:91], a[252:255], v[64:67], 0\n"
        "buffer_load_dwordx4 v242, s[8:11], s35 offen lds\n"
        "v_mfma_f32_16x16x32_bf16 v[88:91], a[252:255], v[68:71], v[88:91]\n"
        "v_mfma_f32_16x16x32_bf16 v[92:95], v[132:135], v[68:71], 0\n"
        "v_mfma_f32_16x16x32_bf16 v[92:95], v[128:131], v[64:67], v[92:95]\n"
        "v_mfma_f32_16x16x32_bf16 v[182:185], v[136:139], v[64:67], 0\n"
        "v_mfma_f32_16x16x32_bf16 v[182:185], v[140:143], v[68:71], v[182:185]\n"
        "v_mfma_f32_16x16x32_bf16 v[202:205], v[148:151], v[68:71], 0\n"
        "s_cmp_eq_u32 s37, 100663296\n"
        "v_cvt_pk_bf16_f32 v220, v162, v163\n"
        "v_cvt_pk_bf16_f32 v221, v164, v165\n"
        "v_mfma_f32_16x16x32_bf16 v[202:205], v[144:147], v[64:67], v[202:205]\n"
        "s_cselect_b32 s46, 0, s37\n"
        "v_cvt_pk_bf16_f32 v226, v162, v163\n"
        "v_cvt_pk_bf16_f32 v227, v164, v165\n"
        "v_mfma_f32_16x16x32_bf16 v[72:75], a[252:255], v[80:83], 0\n"
        "s_add_u32 s38, s46, s43\n"
        "v_rcp_f32 v88, v88\n"
        "v_mfma_f32_16x16x32_bf16 v[72:75], a[252:255], v[84:87], v[72:75]\n"
        "s_add_u32 s39, s46, s44\n"
        "v_mul_f32 v92, v88, v92\n"
        "v_mul_f32 v93, v88, v93\n"
        "v_mfma_f32_16x16x32_bf16 v[76:79], v[132:135], v[84:87], 0\n"
        "s_add_u32 s37, s37, 6291456\n"
        "v_mul_f32 v94, v88, v94\n"
        "v_mul_f32 v95, v88, v95\n"
        "v_mfma_f32_16x16x32_bf16 v[76:79], v[128:131], v[80:83], v[76:79]\n"
        "v_rcp_f32 v72, v72\n"
        "v_mfma_f32_16x16x32_bf16 v[104:107], v[136:139], v[80:83], 0\n"
        "s_add_u32 m0, s41, 13312\n"
        "v_cvt_pk_bf16_f32 v92, v92, v93\n"
        "v_cvt_pk_bf16_f32 v93, v94, v95\n"
        "v_mfma_f32_16x16x32_bf16 v[104:107], v[140:143], v[84:87], v[104:107]\n"
        "v_mul_f32 v182, v88, v182\n"
        "v_mul_f32 v183, v88, v183\n"
        "v_mfma_f32_16x16x32_bf16 v[108:111], v[148:151], v[84:87], 0\n"
        "buffer_load_dwordx4 v243, s[8:11], s35 offen lds\n"
        "v_mfma_f32_16x16x32_bf16 v[108:111], v[144:147], v[80:83], v[108:111]\n"
        "v_mul_f32 v184, v88, v184\n"
        "v_mul_f32 v185, v88, v185\n"
        "v_mfma_f32_16x16x32_bf16 v[64:67], a[252:255], v[96:99], 0\n"
        "v_cvt_pk_bf16_f32 v94, v182, v183\n"
        "v_cvt_pk_bf16_f32 v95, v184, v185\n"
        "v_mfma_f32_16x16x32_bf16 v[64:67], a[252:255], v[100:103], v[64:67]\n"
        "ds_write_b128 v245, v[92:95] offset:0\n"
        "v_mfma_f32_16x16x32_bf16 v[68:71], v[132:135], v[100:103], 0\n"
        "v_mul_f32 v202, v88, v202\n"
        "v_mul_f32 v203, v88, v203\n"
        "v_mfma_f32_16x16x32_bf16 v[68:71], v[128:131], v[96:99], v[68:71]\n"
        "v_mul_f32 v204, v88, v204\n"
        "v_mul_f32 v205, v88, v205\n"
        "v_mfma_f32_16x16x32_bf16 v[120:123], v[136:139], v[96:99], 0\n"
        "v_cvt_pk_bf16_f32 v202, v202, v203\n"
        "v_cvt_pk_bf16_f32 v203, v204, v205\n"
        "v_mfma_f32_16x16x32_bf16 v[120:123], v[140:143], v[100:103], v[120:123]\n"
        "v_mul_f32 v76, v72, v76\n"
        "v_mul_f32 v77, v72, v77\n"
        "v_mfma_f32_16x16x32_bf16 v[124:127], v[148:151], v[100:103], 0\n"
        "ds_write_b64 v246, v[202:203] offset:0\n"
        "v_mfma_f32_16x16x32_bf16 v[124:127], v[144:147], v[96:99], v[124:127]\n"
        "v_rcp_f32 v64, v64\n"
        "v_mfma_f32_16x16x32_bf16 v[80:83], a[252:255], v[112:115], 0\n"
        "v_mul_f32 v78, v72, v78\n"
        "v_mul_f32 v79, v72, v79\n"
        "v_mfma_f32_16x16x32_bf16 v[80:83], a[252:255], v[116:119], v[80:83]\n"
        "v_cvt_pk_bf16_f32 v76, v76, v77\n"
        "v_cvt_pk_bf16_f32 v77, v78, v79\n"
        "v_mfma_f32_16x16x32_bf16 v[84:87], v[132:135], v[116:119], 0\n"
        "s_add_u32 m0, s41, 14336\n"
        "v_mul_f32 v104, v72, v104\n"
        "v_mul_f32 v105, v72, v105\n"
        "v_mfma_f32_16x16x32_bf16 v[84:87], v[128:131], v[112:115], v[84:87]\n"
        "v_mul_f32 v106, v72, v106\n"
        "v_mul_f32 v107, v72, v107\n"
        "v_mfma_f32_16x16x32_bf16 v[222:225], v[136:139], v[112:115], 0\n"
        "buffer_load_dwordx4 v244, s[8:11], s35 offen lds\n"
        "v_mfma_f32_16x16x32_bf16 v[222:225], v[140:143], v[116:119], v[222:225]\n"
        "ds_read_b128 a[224:227], v240 offset:0\n"
        "ds_read_b128 a[228:231], v241 offset:0\n"
        "v_mfma_f32_16x16x32_bf16 v[162:165], v[148:151], v[116:119], 0\n"
        "ds_read_b128 a[232:235], v240 offset:128\n"
        "v_cvt_pk_bf16_f32 v78, v104, v105\n"
        "v_mfma_f32_16x16x32_bf16 v[162:165], v[144:147], v[112:115], v[162:165]\n"
        "v_cvt_pk_bf16_f32 v79, v106, v107\n"
        "v_mul_f32 v108, v72, v108\n"
        "v_mfma_f32_16x16x32_bf16 v[96:99], a[236:239], a[144:147], 0\n"
        "ds_write_b128 v245, v[76:79] offset:256\n"
        "v_mfma_f32_16x16x32_bf16 v[96:99], a[240:243], a[148:151], v[96:99]\n"
        "v_mul_f32 v109, v72, v109\n"
        "v_mul_f32 v110, v72, v110\n"
        "v_mfma_f32_16x16x32_bf16 v[96:99], a[244:247], a[152:155], v[96:99]\n"
        "v_mul_f32 v111, v72, v111\n"
        "v_cvt_pk_bf16_f32 v108, v108, v109\n"
        "v_mfma_f32_16x16x32_bf16 v[96:99], a[248:251], a[156:159], v[96:99]\n"
        "v_cvt_pk_bf16_f32 v109, v110, v111\n"
        "v_mul_f32 v68, v64, v68\n"
        "v_mfma_f32_16x16x32_bf16 v[96:99], a[216:219], a[160:163], v[96:99]\n"
        "v_rcp_f32 v80, v80\n"
        "v_mfma_f32_16x16x32_bf16 v[96:99], a[220:223], a[164:167], v[96:99]\n"
        "ds_write_b64 v246, v[108:109] offset:256\n"
        "v_mfma_f32_16x16x32_bf16 v[100:103], a[236:239], a[168:171], 0\n"
        "v_mul_f32 v69, v64, v69\n"
        "v_mul_f32 v70, v64, v70\n"
        "v_mfma_f32_16x16x32_bf16 v[100:103], a[240:243], a[172:175], v[100:103]\n"
        "v_mul_f32 v71, v64, v71\n"
        "v_cvt_pk_bf16_f32 v68, v68, v69\n"
        "v_mfma_f32_16x16x32_bf16 v[100:103], a[244:247], a[176:179], v[100:103]\n"
        "v_cvt_pk_bf16_f32 v134, v96, v97\n"
        "v_cvt_pk_bf16_f32 v135, v98, v99\n"
        "v_mfma_f32_16x16x32_bf16 v[100:103], a[248:251], a[180:183], v[100:103]\n"
        "v_cvt_pk_bf16_f32 v69, v70, v71\n"
        "v_mul_f32 v120, v64, v120\n"
        "v_mfma_f32_16x16x32_bf16 v[100:103], a[216:219], a[184:187], v[100:103]\n"
        "v_mul_f32 v121, v64, v121\n"
        "v_mul_f32 v122, v64, v122\n"
        "v_mfma_f32_16x16x32_bf16 v[100:103], a[220:223], a[188:191], v[100:103]\n"
        "v_mul_f32 v123, v64, v123\n"
        "v_cvt_pk_bf16_f32 v70, v120, v121\n"
        "v_mfma_f32_16x16x32_bf16 v[112:115], a[236:239], a[192:195], 0\n"
        "ds_read_b128 a[236:239], v241 offset:128\n"
        "v_cvt_pk_bf16_f32 v71, v122, v123\n"
        "v_mfma_f32_16x16x32_bf16 v[112:115], a[240:243], a[196:199], v[112:115]\n"
        "ds_read_b128 a[240:243], v240 offset:256\n"
        "v_mul_f32 v124, v64, v124\n"
        "v_mfma_f32_16x16x32_bf16 v[112:115], a[244:247], a[200:203], v[112:115]\n"
        "ds_read_b128 a[244:247], v241 offset:256\n"
        "v_cvt_pk_bf16_f32 v142, v100, v101\n"
        "v_mfma_f32_16x16x32_bf16 v[112:115], a[248:251], a[204:207], v[112:115]\n"
        "ds_read_b128 a[248:251], v240 offset:1536\n"
        "v_cvt_pk_bf16_f32 v143, v102, v103\n"
        "v_mfma_f32_16x16x32_bf16 v[112:115], a[216:219], a[208:211], v[112:115]\n"
        "ds_read_b128 a[216:219], v241 offset:1536\n"
        "v_mul_f32 v125, v64, v125\n"
        "v_mfma_f32_16x16x32_bf16 v[112:115], a[220:223], a[212:215], v[112:115]\n"
        "s_waitcnt lgkmcnt(9)\n"
        "ds_read_b128 a[220:223], v240 offset:1664\n"
        "v_mul_f32 v126, v64, v126\n"
        "v_mfma_f32_16x16x32_bf16 v[96:99], a[224:227], a[144:147], 0\n"
        "s_waitcnt lgkmcnt(9)\n"
        "ds_write_b128 v245, v[68:71] offset:512\n"
        "v_mfma_f32_16x16x32_bf16 v[96:99], a[228:231], a[148:151], v[96:99]\n"
        "s_waitcnt lgkmcnt(9)\n"
        "v_mul_f32 v127, v64, v127\n"
        "v_cvt_pk_bf16_f32 v124, v124, v125\n"
        "v_mfma_f32_16x16x32_bf16 v[96:99], a[232:235], a[152:155], v[96:99]\n"
        "s_waitcnt lgkmcnt(6)\n"
        "v_cvt_pk_bf16_f32 v150, v112, v113\n"
        "v_cvt_pk_bf16_f32 v151, v114, v115\n"
        "v_mfma_f32_16x16x32_bf16 v[96:99], a[236:239], a[156:159], v[96:99]\n"
        "s_waitcnt lgkmcnt(5)\n"
        "v_cvt_pk_bf16_f32 v125, v126, v127\n"
        "v_mul_f32 v84, v80, v84\n"
        "v_mfma_f32_16x16x32_bf16 v[96:99], a[240:243], a[160:163], v[96:99]\n"
        "s_waitcnt lgkmcnt(4)\n"
        "v_mul_f32 v85, v80, v85\n"
        "v_mul_f32 v86, v80, v86\n"
        "v_mfma_f32_16x16x32_bf16 v[96:99], a[244:247], a[164:167], v[96:99]\n"
        "ds_write_b64 v246, v[124:125] offset:512\n"
        "v_mfma_f32_16x16x32_bf16 v[100:103], a[224:227], a[168:171], 0\n"
        "v_mul_f32 v87, v80, v87\n"
        "v_cvt_pk_bf16_f32 v84, v84, v85\n"
        "v_mfma_f32_16x16x32_bf16 v[100:103], a[228:231], a[172:175], v[100:103]\n"
        "v_cvt_pk_bf16_f32 v85, v86, v87\n"
        "v_mul_f32 v222, v80, v222\n"
        "v_mfma_f32_16x16x32_bf16 v[100:103], a[232:235], a[176:179], v[100:103]\n"
        "v_cvt_pk_bf16_f32 v128, v96, v97\n"
        "v_cvt_pk_bf16_f32 v129, v98, v99\n"
        "v_mfma_f32_16x16x32_bf16 v[100:103], a[236:239], a[180:183], v[100:103]\n"
        "v_mul_f32 v223, v80, v223\n"
        "v_mul_f32 v224, v80, v224\n"
        "v_mfma_f32_16x16x32_bf16 v[100:103], a[240:243], a[184:187], v[100:103]\n"
        "v_mul_f32 v225, v80, v225\n"
        "v_cvt_pk_bf16_f32 v86, v222, v223\n"
        "v_mfma_f32_16x16x32_bf16 v[100:103], a[244:247], a[188:191], v[100:103]\n"
        "v_cvt_pk_bf16_f32 v87, v224, v225\n"
        "v_mul_f32 v162, v80, v162\n"
        "v_mfma_f32_16x16x32_bf16 v[112:115], a[224:227], a[192:195], 0\n"
        "ds_read_b128 a[224:227], v241 offset:1664\n"
        "v_mul_f32 v163, v80, v163\n"
        "v_mfma_f32_16x16x32_bf16 v[112:115], a[228:231], a[196:199], v[112:115]\n"
        "ds_read_b128 a[228:231], v240 offset:1792\n"
        "v_mul_f32 v164, v80, v164\n"
        "v_mfma_f32_16x16x32_bf16 v[112:115], a[232:235], a[200:203], v[112:115]\n"
        "ds_read_b128 a[232:235], v241 offset:1792\n"
        "v_cvt_pk_bf16_f32 v136, v100, v101\n"
        "v_mfma_f32_16x16x32_bf16 v[112:115], a[236:239], a[204:207], v[112:115]\n"
        "ds_read_b128 a[236:239], v240 offset:12288\n"
        "v_cvt_pk_bf16_f32 v137, v102, v103\n"
        "v_mfma_f32_16x16x32_bf16 v[112:115], a[240:243], a[208:211], v[112:115]\n"
        "ds_read_b128 a[240:243], v241 offset:12288\n"
        "v_mul_f32 v165, v80, v165\n"
        "v_mfma_f32_16x16x32_bf16 v[112:115], a[244:247], a[212:215], v[112:115]\n"
        "s_waitcnt lgkmcnt(9)\n"
        "ds_read_b128 a[244:247], v240 offset:12416\n"
        "v_cvt_pk_bf16_f32 v162, v162, v163\n"
        "v_mfma_f32_16x16x32_bf16 v[96:99], a[248:251], a[144:147], 0\n"
        "s_waitcnt lgkmcnt(9)\n"
        "ds_write_b128 v245, v[84:87] offset:768\n"
        "v_mfma_f32_16x16x32_bf16 v[96:99], a[216:219], a[148:151], v[96:99]\n"
        "s_waitcnt lgkmcnt(9)\n"
        "v_cvt_pk_bf16_f32 v163, v164, v165\n"
        "v_mfma_f32_16x16x32_bf16 v[96:99], a[220:223], a[152:155], v[96:99]\n"
        "s_waitcnt lgkmcnt(6)\n"
        "v_mfma_f32_16x16x32_bf16 v[96:99], a[224:227], a[156:159], v[96:99]\n"
        "s_waitcnt lgkmcnt(5)\n"
        "v_cvt_pk_bf16_f32 v144, v112, v113\n"
        "v_cvt_pk_bf16_f32 v145, v114, v115\n"
        "v_mfma_f32_16x16x32_bf16 v[96:99], a[228:231], a[160:163], v[96:99]\n"
        "s_waitcnt lgkmcnt(4)\n"
        "v_mfma_f32_16x16x32_bf16 v[96:99], a[232:235], a[164:167], v[96:99]\n"
        "ds_write_b64 v246, v[162:163] offset:768\n"
        "v_mfma_f32_16x16x32_bf16 v[100:103], a[248:251], a[168:171], 0\n"
        "v_mfma_f32_16x16x32_bf16 v[100:103], a[216:219], a[172:175], v[100:103]\n"
        "v_mfma_f32_16x16x32_bf16 v[100:103], a[220:223], a[176:179], v[100:103]\n"
        "v_mfma_f32_16x16x32_bf16 v[100:103], a[224:227], a[180:183], v[100:103]\n"
        "v_mfma_f32_16x16x32_bf16 v[100:103], a[228:231], a[184:187], v[100:103]\n"
        "v_mfma_f32_16x16x32_bf16 v[100:103], a[232:235], a[188:191], v[100:103]\n"
        "v_mfma_f32_16x16x32_bf16 v[112:115], a[248:251], a[192:195], 0\n"
        "ds_read_b128 a[248:251], v241 offset:12416\n"
        "ds_read_b128 v[162:165], v251 offset:0\n"
        "v_mfma_f32_16x16x32_bf16 v[112:115], a[216:219], a[196:199], v[112:115]\n"
        "ds_read_b128 a[216:219], v240 offset:12544\n"
        "v_cvt_pk_bf16_f32 v130, v96, v97\n"
        "v_mfma_f32_16x16x32_bf16 v[112:115], a[220:223], a[200:203], v[112:115]\n"
        "ds_read_b128 a[220:223], v241 offset:12544\n"
        "v_cvt_pk_bf16_f32 v131, v98, v99\n"
        "v_mfma_f32_16x16x32_bf16 v[112:115], a[224:227], a[204:207], v[112:115]\n"
        "v_cvt_pk_bf16_f32 v138, v100, v101\n"
        "v_cvt_pk_bf16_f32 v139, v102, v103\n"
        "v_mfma_f32_16x16x32_bf16 v[112:115], a[228:231], a[208:211], v[112:115]\n"
        "ds_read_b128 v[182:185], v251 offset:512\n"
        "v_mfma_f32_16x16x32_bf16 v[112:115], a[232:235], a[212:215], v[112:115]\n"
        "s_waitcnt lgkmcnt(9)\n"
        "v_mfma_f32_16x16x32_bf16 v[96:99], a[236:239], a[144:147], 0\n"
        "s_waitcnt lgkmcnt(8)\n"
        "v_mfma_f32_16x16x32_bf16 v[96:99], a[240:243], a[148:151], v[96:99]\n"
        "s_waitcnt lgkmcnt(7)\n"
        "v_mfma_f32_16x16x32_bf16 v[96:99], a[244:247], a[152:155], v[96:99]\n"
        "s_waitcnt lgkmcnt(4)\n"
        "ds_read_b128 v[202:205], v251 offset:4096\n"
        "v_mfma_f32_16x16x32_bf16 v[96:99], a[248:251], a[156:159], v[96:99]\n"
        "s_waitcnt lgkmcnt(3)\n"
        "v_mfma_f32_16x16x32_bf16 v[96:99], a[216:219], a[160:163], v[96:99]\n"
        "s_waitcnt lgkmcnt(2)\n"
        "v_mfma_f32_16x16x32_bf16 v[96:99], a[220:223], a[164:167], v[96:99]\n"
        "s_add_u32 s47, s47, 6291456\n"
        "v_cvt_pk_bf16_f32 v146, v112, v113\n"
        "v_cvt_pk_bf16_f32 v147, v114, v115\n"
        "v_mfma_f32_16x16x32_bf16 v[100:103], a[236:239], a[168:171], 0\n"
        "ds_read_b128 v[222:225], v251 offset:4608\n"
        "s_add_u32 s36, s36, 2097152\n"
        "v_mfma_f32_16x16x32_bf16 v[100:103], a[240:243], a[172:175], v[100:103]\n"
        "s_xor_b32 s48, s48, 8192\n"
        "v_cvt_pk_bf16_f32 v132, v96, v97\n"
        "v_cvt_pk_bf16_f32 v133, v98, v99\n"
        "v_mfma_f32_16x16x32_bf16 v[100:103], a[244:247], a[176:179], v[100:103]\n"
        "v_pk_min_u16 v162, v162, s26\n"
        "v_pk_min_u16 v163, v163, s26\n"
        "v_mfma_f32_16x16x32_bf16 v[100:103], a[248:251], a[180:183], v[100:103]\n"
        "v_pk_min_u16 v164, v164, s26\n"
        "v_pk_min_u16 v165, v165, s26\n"
        "v_mfma_f32_16x16x32_bf16 v[100:103], a[216:219], a[184:187], v[100:103]\n"
        "v_add_u32 v240, s31, v240\n"
        "v_add_u32 v241, s31, v241\n"
        "v_mfma_f32_16x16x32_bf16 v[100:103], a[220:223], a[188:191], v[100:103]\n"
        "v_mfma_f32_16x16x32_bf16 v[112:115], a[236:239], a[192:195], 0\n"
        "s_waitcnt lgkmcnt(2)\n"
        "v_pk_min_u16 v182, v182, s26\n"
        "v_pk_min_u16 v183, v183, s26\n"
        "v_mfma_f32_16x16x32_bf16 v[112:115], a[240:243], a[196:199], v[112:115]\n"
        "v_pk_min_u16 v184, v184, s26\n"
        "v_pk_min_u16 v185, v185, s26\n"
        "v_mfma_f32_16x16x32_bf16 v[112:115], a[244:247], a[200:203], v[112:115]\n"
        "v_mfma_f32_16x16x32_bf16 v[112:115], a[248:251], a[204:207], v[112:115]\n"
        "v_cvt_pk_bf16_f32 v140, v100, v101\n"
        "v_cvt_pk_bf16_f32 v141, v102, v103\n"
        "v_mfma_f32_16x16x32_bf16 v[112:115], a[216:219], a[208:211], v[112:115]\n"
        "s_waitcnt lgkmcnt(1)\n"
        "v_pk_min_u16 v202, v202, s26\n"
        "v_pk_min_u16 v203, v203, s26\n"
        "v_mfma_f32_16x16x32_bf16 v[112:115], a[220:223], a[212:215], v[112:115]\n"
        "v_pk_min_u16 v204, v204, s26\n"
        "v_pk_min_u16 v205, v205, s26\n"
        "v_mfma_f32_16x16x32_bf16 v[64:67], v[152:155], v[156:159], v[0:3]\n"
        "v_mfma_f32_16x16x32_bf16 v[64:67], v[168:171], v[160:163], v[64:67]\n"
        "v_mfma_f32_16x16x32_bf16 v[68:71], v[172:175], v[156:159], v[4:7]\n"
        "s_waitcnt lgkmcnt(0)\n"
        "v_pk_min_u16 v222, v222, s26\n"
        "v_pk_min_u16 v223, v223, s26\n"
        "v_mfma_f32_16x16x32_bf16 v[68:71], v[188:191], v[164:167], v[68:71]\n"
        "ds_read_b128 v[162:165], v252 offset:0\n"
        "v_cvt_pk_bf16_f32 v148, v112, v113\n"
        "v_mfma_f32_16x16x32_bf16 v[80:83], v[152:155], v[176:179], v[16:19]\n"
        "v_cvt_pk_bf16_f32 v149, v114, v115\n"
        "v_pk_min_u16 v224, v224, s26\n"
        "v_mfma_f32_16x16x32_bf16 v[80:83], v[168:171], v[180:183], v[80:83]\n"
        "v_pk_min_u16 v225, v225, s26\n"
        "v_max3_f32 v232, v64, v65, v66\n"
        "v_mfma_f32_16x16x32_bf16 v[84:87], v[172:175], v[176:179], v[20:23]\n"
        "v_max3_f32 v232, v232, v67, v68\n"
        "v_max3_f32 v232, v232, v69, v70\n"
        "v_mfma_f32_16x16x32_bf16 v[84:87], v[188:191], v[184:187], v[84:87]\n"
        "ds_read_b128 v[182:185], v252 offset:512\n"
        "v_mfma_f32_16x16x32_bf16 v[96:99], v[152:155], v[196:199], v[32:35]\n"
        "v_max3_f32 v233, v80, v81, v82\n"
        "v_mfma_f32_16x16x32_bf16 v[96:99], v[168:171], v[200:203], v[96:99]\n"
        "v_mfma_f32_16x16x32_bf16 v[100:103], v[172:175], v[196:199], v[36:39]\n"
        "v_mfma_f32_16x16x32_bf16 v[100:103], v[188:191], v[204:207], v[100:103]\n"
        "ds_read_b128 v[202:205], v252 offset:4096\n"
        "v_mfma_f32_16x16x32_bf16 v[112:115], v[152:155], v[216:219], v[48:51]\n"
        "s_waitcnt lgkmcnt(2)\n"
        "v_pk_min_u16 v162, v162, s26\n"
        "v_pk_min_u16 v163, v163, s26\n"
        "v_mfma_f32_16x16x32_bf16 v[112:115], v[168:171], v[220:223], v[112:115]\n"
        "v_pk_min_u16 v164, v164, s26\n"
        "v_pk_min_u16 v165, v165, s26\n"
        "v_mfma_f32_16x16x32_bf16 v[116:119], v[172:175], v[216:219], v[52:55]\n"
        "v_max3_f32 v233, v233, v83, v84\n"
        "v_max3_f32 v233, v233, v85, v86\n"
        "v_mfma_f32_16x16x32_bf16 v[116:119], v[188:191], v[224:227], v[116:119]\n"
        "ds_read_b128 v[222:225], v252 offset:4608\n"
        "v_max3_f32 v234, v96, v97, v98\n"
        "v_mfma_f32_16x16x32_bf16 v[72:75], v[192:195], v[156:159], v[8:11]\n"
        "s_waitcnt lgkmcnt(2)\n"
        "v_pk_min_u16 v182, v182, s26\n"
        "v_pk_min_u16 v183, v183, s26\n"
        "v_mfma_f32_16x16x32_bf16 v[72:75], v[208:211], v[160:163], v[72:75]\n"
        "v_pk_min_u16 v184, v184, s26\n"
        "v_pk_min_u16 v185, v185, s26\n"
        "v_mfma_f32_16x16x32_bf16 v[76:79], v[212:215], v[156:159], v[12:15]\n"
        "v_max3_f32 v234, v234, v99, v100\n"
        "v_max3_f32 v234, v234, v101, v102\n"
        "v_mfma_f32_16x16x32_bf16 v[76:79], v[228:231], v[164:167], v[76:79]\n"
        "v_max3_f32 v235, v112, v113, v114\n"
        "v_max3_f32 v235, v235, v115, v116\n"
        "v_mfma_f32_16x16x32_bf16 v[88:91], v[192:195], v[176:179], v[24:27]\n"
        "s_barrier\n"
        "ds_read_b128 v[152:155], v247 offset:0\n"
        "ds_read_b128 v[160:163], v247 offset:4096\n"
        "v_mfma_f32_16x16x32_bf16 v[88:91], v[208:211], v[180:183], v[88:91]\n"
        "ds_read_b128 v[156:159], v247 offset:256\n"
        "ds_read_b128 v[164:167], v247 offset:4352\n"
        "v_mfma_f32_16x16x32_bf16 v[92:95], v[212:215], v[176:179], v[28:31]\n"
        "ds_read_b128 a[216:219], v248 offset:0\n"
        "ds_read_b128 a[220:223], v248 offset:6144\n"
        "v_mfma_f32_16x16x32_bf16 v[92:95], v[228:231], v[184:187], v[92:95]\n"
        "s_waitcnt lgkmcnt(7)\n"
        "v_pk_min_u16 v202, v202, s26\n"
        "v_pk_min_u16 v203, v203, s26\n"
        "v_mfma_f32_16x16x32_bf16 v[104:107], v[192:195], v[196:199], v[40:43]\n"
        "ds_read_b128 a[224:227], v254 offset:16\n"
        "ds_read_b128 a[228:231], v254 offset:6160\n"
        "v_mfma_f32_16x16x32_bf16 v[104:107], v[208:211], v[200:203], v[104:107]\n"
        "v_pk_min_u16 v204, v204, s26\n"
        "v_pk_min_u16 v205, v205, s26\n"
        "v_mfma_f32_16x16x32_bf16 v[108:111], v[212:215], v[196:199], v[44:47]\n"
        "s_waitcnt lgkmcnt(8)\n"
        "v_pk_min_u16 v222, v222, s26\n"
        "v_pk_min_u16 v223, v223, s26\n"
        "v_mfma_f32_16x16x32_bf16 v[108:111], v[228:231], v[204:207], v[108:111]\n"
        "v_pk_min_u16 v224, v224, s26\n"
        "v_pk_min_u16 v225, v225, s26\n"
        "v_mfma_f32_16x16x32_bf16 v[120:123], v[192:195], v[216:219], v[56:59]\n"
        "ds_read_b128 a[232:235], v248 offset:32\n"
        "v_max3_f32 v232, v232, v71, v72\n"
        "v_mfma_f32_16x16x32_bf16 v[120:123], v[208:211], v[220:223], v[120:123]\n"
        "v_max3_f32 v232, v232, v73, v74\n"
        "v_max3_f32 v232, v232, v75, v76\n"
        "v_mfma_f32_16x16x32_bf16 v[124:127], v[212:215], v[216:219], v[60:63]\n"
        "v_max3_f32 v232, v232, v77, v78\n"
        "v_max_f32 v232, v232, v79\n"
        "v_mfma_f32_16x16x32_bf16 v[124:127], v[228:231], v[224:227], v[124:127]\n"
        "s_waitcnt lgkmcnt(4)\n"
        "v_max3_f32 v233, v233, v87, v88\n"
        "v_max3_f32 v233, v233, v89, v90\n"
        "v_mfma_f32_16x16x32_bf16 v[172:175], v[152:155], a[216:219], 0\n"
        "s_waitcnt lgkmcnt(3)\n"
        "v_max3_f32 v233, v233, v91, v92\n"
        "v_max3_f32 v233, v233, v93, v94\n"
        "v_mfma_f32_16x16x32_bf16 v[172:175], v[160:163], a[220:223], v[172:175]\n"
        "v_max_f32 v233, v233, v95\n"
        "v_max3_f32 v234, v234, v103, v104\n"
        "v_mfma_f32_16x16x32_bf16 v[180:183], v[164:167], a[220:223], 0\n"
        "ds_read_b128 a[220:223], v248 offset:6176\n"
        "ds_read_b128 a[236:239], v247 offset:8192\n"
        "v_mfma_f32_16x16x32_bf16 v[180:183], v[156:159], a[216:219], v[180:183]\n"
        "s_waitcnt lgkmcnt(4)\n"
        "ds_read_b128 a[216:219], v254 offset:48\n"
        "ds_read_b128 a[244:247], v247 offset:12288\n"
        "v_mfma_f32_16x16x32_bf16 v[184:187], v[156:159], a[224:227], 0\n"
        "s_waitcnt lgkmcnt(5)\n"
        "ds_read_b128 a[240:243], v247 offset:8448\n"
        "ds_read_b128 a[248:251], v247 offset:12544\n"
        "v_mfma_f32_16x16x32_bf16 v[184:187], v[164:167], a[228:231], v[184:187]\n"
        "v_permlane32_swap_b32 v232, v233\n"
        "v_mfma_f32_16x16x32_bf16 v[176:179], v[160:163], a[228:231], 0\n"
        "ds_read_b128 a[228:231], v254 offset:6192\n"
        "v_max_f32 v232, v232, v233\n"
        "v_mfma_f32_16x16x32_bf16 v[176:179], v[152:155], a[224:227], v[176:179]\n"
        "s_waitcnt lgkmcnt(7)\n"
        "ds_read_b128 a[224:227], v248 offset:64\n"
        "v_mov_b32 v233, v232\n"
        "v_mfma_f32_16x16x32_bf16 v[212:215], v[152:155], a[232:235], 0\n"
        "s_waitcnt lgkmcnt(7)\n"
        "v_max3_f32 v234, v234, v105, v106\n"
        "v_max3_f32 v234, v234, v107, v108\n"
        "v_mfma_f32_16x16x32_bf16 v[212:215], v[160:163], a[220:223], v[212:215]\n"
        "v_permlane16_swap_b32 v232, v233\n"
        "v_mfma_f32_16x16x32_bf16 v[192:195], v[164:167], a[220:223], 0\n"
        "ds_read_b128 a[220:223], v248 offset:6208\n"
        "v_max_f32 v232, v232, v233\n"
        "v_mfma_f32_16x16x32_bf16 v[192:195], v[156:159], a[232:235], v[192:195]\n"
        "s_waitcnt lgkmcnt(6)\n"
        "ds_read_b128 a[232:235], v254 offset:80\n"
        "v_mul_f32 v236, s25, v232\n"
        "v_mfma_f32_16x16x32_bf16 v[196:199], v[156:159], a[216:219], 0\n"
        "s_waitcnt lgkmcnt(3)\n"
        "v_mul_f32 v237, s25, v232\n"
        "v_max3_f32 v234, v234, v109, v110\n"
        "v_mfma_f32_16x16x32_bf16 v[196:199], v[164:167], a[228:231], v[196:199]\n"
        "v_permlane32_swap_b32 v236, v237\n"
        "v_mfma_f32_16x16x32_bf16 v[216:219], v[160:163], a[228:231], 0\n"
        "ds_read_b128 a[228:231], v254 offset:6224\n"
        "v_max_f32 v234, v234, v111\n"
        "v_mfma_f32_16x16x32_bf16 v[216:219], v[152:155], a[216:219], v[216:219]\n"
        "s_waitcnt lgkmcnt(3)\n"
        "ds_read_b128 a[216:219], v248 offset:36864\n"
        "v_max3_f32 v235, v235, v117, v118\n"
        "v_mfma_f32_16x16x32_bf16 v[220:223], v[152:155], a[224:227], 0\n"
        "s_waitcnt lgkmcnt(3)\n"
        "v_max3_f32 v235, v235, v119, v120\n"
        "v_max3_f32 v235, v235, v121, v122\n"
        "v_mfma_f32_16x16x32_bf16 v[220:223], v[160:163], a[220:223], v[220:223]\n"
        "v_max3_f32 v235, v235, v123, v124\n"
        "v_max3_f32 v235, v235, v125, v126\n"
        "v_mfma_f32_16x16x32_bf16 v[200:203], v[164:167], a[220:223], 0\n"
        "ds_read_b128 a[220:223], v248 offset:43008\n"
        "v_max_f32 v235, v235, v127\n"
        "v_mfma_f32_16x16x32_bf16 v[200:203], v[156:159], a[224:227], v[200:203]\n"
        "s_waitcnt lgkmcnt(3)\n"
        "ds_read_b128 a[224:227], v254 offset:36880\n"
        "v_fma_f32 v64, v64, s24, v236\n"
        "v_mfma_f32_16x16x32_bf16 v[204:207], v[156:159], a[232:235], 0\n"
        "s_waitcnt lgkmcnt(3)\n"
        "v_permlane32_swap_b32 v234, v235\n"
        "v_mfma_f32_16x16x32_bf16 v[204:207], v[164:167], a[228:231], v[204:207]\n"
        "v_max_f32 v234, v234, v235\n"
        "v_mov_b32 v235, v234\n"
        "v_mfma_f32_16x16x32_bf16 v[224:227], v[160:163], a[228:231], 0\n"
        "ds_read_b128 a[228:231], v254 offset:43024\n"
        "v_fma_f32 v65, v65, s24, v236\n"
        "v_mfma_f32_16x16x32_bf16 v[224:227], v[152:155], a[232:235], v[224:227]\n"
        "s_waitcnt lgkmcnt(3)\n"
        "ds_read_b128 a[232:235], v248 offset:36896\n"
        "v_fma_f32 v66, v66, s24, v236\n"
        "v_mfma_f32_16x16x32_bf16 v[172:175], a[236:239], a[216:219], v[172:175]\n"
        "s_waitcnt lgkmcnt(3)\n"
        "v_permlane16_swap_b32 v234, v235\n"
        "v_mfma_f32_16x16x32_bf16 v[172:175], a[244:247], a[220:223], v[172:175]\n"
        "v_max_f32 v234, v234, v235\n"
        "v_mul_f32 v238, s25, v234\n"
        "v_mfma_f32_16x16x32_bf16 v[180:183], a[248:251], a[220:223], v[180:183]\n"
        "ds_read_b128 a[220:223], v248 offset:43040\n"
        "ds_read_b128 v[152:155], v247 offset:16384\n"
        "v_mfma_f32_16x16x32_bf16 v[180:183], a[240:243], a[216:219], v[180:183]\n"
        "s_waitcnt lgkmcnt(4)\n"
        "ds_read_b128 a[216:219], v254 offset:36912\n"
        "ds_read_b128 v[160:163], v247 offset:20480\n"
        "v_mfma_f32_16x16x32_bf16 v[184:187], a[240:243], a[224:227], v[184:187]\n"
        "s_waitcnt lgkmcnt(5)\n"
        "ds_read_b128 v[156:159], v247 offset:16640\n"
        "ds_read_b128 v[164:167], v247 offset:20736\n"
        "v_mfma_f32_16x16x32_bf16 v[184:187], a[248:251], a[228:231], v[184:187]\n"
        "v_mul_f32 v239, s25, v234\n"
        "v_fma_f32 v67, v67, s24, v236\n"
        "v_mfma_f32_16x16x32_bf16 v[176:179], a[244:247], a[228:231], v[176:179]\n"
        "ds_read_b128 a[228:231], v254 offset:43056\n"
        "v_fma_f32 v68, v68, s24, v236\n"
        "v_mfma_f32_16x16x32_bf16 v[176:179], a[236:239], a[224:227], v[176:179]\n"
        "s_waitcnt lgkmcnt(7)\n"
        "ds_read_b128 a[224:227], v248 offset:36928\n"
        "v_fma_f32 v69, v69, s24, v236\n"
        "v_mfma_f32_16x16x32_bf16 v[212:215], a[236:239], a[232:235], v[212:215]\n"
        "s_waitcnt lgkmcnt(7)\n"
        "v_permlane32_swap_b32 v238, v239\n"
        "v_mfma_f32_16x16x32_bf16 v[212:215], a[244:247], a[220:223], v[212:215]\n"
        "v_exp_f32 v64, v64\n"
        "v_mfma_f32_16x16x32_bf16 v[192:195], a[248:251], a[220:223], v[192:195]\n"
        "ds_read_b128 a[220:223], v248 offset:43072\n"
        "v_fma_f32 v70, v70, s24, v236\n"
        "v_mfma_f32_16x16x32_bf16 v[192:195], a[240:243], a[232:235], v[192:195]\n"
        "s_waitcnt lgkmcnt(6)\n"
        "ds_read_b128 a[232:235], v254 offset:36944\n"
        "v_fma_f32 v71, v71, s24, v236\n"
        "v_mfma_f32_16x16x32_bf16 v[196:199], a[240:243], a[216:219], v[196:199]\n"
        "s_waitcnt lgkmcnt(3)\n"
        "v_exp_f32 v65, v65\n"
        "v_mfma_f32_16x16x32_bf16 v[196:199], a[248:251], a[228:231], v[196:199]\n"
        "v_exp_f32 v66, v66\n"
        "v_mfma_f32_16x16x32_bf16 v[216:219], a[244:247], a[228:231], v[216:219]\n"
        "ds_read_b128 a[228:231], v254 offset:43088\n"
        "v_cvt_pk_bf16_f32 v64, v64, v65\n"
        "v_mfma_f32_16x16x32_bf16 v[216:219], a[236:239], a[216:219], v[216:219]\n"
        "s_waitcnt lgkmcnt(3)\n"
        "ds_read_b128 a[216:219], v248 offset:12288\n"
        "v_fma_f32 v72, v72, s24, v236\n"
        "v_mfma_f32_16x16x32_bf16 v[220:223], a[236:239], a[224:227], v[220:223]\n"
        "s_waitcnt lgkmcnt(3)\n"
        "v_exp_f32 v67, v67\n"
        "v_mfma_f32_16x16x32_bf16 v[220:223], a[244:247], a[220:223], v[220:223]\n"
        "v_exp_f32 v68, v68\n"
        "v_mfma_f32_16x16x32_bf16 v[200:203], a[248:251], a[220:223], v[200:203]\n"
        "ds_read_b128 a[220:223], v248 offset:49152\n"
        "v_cvt_pk_bf16_f32 v65, v66, v67\n"
        "v_mfma_f32_16x16x32_bf16 v[200:203], a[240:243], a[224:227], v[200:203]\n"
        "s_waitcnt lgkmcnt(3)\n"
        "ds_read_b128 a[224:227], v254 offset:12304\n"
        "v_fma_f32 v73, v73, s24, v236\n"
        "v_mfma_f32_16x16x32_bf16 v[204:207], a[240:243], a[232:235], v[204:207]\n"
        "s_waitcnt lgkmcnt(3)\n"
        "v_exp_f32 v69, v69\n"
        "v_mfma_f32_16x16x32_bf16 v[204:207], a[248:251], a[228:231], v[204:207]\n"
        "v_exp_f32 v70, v70\n"
        "v_mfma_f32_16x16x32_bf16 v[224:227], a[244:247], a[228:231], v[224:227]\n"
        "ds_read_b128 a[228:231], v254 offset:49168\n"
        "v_cvt_pk_bf16_f32 v66, v68, v69\n"
        "v_mfma_f32_16x16x32_bf16 v[224:227], a[236:239], a[232:235], v[224:227]\n"
        "s_waitcnt lgkmcnt(3)\n"
        "ds_read_b128 a[232:235], v248 offset:12320\n"
        "v_fma_f32 v74, v74, s24, v236\n"
        "v_mfma_f32_16x16x32_bf16 v[172:175], v[152:155], a[216:219], v[172:175]\n"
        "s_waitcnt lgkmcnt(3)\n"
        "v_exp_f32 v71, v71\n"
        "v_mfma_f32_16x16x32_bf16 v[172:175], v[160:163], a[220:223], v[172:175]\n"
        "v_exp_f32 v72, v72\n"
        "v_mfma_f32_16x16x32_bf16 v[180:183], v[164:167], a[220:223], v[180:183]\n"
        "ds_read_b128 a[220:223], v248 offset:49184\n"
        "v_cvt_pk_bf16_f32 v67, v70, v71\n"
        "v_mfma_f32_16x16x32_bf16 v[180:183], v[156:159], a[216:219], v[180:183]\n"
        "s_waitcnt lgkmcnt(3)\n"
        "ds_read_b128 a[216:219], v254 offset:12336\n"
        "v_fma_f32 v75, v75, s24, v236\n"
        "v_mfma_f32_16x16x32_bf16 v[184:187], v[156:159], a[224:227], v[184:187]\n"
        "s_waitcnt lgkmcnt(3)\n"
        "v_exp_f32 v73, v73\n"
        "v_mfma_f32_16x16x32_bf16 v[184:187], v[164:167], a[228:231], v[184:187]\n"
        "v_cvt_pk_bf16_f32 v68, v72, v73\n"
        "v_xor_b32 v251, 0x2000, v251\n"
        "v_mfma_f32_16x16x32_bf16 v[176:179], v[160:163], a[228:231], v[176:179]\n"
        "ds_read_b128 a[228:231], v254 offset:49200\n"
        "v_xor_b32 v252, 0x2000, v252\n"
        "v_mfma_f32_16x16x32_bf16 v[176:179], v[152:155], a[224:227], v[176:179]\n"
        "s_waitcnt lgkmcnt(3)\n"
        "ds_read_b128 a[224:227], v248 offset:12352\n"
        "v_add_u32 v245, s45, v245\n"
        "v_mfma_f32_16x16x32_bf16 v[212:215], v[152:155], a[232:235], v[212:215]\n"
        "s_waitcnt lgkmcnt(3)\n"
        "s_waitcnt vmcnt(0)\n"
        "s_barrier\n"
        "v_exp_f32 v74, v74\n"
        "v_mfma_f32_16x16x32_bf16 v[212:215], v[160:163], a[220:223], v[212:215]\n"
        "v_exp_f32 v75, v75\n"
        "v_mfma_f32_16x16x32_bf16 v[192:195], v[164:167], a[220:223], v[192:195]\n"
        "ds_read_b128 a[220:223], v248 offset:49216\n"
        "ds_read_b128 a[236:239], v240 offset:0\n"
        "v_mfma_f32_16x16x32_bf16 v[192:195], v[156:159], a[232:235], v[192:195]\n"
        "s_waitcnt lgkmcnt(4)\n"
        "ds_read_b128 a[232:235], v254 offset:12368\n"
        "ds_read_b128 a[240:243], v241 offset:0\n"
        "v_mfma_f32_16x16x32_bf16 v[196:199], v[156:159], a[216:219], v[196:199]\n"
        "s_waitcnt lgkmcnt(5)\n"
        "ds_read_b128 a[244:247], v240 offset:128\n"
        "v_cvt_pk_bf16_f32 v69, v74, v75\n"
        "v_mfma_f32_16x16x32_bf16 v[196:199], v[164:167], a[228:231], v[196:199]\n"
        "ds_read_b128 a[248:251], v241 offset:128\n"
        "v_add_u32 v246, s45, v246\n"
        "v_mfma_f32_16x16x32_bf16 v[216:219], v[160:163], a[228:231], v[216:219]\n"
        "ds_read_b128 a[228:231], v254 offset:49232\n"
        "v_add_u32 v247, s45, v247\n"
        "v_mfma_f32_16x16x32_bf16 v[216:219], v[152:155], a[216:219], v[216:219]\n"
        "s_waitcnt lgkmcnt(7)\n"
        "ds_read_b128 a[216:219], v240 offset:256\n"
        "v_mfma_f32_16x16x32_bf16 v[220:223], v[152:155], a[224:227], v[220:223]\n"
        "s_waitcnt lgkmcnt(7)\n"
        "v_mfma_f32_16x16x32_bf16 v[220:223], v[160:163], a[220:223], v[220:223]\n"
        "v_mfma_f32_16x16x32_bf16 v[200:203], v[164:167], a[220:223], v[200:203]\n"
        "ds_read_b128 a[220:223], v241 offset:256\n"
        "v_mfma_f32_16x16x32_bf16 v[200:203], v[156:159], a[224:227], v[200:203]\n"
        "s_waitcnt lgkmcnt(6)\n"
        "v_mfma_f32_16x16x32_bf16 v[204:207], v[156:159], a[232:235], v[204:207]\n"
        "s_waitcnt lgkmcnt(2)\n"
        "v_mfma_f32_16x16x32_bf16 v[204:207], v[164:167], a[228:231], v[204:207]\n"
        "v_mfma_f32_16x16x32_bf16 v[224:227], v[160:163], a[228:231], v[224:227]\n"
        "v_mfma_f32_16x16x32_bf16 v[224:227], v[152:155], a[232:235], v[224:227]\n"
        "s_waitcnt lgkmcnt(0)\n"
        ".p2align 6\n"
        "10:\n"
        "v_mfma_f32_16x16x32_bf16 v[162:165], a[72:75], a[236:239], 0\n"
        "v_fma_f32 v76, v76, s24, v236\n"
        "v_fma_f32 v77, v77, s24, v236\n"
        "v_mfma_f32_16x16x32_bf16 v[162:165], a[76:79], a[240:243], v[162:165]\n"
        "s_waitcnt lgkmcnt(3)\n"
        "s_add_u32 s41, s30, s40\n"
        "v_cvt_pk_bf16_f32 v232, v172, v176\n"
        "v_cvt_pk_bf16_f32 v233, v212, v216\n"
        "v_mfma_f32_16x16x32_bf16 v[162:165], a[80:83], a[244:247], v[162:165]\n"
        "s_waitcnt lgkmcnt(2)\n"
        "v_cvt_pk_bf16_f32 v234, v220, v224\n"
        "v_fma_f32 v78, v78, s24, v236\n"
        "v_mfma_f32_16x16x32_bf16 v[162:165], a[84:87], a[248:251], v[162:165]\n"
        "s_waitcnt lgkmcnt(1)\n"
        "buffer_store_dwordx3 v[232:234], v250, s[16:19], s38 offen offset:0 sc1\n"
        "v_mfma_f32_16x16x32_bf16 v[162:165], a[88:91], a[216:219], v[162:165]\n"
        "s_waitcnt lgkmcnt(0)\n"
        "v_exp_f32 v76, v76\n"
        "v_mfma_f32_16x16x32_bf16 v[162:165], a[92:95], a[220:223], v[162:165]\n"
        "v_exp_f32 v77, v77\n"
        "v_mfma_f32_16x16x32_bf16 v[72:75], a[116:119], a[220:223], 0\n"
        "v_fma_f32 v79, v79, s24, v236\n"
        "v_cvt_pk_bf16_f32 v70, v76, v77\n"
        "v_mfma_f32_16x16x32_bf16 v[72:75], a[112:115], a[216:219], v[72:75]\n"
        "v_exp_f32 v78, v78\n"
        "v_mfma_f32_16x16x32_bf16 v[72:75], a[108:111], a[248:251], v[72:75]\n"
        "v_exp_f32 v79, v79\n"
        "v_mfma_f32_16x16x32_bf16 v[72:75], a[104:107], a[244:247], v[72:75]\n"
        "v_cvt_pk_bf16_f32 v71, v78, v79\n"
        "v_cvt_pk_bf16_f32 v232, v173, v177\n"
        "v_mfma_f32_16x16x32_bf16 v[72:75], a[100:103], a[240:243], v[72:75]\n"
        "v_cvt_pk_bf16_f32 v233, v213, v217\n"
        "v_cvt_pk_bf16_f32 v234, v221, v225\n"
        "v_mfma_f32_16x16x32_bf16 v[72:75], a[96:99], a[236:239], v[72:75]\n"
        "buffer_store_dwordx3 v[232:234], v250, s[16:19], s38 offen offset:384 sc1\n"
        "v_mfma_f32_16x16x32_bf16 v[76:79], a[120:123], a[236:239], 0\n"
        "s_add_u32 s35, s47, 786432\n"
        "v_cvt_pk_bf16_f32 v152, v162, v163\n"
        "v_cvt_pk_bf16_f32 v153, v164, v165\n"
        "v_mfma_f32_16x16x32_bf16 v[76:79], a[124:127], a[240:243], v[76:79]\n"
        "s_sub_u32 s46, s35, 100663296\n"
        "v_fma_f32 v80, v80, s24, v237\n"
        "v_cvt_pk_bf16_f32 v154, v72, v73\n"
        "v_mfma_f32_16x16x32_bf16 v[76:79], a[128:131], a[244:247], v[76:79]\n"
        "s_cmp_lt_u32 s46, 786432\n"
        "v_exp_f32 v80, v80\n"
        "v_mfma_f32_16x16x32_bf16 v[76:79], a[132:135], a[248:251], v[76:79]\n"
        "s_cselect_b32 s35, s46, s35\n"
        "v_cvt_pk_bf16_f32 v155, v74, v75\n"
        "v_cvt_pk_bf16_f32 v232, v174, v178\n"
        "v_mfma_f32_16x16x32_bf16 v[76:79], a[136:139], a[216:219], v[76:79]\n"
        "v_cvt_pk_bf16_f32 v233, v214, v218\n"
        "v_cvt_pk_bf16_f32 v234, v222, v226\n"
        "v_mfma_f32_16x16x32_bf16 v[76:79], a[140:143], a[220:223], v[76:79]\n"
        "v_fma_f32 v81, v81, s24, v237\n"
        "v_fma_f32 v82, v82, s24, v237\n"
        "v_mfma_f32_16x16x32_bf16 v[162:165], a[20:23], a[220:223], 0\n"
        "v_exp_f32 v81, v81\n"
        "v_mfma_f32_16x16x32_bf16 v[162:165], a[16:19], a[216:219], v[162:165]\n"
        "v_exp_f32 v82, v82\n"
        "v_mfma_f32_16x16x32_bf16 v[162:165], a[12:15], a[248:251], v[162:165]\n"
        "buffer_store_dwordx3 v[232:234], v250, s[16:19], s38 offen offset:768 sc1\n"
        "v_mfma_f32_16x16x32_bf16 v[162:165], a[8:11], a[244:247], v[162:165]\n"
        "v_cvt_pk_bf16_f32 v168, v76, v77\n"
        "v_cvt_pk_bf16_f32 v169, v78, v79\n"
        "v_mfma_f32_16x16x32_bf16 v[162:165], a[4:7], a[240:243], v[162:165]\n"
        "v_cvt_pk_bf16_f32 v80, v80, v81\n"
        "v_fma_f32 v83, v83, s24, v237\n"
        "v_mfma_f32_16x16x32_bf16 v[162:165], a[0:3], a[236:239], v[162:165]\n"
        "v_exp_f32 v83, v83\n"
        "v_mfma_f32_16x16x32_bf16 v[72:75], a[24:27], a[236:239], 0\n"
        "v_cvt_pk_bf16_f32 v232, v175, v179\n"
        "v_cvt_pk_bf16_f32 v233, v215, v219\n"
        "v_mfma_f32_16x16x32_bf16 v[72:75], a[28:31], a[240:243], v[72:75]\n"
        "v_cvt_pk_bf16_f32 v234, v223, v227\n"
        "v_cvt_pk_bf16_f32 v81, v82, v83\n"
        "v_mfma_f32_16x16x32_bf16 v[72:75], a[32:35], a[244:247], v[72:75]\n"
        "v_cvt_pk_bf16_f32 v156, v162, v163\n"
        "v_cvt_pk_bf16_f32 v157, v164, v165\n"
        "v_mfma_f32_16x16x32_bf16 v[72:75], a[36:39], a[248:251], v[72:75]\n"
        "v_fma_f32 v84, v84, s24, v237\n"
        "v_fma_f32 v85, v85, s24, v237\n"
        "v_mfma_f32_16x16x32_bf16 v[72:75], a[40:43], a[216:219], v[72:75]\n"
        "v_exp_f32 v84, v84\n"
        "v_mfma_f32_16x16x32_bf16 v[72:75], a[44:47], a[220:223], v[72:75]\n"
        "buffer_store_dwordx3 v[232:234], v250, s[16:19], s38 offen offset:1152 sc1\n"
        "v_mfma_f32_16x16x32_bf16 v[76:79], a[68:71], a[220:223], 0\n"
        "v_exp_f32 v85, v85\n"
        "v_mfma_f32_16x16x32_bf16 v[76:79], a[64:67], a[216:219], v[76:79]\n"
        "v_cvt_pk_bf16_f32 v82, v84, v85\n"
        "v_fma_f32 v86, v86, s24, v237\n"
        "v_mfma_f32_16x16x32_bf16 v[76:79], a[60:63], a[248:251], v[76:79]\n"
        "ds_read_b128 a[224:227], v240 offset:1536\n"
        "v_cvt_pk_bf16_f32 v158, v72, v73\n"
        "v_mfma_f32_16x16x32_bf16 v[76:79], a[56:59], a[244:247], v[76:79]\n"
        "ds_read_b128 a[228:231], v241 offset:1536\n"
        "v_cvt_pk_bf16_f32 v159, v74, v75\n"
        "v_mfma_f32_16x16x32_bf16 v[76:79], a[52:55], a[240:243], v[76:79]\n"
        "ds_read_b128 a[232:235], v240 offset:1664\n"
        "v_cvt_pk_bf16_f32 v232, v180, v184\n"
        "v_mfma_f32_16x16x32_bf16 v[76:79], a[48:51], a[236:239], v[76:79]\n"
        "v_cvt_pk_bf16_f32 v233, v192, v196\n"
        "v_cvt_pk_bf16_f32 v234, v200, v204\n"
        "v_mfma_f32_16x16x32_bf16 v[162:165], a[236:239], a[144:147], 0\n"
        "v_exp_f32 v86, v86\n"
        "v_mfma_f32_16x16x32_bf16 v[162:165], a[240:243], a[148:151], v[162:165]\n"
        "v_fma_f32 v87, v87, s24, v237\n"
        "v_fma_f32 v88, v88, s24, v237\n"
        "v_mfma_f32_16x16x32_bf16 v[162:165], a[244:247], a[152:155], v[162:165]\n"
        "buffer_store_dwordx3 v[232:234], v250, s[16:19], s39 offen offset:0 sc1\n"
        "v_mfma_f32_16x16x32_bf16 v[162:165], a[248:251], a[156:159], v[162:165]\n"
        "v_exp_f32 v87, v87\n"
        "v_mfma_f32_16x16x32_bf16 v[162:165], a[216:219], a[160:163], v[162:165]\n"
        "v_cvt_pk_bf16_f32 v160, v76, v77\n"
        "v_cvt_pk_bf16_f32 v161, v78, v79\n"
        "v_mfma_f32_16x16x32_bf16 v[162:165], a[220:223], a[164:167], v[162:165]\n"
        "v_cvt_pk_bf16_f32 v166, v76, v77\n"
        "v_cvt_pk_bf16_f32 v167, v78, v79\n"
        "v_mfma_f32_16x16x32_bf16 v[72:75], a[220:223], a[188:191], 0\n"
        "v_exp_f32 v88, v88\n"
        "v_mfma_f32_16x16x32_bf16 v[72:75], a[216:219], a[184:187], v[72:75]\n"
        "v_cvt_pk_bf16_f32 v232, v181, v185\n"
        "v_cvt_pk_bf16_f32 v233, v193, v197\n"
        "v_mfma_f32_16x16x32_bf16 v[72:75], a[248:251], a[180:183], v[72:75]\n"
        "v_cvt_pk_bf16_f32 v234, v201, v205\n"
        "v_cvt_pk_bf16_f32 v212, v162, v163\n"
        "v_mfma_f32_16x16x32_bf16 v[72:75], a[244:247], a[176:179], v[72:75]\n"
        "v_cvt_pk_bf16_f32 v213, v164, v165\n"
        "v_cvt_pk_bf16_f32 v83, v86, v87\n"
        "v_mfma_f32_16x16x32_bf16 v[72:75], a[240:243], a[172:175], v[72:75]\n"
        "v_fma_f32 v89, v89, s24, v237\n"
        "v_fma_f32 v90, v90, s24, v237\n"
        "v_mfma_f32_16x16x32_bf16 v[72:75], a[236:239], a[168:171], v[72:75]\n"
        "buffer_store_dwordx3 v[232:234], v250, s[16:19], s39 offen offset:384 sc1\n"
        "v_mfma_f32_16x16x32_bf16 v[76:79], a[236:239], a[192:195], 0\n"
        "ds_read_b128 a[236:239], v241 offset:1664\n"
        "v_fma_f32 v91, v91, s24, v237\n"
        "v_mfma_f32_16x16x32_bf16 v[76:79], a[240:243], a[196:199], v[76:79]\n"
        "ds_read_b128 a[240:243], v240 offset:1792\n"
        "v_fma_f32 v92, v92, s24, v237\n"
        "v_mfma_f32_16x16x32_bf16 v[76:79], a[244:247], a[200:203], v[76:79]\n"
        "ds_read_b128 a[244:247], v241 offset:1792\n"
        "v_cvt_pk_bf16_f32 v214, v72, v73\n"
        "v_mfma_f32_16x16x32_bf16 v[76:79], a[248:251], a[204:207], v[76:79]\n"
        "v_exp_f32 v89, v89\n"
        "v_mfma_f32_16x16x32_bf16 v[76:79], a[216:219], a[208:211], v[76:79]\n"
        "v_cvt_pk_bf16_f32 v215, v74, v75\n"
        "v_cvt_pk_bf16_f32 v232, v182, v186\n"
        "v_mfma_f32_16x16x32_bf16 v[76:79], a[220:223], a[212:215], v[76:79]\n"
        "s_waitcnt lgkmcnt(5)\n"
        "v_cvt_pk_bf16_f32 v233, v194, v198\n"
        "v_cvt_pk_bf16_f32 v234, v202, v206\n"
        "v_mfma_f32_16x16x32_bf16 v[162:165], a[72:75], a[224:227], 0\n"
        "s_waitcnt lgkmcnt(4)\n"
        "v_exp_f32 v90, v90\n"
        "v_mfma_f32_16x16x32_bf16 v[162:165], a[76:79], a[228:231], v[162:165]\n"
        "s_waitcnt lgkmcnt(3)\n"
        "v_exp_f32 v91, v91\n"
        "v_mfma_f32_16x16x32_bf16 v[162:165], a[80:83], a[232:235], v[162:165]\n"
        "s_waitcnt lgkmcnt(2)\n"
        "buffer_store_dwordx3 v[232:234], v250, s[16:19], s39 offen offset:768 sc1\n"
        "v_mfma_f32_16x16x32_bf16 v[162:165], a[84:87], a[236:239], v[162:165]\n"
        "s_waitcnt lgkmcnt(1)\n"
        "v_exp_f32 v92, v92\n"
        "v_mfma_f32_16x16x32_bf16 v[162:165], a[88:91], a[240:243], v[162:165]\n"
        "s_waitcnt lgkmcnt(0)\n"
        "v_cvt_pk_bf16_f32 v216, v76, v77\n"
        "v_cvt_pk_bf16_f32 v217, v78, v79\n"
        "v_mfma_f32_16x16x32_bf16 v[162:165], a[92:95], a[244:247], v[162:165]\n"
        "v_cvt_pk_bf16_f32 v232, v183, v187\n"
        "v_cvt_pk_bf16_f32 v233, v195, v199\n"
        "v_mfma_f32_16x16x32_bf16 v[72:75], a[116:119], a[244:247], 0\n"
        "v_cvt_pk_bf16_f32 v234, v203, v207\n"
        "v_cvt_pk_bf16_f32 v84, v88, v89\n"
        "v_mfma_f32_16x16x32_bf16 v[72:75], a[112:115], a[240:243], v[72:75]\n"
        "v_cvt_pk_bf16_f32 v85, v90, v91\n"
        "v_fma_f32 v93, v93, s24, v237\n"
        "v_mfma_f32_16x16x32_bf16 v[72:75], a[108:111], a[236:239], v[72:75]\n"
        "v_exp_f32 v93, v93\n"
        "v_mfma_f32_16x16x32_bf16 v[72:75], a[104:107], a[232:235], v[72:75]\n"
        "v_cvt_pk_bf16_f32 v172, v162, v163\n"
        "v_cvt_pk_bf16_f32 v173, v164, v165\n"
        "v_mfma_f32_16x16x32_bf16 v[72:75], a[100:103], a[228:231], v[72:75]\n"
        "v_cvt_pk_bf16_f32 v86, v92, v93\n"
        "v_fma_f32 v94, v94, s24, v237\n"
        "v_mfma_f32_16x16x32_bf16 v[72:75], a[96:99], a[224:227], v[72:75]\n"
        "buffer_store_dwordx3 v[232:234], v250, s[16:19], s39 offen offset:1152 sc1\n"
        "v_mfma_f32_16x16x32_bf16 v[76:79], a[120:123], a[224:227], 0\n"
        "v_exp_f32 v94, v94\n"
        "v_mfma_f32_16x16x32_bf16 v[76:79], a[124:127], a[228:231], v[76:79]\n"
        "v_fma_f32 v95, v95, s24, v237\n"
        "v_fma_f32 v96, v96, s24, v238\n"
        "v_mfma_f32_16x16x32_bf16 v[76:79], a[128:131], a[232:235], v[76:79]\n"
        "v_exp_f32 v95, v95\n"
        "v_mfma_f32_16x16x32_bf16 v[76:79], a[132:135], a[236:239], v[76:79]\n"
        "v_exp_f32 v96, v96\n"
        "v_mfma_f32_16x16x32_bf16 v[76:79], a[136:139], a[240:243], v[76:79]\n"
        "v_cvt_pk_bf16_f32 v174, v72, v73\n"
        "v_cvt_pk_bf16_f32 v175, v74, v75\n"
        "v_mfma_f32_16x16x32_bf16 v[76:79], a[140:143], a[244:247], v[76:79]\n"
        "v_cvt_pk_bf16_f32 v87, v94, v95\n"
        "v_fma_f32 v97, v97, s24, v238\n"
        "v_mfma_f32_16x16x32_bf16 v[162:165], a[20:23], a[244:247], 0\n"
        "s_mov_b32 m0, s48\n"
        "v_exp_f32 v97, v97\n"
        "v_mfma_f32_16x16x32_bf16 v[162:165], a[16:19], a[240:243], v[162:165]\n"
        "v_cvt_pk_bf16_f32 v96, v96, v97\n"
        "v_fma_f32 v98, v98, s24, v238\n"
        "v_mfma_f32_16x16x32_bf16 v[162:165], a[12:15], a[236:239], v[162:165]\n"
        "buffer_load_dwordx4 v249, s[12:15], s36 offen offset:0 lds\n"
        "v_mfma_f32_16x16x32_bf16 v[162:165], a[8:11], a[232:235], v[162:165]\n"
        "v_exp_f32 v98, v98\n"
        "v_mfma_f32_16x16x32_bf16 v[162:165], a[4:7], a[228:231], v[162:165]\n"
        "v_cvt_pk_bf16_f32 v190, v76, v77\n"
        "v_cvt_pk_bf16_f32 v191, v78, v79\n"
        "v_mfma_f32_16x16x32_bf16 v[162:165], a[0:3], a[224:227], v[162:165]\n"
        "v_fma_f32 v99, v99, s24, v238\n"
        "v_fma_f32 v100, v100, s24, v238\n"
        "v_mfma_f32_16x16x32_bf16 v[72:75], a[24:27], a[224:227], 0\n"
        "v_exp_f32 v99, v99\n"
        "v_mfma_f32_16x16x32_bf16 v[72:75], a[28:31], a[228:231], v[72:75]\n"
        "v_exp_f32 v100, v100\n"
        "v_mfma_f32_16x16x32_bf16 v[72:75], a[32:35], a[232:235], v[72:75]\n"
        "v_cvt_pk_bf16_f32 v97, v98, v99\n"
        "v_cvt_pk_bf16_f32 v176, v162, v163\n"
        "v_mfma_f32_16x16x32_bf16 v[72:75], a[36:39], a[236:239], v[72:75]\n"
        "v_cvt_pk_bf16_f32 v177, v164, v165\n"
        "v_fma_f32 v101, v101, s24, v238\n"
        "v_mfma_f32_16x16x32_bf16 v[72:75], a[40:43], a[240:243], v[72:75]\n"
        "v_exp_f32 v101, v101\n"
        "v_mfma_f32_16x16x32_bf16 v[72:75], a[44:47], a[244:247], v[72:75]\n"
        "v_cvt_pk_bf16_f32 v98, v100, v101\n"
        "v_fma_f32 v102, v102, s24, v238\n"
        "v_mfma_f32_16x16x32_bf16 v[76:79], a[68:71], a[244:247], 0\n"
        "v_xor_b32 v253, 32, v249\n"
        "v_fma_f32 v103, v103, s24, v238\n"
        "v_mfma_f32_16x16x32_bf16 v[76:79], a[64:67], a[240:243], v[76:79]\n"
        "v_exp_f32 v102, v102\n"
        "v_mfma_f32_16x16x32_bf16 v[76:79], a[60:63], a[236:239], v[76:79]\n"
        "ds_read_b128 a[248:251], v240 offset:12288\n"
        "v_cvt_pk_bf16_f32 v178, v72, v73\n"
        "v_mfma_f32_16x16x32_bf16 v[76:79], a[56:59], a[232:235], v[76:79]\n"
        "ds_read_b128 a[216:219], v241 offset:12288\n"
        "v_cvt_pk_bf16_f32 v179, v74, v75\n"
        "v_mfma_f32_16x16x32_bf16 v[76:79], a[52:55], a[228:231], v[76:79]\n"
        "buffer_load_dwordx4 v253, s[12:15], s36 offen offset:1024 lds\n"
        "v_mfma_f32_16x16x32_bf16 v[76:79], a[48:51], a[224:227], v[76:79]\n"
        "ds_read_b128 a[220:223], v240 offset:12416\n"
        "v_fma_f32 v104, v104, s24, v238\n"
        "v_mfma_f32_16x16x32_bf16 v[162:165], a[224:227], a[144:147], 0\n"
        "v_exp_f32 v103, v103\n"
        "v_mfma_f32_16x16x32_bf16 v[162:165], a[228:231], a[148:151], v[162:165]\n"
        "v_exp_f32 v104, v104\n"
        "v_mfma_f32_16x16x32_bf16 v[162:165], a[232:235], a[152:155], v[162:165]\n"
        "v_cvt_pk_bf16_f32 v99, v102, v103\n"
        "v_cvt_pk_bf16_f32 v180, v76, v77\n"
        "v_mfma_f32_16x16x32_bf16 v[162:165], a[236:239], a[156:159], v[162:165]\n"
        "v_cvt_pk_bf16_f32 v181, v78, v79\n"
        "v_cvt_pk_bf16_f32 v186, v76, v77\n"
        "v_mfma_f32_16x16x32_bf16 v[162:165], a[240:243], a[160:163], v[162:165]\n"
        "v_cvt_pk_bf16_f32 v187, v78, v79\n"
        "v_fma_f32 v105, v105, s24, v238\n"
        "v_mfma_f32_16x16x32_bf16 v[162:165], a[244:247], a[164:167], v[162:165]\n"
        "v_exp_f32 v105, v105\n"
        "v_mfma_f32_16x16x32_bf16 v[72:75], a[244:247], a[188:191], 0\n"
        "v_cvt_pk_bf16_f32 v100, v104, v105\n"
        "v_fma_f32 v106, v106, s24, v238\n"
        "v_mfma_f32_16x16x32_bf16 v[72:75], a[240:243], a[184:187], v[72:75]\n"
        "v_exp_f32 v106, v106\n"
        "v_mfma_f32_16x16x32_bf16 v[72:75], a[236:239], a[180:183], v[72:75]\n"
        "v_fma_f32 v107, v107, s24, v238\n"
        "v_cvt_pk_bf16_f32 v218, v162, v163\n"
        "v_mfma_f32_16x16x32_bf16 v[72:75], a[232:235], a[176:179], v[72:75]\n"
        "v_exp_f32 v107, v107\n"
        "v_mfma_f32_16x16x32_bf16 v[72:75], a[228:231], a[172:175], v[72:75]\n"
        "s_add_u32 m0, s41, 0\n"
        "v_cvt_pk_bf16_f32 v219, v164, v165\n"
        "v_cvt_pk_bf16_f32 v101, v106, v107\n"
        "v_mfma_f32_16x16x32_bf16 v[72:75], a[224:227], a[168:171], v[72:75]\n"
        "v_fma_f32 v108, v108, s24, v238\n"
        "v_fma_f32 v109, v109, s24, v238\n"
        "v_mfma_f32_16x16x32_bf16 v[76:79], a[224:227], a[192:195], 0\n"
        "ds_read_b128 a[224:227], v241 offset:12416\n"
        "v_fma_f32 v110, v110, s24, v238\n"
        "v_mfma_f32_16x16x32_bf16 v[76:79], a[228:231], a[196:199], v[76:79]\n"
        "ds_read_b128 a[228:231], v240 offset:12544\n"
        "v_fma_f32 v111, v111, s24, v238\n"
        "v_mfma_f32_16x16x32_bf16 v[76:79], a[232:235], a[200:203], v[76:79]\n"
        "buffer_load_dwordx4 v242, s[8:11], s47 offen lds\n"
        "s_sub_u32 s31, s30, s28\n"
        "v_mfma_f32_16x16x32_bf16 v[76:79], a[236:239], a[204:207], v[76:79]\n"
        "ds_read_b128 a[232:235], v241 offset:12544\n"
        "s_sub_u32 s45, s28, s29\n"
        "v_cvt_pk_bf16_f32 v220, v72, v73\n"
        "v_mfma_f32_16x16x32_bf16 v[76:79], a[240:243], a[208:211], v[76:79]\n"
        "s_add_u32 s28, s28, s31\n"
        "v_cvt_pk_bf16_f32 v221, v74, v75\n"
        "v_fma_f32 v112, v112, s24, v239\n"
        "v_mfma_f32_16x16x32_bf16 v[76:79], a[244:247], a[212:215], v[76:79]\n"
        "s_waitcnt lgkmcnt(5)\n"
        "v_exp_f32 v108, v108\n"
        "v_mfma_f32_16x16x32_bf16 v[162:165], a[72:75], a[248:251], 0\n"
        "s_waitcnt lgkmcnt(4)\n"
        "v_exp_f32 v109, v109\n"
        "v_mfma_f32_16x16x32_bf16 v[162:165], a[76:79], a[216:219], v[162:165]\n"
        "s_waitcnt lgkmcnt(3)\n"
        "v_exp_f32 v110, v110\n"
        "v_mfma_f32_16x16x32_bf16 v[162:165], a[80:83], a[220:223], v[162:165]\n"
        "s_waitcnt lgkmcnt(2)\n"
        "v_exp_f32 v111, v111\n"
        "v_mfma_f32_16x16x32_bf16 v[162:165], a[84:87], a[224:227], v[162:165]\n"
        "s_waitcnt lgkmcnt(1)\n"
        "v_exp_f32 v112, v112\n"
        "v_mfma_f32_16x16x32_bf16 v[162:165], a[88:91], a[228:231], v[162:165]\n"
        "s_waitcnt lgkmcnt(0)\n"
        "v_cvt_pk_bf16_f32 v222, v76, v77\n"
        "v_cvt_pk_bf16_f32 v223, v78, v79\n"
        "v_mfma_f32_16x16x32_bf16 v[162:165], a[92:95], a[232:235], v[162:165]\n"
        "s_mov_b32 s30, s29\n"
        "v_cvt_pk_bf16_f32 v102, v108, v109\n"
        "v_cvt_pk_bf16_f32 v103, v110, v111\n"
        "v_mfma_f32_16x16x32_bf16 v[72:75], a[116:119], a[232:235], 0\n"
        "s_add_u32 m0, s41, 1024\n"
        "v_fma_f32 v113, v113, s24, v239\n"
        "v_fma_f32 v114, v114, s24, v239\n"
        "v_mfma_f32_16x16x32_bf16 v[72:75], a[112:115], a[228:231], v[72:75]\n"
        "s_add_u32 s29, s29, s45\n"
        "v_exp_f32 v113, v113\n"
        "v_mfma_f32_16x16x32_bf16 v[72:75], a[108:111], a[224:227], v[72:75]\n"
        "buffer_load_dwordx4 v243, s[8:11], s47 offen lds\n"
        "v_mfma_f32_16x16x32_bf16 v[72:75], a[104:107], a[220:223], v[72:75]\n"
        "v_exp_f32 v114, v114\n"
        "v_mfma_f32_16x16x32_bf16 v[72:75], a[100:103], a[216:219], v[72:75]\n"
        "v_cvt_pk_bf16_f32 v192, v162, v163\n"
        "v_cvt_pk_bf16_f32 v193, v164, v165\n"
        "v_mfma_f32_16x16x32_bf16 v[72:75], a[96:99], a[248:251], v[72:75]\n"
        "v_cvt_pk_bf16_f32 v112, v112, v113\n"
        "v_fma_f32 v115, v115, s24, v239\n"
        "v_mfma_f32_16x16x32_bf16 v[76:79], a[120:123], a[248:251], 0\n"
        "v_exp_f32 v115, v115\n"
        "v_mfma_f32_16x16x32_bf16 v[76:79], a[124:127], a[216:219], v[76:79]\n"
        "v_cvt_pk_bf16_f32 v113, v114, v115\n"
        "v_fma_f32 v116, v116, s24, v239\n"
        "v_mfma_f32_16x16x32_bf16 v[76:79], a[128:131], a[220:223], v[76:79]\n"
        "v_exp_f32 v116, v116\n"
        "v_mfma_f32_16x16x32_bf16 v[76:79], a[132:135], a[224:227], v[76:79]\n"
        "v_cvt_pk_bf16_f32 v194, v72, v73\n"
        "v_cvt_pk_bf16_f32 v195, v74, v75\n"
        "v_mfma_f32_16x16x32_bf16 v[76:79], a[136:139], a[228:231], v[76:79]\n"
        "v_fma_f32 v117, v117, s24, v239\n"
        "v_fma_f32 v118, v118, s24, v239\n"
        "v_mfma_f32_16x16x32_bf16 v[76:79], a[140:143], a[232:235], v[76:79]\n"
        "v_exp_f32 v117, v117\n"
        "v_mfma_f32_16x16x32_bf16 v[162:165], a[20:23], a[232:235], 0\n"
        "v_exp_f32 v118, v118\n"
        "v_mfma_f32_16x16x32_bf16 v[162:165], a[16:19], a[228:231], v[162:165]\n"
        "v_cvt_pk_bf16_f32 v114, v116, v117\n"
        "v_fma_f32 v119, v119, s24, v239\n"
        "v_mfma_f32_16x16x32_bf16 v[162:165], a[12:15], a[224:227], v[162:165]\n"
        "s_add_u32 m0, s41, 2048\n"
        "v_exp_f32 v119, v119\n"
        "v_mfma_f32_16x16x32_bf16 v[162:165], a[8:11], a[220:223], v[162:165]\n"
        "v_cvt_pk_bf16_f32 v208, v76, v77\n"
        "v_cvt_pk_bf16_f32 v209, v78, v79\n"
        "v_mfma_f32_16x16x32_bf16 v[162:165], a[4:7], a[216:219], v[162:165]\n"
        "buffer_load_dwordx4 v244, s[8:11], s47 offen lds\n"
        "v_mfma_f32_16x16x32_bf16 v[162:165], a[0:3], a[248:251], v[162:165]\n"
        "v_cvt_pk_bf16_f32 v115, v118, v119\n"
        "v_fma_f32 v120, v120, s24, v239\n"
        "v_mfma_f32_16x16x32_bf16 v[72:75], a[24:27], a[248:251], 0\n"
        "v_exp_f32 v120, v120\n"
        "v_mfma_f32_16x16x32_bf16 v[72:75], a[28:31], a[216:219], v[72:75]\n"
        "v_fma_f32 v121, v121, s24, v239\n"
        "v_fma_f32 v122, v122, s24, v239\n"
        "v_mfma_f32_16x16x32_bf16 v[72:75], a[32:35], a[220:223], v[72:75]\n"
        "v_exp_f32 v121, v121\n"
        "v_mfma_f32_16x16x32_bf16 v[72:75], a[36:39], a[224:227], v[72:75]\n"
        "v_exp_f32 v122, v122\n"
        "v_mfma_f32_16x16x32_bf16 v[72:75], a[40:43], a[228:231], v[72:75]\n"
        "v_cvt_pk_bf16_f32 v196, v162, v163\n"
        "v_cvt_pk_bf16_f32 v197, v164, v165\n"
        "v_mfma_f32_16x16x32_bf16 v[72:75], a[44:47], a[232:235], v[72:75]\n"
        "v_cvt_pk_bf16_f32 v116, v120, v121\n"
        "v_fma_f32 v123, v123, s24, v239\n"
        "v_mfma_f32_16x16x32_bf16 v[76:79], a[68:71], a[232:235], 0\n"
        "v_exp_f32 v123, v123\n"
        "v_mfma_f32_16x16x32_bf16 v[76:79], a[64:67], a[228:231], v[76:79]\n"
        "v_cvt_pk_bf16_f32 v117, v122, v123\n"
        "v_fma_f32 v124, v124, s24, v239\n"
        "v_mfma_f32_16x16x32_bf16 v[76:79], a[60:63], a[224:227], v[76:79]\n"
        "v_exp_f32 v124, v124\n"
        "v_mfma_f32_16x16x32_bf16 v[76:79], a[56:59], a[220:223], v[76:79]\n"
        "v_cvt_pk_bf16_f32 v198, v72, v73\n"
        "v_cvt_pk_bf16_f32 v199, v74, v75\n"
        "v_mfma_f32_16x16x32_bf16 v[76:79], a[52:55], a[216:219], v[76:79]\n"
        "s_add_u32 m0, s41, 12288\n"
        "v_fma_f32 v125, v125, s24, v239\n"
        "v_fma_f32 v126, v126, s24, v239\n"
        "v_mfma_f32_16x16x32_bf16 v[76:79], a[48:51], a[248:251], v[76:79]\n"
        "v_exp_f32 v125, v125\n"
        "v_mfma_f32_16x16x32_bf16 v[182:185], a[252:255], v[64:67], 0\n"
        "buffer_load_dwordx4 v242, s[8:11], s35 offen lds\n"
        "v_mfma_f32_16x16x32_bf16 v[182:185], a[252:255], v[68:71], v[182:185]\n"
        "v_exp_f32 v126, v126\n"
        "v_mfma_f32_16x16x32_bf16 v[202:205], v[132:135], v[68:71], 0\n"
        "v_cvt_pk_bf16_f32 v118, v124, v125\n"
        "v_fma_f32 v127, v127, s24, v239\n"
        "v_mfma_f32_16x16x32_bf16 v[202:205], v[128:131], v[64:67], v[202:205]\n"
        "v_exp_f32 v127, v127\n"
        "v_mfma_f32_16x16x32_bf16 v[88:91], v[136:139], v[64:67], 0\n"
        "v_cvt_pk_bf16_f32 v200, v76, v77\n"
        "v_cvt_pk_bf16_f32 v201, v78, v79\n"
        "v_mfma_f32_16x16x32_bf16 v[88:91], v[140:143], v[68:71], v[88:91]\n"
        "v_cvt_pk_bf16_f32 v206, v76, v77\n"
        "v_cvt_pk_bf16_f32 v207, v78, v79\n"
        "v_mfma_f32_16x16x32_bf16 v[92:95], v[148:151], v[68:71], 0\n"
        "s_cmp_eq_u32 s37, 100663296\n"
        "v_rcp_f32 v182, v182\n"
        "v_mfma_f32_16x16x32_bf16 v[92:95], v[144:147], v[64:67], v[92:95]\n"
        "s_cselect_b32 s46, 0, s37\n"
        "v_cvt_pk_bf16_f32 v119, v126, v127\n"
        "v_mul_f32 v202, v182, v202\n"
        "v_mfma_f32_16x16x32_bf16 v[64:67], a[252:255], v[80:83], 0\n"
        "s_add_u32 s38, s46, s43\n"
        "v_mul_f32 v203, v182, v203\n"
        "v_mul_f32 v204, v182, v204\n"
        "v_mfma_f32_16x16x32_bf16 v[64:67], a[252:255], v[84:87], v[64:67]\n"
        "s_add_u32 s39, s46, s44\n"
        "v_mul_f32 v205, v182, v205\n"
        "v_cvt_pk_bf16_f32 v202, v202, v203\n"
        "v_mfma_f32_16x16x32_bf16 v[68:71], v[132:135], v[84:87], 0\n"
        "s_add_u32 s37, s37, 6291456\n"
        "v_cvt_pk_bf16_f32 v203, v204, v205\n"
        "v_mul_f32 v88, v182, v88\n"
        "v_mfma_f32_16x16x32_bf16 v[68:71], v[128:131], v[80:83], v[68:71]\n"
        "v_rcp_f32 v64, v64\n"
        "v_mfma_f32_16x16x32_bf16 v[72:75], v[136:139], v[80:83], 0\n"
        "s_add_u32 m0, s41, 13312\n"
        "v_mul_f32 v89, v182, v89\n"
        "v_mul_f32 v90, v182, v90\n"
        "v_mfma_f32_16x16x32_bf16 v[72:75], v[140:143], v[84:87], v[72:75]\n"
        "v_mul_f32 v91, v182, v91\n"
        "v_cvt_pk_bf16_f32 v204, v88, v89\n"
        "v_mfma_f32_16x16x32_bf16 v[162:165], v[148:151], v[84:87], 0\n"
        "buffer_load_dwordx4 v243, s[8:11], s35 offen lds\n"
        "v_mfma_f32_16x16x32_bf16 v[162:165], v[144:147], v[80:83], v[162:165]\n"
        "v_cvt_pk_bf16_f32 v205, v90, v91\n"
        "v_mul_f32 v92, v182, v92\n"
        "v_mfma_f32_16x16x32_bf16 v[80:83], a[252:255], v[96:99], 0\n"
        "ds_write_b128 v245, v[202:205] offset:0\n"
        "v_mfma_f32_16x16x32_bf16 v[80:83], a[252:255], v[100:103], v[80:83]\n"
        "v_mul_f32 v93, v182, v93\n"
        "v_mul_f32 v94, v182, v94\n"
        "v_mfma_f32_16x16x32_bf16 v[84:87], v[132:135], v[100:103], 0\n"
        "v_mul_f32 v95, v182, v95\n"
        "v_cvt_pk_bf16_f32 v92, v92, v93\n"
        "v_mfma_f32_16x16x32_bf16 v[84:87], v[128:131], v[96:99], v[84:87]\n"
        "v_cvt_pk_bf16_f32 v93, v94, v95\n"
        "v_mul_f32 v68, v64, v68\n"
        "v_mfma_f32_16x16x32_bf16 v[76:79], v[136:139], v[96:99], 0\n"
        "v_rcp_f32 v80, v80\n"
        "v_mfma_f32_16x16x32_bf16 v[76:79], v[140:143], v[100:103], v[76:79]\n"
        "ds_write_b64 v246, v[92:93] offset:0\n"
        "v_mfma_f32_16x16x32_bf16 v[104:107], v[148:151], v[100:103], 0\n"
        "v_mul_f32 v69, v64, v69\n"
        "v_mul_f32 v70, v64, v70\n"
        "v_mfma_f32_16x16x32_bf16 v[104:107], v[144:147], v[96:99], v[104:107]\n"
        "v_mul_f32 v71, v64, v71\n"
        "v_cvt_pk_bf16_f32 v68, v68, v69\n"
        "v_mfma_f32_16x16x32_bf16 v[96:99], a[252:255], v[112:115], 0\n"
        "v_cvt_pk_bf16_f32 v69, v70, v71\n"
        "v_mul_f32 v72, v64, v72\n"
        "v_mfma_f32_16x16x32_bf16 v[96:99], a[252:255], v[116:119], v[96:99]\n"
        "v_mul_f32 v73, v64, v73\n"
        "v_mul_f32 v74, v64, v74\n"
        "v_mfma_f32_16x16x32_bf16 v[108:111], v[132:135], v[116:119], 0\n"
        "s_add_u32 m0, s41, 14336\n"
        "v_mul_f32 v75, v64, v75\n"
        "v_cvt_pk_bf16_f32 v70, v72, v73\n"
        "v_mfma_f32_16x16x32_bf16 v[108:111], v[128:131], v[112:115], v[108:111]\n"
        "v_mov_b32 v128, v212\n"
        "v_mov_b32 v129, v213\n"
        "v_mfma_f32_16x16x32_bf16 v[120:123], v[136:139], v[112:115], 0\n"
        "buffer_load_dwordx4 v244, s[8:11], s35 offen lds\n"
        "v_mfma_f32_16x16x32_bf16 v[120:123], v[140:143], v[116:119], v[120:123]\n"
        "ds_read_b128 a[236:239], v240 offset:13824\n"
        "v_mov_b32 v136, v214\n"
        "v_mfma_f32_16x16x32_bf16 v[124:127], v[148:151], v[116:119], 0\n"
        "ds_read_b128 a[240:243], v241 offset:13824\n"
        "v_mov_b32 v137, v215\n"
        "v_mfma_f32_16x16x32_bf16 v[124:127], v[144:147], v[112:115], v[124:127]\n"
        "ds_read_b128 a[244:247], v240 offset:13952\n"
        "v_mov_b32 v144, v216\n"
        "v_mfma_f32_16x16x32_bf16 v[100:103], a[248:251], a[144:147], 0\n"
        "v_mov_b32 v145, v217\n"
        "v_cvt_pk_bf16_f32 v71, v74, v75\n"
        "v_mfma_f32_16x16x32_bf16 v[100:103], a[216:219], a[148:151], v[100:103]\n"
        "ds_write_b128 v245, v[68:71] offset:256\n"
        "v_mfma_f32_16x16x32_bf16 v[100:103], a[220:223], a[152:155], v[100:103]\n"
        "v_mov_b32 v130, v218\n"
        "v_mov_b32 v131, v219\n"
        "v_mfma_f32_16x16x32_bf16 v[100:103], a[224:227], a[156:159], v[100:103]\n"
        "v_mov_b32 v138, v220\n"
        "v_mov_b32 v139, v221\n"
        "v_mfma_f32_16x16x32_bf16 v[100:103], a[228:231], a[160:163], v[100:103]\n"
        "v_mul_f32 v162, v64, v162\n"
        "v_mul_f32 v163, v64, v163\n"
        "v_mfma_f32_16x16x32_bf16 v[100:103], a[232:235], a[164:167], v[100:103]\n"
        "v_mul_f32 v164, v64, v164\n"
        "v_mul_f32 v165, v64, v165\n"
        "v_mfma_f32_16x16x32_bf16 v[112:115], a[232:235], a[188:191], 0\n"
        "v_mov_b32 v146, v222\n"
        "v_mov_b32 v147, v223\n"
        "v_mfma_f32_16x16x32_bf16 v[112:115], a[228:231], a[184:187], v[112:115]\n"
        "v_cvt_pk_bf16_f32 v162, v162, v163\n"
        "v_cvt_pk_bf16_f32 v163, v164, v165\n"
        "v_mfma_f32_16x16x32_bf16 v[112:115], a[224:227], a[180:183], v[112:115]\n"
        "ds_write_b64 v246, v[162:163] offset:256\n"
        "v_mfma_f32_16x16x32_bf16 v[112:115], a[220:223], a[176:179], v[112:115]\n"
        "v_cvt_pk_bf16_f32 v132, v100, v101\n"
        "v_cvt_pk_bf16_f32 v133, v102, v103\n"
        "v_mfma_f32_16x16x32_bf16 v[112:115], a[216:219], a[172:175], v[112:115]\n"
        "v_mul_f32 v84, v80, v84\n"
        "v_mul_f32 v85, v80, v85\n"
        "v_mfma_f32_16x16x32_bf16 v[112:115], a[248:251], a[168:171], v[112:115]\n"
        "v_rcp_f32 v96, v96\n"
        "v_mfma_f32_16x16x32_bf16 v[116:119], a[248:251], a[192:195], 0\n"
        "ds_read_b128 a[248:251], v241 offset:13952\n"
        "v_mul_f32 v86, v80, v86\n"
        "v_mfma_f32_16x16x32_bf16 v[116:119], a[216:219], a[196:199], v[116:119]\n"
        "ds_read_b128 a[216:219], v240 offset:14080\n"
        "v_mul_f32 v87, v80, v87\n"
        "v_mfma_f32_16x16x32_bf16 v[116:119], a[220:223], a[200:203], v[116:119]\n"
        "ds_read_b128 a[220:223], v241 offset:14080\n"
        "v_cvt_pk_bf16_f32 v140, v112, v113\n"
        "v_mfma_f32_16x16x32_bf16 v[116:119], a[224:227], a[204:207], v[116:119]\n"
        "v_cvt_pk_bf16_f32 v141, v114, v115\n"
        "v_cvt_pk_bf16_f32 v84, v84, v85\n"
        "v_mfma_f32_16x16x32_bf16 v[116:119], a[228:231], a[208:211], v[116:119]\n"
        "v_cvt_pk_bf16_f32 v85, v86, v87\n"
        "v_mul_f32 v76, v80, v76\n"
        "v_mfma_f32_16x16x32_bf16 v[116:119], a[232:235], a[212:215], v[116:119]\n"
        "s_waitcnt lgkmcnt(7)\n"
        "v_mul_f32 v77, v80, v77\n"
        "v_mul_f32 v78, v80, v78\n"
        "v_mfma_f32_16x16x32_bf16 v[100:103], a[236:239], a[144:147], 0\n"
        "s_waitcnt lgkmcnt(6)\n"
        "v_mul_f32 v79, v80, v79\n"
        "v_cvt_pk_bf16_f32 v86, v76, v77\n"
        "v_mfma_f32_16x16x32_bf16 v[100:103], a[240:243], a[148:151], v[100:103]\n"
        "s_waitcnt lgkmcnt(5)\n"
        "v_cvt_pk_bf16_f32 v87, v78, v79\n"
        "v_mul_f32 v104, v80, v104\n"
        "v_mfma_f32_16x16x32_bf16 v[100:103], a[244:247], a[152:155], v[100:103]\n"
        "s_waitcnt lgkmcnt(2)\n"
        "ds_write_b128 v245, v[84:87] offset:512\n"
        "v_mfma_f32_16x16x32_bf16 v[100:103], a[248:251], a[156:159], v[100:103]\n"
        "s_waitcnt lgkmcnt(2)\n"
        "v_cvt_pk_bf16_f32 v148, v116, v117\n"
        "v_cvt_pk_bf16_f32 v149, v118, v119\n"
        "v_mfma_f32_16x16x32_bf16 v[100:103], a[216:219], a[160:163], v[100:103]\n"
        "s_waitcnt lgkmcnt(1)\n"
        "v_mul_f32 v105, v80, v105\n"
        "v_mul_f32 v106, v80, v106\n"
        "v_mfma_f32_16x16x32_bf16 v[100:103], a[220:223], a[164:167], v[100:103]\n"
        "v_mul_f32 v107, v80, v107\n"
        "v_cvt_pk_bf16_f32 v104, v104, v105\n"
        "v_mfma_f32_16x16x32_bf16 v[112:115], a[220:223], a[188:191], 0\n"
        "v_cvt_pk_bf16_f32 v105, v106, v107\n"
        "v_mul_f32 v108, v96, v108\n"
        "v_mfma_f32_16x16x32_bf16 v[112:115], a[216:219], a[184:187], v[112:115]\n"
        "ds_write_b64 v246, v[104:105] offset:512\n"
        "v_mfma_f32_16x16x32_bf16 v[112:115], a[248:251], a[180:183], v[112:115]\n"
        "v_cvt_pk_bf16_f32 v134, v100, v101\n"
        "v_cvt_pk_bf16_f32 v135, v102, v103\n"
        "v_mfma_f32_16x16x32_bf16 v[112:115], a[244:247], a[176:179], v[112:115]\n"
        "v_mul_f32 v109, v96, v109\n"
        "v_mul_f32 v110, v96, v110\n"
        "v_mfma_f32_16x16x32_bf16 v[112:115], a[240:243], a[172:175], v[112:115]\n"
        "v_mul_f32 v111, v96, v111\n"
        "v_cvt_pk_bf16_f32 v108, v108, v109\n"
        "v_mfma_f32_16x16x32_bf16 v[112:115], a[236:239], a[168:171], v[112:115]\n"
        "v_cvt_pk_bf16_f32 v109, v110, v111\n"
        "v_mul_f32 v120, v96, v120\n"
        "v_mfma_f32_16x16x32_bf16 v[116:119], a[236:239], a[192:195], 0\n"
        "v_mul_f32 v121, v96, v121\n"
        "v_mul_f32 v122, v96, v122\n"
        "v_mfma_f32_16x16x32_bf16 v[116:119], a[240:243], a[196:199], v[116:119]\n"
        "v_mul_f32 v123, v96, v123\n"
        "v_cvt_pk_bf16_f32 v110, v120, v121\n"
        "v_mfma_f32_16x16x32_bf16 v[116:119], a[244:247], a[200:203], v[116:119]\n"
        "v_cvt_pk_bf16_f32 v142, v112, v113\n"
        "v_cvt_pk_bf16_f32 v143, v114, v115\n"
        "v_mfma_f32_16x16x32_bf16 v[116:119], a[248:251], a[204:207], v[116:119]\n"
        "v_cvt_pk_bf16_f32 v111, v122, v123\n"
        "v_mul_f32 v124, v96, v124\n"
        "v_mfma_f32_16x16x32_bf16 v[116:119], a[216:219], a[208:211], v[116:119]\n"
        "ds_write_b128 v245, v[108:111] offset:768\n"
        "v_mfma_f32_16x16x32_bf16 v[116:119], a[220:223], a[212:215], v[116:119]\n"
        "v_mul_f32 v125, v96, v125\n"
        "v_mul_f32 v126, v96, v126\n"
        "v_mfma_f32_16x16x32_bf16 v[100:103], a[20:23], a[220:223], 0\n"
        "v_mul_f32 v127, v96, v127\n"
        "v_cvt_pk_bf16_f32 v124, v124, v125\n"
        "v_mfma_f32_16x16x32_bf16 v[100:103], a[16:19], a[216:219], v[100:103]\n"
        "v_cvt_pk_bf16_f32 v125, v126, v127\n"
        "v_mfma_f32_16x16x32_bf16 v[100:103], a[12:15], a[248:251], v[100:103]\n"
        "v_cvt_pk_bf16_f32 v150, v116, v117\n"
        "v_cvt_pk_bf16_f32 v151, v118, v119\n"
        "v_mfma_f32_16x16x32_bf16 v[100:103], a[8:11], a[244:247], v[100:103]\n"
        "ds_write_b64 v246, v[124:125] offset:768\n"
        "v_mfma_f32_16x16x32_bf16 v[100:103], a[4:7], a[240:243], v[100:103]\n"
        "v_mfma_f32_16x16x32_bf16 v[100:103], a[0:3], a[236:239], v[100:103]\n"
        "v_mfma_f32_16x16x32_bf16 v[112:115], a[24:27], a[236:239], 0\n"
        "v_mfma_f32_16x16x32_bf16 v[112:115], a[28:31], a[240:243], v[112:115]\n"
        "v_mfma_f32_16x16x32_bf16 v[112:115], a[32:35], a[244:247], v[112:115]\n"
        "v_mfma_f32_16x16x32_bf16 v[112:115], a[36:39], a[248:251], v[112:115]\n"
        "v_mfma_f32_16x16x32_bf16 v[112:115], a[40:43], a[216:219], v[112:115]\n"
        "v_mfma_f32_16x16x32_bf16 v[112:115], a[44:47], a[220:223], v[112:115]\n"
        "v_mfma_f32_16x16x32_bf16 v[116:119], a[68:71], a[220:223], 0\n"
        "ds_read_b128 v[162:165], v251 offset:0\n"
        "v_cvt_pk_bf16_f32 v216, v100, v101\n"
        "v_mfma_f32_16x16x32_bf16 v[116:119], a[64:67], a[216:219], v[116:119]\n"
        "v_cvt_pk_bf16_f32 v217, v102, v103\n"
        "v_mfma_f32_16x16x32_bf16 v[116:119], a[60:63], a[248:251], v[116:119]\n"
        "v_mfma_f32_16x16x32_bf16 v[116:119], a[56:59], a[244:247], v[116:119]\n"
        "v_mfma_f32_16x16x32_bf16 v[116:119], a[52:55], a[240:243], v[116:119]\n"
        "ds_read_b128 v[182:185], v251 offset:512\n"
        "v_cvt_pk_bf16_f32 v218, v112, v113\n"
        "v_mfma_f32_16x16x32_bf16 v[116:119], a[48:51], a[236:239], v[116:119]\n"
        "v_cvt_pk_bf16_f32 v219, v114, v115\n"
        "v_mfma_f32_16x16x32_bf16 v[100:103], a[72:75], a[236:239], 0\n"
        "s_add_u32 s47, s47, 6291456\n"
        "v_mfma_f32_16x16x32_bf16 v[100:103], a[76:79], a[240:243], v[100:103]\n"
        "s_add_u32 s36, s36, 2097152\n"
        "v_mfma_f32_16x16x32_bf16 v[100:103], a[80:83], a[244:247], v[100:103]\n"
        "ds_read_b128 v[202:205], v251 offset:4096\n"
        "s_xor_b32 s48, s48, 8192\n"
        "v_cvt_pk_bf16_f32 v220, v116, v117\n"
        "v_mfma_f32_16x16x32_bf16 v[100:103], a[84:87], a[248:251], v[100:103]\n"
        "v_cvt_pk_bf16_f32 v221, v118, v119\n"
        "v_cvt_pk_bf16_f32 v226, v116, v117\n"
        "v_mfma_f32_16x16x32_bf16 v[100:103], a[88:91], a[216:219], v[100:103]\n"
        "v_cvt_pk_bf16_f32 v227, v118, v119\n"
        "v_mfma_f32_16x16x32_bf16 v[100:103], a[92:95], a[220:223], v[100:103]\n"
        "v_mfma_f32_16x16x32_bf16 v[112:115], a[116:119], a[220:223], 0\n"
        "ds_read_b128 v[222:225], v251 offset:4608\n"
        "v_mfma_f32_16x16x32_bf16 v[112:115], a[112:115], a[216:219], v[112:115]\n"
        "v_mfma_f32_16x16x32_bf16 v[112:115], a[108:111], a[248:251], v[112:115]\n"
        "s_waitcnt lgkmcnt(3)\n"
        "v_pk_min_u16 v162, v162, s26\n"
        "v_pk_min_u16 v163, v163, s26\n"
        "v_mfma_f32_16x16x32_bf16 v[112:115], a[104:107], a[244:247], v[112:115]\n"
        "v_pk_min_u16 v164, v164, s26\n"
        "v_cvt_pk_bf16_f32 v212, v100, v101\n"
        "v_mfma_f32_16x16x32_bf16 v[112:115], a[100:103], a[240:243], v[112:115]\n"
        "v_cvt_pk_bf16_f32 v213, v102, v103\n"
        "v_pk_min_u16 v165, v165, s26\n"
        "v_mfma_f32_16x16x32_bf16 v[112:115], a[96:99], a[236:239], v[112:115]\n"
        "v_add_u32 v240, s31, v240\n"
        "v_add_u32 v241, s31, v241\n"
        "v_mfma_f32_16x16x32_bf16 v[116:119], a[120:123], a[236:239], 0\n"
        "s_waitcnt lgkmcnt(2)\n"
        "v_pk_min_u16 v182, v182, s26\n"
        "v_pk_min_u16 v183, v183, s26\n"
        "v_mfma_f32_16x16x32_bf16 v[116:119], a[124:127], a[240:243], v[116:119]\n"
        "v_pk_min_u16 v184, v184, s26\n"
        "v_pk_min_u16 v185, v185, s26\n"
        "v_mfma_f32_16x16x32_bf16 v[116:119], a[128:131], a[244:247], v[116:119]\n"
        "v_cvt_pk_bf16_f32 v214, v112, v113\n"
        "v_cvt_pk_bf16_f32 v215, v114, v115\n"
        "v_mfma_f32_16x16x32_bf16 v[116:119], a[132:135], a[248:251], v[116:119]\n"
        "v_mfma_f32_16x16x32_bf16 v[116:119], a[136:139], a[216:219], v[116:119]\n"
        "s_waitcnt lgkmcnt(1)\n"
        "v_pk_min_u16 v202, v202, s26\n"
        "v_pk_min_u16 v203, v203, s26\n"
        "v_mfma_f32_16x16x32_bf16 v[116:119], a[140:143], a[220:223], v[116:119]\n"
        "v_pk_min_u16 v204, v204, s26\n"
        "v_pk_min_u16 v205, v205, s26\n"
        "v_mfma_f32_16x16x32_bf16 v[64:67], v[152:155], v[156:159], v[0:3]\n"
        "v_mfma_f32_16x16x32_bf16 v[64:67], v[168:171], v[160:163], v[64:67]\n"
        "v_mfma_f32_16x16x32_bf16 v[68:71], v[172:175], v[156:159], v[4:7]\n"
        "s_waitcnt lgkmcnt(0)\n"
        "v_pk_min_u16 v222, v222, s26\n"
        "v_pk_min_u16 v223, v223, s26\n"
        "v_mfma_f32_16x16x32_bf16 v[68:71], v[188:191], v[164:167], v[68:71]\n"
        "ds_read_b128 v[162:165], v252 offset:0\n"
        "v_cvt_pk_bf16_f32 v230, v116, v117\n"
        "v_mfma_f32_16x16x32_bf16 v[80:83], v[152:155], v[176:179], v[16:19]\n"
        "v_cvt_pk_bf16_f32 v231, v118, v119\n"
        "v_pk_min_u16 v224, v224, s26\n"
        "v_mfma_f32_16x16x32_bf16 v[80:83], v[168:171], v[180:183], v[80:83]\n"
        "v_pk_min_u16 v225, v225, s26\n"
        "v_max3_f32 v232, v64, v65, v66\n"
        "v_mfma_f32_16x16x32_bf16 v[84:87], v[172:175], v[176:179], v[20:23]\n"
        "v_max3_f32 v232, v232, v67, v68\n"
        "v_max3_f32 v232, v232, v69, v70\n"
        "v_mfma_f32_16x16x32_bf16 v[84:87], v[188:191], v[184:187], v[84:87]\n"
        "ds_read_b128 v[182:185], v252 offset:512\n"
        "v_mfma_f32_16x16x32_bf16 v[96:99], v[152:155], v[196:199], v[32:35]\n"
        "v_max3_f32 v233, v80, v81, v82\n"
        "v_mfma_f32_16x16x32_bf16 v[96:99], v[168:171], v[200:203], v[96:99]\n"
        "v_mfma_f32_16x16x32_bf16 v[100:103], v[172:175], v[196:199], v[36:39]\n"
        "v_mfma_f32_16x16x32_bf16 v[100:103], v[188:191], v[204:207], v[100:103]\n"
        "ds_read_b128 v[202:205], v252 offset:4096\n"
        "v_mfma_f32_16x16x32_bf16 v[112:115], v[152:155], v[216:219], v[48:51]\n"
        "s_waitcnt lgkmcnt(2)\n"
        "v_pk_min_u16 v162, v162, s26\n"
        "v_pk_min_u16 v163, v163, s26\n"
        "v_mfma_f32_16x16x32_bf16 v[112:115], v[168:171], v[220:223], v[112:115]\n"
        "v_pk_min_u16 v164, v164, s26\n"
        "v_pk_min_u16 v165, v165, s26\n"
        "v_mfma_f32_16x16x32_bf16 v[116:119], v[172:175], v[216:219], v[52:55]\n"
        "v_max3_f32 v233, v233, v83, v84\n"
        "v_max3_f32 v233, v233, v85, v86\n"
        "v_mfma_f32_16x16x32_bf16 v[116:119], v[188:191], v[224:227], v[116:119]\n"
        "ds_read_b128 v[222:225], v252 offset:4608\n"
        "v_max3_f32 v234, v96, v97, v98\n"
        "v_mfma_f32_16x16x32_bf16 v[72:75], v[192:195], v[156:159], v[8:11]\n"
        "s_waitcnt lgkmcnt(2)\n"
        "v_pk_min_u16 v182, v182, s26\n"
        "v_pk_min_u16 v183, v183, s26\n"
        "v_mfma_f32_16x16x32_bf16 v[72:75], v[208:211], v[160:163], v[72:75]\n"
        "v_pk_min_u16 v184, v184, s26\n"
        "v_pk_min_u16 v185, v185, s26\n"
        "v_mfma_f32_16x16x32_bf16 v[76:79], v[212:215], v[156:159], v[12:15]\n"
        "v_max3_f32 v234, v234, v99, v100\n"
        "v_max3_f32 v234, v234, v101, v102\n"
        "v_mfma_f32_16x16x32_bf16 v[76:79], v[228:231], v[164:167], v[76:79]\n"
        "v_max3_f32 v235, v112, v113, v114\n"
        "v_max3_f32 v235, v235, v115, v116\n"
        "v_mfma_f32_16x16x32_bf16 v[88:91], v[192:195], v[176:179], v[24:27]\n"
        "s_barrier\n"
        "ds_read_b128 v[152:155], v247 offset:0\n"
        "s_waitcnt lgkmcnt(2)\n"
        "v_pk_min_u16 v202, v202, s26\n"
        "v_mfma_f32_16x16x32_bf16 v[88:91], v[208:211], v[180:183], v[88:91]\n"
        "ds_read_b128 v[160:163], v247 offset:4096\n"
        "v_pk_min_u16 v203, v203, s26\n"
        "v_mfma_f32_16x16x32_bf16 v[92:95], v[212:215], v[176:179], v[28:31]\n"
        "ds_read_b128 v[156:159], v247 offset:256\n"
        "v_pk_min_u16 v204, v204, s26\n"
        "v_mfma_f32_16x16x32_bf16 v[92:95], v[228:231], v[184:187], v[92:95]\n"
        "ds_read_b128 v[164:167], v247 offset:4352\n"
        "v_pk_min_u16 v205, v205, s26\n"
        "v_mfma_f32_16x16x32_bf16 v[104:107], v[192:195], v[196:199], v[40:43]\n"
        "ds_read_b128 a[216:219], v248 offset:0\n"
        "s_waitcnt lgkmcnt(5)\n"
        "v_pk_min_u16 v222, v222, s26\n"
        "v_mfma_f32_16x16x32_bf16 v[104:107], v[208:211], v[200:203], v[104:107]\n"
        "ds_read_b128 a[220:223], v248 offset:6144\n"
        "v_pk_min_u16 v223, v223, s26\n"
        "v_mfma_f32_16x16x32_bf16 v[108:111], v[212:215], v[196:199], v[44:47]\n"
        "ds_read_b128 a[224:227], v254 offset:16\n"
        "v_pk_min_u16 v224, v224, s26\n"
        "v_mfma_f32_16x16x32_bf16 v[108:111], v[228:231], v[204:207], v[108:111]\n"
        "ds_read_b128 a[228:231], v254 offset:6160\n"
        "v_pk_min_u16 v225, v225, s26\n"
        "v_mfma_f32_16x16x32_bf16 v[120:123], v[192:195], v[216:219], v[56:59]\n"
        "ds_read_b128 a[232:235], v248 offset:32\n"
        "v_max3_f32 v232, v232, v71, v72\n"
        "v_mfma_f32_16x16x32_bf16 v[120:123], v[208:211], v[220:223], v[120:123]\n"
        "v_max3_f32 v232, v232, v73, v74\n"
        "v_max3_f32 v232, v232, v75, v76\n"
        "v_mfma_f32_16x16x32_bf16 v[124:127], v[212:215], v[216:219], v[60:63]\n"
        "v_max3_f32 v232, v232, v77, v78\n"
        "v_max_f32 v232, v232, v79\n"
        "v_mfma_f32_16x16x32_bf16 v[124:127], v[228:231], v[224:227], v[124:127]\n"
        "s_waitcnt lgkmcnt(4)\n"
        "v_max3_f32 v233, v233, v87, v88\n"
        "v_max3_f32 v233, v233, v89, v90\n"
        "v_mfma_f32_16x16x32_bf16 v[172:175], v[152:155], a[216:219], 0\n"
        "s_waitcnt lgkmcnt(3)\n"
        "v_max3_f32 v233, v233, v91, v92\n"
        "v_max3_f32 v233, v233, v93, v94\n"
        "v_mfma_f32_16x16x32_bf16 v[172:175], v[160:163], a[220:223], v[172:175]\n"
        "v_max_f32 v233, v233, v95\n"
        "v_max3_f32 v234, v234, v103, v104\n"
        "v_mfma_f32_16x16x32_bf16 v[180:183], v[164:167], a[220:223], 0\n"
        "ds_read_b128 a[220:223], v248 offset:6176\n"
        "v_max3_f32 v234, v234, v105, v106\n"
        "v_mfma_f32_16x16x32_bf16 v[180:183], v[156:159], a[216:219], v[180:183]\n"
        "s_waitcnt lgkmcnt(3)\n"
        "ds_read_b128 a[216:219], v254 offset:48\n"
        "v_max3_f32 v234, v234, v107, v108\n"
        "v_mfma_f32_16x16x32_bf16 v[184:187], v[156:159], a[224:227], 0\n"
        "s_waitcnt lgkmcnt(3)\n"
        "ds_read_b128 a[236:239], v247 offset:8192\n"
        "v_max3_f32 v234, v234, v109, v110\n"
        "v_mfma_f32_16x16x32_bf16 v[184:187], v[164:167], a[228:231], v[184:187]\n"
        "ds_read_b128 a[244:247], v247 offset:12288\n"
        "v_max_f32 v234, v234, v111\n"
        "v_mfma_f32_16x16x32_bf16 v[176:179], v[160:163], a[228:231], 0\n"
        "ds_read_b128 a[228:231], v254 offset:6192\n"
        "v_max3_f32 v235, v235, v117, v118\n"
        "v_mfma_f32_16x16x32_bf16 v[176:179], v[152:155], a[224:227], v[176:179]\n"
        "s_waitcnt lgkmcnt(5)\n"
        "ds_read_b128 a[224:227], v248 offset:64\n"
        "v_max3_f32 v235, v235, v119, v120\n"
        "v_mfma_f32_16x16x32_bf16 v[212:215], v[152:155], a[232:235], 0\n"
        "s_waitcnt lgkmcnt(5)\n"
        "ds_read_b128 a[240:243], v247 offset:8448\n"
        "v_max3_f32 v235, v235, v121, v122\n"
        "v_mfma_f32_16x16x32_bf16 v[212:215], v[160:163], a[220:223], v[212:215]\n"
        "ds_read_b128 a[248:251], v247 offset:12544\n"
        "v_max3_f32 v235, v235, v123, v124\n"
        "v_mfma_f32_16x16x32_bf16 v[192:195], v[164:167], a[220:223], 0\n"
        "ds_read_b128 a[220:223], v248 offset:6208\n"
        "v_max3_f32 v235, v235, v125, v126\n"
        "v_mfma_f32_16x16x32_bf16 v[192:195], v[156:159], a[232:235], v[192:195]\n"
        "s_waitcnt lgkmcnt(7)\n"
        "ds_read_b128 a[232:235], v254 offset:80\n"
        "v_max_f32 v235, v235, v127\n"
        "v_mfma_f32_16x16x32_bf16 v[196:199], v[156:159], a[216:219], 0\n"
        "s_waitcnt lgkmcnt(5)\n"
        "v_permlane32_swap_b32 v232, v233\n"
        "v_mfma_f32_16x16x32_bf16 v[196:199], v[164:167], a[228:231], v[196:199]\n"
        "v_max_f32 v232, v232, v233\n"
        "v_mov_b32 v233, v232\n"
        "v_mfma_f32_16x16x32_bf16 v[216:219], v[160:163], a[228:231], 0\n"
        "ds_read_b128 a[228:231], v254 offset:6224\n"
        "v_xor_b32 v251, 0x2000, v251\n"
        "v_mfma_f32_16x16x32_bf16 v[216:219], v[152:155], a[216:219], v[216:219]\n"
        "s_waitcnt lgkmcnt(5)\n"
        "ds_read_b128 a[216:219], v248 offset:36864\n"
        "v_xor_b32 v252, 0x2000, v252\n"
        "v_mfma_f32_16x16x32_bf16 v[220:223], v[152:155], a[224:227], 0\n"
        "s_waitcnt lgkmcnt(3)\n"
        "v_permlane16_swap_b32 v232, v233\n"
        "v_mfma_f32_16x16x32_bf16 v[220:223], v[160:163], a[220:223], v[220:223]\n"
        "v_max_f32 v232, v232, v233\n"
        "v_mul_f32 v236, s25, v232\n"
        "v_mfma_f32_16x16x32_bf16 v[200:203], v[164:167], a[220:223], 0\n"
        "ds_read_b128 a[220:223], v248 offset:43008\n"
        "v_mul_f32 v237, s25, v232\n"
        "v_mfma_f32_16x16x32_bf16 v[200:203], v[156:159], a[224:227], v[200:203]\n"
        "s_waitcnt lgkmcnt(3)\n"
        "ds_read_b128 a[224:227], v254 offset:36880\n"
        "v_add_u32 v245, s45, v245\n"
        "v_mfma_f32_16x16x32_bf16 v[204:207], v[156:159], a[232:235], 0\n"
        "s_waitcnt lgkmcnt(3)\n"
        "v_permlane32_swap_b32 v236, v237\n"
        "v_mfma_f32_16x16x32_bf16 v[204:207], v[164:167], a[228:231], v[204:207]\n"
        "v_permlane32_swap_b32 v234, v235\n"
        "v_mfma_f32_16x16x32_bf16 v[224:227], v[160:163], a[228:231], 0\n"
        "ds_read_b128 a[228:231], v254 offset:43024\n"
        "v_max_f32 v234, v234, v235\n"
        "v_mfma_f32_16x16x32_bf16 v[224:227], v[152:155], a[232:235], v[224:227]\n"
        "s_waitcnt lgkmcnt(3)\n"
        "ds_read_b128 a[232:235], v248 offset:36896\n"
        "v_mov_b32 v235, v234\n"
        "v_mfma_f32_16x16x32_bf16 v[172:175], a[236:239], a[216:219], v[172:175]\n"
        "s_waitcnt lgkmcnt(3)\n"
        "v_fma_f32 v64, v64, s24, v236\n"
        "v_fma_f32 v65, v65, s24, v236\n"
        "v_mfma_f32_16x16x32_bf16 v[172:175], a[244:247], a[220:223], v[172:175]\n"
        "v_permlane16_swap_b32 v234, v235\n"
        "v_mfma_f32_16x16x32_bf16 v[180:183], a[248:251], a[220:223], v[180:183]\n"
        "ds_read_b128 a[220:223], v248 offset:43040\n"
        "v_max_f32 v234, v234, v235\n"
        "v_mfma_f32_16x16x32_bf16 v[180:183], a[240:243], a[216:219], v[180:183]\n"
        "s_waitcnt lgkmcnt(3)\n"
        "ds_read_b128 a[216:219], v254 offset:36912\n"
        "v_mul_f32 v238, s25, v234\n"
        "v_mfma_f32_16x16x32_bf16 v[184:187], a[240:243], a[224:227], v[184:187]\n"
        "s_waitcnt lgkmcnt(3)\n"
        "ds_read_b128 v[152:155], v247 offset:16384\n"
        "v_mul_f32 v239, s25, v234\n"
        "v_mfma_f32_16x16x32_bf16 v[184:187], a[248:251], a[228:231], v[184:187]\n"
        "ds_read_b128 v[160:163], v247 offset:20480\n"
        "v_fma_f32 v66, v66, s24, v236\n"
        "v_mfma_f32_16x16x32_bf16 v[176:179], a[244:247], a[228:231], v[176:179]\n"
        "ds_read_b128 a[228:231], v254 offset:43056\n"
        "v_fma_f32 v67, v67, s24, v236\n"
        "v_mfma_f32_16x16x32_bf16 v[176:179], a[236:239], a[224:227], v[176:179]\n"
        "s_waitcnt lgkmcnt(5)\n"
        "ds_read_b128 a[224:227], v248 offset:36928\n"
        "v_fma_f32 v68, v68, s24, v236\n"
        "v_mfma_f32_16x16x32_bf16 v[212:215], a[236:239], a[232:235], v[212:215]\n"
        "s_waitcnt lgkmcnt(5)\n"
        "ds_read_b128 v[156:159], v247 offset:16640\n"
        "v_fma_f32 v69, v69, s24, v236\n"
        "v_mfma_f32_16x16x32_bf16 v[212:215], a[244:247], a[220:223], v[212:215]\n"
        "ds_read_b128 v[164:167], v247 offset:20736\n"
        "v_fma_f32 v70, v70, s24, v236\n"
        "v_mfma_f32_16x16x32_bf16 v[192:195], a[248:251], a[220:223], v[192:195]\n"
        "ds_read_b128 a[220:223], v248 offset:43072\n"
        "v_fma_f32 v71, v71, s24, v236\n"
        "v_mfma_f32_16x16x32_bf16 v[192:195], a[240:243], a[232:235], v[192:195]\n"
        "s_waitcnt lgkmcnt(7)\n"
        "ds_read_b128 a[232:235], v254 offset:36944\n"
        "v_fma_f32 v72, v72, s24, v236\n"
        "v_mfma_f32_16x16x32_bf16 v[196:199], a[240:243], a[216:219], v[196:199]\n"
        "s_waitcnt lgkmcnt(5)\n"
        "v_permlane32_swap_b32 v238, v239\n"
        "v_mfma_f32_16x16x32_bf16 v[196:199], a[248:251], a[228:231], v[196:199]\n"
        "v_exp_f32 v64, v64\n"
        "v_mfma_f32_16x16x32_bf16 v[216:219], a[244:247], a[228:231], v[216:219]\n"
        "ds_read_b128 a[228:231], v254 offset:43088\n"
        "v_fma_f32 v73, v73, s24, v236\n"
        "v_mfma_f32_16x16x32_bf16 v[216:219], a[236:239], a[216:219], v[216:219]\n"
        "s_waitcnt lgkmcnt(5)\n"
        "ds_read_b128 a[216:219], v248 offset:12288\n"
        "v_fma_f32 v74, v74, s24, v236\n"
        "v_mfma_f32_16x16x32_bf16 v[220:223], a[236:239], a[224:227], v[220:223]\n"
        "s_waitcnt lgkmcnt(3)\n"
        "v_exp_f32 v65, v65\n"
        "v_mfma_f32_16x16x32_bf16 v[220:223], a[244:247], a[220:223], v[220:223]\n"
        "v_exp_f32 v66, v66\n"
        "v_mfma_f32_16x16x32_bf16 v[200:203], a[248:251], a[220:223], v[200:203]\n"
        "ds_read_b128 a[220:223], v248 offset:49152\n"
        "v_cvt_pk_bf16_f32 v64, v64, v65\n"
        "v_mfma_f32_16x16x32_bf16 v[200:203], a[240:243], a[224:227], v[200:203]\n"
        "s_waitcnt lgkmcnt(3)\n"
        "ds_read_b128 a[224:227], v254 offset:12304\n"
        "v_fma_f32 v75, v75, s24, v236\n"
        "v_mfma_f32_16x16x32_bf16 v[204:207], a[240:243], a[232:235], v[204:207]\n"
        "s_waitcnt lgkmcnt(3)\n"
        "v_exp_f32 v67, v67\n"
        "v_mfma_f32_16x16x32_bf16 v[204:207], a[248:251], a[228:231], v[204:207]\n"
        "v_exp_f32 v68, v68\n"
        "v_mfma_f32_16x16x32_bf16 v[224:227], a[244:247], a[228:231], v[224:227]\n"
        "ds_read_b128 a[228:231], v254 offset:49168\n"
        "v_cvt_pk_bf16_f32 v65, v66, v67\n"
        "v_mfma_f32_16x16x32_bf16 v[224:227], a[236:239], a[232:235], v[224:227]\n"
        "s_waitcnt lgkmcnt(3)\n"
        "ds_read_b128 a[232:235], v248 offset:12320\n"
        "v_add_u32 v246, s45, v246\n"
        "v_mfma_f32_16x16x32_bf16 v[172:175], v[152:155], a[216:219], v[172:175]\n"
        "s_waitcnt lgkmcnt(3)\n"
        "v_exp_f32 v69, v69\n"
        "v_mfma_f32_16x16x32_bf16 v[172:175], v[160:163], a[220:223], v[172:175]\n"
        "v_cvt_pk_bf16_f32 v66, v68, v69\n"
        "v_add_u32 v247, s45, v247\n"
        "v_mfma_f32_16x16x32_bf16 v[180:183], v[164:167], a[220:223], v[180:183]\n"
        "ds_read_b128 a[220:223], v248 offset:49184\n"
        "v_mfma_f32_16x16x32_bf16 v[180:183], v[156:159], a[216:219], v[180:183]\n"
        "s_waitcnt lgkmcnt(3)\n"
        "ds_read_b128 a[216:219], v254 offset:12336\n"
        "v_mfma_f32_16x16x32_bf16 v[184:187], v[156:159], a[224:227], v[184:187]\n"
        "s_waitcnt lgkmcnt(3)\n"
        "v_exp_f32 v70, v70\n"
        "v_mfma_f32_16x16x32_bf16 v[184:187], v[164:167], a[228:231], v[184:187]\n"
        "v_exp_f32 v71, v71\n"
        "v_mfma_f32_16x16x32_bf16 v[176:179], v[160:163], a[228:231], v[176:179]\n"
        "ds_read_b128 a[228:231], v254 offset:49200\n"
        "v_cvt_pk_bf16_f32 v67, v70, v71\n"
        "v_mfma_f32_16x16x32_bf16 v[176:179], v[152:155], a[224:227], v[176:179]\n"
        "s_waitcnt lgkmcnt(3)\n"
        "ds_read_b128 a[224:227], v248 offset:12352\n"
        "v_mfma_f32_16x16x32_bf16 v[212:215], v[152:155], a[232:235], v[212:215]\n"
        "s_waitcnt lgkmcnt(3)\n"
        "s_waitcnt vmcnt(0)\n"
        "s_barrier\n"
        "v_exp_f32 v72, v72\n"
        "v_mfma_f32_16x16x32_bf16 v[212:215], v[160:163], a[220:223], v[212:215]\n"
        "v_exp_f32 v73, v73\n"
        "v_mfma_f32_16x16x32_bf16 v[192:195], v[164:167], a[220:223], v[192:195]\n"
        "ds_read_b128 a[220:223], v248 offset:49216\n"
        "v_cvt_pk_bf16_f32 v68, v72, v73\n"
        "v_mfma_f32_16x16x32_bf16 v[192:195], v[156:159], a[232:235], v[192:195]\n"
        "s_waitcnt lgkmcnt(3)\n"
        "ds_read_b128 a[232:235], v254 offset:12368\n"
        "v_mfma_f32_16x16x32_bf16 v[196:199], v[156:159], a[216:219], v[196:199]\n"
        "s_waitcnt lgkmcnt(3)\n"
        "ds_read_b128 a[236:239], v240 offset:0\n"
        "v_mfma_f32_16x16x32_bf16 v[196:199], v[164:167], a[228:231], v[196:199]\n"
        "ds_read_b128 a[240:243], v241 offset:0\n"
        "v_mfma_f32_16x16x32_bf16 v[216:219], v[160:163], a[228:231], v[216:219]\n"
        "ds_read_b128 a[228:231], v254 offset:49232\n"
        "v_mfma_f32_16x16x32_bf16 v[216:219], v[152:155], a[216:219], v[216:219]\n"
        "s_waitcnt lgkmcnt(5)\n"
        "ds_read_b128 a[244:247], v240 offset:128\n"
        "v_mfma_f32_16x16x32_bf16 v[220:223], v[152:155], a[224:227], v[220:223]\n"
        "s_waitcnt lgkmcnt(5)\n"
        "ds_read_b128 a[248:251], v241 offset:128\n"
        "v_mfma_f32_16x16x32_bf16 v[220:223], v[160:163], a[220:223], v[220:223]\n"
        "ds_read_b128 a[216:219], v240 offset:256\n"
        "v_mfma_f32_16x16x32_bf16 v[200:203], v[164:167], a[220:223], v[200:203]\n"
        "ds_read_b128 a[220:223], v241 offset:256\n"
        "v_mfma_f32_16x16x32_bf16 v[200:203], v[156:159], a[224:227], v[200:203]\n"
        "s_waitcnt lgkmcnt(7)\n"
        "v_exp_f32 v74, v74\n"
        "v_mfma_f32_16x16x32_bf16 v[204:207], v[156:159], a[232:235], v[204:207]\n"
        "s_waitcnt lgkmcnt(4)\n"
        "v_exp_f32 v75, v75\n"
        "v_mfma_f32_16x16x32_bf16 v[204:207], v[164:167], a[228:231], v[204:207]\n"
        "s_sub_u32 s27, s27, 1\n"
        "v_cvt_pk_bf16_f32 v69, v74, v75\n"
        "v_mfma_f32_16x16x32_bf16 v[224:227], v[160:163], a[228:231], v[224:227]\n"
        "s_cmp_lg_u32 s27, 0\n"
        "v_mfma_f32_16x16x32_bf16 v[224:227], v[152:155], a[232:235], v[224:227]\n"
        "s_cbranch_scc1 10b\n"
        "20:\n"
        "s_waitcnt lgkmcnt(0)\n"
        "s_nop 7\n"
        "v_mfma_f32_16x16x32_bf16 v[48:51], a[252:255], v[64:67], 0\n"
        "s_and_b32 s46, s42, 1\n"
        "s_lshl_b32 s31, s46, 8\n"
        "s_mul_i32 s45, s46, 786432\n"
        "s_sub_u32 s37, s37, s45\n"
        "s_cmp_eq_u32 s46, 0\n"
        "s_cselect_b32 s43, s43, s44\n"
        "s_add_u32 s28, s37, 786432\n"
        "s_cmp_eq_u32 s28, 100663296\n"
        "s_cselect_b32 s28, 0, s28\n"
        "v_fma_f32 v76, v76, s24, v236\n"
        "v_exp_f32 v76, v76\n"
        "v_fma_f32 v77, v77, s24, v236\n"
        "v_exp_f32 v77, v77\n"
        "v_fma_f32 v78, v78, s24, v236\n"
        "v_cvt_pk_bf16_f32 v70, v76, v77\n"
        "v_exp_f32 v78, v78\n"
        "v_fma_f32 v79, v79, s24, v236\n"
        "v_exp_f32 v79, v79\n"
        "v_subrev_u32 v247, s31, v247\n"
        "v_cvt_pk_bf16_f32 v71, v78, v79\n"
        "s_nop 1\n"
        "v_mfma_f32_16x16x32_bf16 v[48:51], a[252:255], v[68:71], v[48:51]\n"
        "v_cvt_pk_bf16_f32 v232, v172, v176\n"
        "v_cvt_pk_bf16_f32 v233, v212, v216\n"
        "v_cvt_pk_bf16_f32 v234, v220, v224\n"
        "v_fma_f32 v80, v80, s24, v237\n"
        "v_exp_f32 v80, v80\n"
        "v_mfma_f32_16x16x32_bf16 v[52:55], v[132:135], v[68:71], 0\n"
        "buffer_store_dwordx3 v[232:234], v250, s[16:19], s38 offen offset:0 sc1\n"
        "ds_read_b128 a[216:219], v248 offset:0\n"
        "v_mfma_f32_16x16x32_bf16 v[52:55], v[128:131], v[64:67], v[52:55]\n"
        "ds_read_b128 a[220:223], v248 offset:6144\n"
        "v_cvt_pk_bf16_f32 v232, v173, v177\n"
        "v_cvt_pk_bf16_f32 v233, v213, v217\n"
        "v_cvt_pk_bf16_f32 v234, v221, v225\n"
        "v_fma_f32 v81, v81, s24, v237\n"
        "v_fma_f32 v82, v82, s24, v237\n"
        "v_mfma_f32_16x16x32_bf16 v[56:59], v[136:139], v[64:67], 0\n"
        "buffer_store_dwordx3 v[232:234], v250, s[16:19], s38 offen offset:384 sc1\n"
        "ds_read_b128 a[224:227], v248 offset:36864\n"
        "v_mfma_f32_16x16x32_bf16 v[56:59], v[140:143], v[68:71], v[56:59]\n"
        "ds_read_b128 a[228:231], v248 offset:43008\n"
        "v_cvt_pk_bf16_f32 v232, v174, v178\n"
        "v_cvt_pk_bf16_f32 v233, v214, v218\n"
        "v_cvt_pk_bf16_f32 v234, v222, v226\n"
        "v_exp_f32 v81, v81\n"
        "v_mfma_f32_16x16x32_bf16 v[60:63], v[148:151], v[68:71], 0\n"
        "buffer_store_dwordx3 v[232:234], v250, s[16:19], s38 offen offset:768 sc1\n"
        "ds_read_b128 a[232:235], v248 offset:12288\n"
        "v_mfma_f32_16x16x32_bf16 v[60:63], v[144:147], v[64:67], v[60:63]\n"
        "ds_read_b128 a[236:239], v248 offset:49152\n"
        "v_cvt_pk_bf16_f32 v232, v175, v179\n"
        "v_cvt_pk_bf16_f32 v233, v215, v219\n"
        "v_cvt_pk_bf16_f32 v234, v223, v227\n"
        "v_cvt_pk_bf16_f32 v80, v80, v81\n"
        "v_fma_f32 v83, v83, s24, v237\n"
        "v_exp_f32 v82, v82\n"
        "v_exp_f32 v83, v83\n"
        "v_fma_f32 v84, v84, s24, v237\n"
        "v_cvt_pk_bf16_f32 v81, v82, v83\n"
        "v_exp_f32 v84, v84\n"
        "v_fma_f32 v85, v85, s24, v237\n"
        "v_exp_f32 v85, v85\n"
        "v_fma_f32 v86, v86, s24, v237\n"
        "v_cvt_pk_bf16_f32 v82, v84, v85\n"
        "v_exp_f32 v86, v86\n"
        "v_fma_f32 v87, v87, s24, v237\n"
        "v_exp_f32 v87, v87\n"
        "v_fma_f32 v88, v88, s24, v237\n"
        "v_cvt_pk_bf16_f32 v83, v86, v87\n"
        "s_nop 1\n"
        "v_mfma_f32_16x16x32_bf16 v[168:171], a[252:255], v[80:83], 0\n"
        "buffer_store_dwordx3 v[232:234], v250, s[16:19], s38 offen offset:1152 sc1\n"
        "ds_read_b128 a[240:243], v254 offset:16\n"
        "v_exp_f32 v88, v88\n"
        "v_fma_f32 v89, v89, s24, v237\n"
        "v_exp_f32 v89, v89\n"
        "v_fma_f32 v90, v90, s24, v237\n"
        "v_cvt_pk_bf16_f32 v84, v88, v89\n"
        "v_exp_f32 v90, v90\n"
        "v_fma_f32 v91, v91, s24, v237\n"
        "v_exp_f32 v91, v91\n"
        "v_fma_f32 v92, v92, s24, v237\n"
        "v_cvt_pk_bf16_f32 v85, v90, v91\n"
        "v_exp_f32 v92, v92\n"
        "v_fma_f32 v93, v93, s24, v237\n"
        "v_exp_f32 v93, v93\n"
        "v_fma_f32 v94, v94, s24, v237\n"
        "v_cvt_pk_bf16_f32 v86, v92, v93\n"
        "v_exp_f32 v94, v94\n"
        "v_fma_f32 v95, v95, s24, v237\n"
        "v_exp_f32 v95, v95\n"
        "v_cvt_pk_bf16_f32 v232, v180, v184\n"
        "v_cvt_pk_bf16_f32 v87, v94, v95\n"
        "s_nop 1\n"
        "v_mfma_f32_16x16x32_bf16 v[168:171], a[252:255], v[84:87], v[168:171]\n"
        "ds_read_b128 a[244:247], v254 offset:6160\n"
        "v_cvt_pk_bf16_f32 v233, v192, v196\n"
        "v_cvt_pk_bf16_f32 v234, v200, v204\n"
        "v_rcp_f32 v48, v48\n"
        "v_fma_f32 v96, v96, s24, v238\n"
        "v_mfma_f32_16x16x32_bf16 v[188:191], v[132:135], v[84:87], 0\n"
        "buffer_store_dwordx3 v[232:234], v250, s[16:19], s39 offen offset:0 sc1\n"
        "ds_read_b128 a[248:251], v254 offset:36880\n"
        "v_mfma_f32_16x16x32_bf16 v[188:191], v[128:131], v[80:83], v[188:191]\n"
        "v_cvt_pk_bf16_f32 v232, v181, v185\n"
        "v_cvt_pk_bf16_f32 v233, v193, v197\n"
        "v_cvt_pk_bf16_f32 v234, v201, v205\n"
        "v_mul_f32 v52, v48, v52\n"
        "v_mul_f32 v53, v48, v53\n"
        "v_mul_f32 v54, v48, v54\n"
        "v_mfma_f32_16x16x32_bf16 v[208:211], v[136:139], v[80:83], 0\n"
        "buffer_store_dwordx3 v[232:234], v250, s[16:19], s39 offen offset:384 sc1\n"
        "v_mul_f32 v55, v48, v55\n"
        "v_mfma_f32_16x16x32_bf16 v[208:211], v[140:143], v[84:87], v[208:211]\n"
        "v_cvt_pk_bf16_f32 v232, v182, v186\n"
        "v_cvt_pk_bf16_f32 v233, v194, v198\n"
        "v_cvt_pk_bf16_f32 v234, v202, v206\n"
        "v_cvt_pk_bf16_f32 v52, v52, v53\n"
        "v_cvt_pk_bf16_f32 v53, v54, v55\n"
        "v_mul_f32 v56, v48, v56\n"
        "v_mfma_f32_16x16x32_bf16 v[228:231], v[148:151], v[84:87], 0\n"
        "buffer_store_dwordx3 v[232:234], v250, s[16:19], s39 offen offset:768 sc1\n"
        "v_mul_f32 v57, v48, v57\n"
        "v_mfma_f32_16x16x32_bf16 v[228:231], v[144:147], v[80:83], v[228:231]\n"
        "v_cvt_pk_bf16_f32 v232, v183, v187\n"
        "v_cvt_pk_bf16_f32 v233, v195, v199\n"
        "v_cvt_pk_bf16_f32 v234, v203, v207\n"
        "v_mul_f32 v58, v48, v58\n"
        "v_mul_f32 v59, v48, v59\n"
        "v_cvt_pk_bf16_f32 v54, v56, v57\n"
        "v_cvt_pk_bf16_f32 v55, v58, v59\n"
        "v_mul_f32 v60, v48, v60\n"
        "v_mul_f32 v61, v48, v61\n"
        "v_mul_f32 v62, v48, v62\n"
        "v_mul_f32 v63, v48, v63\n"
        "v_cvt_pk_bf16_f32 v60, v60, v61\n"
        "v_cvt_pk_bf16_f32 v61, v62, v63\n"
        "ds_write_b128 v245, v[52:55] offset:0\n"
        "ds_write_b64 v246, v[60:61] offset:0\n"
        "v_rcp_f32 v168, v168\n"
        "v_exp_f32 v96, v96\n"
        "v_mul_f32 v188, v168, v188\n"
        "v_mul_f32 v189, v168, v189\n"
        "v_mul_f32 v190, v168, v190\n"
        "v_mul_f32 v191, v168, v191\n"
        "v_cvt_pk_bf16_f32 v188, v188, v189\n"
        "v_cvt_pk_bf16_f32 v189, v190, v191\n"
        "v_mul_f32 v208, v168, v208\n"
        "v_mul_f32 v209, v168, v209\n"
        "v_mul_f32 v210, v168, v210\n"
        "v_mul_f32 v211, v168, v211\n"
        "v_cvt_pk_bf16_f32 v190, v208, v209\n"
        "v_cvt_pk_bf16_f32 v191, v210, v211\n"
        "v_mul_f32 v228, v168, v228\n"
        "v_mul_f32 v229, v168, v229\n"
        "v_mul_f32 v230, v168, v230\n"
        "v_mul_f32 v231, v168, v231\n"
        "v_cvt_pk_bf16_f32 v228, v228, v229\n"
        "v_cvt_pk_bf16_f32 v229, v230, v231\n"
        "ds_write_b128 v245, v[188:191] offset:256\n"
        "ds_write_b64 v246, v[228:229] offset:256\n"
        "s_waitcnt lgkmcnt(0)\n"
        "s_barrier\n"
        "ds_read_b128 v[152:155], v247 offset:0\n"
        "s_waitcnt lgkmcnt(0)\n"
        "v_mfma_f32_16x16x32_bf16 v[0:3], v[152:155], a[216:219], 0\n"
        "buffer_store_dwordx3 v[232:234], v250, s[16:19], s39 offen offset:1152 sc1\n"
        "ds_read_b128 v[156:159], v247 offset:4096\n"
        "s_waitcnt lgkmcnt(0)\n"
        "v_mfma_f32_16x16x32_bf16 v[0:3], v[156:159], a[220:223], v[0:3]\n"
        "ds_read_b128 v[172:175], v247 offset:8192\n"
        "ds_read_b128 v[176:179], v247 offset:12288\n"
        "ds_read_b128 v[212:215], v247 offset:16384\n"
        "ds_read_b128 v[216:219], v247 offset:20480\n"
        "s_waitcnt lgkmcnt(3)\n"
        "v_mfma_f32_16x16x32_bf16 v[0:3], v[172:175], a[224:227], v[0:3]\n"
        "s_waitcnt lgkmcnt(2)\n"
        "ds_read_b128 a[216:219], v254 offset:43024\n"
        "ds_read_b128 a[220:223], v254 offset:12304\n"
        "v_fma_f32 v97, v97, s24, v238\n"
        "v_fma_f32 v98, v98, s24, v238\n"
        "v_mfma_f32_16x16x32_bf16 v[0:3], v[176:179], a[228:231], v[0:3]\n"
        "s_waitcnt lgkmcnt(3)\n"
        "ds_read_b128 a[224:227], v254 offset:49168\n"
        "ds_read_b128 a[228:231], v248 offset:32\n"
        "v_exp_f32 v97, v97\n"
        "v_mfma_f32_16x16x32_bf16 v[0:3], v[212:215], a[232:235], v[0:3]\n"
        "s_waitcnt lgkmcnt(4)\n"
        "ds_read_b128 a[232:235], v248 offset:6176\n"
        "v_exp_f32 v98, v98\n"
        "v_cvt_pk_bf16_f32 v96, v96, v97\n"
        "v_mfma_f32_16x16x32_bf16 v[0:3], v[216:219], a[236:239], v[0:3]\n"
        "ds_read_b128 a[236:239], v248 offset:36896\n"
        "v_fma_f32 v99, v99, s24, v238\n"
        "v_exp_f32 v99, v99\n"
        "v_mfma_f32_16x16x32_bf16 v[4:7], v[152:155], a[240:243], 0\n"
        "ds_read_b128 a[240:243], v248 offset:43040\n"
        "v_cvt_pk_bf16_f32 v97, v98, v99\n"
        "v_fma_f32 v100, v100, s24, v238\n"
        "v_fma_f32 v101, v101, s24, v238\n"
        "v_mfma_f32_16x16x32_bf16 v[4:7], v[156:159], a[244:247], v[4:7]\n"
        "ds_read_b128 a[244:247], v248 offset:12320\n"
        "v_exp_f32 v100, v100\n"
        "v_fma_f32 v102, v102, s24, v238\n"
        "v_mfma_f32_16x16x32_bf16 v[4:7], v[172:175], a[248:251], v[4:7]\n"
        "s_waitcnt lgkmcnt(7)\n"
        "ds_read_b128 a[248:251], v248 offset:49184\n"
        "v_exp_f32 v101, v101\n"
        "v_fma_f32 v103, v103, s24, v238\n"
        "v_mfma_f32_16x16x32_bf16 v[4:7], v[176:179], a[216:219], v[4:7]\n"
        "s_waitcnt lgkmcnt(7)\n"
        "v_cvt_pk_bf16_f32 v98, v100, v101\n"
        "v_exp_f32 v102, v102\n"
        "v_fma_f32 v104, v104, s24, v238\n"
        "v_mfma_f32_16x16x32_bf16 v[4:7], v[212:215], a[220:223], v[4:7]\n"
        "s_waitcnt lgkmcnt(6)\n"
        "v_exp_f32 v103, v103\n"
        "v_exp_f32 v104, v104\n"
        "v_mfma_f32_16x16x32_bf16 v[4:7], v[216:219], a[224:227], v[4:7]\n"
        "s_waitcnt lgkmcnt(5)\n"
        "v_cvt_pk_bf16_f32 v99, v102, v103\n"
        "v_fma_f32 v105, v105, s24, v238\n"
        "v_exp_f32 v105, v105\n"
        "v_mfma_f32_16x16x32_bf16 v[8:11], v[152:155], a[228:231], 0\n"
        "s_waitcnt lgkmcnt(4)\n"
        "ds_read_b128 a[216:219], v254 offset:48\n"
        "v_cvt_pk_bf16_f32 v100, v104, v105\n"
        "v_fma_f32 v106, v106, s24, v238\n"
        "v_fma_f32 v107, v107, s24, v238\n"
        "v_mfma_f32_16x16x32_bf16 v[8:11], v[156:159], a[232:235], v[8:11]\n"
        "s_waitcnt lgkmcnt(4)\n"
        "ds_read_b128 a[220:223], v254 offset:6192\n"
        "v_exp_f32 v106, v106\n"
        "v_fma_f32 v108, v108, s24, v238\n"
        "v_mfma_f32_16x16x32_bf16 v[8:11], v[172:175], a[236:239], v[8:11]\n"
        "s_waitcnt lgkmcnt(4)\n"
        "ds_read_b128 a[224:227], v254 offset:36912\n"
        "v_exp_f32 v107, v107\n"
        "v_fma_f32 v109, v109, s24, v238\n"
        "v_mfma_f32_16x16x32_bf16 v[8:11], v[176:179], a[240:243], v[8:11]\n"
        "s_waitcnt lgkmcnt(4)\n"
        "ds_read_b128 a[228:231], v254 offset:43056\n"
        "v_cvt_pk_bf16_f32 v101, v106, v107\n"
        "v_exp_f32 v108, v108\n"
        "v_mfma_f32_16x16x32_bf16 v[8:11], v[212:215], a[244:247], v[8:11]\n"
        "s_waitcnt lgkmcnt(4)\n"
        "ds_read_b128 a[232:235], v254 offset:12336\n"
        "v_exp_f32 v109, v109\n"
        "v_fma_f32 v110, v110, s24, v238\n"
        "v_mfma_f32_16x16x32_bf16 v[8:11], v[216:219], a[248:251], v[8:11]\n"
        "ds_read_b128 a[236:239], v254 offset:49200\n"
        "s_add_u32 s38, s37, s43\n"
        "v_cvt_pk_bf16_f32 v102, v108, v109\n"
        "v_exp_f32 v110, v110\n"
        "v_mfma_f32_16x16x32_bf16 v[64:67], a[252:255], v[96:99], 0\n"
        "ds_read_b128 a[240:243], v248 offset:64\n"
        "s_add_u32 s39, s28, s43\n"
        "v_fma_f32 v111, v111, s24, v238\n"
        "v_exp_f32 v111, v111\n"
        "v_fma_f32 v112, v112, s24, v239\n"
        "v_cvt_pk_bf16_f32 v103, v110, v111\n"
        "s_nop 1\n"
        "v_mfma_f32_16x16x32_bf16 v[64:67], a[252:255], v[100:103], v[64:67]\n"
        "ds_read_b128 a[244:247], v248 offset:6208\n"
        "v_exp_f32 v112, v112\n"
        "v_fma_f32 v113, v113, s24, v239\n"
        "v_mfma_f32_16x16x32_bf16 v[68:71], v[132:135], v[100:103], 0\n"
        "ds_read_b128 a[248:251], v248 offset:36928\n"
        "v_exp_f32 v113, v113\n"
        "v_fma_f32 v114, v114, s24, v239\n"
        "v_mfma_f32_16x16x32_bf16 v[68:71], v[128:131], v[96:99], v[68:71]\n"
        "v_rcp_f32 v64, v64\n"
        "v_exp_f32 v114, v114\n"
        "v_mfma_f32_16x16x32_bf16 v[72:75], v[136:139], v[96:99], 0\n"
        "v_cvt_pk_bf16_f32 v112, v112, v113\n"
        "v_fma_f32 v115, v115, s24, v239\n"
        "v_exp_f32 v115, v115\n"
        "v_mfma_f32_16x16x32_bf16 v[72:75], v[140:143], v[100:103], v[72:75]\n"
        "v_cvt_pk_bf16_f32 v113, v114, v115\n"
        "v_mul_f32 v68, v64, v68\n"
        "v_mul_f32 v69, v64, v69\n"
        "v_mul_f32 v70, v64, v70\n"
        "v_mfma_f32_16x16x32_bf16 v[76:79], v[148:151], v[100:103], 0\n"
        "v_mul_f32 v71, v64, v71\n"
        "v_cvt_pk_bf16_f32 v68, v68, v69\n"
        "v_cvt_pk_bf16_f32 v69, v70, v71\n"
        "v_mul_f32 v72, v64, v72\n"
        "v_mfma_f32_16x16x32_bf16 v[76:79], v[144:147], v[96:99], v[76:79]\n"
        "s_waitcnt lgkmcnt(8)\n"
        "v_mul_f32 v73, v64, v73\n"
        "v_mul_f32 v74, v64, v74\n"
        "v_mul_f32 v75, v64, v75\n"
        "v_cvt_pk_bf16_f32 v70, v72, v73\n"
        "v_mfma_f32_16x16x32_bf16 v[12:15], v[152:155], a[216:219], 0\n"
        "s_waitcnt lgkmcnt(7)\n"
        "ds_read_b128 a[216:219], v248 offset:43072\n"
        "v_cvt_pk_bf16_f32 v71, v74, v75\n"
        "v_fma_f32 v116, v116, s24, v239\n"
        "v_mul_f32 v76, v64, v76\n"
        "v_mfma_f32_16x16x32_bf16 v[12:15], v[156:159], a[220:223], v[12:15]\n"
        "s_waitcnt lgkmcnt(7)\n"
        "ds_read_b128 a[220:223], v248 offset:12352\n"
        "v_mul_f32 v77, v64, v77\n"
        "v_mul_f32 v78, v64, v78\n"
        "v_mul_f32 v79, v64, v79\n"
        "v_mfma_f32_16x16x32_bf16 v[12:15], v[172:175], a[224:227], v[12:15]\n"
        "s_waitcnt lgkmcnt(7)\n"
        "ds_write_b128 v245, v[68:71] offset:512\n"
        "v_mfma_f32_16x16x32_bf16 v[12:15], v[176:179], a[228:231], v[12:15]\n"
        "s_waitcnt lgkmcnt(7)\n"
        "ds_read_b128 a[224:227], v248 offset:49216\n"
        "ds_read_b128 a[228:231], v254 offset:80\n"
        "v_cvt_pk_bf16_f32 v76, v76, v77\n"
        "v_cvt_pk_bf16_f32 v77, v78, v79\n"
        "v_mfma_f32_16x16x32_bf16 v[12:15], v[212:215], a[232:235], v[12:15]\n"
        "s_waitcnt lgkmcnt(8)\n"
        "ds_read_b128 a[232:235], v254 offset:6224\n"
        "v_exp_f32 v116, v116\n"
        "v_fma_f32 v117, v117, s24, v239\n"
        "v_mfma_f32_16x16x32_bf16 v[12:15], v[216:219], a[236:239], v[12:15]\n"
        "s_waitcnt lgkmcnt(8)\n"
        "ds_write_b64 v246, v[76:77] offset:512\n"
        "ds_read_b128 a[236:239], v254 offset:36944\n"
        "v_mfma_f32_16x16x32_bf16 v[16:19], v[152:155], a[240:243], 0\n"
        "s_waitcnt lgkmcnt(9)\n"
        "ds_read_b128 a[240:243], v254 offset:43088\n"
        "v_exp_f32 v117, v117\n"
        "v_fma_f32 v118, v118, s24, v239\n"
        "v_mfma_f32_16x16x32_bf16 v[16:19], v[156:159], a[244:247], v[16:19]\n"
        "s_waitcnt lgkmcnt(9)\n"
        "ds_read_b128 a[244:247], v254 offset:12368\n"
        "v_exp_f32 v118, v118\n"
        "v_cvt_pk_bf16_f32 v114, v116, v117\n"
        "v_mfma_f32_16x16x32_bf16 v[16:19], v[172:175], a[248:251], v[16:19]\n"
        "s_waitcnt lgkmcnt(9)\n"
        "ds_read_b128 a[248:251], v254 offset:49232\n"
        "v_fma_f32 v119, v119, s24, v239\n"
        "v_exp_f32 v119, v119\n"
        "v_mfma_f32_16x16x32_bf16 v[16:19], v[176:179], a[216:219], v[16:19]\n"
        "s_waitcnt lgkmcnt(9)\n"
        "v_cvt_pk_bf16_f32 v115, v118, v119\n"
        "v_fma_f32 v120, v120, s24, v239\n"
        "v_exp_f32 v120, v120\n"
        "v_mfma_f32_16x16x32_bf16 v[16:19], v[212:215], a[220:223], v[16:19]\n"
        "s_waitcnt lgkmcnt(7)\n"
        "v_fma_f32 v121, v121, s24, v239\n"
        "v_exp_f32 v121, v121\n"
        "v_fma_f32 v122, v122, s24, v239\n"
        "v_mfma_f32_16x16x32_bf16 v[16:19], v[216:219], a[224:227], v[16:19]\n"
        "s_waitcnt lgkmcnt(6)\n"
        "v_cvt_pk_bf16_f32 v116, v120, v121\n"
        "v_exp_f32 v122, v122\n"
        "v_fma_f32 v123, v123, s24, v239\n"
        "v_mfma_f32_16x16x32_bf16 v[20:23], v[152:155], a[228:231], 0\n"
        "s_waitcnt lgkmcnt(5)\n"
        "ds_read_b128 a[216:219], v248 offset:0\n"
        "v_exp_f32 v123, v123\n"
        "v_fma_f32 v124, v124, s24, v239\n"
        "v_mfma_f32_16x16x32_bf16 v[20:23], v[156:159], a[232:235], v[20:23]\n"
        "s_waitcnt lgkmcnt(4)\n"
        "ds_read_b128 a[220:223], v248 offset:6144\n"
        "v_cvt_pk_bf16_f32 v117, v122, v123\n"
        "v_exp_f32 v124, v124\n"
        "v_mfma_f32_16x16x32_bf16 v[20:23], v[172:175], a[236:239], v[20:23]\n"
        "s_waitcnt lgkmcnt(4)\n"
        "ds_read_b128 a[224:227], v248 offset:36864\n"
        "v_fma_f32 v125, v125, s24, v239\n"
        "v_exp_f32 v125, v125\n"
        "v_mfma_f32_16x16x32_bf16 v[20:23], v[176:179], a[240:243], v[20:23]\n"
        "s_waitcnt lgkmcnt(4)\n"
        "ds_read_b128 a[228:231], v248 offset:43008\n"
        "v_cvt_pk_bf16_f32 v118, v124, v125\n"
        "v_fma_f32 v126, v126, s24, v239\n"
        "v_fma_f32 v127, v127, s24, v239\n"
        "v_mfma_f32_16x16x32_bf16 v[20:23], v[212:215], a[244:247], v[20:23]\n"
        "s_waitcnt lgkmcnt(4)\n"
        "ds_read_b128 a[232:235], v248 offset:12288\n"
        "v_exp_f32 v126, v126\n"
        "v_cvt_pk_bf16_f32 v192, v0, v4\n"
        "v_mfma_f32_16x16x32_bf16 v[20:23], v[216:219], a[248:251], v[20:23]\n"
        "ds_read_b128 a[236:239], v248 offset:49152\n"
        "v_exp_f32 v127, v127\n"
        "v_cvt_pk_bf16_f32 v193, v8, v12\n"
        "v_mfma_f32_16x16x32_bf16 v[80:83], a[252:255], v[112:115], 0\n"
        "ds_read_b128 a[240:243], v254 offset:16\n"
        "v_cvt_pk_bf16_f32 v119, v126, v127\n"
        "v_cvt_pk_bf16_f32 v196, v1, v5\n"
        "v_cvt_pk_bf16_f32 v197, v9, v13\n"
        "v_mfma_f32_16x16x32_bf16 v[80:83], a[252:255], v[116:119], v[80:83]\n"
        "ds_read_b128 a[244:247], v254 offset:6160\n"
        "v_cvt_pk_bf16_f32 v194, v16, v20\n"
        "v_cvt_pk_bf16_f32 v198, v17, v21\n"
        "v_cvt_pk_bf16_f32 v200, v2, v6\n"
        "v_mfma_f32_16x16x32_bf16 v[84:87], v[132:135], v[116:119], 0\n"
        "buffer_store_dwordx3 v[192:194], v250, s[16:19], s38 offen offset:0 sc1\n"
        "v_mfma_f32_16x16x32_bf16 v[84:87], v[128:131], v[112:115], v[84:87]\n"
        "ds_read_b128 a[248:251], v254 offset:36880\n"
        "v_rcp_f32 v80, v80\n"
        "v_cvt_pk_bf16_f32 v201, v10, v14\n"
        "v_mfma_f32_16x16x32_bf16 v[88:91], v[136:139], v[112:115], 0\n"
        "v_cvt_pk_bf16_f32 v202, v18, v22\n"
        "v_cvt_pk_bf16_f32 v204, v3, v7\n"
        "v_cvt_pk_bf16_f32 v205, v11, v15\n"
        "v_cvt_pk_bf16_f32 v206, v19, v23\n"
        "v_mfma_f32_16x16x32_bf16 v[88:91], v[140:143], v[116:119], v[88:91]\n"
        "buffer_store_dwordx3 v[196:198], v250, s[16:19], s38 offen offset:384 sc1\n"
        "v_mfma_f32_16x16x32_bf16 v[92:95], v[148:151], v[116:119], 0\n"
        "v_mul_f32 v84, v80, v84\n"
        "v_mul_f32 v85, v80, v85\n"
        "v_mul_f32 v86, v80, v86\n"
        "v_mul_f32 v87, v80, v87\n"
        "v_mfma_f32_16x16x32_bf16 v[92:95], v[144:147], v[112:115], v[92:95]\n"
        "v_cvt_pk_bf16_f32 v84, v84, v85\n"
        "v_cvt_pk_bf16_f32 v85, v86, v87\n"
        "v_mul_f32 v88, v80, v88\n"
        "v_mul_f32 v89, v80, v89\n"
        "v_mul_f32 v90, v80, v90\n"
        "v_mul_f32 v91, v80, v91\n"
        "v_cvt_pk_bf16_f32 v86, v88, v89\n"
        "v_cvt_pk_bf16_f32 v87, v90, v91\n"
        "v_mul_f32 v92, v80, v92\n"
        "v_mul_f32 v93, v80, v93\n"
        "v_mul_f32 v94, v80, v94\n"
        "v_mul_f32 v95, v80, v95\n"
        "v_cvt_pk_bf16_f32 v92, v92, v93\n"
        "v_cvt_pk_bf16_f32 v93, v94, v95\n"
        "ds_write_b128 v245, v[84:87] offset:768\n"
        "ds_write_b64 v246, v[92:93] offset:768\n"
        "s_waitcnt lgkmcnt(0)\n"
        "s_barrier\n"
        "ds_read_b128 v[128:131], v247 offset:512\n"
        "s_waitcnt lgkmcnt(0)\n"
        "v_mfma_f32_16x16x32_bf16 v[24:27], v[128:131], a[216:219], 0\n"
        "ds_read_b128 v[132:135], v247 offset:4608\n"
        "ds_read_b128 v[136:139], v247 offset:8704\n"
        "s_waitcnt lgkmcnt(1)\n"
        "v_mfma_f32_16x16x32_bf16 v[24:27], v[132:135], a[220:223], v[24:27]\n"
        "s_waitcnt lgkmcnt(0)\n"
        "ds_read_b128 v[140:143], v247 offset:12800\n"
        "ds_read_b128 v[144:147], v247 offset:16896\n"
        "v_mfma_f32_16x16x32_bf16 v[24:27], v[136:139], a[224:227], v[24:27]\n"
        "s_waitcnt lgkmcnt(1)\n"
        "ds_read_b128 v[148:151], v247 offset:20992\n"
        "ds_read_b128 a[216:219], v254 offset:43024\n"
        "v_mfma_f32_16x16x32_bf16 v[24:27], v[140:143], a[228:231], v[24:27]\n"
        "s_waitcnt lgkmcnt(2)\n"
        "buffer_store_dwordx3 v[200:202], v250, s[16:19], s38 offen offset:768 sc1\n"
        "v_mfma_f32_16x16x32_bf16 v[24:27], v[144:147], a[232:235], v[24:27]\n"
        "s_waitcnt lgkmcnt(1)\n"
        "ds_read_b128 a[220:223], v254 offset:12304\n"
        "ds_read_b128 a[224:227], v254 offset:49168\n"
        "v_mfma_f32_16x16x32_bf16 v[24:27], v[148:151], a[236:239], v[24:27]\n"
        "ds_read_b128 a[228:231], v248 offset:32\n"
        "ds_read_b128 a[232:235], v248 offset:6176\n"
        "v_mfma_f32_16x16x32_bf16 v[28:31], v[128:131], a[240:243], 0\n"
        "buffer_store_dwordx3 v[204:206], v250, s[16:19], s38 offen offset:1152 sc1\n"
        "v_mfma_f32_16x16x32_bf16 v[28:31], v[132:135], a[244:247], v[28:31]\n"
        "ds_read_b128 a[236:239], v248 offset:36896\n"
        "ds_read_b128 a[240:243], v248 offset:43040\n"
        "v_mfma_f32_16x16x32_bf16 v[28:31], v[136:139], a[248:251], v[28:31]\n"
        "s_waitcnt lgkmcnt(6)\n"
        "ds_read_b128 a[244:247], v248 offset:12320\n"
        "ds_read_b128 a[248:251], v248 offset:49184\n"
        "v_mfma_f32_16x16x32_bf16 v[28:31], v[140:143], a[216:219], v[28:31]\n"
        "s_waitcnt lgkmcnt(7)\n"
        "ds_read_b128 a[216:219], v254 offset:48\n"
        "v_mfma_f32_16x16x32_bf16 v[28:31], v[144:147], a[220:223], v[28:31]\n"
        "s_waitcnt lgkmcnt(7)\n"
        "ds_read_b128 a[220:223], v254 offset:6192\n"
        "v_mfma_f32_16x16x32_bf16 v[28:31], v[148:151], a[224:227], v[28:31]\n"
        "s_waitcnt lgkmcnt(7)\n"
        "ds_read_b128 a[224:227], v254 offset:36912\n"
        "v_mfma_f32_16x16x32_bf16 v[32:35], v[128:131], a[228:231], 0\n"
        "s_waitcnt lgkmcnt(7)\n"
        "ds_read_b128 a[228:231], v254 offset:43056\n"
        "v_mfma_f32_16x16x32_bf16 v[32:35], v[132:135], a[232:235], v[32:35]\n"
        "s_waitcnt lgkmcnt(7)\n"
        "ds_read_b128 a[232:235], v254 offset:12336\n"
        "v_mfma_f32_16x16x32_bf16 v[32:35], v[136:139], a[236:239], v[32:35]\n"
        "s_waitcnt lgkmcnt(7)\n"
        "ds_read_b128 a[236:239], v254 offset:49200\n"
        "v_mfma_f32_16x16x32_bf16 v[32:35], v[140:143], a[240:243], v[32:35]\n"
        "s_waitcnt lgkmcnt(7)\n"
        "ds_read_b128 a[240:243], v248 offset:64\n"
        "v_cvt_pk_bf16_f32 v180, v24, v28\n"
        "v_mfma_f32_16x16x32_bf16 v[32:35], v[144:147], a[244:247], v[32:35]\n"
        "s_waitcnt lgkmcnt(7)\n"
        "ds_read_b128 a[244:247], v248 offset:6208\n"
        "v_cvt_pk_bf16_f32 v184, v25, v29\n"
        "v_mfma_f32_16x16x32_bf16 v[32:35], v[148:151], a[248:251], v[32:35]\n"
        "s_waitcnt lgkmcnt(7)\n"
        "ds_read_b128 a[248:251], v248 offset:36928\n"
        "v_cvt_pk_bf16_f32 v220, v26, v30\n"
        "v_mfma_f32_16x16x32_bf16 v[36:39], v[128:131], a[216:219], 0\n"
        "s_waitcnt lgkmcnt(7)\n"
        "ds_read_b128 a[216:219], v248 offset:43072\n"
        "v_cvt_pk_bf16_f32 v224, v27, v31\n"
        "v_mfma_f32_16x16x32_bf16 v[36:39], v[132:135], a[220:223], v[36:39]\n"
        "s_waitcnt lgkmcnt(7)\n"
        "ds_read_b128 a[220:223], v248 offset:12352\n"
        "v_mfma_f32_16x16x32_bf16 v[36:39], v[136:139], a[224:227], v[36:39]\n"
        "s_waitcnt lgkmcnt(7)\n"
        "ds_read_b128 a[224:227], v248 offset:49216\n"
        "v_mfma_f32_16x16x32_bf16 v[36:39], v[140:143], a[228:231], v[36:39]\n"
        "s_waitcnt lgkmcnt(7)\n"
        "ds_read_b128 a[228:231], v254 offset:80\n"
        "v_mfma_f32_16x16x32_bf16 v[36:39], v[144:147], a[232:235], v[36:39]\n"
        "s_waitcnt lgkmcnt(7)\n"
        "ds_read_b128 a[232:235], v254 offset:6224\n"
        "v_mfma_f32_16x16x32_bf16 v[36:39], v[148:151], a[236:239], v[36:39]\n"
        "s_waitcnt lgkmcnt(7)\n"
        "ds_read_b128 a[236:239], v254 offset:36944\n"
        "v_mfma_f32_16x16x32_bf16 v[40:43], v[128:131], a[240:243], 0\n"
        "s_waitcnt lgkmcnt(7)\n"
        "ds_read_b128 a[240:243], v254 offset:43088\n"
        "v_mfma_f32_16x16x32_bf16 v[40:43], v[132:135], a[244:247], v[40:43]\n"
        "s_waitcnt lgkmcnt(7)\n"
        "ds_read_b128 a[244:247], v254 offset:12368\n"
        "v_mfma_f32_16x16x32_bf16 v[40:43], v[136:139], a[248:251], v[40:43]\n"
        "s_waitcnt lgkmcnt(7)\n"
        "ds_read_b128 a[248:251], v254 offset:49232\n"
        "v_mfma_f32_16x16x32_bf16 v[40:43], v[140:143], a[216:219], v[40:43]\n"
        "s_waitcnt lgkmcnt(7)\n"
        "v_cvt_pk_bf16_f32 v181, v32, v36\n"
        "v_cvt_pk_bf16_f32 v185, v33, v37\n"
        "v_mfma_f32_16x16x32_bf16 v[40:43], v[144:147], a[220:223], v[40:43]\n"
        "s_waitcnt lgkmcnt(6)\n"
        "v_cvt_pk_bf16_f32 v221, v34, v38\n"
        "v_cvt_pk_bf16_f32 v225, v35, v39\n"
        "v_mfma_f32_16x16x32_bf16 v[40:43], v[148:151], a[224:227], v[40:43]\n"
        "s_waitcnt lgkmcnt(5)\n"
        "v_mfma_f32_16x16x32_bf16 v[44:47], v[128:131], a[228:231], 0\n"
        "s_waitcnt lgkmcnt(4)\n"
        "v_mfma_f32_16x16x32_bf16 v[44:47], v[132:135], a[232:235], v[44:47]\n"
        "s_waitcnt lgkmcnt(3)\n"
        "v_mfma_f32_16x16x32_bf16 v[44:47], v[136:139], a[236:239], v[44:47]\n"
        "s_waitcnt lgkmcnt(2)\n"
        "v_mfma_f32_16x16x32_bf16 v[44:47], v[140:143], a[240:243], v[44:47]\n"
        "s_waitcnt lgkmcnt(1)\n"
        "v_mfma_f32_16x16x32_bf16 v[44:47], v[144:147], a[244:247], v[44:47]\n"
        "s_waitcnt lgkmcnt(0)\n"
        "v_mfma_f32_16x16x32_bf16 v[44:47], v[148:151], a[248:251], v[44:47]\n"
        "s_nop 7\n"
        "v_cvt_pk_bf16_f32 v182, v40, v44\n"
        "buffer_store_dwordx3 v[180:182], v250, s[16:19], s39 offen offset:0 sc1\n"
        "v_cvt_pk_bf16_f32 v186, v41, v45\n"
        "buffer_store_dwordx3 v[184:186], v250, s[16:19], s39 offen offset:384 sc1\n"
        "v_cvt_pk_bf16_f32 v222, v42, v46\n"
        "buffer_store_dwordx3 v[220:222], v250, s[16:19], s39 offen offset:768 sc1\n"
        "v_cvt_pk_bf16_f32 v226, v43, v47\n"
        "buffer_store_dwordx3 v[224:226], v250, s[16:19], s39 offen offset:1152 sc1\n"
        : "+{v240}"(xr[0]), "+{v241}"(xr[1]), "+{v251}"(mr[0]), "+{v252}"(mr[1]), "+{v242}"(dm[0]), "+{v243}"(dm[1]), "+{v244}"(dm[2]), "+{v245}"(ow), "+{v246}"(ow2), "+{v247}"(ord), "+{v248}"(wor), "+{v249}"(ms), "+{v250}"(st), "+{v142}"(l16), "+{v170}"(id0), "+{v171}"(id1), "+{v139}"(rs), "+{v144}"(rr), "+{v140}"(oa), "+{v141}"(ob), "+{v147}"(od), "+{v145}"(rbs), "+{v146}"(rbw), "+{v254}"(wor2), "+{v255}"(ml), "+{v148}"(pf0), "+{v149}"(pf1), "+{v128}"(wsrc[0]), "+{v129}"(wsrc[1]), "+{v130}"(wsrc[2]), "+{v131}"(wrd[0]), "+{v132}"(wrd[1]), "+{v133}"(wrd[2]), "+{s47}"(ya), "+{s36}"(mk), "+{s37}"(raw), "+{s43}"(x0), "+{s44}"(x1)
        : "{s[8:11]}"(srdx), "{s[12:15]}"(srdm), "{s[16:19]}"(srdo), "{s[20:23]}"(srdw), "{s[52:55]}"(srdwo), "{s[56:59]}"(srdrp), "{s[60:63]}"(srdrb), "{s42}"(wv), "s"(ldsb)
        : "v0", "v1", "v2", "v3", "v4", "v5", "v6", "v7", "v8", "v9", "v10", "v11", "v12", "v13", "v14", "v15", "v16", "v17", "v18", "v19", "v20", "v21", "v22", "v23", "v24", "v25", "v26", "v27", "v28", "v29", "v30", "v31", "v32", "v33", "v34", "v35", "v36", "v37", "v38", "v39", "v40", "v41", "v42", "v43", "v44", "v45", "v46", "v47", "v48", "v49", "v50", "v51", "v52", "v53", "v54", "v55", "v56", "v57", "v58", "v59", "v60", "v61", "v62", "v63", "v64", "v65", "v66", "v67", "v68", "v69", "v70", "v71", "v72", "v73", "v74", "v75", "v76", "v77", "v78", "v79", "v80", "v81", "v82", "v83", "v84", "v85", "v86", "v87", "v88", "v89", "v90", "v91", "v92", "v93", "v94", "v95", "v96", "v97", "v98", "v99", "v100", "v101", "v102", "v103", "v104", "v105", "v106", "v107", "v108", "v109", "v110", "v111", "v112", "v113", "v114", "v115", "v116", "v117", "v118", "v119", "v120", "v121", "v122", "v123", "v124", "v125", "v126", "v127", "v134", "v135", "v136", "v137", "v138", "v143", "v150", "v151", "v152", "v153", "v154", "v155", "v156", "v157", "v158", "v159", "v160", "v161", "v162", "v163", "v164", "v165", "v166", "v167", "v168", "v169", "v172", "v173", "v174", "v175", "v176", "v177", "v178", "v179", "v180", "v181", "v182", "v183", "v184", "v185", "v186", "v187", "v188", "v189", "v190", "v191", "v192", "v193", "v194", "v195", "v196", "v197", "v198", "v199", "v200", "v201", "v202", "v203", "v204", "v205", "v206", "v207", "v208", "v209", "v210", "v211", "v212", "v213", "v214", "v215", "v216", "v217", "v218", "v219", "v220", "v221", "v222", "v223", "v224", "v225", "v226", "v227", "v228", "v229", "v230", "v231", "v232", "v233", "v234", "v235", "v236", "v237", "v238", "v239", "v253", "a0", "a1", "a2", "a3", "a4", "a5", "a6", "a7", "a8", "a9", "a10", "a11", "a12", "a13", "a14", "a15", "a16", "a17", "a18", "a19", "a20", "a21", "a22", "a23", "a24", "a25", "a26", "a27", "a28", "a29", "a30", "a31", "a32", "a33", "a34", "a35", "a36", "a37", "a38", "a39", "a40", "a41", "a42", "a43", "a44", "a45", "a46", "a47", "a48", "a49", "a50", "a51", "a52", "a53", "a54", "a55", "a56", "a57", "a58", "a59", "a60", "a61", "a62", "a63", "a64", "a65", "a66", "a67", "a68", "a69", "a70", "a71", "a72", "a73", "a74", "a75", "a76", "a77", "a78", "a79", "a80", "a81", "a82", "a83", "a84", "a85", "a86", "a87", "a88", "a89", "a90", "a91", "a92", "a93", "a94", "a95", "a96", "a97", "a98", "a99", "a100", "a101", "a102", "a103", "a104", "a105", "a106", "a107", "a108", "a109", "a110", "a111", "a112", "a113", "a114", "a115", "a116", "a117", "a118", "a119", "a120", "a121", "a122", "a123", "a124", "a125", "a126", "a127", "a128", "a129", "a130", "a131", "a132", "a133", "a134", "a135", "a136", "a137", "a138", "a139", "a140", "a141", "a142", "a143", "a144", "a145", "a146", "a147", "a148", "a149", "a150", "a151", "a152", "a153", "a154", "a155", "a156", "a157", "a158", "a159", "a160", "a161", "a162", "a163", "a164", "a165", "a166", "a167", "a168", "a169", "a170", "a171", "a172", "a173", "a174", "a175", "a176", "a177", "a178", "a179", "a180", "a181", "a182", "a183", "a184", "a185", "a186", "a187", "a188", "a189", "a190", "a191", "a192", "a193", "a194", "a195", "a196", "a197", "a198", "a199", "a200", "a201", "a202", "a203", "a204", "a205", "a206", "a207", "a208", "a209", "a210", "a211", "a212", "a213", "a214", "a215", "a216", "a217", "a218", "a219", "a220", "a221", "a222", "a223", "a224", "a225", "a226", "a227", "a228", "a229", "a230", "a231", "a232", "a233", "a234", "a235", "a236", "a237", "a238", "a239", "a240", "a241", "a242", "a243", "a244", "a245", "a246", "a247", "a248", "a249", "a250", "a251", "a252", "a253", "a254", "a255", "s24", "s25", "s26", "s27", "s28", "s29", "s30", "s31", "s34", "s35", "s38", "s39", "s40", "s41", "s45", "s46", "s48", "s49", "s50", "s51", "s64", "s65", "s66", "s67", "vcc", "scc", "memory");
}


__global__ __launch_bounds__(256, 1) __attribute__((aligned(4096))) void win_attn_kernel(const char* __restrict__ x, const char* __restrict__ wqkv,
                                                          const char* __restrict__ wo, const char* __restrict__ rpb,
                                                          const char* __restrict__ rpi, const char* __restrict__ mask,
                                                          char* __restrict__ out) {
    __shared__ __attribute__((aligned(16))) char smem[163840];
    unsigned tid = threadIdx.x, w;
    const unsigned ldsb = (unsigned)(size_t)smem;
    asm volatile(
        "s_load_dwordx2 s[8:9], s[0:1], 0\n"
        "s_load_dwordx2 s[20:21], s[0:1], 8\n"
        "s_load_dwordx2 s[52:53], s[0:1], 16\n"
        "s_load_dwordx2 s[60:61], s[0:1], 24\n"
        "s_load_dwordx2 s[56:57], s[0:1], 32\n"
        "s_load_dwordx2 s[12:13], s[0:1], 40\n"
        "s_load_dwordx2 s[16:17], s[0:1], 48\n"
        "v_readfirstlane_b32 s42, v0\n"
        "v_and_b32 v1, 63, v0\n"
        "v_lshlrev_b32 v145, 3, v0\n"
        "s_lshr_b32 s42, s42, 6\n"
        "s_bfe_u32 s41, s2, 0x50003\n"
        "s_lshl2_add_u32 s41, s41, s42\n"
        "s_lshl_b32 s49, s41, 8\n"
        "v_and_b32 v10, 1, v0\n"
        "v_lshl_add_u32 v10, v10, 7, s49\n"
        "v_min_u32 v10, 26112, v10\n"
        "s_getpc_b64 s[38:39]\n"
        "global_load_dword v66, v10, s[38:39]\n"
        "v_bfe_u32 v2, v0, 4, 2\n"
        "s_and_b32 s51, s42, 1\n"
        "s_lshl_b32 s66, s51, 3\n"
        "v_xor_b32 v11, v1, v2\n"
        "v_xor_b32 v11, s66, v11\n"
        "v_lshlrev_b32 v139, 4, v11\n"
        "v_xor_b32 v144, 64, v139\n"
        "s_lshl_b32 s34, s42, 12\n"
        "s_mul_i32 s40, s42, 3072\n"
        "s_and_b32 s31, s2, 63\n"
        "s_lshl_b32 s31, s31, 3\n"
        "s_add_u32 s31, s31, 4\n"
        "s_lshr_b32 s45, s2, 6\n"
        "s_lshl_b32 s45, s45, 3\n"
        "s_add_u32 s47, s45, s42\n"
        "s_add_u32 s47, s47, 68\n"
        "s_mul_i32 s47, s47, 196608\n"
        "s_lshl_b32 s46, s42, 1\n"
        "s_lshl_b32 s49, s41, 11\n"
        "v_lshl_add_u32 v148, v1, 7, s49\n"
        "v_lshl_or_b32 v148, v2, 28, v148\n"
        "s_mov_b32 s62, 1800\n"
        "s_mov_b32 s63, 0x20000\n"
        "s_mov_b32 s58, 16384\n"
        "s_mov_b32 s59, 0x20000\n"
        "s_mov_b32 s22, 221184\n"
        "s_mov_b32 s23, 0x20000\n"
        "s_mov_b32 s10, 100663296\n"
        "s_mov_b32 s11, 0x20000\n"
        "v_mul_u32_u24 v15, 171, v1\n"
        "v_lshrrev_b32 v15, 12, v15\n"
        "v_mul_u32_u24 v13, 24, v15\n"
        "v_sub_u32 v18, v1, v13\n"
        "v_bfe_u32 v21, v15, 1, 1\n"
        "v_add_u32 v13, s31, v15\n"
        "v_and_b32 v13, 0x1ff, v13\n"
        "v_mul_u32_u24 v13, 384, v13\n"
        "v_bitop3_b32 v14, v21, v18, s46 bitop3:0x36\n"
        "v_lshl_add_u32 v242, v14, 4, v13\n"
        "v_add_u32 v12, 64, v1\n"
        "v_mul_u32_u24 v16, 171, v12\n"
        "v_lshrrev_b32 v16, 12, v16\n"
        "v_mul_u32_u24 v13, 24, v16\n"
        "v_sub_u32 v19, v12, v13\n"
        "v_bfe_u32 v22, v16, 1, 1\n"
        "v_add_u32 v13, s31, v16\n"
        "v_and_b32 v13, 0x1ff, v13\n"
        "v_mul_u32_u24 v13, 384, v13\n"
        "v_bitop3_b32 v14, v22, v19, s46 bitop3:0x36\n"
        "v_lshl_add_u32 v243, v14, 4, v13\n"
        "v_add_u32 v12, 128, v1\n"
        "v_mul_u32_u24 v17, 171, v12\n"
        "v_lshrrev_b32 v17, 12, v17\n"
        "v_mul_u32_u24 v13, 24, v17\n"
        "v_sub_u32 v20, v12, v13\n"
        "v_bfe_u32 v23, v17, 1, 1\n"
        "v_add_u32 v13, s31, v17\n"
        "v_and_b32 v13, 0x1ff, v13\n"
        "v_mul_u32_u24 v13, 384, v13\n"
        "v_bitop3_b32 v14, v23, v20, s46 bitop3:0x36\n"
        "v_lshl_add_u32 v244, v14, 4, v13\n"
        "s_mov_b32 s14, 33554432\n"
        "s_mov_b32 s15, 0x20000\n"
        "v_bfe_u32 v4, v0, 4, 1\n"
        "s_lshl_b32 s66, s51, 2\n"
        "v_bitop3_b32 v11, v4, v1, s66 bitop3:0x36\n"
        "v_lshlrev_b32 v249, 4, v11\n"
        "s_waitcnt lgkmcnt(0)\n"
        "buffer_load_dword v64, v148, s[20:23], 0 offen\n"
        "buffer_load_dwordx2 v[28:29], v145, s[60:63], 0 offen\n"
        "s_add_u32 m0, s34, 122880\n"
        "s_mov_b32 s64, s34\n"
        "buffer_load_dwordx4 v139, s[56:59], s64 offen lds\n"
        "v_xor_b32 v149, 32, v249\n"
        "s_lshl_b32 s48, s42, 11\n"
        "s_lshl_b32 s36, s2, 13\n"
        "s_add_u32 s36, s36, s48\n"
        "s_add_u32 s36, s36, 4194304\n"
        "s_add_u32 s48, s48, 147456\n"
        "s_mov_b32 s54, 73728\n"
        "s_mov_b32 s55, 0x20000\n"
        "s_mov_b32 s18, 100663296\n"
        "s_mov_b32 s19, 0x20000\n"
        "s_mul_i32 s65, s42, 9216\n"
        "v_lshl_or_b32 v13, v21, 1, s66\n"
        "buffer_load_dwordx4 v139, s[56:59], s64 offen offset:1024 lds\n"
        "v_xor_b32 v13, v13, v18\n"
        "v_mul_u32_u24 v14, 1152, v15\n"
        "v_lshl_add_u32 v128, v13, 4, v14\n"
        "v_lshl_or_b32 v13, v22, 1, s66\n"
        "v_xor_b32 v13, v13, v19\n"
        "v_mul_u32_u24 v14, 1152, v16\n"
        "v_lshl_add_u32 v129, v13, 4, v14\n"
        "v_subrev_u32 v129, 1024, v129\n"
        "v_lshl_or_b32 v13, v23, 1, s66\n"
        "v_xor_b32 v13, v13, v20\n"
        "v_mul_u32_u24 v14, 1152, v17\n"
        "v_lshl_add_u32 v130, v13, 4, v14\n"
        "buffer_load_dwordx4 v144, s[56:59], s64 offen offset:2048 lds\n"
        "v_subrev_u32 v130, 2048, v130\n"
        "v_bfe_u32 v6, v0, 2, 2\n"
        "v_and_b32 v7, 3, v0\n"
        "v_lshlrev_b32 v150, 4, v1\n"
        "v_lshl_add_u32 v8, v6, 3, v7\n"
        "v_and_b32 v9, 0xf0, v150\n"
        "v_lshlrev_b32 v146, 2, v0\n"
        "v_add_u32 v146, 139264, v146\n"
        "v_lshlrev_b32 v10, 5, v2\n"
        "v_xor_b32 v10, v10, v9\n"
        "v_lshl_add_u32 v10, v8, 8, v10\n"
        "v_add_u32 v140, 122880, v10\n"
        "buffer_load_dwordx4 v144, s[56:59], s64 offen offset:3072 lds\n"
        "v_xor_b32 v141, 16, v140\n"
        "v_xor_b32 v142, 128, v140\n"
        "v_xor_b32 v143, 144, v140\n"
        "s_lshl_b32 s67, s42, 10\n"
        "s_add_u32 s67, s67, 139264\n"
        "v_bfe_u32 v10, v0, 1, 3\n"
        "v_xor_b32 v10, v10, v2\n"
        "v_lshlrev_b32 v10, 4, v10\n"
        "v_mul_u32_u24 v11, 384, v8\n"
        "v_add_u32 v11, 73728, v11\n"
        "v_add_u32 v240, v10, v11\n"
        "v_xor_b32 v241, 64, v240\n"
        "s_add_u32 m0, s40, 73728\n"
        "s_sub_u32 s64, s47, 12582912\n"
        "buffer_load_dwordx4 v242, s[8:11], s64 offen nt lds\n"
        "v_lshl_add_u32 v10, v8, 7, v10\n"
        "v_add_u32 v251, 147456, v10\n"
        "v_xor_b32 v252, 64, v251\n"
        "v_lshl_add_u32 v10, v2, 3, v6\n"
        "v_mul_u32_u24 v10, 384, v10\n"
        "v_and_b32 v11, 1, v0\n"
        "v_lshl_add_u32 v10, v11, 3, v10\n"
        "v_and_b32 v12, 2, v6\n"
        "v_lshl_or_b32 v12, v4, 2, v12\n"
        "s_mul_i32 s35, s42, 6\n"
        "v_bfe_u32 v13, v0, 1, 1\n"
        "v_add_u32 v13, s35, v13\n"
        "s_add_u32 m0, s40, 74752\n"
        "s_sub_u32 s64, s47, 12582912\n"
        "buffer_load_dwordx4 v243, s[8:11], s64 offen nt lds\n"
        "v_xor_b32 v14, v13, v12\n"
        "v_lshl_add_u32 v131, v14, 4, v10\n"
        "v_add_u32 v136, 98304, v131\n"
        "v_add_u32 v13, 2, v13\n"
        "v_xor_b32 v14, v13, v12\n"
        "v_lshl_add_u32 v132, v14, 4, v10\n"
        "v_add_u32 v137, 98304, v132\n"
        "v_add_u32 v13, 2, v13\n"
        "v_xor_b32 v14, v13, v12\n"
        "v_lshl_add_u32 v133, v14, 4, v10\n"
        "v_add_u32 v138, 98304, v133\n"
        "v_lshlrev_b32 v10, 4, v11\n"
        "s_add_u32 m0, s40, 75776\n"
        "s_sub_u32 s64, s47, 12582912\n"
        "buffer_load_dwordx4 v244, s[8:11], s64 offen nt lds\n"
        "v_mov_b32 v12, 0x3fb9\n"
        "v_cmp_eq_u32 vcc, v6, v2\n"
        "v_lshlrev_b32 v10, v10, v12\n"
        "v_bfe_i32 v13, v0, 0, 1\n"
        "v_cndmask_b32 v10, 0, v10, vcc\n"
        "v_cmp_gt_u32 vcc, 2, v7\n"
        "v_and_b32 v13, 0xaaaaaaaa, v13\n"
        "v_or_b32 v255, 0x44444444, v13\n"
        "v_cndmask_b32 v170, 0, v10, vcc\n"
        "v_xor_b32 v171, v170, v10\n"
        "v_mov_b64 v[188:189], v[170:171]\n"
        "v_mov_b64 v[210:211], v[170:171]\n"
        "s_add_u32 m0, s40, 86016\n"
        "s_sub_u32 s64, s47, 11796480\n"
        "buffer_load_dwordx4 v242, s[8:11], s64 offen nt lds\n"
        "v_mov_b64 v[228:229], v[170:171]\n"
        "v_bfe_u32 v5, v0, 5, 1\n"
        "v_and_b32 v3, 15, v0\n"
        "v_lshl_add_u32 v10, v2, 10, v9\n"
        "s_add_u32 s49, s34, 73728\n"
        "v_add_u32 v245, s49, v10\n"
        "s_lshl_b32 s49, s51, 9\n"
        "s_add_u32 s49, s49, 73728\n"
        "v_add_u32 v247, s49, v10\n"
        "v_lshl_add_u32 v10, v5, 10, v9\n"
        "v_lshl_add_u32 v10, v4, 3, v10\n"
        "s_lshl_b32 s49, s42, 11\n"
        "s_add_u32 m0, s40, 87040\n"
        "s_sub_u32 s64, s47, 11796480\n"
        "buffer_load_dwordx4 v243, s[8:11], s64 offen nt lds\n"
        "s_add_u32 s49, s49, 90112\n"
        "v_add_u32 v246, s49, v10\n"
        "s_lshr_b32 s35, s42, 1\n"
        "s_mul_i32 s49, s35, 18432\n"
        "v_mul_u32_u24 v12, 1536, v2\n"
        "v_add_u32 v12, s49, v12\n"
        "v_mad_u32_u24 v13, v9, 6, v12\n"
        "v_lshl_add_u32 v248, v4, 4, v13\n"
        "v_mad_i32_i24 v254, v4, -16, v13\n"
        "v_mul_u32_u24 v10, 196608, v2\n"
        "v_lshl_add_u32 v10, v3, 2, v10\n"
        "s_lshl_b32 s49, s35, 6\n"
        "s_add_u32 m0, s40, 88064\n"
        "s_sub_u32 s64, s47, 11796480\n"
        "buffer_load_dwordx4 v244, s[8:11], s64 offen nt lds\n"
        "s_add_u32 s49, s49, 128\n"
        "v_add_u32 v250, s49, v10\n"
        "s_mul_i32 s26, s35, 192\n"
        "s_sub_u32 s26, s26, 128\n"
        "s_mul_i32 s43, s31, 384\n"
        "s_add_u32 s44, s31, 4\n"
        "s_and_b32 s44, s44, 511\n"
        "s_mul_i32 s44, s44, 384\n"
        "s_add_u32 s37, s45, s66\n"
        "s_add_u32 s37, s37, 4\n"
        "s_mul_i32 s37, s37, 196608\n"
        "s_mul_i32 s51, s51, 36864\n"
        "s_mov_b32 m0, s48\n"
        "s_sub_u32 s64, s36, 4194304\n"
        "buffer_load_dwordx4 v249, s[12:15], s64 offen nt lds\n"
        "s_mul_i32 s66, s35, 18432\n"
        "s_add_u32 s66, s66, s51\n"
        "v_add_u32 v12, s51, v12\n"
        "v_bfrev_b32 v14, v0\n"
        "v_bfe_u32 v10, v14, 28, 2\n"
        "v_mad_u32_u24 v10, v10, 6, v7\n"
        "v_bfe_u32 v13, v0, 1, 3\n"
        "v_mad_u32_u24 v13, v13, 6, v11\n"
        "v_add_u32 v13, 4, v13\n"
        "v_xor_b32 v10, v10, v4\n"
        "v_xor_b32 v13, v13, v4\n"
        "v_lshl_add_u32 v236, v10, 4, v12\n"
        "buffer_load_dwordx4 v149, s[12:15], s64 offen offset:1024 nt lds\n"
        "v_lshl_add_u32 v237, v13, 4, v12\n"
        "v_lshrrev_b32 v10, 2, v1\n"
        "v_mul_u32_u24 v10, 192, v10\n"
        "v_add_u32 v10, s66, v10\n"
        "v_lshlrev_b32 v13, 5, v4\n"
        "v_add_u32 v12, v10, v13\n"
        "v_sub_u32 v13, v10, v13\n"
        "v_lshrrev_b32 v14, 30, v14\n"
        "v_lshl_add_u32 v234, v14, 3, v12\n"
        "v_lshl_add_u32 v235, v14, 3, v13\n"
        "v_lshl_add_u32 v238, v7, 3, v12\n"
        "v_lshl_add_u32 v239, v7, 3, v13\n"
        "s_lshl_b32 s46, s35, 8\n"
        "s_mul_i32 s35, s35, -192\n"
        "s_movk_i32 s49, 1536\n"
        "v_mul_u32_u24 v10, 171, v1\n"
        "v_lshrrev_b32 v10, 11, v10\n"
        "v_mad_i32_i24 v13, v10, -12, v1\n"
        "v_lshrrev_b32 v13, 3, v13\n"
        "v_mul_u32_u24 v14, 192, v10\n"
        "v_add_u32 v14, s46, v14\n"
        "v_mad_i32_i24 v14, v13, s35, v14\n"
        "v_and_b32 v13, 4, v10\n"
        "v_lshlrev_b32 v13, 3, v13\n"
        "v_xad_u32 v124, v150, v13, v14\n"
        "v_lshrrev_b32 v13, 2, v10\n"
        "v_and_or_b32 v13, v10, 8, v13\n"
        "v_mad_u32_u24 v127, v13, s49, v124\n"
        "v_add_u32 v12, 64, v1\n"
        "v_mul_u32_u24 v10, 171, v12\n"
        "v_lshrrev_b32 v10, 11, v10\n"
        "v_mad_i32_i24 v13, v10, -12, v12\n"
        "v_lshrrev_b32 v13, 3, v13\n"
        "v_mul_u32_u24 v14, 192, v10\n"
        "v_add_u32 v14, s46, v14\n"
        "v_mad_i32_i24 v14, v13, s35, v14\n"
        "v_and_b32 v13, 4, v10\n"
        "v_lshlrev_b32 v13, 3, v13\n"
        "v_xad_u32 v125, v150, v13, v14\n"
        "v_lshrrev_b32 v13, 2, v10\n"
        "v_and_or_b32 v13, v10, 8, v13\n"
        "v_mad_u32_u24 v232, v13, s49, v125\n"
        "v_add_u32 v12, 128, v1\n"
        "v_mul_u32_u24 v10, 171, v12\n"
        "v_lshrrev_b32 v10, 11, v10\n"
        "v_mad_i32_i24 v13, v10, -12, v12\n"
        "v_lshrrev_b32 v13, 3, v13\n"
        "v_mul_u32_u24 v14, 192, v10\n"
        "v_add_u32 v14, s46, v14\n"
        "v_mad_i32_i24 v14, v13, s35, v14\n"
        "v_and_b32 v13, 4, v10\n"
        "v_lshlrev_b32 v13, 3, v13\n"
        "v_xad_u32 v126, v150, v13, v14\n"
        "v_lshrrev_b32 v13, 2, v10\n"
        "v_and_or_b32 v13, v10, 8, v13\n"
        "v_mad_u32_u24 v233, v13, s49, v126\n"
        "s_mov_b32 s24, 0x3fb8aa3b\n"
        "s_mov_b32 s25, 0x4f800000\n"
        "s_mov_b32 s50, 0x3e553b95\n"
        "s_mov_b32 s27, 14\n"
        "s_mov_b32 s28, 98304\n"
        "s_mov_b32 s30, 73728\n"
        "s_mov_b32 s29, 122880\n"
        "v_mov_b32 v253, 0x3f803f80\n"
        "s_nop 0\n"
        "v_accvgpr_write_b32 a252, v253\n"
        "v_accvgpr_write_b32 a253, v253\n"
        "v_accvgpr_write_b32 a254, v253\n"
        "v_accvgpr_write_b32 a255, v253\n"
        "s_waitcnt vmcnt(12)\n"
        "v_lshlrev_b32 v32, 16, v28\n"
        "v_and_b32 v33, 0xffff0000, v28\n"
        "v_lshlrev_b32 v34, 16, v29\n"
        "v_and_b32 v35, 0xffff0000, v29\n"
        "v_mul_f32 v32, s24, v32\n"
        "v_mul_f32 v33, s24, v33\n"
        "v_mul_f32 v34, s24, v34\n"
        "v_mul_f32 v35, s24, v35\n"
        "ds_write_b32 v146, v32 offset:0\n"
        "ds_write_b32 v146, v33 offset:1024\n"
        "ds_write_b32 v146, v34 offset:2048\n"
        "ds_write_b32 v146, v35 offset:3072\n"
        "s_waitcnt lgkmcnt(0)\n"
        "s_waitcnt vmcnt(8)\n"
        "s_barrier\n"
        "ds_read_b128 v[0:3], v140 offset:0\n"
        "ds_read_b128 v[4:7], v141 offset:0\n"
        "ds_read_b128 v[8:11], v142 offset:0\n"
        "ds_read_b128 v[12:15], v143 offset:0\n"
        "ds_read_b128 v[16:19], v140 offset:1024\n"
        "ds_read_b128 v[20:23], v141 offset:1024\n"
        "ds_read_b128 v[24:27], v142 offset:1024\n"
        "ds_read_b128 v[28:31], v143 offset:1024\n"
        "ds_read_b128 v[32:35], v140 offset:8192\n"
        "ds_read_b128 v[36:39], v141 offset:8192\n"
        "ds_read_b128 v[40:43], v142 offset:8192\n"
        "ds_read_b128 v[44:47], v143 offset:8192\n"
        "ds_read_b128 v[48:51], v140 offset:9216\n"
        "ds_read_b128 v[52:55], v141 offset:9216\n"
        "ds_read_b128 v[56:59], v142 offset:9216\n"
        "ds_read_b128 v[60:63], v143 offset:9216\n"
        "s_add_u32 m0, s40, 0\n"
        "s_add_u32 s64, s65, 0\n"
        "buffer_load_dwordx4 v128, s[20:23], s64 offen lds\n"
        "s_waitcnt lgkmcnt(15)\n"
        "v_lshl_add_u32 v0, v0, 2, s67\n"
        "v_lshl_add_u32 v1, v1, 2, s67\n"
        "v_lshl_add_u32 v2, v2, 2, s67\n"
        "v_lshl_add_u32 v3, v3, 2, s67\n"
        "s_waitcnt lgkmcnt(14)\n"
        "v_lshl_add_u32 v4, v4, 2, s67\n"
        "v_lshl_add_u32 v5, v5, 2, s67\n"
        "v_lshl_add_u32 v6, v6, 2, s67\n"
        "v_lshl_add_u32 v7, v7, 2, s67\n"
        "ds_read_b32 v0, v0\n"
        "ds_read_b32 v1, v1\n"
        "ds_read_b32 v2, v2\n"
        "ds_read_b32 v3, v3\n"
        "ds_read_b32 v4, v4\n"
        "ds_read_b32 v5, v5\n"
        "ds_read_b32 v6, v6\n"
        "ds_read_b32 v7, v7\n"
        "buffer_load_dwordx4 v129, s[20:23], s64 offen offset:1024 lds\n"
        "s_waitcnt lgkmcnt(15)\n"
        "v_lshl_add_u32 v8, v8, 2, s67\n"
        "v_lshl_add_u32 v9, v9, 2, s67\n"
        "v_lshl_add_u32 v10, v10, 2, s67\n"
        "v_lshl_add_u32 v11, v11, 2, s67\n"
        "v_lshl_add_u32 v12, v12, 2, s67\n"
        "v_lshl_add_u32 v13, v13, 2, s67\n"
        "v_lshl_add_u32 v14, v14, 2, s67\n"
        "v_lshl_add_u32 v15, v15, 2, s67\n"
        "ds_read_b32 v8, v8\n"
        "ds_read_b32 v9, v9\n"
        "ds_read_b32 v10, v10\n"
        "ds_read_b32 v11, v11\n"
        "ds_read_b32 v12, v12\n"
        "ds_read_b32 v13, v13\n"
        "ds_read_b32 v14, v14\n"
        "ds_read_b32 v15, v15\n"
        "buffer_load_dwordx4 v130, s[20:23], s64 offen offset:2048 lds\n"
        "v_lshl_add_u32 v16, v16, 2, s67\n"
        "v_lshl_add_u32 v17, v17, 2, s67\n"
        "v_lshl_add_u32 v18, v18, 2, s67\n"
        "v_lshl_add_u32 v19, v19, 2, s67\n"
        "v_lshl_add_u32 v20, v20, 2, s67\n"
        "v_lshl_add_u32 v21, v21, 2, s67\n"
        "v_lshl_add_u32 v22, v22, 2, s67\n"
        "v_lshl_add_u32 v23, v23, 2, s67\n"
        "ds_read_b32 v16, v16\n"
        "ds_read_b32 v17, v17\n"
        "ds_read_b32 v18, v18\n"
        "ds_read_b32 v19, v19\n"
        "ds_read_b32 v20, v20\n"
        "ds_read_b32 v21, v21\n"
        "ds_read_b32 v22, v22\n"
        "ds_read_b32 v23, v23\n"
        "s_add_u32 m0, s40, 12288\n"
        "s_add_u32 s64, s65, 36864\n"
        "buffer_load_dwordx4 v128, s[20:23], s64 offen lds\n"
        "v_lshl_add_u32 v24, v24, 2, s67\n"
        "v_lshl_add_u32 v25, v25, 2, s67\n"
        "v_lshl_add_u32 v26, v26, 2, s67\n"
        "v_lshl_add_u32 v27, v27, 2, s67\n"
        "v_lshl_add_u32 v28, v28, 2, s67\n"
        "v_lshl_add_u32 v29, v29, 2, s67\n"
        "v_lshl_add_u32 v30, v30, 2, s67\n"
        "v_lshl_add_u32 v31, v31, 2, s67\n"
        "ds_read_b32 v24, v24\n"
        "ds_read_b32 v25, v25\n"
        "ds_read_b32 v26, v26\n"
        "ds_read_b32 v27, v27\n"
        "ds_read_b32 v28, v28\n"
        "ds_read_b32 v29, v29\n"
        "ds_read_b32 v30, v30\n"
        "ds_read_b32 v31, v31\n"
        "buffer_load_dwordx4 v129, s[20:23], s64 offen offset:1024 lds\n"
        "buffer_load_dwordx4 v130, s[20:23], s64 offen offset:2048 lds\n"
        "s_mov_b64 exec, 0\n"
        "s_cmp_eq_u32 s42, 0\n"
        "s_cbranch_scc1 41f\n"
        "s_cmp_eq_u32 s42, 1\n"
        "s_cbranch_scc1 42f\n"
        "s_cmp_eq_u32 s42, 2\n"
        "s_cbranch_scc1 43f\n"
        "s_branch 40f\n"
        ".fill 364, 4, 0xbf800000\n"
        "40:\n"
        "s_mov_b64 exec, -1\n"
        "s_add_u32 m0, s40, 24576\n"
        "s_add_u32 s64, s65, 73728\n"
        "buffer_load_dwordx4 v128, s[20:23], s64 offen lds\n"
        "v_lshl_add_u32 v32, v32, 2, s67\n"
        "v_lshl_add_u32 v33, v33, 2, s67\n"
        "v_lshl_add_u32 v34, v34, 2, s67\n"
        "v_lshl_add_u32 v35, v35, 2, s67\n"
        "s_waitcnt lgkmcnt(15)\n"
        "v_lshl_add_u32 v36, v36, 2, s67\n"
        "v_lshl_add_u32 v37, v37, 2, s67\n"
        "v_lshl_add_u32 v38, v38, 2, s67\n"
        "v_lshl_add_u32 v39, v39, 2, s67\n"
        "ds_read_b32 v32, v32\n"
        "ds_read_b32 v33, v33\n"
        "ds_read_b32 v34, v34\n"
        "ds_read_b32 v35, v35\n"
        "ds_read_b32 v36, v36\n"
        "ds_read_b32 v37, v37\n"
        "ds_read_b32 v38, v38\n"
        "ds_read_b32 v39, v39\n"
        "buffer_load_dwordx4 v129, s[20:23], s64 offen offset:1024 lds\n"
        "v_lshl_add_u32 v40, v40, 2, s67\n"
        "v_lshl_add_u32 v41, v41, 2, s67\n"
        "v_lshl_add_u32 v42, v42, 2, s67\n"
        "v_lshl_add_u32 v43, v43, 2, s67\n"
        "v_lshl_add_u32 v44, v44, 2, s67\n"
        "v_lshl_add_u32 v45, v45, 2, s67\n"
        "v_lshl_add_u32 v46, v46, 2, s67\n"
        "v_lshl_add_u32 v47, v47, 2, s67\n"
        "ds_read_b32 v40, v40\n"
        "ds_read_b32 v41, v41\n"
        "ds_read_b32 v42, v42\n"
        "ds_read_b32 v43, v43\n"
        "ds_read_b32 v44, v44\n"
        "ds_read_b32 v45, v45\n"
        "ds_read_b32 v46, v46\n"
        "ds_read_b32 v47, v47\n"
        "buffer_load_dwordx4 v130, s[20:23], s64 offen offset:2048 lds\n"
        "v_lshl_add_u32 v48, v48, 2, s67\n"
        "v_lshl_add_u32 v49, v49, 2, s67\n"
        "v_lshl_add_u32 v50, v50, 2, s67\n"
        "v_lshl_add_u32 v51, v51, 2, s67\n"
        "v_lshl_add_u32 v52, v52, 2, s67\n"
        "v_lshl_add_u32 v53, v53, 2, s67\n"
        "v_lshl_add_u32 v54, v54, 2, s67\n"
        "v_lshl_add_u32 v55, v55, 2, s67\n"
        "ds_read_b32 v48, v48\n"
        "ds_read_b32 v49, v49\n"
        "ds_read_b32 v50, v50\n"
        "ds_read_b32 v51, v51\n"
        "ds_read_b32 v52, v52\n"
        "ds_read_b32 v53, v53\n"
        "ds_read_b32 v54, v54\n"
        "ds_read_b32 v55, v55\n"
        "s_add_u32 m0, s40, 36864\n"
        "s_add_u32 s64, s65, 110592\n"
        "buffer_load_dwordx4 v128, s[20:23], s64 offen lds\n"
        "v_lshl_add_u32 v56, v56, 2, s67\n"
        "v_lshl_add_u32 v57, v57, 2, s67\n"
        "v_lshl_add_u32 v58, v58, 2, s67\n"
        "v_lshl_add_u32 v59, v59, 2, s67\n"
        "v_lshl_add_u32 v60, v60, 2, s67\n"
        "v_lshl_add_u32 v61, v61, 2, s67\n"
        "v_lshl_add_u32 v62, v62, 2, s67\n"
        "v_lshl_add_u32 v63, v63, 2, s67\n"
        "ds_read_b32 v56, v56\n"
        "ds_read_b32 v57, v57\n"
        "ds_read_b32 v58, v58\n"
        "ds_read_b32 v59, v59\n"
        "ds_read_b32 v60, v60\n"
        "ds_read_b32 v61, v61\n"
        "ds_read_b32 v62, v62\n"
        "ds_read_b32 v63, v63\n"
        "buffer_load_dwordx4 v129, s[20:23], s64 offen offset:1024 lds\n"
        "buffer_load_dwordx4 v130, s[20:23], s64 offen offset:2048 lds\n"
        "s_add_u32 m0, s40, 49152\n"
        "s_add_u32 s64, s65, 147456\n"
        "buffer_load_dwordx4 v128, s[20:23], s64 offen lds\n"
        "buffer_load_dwordx4 v129, s[20:23], s64 offen offset:1024 lds\n"
        "buffer_load_dwordx4 v130, s[20:23], s64 offen offset:2048 lds\n"
        "s_add_u32 m0, s40, 61440\n"
        "s_add_u32 s64, s65, 184320\n"
        "buffer_load_dwordx4 v128, s[20:23], s64 offen lds\n"
        "buffer_load_dwordx4 v129, s[20:23], s64 offen offset:1024 lds\n"
        "buffer_load_dwordx4 v130, s[20:23], s64 offen offset:2048 lds\n"
        "s_waitcnt vmcnt(12)\n"
        "s_barrier\n"
        "ds_read_b64_tr_b16 a[0:1], v131 offset:0\n"
        "ds_read_b128 a[144:147], v240 offset:0\n"
        "ds_read_b64_tr_b16 a[2:3], v131 offset:1536\n"
        "ds_read_b64_tr_b16 a[4:5], v131 offset:12288\n"
        "ds_read_b128 a[148:151], v241 offset:0\n"
        "ds_read_b64_tr_b16 a[6:7], v131 offset:13824\n"
        "ds_read_b64_tr_b16 a[24:25], v132 offset:0\n"
        "ds_read_b64_tr_b16 a[26:27], v132 offset:1536\n"
        "ds_read_b64_tr_b16 a[28:29], v132 offset:12288\n"
        "ds_read_b64_tr_b16 a[30:31], v132 offset:13824\n"
        "ds_read_b64_tr_b16 a[48:49], v133 offset:0\n"
        "ds_read_b64_tr_b16 a[50:51], v133 offset:1536\n"
        "ds_read_b64_tr_b16 a[52:53], v133 offset:12288\n"
        "ds_read_b64_tr_b16 a[54:55], v133 offset:13824\n"
        "ds_read_b128 a[160:163], v240 offset:1536\n"
        "ds_read_b128 a[164:167], v241 offset:1536\n"
        "s_waitcnt lgkmcnt(13)\n"
        "v_mfma_f32_16x16x32_bf16 v[96:99], a[0:3], a[144:147], 0\n"
        "s_waitcnt lgkmcnt(10)\n"
        "s_add_u32 m0, s40, 98304\n"
        "s_add_u32 s64, s65, 384\n"
        "ds_read_b128 a[176:179], v240 offset:12288\n"
        "v_mfma_f32_16x16x32_bf16 v[96:99], a[4:7], a[148:151], v[96:99]\n"
        "s_waitcnt lgkmcnt(7)\n"
        "buffer_load_dwordx4 v128, s[20:23], s64 offen lds\n"
        "v_mfma_f32_16x16x32_bf16 v[100:103], a[28:31], a[148:151], 0\n"
        "ds_read_b128 a[180:183], v241 offset:12288\n"
        "v_mfma_f32_16x16x32_bf16 v[100:103], a[24:27], a[144:147], v[100:103]\n"
        "s_waitcnt lgkmcnt(6)\n"
        "ds_read_b128 a[192:195], v240 offset:13824\n"
        "v_mfma_f32_16x16x32_bf16 v[104:107], a[48:51], a[144:147], 0\n"
        "s_waitcnt lgkmcnt(5)\n"
        "ds_read_b128 a[196:199], v241 offset:13824\n"
        "v_mfma_f32_16x16x32_bf16 v[104:107], a[52:55], a[148:151], v[104:107]\n"
        "s_waitcnt lgkmcnt(4)\n"
        "buffer_load_dwordx4 v129, s[20:23], s64 offen offset:1024 lds\n"
        "v_mfma_f32_16x16x32_bf16 v[108:111], a[4:7], a[164:167], 0\n"
        "ds_read_b128 a[152:155], v240 offset:128\n"
        "v_mfma_f32_16x16x32_bf16 v[108:111], a[0:3], a[160:163], v[108:111]\n"
        "ds_read_b128 a[156:159], v241 offset:128\n"
        "v_mfma_f32_16x16x32_bf16 v[112:115], a[24:27], a[160:163], 0\n"
        "ds_read_b128 a[168:171], v240 offset:1664\n"
        "v_mfma_f32_16x16x32_bf16 v[112:115], a[28:31], a[164:167], v[112:115]\n"
        "buffer_load_dwordx4 v130, s[20:23], s64 offen offset:2048 lds\n"
        "v_mfma_f32_16x16x32_bf16 v[116:119], a[52:55], a[164:167], 0\n"
        "s_add_u32 m0, s40, 110592\n"
        "ds_read_b128 a[172:175], v241 offset:1664\n"
        "v_mfma_f32_16x16x32_bf16 v[116:119], a[48:51], a[160:163], v[116:119]\n"
        "s_waitcnt lgkmcnt(7)\n"
        "s_add_u32 s64, s65, 37248\n"
        "v_mfma_f32_16x16x32_bf16 v[120:123], a[0:3], a[176:179], 0\n"
        "s_waitcnt lgkmcnt(6)\n"
        "s_waitcnt vmcnt(9)\n"
        "s_barrier\n"
        "ds_read_b128 a[184:187], v240 offset:12416\n"
        "v_mfma_f32_16x16x32_bf16 v[120:123], a[4:7], a[180:183], v[120:123]\n"
        "buffer_load_dwordx4 v128, s[20:23], s64 offen lds\n"
        "v_mfma_f32_16x16x32_bf16 v[92:95], a[28:31], a[180:183], 0\n"
        "ds_read_b64_tr_b16 a[8:9], v131 offset:24576\n"
        "ds_read_b64_tr_b16 a[10:11], v131 offset:26112\n"
        "v_mfma_f32_16x16x32_bf16 v[92:95], a[24:27], a[176:179], v[92:95]\n"
        "ds_read_b64_tr_b16 a[12:13], v131 offset:36864\n"
        "v_mfma_f32_16x16x32_bf16 v[152:155], a[48:51], a[176:179], 0\n"
        "ds_read_b64_tr_b16 a[14:15], v131 offset:38400\n"
        "v_mfma_f32_16x16x32_bf16 v[152:155], a[52:55], a[180:183], v[152:155]\n"
        "s_waitcnt lgkmcnt(9)\n"
        "buffer_load_dwordx4 v129, s[20:23], s64 offen offset:1024 lds\n"
        "v_mfma_f32_16x16x32_bf16 v[172:175], a[4:7], a[196:199], 0\n"
        "ds_read_b64_tr_b16 a[32:33], v132 offset:24576\n"
        "ds_read_b64_tr_b16 a[34:35], v132 offset:26112\n"
        "v_mfma_f32_16x16x32_bf16 v[172:175], a[0:3], a[192:195], v[172:175]\n"
        "ds_read_b64_tr_b16 a[36:37], v132 offset:36864\n"
        "v_mfma_f32_16x16x32_bf16 v[192:195], a[24:27], a[192:195], 0\n"
        "ds_read_b64_tr_b16 a[38:39], v132 offset:38400\n"
        "v_mfma_f32_16x16x32_bf16 v[192:195], a[28:31], a[196:199], v[192:195]\n"
        "buffer_load_dwordx4 v130, s[20:23], s64 offen offset:2048 lds\n"
        "v_mfma_f32_16x16x32_bf16 v[212:215], a[52:55], a[196:199], 0\n"
        "s_add_u32 m0, s40, 0\n"
        "ds_read_b64_tr_b16 a[56:57], v133 offset:24576\n"
        "ds_read_b64_tr_b16 a[58:59], v133 offset:26112\n"
        "v_mfma_f32_16x16x32_bf16 v[212:215], a[48:51], a[192:195], v[212:215]\n"
        "s_waitcnt lgkmcnt(8)\n"
        "s_add_u32 s64, s65, 74112\n"
        "ds_read_b64_tr_b16 a[60:61], v133 offset:36864\n"
        "v_mfma_f32_16x16x32_bf16 v[96:99], a[8:11], a[152:155], v[96:99]\n"
        "s_waitcnt lgkmcnt(7)\n"
        "ds_read_b64_tr_b16 a[62:63], v133 offset:38400\n"
        "v_mfma_f32_16x16x32_bf16 v[96:99], a[12:15], a[156:159], v[96:99]\n"
        "s_waitcnt lgkmcnt(4)\n"
        "buffer_load_dwordx4 v128, s[20:23], s64 offen lds\n"
        "v_mfma_f32_16x16x32_bf16 v[100:103], a[36:39], a[156:159], v[100:103]\n"
        "ds_read_b128 a[188:191], v241 offset:12416\n"
        "v_mfma_f32_16x16x32_bf16 v[100:103], a[32:35], a[152:155], v[100:103]\n"
        "s_waitcnt lgkmcnt(3)\n"
        "ds_read_b128 a[200:203], v240 offset:13952\n"
        "v_mfma_f32_16x16x32_bf16 v[104:107], a[56:59], a[152:155], v[104:107]\n"
        "s_waitcnt lgkmcnt(2)\n"
        "ds_read_b128 a[204:207], v241 offset:13952\n"
        "v_mfma_f32_16x16x32_bf16 v[104:107], a[60:63], a[156:159], v[104:107]\n"
        "buffer_load_dwordx4 v129, s[20:23], s64 offen offset:1024 lds\n"
        "v_mfma_f32_16x16x32_bf16 v[108:111], a[12:15], a[172:175], v[108:111]\n"
        "ds_read_b128 a[216:219], v240 offset:256\n"
        "v_mfma_f32_16x16x32_bf16 v[108:111], a[8:11], a[168:171], v[108:111]\n"
        "ds_read_b128 a[220:223], v241 offset:256\n"
        "v_mfma_f32_16x16x32_bf16 v[112:115], a[32:35], a[168:171], v[112:115]\n"
        "ds_read_b128 a[224:227], v240 offset:1792\n"
        "v_mfma_f32_16x16x32_bf16 v[112:115], a[36:39], a[172:175], v[112:115]\n"
        "buffer_load_dwordx4 v130, s[20:23], s64 offen offset:2048 lds\n"
        "v_mfma_f32_16x16x32_bf16 v[116:119], a[60:63], a[172:175], v[116:119]\n"
        "s_waitcnt vmcnt(9)\n"
        "s_barrier\n"
        "ds_read_b128 a[228:231], v241 offset:1792\n"
        "v_mfma_f32_16x16x32_bf16 v[116:119], a[56:59], a[168:171], v[116:119]\n"
        "s_add_u32 m0, s40, 12288\n"
        "ds_read_b64_tr_b16 a[16:17], v131 offset:49152\n"
        "v_mfma_f32_16x16x32_bf16 v[120:123], a[8:11], a[184:187], v[120:123]\n"
        "s_waitcnt lgkmcnt(7)\n"
        "s_add_u32 s64, s65, 110976\n"
        "ds_read_b64_tr_b16 a[18:19], v131 offset:50688\n"
        "v_mfma_f32_16x16x32_bf16 v[120:123], a[12:15], a[188:191], v[120:123]\n"
        "buffer_load_dwordx4 v128, s[20:23], s64 offen lds\n"
        "v_mfma_f32_16x16x32_bf16 v[92:95], a[36:39], a[188:191], v[92:95]\n"
        "ds_read_b64_tr_b16 a[20:21], v131 offset:61440\n"
        "ds_read_b64_tr_b16 a[22:23], v131 offset:62976\n"
        "v_mfma_f32_16x16x32_bf16 v[92:95], a[32:35], a[184:187], v[92:95]\n"
        "ds_read_b64_tr_b16 a[40:41], v132 offset:49152\n"
        "v_mfma_f32_16x16x32_bf16 v[152:155], a[56:59], a[184:187], v[152:155]\n"
        "ds_read_b64_tr_b16 a[42:43], v132 offset:50688\n"
        "v_mfma_f32_16x16x32_bf16 v[152:155], a[60:63], a[188:191], v[152:155]\n"
        "s_waitcnt lgkmcnt(10)\n"
        "buffer_load_dwordx4 v129, s[20:23], s64 offen offset:1024 lds\n"
        "v_mfma_f32_16x16x32_bf16 v[172:175], a[12:15], a[204:207], v[172:175]\n"
        "ds_read_b64_tr_b16 a[44:45], v132 offset:61440\n"
        "ds_read_b64_tr_b16 a[46:47], v132 offset:62976\n"
        "v_mfma_f32_16x16x32_bf16 v[172:175], a[8:11], a[200:203], v[172:175]\n"
        "ds_read_b64_tr_b16 a[64:65], v133 offset:49152\n"
        "v_mfma_f32_16x16x32_bf16 v[192:195], a[32:35], a[200:203], v[192:195]\n"
        "ds_read_b64_tr_b16 a[66:67], v133 offset:50688\n"
        "v_mfma_f32_16x16x32_bf16 v[192:195], a[36:39], a[204:207], v[192:195]\n"
        "buffer_load_dwordx4 v130, s[20:23], s64 offen offset:2048 lds\n"
        "v_mfma_f32_16x16x32_bf16 v[212:215], a[60:63], a[204:207], v[212:215]\n"
        "s_add_u32 m0, s40, 24576\n"
        "ds_read_b64_tr_b16 a[68:69], v133 offset:61440\n"
        "ds_read_b64_tr_b16 a[70:71], v133 offset:62976\n"
        "v_mfma_f32_16x16x32_bf16 v[212:215], a[56:59], a[200:203], v[212:215]\n"
        "s_waitcnt lgkmcnt(10)\n"
        "s_add_u32 s64, s65, 147840\n"
        "ds_read_b128 a[232:235], v240 offset:12544\n"
        "v_mfma_f32_16x16x32_bf16 v[96:99], a[16:19], a[216:219], v[96:99]\n"
        "s_waitcnt lgkmcnt(9)\n"
        "ds_read_b128 a[236:239], v241 offset:12544\n"
        "v_mfma_f32_16x16x32_bf16 v[96:99], a[20:23], a[220:223], v[96:99]\n"
        "s_waitcnt lgkmcnt(6)\n"
        "buffer_load_dwordx4 v128, s[20:23], s64 offen lds\n"
        "v_mfma_f32_16x16x32_bf16 v[100:103], a[44:47], a[220:223], v[100:103]\n"
        "ds_read_b128 a[240:243], v240 offset:14080\n"
        "v_mfma_f32_16x16x32_bf16 v[100:103], a[40:43], a[216:219], v[100:103]\n"
        "s_waitcnt lgkmcnt(5)\n"
        "ds_read_b128 a[244:247], v241 offset:14080\n"
        "v_mfma_f32_16x16x32_bf16 v[104:107], a[64:67], a[216:219], v[104:107]\n"
        "s_waitcnt lgkmcnt(4)\n"
        "v_mfma_f32_16x16x32_bf16 v[104:107], a[68:71], a[220:223], v[104:107]\n"
        "buffer_load_dwordx4 v129, s[20:23], s64 offen offset:1024 lds\n"
        "v_mfma_f32_16x16x32_bf16 v[108:111], a[20:23], a[228:231], v[108:111]\n"
        "v_mul_f32 v96, s50, v96\n"
        "v_mul_f32 v97, s50, v97\n"
        "v_mfma_f32_16x16x32_bf16 v[108:111], a[16:19], a[224:227], v[108:111]\n"
        "v_mul_f32 v98, s50, v98\n"
        "v_mul_f32 v99, s50, v99\n"
        "v_mfma_f32_16x16x32_bf16 v[112:115], a[40:43], a[224:227], v[112:115]\n"
        "v_cvt_pk_bf16_f32 v156, v96, v97\n"
        "v_cvt_pk_bf16_f32 v157, v98, v99\n"
        "v_mfma_f32_16x16x32_bf16 v[112:115], a[44:47], a[228:231], v[112:115]\n"
        "s_waitcnt vmcnt(8)\n"
        "s_barrier\n"
        "buffer_load_dwordx4 v130, s[20:23], s64 offen offset:2048 lds\n"
        "v_mfma_f32_16x16x32_bf16 v[116:119], a[68:71], a[228:231], v[116:119]\n"
        "s_add_u32 m0, s40, 36864\n"
        "ds_read_b64_tr_b16 a[72:73], v136 offset:0\n"
        "v_mul_f32 v100, s50, v100\n"
        "v_mfma_f32_16x16x32_bf16 v[116:119], a[64:67], a[224:227], v[116:119]\n"
        "s_waitcnt lgkmcnt(4)\n"
        "s_add_u32 s64, s65, 184704\n"
        "ds_read_b64_tr_b16 a[74:75], v136 offset:1536\n"
        "v_mul_f32 v101, s50, v101\n"
        "v_mfma_f32_16x16x32_bf16 v[120:123], a[16:19], a[232:235], v[120:123]\n"
        "s_waitcnt lgkmcnt(4)\n"
        "ds_read_b64_tr_b16 a[76:77], v136 offset:12288\n"
        "v_mul_f32 v102, s50, v102\n"
        "v_mfma_f32_16x16x32_bf16 v[120:123], a[20:23], a[236:239], v[120:123]\n"
        "buffer_load_dwordx4 v128, s[20:23], s64 offen lds\n"
        "v_mfma_f32_16x16x32_bf16 v[92:95], a[44:47], a[236:239], v[92:95]\n"
        "ds_read_b64_tr_b16 a[78:79], v136 offset:13824\n"
        "ds_read_b64_tr_b16 a[96:97], v137 offset:0\n"
        "v_mfma_f32_16x16x32_bf16 v[92:95], a[40:43], a[232:235], v[92:95]\n"
        "ds_read_b64_tr_b16 a[98:99], v137 offset:1536\n"
        "v_mul_f32 v103, s50, v103\n"
        "v_mfma_f32_16x16x32_bf16 v[152:155], a[64:67], a[232:235], v[152:155]\n"
        "ds_read_b64_tr_b16 a[100:101], v137 offset:12288\n"
        "v_cvt_pk_bf16_f32 v158, v100, v101\n"
        "v_mfma_f32_16x16x32_bf16 v[152:155], a[68:71], a[236:239], v[152:155]\n"
        "s_waitcnt lgkmcnt(7)\n"
        "buffer_load_dwordx4 v129, s[20:23], s64 offen offset:1024 lds\n"
        "v_mfma_f32_16x16x32_bf16 v[172:175], a[20:23], a[244:247], v[172:175]\n"
        "ds_read_b64_tr_b16 a[102:103], v137 offset:13824\n"
        "ds_read_b64_tr_b16 a[120:121], v138 offset:0\n"
        "v_mfma_f32_16x16x32_bf16 v[172:175], a[16:19], a[240:243], v[172:175]\n"
        "ds_read_b64_tr_b16 a[122:123], v138 offset:1536\n"
        "v_cvt_pk_bf16_f32 v159, v102, v103\n"
        "v_mfma_f32_16x16x32_bf16 v[192:195], a[40:43], a[240:243], v[192:195]\n"
        "ds_read_b64_tr_b16 a[124:125], v138 offset:12288\n"
        "v_mul_f32 v104, s50, v104\n"
        "v_mfma_f32_16x16x32_bf16 v[192:195], a[44:47], a[244:247], v[192:195]\n"
        "buffer_load_dwordx4 v130, s[20:23], s64 offen offset:2048 lds\n"
        "v_mfma_f32_16x16x32_bf16 v[212:215], a[68:71], a[244:247], v[212:215]\n"
        "s_add_u32 m0, s40, 49152\n"
        "ds_read_b64_tr_b16 a[126:127], v138 offset:13824\n"
        "v_mul_f32 v105, s50, v105\n"
        "v_mfma_f32_16x16x32_bf16 v[212:215], a[64:67], a[240:243], v[212:215]\n"
        "s_waitcnt lgkmcnt(10)\n"
        "s_add_u32 s64, s65, 768\n"
        "v_mul_f32 v106, s50, v106\n"
        "v_mul_f32 v107, s50, v107\n"
        "v_mfma_f32_16x16x32_bf16 v[64:67], a[72:75], a[144:147], 0\n"
        "s_waitcnt lgkmcnt(8)\n"
        "v_cvt_pk_bf16_f32 v160, v104, v105\n"
        "v_cvt_pk_bf16_f32 v161, v106, v107\n"
        "v_mfma_f32_16x16x32_bf16 v[64:67], a[76:79], a[148:151], v[64:67]\n"
        "s_waitcnt lgkmcnt(4)\n"
        "buffer_load_dwordx4 v128, s[20:23], s64 offen lds\n"
        "v_mfma_f32_16x16x32_bf16 v[68:71], a[100:103], a[148:151], 0\n"
        "v_mul_f32 v108, s50, v108\n"
        "v_mul_f32 v109, s50, v109\n"
        "v_mfma_f32_16x16x32_bf16 v[68:71], a[96:99], a[144:147], v[68:71]\n"
        "s_waitcnt lgkmcnt(2)\n"
        "v_mov_b64 v[166:167], v[160:161]\n"
        "v_mul_f32 v110, s50, v110\n"
        "v_mfma_f32_16x16x32_bf16 v[72:75], a[120:123], a[144:147], 0\n"
        "s_waitcnt lgkmcnt(0)\n"
        "v_mul_f32 v111, s50, v111\n"
        "v_cvt_pk_bf16_f32 v176, v108, v109\n"
        "v_mfma_f32_16x16x32_bf16 v[72:75], a[124:127], a[148:151], v[72:75]\n"
        "buffer_load_dwordx4 v129, s[20:23], s64 offen offset:1024 lds\n"
        "v_mfma_f32_16x16x32_bf16 v[76:79], a[76:79], a[164:167], 0\n"
        "v_cvt_pk_bf16_f32 v177, v110, v111\n"
        "v_mul_f32 v112, s50, v112\n"
        "v_mfma_f32_16x16x32_bf16 v[76:79], a[72:75], a[160:163], v[76:79]\n"
        "v_mul_f32 v113, s50, v113\n"
        "v_mul_f32 v114, s50, v114\n"
        "v_mfma_f32_16x16x32_bf16 v[80:83], a[96:99], a[160:163], 0\n"
        "s_waitcnt vmcnt(8)\n"
        "s_barrier\n"
        "v_mul_f32 v115, s50, v115\n"
        "v_cvt_pk_bf16_f32 v178, v112, v113\n"
        "v_mfma_f32_16x16x32_bf16 v[80:83], a[100:103], a[164:167], v[80:83]\n"
        "buffer_load_dwordx4 v130, s[20:23], s64 offen offset:2048 lds\n"
        "v_mfma_f32_16x16x32_bf16 v[84:87], a[124:127], a[164:167], 0\n"
        "s_add_u32 m0, s40, 61440\n"
        "ds_read_b64_tr_b16 a[80:81], v131 offset:0\n"
        "ds_read_b64_tr_b16 a[82:83], v131 offset:1536\n"
        "v_mfma_f32_16x16x32_bf16 v[84:87], a[120:123], a[160:163], v[84:87]\n"
        "s_add_u32 s64, s65, 37632\n"
        "ds_read_b64_tr_b16 a[84:85], v131 offset:12288\n"
        "v_cvt_pk_bf16_f32 v179, v114, v115\n"
        "v_mfma_f32_16x16x32_bf16 v[88:91], a[72:75], a[176:179], 0\n"
        "ds_read_b64_tr_b16 a[86:87], v131 offset:13824\n"
        "v_mul_f32 v116, s50, v116\n"
        "v_mfma_f32_16x16x32_bf16 v[88:91], a[76:79], a[180:183], v[88:91]\n"
        "buffer_load_dwordx4 v128, s[20:23], s64 offen lds\n"
        "v_mfma_f32_16x16x32_bf16 v[108:111], a[100:103], a[180:183], 0\n"
        "ds_read_b64_tr_b16 a[104:105], v132 offset:0\n"
        "ds_read_b64_tr_b16 a[106:107], v132 offset:1536\n"
        "v_mfma_f32_16x16x32_bf16 v[108:111], a[96:99], a[176:179], v[108:111]\n"
        "ds_read_b64_tr_b16 a[108:109], v132 offset:12288\n"
        "v_mul_f32 v117, s50, v117\n"
        "v_mfma_f32_16x16x32_bf16 v[112:115], a[120:123], a[176:179], 0\n"
        "ds_read_b64_tr_b16 a[110:111], v132 offset:13824\n"
        "v_mul_f32 v118, s50, v118\n"
        "v_mfma_f32_16x16x32_bf16 v[112:115], a[124:127], a[180:183], v[112:115]\n"
        "buffer_load_dwordx4 v129, s[20:23], s64 offen offset:1024 lds\n"
        "v_mfma_f32_16x16x32_bf16 v[96:99], a[76:79], a[196:199], 0\n"
        "ds_read_b64_tr_b16 a[128:129], v133 offset:0\n"
        "ds_read_b64_tr_b16 a[130:131], v133 offset:1536\n"
        "v_mfma_f32_16x16x32_bf16 v[96:99], a[72:75], a[192:195], v[96:99]\n"
        "ds_read_b64_tr_b16 a[132:133], v133 offset:12288\n"
        "v_mul_f32 v119, s50, v119\n"
        "v_mfma_f32_16x16x32_bf16 v[100:103], a[96:99], a[192:195], 0\n"
        "ds_read_b64_tr_b16 a[134:135], v133 offset:13824\n"
        "v_cvt_pk_bf16_f32 v180, v116, v117\n"
        "v_mfma_f32_16x16x32_bf16 v[100:103], a[100:103], a[196:199], v[100:103]\n"
        "buffer_load_dwordx4 v130, s[20:23], s64 offen offset:2048 lds\n"
        "v_mfma_f32_16x16x32_bf16 v[104:107], a[124:127], a[196:199], 0\n"
        "v_cvt_pk_bf16_f32 v181, v118, v119\n"
        "v_mul_f32 v120, s50, v120\n"
        "v_mfma_f32_16x16x32_bf16 v[104:107], a[120:123], a[192:195], v[104:107]\n"
        "s_waitcnt lgkmcnt(10)\n"
        "v_mul_f32 v121, s50, v121\n"
        "v_mov_b64 v[186:187], v[180:181]\n"
        "v_mfma_f32_16x16x32_bf16 v[64:67], a[80:83], a[152:155], v[64:67]\n"
        "s_waitcnt lgkmcnt(8)\n"
        "v_mul_f32 v122, s50, v122\n"
        "v_mul_f32 v123, s50, v123\n"
        "v_mfma_f32_16x16x32_bf16 v[64:67], a[84:87], a[156:159], v[64:67]\n"
        "s_waitcnt lgkmcnt(4)\n"
        "v_cvt_pk_bf16_f32 v196, v120, v121\n"
        "v_cvt_pk_bf16_f32 v197, v122, v123\n"
        "v_mfma_f32_16x16x32_bf16 v[68:71], a[108:111], a[156:159], v[68:71]\n"
        "v_mul_f32 v92, s50, v92\n"
        "v_mul_f32 v93, s50, v93\n"
        "v_mfma_f32_16x16x32_bf16 v[68:71], a[104:107], a[152:155], v[68:71]\n"
        "s_waitcnt lgkmcnt(2)\n"
        "v_mul_f32 v94, s50, v94\n"
        "v_mul_f32 v95, s50, v95\n"
        "v_mfma_f32_16x16x32_bf16 v[72:75], a[128:131], a[152:155], v[72:75]\n"
        "s_waitcnt lgkmcnt(0)\n"
        "v_cvt_pk_bf16_f32 v198, v92, v93\n"
        "v_cvt_pk_bf16_f32 v199, v94, v95\n"
        "v_mfma_f32_16x16x32_bf16 v[72:75], a[132:135], a[156:159], v[72:75]\n"
        "v_mul_f32 v152, s50, v152\n"
        "v_mul_f32 v153, s50, v153\n"
        "v_mfma_f32_16x16x32_bf16 v[76:79], a[84:87], a[172:175], v[76:79]\n"
        "v_mul_f32 v154, s50, v154\n"
        "v_mul_f32 v155, s50, v155\n"
        "v_mfma_f32_16x16x32_bf16 v[76:79], a[80:83], a[168:171], v[76:79]\n"
        "v_cvt_pk_bf16_f32 v200, v152, v153\n"
        "v_cvt_pk_bf16_f32 v201, v154, v155\n"
        "v_mfma_f32_16x16x32_bf16 v[80:83], a[104:107], a[168:171], v[80:83]\n"
        "s_waitcnt vmcnt(6)\n"
        "s_barrier\n"
        "s_add_u32 m0, s40, 0\n"
        "v_mul_f32 v172, s50, v172\n"
        "v_mfma_f32_16x16x32_bf16 v[80:83], a[108:111], a[172:175], v[80:83]\n"
        "s_add_u32 s64, s65, 74496\n"
        "ds_read_b64_tr_b16 a[88:89], v131 offset:24576\n"
        "v_mul_f32 v173, s50, v173\n"
        "v_mfma_f32_16x16x32_bf16 v[84:87], a[132:135], a[172:175], v[84:87]\n"
        "ds_read_b64_tr_b16 a[90:91], v131 offset:26112\n"
        "v_mov_b64 v[206:207], v[200:201]\n"
        "v_mfma_f32_16x16x32_bf16 v[84:87], a[128:131], a[168:171], v[84:87]\n"
        "buffer_load_dwordx4 v128, s[20:23], s64 offen lds\n"
        "v_mfma_f32_16x16x32_bf16 v[88:91], a[80:83], a[184:187], v[88:91]\n"
        "ds_read_b64_tr_b16 a[92:93], v131 offset:36864\n"
        "ds_read_b64_tr_b16 a[94:95], v131 offset:38400\n"
        "v_mfma_f32_16x16x32_bf16 v[88:91], a[84:87], a[188:191], v[88:91]\n"
        "ds_read_b64_tr_b16 a[112:113], v132 offset:24576\n"
        "v_mul_f32 v174, s50, v174\n"
        "v_mfma_f32_16x16x32_bf16 v[108:111], a[108:111], a[188:191], v[108:111]\n"
        "ds_read_b64_tr_b16 a[114:115], v132 offset:26112\n"
        "v_mul_f32 v175, s50, v175\n"
        "v_mfma_f32_16x16x32_bf16 v[108:111], a[104:107], a[184:187], v[108:111]\n"
        "buffer_load_dwordx4 v129, s[20:23], s64 offen offset:1024 lds\n"
        "v_mfma_f32_16x16x32_bf16 v[112:115], a[128:131], a[184:187], v[112:115]\n"
        "ds_read_b64_tr_b16 a[116:117], v132 offset:36864\n"
        "ds_read_b64_tr_b16 a[118:119], v132 offset:38400\n"
        "v_mfma_f32_16x16x32_bf16 v[112:115], a[132:135], a[188:191], v[112:115]\n"
        "ds_read_b64_tr_b16 a[136:137], v133 offset:24576\n"
        "v_cvt_pk_bf16_f32 v216, v172, v173\n"
        "v_mfma_f32_16x16x32_bf16 v[96:99], a[84:87], a[204:207], v[96:99]\n"
        "ds_read_b64_tr_b16 a[138:139], v133 offset:26112\n"
        "v_cvt_pk_bf16_f32 v217, v174, v175\n"
        "v_mfma_f32_16x16x32_bf16 v[96:99], a[80:83], a[200:203], v[96:99]\n"
        "buffer_load_dwordx4 v130, s[20:23], s64 offen offset:2048 lds\n"
        "v_mfma_f32_16x16x32_bf16 v[100:103], a[104:107], a[200:203], v[100:103]\n"
        "s_add_u32 m0, s40, 12288\n"
        "ds_read_b64_tr_b16 a[140:141], v133 offset:36864\n"
        "ds_read_b64_tr_b16 a[142:143], v133 offset:38400\n"
        "v_mfma_f32_16x16x32_bf16 v[100:103], a[108:111], a[204:207], v[100:103]\n"
        "s_add_u32 s64, s65, 111360\n"
        "v_mul_f32 v192, s50, v192\n"
        "v_mul_f32 v193, s50, v193\n"
        "v_mfma_f32_16x16x32_bf16 v[104:107], a[132:135], a[204:207], v[104:107]\n"
        "buffer_load_dwordx4 v128, s[20:23], s64 offen lds\n"
        "v_mfma_f32_16x16x32_bf16 v[104:107], a[128:131], a[200:203], v[104:107]\n"
        "s_waitcnt lgkmcnt(10)\n"
        "v_mul_f32 v194, s50, v194\n"
        "v_mul_f32 v195, s50, v195\n"
        "v_mfma_f32_16x16x32_bf16 v[64:67], a[88:91], a[216:219], v[64:67]\n"
        "s_waitcnt lgkmcnt(8)\n"
        "v_cvt_pk_bf16_f32 v218, v192, v193\n"
        "v_cvt_pk_bf16_f32 v219, v194, v195\n"
        "v_mfma_f32_16x16x32_bf16 v[64:67], a[92:95], a[220:223], v[64:67]\n"
        "s_waitcnt lgkmcnt(4)\n"
        "v_mul_f32 v212, s50, v212\n"
        "v_mul_f32 v213, s50, v213\n"
        "v_mfma_f32_16x16x32_bf16 v[68:71], a[116:119], a[220:223], v[68:71]\n"
        "buffer_load_dwordx4 v129, s[20:23], s64 offen offset:1024 lds\n"
        "v_mfma_f32_16x16x32_bf16 v[68:71], a[112:115], a[216:219], v[68:71]\n"
        "s_waitcnt lgkmcnt(2)\n"
        "v_mul_f32 v214, s50, v214\n"
        "v_mul_f32 v215, s50, v215\n"
        "v_mfma_f32_16x16x32_bf16 v[72:75], a[136:139], a[216:219], v[72:75]\n"
        "s_waitcnt lgkmcnt(0)\n"
        "v_cvt_pk_bf16_f32 v220, v212, v213\n"
        "v_cvt_pk_bf16_f32 v221, v214, v215\n"
        "v_mfma_f32_16x16x32_bf16 v[72:75], a[140:143], a[220:223], v[72:75]\n"
        "v_mov_b64 v[226:227], v[220:221]\n"
        "v_cvt_pk_bf16_f32 v152, v64, v65\n"
        "v_mfma_f32_16x16x32_bf16 v[76:79], a[92:95], a[228:231], v[76:79]\n"
        "buffer_load_dwordx4 v130, s[20:23], s64 offen offset:2048 lds\n"
        "v_mfma_f32_16x16x32_bf16 v[76:79], a[88:91], a[224:227], v[76:79]\n"
        "v_cvt_pk_bf16_f32 v153, v66, v67\n"
        "v_cvt_pk_bf16_f32 v154, v68, v69\n"
        "v_mfma_f32_16x16x32_bf16 v[80:83], a[112:115], a[224:227], v[80:83]\n"
        "v_cvt_pk_bf16_f32 v155, v70, v71\n"
        "v_cvt_pk_bf16_f32 v168, v72, v73\n"
        "v_mfma_f32_16x16x32_bf16 v[80:83], a[116:119], a[228:231], v[80:83]\n"
        "ds_read_b128 v[162:165], v251 offset:0\n"
        "v_cvt_pk_bf16_f32 v169, v74, v75\n"
        "v_mfma_f32_16x16x32_bf16 v[84:87], a[140:143], a[228:231], v[84:87]\n"
        "v_cvt_pk_bf16_f32 v172, v76, v77\n"
        "v_cvt_pk_bf16_f32 v173, v78, v79\n"
        "v_mfma_f32_16x16x32_bf16 v[84:87], a[136:139], a[224:227], v[84:87]\n"
        "ds_read_b128 v[182:185], v251 offset:512\n"
        "v_mfma_f32_16x16x32_bf16 v[88:91], a[88:91], a[232:235], v[88:91]\n"
        "s_waitcnt vmcnt(6)\n"
        "s_barrier\n"
        "s_add_u32 m0, s40, 24576\n"
        "v_cvt_pk_bf16_f32 v174, v80, v81\n"
        "v_mfma_f32_16x16x32_bf16 v[88:91], a[92:95], a[236:239], v[88:91]\n"
        "s_add_u32 s64, s65, 148224\n"
        "ds_read_b64_tr_b16 a[144:145], v131 offset:49152\n"
        "v_cvt_pk_bf16_f32 v175, v82, v83\n"
        "v_mfma_f32_16x16x32_bf16 v[108:111], a[116:119], a[236:239], v[108:111]\n"
        "ds_read_b64_tr_b16 a[146:147], v131 offset:50688\n"
        "v_cvt_pk_bf16_f32 v190, v84, v85\n"
        "v_mfma_f32_16x16x32_bf16 v[108:111], a[112:115], a[232:235], v[108:111]\n"
        "buffer_load_dwordx4 v128, s[20:23], s64 offen lds\n"
        "v_mfma_f32_16x16x32_bf16 v[112:115], a[136:139], a[232:235], v[112:115]\n"
        "ds_read_b64_tr_b16 a[148:149], v131 offset:61440\n"
        "ds_read_b64_tr_b16 a[150:151], v131 offset:62976\n"
        "v_mfma_f32_16x16x32_bf16 v[112:115], a[140:143], a[236:239], v[112:115]\n"
        "ds_read_b64_tr_b16 a[168:169], v132 offset:49152\n"
        "v_cvt_pk_bf16_f32 v191, v86, v87\n"
        "v_mfma_f32_16x16x32_bf16 v[96:99], a[92:95], a[244:247], v[96:99]\n"
        "ds_read_b64_tr_b16 a[170:171], v132 offset:50688\n"
        "v_cvt_pk_bf16_f32 v192, v88, v89\n"
        "v_mfma_f32_16x16x32_bf16 v[96:99], a[88:91], a[240:243], v[96:99]\n"
        "buffer_load_dwordx4 v129, s[20:23], s64 offen offset:1024 lds\n"
        "v_mfma_f32_16x16x32_bf16 v[100:103], a[112:115], a[240:243], v[100:103]\n"
        "ds_read_b64_tr_b16 a[172:173], v132 offset:61440\n"
        "ds_read_b64_tr_b16 a[174:175], v132 offset:62976\n"
        "v_mfma_f32_16x16x32_bf16 v[100:103], a[116:119], a[244:247], v[100:103]\n"
        "ds_read_b64_tr_b16 a[192:193], v133 offset:49152\n"
        "v_cvt_pk_bf16_f32 v193, v90, v91\n"
        "v_mfma_f32_16x16x32_bf16 v[104:107], a[140:143], a[244:247], v[104:107]\n"
        "41:\n"
        "s_cbranch_execz 44f\n"
        "ds_read_b64_tr_b16 a[194:195], v133 offset:50688\n"
        "ds_read_b128 v[202:205], v251 offset:4096\n"
        "v_mfma_f32_16x16x32_bf16 v[104:107], a[136:139], a[240:243], v[104:107]\n"
        "buffer_load_dwordx4 v130, s[20:23], s64 offen offset:2048 lds\n"
        "v_mfma_f32_16x16x32_bf16 v[64:67], v[152:155], v[156:159], v[0:3]\n"
        "s_waitcnt lgkmcnt(12)\n"
        "s_add_u32 m0, s40, 36864\n"
        "ds_read_b64_tr_b16 a[196:197], v133 offset:61440\n"
        "ds_read_b64_tr_b16 a[198:199], v133 offset:62976\n"
        "v_mfma_f32_16x16x32_bf16 v[64:67], v[168:171], v[160:163], v[64:67]\n"
        "s_add_u32 s64, s65, 185088\n"
        "v_cvt_pk_bf16_f32 v194, v108, v109\n"
        "v_cvt_pk_bf16_f32 v195, v110, v111\n"
        "v_mfma_f32_16x16x32_bf16 v[68:71], v[172:175], v[156:159], v[4:7]\n"
        "buffer_load_dwordx4 v128, s[20:23], s64 offen lds\n"
        "v_mfma_f32_16x16x32_bf16 v[68:71], v[188:191], v[164:167], v[68:71]\n"
        "ds_read_b128 v[222:225], v251 offset:4608\n"
        "v_cvt_pk_bf16_f32 v208, v112, v113\n"
        "v_mfma_f32_16x16x32_bf16 v[72:75], v[152:155], v[176:179], v[16:19]\n"
        "s_waitcnt lgkmcnt(14)\n"
        "v_cvt_pk_bf16_f32 v209, v114, v115\n"
        "v_cvt_pk_bf16_f32 v212, v96, v97\n"
        "v_mfma_f32_16x16x32_bf16 v[72:75], v[168:171], v[180:183], v[72:75]\n"
        "v_cvt_pk_bf16_f32 v213, v98, v99\n"
        "v_cvt_pk_bf16_f32 v214, v100, v101\n"
        "v_mfma_f32_16x16x32_bf16 v[76:79], v[172:175], v[176:179], v[20:23]\n"
        "buffer_load_dwordx4 v129, s[20:23], s64 offen offset:1024 lds\n"
        "v_mfma_f32_16x16x32_bf16 v[76:79], v[188:191], v[184:187], v[76:79]\n"
        "ds_read_b128 v[162:165], v252 offset:0\n"
        "v_cvt_pk_bf16_f32 v215, v102, v103\n"
        "v_mfma_f32_16x16x32_bf16 v[80:83], v[152:155], v[196:199], v[32:35]\n"
        "s_waitcnt lgkmcnt(4)\n"
        "ds_read_b128 v[182:185], v252 offset:512\n"
        "v_cvt_pk_bf16_f32 v230, v104, v105\n"
        "v_mfma_f32_16x16x32_bf16 v[80:83], v[168:171], v[200:203], v[80:83]\n"
        "ds_read_b128 a[248:251], v240 offset:0\n"
        "v_cvt_pk_bf16_f32 v231, v106, v107\n"
        "v_mfma_f32_16x16x32_bf16 v[84:87], v[172:175], v[196:199], v[36:39]\n"
        "buffer_load_dwordx4 v130, s[20:23], s64 offen offset:2048 lds\n"
        "v_mfma_f32_16x16x32_bf16 v[84:87], v[188:191], v[204:207], v[84:87]\n"
        "s_add_u32 m0, s40, 98304\n"
        "ds_read_b128 v[202:205], v252 offset:4096\n"
        "v_mfma_f32_16x16x32_bf16 v[88:91], v[152:155], v[216:219], v[48:51]\n"
        "s_waitcnt lgkmcnt(4)\n"
        "s_waitcnt lgkmcnt(5)\n"
        "s_waitcnt vmcnt(6)\n"
        "s_barrier\n"
        "v_exp_f32 v64, v64\n"
        "v_mfma_f32_16x16x32_bf16 v[88:91], v[168:171], v[220:223], v[88:91]\n"
        "ds_read_b64_tr_b16 a[152:153], v131 offset:0\n"
        "ds_read_b128 a[216:219], v241 offset:0\n"
        "v_mfma_f32_16x16x32_bf16 v[92:95], v[172:175], v[216:219], v[52:55]\n"
        "ds_read_b64_tr_b16 a[154:155], v131 offset:1536\n"
        "ds_read_b128 a[220:223], v240 offset:128\n"
        "v_mfma_f32_16x16x32_bf16 v[92:95], v[188:191], v[224:227], v[92:95]\n"
        "buffer_load_dword v148, s[52:55], 0 offen lds\n"
        "v_mfma_f32_16x16x32_bf16 v[108:111], v[212:215], v[156:159], v[12:15]\n"
        "s_waitcnt lgkmcnt(7)\n"
        "s_add_u32 m0, s40, 98304\n"
        "s_sub_u32 s64, s47, 6291456\n"
        "ds_read_b64_tr_b16 a[156:157], v131 offset:12288\n"
        "v_mfma_f32_16x16x32_bf16 v[108:111], v[228:231], v[164:167], v[108:111]\n"
        "ds_read_b128 a[224:227], v241 offset:128\n"
        "ds_read_b64_tr_b16 a[158:159], v131 offset:13824\n"
        "v_mfma_f32_16x16x32_bf16 v[112:115], v[212:215], v[176:179], v[28:31]\n"
        "s_waitcnt lgkmcnt(9)\n"
        "buffer_load_dwordx4 v242, s[8:11], s64 offen lds\n"
        "v_mfma_f32_16x16x32_bf16 v[112:115], v[228:231], v[184:187], v[112:115]\n"
        "s_add_u32 m0, s40, 99328\n"
        "s_sub_u32 s64, s47, 6291456\n"
        "ds_read_b64_tr_b16 a[176:177], v132 offset:0\n"
        "v_mfma_f32_16x16x32_bf16 v[116:119], v[212:215], v[196:199], v[44:47]\n"
        "s_waitcnt lgkmcnt(8)\n"
        "ds_read_b64_tr_b16 a[178:179], v132 offset:1536\n"
        "ds_read_b64_tr_b16 a[180:181], v132 offset:12288\n"
        "v_mfma_f32_16x16x32_bf16 v[116:119], v[228:231], v[204:207], v[116:119]\n"
        "ds_read_b64_tr_b16 a[182:183], v132 offset:13824\n"
        "ds_read_b64_tr_b16 a[200:201], v133 offset:0\n"
        "v_mfma_f32_16x16x32_bf16 v[120:123], a[248:251], a[144:147], 0\n"
        "s_waitcnt lgkmcnt(10)\n"
        "buffer_load_dwordx4 v243, s[8:11], s64 offen lds\n"
        "v_mfma_f32_16x16x32_bf16 v[120:123], a[216:219], a[148:151], v[120:123]\n"
        "s_waitcnt lgkmcnt(8)\n"
        "s_add_u32 m0, s40, 100352\n"
        "s_sub_u32 s64, s47, 6291456\n"
        "ds_read_b64_tr_b16 a[202:203], v133 offset:1536\n"
        "v_mfma_f32_16x16x32_bf16 v[120:123], a[220:223], a[152:155], v[120:123]\n"
        "s_waitcnt lgkmcnt(6)\n"
        "ds_read_b64_tr_b16 a[204:205], v133 offset:12288\n"
        "ds_read_b64_tr_b16 a[206:207], v133 offset:13824\n"
        "v_mfma_f32_16x16x32_bf16 v[120:123], a[224:227], a[156:159], v[120:123]\n"
        "s_waitcnt lgkmcnt(4)\n"
        "buffer_load_dwordx4 v244, s[8:11], s64 offen lds\n"
        "v_mfma_f32_16x16x32_bf16 v[96:99], a[224:227], a[180:183], 0\n"
        "s_add_u32 m0, s40, 110592\n"
        "ds_read_b128 a[228:231], v240 offset:1536\n"
        "v_mfma_f32_16x16x32_bf16 v[96:99], a[220:223], a[176:179], v[96:99]\n"
        "s_sub_u32 s64, s47, 5505024\n"
        "ds_read_b128 a[232:235], v241 offset:1536\n"
        "v_mfma_f32_16x16x32_bf16 v[96:99], a[216:219], a[172:175], v[96:99]\n"
        "buffer_load_dwordx4 v242, s[8:11], s64 offen lds\n"
        "v_mfma_f32_16x16x32_bf16 v[96:99], a[248:251], a[168:171], v[96:99]\n"
        "s_add_u32 m0, s40, 111616\n"
        "ds_read_b128 a[236:239], v240 offset:1664\n"
        "ds_read_b128 a[240:243], v241 offset:1664\n"
        "v_mfma_f32_16x16x32_bf16 v[100:103], a[248:251], a[192:195], 0\n"
        "s_sub_u32 s64, s47, 5505024\n"
        "v_exp_f32 v65, v65\n"
        "v_mfma_f32_16x16x32_bf16 v[100:103], a[216:219], a[196:199], v[100:103]\n"
        "s_waitcnt lgkmcnt(6)\n"
        "v_exp_f32 v66, v66\n"
        "v_mfma_f32_16x16x32_bf16 v[100:103], a[220:223], a[200:203], v[100:103]\n"
        "s_waitcnt lgkmcnt(4)\n"
        "buffer_load_dwordx4 v243, s[8:11], s64 offen lds\n"
        "v_mfma_f32_16x16x32_bf16 v[100:103], a[224:227], a[204:207], v[100:103]\n"
        "s_add_u32 m0, s40, 112640\n"
        "ds_read_b128 a[244:247], v240 offset:12288\n"
        "v_cvt_pk_bf16_f32 v64, v64, v65\n"
        "v_exp_f32 v68, v68\n"
        "v_exp_f32 v67, v67\n"
        "v_exp_f32 v69, v69\n"
        "v_cvt_pk_bf16_f32 v65, v66, v67\n"
        "v_exp_f32 v70, v70\n"
        "v_cvt_pk_bf16_f32 v66, v68, v69\n"
        "v_exp_f32 v71, v71\n"
        "v_exp_f32 v72, v72\n"
        "v_cvt_pk_bf16_f32 v67, v70, v71\n"
        "v_mfma_f32_16x16x32_bf16 v[68:71], v[192:195], v[156:159], v[8:11]\n"
        "s_sub_u32 s64, s47, 5505024\n"
        "v_exp_f32 v73, v73\n"
        "v_mfma_f32_16x16x32_bf16 v[68:71], v[208:211], v[160:163], v[68:71]\n"
        "s_waitcnt lgkmcnt(1)\n"
        "v_exp_f32 v74, v74\n"
        "v_mfma_f32_16x16x32_bf16 v[104:107], a[240:243], a[156:159], 0\n"
        "buffer_load_dwordx4 v244, s[8:11], s64 offen lds\n"
        "v_mfma_f32_16x16x32_bf16 v[104:107], a[236:239], a[152:155], v[104:107]\n"
        "s_xor_b32 m0, s48, 8192\n"
        "ds_read_b128 a[248:251], v241 offset:12288\n"
        "v_cvt_pk_bf16_f32 v72, v72, v73\n"
        "v_mfma_f32_16x16x32_bf16 v[104:107], a[232:235], a[148:151], v[104:107]\n"
        "s_sub_u32 s64, s36, 2097152\n"
        "v_exp_f32 v75, v75\n"
        "v_mfma_f32_16x16x32_bf16 v[104:107], a[228:231], a[144:147], v[104:107]\n"
        "ds_read_b128 a[216:219], v240 offset:12416\n"
        "v_cvt_pk_bf16_f32 v73, v74, v75\n"
        "v_mfma_f32_16x16x32_bf16 v[156:159], a[228:231], a[168:171], 0\n"
        "s_waitcnt vmcnt(7)\n"
        "s_barrier\n"
        "buffer_load_dwordx4 v249, s[12:15], s64 offen lds\n"
        "v_mfma_f32_16x16x32_bf16 v[156:159], a[232:235], a[172:175], v[156:159]\n"
        "ds_read_b64_tr_b16 a[160:161], v131 offset:24576\n"
        "v_mfma_f32_16x16x32_bf16 v[156:159], a[236:239], a[176:179], v[156:159]\n"
        "ds_read_b64_tr_b16 a[162:163], v131 offset:26112\n"
        "v_mfma_f32_16x16x32_bf16 v[156:159], a[240:243], a[180:183], v[156:159]\n"
        "ds_read_b64_tr_b16 a[164:165], v131 offset:36864\n"
        "ds_read_b128 a[220:223], v241 offset:12416\n"
        "v_mfma_f32_16x16x32_bf16 v[160:163], a[240:243], a[204:207], 0\n"
        "ds_read_b64_tr_b16 a[166:167], v131 offset:38400\n"
        "v_mfma_f32_16x16x32_bf16 v[160:163], a[236:239], a[200:203], v[160:163]\n"
        "buffer_load_dwordx4 v149, s[12:15], s64 offen offset:1024 lds\n"
        "v_mfma_f32_16x16x32_bf16 v[160:163], a[232:235], a[196:199], v[160:163]\n"
        "ds_read_b64_tr_b16 a[184:185], v132 offset:24576\n"
        "ds_read_b64_tr_b16 a[186:187], v132 offset:26112\n"
        "v_mfma_f32_16x16x32_bf16 v[160:163], a[228:231], a[192:195], v[160:163]\n"
        "ds_read_b64_tr_b16 a[188:189], v132 offset:36864\n"
        "v_exp_f32 v76, v76\n"
        "v_exp_f32 v77, v77\n"
        "v_exp_f32 v78, v78\n"
        "v_cvt_pk_bf16_f32 v74, v76, v77\n"
        "v_exp_f32 v79, v79\n"
        "v_exp_f32 v80, v80\n"
        "v_cvt_pk_bf16_f32 v75, v78, v79\n"
        "v_mfma_f32_16x16x32_bf16 v[76:79], v[192:195], v[176:179], v[24:27]\n"
        "ds_read_b64_tr_b16 a[190:191], v132 offset:38400\n"
        "v_mfma_f32_16x16x32_bf16 v[76:79], v[208:211], v[180:183], v[76:79]\n"
        "s_waitcnt lgkmcnt(11)\n"
        "ds_read_b64_tr_b16 a[208:209], v133 offset:24576\n"
        "v_mfma_f32_16x16x32_bf16 v[164:167], a[244:247], a[144:147], 0\n"
        "s_waitcnt lgkmcnt(11)\n"
        "ds_read_b64_tr_b16 a[210:211], v133 offset:26112\n"
        "v_mfma_f32_16x16x32_bf16 v[164:167], a[248:251], a[148:151], v[164:167]\n"
        "s_waitcnt lgkmcnt(11)\n"
        "ds_read_b64_tr_b16 a[212:213], v133 offset:36864\n"
        "v_mfma_f32_16x16x32_bf16 v[164:167], a[216:219], a[152:155], v[164:167]\n"
        "s_waitcnt lgkmcnt(8)\n"
        "ds_read_b64_tr_b16 a[214:215], v133 offset:38400\n"
        "v_mfma_f32_16x16x32_bf16 v[164:167], a[220:223], a[156:159], v[164:167]\n"
        "v_exp_f32 v81, v81\n"
        "v_mfma_f32_16x16x32_bf16 v[176:179], a[220:223], a[180:183], 0\n"
        "ds_read_b128 a[224:227], v240 offset:13824\n"
        "v_cvt_pk_bf16_f32 v80, v80, v81\n"
        "v_mfma_f32_16x16x32_bf16 v[176:179], a[216:219], a[176:179], v[176:179]\n"
        "ds_read_b128 a[228:231], v241 offset:13824\n"
        "v_mfma_f32_16x16x32_bf16 v[176:179], a[248:251], a[172:175], v[176:179]\n"
        "ds_read_b128 a[232:235], v240 offset:13952\n"
        "v_mfma_f32_16x16x32_bf16 v[176:179], a[244:247], a[168:171], v[176:179]\n"
        "ds_read_b128 a[236:239], v241 offset:13952\n"
        "v_mfma_f32_16x16x32_bf16 v[180:183], a[244:247], a[192:195], 0\n"
        "v_exp_f32 v82, v82\n"
        "v_mfma_f32_16x16x32_bf16 v[180:183], a[248:251], a[196:199], v[180:183]\n"
        "v_exp_f32 v83, v83\n"
        "v_mfma_f32_16x16x32_bf16 v[180:183], a[216:219], a[200:203], v[180:183]\n"
        "ds_read_b128 v[222:225], v252 offset:4608\n"
        "v_cvt_pk_bf16_f32 v81, v82, v83\n"
        "v_mfma_f32_16x16x32_bf16 v[180:183], a[220:223], a[204:207], v[180:183]\n"
        "v_exp_f32 v84, v84\n"
        "v_exp_f32 v85, v85\n"
        "v_exp_f32 v86, v86\n"
        "v_cvt_pk_bf16_f32 v82, v84, v85\n"
        "v_exp_f32 v87, v87\n"
        "v_exp_f32 v88, v88\n"
        "v_cvt_pk_bf16_f32 v83, v86, v87\n"
        "v_mfma_f32_16x16x32_bf16 v[84:87], v[192:195], v[196:199], v[40:43]\n"
        "v_exp_f32 v89, v89\n"
        "v_mfma_f32_16x16x32_bf16 v[84:87], v[208:211], v[200:203], v[84:87]\n"
        "s_waitcnt lgkmcnt(1)\n"
        "v_exp_f32 v90, v90\n"
        "v_mfma_f32_16x16x32_bf16 v[184:187], a[236:239], a[156:159], 0\n"
        "ds_read_b128 a[240:243], v240 offset:256\n"
        "v_cvt_pk_bf16_f32 v88, v88, v89\n"
        "v_mfma_f32_16x16x32_bf16 v[184:187], a[232:235], a[152:155], v[184:187]\n"
        "v_exp_f32 v91, v91\n"
        "v_mfma_f32_16x16x32_bf16 v[184:187], a[228:231], a[148:151], v[184:187]\n"
        "ds_read_b128 a[244:247], v241 offset:256\n"
        "v_cvt_pk_bf16_f32 v89, v90, v91\n"
        "v_mfma_f32_16x16x32_bf16 v[184:187], a[224:227], a[144:147], v[184:187]\n"
        "v_exp_f32 v92, v92\n"
        "v_mfma_f32_16x16x32_bf16 v[196:199], a[224:227], a[168:171], 0\n"
        "v_exp_f32 v93, v93\n"
        "v_mfma_f32_16x16x32_bf16 v[196:199], a[228:231], a[172:175], v[196:199]\n"
        "v_exp_f32 v94, v94\n"
        "v_mfma_f32_16x16x32_bf16 v[196:199], a[232:235], a[176:179], v[196:199]\n"
        "ds_read_b128 a[248:251], v240 offset:1792\n"
        "v_cvt_pk_bf16_f32 v90, v92, v93\n"
        "v_mfma_f32_16x16x32_bf16 v[196:199], a[236:239], a[180:183], v[196:199]\n"
        "v_exp_f32 v95, v95\n"
        "v_mfma_f32_16x16x32_bf16 v[200:203], a[236:239], a[204:207], 0\n"
        "ds_read_b128 a[216:219], v241 offset:1792\n"
        "v_cvt_pk_bf16_f32 v91, v94, v95\n"
        "v_mfma_f32_16x16x32_bf16 v[200:203], a[232:235], a[200:203], v[200:203]\n"
        "v_exp_f32 v108, v108\n"
        "v_mfma_f32_16x16x32_bf16 v[200:203], a[228:231], a[196:199], v[200:203]\n"
        "v_exp_f32 v109, v109\n"
        "v_mfma_f32_16x16x32_bf16 v[200:203], a[224:227], a[192:195], v[200:203]\n"
        "v_exp_f32 v110, v110\n"
        "v_exp_f32 v111, v111\n"
        "v_exp_f32 v70, v70\n"
        "v_exp_f32 v69, v69\n"
        "v_exp_f32 v68, v68\n"
        "v_exp_f32 v71, v71\n"
        "v_cvt_pk_bf16_f32 v68, v68, v69\n"
        "v_cvt_pk_bf16_f32 v69, v70, v71\n"
        "v_cvt_pk_bf16_f32 v70, v108, v109\n"
        "v_cvt_pk_bf16_f32 v71, v110, v111\n"
        "v_mfma_f32_16x16x32_bf16 v[108:111], v[192:195], v[216:219], v[56:59]\n"
        "s_waitcnt lgkmcnt(4)\n"
        "v_exp_f32 v112, v112\n"
        "v_mfma_f32_16x16x32_bf16 v[108:111], v[208:211], v[220:223], v[108:111]\n"
        "v_exp_f32 v113, v113\n"
        "v_exp_f32 v114, v114\n"
        "v_exp_f32 v115, v115\n"
        "v_exp_f32 v78, v78\n"
        "v_exp_f32 v77, v77\n"
        "v_exp_f32 v76, v76\n"
        "v_exp_f32 v79, v79\n"
        "v_cvt_pk_bf16_f32 v76, v76, v77\n"
        "v_cvt_pk_bf16_f32 v77, v78, v79\n"
        "v_cvt_pk_bf16_f32 v78, v112, v113\n"
        "v_cvt_pk_bf16_f32 v79, v114, v115\n"
        "v_mfma_f32_16x16x32_bf16 v[112:115], v[212:215], v[216:219], v[60:63]\n"
        "v_exp_f32 v116, v116\n"
        "v_mfma_f32_16x16x32_bf16 v[112:115], v[228:231], v[224:227], v[112:115]\n"
        "s_waitcnt lgkmcnt(3)\n"
        "v_exp_f32 v117, v117\n"
        "v_mfma_f32_16x16x32_bf16 v[120:123], a[240:243], a[160:163], v[120:123]\n"
        "s_waitcnt lgkmcnt(2)\n"
        "v_exp_f32 v118, v118\n"
        "v_mfma_f32_16x16x32_bf16 v[120:123], a[244:247], a[164:167], v[120:123]\n"
        "v_exp_f32 v119, v119\n"
        "v_mfma_f32_16x16x32_bf16 v[96:99], a[244:247], a[188:191], v[96:99]\n"
        "ds_read_b128 a[220:223], v240 offset:12544\n"
        "v_mfma_f32_16x16x32_bf16 v[96:99], a[240:243], a[184:187], v[96:99]\n"
        "ds_read_b128 a[224:227], v241 offset:12544\n"
        "v_mfma_f32_16x16x32_bf16 v[100:103], a[240:243], a[208:211], v[100:103]\n"
        "v_exp_f32 v84, v84\n"
        "v_mfma_f32_16x16x32_bf16 v[100:103], a[244:247], a[212:215], v[100:103]\n"
        "s_waitcnt lgkmcnt(2)\n"
        "v_exp_f32 v85, v85\n"
        "v_mfma_f32_16x16x32_bf16 v[104:107], a[216:219], a[164:167], v[104:107]\n"
        "v_exp_f32 v86, v86\n"
        "v_mfma_f32_16x16x32_bf16 v[104:107], a[248:251], a[160:163], v[104:107]\n"
        "v_cvt_pk_bf16_f32 v84, v84, v85\n"
        "v_cvt_pk_bf16_f32 v128, v120, v121\n"
        "v_mfma_f32_16x16x32_bf16 v[156:159], a[248:251], a[184:187], v[156:159]\n"
        "ds_read_b128 a[228:231], v240 offset:14080\n"
        "v_cvt_pk_bf16_f32 v129, v122, v123\n"
        "v_mfma_f32_16x16x32_bf16 v[156:159], a[216:219], a[188:191], v[156:159]\n"
        "ds_read_b128 a[232:235], v241 offset:14080\n"
        "v_cvt_pk_bf16_f32 v136, v96, v97\n"
        "v_mfma_f32_16x16x32_bf16 v[160:163], a[216:219], a[212:215], v[160:163]\n"
        "v_exp_f32 v87, v87\n"
        "v_mfma_f32_16x16x32_bf16 v[160:163], a[248:251], a[208:211], v[160:163]\n"
        "s_waitcnt lgkmcnt(3)\n"
        "v_cvt_pk_bf16_f32 v85, v86, v87\n"
        "v_cvt_pk_bf16_f32 v86, v116, v117\n"
        "v_mfma_f32_16x16x32_bf16 v[164:167], a[220:223], a[160:163], v[164:167]\n"
        "s_waitcnt lgkmcnt(2)\n"
        "v_cvt_pk_bf16_f32 v87, v118, v119\n"
        "v_cvt_pk_bf16_f32 v137, v98, v99\n"
        "v_mfma_f32_16x16x32_bf16 v[164:167], a[224:227], a[164:167], v[164:167]\n"
        "v_exp_f32 v108, v108\n"
        "v_mfma_f32_16x16x32_bf16 v[176:179], a[224:227], a[188:191], v[176:179]\n"
        "v_exp_f32 v109, v109\n"
        "v_mfma_f32_16x16x32_bf16 v[176:179], a[220:223], a[184:187], v[176:179]\n"
        "v_exp_f32 v110, v110\n"
        "v_mfma_f32_16x16x32_bf16 v[180:183], a[220:223], a[208:211], v[180:183]\n"
        "s_waitcnt lgkmcnt(0)\n"
        "s_barrier\n"
        "v_cvt_pk_bf16_f32 v92, v108, v109\n"
        "v_cvt_pk_bf16_f32 v144, v100, v101\n"
        "v_mfma_f32_16x16x32_bf16 v[180:183], a[224:227], a[212:215], v[180:183]\n"
        "v_exp_f32 v111, v111\n"
        "v_mfma_f32_16x16x32_bf16 v[184:187], a[232:235], a[164:167], v[184:187]\n"
        "v_cvt_pk_bf16_f32 v93, v110, v111\n"
        "v_cvt_pk_bf16_f32 v145, v102, v103\n"
        "v_mfma_f32_16x16x32_bf16 v[184:187], a[228:231], a[160:163], v[184:187]\n"
        "v_exp_f32 v112, v112\n"
        "v_mfma_f32_16x16x32_bf16 v[196:199], a[228:231], a[184:187], v[196:199]\n"
        "v_exp_f32 v113, v113\n"
        "v_mfma_f32_16x16x32_bf16 v[196:199], a[232:235], a[188:191], v[196:199]\n"
        "v_exp_f32 v114, v114\n"
        "v_mfma_f32_16x16x32_bf16 v[200:203], a[232:235], a[212:215], v[200:203]\n"
        "v_cvt_pk_bf16_f32 v94, v112, v113\n"
        "v_cvt_pk_bf16_f32 v130, v104, v105\n"
        "v_mfma_f32_16x16x32_bf16 v[200:203], a[228:231], a[208:211], v[200:203]\n"
        "v_exp_f32 v115, v115\n"
        "v_mov_b64 v[120:121], 0\n"
        "s_nop 1\n"
        "v_smfmac_f32_16x16x64_bf16 v[120:123], a[252:255], v[64:71], v255\n"
        "v_cvt_pk_bf16_f32 v95, v114, v115\n"
        "v_cvt_pk_bf16_f32 v131, v106, v107\n"
        "s_nop 1\n"
        "v_mfma_f32_16x16x32_bf16 v[116:119], v[128:131], v[64:67], 0\n"
        "v_cvt_pk_bf16_f32 v138, v156, v157\n"
        "v_cvt_pk_bf16_f32 v139, v158, v159\n"
        "v_cvt_pk_bf16_f32 v132, v164, v165\n"
        "v_cvt_pk_bf16_f32 v133, v166, v167\n"
        "v_cvt_pk_bf16_f32 v134, v184, v185\n"
        "v_cvt_pk_bf16_f32 v135, v186, v187\n"
        "s_nop 1\n"
        "v_mfma_f32_16x16x32_bf16 v[116:119], v[132:135], v[68:71], v[116:119]\n"
        "v_cvt_pk_bf16_f32 v146, v160, v161\n"
        "v_cvt_pk_bf16_f32 v147, v162, v163\n"
        "v_cvt_pk_bf16_f32 v140, v176, v177\n"
        "v_cvt_pk_bf16_f32 v141, v178, v179\n"
        "v_cvt_pk_bf16_f32 v142, v196, v197\n"
        "v_cvt_pk_bf16_f32 v143, v198, v199\n"
        "s_nop 1\n"
        "v_mfma_f32_16x16x32_bf16 v[152:155], v[140:143], v[68:71], 0\n"
        "v_mov_b64 v[172:173], 0\n"
        "v_cvt_pk_bf16_f32 v148, v180, v181\n"
        "v_mfma_f32_16x16x32_bf16 v[152:155], v[136:139], v[64:67], v[152:155]\n"
        "v_cvt_pk_bf16_f32 v149, v182, v183\n"
        "v_cvt_pk_bf16_f32 v150, v200, v201\n"
        "v_mfma_f32_16x16x32_bf16 v[160:163], v[144:147], v[64:67], 0\n"
        "v_cvt_pk_bf16_f32 v151, v202, v203\n"
        "v_mov_b64 v[192:193], 0\n"
        "s_nop 0\n"
        "v_mfma_f32_16x16x32_bf16 v[160:163], v[148:151], v[68:71], v[160:163]\n"
        "v_add_f32 v120, v120, v121\n"
        "v_add_u32 v240, 24576, v240\n"
        "v_smfmac_f32_16x16x64_bf16 v[172:175], a[252:255], v[72:79], v255\n"
        "v_add_u32 v241, 24576, v241\n"
        "v_mfma_f32_16x16x32_bf16 v[176:179], v[128:131], v[72:75], 0\n"
        "v_rcp_f32 v121, v120\n"
        "v_mfma_f32_16x16x32_bf16 v[176:179], v[132:135], v[76:79], v[176:179]\n"
        "v_mov_b64 v[212:213], 0\n"
        "v_mul_f32 v116, v121, v116\n"
        "v_mfma_f32_16x16x32_bf16 v[180:183], v[140:143], v[76:79], 0\n"
        "v_mul_f32 v117, v121, v117\n"
        "v_mul_f32 v118, v121, v118\n"
        "v_mfma_f32_16x16x32_bf16 v[180:183], v[136:139], v[72:75], v[180:183]\n"
        "v_mul_f32 v119, v121, v119\n"
        "v_cvt_pk_bf16_f32 v116, v116, v117\n"
        "v_mfma_f32_16x16x32_bf16 v[184:187], v[144:147], v[72:75], 0\n"
        "v_cvt_pk_bf16_f32 v117, v118, v119\n"
        "v_mul_f32 v152, v121, v152\n"
        "v_mfma_f32_16x16x32_bf16 v[184:187], v[148:151], v[76:79], v[184:187]\n"
        "v_mul_f32 v153, v121, v153\n"
        "v_maximum3_f32 v253, v253, v120, v121\n"
        "v_smfmac_f32_16x16x64_bf16 v[192:195], a[252:255], v[80:87], v255\n"
        "s_waitcnt vmcnt(5)\n"
        "s_barrier\n"
        "ds_read_b128 a[236:239], v240 offset:0\n"
        "v_mul_f32 v154, v121, v154\n"
        "v_mfma_f32_16x16x32_bf16 v[196:199], v[128:131], v[80:83], 0\n"
        "ds_read_b128 a[240:243], v241 offset:0\n"
        "v_mul_f32 v155, v121, v155\n"
        "v_mfma_f32_16x16x32_bf16 v[196:199], v[132:135], v[84:87], v[196:199]\n"
        "v_cvt_pk_bf16_f32 v118, v152, v153\n"
        "v_cvt_pk_bf16_f32 v119, v154, v155\n"
        "v_mfma_f32_16x16x32_bf16 v[200:203], v[140:143], v[84:87], 0\n"
        "ds_read_b128 a[244:247], v240 offset:128\n"
        "v_mul_f32 v160, v121, v160\n"
        "v_mfma_f32_16x16x32_bf16 v[200:203], v[136:139], v[80:83], v[200:203]\n"
        "ds_read_b128 a[248:251], v241 offset:128\n"
        "v_mul_f32 v161, v121, v161\n"
        "v_mfma_f32_16x16x32_bf16 v[204:207], v[144:147], v[80:83], 0\n"
        "ds_read_b128 a[216:219], v240 offset:256\n"
        "v_mul_f32 v162, v121, v162\n"
        "v_mfma_f32_16x16x32_bf16 v[204:207], v[148:151], v[84:87], v[204:207]\n"
        "ds_read_b128 a[220:223], v241 offset:256\n"
        "v_mul_f32 v163, v121, v163\n"
        "v_smfmac_f32_16x16x64_bf16 v[212:215], a[252:255], v[88:95], v255\n"
        "ds_write_b128 v245, v[116:119] offset:0\n"
        "v_mfma_f32_16x16x32_bf16 v[216:219], v[128:131], v[88:91], 0\n"
        "v_cvt_pk_bf16_f32 v160, v160, v161\n"
        "v_cvt_pk_bf16_f32 v161, v162, v163\n"
        "v_mfma_f32_16x16x32_bf16 v[216:219], v[132:135], v[92:95], v[216:219]\n"
        "v_add_f32 v172, v172, v173\n"
        "v_add_f32 v192, v192, v193\n"
        "v_mfma_f32_16x16x32_bf16 v[220:223], v[140:143], v[92:95], 0\n"
        "v_rcp_f32 v173, v172\n"
        "v_mfma_f32_16x16x32_bf16 v[220:223], v[136:139], v[88:91], v[220:223]\n"
        "s_add_u32 m0, s66, 0\n"
        "ds_write_b64 v246, v[160:161] offset:0\n"
        "v_mfma_f32_16x16x32_bf16 v[224:227], v[144:147], v[88:91], 0\n"
        "s_add_u32 s64, s51, 0\n"
        "v_rcp_f32 v193, v192\n"
        "v_mfma_f32_16x16x32_bf16 v[224:227], v[148:151], v[92:95], v[224:227]\n"
        "s_waitcnt lgkmcnt(7)\n"
        "v_mul_f32 v176, v173, v176\n"
        "v_mul_f32 v177, v173, v177\n"
        "v_mfma_f32_16x16x32_bf16 v[96:99], a[72:75], a[236:239], 0\n"
        "s_waitcnt lgkmcnt(6)\n"
        "v_mul_f32 v178, v173, v178\n"
        "v_mul_f32 v179, v173, v179\n"
        "v_mfma_f32_16x16x32_bf16 v[96:99], a[76:79], a[240:243], v[96:99]\n"
        "s_waitcnt lgkmcnt(5)\n"
        "buffer_load_dwordx4 v124, s[52:55], s64 offen lds\n"
        "v_mfma_f32_16x16x32_bf16 v[96:99], a[80:83], a[244:247], v[96:99]\n"
        "s_waitcnt lgkmcnt(4)\n"
        "v_cvt_pk_bf16_f32 v176, v176, v177\n"
        "v_cvt_pk_bf16_f32 v177, v178, v179\n"
        "v_mfma_f32_16x16x32_bf16 v[96:99], a[84:87], a[248:251], v[96:99]\n"
        "s_waitcnt lgkmcnt(3)\n"
        "v_mul_f32 v180, v173, v180\n"
        "v_mul_f32 v181, v173, v181\n"
        "v_mfma_f32_16x16x32_bf16 v[96:99], a[88:91], a[216:219], v[96:99]\n"
        "s_waitcnt lgkmcnt(2)\n"
        "v_maximum3_f32 v253, v253, v172, v173\n"
        "v_mul_f32 v182, v173, v182\n"
        "v_mfma_f32_16x16x32_bf16 v[96:99], a[92:95], a[220:223], v[96:99]\n"
        "s_add_u32 s41, s29, s40\n"
        "v_mul_f32 v183, v173, v183\n"
        "v_cvt_pk_bf16_f32 v178, v180, v181\n"
        "v_mfma_f32_16x16x32_bf16 v[100:103], a[116:119], a[220:223], 0\n"
        "s_add_u32 s35, s47, 786432\n"
        "v_cvt_pk_bf16_f32 v179, v182, v183\n"
        "v_mul_f32 v184, v173, v184\n"
        "v_mfma_f32_16x16x32_bf16 v[100:103], a[112:115], a[216:219], v[100:103]\n"
        "s_sub_u32 s46, s35, 100663296\n"
        "buffer_load_dwordx4 v125, s[52:55], s64 offen offset:1024 lds\n"
        "v_mfma_f32_16x16x32_bf16 v[100:103], a[108:111], a[248:251], v[100:103]\n"
        "s_cmp_lt_u32 s46, 786432\n"
        "v_cvt_pk_bf16_f32 v152, v96, v97\n"
        "v_cvt_pk_bf16_f32 v153, v98, v99\n"
        "v_mfma_f32_16x16x32_bf16 v[100:103], a[104:107], a[244:247], v[100:103]\n"
        "s_cselect_b32 s35, s46, s35\n"
        "ds_write_b128 v245, v[176:179] offset:256\n"
        "v_mfma_f32_16x16x32_bf16 v[100:103], a[100:103], a[240:243], v[100:103]\n"
        "s_sub_u32 s31, s28, s30\n"
        "v_mul_f32 v185, v173, v185\n"
        "v_mul_f32 v186, v173, v186\n"
        "v_mfma_f32_16x16x32_bf16 v[100:103], a[96:99], a[236:239], v[100:103]\n"
        "s_sub_u32 s45, s29, s28\n"
        "v_mul_f32 v187, v173, v187\n"
        "v_cvt_pk_bf16_f32 v184, v184, v185\n"
        "v_mfma_f32_16x16x32_bf16 v[104:107], a[120:123], a[236:239], 0\n"
        "s_mov_b32 s29, s30\n"
        "v_cvt_pk_bf16_f32 v185, v186, v187\n"
        "v_mul_f32 v196, v193, v196\n"
        "v_mfma_f32_16x16x32_bf16 v[104:107], a[124:127], a[240:243], v[104:107]\n"
        "s_mov_b32 s30, s28\n"
        "v_cvt_pk_bf16_f32 v154, v100, v101\n"
        "v_cvt_pk_bf16_f32 v155, v102, v103\n"
        "v_mfma_f32_16x16x32_bf16 v[104:107], a[128:131], a[244:247], v[104:107]\n"
        "s_add_u32 s28, s28, s45\n"
        "buffer_load_dwordx4 v126, s[52:55], s64 offen offset:2048 lds\n"
        "v_mfma_f32_16x16x32_bf16 v[104:107], a[132:135], a[248:251], v[104:107]\n"
        "ds_write_b64 v246, v[184:185] offset:256\n"
        "v_mfma_f32_16x16x32_bf16 v[104:107], a[136:139], a[216:219], v[104:107]\n"
        "v_mul_f32 v197, v193, v197\n"
        "v_mul_f32 v198, v193, v198\n"
        "v_mfma_f32_16x16x32_bf16 v[104:107], a[140:143], a[220:223], v[104:107]\n"
        "v_mul_f32 v199, v193, v199\n"
        "v_cvt_pk_bf16_f32 v196, v196, v197\n"
        "v_mfma_f32_16x16x32_bf16 v[96:99], a[20:23], a[220:223], 0\n"
        "s_add_u32 m0, s66, 3072\n"
        "v_cvt_pk_bf16_f32 v197, v198, v199\n"
        "v_mul_f32 v200, v193, v200\n"
        "v_mfma_f32_16x16x32_bf16 v[96:99], a[16:19], a[216:219], v[96:99]\n"
        "s_add_u32 s64, s51, 6144\n"
        "v_cvt_pk_bf16_f32 v168, v104, v105\n"
        "v_cvt_pk_bf16_f32 v169, v106, v107\n"
        "v_mfma_f32_16x16x32_bf16 v[96:99], a[12:15], a[248:251], v[96:99]\n"
        "v_mul_f32 v201, v193, v201\n"
        "v_maximum3_f32 v253, v253, v192, v193\n"
        "v_mfma_f32_16x16x32_bf16 v[96:99], a[8:11], a[244:247], v[96:99]\n"
        "buffer_load_dwordx4 v124, s[52:55], s64 offen lds\n"
        "v_mfma_f32_16x16x32_bf16 v[96:99], a[4:7], a[240:243], v[96:99]\n"
        "v_mul_f32 v202, v193, v202\n"
        "v_mul_f32 v203, v193, v203\n"
        "v_mfma_f32_16x16x32_bf16 v[96:99], a[0:3], a[236:239], v[96:99]\n"
        "v_cvt_pk_bf16_f32 v198, v200, v201\n"
        "v_cvt_pk_bf16_f32 v199, v202, v203\n"
        "v_mfma_f32_16x16x32_bf16 v[100:103], a[24:27], a[236:239], 0\n"
        "ds_write_b128 v245, v[196:199] offset:512\n"
        "v_mfma_f32_16x16x32_bf16 v[100:103], a[28:31], a[240:243], v[100:103]\n"
        "v_mul_f32 v204, v193, v204\n"
        "v_mul_f32 v205, v193, v205\n"
        "v_mfma_f32_16x16x32_bf16 v[100:103], a[32:35], a[244:247], v[100:103]\n"
        "v_mul_f32 v96, s50, v96\n"
        "v_mul_f32 v97, s50, v97\n"
        "v_mfma_f32_16x16x32_bf16 v[100:103], a[36:39], a[248:251], v[100:103]\n"
        "v_mul_f32 v98, s50, v98\n"
        "v_mul_f32 v99, s50, v99\n"
        "v_mfma_f32_16x16x32_bf16 v[100:103], a[40:43], a[216:219], v[100:103]\n"
        "buffer_load_dwordx4 v125, s[52:55], s64 offen offset:1024 lds\n"
        "v_mfma_f32_16x16x32_bf16 v[100:103], a[44:47], a[220:223], v[100:103]\n"
        "v_cvt_pk_bf16_f32 v156, v96, v97\n"
        "v_cvt_pk_bf16_f32 v157, v98, v99\n"
        "v_mfma_f32_16x16x32_bf16 v[104:107], a[68:71], a[220:223], 0\n"
        "v_mul_f32 v206, v193, v206\n"
        "v_mul_f32 v207, v193, v207\n"
        "v_mfma_f32_16x16x32_bf16 v[104:107], a[64:67], a[216:219], v[104:107]\n"
        "v_cvt_pk_bf16_f32 v204, v204, v205\n"
        "v_cvt_pk_bf16_f32 v205, v206, v207\n"
        "v_mfma_f32_16x16x32_bf16 v[104:107], a[60:63], a[248:251], v[104:107]\n"
        "v_mul_f32 v100, s50, v100\n"
        "v_mul_f32 v101, s50, v101\n"
        "v_mfma_f32_16x16x32_bf16 v[104:107], a[56:59], a[244:247], v[104:107]\n"
        "v_mul_f32 v102, s50, v102\n"
        "v_mul_f32 v103, s50, v103\n"
        "v_mfma_f32_16x16x32_bf16 v[104:107], a[52:55], a[240:243], v[104:107]\n"
        "v_cvt_pk_bf16_f32 v158, v100, v101\n"
        "v_cvt_pk_bf16_f32 v159, v102, v103\n"
        "v_mfma_f32_16x16x32_bf16 v[104:107], a[48:51], a[236:239], v[104:107]\n"
        "buffer_load_dwordx4 v126, s[52:55], s64 offen offset:2048 lds\n"
        "v_mfma_f32_16x16x32_bf16 v[96:99], a[236:239], a[144:147], 0\n"
        "ds_write_b64 v246, v[204:205] offset:512\n"
        "v_mfma_f32_16x16x32_bf16 v[96:99], a[240:243], a[148:151], v[96:99]\n"
        "v_add_f32 v212, v212, v213\n"
        "v_xor_b32 v251, 0x2000, v251\n"
        "v_mfma_f32_16x16x32_bf16 v[96:99], a[244:247], a[152:155], v[96:99]\n"
        "v_rcp_f32 v213, v212\n"
        "v_mfma_f32_16x16x32_bf16 v[96:99], a[248:251], a[156:159], v[96:99]\n"
        "s_add_u32 m0, s66, 6144\n"
        "v_mul_f32 v104, s50, v104\n"
        "v_mul_f32 v105, s50, v105\n"
        "v_mfma_f32_16x16x32_bf16 v[96:99], a[216:219], a[160:163], v[96:99]\n"
        "s_add_u32 s64, s51, 18432\n"
        "v_mul_f32 v106, s50, v106\n"
        "v_mul_f32 v107, s50, v107\n"
        "v_mfma_f32_16x16x32_bf16 v[96:99], a[220:223], a[164:167], v[96:99]\n"
        "v_cvt_pk_bf16_f32 v160, v104, v105\n"
        "v_cvt_pk_bf16_f32 v161, v106, v107\n"
        "v_mfma_f32_16x16x32_bf16 v[100:103], a[220:223], a[188:191], 0\n"
        "buffer_load_dwordx4 v124, s[52:55], s64 offen lds\n"
        "v_mfma_f32_16x16x32_bf16 v[100:103], a[216:219], a[184:187], v[100:103]\n"
        "v_mov_b64 v[166:167], v[160:161]\n"
        "v_mul_f32 v216, v213, v216\n"
        "v_mfma_f32_16x16x32_bf16 v[100:103], a[248:251], a[180:183], v[100:103]\n"
        "ds_read_b128 a[224:227], v240 offset:1536\n"
        "v_mul_f32 v217, v213, v217\n"
        "v_mfma_f32_16x16x32_bf16 v[100:103], a[244:247], a[176:179], v[100:103]\n"
        "ds_read_b128 a[228:231], v241 offset:1536\n"
        "v_mul_f32 v218, v213, v218\n"
        "v_mfma_f32_16x16x32_bf16 v[100:103], a[240:243], a[172:175], v[100:103]\n"
        "ds_read_b128 a[232:235], v240 offset:1664\n"
        "v_mul_f32 v219, v213, v219\n"
        "v_mfma_f32_16x16x32_bf16 v[100:103], a[236:239], a[168:171], v[100:103]\n"
        "v_cvt_pk_bf16_f32 v216, v216, v217\n"
        "v_cvt_pk_bf16_f32 v217, v218, v219\n"
        "v_mfma_f32_16x16x32_bf16 v[104:107], a[236:239], a[192:195], 0\n"
        "ds_read_b128 a[236:239], v241 offset:1664\n"
        "v_cvt_pk_bf16_f32 v128, v96, v97\n"
        "v_mfma_f32_16x16x32_bf16 v[104:107], a[240:243], a[196:199], v[104:107]\n"
        "ds_read_b128 a[240:243], v240 offset:1792\n"
        "v_cvt_pk_bf16_f32 v129, v98, v99\n"
        "v_mfma_f32_16x16x32_bf16 v[104:107], a[244:247], a[200:203], v[104:107]\n"
        "buffer_load_dwordx4 v125, s[52:55], s64 offen offset:1024 lds\n"
        "v_mfma_f32_16x16x32_bf16 v[104:107], a[248:251], a[204:207], v[104:107]\n"
        "ds_read_b128 a[244:247], v241 offset:1792\n"
        "v_mul_f32 v220, v213, v220\n"
        "v_mfma_f32_16x16x32_bf16 v[104:107], a[216:219], a[208:211], v[104:107]\n"
        "v_mul_f32 v221, v213, v221\n"
        "v_maximum3_f32 v253, v253, v212, v213\n"
        "v_mfma_f32_16x16x32_bf16 v[104:107], a[220:223], a[212:215], v[104:107]\n"
        "s_waitcnt lgkmcnt(5)\n"
        "v_mul_f32 v222, v213, v222\n"
        "v_mul_f32 v223, v213, v223\n"
        "v_mfma_f32_16x16x32_bf16 v[96:99], a[72:75], a[224:227], 0\n"
        "s_waitcnt lgkmcnt(4)\n"
        "v_cvt_pk_bf16_f32 v218, v220, v221\n"
        "v_cvt_pk_bf16_f32 v219, v222, v223\n"
        "v_mfma_f32_16x16x32_bf16 v[96:99], a[76:79], a[228:231], v[96:99]\n"
        "42:\n"
        "s_cbranch_execz 45f\n"
        "s_waitcnt lgkmcnt(3)\n"
        "v_xor_b32 v252, 0x2000, v252\n"
        "v_mul_f32 v224, v213, v224\n"
        "v_mfma_f32_16x16x32_bf16 v[96:99], a[80:83], a[232:235], v[96:99]\n"
        "s_waitcnt lgkmcnt(2)\n"
        "buffer_load_dwordx4 v126, s[52:55], s64 offen offset:2048 lds\n"
        "v_mfma_f32_16x16x32_bf16 v[96:99], a[84:87], a[236:239], v[96:99]\n"
        "s_waitcnt lgkmcnt(1)\n"
        "ds_write_b128 v245, v[216:219] offset:768\n"
        "v_mfma_f32_16x16x32_bf16 v[96:99], a[88:91], a[240:243], v[96:99]\n"
        "s_waitcnt lgkmcnt(1)\n"
        "v_mul_f32 v225, v213, v225\n"
        "v_cvt_pk_bf16_f32 v136, v100, v101\n"
        "v_mfma_f32_16x16x32_bf16 v[96:99], a[92:95], a[244:247], v[96:99]\n"
        "v_cvt_pk_bf16_f32 v137, v102, v103\n"
        "v_mul_f32 v226, v213, v226\n"
        "v_mfma_f32_16x16x32_bf16 v[100:103], a[116:119], a[244:247], 0\n"
        "s_waitcnt vmcnt(9)\n"
        "s_barrier\n"
        "v_mul_f32 v227, v213, v227\n"
        "v_cvt_pk_bf16_f32 v224, v224, v225\n"
        "v_mfma_f32_16x16x32_bf16 v[100:103], a[112:115], a[240:243], v[100:103]\n"
        "s_add_u32 m0, s66, 9216\n"
        "v_cvt_pk_bf16_f32 v225, v226, v227\n"
        "v_cvt_pk_bf16_f32 v144, v104, v105\n"
        "v_mfma_f32_16x16x32_bf16 v[100:103], a[108:111], a[236:239], v[100:103]\n"
        "s_add_u32 s64, s51, 24576\n"
        "ds_write_b64 v246, v[224:225] offset:768\n"
        "v_mfma_f32_16x16x32_bf16 v[100:103], a[104:107], a[232:235], v[100:103]\n"
        "v_cvt_pk_bf16_f32 v145, v106, v107\n"
        "v_cvt_pk_bf16_f32 v172, v96, v97\n"
        "v_mfma_f32_16x16x32_bf16 v[100:103], a[100:103], a[228:231], v[100:103]\n"
        "v_cvt_pk_bf16_f32 v173, v98, v99\n"
        "v_add_u32 v245, s31, v245\n"
        "v_mfma_f32_16x16x32_bf16 v[100:103], a[96:99], a[224:227], v[100:103]\n"
        "buffer_load_dwordx4 v124, s[52:55], s64 offen lds\n"
        "v_mfma_f32_16x16x32_bf16 v[104:107], a[120:123], a[224:227], 0\n"
        "s_waitcnt vmcnt(4)\n"
        "ds_read_b64_tr_b16 v[88:89], v234 offset:0\n"
        "ds_read_b64_tr_b16 v[90:91], v234 offset:3072\n"
        "v_mfma_f32_16x16x32_bf16 v[104:107], a[124:127], a[228:231], v[104:107]\n"
        "ds_read_b64_tr_b16 v[92:93], v235 offset:32\n"
        "ds_read_b64_tr_b16 v[94:95], v235 offset:3104\n"
        "v_mfma_f32_16x16x32_bf16 v[104:107], a[128:131], a[232:235], v[104:107]\n"
        "ds_read_b64_tr_b16 v[120:121], v234 offset:64\n"
        "ds_read_b128 v[162:165], v251 offset:0\n"
        "v_mfma_f32_16x16x32_bf16 v[104:107], a[132:135], a[236:239], v[104:107]\n"
        "ds_read_b64_tr_b16 v[122:123], v234 offset:3136\n"
        "v_cvt_pk_bf16_f32 v174, v100, v101\n"
        "v_mfma_f32_16x16x32_bf16 v[104:107], a[136:139], a[240:243], v[104:107]\n"
        "ds_read_b64_tr_b16 v[216:217], v235 offset:96\n"
        "v_cvt_pk_bf16_f32 v175, v102, v103\n"
        "v_mfma_f32_16x16x32_bf16 v[104:107], a[140:143], a[244:247], v[104:107]\n"
        "buffer_load_dwordx4 v125, s[52:55], s64 offen offset:1024 lds\n"
        "v_mfma_f32_16x16x32_bf16 v[96:99], a[20:23], a[244:247], 0\n"
        "ds_read_b64_tr_b16 v[218:219], v235 offset:3168\n"
        "ds_read_b64_tr_b16 v[220:221], v238 offset:128\n"
        "v_mfma_f32_16x16x32_bf16 v[96:99], a[16:19], a[240:243], v[96:99]\n"
        "ds_read_b64_tr_b16 v[222:223], v238 offset:3200\n"
        "v_add_u32 v246, s31, v246\n"
        "v_mfma_f32_16x16x32_bf16 v[96:99], a[12:15], a[236:239], v[96:99]\n"
        "ds_read_b64_tr_b16 v[224:225], v239 offset:160\n"
        "v_cvt_pk_bf16_f32 v190, v104, v105\n"
        "v_mfma_f32_16x16x32_bf16 v[96:99], a[8:11], a[232:235], v[96:99]\n"
        "ds_read_b64_tr_b16 v[226:227], v239 offset:3232\n"
        "v_cvt_pk_bf16_f32 v191, v106, v107\n"
        "v_mfma_f32_16x16x32_bf16 v[96:99], a[4:7], a[228:231], v[96:99]\n"
        "ds_read_b128 v[182:185], v251 offset:512\n"
        "v_mfma_f32_16x16x32_bf16 v[96:99], a[0:3], a[224:227], v[96:99]\n"
        "buffer_load_dwordx4 v126, s[52:55], s64 offen offset:2048 lds\n"
        "v_mfma_f32_16x16x32_bf16 v[64:67], v[152:155], v[156:159], v[0:3]\n"
        "s_waitcnt lgkmcnt(8)\n"
        "s_waitcnt lgkmcnt(12)\n"
        "ds_write_b128 v236, v[88:91] offset:0\n"
        "v_mfma_f32_16x16x32_bf16 v[64:67], v[168:171], v[160:163], v[64:67]\n"
        "v_mfma_f32_16x16x32_bf16 v[68:71], v[172:175], v[156:159], v[4:7]\n"
        "v_mfma_f32_16x16x32_bf16 v[68:71], v[188:191], v[164:167], v[68:71]\n"
        "s_add_u32 m0, s66, 12288\n"
        "v_mfma_f32_16x16x32_bf16 v[100:103], a[24:27], a[224:227], 0\n"
        "s_add_u32 s64, s51, 12288\n"
        "v_mul_f32 v96, s50, v96\n"
        "v_mul_f32 v97, s50, v97\n"
        "v_mfma_f32_16x16x32_bf16 v[100:103], a[28:31], a[228:231], v[100:103]\n"
        "v_mul_f32 v98, s50, v98\n"
        "v_mul_f32 v99, s50, v99\n"
        "v_mfma_f32_16x16x32_bf16 v[100:103], a[32:35], a[232:235], v[100:103]\n"
        "buffer_load_dwordx4 v127, s[52:55], s64 offen lds\n"
        "v_mfma_f32_16x16x32_bf16 v[100:103], a[36:39], a[236:239], v[100:103]\n"
        "v_cvt_pk_bf16_f32 v176, v96, v97\n"
        "v_cvt_pk_bf16_f32 v177, v98, v99\n"
        "v_mfma_f32_16x16x32_bf16 v[100:103], a[40:43], a[240:243], v[100:103]\n"
        "ds_write_b128 v236, v[92:95] offset:384\n"
        "v_mfma_f32_16x16x32_bf16 v[100:103], a[44:47], a[244:247], v[100:103]\n"
        "v_exp_f32 v64, v64\n"
        "v_mfma_f32_16x16x32_bf16 v[104:107], a[68:71], a[244:247], 0\n"
        "v_exp_f32 v65, v65\n"
        "v_mfma_f32_16x16x32_bf16 v[104:107], a[64:67], a[240:243], v[104:107]\n"
        "v_exp_f32 v66, v66\n"
        "v_mfma_f32_16x16x32_bf16 v[104:107], a[60:63], a[236:239], v[104:107]\n"
        "v_exp_f32 v67, v67\n"
        "v_mfma_f32_16x16x32_bf16 v[104:107], a[56:59], a[232:235], v[104:107]\n"
        "buffer_load_dwordx4 v232, s[52:55], s64 offen offset:1024 lds\n"
        "v_mfma_f32_16x16x32_bf16 v[104:107], a[52:55], a[228:231], v[104:107]\n"
        "s_waitcnt lgkmcnt(9)\n"
        "ds_write_b128 v236, v[120:123] offset:768\n"
        "v_mfma_f32_16x16x32_bf16 v[104:107], a[48:51], a[224:227], v[104:107]\n"
        "v_mul_f32 v100, s50, v100\n"
        "v_mul_f32 v101, s50, v101\n"
        "v_mfma_f32_16x16x32_bf16 v[96:99], a[224:227], a[144:147], 0\n"
        "v_mul_f32 v102, s50, v102\n"
        "v_mul_f32 v103, s50, v103\n"
        "v_mfma_f32_16x16x32_bf16 v[96:99], a[228:231], a[148:151], v[96:99]\n"
        "v_cvt_pk_bf16_f32 v178, v100, v101\n"
        "v_cvt_pk_bf16_f32 v179, v102, v103\n"
        "v_mfma_f32_16x16x32_bf16 v[96:99], a[232:235], a[152:155], v[96:99]\n"
        "s_waitcnt lgkmcnt(8)\n"
        "ds_write_b128 v236, v[216:219] offset:1152\n"
        "v_mfma_f32_16x16x32_bf16 v[96:99], a[236:239], a[156:159], v[96:99]\n"
        "v_mul_f32 v104, s50, v104\n"
        "v_mul_f32 v105, s50, v105\n"
        "v_mfma_f32_16x16x32_bf16 v[96:99], a[240:243], a[160:163], v[96:99]\n"
        "v_mul_f32 v106, s50, v106\n"
        "v_mul_f32 v107, s50, v107\n"
        "v_mfma_f32_16x16x32_bf16 v[96:99], a[244:247], a[164:167], v[96:99]\n"
        "v_cvt_pk_bf16_f32 v180, v104, v105\n"
        "v_cvt_pk_bf16_f32 v181, v106, v107\n"
        "v_mfma_f32_16x16x32_bf16 v[72:75], v[152:155], v[176:179], v[16:19]\n"
        "s_waitcnt lgkmcnt(4)\n"
        "buffer_load_dwordx4 v233, s[52:55], s64 offen offset:2048 lds\n"
        "v_mfma_f32_16x16x32_bf16 v[72:75], v[168:171], v[180:183], v[72:75]\n"
        "ds_write_b128 v237, v[220:223] offset:0\n"
        "v_mfma_f32_16x16x32_bf16 v[76:79], v[172:175], v[176:179], v[20:23]\n"
        "s_add_u32 m0, s66, 15360\n"
        "v_mov_b64 v[186:187], v[180:181]\n"
        "v_cvt_pk_bf16_f32 v130, v96, v97\n"
        "s_nop 0\n"
        "v_mfma_f32_16x16x32_bf16 v[76:79], v[188:191], v[184:187], v[76:79]\n"
        "s_add_u32 s64, s51, 13824\n"
        "v_cvt_pk_bf16_f32 v131, v98, v99\n"
        "v_cvt_pk_bf16_f32 v64, v64, v65\n"
        "v_mfma_f32_16x16x32_bf16 v[100:103], a[244:247], a[188:191], 0\n"
        "v_exp_f32 v68, v68\n"
        "v_mfma_f32_16x16x32_bf16 v[100:103], a[240:243], a[184:187], v[100:103]\n"
        "buffer_load_dwordx4 v127, s[52:55], s64 offen lds\n"
        "v_mfma_f32_16x16x32_bf16 v[100:103], a[236:239], a[180:183], v[100:103]\n"
        "ds_read_b128 a[248:251], v240 offset:12288\n"
        "v_cvt_pk_bf16_f32 v65, v66, v67\n"
        "v_mfma_f32_16x16x32_bf16 v[100:103], a[232:235], a[176:179], v[100:103]\n"
        "ds_read_b128 a[216:219], v241 offset:12288\n"
        "v_mfma_f32_16x16x32_bf16 v[100:103], a[228:231], a[172:175], v[100:103]\n"
        "ds_read_b128 a[220:223], v240 offset:12416\n"
        "v_mfma_f32_16x16x32_bf16 v[100:103], a[224:227], a[168:171], v[100:103]\n"
        "ds_write_b128 v237, v[224:227] offset:768\n"
        "v_mfma_f32_16x16x32_bf16 v[104:107], a[224:227], a[192:195], 0\n"
        "v_exp_f32 v69, v69\n"
        "v_mfma_f32_16x16x32_bf16 v[104:107], a[228:231], a[196:199], v[104:107]\n"
        "ds_read_b128 a[224:227], v241 offset:12416\n"
        "v_cvt_pk_bf16_f32 v66, v68, v69\n"
        "v_mfma_f32_16x16x32_bf16 v[104:107], a[232:235], a[200:203], v[104:107]\n"
        "ds_read_b128 a[228:231], v240 offset:12544\n"
        "v_cvt_pk_bf16_f32 v138, v100, v101\n"
        "v_mfma_f32_16x16x32_bf16 v[104:107], a[236:239], a[204:207], v[104:107]\n"
        "ds_read_b128 a[232:235], v241 offset:12544\n"
        "v_cvt_pk_bf16_f32 v139, v102, v103\n"
        "v_mfma_f32_16x16x32_bf16 v[104:107], a[240:243], a[208:211], v[104:107]\n"
        "buffer_load_dwordx4 v232, s[52:55], s64 offen offset:1024 lds\n"
        "v_mfma_f32_16x16x32_bf16 v[104:107], a[244:247], a[212:215], v[104:107]\n"
        "s_waitcnt lgkmcnt(6)\n"
        "v_exp_f32 v70, v70\n"
        "v_mfma_f32_16x16x32_bf16 v[96:99], a[72:75], a[248:251], 0\n"
        "s_waitcnt lgkmcnt(5)\n"
        "v_exp_f32 v71, v71\n"
        "v_mfma_f32_16x16x32_bf16 v[96:99], a[76:79], a[216:219], v[96:99]\n"
        "s_waitcnt lgkmcnt(4)\n"
        "v_exp_f32 v72, v72\n"
        "v_mfma_f32_16x16x32_bf16 v[96:99], a[80:83], a[220:223], v[96:99]\n"
        "s_waitcnt lgkmcnt(2)\n"
        "v_exp_f32 v73, v73\n"
        "v_mfma_f32_16x16x32_bf16 v[96:99], a[84:87], a[224:227], v[96:99]\n"
        "s_waitcnt lgkmcnt(1)\n"
        "buffer_load_dwordx4 v233, s[52:55], s64 offen offset:2048 lds\n"
        "v_mfma_f32_16x16x32_bf16 v[96:99], a[88:91], a[228:231], v[96:99]\n"
        "s_waitcnt lgkmcnt(0)\n"
        "s_waitcnt vmcnt(6)\n"
        "ds_read_b64_tr_b16 v[88:89], v234 offset:6144\n"
        "v_cvt_pk_bf16_f32 v146, v104, v105\n"
        "v_mfma_f32_16x16x32_bf16 v[96:99], a[92:95], a[232:235], v[96:99]\n"
        "ds_read_b64_tr_b16 v[90:91], v234 offset:9216\n"
        "v_cvt_pk_bf16_f32 v147, v106, v107\n"
        "v_mfma_f32_16x16x32_bf16 v[100:103], a[116:119], a[232:235], 0\n"
        "ds_read_b64_tr_b16 v[92:93], v235 offset:6176\n"
        "v_cvt_pk_bf16_f32 v67, v70, v71\n"
        "v_mfma_f32_16x16x32_bf16 v[100:103], a[112:115], a[228:231], v[100:103]\n"
        "ds_read_b64_tr_b16 v[94:95], v235 offset:9248\n"
        "v_cvt_pk_bf16_f32 v72, v72, v73\n"
        "v_mfma_f32_16x16x32_bf16 v[100:103], a[108:111], a[224:227], v[100:103]\n"
        "s_add_u32 m0, s41, 0\n"
        "ds_read_b64_tr_b16 v[120:121], v234 offset:6208\n"
        "v_cvt_pk_bf16_f32 v192, v96, v97\n"
        "v_mfma_f32_16x16x32_bf16 v[100:103], a[104:107], a[220:223], v[100:103]\n"
        "ds_read_b64_tr_b16 v[122:123], v234 offset:9280\n"
        "v_cvt_pk_bf16_f32 v193, v98, v99\n"
        "v_mfma_f32_16x16x32_bf16 v[100:103], a[100:103], a[216:219], v[100:103]\n"
        "ds_read_b64_tr_b16 v[216:217], v235 offset:6240\n"
        "v_mfma_f32_16x16x32_bf16 v[100:103], a[96:99], a[248:251], v[100:103]\n"
        "buffer_load_dwordx4 v242, s[8:11], s47 offen lds\n"
        "v_mfma_f32_16x16x32_bf16 v[104:107], a[120:123], a[248:251], 0\n"
        "ds_read_b64_tr_b16 v[218:219], v235 offset:9312\n"
        "ds_read_b64_tr_b16 v[220:221], v238 offset:6272\n"
        "v_mfma_f32_16x16x32_bf16 v[104:107], a[124:127], a[216:219], v[104:107]\n"
        "s_add_u32 m0, s41, 1024\n"
        "ds_read_b64_tr_b16 v[222:223], v238 offset:9344\n"
        "v_mfma_f32_16x16x32_bf16 v[104:107], a[128:131], a[220:223], v[104:107]\n"
        "ds_read_b64_tr_b16 v[224:225], v239 offset:6304\n"
        "v_cvt_pk_bf16_f32 v194, v100, v101\n"
        "v_mfma_f32_16x16x32_bf16 v[104:107], a[132:135], a[224:227], v[104:107]\n"
        "ds_read_b64_tr_b16 v[226:227], v239 offset:9376\n"
        "v_cvt_pk_bf16_f32 v195, v102, v103\n"
        "v_mfma_f32_16x16x32_bf16 v[104:107], a[136:139], a[228:231], v[104:107]\n"
        "buffer_load_dwordx4 v243, s[8:11], s47 offen lds\n"
        "v_mfma_f32_16x16x32_bf16 v[104:107], a[140:143], a[232:235], v[104:107]\n"
        "s_waitcnt lgkmcnt(10)\n"
        "ds_write_b128 v236, v[88:91] offset:6144\n"
        "v_mfma_f32_16x16x32_bf16 v[96:99], a[20:23], a[232:235], 0\n"
        "s_add_u32 m0, s41, 2048\n"
        "v_exp_f32 v74, v74\n"
        "v_mfma_f32_16x16x32_bf16 v[96:99], a[16:19], a[228:231], v[96:99]\n"
        "v_exp_f32 v75, v75\n"
        "v_mfma_f32_16x16x32_bf16 v[96:99], a[12:15], a[224:227], v[96:99]\n"
        "v_exp_f32 v76, v76\n"
        "v_mfma_f32_16x16x32_bf16 v[96:99], a[8:11], a[220:223], v[96:99]\n"
        "buffer_load_dwordx4 v244, s[8:11], s47 offen lds\n"
        "v_mfma_f32_16x16x32_bf16 v[96:99], a[4:7], a[216:219], v[96:99]\n"
        "s_waitcnt lgkmcnt(9)\n"
        "ds_write_b128 v236, v[92:95] offset:6528\n"
        "v_mfma_f32_16x16x32_bf16 v[96:99], a[0:3], a[248:251], v[96:99]\n"
        "s_add_u32 m0, s41, 12288\n"
        "v_cvt_pk_bf16_f32 v208, v104, v105\n"
        "v_cvt_pk_bf16_f32 v209, v106, v107\n"
        "v_mfma_f32_16x16x32_bf16 v[100:103], a[24:27], a[248:251], 0\n"
        "v_exp_f32 v77, v77\n"
        "v_mfma_f32_16x16x32_bf16 v[100:103], a[28:31], a[216:219], v[100:103]\n"
        "v_exp_f32 v78, v78\n"
        "v_mfma_f32_16x16x32_bf16 v[100:103], a[32:35], a[220:223], v[100:103]\n"
        "buffer_load_dwordx4 v242, s[8:11], s35 offen lds\n"
        "v_mfma_f32_16x16x32_bf16 v[100:103], a[36:39], a[224:227], v[100:103]\n"
        "ds_read_b128 v[162:165], v252 offset:0\n"
        "v_mul_f32 v96, s50, v96\n"
        "v_mfma_f32_16x16x32_bf16 v[100:103], a[40:43], a[228:231], v[100:103]\n"
        "s_add_u32 m0, s41, 13312\n"
        "s_waitcnt lgkmcnt(9)\n"
        "ds_write_b128 v236, v[120:123] offset:6912\n"
        "v_mfma_f32_16x16x32_bf16 v[100:103], a[44:47], a[232:235], v[100:103]\n"
        "v_mul_f32 v97, s50, v97\n"
        "v_mul_f32 v98, s50, v98\n"
        "v_mfma_f32_16x16x32_bf16 v[104:107], a[68:71], a[232:235], 0\n"
        "v_mul_f32 v99, s50, v99\n"
        "v_cvt_pk_bf16_f32 v196, v96, v97\n"
        "v_mfma_f32_16x16x32_bf16 v[104:107], a[64:67], a[228:231], v[104:107]\n"
        "v_cvt_pk_bf16_f32 v197, v98, v99\n"
        "v_cvt_pk_bf16_f32 v73, v74, v75\n"
        "v_mfma_f32_16x16x32_bf16 v[104:107], a[60:63], a[224:227], v[104:107]\n"
        "buffer_load_dwordx4 v243, s[8:11], s35 offen lds\n"
        "v_mfma_f32_16x16x32_bf16 v[104:107], a[56:59], a[220:223], v[104:107]\n"
        "s_add_u32 m0, s41, 14336\n"
        "v_mul_f32 v100, s50, v100\n"
        "v_mul_f32 v101, s50, v101\n"
        "v_mfma_f32_16x16x32_bf16 v[104:107], a[52:55], a[216:219], v[104:107]\n"
        "ds_read_b128 v[182:185], v252 offset:512\n"
        "v_mul_f32 v102, s50, v102\n"
        "v_mfma_f32_16x16x32_bf16 v[104:107], a[48:51], a[248:251], v[104:107]\n"
        "buffer_load_dwordx4 v244, s[8:11], s35 offen lds\n"
        "v_mfma_f32_16x16x32_bf16 v[68:71], v[192:195], v[156:159], v[8:11]\n"
        "s_waitcnt lgkmcnt(2)\n"
        "s_waitcnt lgkmcnt(9)\n"
        "ds_write_b128 v236, v[216:219] offset:7296\n"
        "v_mfma_f32_16x16x32_bf16 v[68:71], v[208:211], v[160:163], v[68:71]\n"
        "v_mul_f32 v103, s50, v103\n"
        "v_cvt_pk_bf16_f32 v198, v100, v101\n"
        "v_mfma_f32_16x16x32_bf16 v[96:99], a[248:251], a[144:147], 0\n"
        "s_mov_b32 m0, s48\n"
        "v_cvt_pk_bf16_f32 v199, v102, v103\n"
        "v_mul_f32 v104, s50, v104\n"
        "v_mfma_f32_16x16x32_bf16 v[96:99], a[216:219], a[148:151], v[96:99]\n"
        "v_mul_f32 v105, s50, v105\n"
        "v_mul_f32 v106, s50, v106\n"
        "v_mfma_f32_16x16x32_bf16 v[96:99], a[220:223], a[152:155], v[96:99]\n"
        "buffer_load_dwordx4 v249, s[12:15], s36 offen offset:0 lds\n"
        "v_mfma_f32_16x16x32_bf16 v[96:99], a[224:227], a[156:159], v[96:99]\n"
        "v_mul_f32 v107, s50, v107\n"
        "v_cvt_pk_bf16_f32 v200, v104, v105\n"
        "v_mfma_f32_16x16x32_bf16 v[96:99], a[228:231], a[160:163], v[96:99]\n"
        "ds_read_b128 v[202:205], v251 offset:4096\n"
        "v_cvt_pk_bf16_f32 v201, v106, v107\n"
        "v_mfma_f32_16x16x32_bf16 v[96:99], a[232:235], a[164:167], v[96:99]\n"
        "ds_write_b128 v237, v[220:223] offset:6144\n"
        "v_cvt_pk_bf16_f32 v74, v76, v77\n"
        "v_exp_f32 v79, v79\n"
        "v_mov_b64 v[206:207], v[200:201]\n"
        "v_cvt_pk_bf16_f32 v75, v78, v79\n"
        "v_mfma_f32_16x16x32_bf16 v[76:79], v[192:195], v[176:179], v[24:27]\n"
        "s_waitcnt lgkmcnt(3)\n"
        "v_xor_b32 v116, 32, v249\n"
        "v_mfma_f32_16x16x32_bf16 v[76:79], v[208:211], v[180:183], v[76:79]\n"
        "v_cvt_pk_bf16_f32 v132, v96, v97\n"
        "v_cvt_pk_bf16_f32 v133, v98, v99\n"
        "v_mfma_f32_16x16x32_bf16 v[100:103], a[232:235], a[188:191], 0\n"
        "v_exp_f32 v68, v68\n"
        "v_mfma_f32_16x16x32_bf16 v[100:103], a[228:231], a[184:187], v[100:103]\n"
        "buffer_load_dwordx4 v116, s[12:15], s36 offen offset:1024 lds\n"
        "v_mfma_f32_16x16x32_bf16 v[100:103], a[224:227], a[180:183], v[100:103]\n"
        "ds_write_b128 v237, v[224:227] offset:6912\n"
        "v_mfma_f32_16x16x32_bf16 v[100:103], a[220:223], a[176:179], v[100:103]\n"
        "v_exp_f32 v69, v69\n"
        "v_mfma_f32_16x16x32_bf16 v[100:103], a[216:219], a[172:175], v[100:103]\n"
        "v_exp_f32 v70, v70\n"
        "v_mfma_f32_16x16x32_bf16 v[100:103], a[248:251], a[168:171], v[100:103]\n"
        "v_exp_f32 v71, v71\n"
        "v_mfma_f32_16x16x32_bf16 v[80:83], v[152:155], v[196:199], v[32:35]\n"
        "s_waitcnt lgkmcnt(2)\n"
        "s_waitcnt vmcnt(8)\n"
        "ds_read_b64_tr_b16 v[220:221], v234 offset:12288\n"
        "ds_read_b64_tr_b16 v[222:223], v234 offset:15360\n"
        "v_mfma_f32_16x16x32_bf16 v[80:83], v[168:171], v[200:203], v[80:83]\n"
        "ds_read_b64_tr_b16 v[224:225], v235 offset:12320\n"
        "ds_read_b128 a[236:239], v240 offset:13824\n"
        "v_mfma_f32_16x16x32_bf16 v[104:107], a[248:251], a[192:195], 0\n"
        "ds_read_b64_tr_b16 v[226:227], v235 offset:15392\n"
        "ds_read_b128 a[240:243], v241 offset:13824\n"
        "v_mfma_f32_16x16x32_bf16 v[104:107], a[216:219], a[196:199], v[104:107]\n"
        "ds_read_b64_tr_b16 v[88:89], v234 offset:12352\n"
        "ds_read_b128 a[244:247], v240 offset:13952\n"
        "v_mfma_f32_16x16x32_bf16 v[104:107], a[220:223], a[200:203], v[104:107]\n"
        "ds_read_b64_tr_b16 v[90:91], v234 offset:15424\n"
        "ds_read_b128 a[248:251], v241 offset:13952\n"
        "v_mfma_f32_16x16x32_bf16 v[104:107], a[224:227], a[204:207], v[104:107]\n"
        "ds_read_b64_tr_b16 v[92:93], v235 offset:12384\n"
        "ds_read_b128 a[216:219], v240 offset:14080\n"
        "v_mfma_f32_16x16x32_bf16 v[104:107], a[228:231], a[208:211], v[104:107]\n"
        "ds_read_b64_tr_b16 v[94:95], v235 offset:15456\n"
        "ds_read_b128 a[220:223], v241 offset:14080\n"
        "v_mfma_f32_16x16x32_bf16 v[104:107], a[232:235], a[212:215], v[104:107]\n"
        "ds_read_b64_tr_b16 v[120:121], v238 offset:12416\n"
        "ds_read_b64_tr_b16 v[122:123], v238 offset:15488\n"
        "v_mfma_f32_16x16x32_bf16 v[84:87], v[172:175], v[196:199], v[36:39]\n"
        "ds_read_b64_tr_b16 v[124:125], v239 offset:12448\n"
        "v_cvt_pk_bf16_f32 v140, v100, v101\n"
        "v_mfma_f32_16x16x32_bf16 v[84:87], v[188:191], v[204:207], v[84:87]\n"
        "s_waitcnt lgkmcnt(13)\n"
        "ds_read_b64_tr_b16 v[126:127], v239 offset:15520\n"
        "v_cvt_pk_bf16_f32 v141, v102, v103\n"
        "v_mfma_f32_16x16x32_bf16 v[96:99], a[72:75], a[236:239], 0\n"
        "s_waitcnt lgkmcnt(12)\n"
        "v_cvt_pk_bf16_f32 v148, v104, v105\n"
        "v_cvt_pk_bf16_f32 v149, v106, v107\n"
        "v_mfma_f32_16x16x32_bf16 v[96:99], a[76:79], a[240:243], v[96:99]\n"
        "s_waitcnt lgkmcnt(10)\n"
        "ds_write_b128 v236, v[220:223] offset:12288\n"
        "v_mfma_f32_16x16x32_bf16 v[96:99], a[80:83], a[244:247], v[96:99]\n"
        "s_waitcnt lgkmcnt(9)\n"
        "v_exp_f32 v76, v76\n"
        "v_mfma_f32_16x16x32_bf16 v[96:99], a[84:87], a[248:251], v[96:99]\n"
        "s_waitcnt lgkmcnt(7)\n"
        "v_exp_f32 v77, v77\n"
        "v_mfma_f32_16x16x32_bf16 v[96:99], a[88:91], a[216:219], v[96:99]\n"
        "s_waitcnt lgkmcnt(5)\n"
        "v_exp_f32 v78, v78\n"
        "v_mfma_f32_16x16x32_bf16 v[96:99], a[92:95], a[220:223], v[96:99]\n"
        "ds_read_b128 v[202:205], v252 offset:4096\n"
        "v_cvt_pk_bf16_f32 v68, v68, v69\n"
        "v_mfma_f32_16x16x32_bf16 v[100:103], a[116:119], a[220:223], 0\n"
        "ds_write_b128 v236, v[224:227] offset:12672\n"
        "v_mfma_f32_16x16x32_bf16 v[100:103], a[112:115], a[216:219], v[100:103]\n"
        "v_exp_f32 v79, v79\n"
        "v_mfma_f32_16x16x32_bf16 v[100:103], a[108:111], a[248:251], v[100:103]\n"
        "v_exp_f32 v80, v80\n"
        "v_mfma_f32_16x16x32_bf16 v[100:103], a[104:107], a[244:247], v[100:103]\n"
        "v_cvt_pk_bf16_f32 v212, v96, v97\n"
        "v_cvt_pk_bf16_f32 v213, v98, v99\n"
        "v_mfma_f32_16x16x32_bf16 v[100:103], a[100:103], a[240:243], v[100:103]\n"
        "v_exp_f32 v81, v81\n"
        "v_mfma_f32_16x16x32_bf16 v[100:103], a[96:99], a[236:239], v[100:103]\n"
        "ds_write_b128 v236, v[88:91] offset:13056\n"
        "v_mfma_f32_16x16x32_bf16 v[104:107], a[120:123], a[236:239], 0\n"
        "v_exp_f32 v82, v82\n"
        "v_mfma_f32_16x16x32_bf16 v[104:107], a[124:127], a[240:243], v[104:107]\n"
        "v_exp_f32 v83, v83\n"
        "v_mfma_f32_16x16x32_bf16 v[104:107], a[128:131], a[244:247], v[104:107]\n"
        "v_exp_f32 v84, v84\n"
        "v_mfma_f32_16x16x32_bf16 v[104:107], a[132:135], a[248:251], v[104:107]\n"
        "v_cvt_pk_bf16_f32 v214, v100, v101\n"
        "v_cvt_pk_bf16_f32 v215, v102, v103\n"
        "v_mfma_f32_16x16x32_bf16 v[104:107], a[136:139], a[216:219], v[104:107]\n"
        "v_exp_f32 v85, v85\n"
        "v_mfma_f32_16x16x32_bf16 v[104:107], a[140:143], a[220:223], v[104:107]\n"
        "v_cvt_pk_bf16_f32 v69, v70, v71\n"
        "v_cvt_pk_bf16_f32 v76, v76, v77\n"
        "v_mfma_f32_16x16x32_bf16 v[96:99], a[20:23], a[220:223], 0\n"
        "ds_write_b128 v236, v[92:95] offset:13440\n"
        "v_mfma_f32_16x16x32_bf16 v[96:99], a[16:19], a[216:219], v[96:99]\n"
        "v_exp_f32 v86, v86\n"
        "v_mfma_f32_16x16x32_bf16 v[96:99], a[12:15], a[248:251], v[96:99]\n"
        "v_exp_f32 v87, v87\n"
        "v_mfma_f32_16x16x32_bf16 v[96:99], a[8:11], a[244:247], v[96:99]\n"
        "v_cvt_pk_bf16_f32 v230, v104, v105\n"
        "v_cvt_pk_bf16_f32 v231, v106, v107\n"
        "v_mfma_f32_16x16x32_bf16 v[96:99], a[4:7], a[240:243], v[96:99]\n"
        "s_cmp_eq_u32 s37, 100663296\n"
        "v_cvt_pk_bf16_f32 v77, v78, v79\n"
        "v_cvt_pk_bf16_f32 v80, v80, v81\n"
        "v_mfma_f32_16x16x32_bf16 v[96:99], a[0:3], a[236:239], v[96:99]\n"
        "s_cselect_b32 s46, 0, s37\n"
        "s_waitcnt lgkmcnt(7)\n"
        "ds_write_b128 v237, v[120:123] offset:12288\n"
        "v_mfma_f32_16x16x32_bf16 v[108:111], v[212:215], v[156:159], v[12:15]\n"
        "s_add_u32 s38, s46, s43\n"
        "v_cvt_pk_bf16_f32 v81, v82, v83\n"
        "v_cvt_pk_bf16_f32 v82, v84, v85\n"
        "v_mfma_f32_16x16x32_bf16 v[108:111], v[228:231], v[164:167], v[108:111]\n"
        "s_add_u32 s39, s46, s44\n"
        "v_mul_f32 v96, s50, v96\n"
        "v_mul_f32 v97, s50, v97\n"
        "v_mfma_f32_16x16x32_bf16 v[112:115], v[212:215], v[176:179], v[28:31]\n"
        "s_add_u32 s37, s37, 6291456\n"
        "v_mul_f32 v98, s50, v98\n"
        "v_mul_f32 v99, s50, v99\n"
        "v_mfma_f32_16x16x32_bf16 v[112:115], v[228:231], v[184:187], v[112:115]\n"
        "v_cvt_pk_bf16_f32 v216, v96, v97\n"
        "v_cvt_pk_bf16_f32 v217, v98, v99\n"
        "v_mfma_f32_16x16x32_bf16 v[116:119], v[212:215], v[196:199], v[44:47]\n"
        "s_waitcnt lgkmcnt(4)\n"
        "s_waitcnt lgkmcnt(6)\n"
        "ds_write_b128 v237, v[124:127] offset:13056\n"
        "v_mfma_f32_16x16x32_bf16 v[116:119], v[228:231], v[204:207], v[116:119]\n"
        "v_exp_f32 v108, v108\n"
        "v_mfma_f32_16x16x32_bf16 v[100:103], a[24:27], a[236:239], 0\n"
        "v_exp_f32 v109, v109\n"
        "v_mfma_f32_16x16x32_bf16 v[100:103], a[28:31], a[240:243], v[100:103]\n"
        "v_exp_f32 v110, v110\n"
        "v_mfma_f32_16x16x32_bf16 v[100:103], a[32:35], a[244:247], v[100:103]\n"
        "ds_read_b128 v[222:225], v251 offset:4608\n"
        "v_cvt_pk_bf16_f32 v83, v86, v87\n"
        "v_mfma_f32_16x16x32_bf16 v[100:103], a[36:39], a[248:251], v[100:103]\n"
        "v_exp_f32 v111, v111\n"
        "v_mfma_f32_16x16x32_bf16 v[100:103], a[40:43], a[216:219], v[100:103]\n"
        "v_add_u32 v240, s45, v240\n"
        "v_add_u32 v241, s45, v241\n"
        "v_mfma_f32_16x16x32_bf16 v[100:103], a[44:47], a[220:223], v[100:103]\n"
        "v_exp_f32 v112, v112\n"
        "v_mfma_f32_16x16x32_bf16 v[84:87], v[192:195], v[196:199], v[40:43]\n"
        "v_exp_f32 v113, v113\n"
        "v_mfma_f32_16x16x32_bf16 v[84:87], v[208:211], v[200:203], v[84:87]\n"
        "v_exp_f32 v114, v114\n"
        "v_mfma_f32_16x16x32_bf16 v[104:107], a[68:71], a[220:223], 0\n"
        "v_exp_f32 v115, v115\n"
        "v_mfma_f32_16x16x32_bf16 v[104:107], a[64:67], a[216:219], v[104:107]\n"
        "v_mul_f32 v100, s50, v100\n"
        "v_mul_f32 v101, s50, v101\n"
        "v_mfma_f32_16x16x32_bf16 v[104:107], a[60:63], a[248:251], v[104:107]\n"
        "v_mul_f32 v102, s50, v102\n"
        "v_mul_f32 v103, s50, v103\n"
        "v_mfma_f32_16x16x32_bf16 v[104:107], a[56:59], a[244:247], v[104:107]\n"
        "v_cvt_pk_bf16_f32 v218, v100, v101\n"
        "v_cvt_pk_bf16_f32 v219, v102, v103\n"
        "v_mfma_f32_16x16x32_bf16 v[104:107], a[52:55], a[240:243], v[104:107]\n"
        "s_waitcnt lgkmcnt(1)\n"
        "s_barrier\n"
        "v_exp_f32 v116, v116\n"
        "v_mfma_f32_16x16x32_bf16 v[104:107], a[48:51], a[236:239], v[104:107]\n"
        "v_exp_f32 v117, v117\n"
        "v_mfma_f32_16x16x32_bf16 v[96:99], a[236:239], a[144:147], 0\n"
        "v_exp_f32 v118, v118\n"
        "v_mfma_f32_16x16x32_bf16 v[96:99], a[240:243], a[148:151], v[96:99]\n"
        "v_exp_f32 v119, v119\n"
        "v_mfma_f32_16x16x32_bf16 v[96:99], a[244:247], a[152:155], v[96:99]\n"
        "v_exp_f32 v84, v84\n"
        "v_mfma_f32_16x16x32_bf16 v[96:99], a[248:251], a[156:159], v[96:99]\n"
        "v_mul_f32 v104, s50, v104\n"
        "v_mul_f32 v105, s50, v105\n"
        "v_mfma_f32_16x16x32_bf16 v[96:99], a[216:219], a[160:163], v[96:99]\n"
        "v_mul_f32 v106, s50, v106\n"
        "v_mul_f32 v107, s50, v107\n"
        "v_mfma_f32_16x16x32_bf16 v[96:99], a[220:223], a[164:167], v[96:99]\n"
        "v_cvt_pk_bf16_f32 v220, v104, v105\n"
        "v_cvt_pk_bf16_f32 v221, v106, v107\n"
        "v_mfma_f32_16x16x32_bf16 v[88:91], v[152:155], v[216:219], v[48:51]\n"
        "s_waitcnt lgkmcnt(0)\n"
        "ds_read_b128 v[156:159], v247 offset:0\n"
        "v_mov_b64 v[226:227], v[220:221]\n"
        "v_mfma_f32_16x16x32_bf16 v[88:91], v[168:171], v[220:223], v[88:91]\n"
        "ds_read_b128 v[164:167], v247 offset:4096\n"
        "v_cvt_pk_bf16_f32 v70, v108, v109\n"
        "v_mfma_f32_16x16x32_bf16 v[92:95], v[172:175], v[216:219], v[52:55]\n"
        "s_add_u32 s47, s47, 6291456\n"
        "ds_read_b128 v[160:163], v247 offset:256\n"
        "v_cvt_pk_bf16_f32 v134, v96, v97\n"
        "v_mfma_f32_16x16x32_bf16 v[92:95], v[188:191], v[224:227], v[92:95]\n"
        "s_add_u32 s36, s36, 2097152\n"
        "ds_read_b128 v[222:225], v252 offset:4608\n"
        "v_cvt_pk_bf16_f32 v135, v98, v99\n"
        "v_mfma_f32_16x16x32_bf16 v[100:103], a[220:223], a[188:191], 0\n"
        "s_xor_b32 s48, s48, 8192\n"
        "ds_read_b128 v[152:155], v247 offset:4352\n"
        "v_cvt_pk_bf16_f32 v71, v110, v111\n"
        "v_mfma_f32_16x16x32_bf16 v[100:103], a[216:219], a[184:187], v[100:103]\n"
        "v_exp_f32 v85, v85\n"
        "v_mfma_f32_16x16x32_bf16 v[100:103], a[248:251], a[180:183], v[100:103]\n"
        "v_exp_f32 v86, v86\n"
        "v_mfma_f32_16x16x32_bf16 v[100:103], a[244:247], a[176:179], v[100:103]\n"
        "v_exp_f32 v87, v87\n"
        "v_mfma_f32_16x16x32_bf16 v[100:103], a[240:243], a[172:175], v[100:103]\n"
        "v_exp_f32 v88, v88\n"
        "v_mfma_f32_16x16x32_bf16 v[100:103], a[236:239], a[168:171], v[100:103]\n"
        "v_exp_f32 v89, v89\n"
        "v_mfma_f32_16x16x32_bf16 v[104:107], a[236:239], a[192:195], 0\n"
        "ds_read_b128 a[236:239], v248 offset:0\n"
        "v_cvt_pk_bf16_f32 v78, v112, v113\n"
        "v_mfma_f32_16x16x32_bf16 v[104:107], a[240:243], a[196:199], v[104:107]\n"
        "ds_read_b128 a[240:243], v248 offset:6144\n"
        "v_cvt_pk_bf16_f32 v79, v114, v115\n"
        "v_mfma_f32_16x16x32_bf16 v[104:107], a[244:247], a[200:203], v[104:107]\n"
        "v_cvt_pk_bf16_f32 v142, v100, v101\n"
        "v_cvt_pk_bf16_f32 v143, v102, v103\n"
        "v_mfma_f32_16x16x32_bf16 v[104:107], a[248:251], a[204:207], v[104:107]\n"
        "v_exp_f32 v90, v90\n"
        "v_mfma_f32_16x16x32_bf16 v[104:107], a[216:219], a[208:211], v[104:107]\n"
        "v_exp_f32 v91, v91\n"
        "v_mfma_f32_16x16x32_bf16 v[104:107], a[220:223], a[212:215], v[104:107]\n"
        "ds_read_b128 a[244:247], v254 offset:16\n"
        "v_cvt_pk_bf16_f32 v84, v84, v85\n"
        "v_mfma_f32_16x16x32_bf16 v[108:111], v[192:195], v[216:219], v[56:59]\n"
        "s_waitcnt lgkmcnt(4)\n"
        "ds_read_b128 a[248:251], v254 offset:6160\n"
        "v_cvt_pk_bf16_f32 v85, v86, v87\n"
        "v_mfma_f32_16x16x32_bf16 v[108:111], v[208:211], v[220:223], v[108:111]\n"
        "v_cvt_pk_bf16_f32 v86, v116, v117\n"
        "v_mov_b64 v[116:117], 0\n"
        "v_mfma_f32_16x16x32_bf16 v[112:115], v[212:215], v[216:219], v[60:63]\n"
        "v_cvt_pk_bf16_f32 v150, v104, v105\n"
        "v_cvt_pk_bf16_f32 v151, v106, v107\n"
        "v_mfma_f32_16x16x32_bf16 v[112:115], v[228:231], v[224:227], v[112:115]\n"
        "s_waitcnt lgkmcnt(3)\n"
        "ds_read_b128 a[216:219], v248 offset:32\n"
        "v_cvt_pk_bf16_f32 v87, v118, v119\n"
        "v_mfma_f32_16x16x32_bf16 v[172:175], v[156:159], a[236:239], 0\n"
        "s_waitcnt lgkmcnt(3)\n"
        "ds_read_b128 a[220:223], v248 offset:6176\n"
        "v_cvt_pk_bf16_f32 v88, v88, v89\n"
        "v_mfma_f32_16x16x32_bf16 v[172:175], v[164:167], a[240:243], v[172:175]\n"
        "v_exp_f32 v92, v92\n"
        "v_mfma_f32_16x16x32_bf16 v[200:203], v[152:155], a[240:243], 0\n"
        "v_exp_f32 v93, v93\n"
        "v_mfma_f32_16x16x32_bf16 v[200:203], v[160:163], a[236:239], v[200:203]\n"
        "ds_read_b128 a[224:227], v254 offset:48\n"
        "v_cvt_pk_bf16_f32 v89, v90, v91\n"
        "v_smfmac_f32_16x16x64_bf16 v[116:119], a[252:255], v[64:71], v255\n"
        "ds_read_b128 a[228:231], v254 offset:6192\n"
        "v_cvt_pk_bf16_f32 v90, v92, v93\n"
        "v_mfma_f32_16x16x32_bf16 v[120:123], v[128:131], v[64:67], 0\n"
        "v_exp_f32 v94, v94\n"
        "v_mfma_f32_16x16x32_bf16 v[120:123], v[132:135], v[68:71], v[120:123]\n"
        "v_exp_f32 v95, v95\n"
        "v_mfma_f32_16x16x32_bf16 v[124:127], v[140:143], v[68:71], 0\n"
        "ds_read_b128 a[232:235], v248 offset:64\n"
        "v_add_f32 v116, v116, v117\n"
        "v_mfma_f32_16x16x32_bf16 v[124:127], v[136:139], v[64:67], v[124:127]\n"
        "ds_read_b128 a[236:239], v248 offset:6208\n"
        "v_cvt_pk_bf16_f32 v91, v94, v95\n"
        "v_mfma_f32_16x16x32_bf16 v[96:99], v[144:147], v[64:67], 0\n"
        "v_exp_f32 v108, v108\n"
        "v_mfma_f32_16x16x32_bf16 v[96:99], v[148:151], v[68:71], v[96:99]\n"
        "s_waitcnt lgkmcnt(7)\n"
        "v_rcp_f32 v117, v116\n"
        "v_mfma_f32_16x16x32_bf16 v[204:207], v[160:163], a[244:247], 0\n"
        "s_waitcnt lgkmcnt(6)\n"
        "v_exp_f32 v109, v109\n"
        "v_mfma_f32_16x16x32_bf16 v[204:207], v[152:155], a[248:251], v[204:207]\n"
        "v_exp_f32 v110, v110\n"
        "v_mfma_f32_16x16x32_bf16 v[176:179], v[164:167], a[248:251], 0\n"
        "v_exp_f32 v111, v111\n"
        "v_mfma_f32_16x16x32_bf16 v[176:179], v[156:159], a[244:247], v[176:179]\n"
        "s_waitcnt lgkmcnt(5)\n"
        "v_mul_f32 v120, v117, v120\n"
        "v_mul_f32 v121, v117, v121\n"
        "v_mfma_f32_16x16x32_bf16 v[180:183], v[156:159], a[216:219], 0\n"
        "s_waitcnt lgkmcnt(4)\n"
        "v_mov_b64 v[100:101], 0\n"
        "v_mul_f32 v122, v117, v122\n"
        "v_mfma_f32_16x16x32_bf16 v[180:183], v[164:167], a[220:223], v[180:183]\n"
        "ds_read_b128 v[232:235], v247 offset:8192\n"
        "v_mul_f32 v123, v117, v123\n"
        "v_mfma_f32_16x16x32_bf16 v[212:215], v[152:155], a[220:223], 0\n"
        "ds_read_b128 v[64:67], v247 offset:12288\n"
        "v_cvt_pk_bf16_f32 v92, v108, v109\n"
        "v_mfma_f32_16x16x32_bf16 v[212:215], v[160:163], a[216:219], v[212:215]\n"
        "s_waitcnt lgkmcnt(5)\n"
        "ds_read_b128 a[240:243], v254 offset:80\n"
        "v_cvt_pk_bf16_f32 v120, v120, v121\n"
        "v_mfma_f32_16x16x32_bf16 v[216:219], v[160:163], a[224:227], 0\n"
        "s_waitcnt lgkmcnt(5)\n"
        "ds_read_b128 a[244:247], v254 offset:6224\n"
        "v_cvt_pk_bf16_f32 v121, v122, v123\n"
        "v_mfma_f32_16x16x32_bf16 v[216:219], v[152:155], a[228:231], v[216:219]\n"
        "43:\n"
        "s_cbranch_execz 40b\n"
        "ds_write_b32 v245, v120 offset:0\n"
        "v_mfma_f32_16x16x32_bf16 v[184:187], v[164:167], a[228:231], 0\n"
        "v_cvt_pk_bf16_f32 v93, v110, v111\n"
        "v_mul_f32 v124, v117, v124\n"
        "v_mfma_f32_16x16x32_bf16 v[184:187], v[156:159], a[224:227], v[184:187]\n"
        "s_waitcnt lgkmcnt(6)\n"
        "v_exp_f32 v112, v112\n"
        "v_mfma_f32_16x16x32_bf16 v[192:195], v[156:159], a[232:235], 0\n"
        "s_waitcnt lgkmcnt(5)\n"
        "ds_write_b32 v245, v121 offset:4\n"
        "v_mfma_f32_16x16x32_bf16 v[192:195], v[164:167], a[236:239], v[192:195]\n"
        "v_exp_f32 v113, v113\n"
        "v_mfma_f32_16x16x32_bf16 v[220:223], v[152:155], a[236:239], 0\n"
        "v_mul_f32 v125, v117, v125\n"
        "v_maximum3_f32 v253, v253, v116, v117\n"
        "v_mfma_f32_16x16x32_bf16 v[220:223], v[160:163], a[232:235], v[220:223]\n"
        "v_exp_f32 v114, v114\n"
        "v_smfmac_f32_16x16x64_bf16 v[100:103], a[252:255], v[72:79], v255\n"
        "v_mul_f32 v126, v117, v126\n"
        "v_mul_f32 v127, v117, v127\n"
        "v_mfma_f32_16x16x32_bf16 v[104:107], v[128:131], v[72:75], 0\n"
        "ds_read_b128 v[236:239], v247 offset:8448\n"
        "ds_read_b128 a[248:251], v248 offset:36864\n"
        "v_mfma_f32_16x16x32_bf16 v[104:107], v[132:135], v[76:79], v[104:107]\n"
        "ds_read_b128 v[68:71], v247 offset:12544\n"
        "ds_read_b128 a[216:219], v248 offset:43008\n"
        "v_mfma_f32_16x16x32_bf16 v[108:111], v[140:143], v[76:79], 0\n"
        "v_cvt_pk_bf16_f32 v94, v112, v113\n"
        "v_cvt_pk_bf16_f32 v124, v124, v125\n"
        "v_mfma_f32_16x16x32_bf16 v[108:111], v[136:139], v[72:75], v[108:111]\n"
        "v_exp_f32 v115, v115\n"
        "v_cvt_pk_bf16_f32 v125, v126, v127\n"
        "v_cvt_pk_bf16_f32 v95, v114, v115\n"
        "v_mfma_f32_16x16x32_bf16 v[112:115], v[144:147], v[72:75], 0\n"
        "ds_read_b128 a[220:223], v254 offset:36880\n"
        "v_mul_f32 v96, v117, v96\n"
        "v_mfma_f32_16x16x32_bf16 v[112:115], v[148:151], v[76:79], v[112:115]\n"
        "s_waitcnt lgkmcnt(8)\n"
        "ds_read_b128 a[224:227], v254 offset:43024\n"
        "v_mul_f32 v97, v117, v97\n"
        "v_mfma_f32_16x16x32_bf16 v[224:227], v[160:163], a[240:243], 0\n"
        "s_waitcnt lgkmcnt(8)\n"
        "ds_write_b32 v245, v124 offset:8\n"
        "v_mfma_f32_16x16x32_bf16 v[224:227], v[152:155], a[244:247], v[224:227]\n"
        "ds_write_b32 v245, v125 offset:12\n"
        "v_mfma_f32_16x16x32_bf16 v[196:199], v[164:167], a[244:247], 0\n"
        "ds_read_b128 a[228:231], v248 offset:36896\n"
        "v_mul_f32 v98, v117, v98\n"
        "v_mfma_f32_16x16x32_bf16 v[196:199], v[156:159], a[240:243], v[196:199]\n"
        "s_waitcnt lgkmcnt(7)\n"
        "ds_read_b128 a[232:235], v248 offset:43040\n"
        "v_mul_f32 v99, v117, v99\n"
        "v_mfma_f32_16x16x32_bf16 v[172:175], v[232:235], a[248:251], v[172:175]\n"
        "s_waitcnt lgkmcnt(6)\n"
        "v_cvt_pk_bf16_f32 v96, v96, v97\n"
        "v_cvt_pk_bf16_f32 v97, v98, v99\n"
        "v_mfma_f32_16x16x32_bf16 v[172:175], v[64:67], a[216:219], v[172:175]\n"
        "ds_write_b32 v246, v96 offset:0\n"
        "v_mfma_f32_16x16x32_bf16 v[200:203], v[68:71], a[216:219], v[200:203]\n"
        "ds_read_b128 a[236:239], v254 offset:36912\n"
        "v_add_f32 v100, v100, v101\n"
        "v_mfma_f32_16x16x32_bf16 v[200:203], v[236:239], a[248:251], v[200:203]\n"
        "s_waitcnt lgkmcnt(7)\n"
        "ds_read_b128 a[240:243], v254 offset:43056\n"
        "v_mfma_f32_16x16x32_bf16 v[204:207], v[236:239], a[220:223], v[204:207]\n"
        "s_waitcnt lgkmcnt(7)\n"
        "ds_write_b32 v246, v97 offset:4\n"
        "v_mfma_f32_16x16x32_bf16 v[204:207], v[68:71], a[224:227], v[204:207]\n"
        "v_rcp_f32 v101, v100\n"
        "v_mfma_f32_16x16x32_bf16 v[176:179], v[64:67], a[224:227], v[176:179]\n"
        "v_mul_f32 v104, v101, v104\n"
        "v_mul_f32 v105, v101, v105\n"
        "v_mfma_f32_16x16x32_bf16 v[176:179], v[232:235], a[220:223], v[176:179]\n"
        "s_waitcnt lgkmcnt(5)\n"
        "v_mul_f32 v106, v101, v106\n"
        "v_mul_f32 v107, v101, v107\n"
        "v_mfma_f32_16x16x32_bf16 v[180:183], v[232:235], a[228:231], v[180:183]\n"
        "s_waitcnt lgkmcnt(4)\n"
        "v_cvt_pk_bf16_f32 v104, v104, v105\n"
        "v_cvt_pk_bf16_f32 v105, v106, v107\n"
        "v_mfma_f32_16x16x32_bf16 v[180:183], v[64:67], a[232:235], v[180:183]\n"
        "v_mov_b64 v[116:117], 0\n"
        "v_mul_f32 v108, v101, v108\n"
        "v_mfma_f32_16x16x32_bf16 v[212:215], v[68:71], a[232:235], v[212:215]\n"
        "ds_write_b32 v245, v104 offset:256\n"
        "v_mfma_f32_16x16x32_bf16 v[212:215], v[236:239], a[228:231], v[212:215]\n"
        "s_waitcnt lgkmcnt(3)\n"
        "ds_write_b32 v245, v105 offset:260\n"
        "v_mfma_f32_16x16x32_bf16 v[216:219], v[236:239], a[236:239], v[216:219]\n"
        "s_waitcnt lgkmcnt(3)\n"
        "v_mul_f32 v109, v101, v109\n"
        "v_maximum3_f32 v253, v253, v100, v101\n"
        "v_mfma_f32_16x16x32_bf16 v[216:219], v[68:71], a[240:243], v[216:219]\n"
        "ds_read_b128 a[244:247], v248 offset:36928\n"
        "v_mul_f32 v110, v101, v110\n"
        "v_mfma_f32_16x16x32_bf16 v[184:187], v[64:67], a[240:243], v[184:187]\n"
        "ds_read_b128 a[248:251], v248 offset:43072\n"
        "v_mul_f32 v111, v101, v111\n"
        "v_mfma_f32_16x16x32_bf16 v[184:187], v[232:235], a[236:239], v[184:187]\n"
        "v_cvt_pk_bf16_f32 v108, v108, v109\n"
        "v_cvt_pk_bf16_f32 v109, v110, v111\n"
        "v_smfmac_f32_16x16x64_bf16 v[116:119], a[252:255], v[80:87], v255\n"
        "ds_write_b32 v245, v108 offset:264\n"
        "v_mfma_f32_16x16x32_bf16 v[120:123], v[128:131], v[80:83], 0\n"
        "ds_read_b128 a[216:219], v254 offset:36944\n"
        "v_mul_f32 v112, v101, v112\n"
        "v_mfma_f32_16x16x32_bf16 v[120:123], v[132:135], v[84:87], v[120:123]\n"
        "ds_read_b128 a[220:223], v254 offset:43088\n"
        "v_mul_f32 v113, v101, v113\n"
        "v_mfma_f32_16x16x32_bf16 v[124:127], v[140:143], v[84:87], 0\n"
        "ds_read_b128 v[156:159], v247 offset:16384\n"
        "v_mul_f32 v114, v101, v114\n"
        "v_mfma_f32_16x16x32_bf16 v[124:127], v[136:139], v[80:83], v[124:127]\n"
        "ds_read_b128 v[164:167], v247 offset:20480\n"
        "v_mul_f32 v115, v101, v115\n"
        "v_mfma_f32_16x16x32_bf16 v[96:99], v[144:147], v[80:83], 0\n"
        "ds_read_b128 v[160:163], v247 offset:16640\n"
        "ds_read_b128 a[224:227], v248 offset:12288\n"
        "v_mfma_f32_16x16x32_bf16 v[96:99], v[148:151], v[84:87], v[96:99]\n"
        "s_waitcnt lgkmcnt(8)\n"
        "ds_read_b128 v[152:155], v247 offset:20736\n"
        "ds_read_b128 a[228:231], v248 offset:49152\n"
        "v_mfma_f32_16x16x32_bf16 v[192:195], v[232:235], a[244:247], v[192:195]\n"
        "s_waitcnt lgkmcnt(9)\n"
        "ds_write_b32 v245, v109 offset:268\n"
        "v_mfma_f32_16x16x32_bf16 v[192:195], v[64:67], a[248:251], v[192:195]\n"
        "ds_read_b128 a[232:235], v254 offset:12304\n"
        "v_cvt_pk_bf16_f32 v112, v112, v113\n"
        "v_mfma_f32_16x16x32_bf16 v[220:223], v[68:71], a[248:251], v[220:223]\n"
        "ds_read_b128 a[236:239], v254 offset:49168\n"
        "v_cvt_pk_bf16_f32 v113, v114, v115\n"
        "v_mfma_f32_16x16x32_bf16 v[220:223], v[236:239], a[244:247], v[220:223]\n"
        "s_waitcnt lgkmcnt(10)\n"
        "ds_write_b32 v246, v112 offset:256\n"
        "v_mfma_f32_16x16x32_bf16 v[224:227], v[236:239], a[216:219], v[224:227]\n"
        "s_waitcnt lgkmcnt(10)\n"
        "ds_write_b32 v246, v113 offset:260\n"
        "v_mfma_f32_16x16x32_bf16 v[224:227], v[68:71], a[220:223], v[224:227]\n"
        "ds_read_b128 a[240:243], v248 offset:12320\n"
        "v_add_f32 v116, v116, v117\n"
        "v_mfma_f32_16x16x32_bf16 v[196:199], v[64:67], a[220:223], v[196:199]\n"
        "v_rcp_f32 v117, v116\n"
        "v_mfma_f32_16x16x32_bf16 v[196:199], v[232:235], a[216:219], v[196:199]\n"
        "s_waitcnt lgkmcnt(8)\n"
        "ds_read_b128 a[244:247], v248 offset:49184\n"
        "v_mul_f32 v120, v117, v120\n"
        "v_mfma_f32_16x16x32_bf16 v[172:175], v[156:159], a[224:227], v[172:175]\n"
        "s_waitcnt lgkmcnt(7)\n"
        "v_mul_f32 v121, v117, v121\n"
        "v_mul_f32 v122, v117, v122\n"
        "v_mfma_f32_16x16x32_bf16 v[172:175], v[164:167], a[228:231], v[172:175]\n"
        "v_mul_f32 v123, v117, v123\n"
        "v_cvt_pk_bf16_f32 v120, v120, v121\n"
        "v_mfma_f32_16x16x32_bf16 v[200:203], v[152:155], a[228:231], v[200:203]\n"
        "ds_read_b128 a[248:251], v254 offset:12336\n"
        "v_cvt_pk_bf16_f32 v121, v122, v123\n"
        "v_mfma_f32_16x16x32_bf16 v[200:203], v[160:163], a[224:227], v[200:203]\n"
        "s_waitcnt lgkmcnt(6)\n"
        "ds_read_b128 a[216:219], v254 offset:49200\n"
        "v_mul_f32 v124, v117, v124\n"
        "v_mfma_f32_16x16x32_bf16 v[204:207], v[160:163], a[232:235], v[204:207]\n"
        "s_waitcnt lgkmcnt(6)\n"
        "ds_write_b32 v245, v120 offset:512\n"
        "v_mfma_f32_16x16x32_bf16 v[204:207], v[152:155], a[236:239], v[204:207]\n"
        "ds_write_b32 v245, v121 offset:516\n"
        "v_mfma_f32_16x16x32_bf16 v[176:179], v[164:167], a[236:239], v[176:179]\n"
        "ds_read_b128 a[220:223], v248 offset:12352\n"
        "v_mul_f32 v125, v117, v125\n"
        "v_mfma_f32_16x16x32_bf16 v[176:179], v[156:159], a[232:235], v[176:179]\n"
        "s_waitcnt lgkmcnt(6)\n"
        "ds_read_b128 a[224:227], v248 offset:49216\n"
        "v_maximum3_f32 v253, v253, v116, v117\n"
        "v_mfma_f32_16x16x32_bf16 v[180:183], v[156:159], a[240:243], v[180:183]\n"
        "s_waitcnt lgkmcnt(6)\n"
        "v_mul_f32 v126, v117, v126\n"
        "v_mul_f32 v127, v117, v127\n"
        "v_mfma_f32_16x16x32_bf16 v[180:183], v[164:167], a[244:247], v[180:183]\n"
        "v_cvt_pk_bf16_f32 v124, v124, v125\n"
        "v_cvt_pk_bf16_f32 v125, v126, v127\n"
        "v_mfma_f32_16x16x32_bf16 v[212:215], v[152:155], a[244:247], v[212:215]\n"
        "ds_read_b128 a[228:231], v254 offset:12368\n"
        "v_mul_f32 v96, v117, v96\n"
        "v_mfma_f32_16x16x32_bf16 v[212:215], v[160:163], a[240:243], v[212:215]\n"
        "s_waitcnt lgkmcnt(6)\n"
        "ds_read_b128 a[232:235], v254 offset:49232\n"
        "v_mul_f32 v97, v117, v97\n"
        "v_mfma_f32_16x16x32_bf16 v[216:219], v[160:163], a[248:251], v[216:219]\n"
        "s_waitcnt lgkmcnt(6)\n"
        "ds_write_b32 v245, v124 offset:520\n"
        "v_mfma_f32_16x16x32_bf16 v[216:219], v[152:155], a[216:219], v[216:219]\n"
        "ds_write_b32 v245, v125 offset:524\n"
        "v_mfma_f32_16x16x32_bf16 v[184:187], v[164:167], a[216:219], v[184:187]\n"
        "v_mul_f32 v98, v117, v98\n"
        "v_mul_f32 v99, v117, v99\n"
        "v_mfma_f32_16x16x32_bf16 v[184:187], v[156:159], a[248:251], v[184:187]\n"
        "s_waitcnt lgkmcnt(5)\n"
        "s_waitcnt vmcnt(0)\n"
        "s_barrier\n"
        "v_cvt_pk_bf16_f32 v96, v96, v97\n"
        "v_cvt_pk_bf16_f32 v97, v98, v99\n"
        "v_mfma_f32_16x16x32_bf16 v[192:195], v[156:159], a[220:223], v[192:195]\n"
        "s_waitcnt lgkmcnt(4)\n"
        "ds_write_b32 v246, v96 offset:512\n"
        "v_mfma_f32_16x16x32_bf16 v[192:195], v[164:167], a[224:227], v[192:195]\n"
        "ds_write_b32 v246, v97 offset:516\n"
        "v_mfma_f32_16x16x32_bf16 v[220:223], v[152:155], a[224:227], v[220:223]\n"
        "ds_read_b128 a[236:239], v240 offset:0\n"
        "v_mov_b64 v[80:81], 0\n"
        "v_mfma_f32_16x16x32_bf16 v[220:223], v[160:163], a[220:223], v[220:223]\n"
        "s_waitcnt lgkmcnt(6)\n"
        "ds_read_b128 a[240:243], v241 offset:0\n"
        "v_add_u32 v247, s31, v247\n"
        "v_mfma_f32_16x16x32_bf16 v[224:227], v[160:163], a[228:231], v[224:227]\n"
        "s_waitcnt lgkmcnt(6)\n"
        "ds_read_b128 a[244:247], v240 offset:128\n"
        "v_mfma_f32_16x16x32_bf16 v[224:227], v[152:155], a[232:235], v[224:227]\n"
        "ds_read_b128 a[248:251], v241 offset:128\n"
        "v_mfma_f32_16x16x32_bf16 v[196:199], v[164:167], a[232:235], v[196:199]\n"
        "ds_read_b128 a[216:219], v240 offset:256\n"
        "v_mfma_f32_16x16x32_bf16 v[196:199], v[156:159], a[228:231], v[196:199]\n"
        "ds_read_b128 a[220:223], v241 offset:256\n"
        "v_smfmac_f32_16x16x64_bf16 v[80:83], a[252:255], v[88:95], v255\n"
        "v_mfma_f32_16x16x32_bf16 v[84:87], v[128:131], v[88:91], 0\n"
        "v_mfma_f32_16x16x32_bf16 v[84:87], v[132:135], v[92:95], v[84:87]\n"
        "v_mfma_f32_16x16x32_bf16 v[232:235], v[140:143], v[92:95], 0\n"
        "v_mfma_f32_16x16x32_bf16 v[232:235], v[136:139], v[88:91], v[232:235]\n"
        "v_mfma_f32_16x16x32_bf16 v[236:239], v[144:147], v[88:91], 0\n"
        "v_mfma_f32_16x16x32_bf16 v[236:239], v[148:151], v[92:95], v[236:239]\n"
        "s_waitcnt lgkmcnt(0)\n"
        ".p2align 6\n"
        "10:\n"
        "s_waitcnt lgkmcnt(5)\n"
        "v_mfma_f32_16x16x32_bf16 v[96:99], a[72:75], a[236:239], 0\n"
        "s_waitcnt lgkmcnt(4)\n"
        "v_and_b32 v117, 60, v250\n"
        "v_add3_u32 v117, v250, v117, s26\n"
        "v_mfma_f32_16x16x32_bf16 v[96:99], a[76:79], a[240:243], v[96:99]\n"
        "s_waitcnt lgkmcnt(3)\n"
        "v_cvt_pk_bf16_f32 v108, v172, v176\n"
        "v_cvt_pk_bf16_f32 v109, v180, v184\n"
        "v_mfma_f32_16x16x32_bf16 v[96:99], a[80:83], a[244:247], v[96:99]\n"
        "s_waitcnt lgkmcnt(2)\n"
        "v_cvt_pk_bf16_f32 v110, v192, v196\n"
        "v_add_f32 v80, v80, v81\n"
        "v_mfma_f32_16x16x32_bf16 v[96:99], a[84:87], a[248:251], v[96:99]\n"
        "s_waitcnt lgkmcnt(1)\n"
        "buffer_store_dwordx2 v[108:109], v117, s[16:19], s38 offen offset:0 sc1\n"
        "v_mfma_f32_16x16x32_bf16 v[96:99], a[88:91], a[216:219], v[96:99]\n"
        "s_waitcnt lgkmcnt(0)\n"
        "v_rcp_f32 v81, v80\n"
        "v_mfma_f32_16x16x32_bf16 v[96:99], a[92:95], a[220:223], v[96:99]\n"
        "s_add_u32 s41, s29, s40\n"
        "v_cvt_pk_bf16_f32 v112, v173, v177\n"
        "v_cvt_pk_bf16_f32 v113, v181, v185\n"
        "v_mfma_f32_16x16x32_bf16 v[100:103], a[116:119], a[220:223], 0\n"
        "s_add_u32 s35, s47, 786432\n"
        "v_mul_f32 v84, v81, v84\n"
        "v_mul_f32 v85, v81, v85\n"
        "v_mfma_f32_16x16x32_bf16 v[100:103], a[112:115], a[216:219], v[100:103]\n"
        "s_sub_u32 s46, s35, 100663296\n"
        "v_cvt_pk_bf16_f32 v152, v96, v97\n"
        "v_cvt_pk_bf16_f32 v153, v98, v99\n"
        "v_mfma_f32_16x16x32_bf16 v[100:103], a[108:111], a[248:251], v[100:103]\n"
        "s_cmp_lt_u32 s46, 786432\n"
        "buffer_store_dword v110, v250, s[16:19], s38 offen offset:0 sc1\n"
        "v_mfma_f32_16x16x32_bf16 v[100:103], a[104:107], a[244:247], v[100:103]\n"
        "s_cselect_b32 s35, s46, s35\n"
        "v_cvt_pk_bf16_f32 v114, v193, v197\n"
        "v_mul_f32 v86, v81, v86\n"
        "v_mfma_f32_16x16x32_bf16 v[100:103], a[100:103], a[240:243], v[100:103]\n"
        "s_sub_u32 s31, s28, s30\n"
        "v_mul_f32 v87, v81, v87\n"
        "v_cvt_pk_bf16_f32 v84, v84, v85\n"
        "v_mfma_f32_16x16x32_bf16 v[100:103], a[96:99], a[236:239], v[100:103]\n"
        "s_sub_u32 s45, s29, s28\n"
        "ds_write_b32 v245, v84 offset:768\n"
        "v_mfma_f32_16x16x32_bf16 v[104:107], a[120:123], a[236:239], 0\n"
        "s_mov_b32 s29, s30\n"
        "v_cvt_pk_bf16_f32 v85, v86, v87\n"
        "v_mul_f32 v232, v81, v232\n"
        "v_mfma_f32_16x16x32_bf16 v[104:107], a[124:127], a[240:243], v[104:107]\n"
        "s_mov_b32 s30, s28\n"
        "buffer_store_dwordx2 v[112:113], v117, s[16:19], s38 offen offset:384 sc1\n"
        "v_mfma_f32_16x16x32_bf16 v[104:107], a[128:131], a[244:247], v[104:107]\n"
        "s_add_u32 s28, s28, s45\n"
        "v_cvt_pk_bf16_f32 v154, v100, v101\n"
        "v_cvt_pk_bf16_f32 v155, v102, v103\n"
        "v_mfma_f32_16x16x32_bf16 v[104:107], a[132:135], a[248:251], v[104:107]\n"
        "ds_write_b32 v245, v85 offset:772\n"
        "v_mfma_f32_16x16x32_bf16 v[104:107], a[136:139], a[216:219], v[104:107]\n"
        "v_cvt_pk_bf16_f32 v108, v174, v178\n"
        "v_cvt_pk_bf16_f32 v109, v182, v186\n"
        "v_mfma_f32_16x16x32_bf16 v[104:107], a[140:143], a[220:223], v[104:107]\n"
        "v_mul_f32 v233, v81, v233\n"
        "v_maximum3_f32 v253, v253, v80, v81\n"
        "v_mfma_f32_16x16x32_bf16 v[96:99], a[20:23], a[220:223], 0\n"
        "buffer_store_dword v114, v250, s[16:19], s38 offen offset:384 sc1\n"
        "v_mfma_f32_16x16x32_bf16 v[96:99], a[16:19], a[216:219], v[96:99]\n"
        "v_cvt_pk_bf16_f32 v110, v194, v198\n"
        "v_mul_f32 v234, v81, v234\n"
        "v_mfma_f32_16x16x32_bf16 v[96:99], a[12:15], a[248:251], v[96:99]\n"
        "v_cvt_pk_bf16_f32 v168, v104, v105\n"
        "v_cvt_pk_bf16_f32 v169, v106, v107\n"
        "v_mfma_f32_16x16x32_bf16 v[96:99], a[8:11], a[244:247], v[96:99]\n"
        "v_mul_f32 v235, v81, v235\n"
        "v_cvt_pk_bf16_f32 v232, v232, v233\n"
        "v_mfma_f32_16x16x32_bf16 v[96:99], a[4:7], a[240:243], v[96:99]\n"
        "ds_write_b32 v245, v232 offset:776\n"
        "v_mfma_f32_16x16x32_bf16 v[96:99], a[0:3], a[236:239], v[96:99]\n"
        "buffer_store_dwordx2 v[108:109], v117, s[16:19], s38 offen offset:768 sc1\n"
        "v_mfma_f32_16x16x32_bf16 v[100:103], a[24:27], a[236:239], 0\n"
        "v_cvt_pk_bf16_f32 v112, v175, v179\n"
        "v_cvt_pk_bf16_f32 v113, v183, v187\n"
        "v_mfma_f32_16x16x32_bf16 v[100:103], a[28:31], a[240:243], v[100:103]\n"
        "v_cvt_pk_bf16_f32 v233, v234, v235\n"
        "v_xor_b32 v251, 0x2000, v251\n"
        "v_mfma_f32_16x16x32_bf16 v[100:103], a[32:35], a[244:247], v[100:103]\n"
        "v_mul_f32 v96, s50, v96\n"
        "v_mul_f32 v97, s50, v97\n"
        "v_mfma_f32_16x16x32_bf16 v[100:103], a[36:39], a[248:251], v[100:103]\n"
        "v_mul_f32 v98, s50, v98\n"
        "v_mul_f32 v99, s50, v99\n"
        "v_mfma_f32_16x16x32_bf16 v[100:103], a[40:43], a[216:219], v[100:103]\n"
        "v_cvt_pk_bf16_f32 v156, v96, v97\n"
        "v_cvt_pk_bf16_f32 v157, v98, v99\n"
        "v_mfma_f32_16x16x32_bf16 v[100:103], a[44:47], a[220:223], v[100:103]\n"
        "buffer_store_dword v110, v250, s[16:19], s38 offen offset:768 sc1\n"
        "v_mfma_f32_16x16x32_bf16 v[104:107], a[68:71], a[220:223], 0\n"
        "ds_write_b32 v245, v233 offset:780\n"
        "v_mfma_f32_16x16x32_bf16 v[104:107], a[64:67], a[216:219], v[104:107]\n"
        "v_cvt_pk_bf16_f32 v114, v195, v199\n"
        "v_xor_b32 v252, 0x2000, v252\n"
        "v_mfma_f32_16x16x32_bf16 v[104:107], a[60:63], a[248:251], v[104:107]\n"
        "ds_read_b128 a[224:227], v240 offset:1536\n"
        "v_mul_f32 v100, s50, v100\n"
        "v_mfma_f32_16x16x32_bf16 v[104:107], a[56:59], a[244:247], v[104:107]\n"
        "v_mul_f32 v101, s50, v101\n"
        "v_mul_f32 v102, s50, v102\n"
        "v_mfma_f32_16x16x32_bf16 v[104:107], a[52:55], a[240:243], v[104:107]\n"
        "v_mul_f32 v103, s50, v103\n"
        "v_cvt_pk_bf16_f32 v158, v100, v101\n"
        "v_mfma_f32_16x16x32_bf16 v[104:107], a[48:51], a[236:239], v[104:107]\n"
        "buffer_store_dwordx2 v[112:113], v117, s[16:19], s38 offen offset:1152 sc1\n"
        "v_mfma_f32_16x16x32_bf16 v[96:99], a[236:239], a[144:147], 0\n"
        "ds_read_b128 a[228:231], v241 offset:1536\n"
        "v_cvt_pk_bf16_f32 v159, v102, v103\n"
        "v_mfma_f32_16x16x32_bf16 v[96:99], a[240:243], a[148:151], v[96:99]\n"
        "ds_read_b128 a[232:235], v240 offset:1664\n"
        "v_cvt_pk_bf16_f32 v108, v200, v204\n"
        "v_mfma_f32_16x16x32_bf16 v[96:99], a[244:247], a[152:155], v[96:99]\n"
        "v_mul_f32 v104, s50, v104\n"
        "v_mul_f32 v105, s50, v105\n"
        "v_mfma_f32_16x16x32_bf16 v[96:99], a[248:251], a[156:159], v[96:99]\n"
        "buffer_store_dword v114, v250, s[16:19], s38 offen offset:1152 sc1\n"
        "v_mfma_f32_16x16x32_bf16 v[96:99], a[216:219], a[160:163], v[96:99]\n"
        "v_mul_f32 v106, s50, v106\n"
        "v_mul_f32 v107, s50, v107\n"
        "v_mfma_f32_16x16x32_bf16 v[96:99], a[220:223], a[164:167], v[96:99]\n"
        "v_cvt_pk_bf16_f32 v160, v104, v105\n"
        "v_cvt_pk_bf16_f32 v161, v106, v107\n"
        "v_mfma_f32_16x16x32_bf16 v[100:103], a[220:223], a[188:191], 0\n"
        "v_mov_b64 v[166:167], v[160:161]\n"
        "v_cvt_pk_bf16_f32 v109, v212, v216\n"
        "v_mfma_f32_16x16x32_bf16 v[100:103], a[216:219], a[184:187], v[100:103]\n"
        "buffer_store_dwordx2 v[108:109], v117, s[16:19], s39 offen offset:0 sc1\n"
        "v_mfma_f32_16x16x32_bf16 v[100:103], a[248:251], a[180:183], v[100:103]\n"
        "v_cvt_pk_bf16_f32 v128, v96, v97\n"
        "v_cvt_pk_bf16_f32 v129, v98, v99\n"
        "v_mfma_f32_16x16x32_bf16 v[100:103], a[244:247], a[176:179], v[100:103]\n"
        "v_cvt_pk_bf16_f32 v110, v220, v224\n"
        "v_cvt_pk_bf16_f32 v112, v201, v205\n"
        "v_mfma_f32_16x16x32_bf16 v[100:103], a[240:243], a[172:175], v[100:103]\n"
        "v_cvt_pk_bf16_f32 v113, v213, v217\n"
        "v_mul_f32 v236, v81, v236\n"
        "v_mfma_f32_16x16x32_bf16 v[100:103], a[236:239], a[168:171], v[100:103]\n"
        "v_mul_f32 v237, v81, v237\n"
        "v_mul_f32 v238, v81, v238\n"
        "v_mfma_f32_16x16x32_bf16 v[104:107], a[236:239], a[192:195], 0\n"
        "ds_read_b128 a[236:239], v241 offset:1664\n"
        "v_mul_f32 v239, v81, v239\n"
        "v_mfma_f32_16x16x32_bf16 v[104:107], a[240:243], a[196:199], v[104:107]\n"
        "ds_read_b128 a[240:243], v240 offset:1792\n"
        "v_cvt_pk_bf16_f32 v236, v236, v237\n"
        "v_mfma_f32_16x16x32_bf16 v[104:107], a[244:247], a[200:203], v[104:107]\n"
        "ds_read_b128 a[244:247], v241 offset:1792\n"
        "v_cvt_pk_bf16_f32 v136, v100, v101\n"
        "v_mfma_f32_16x16x32_bf16 v[104:107], a[248:251], a[204:207], v[104:107]\n"
        "buffer_store_dword v110, v250, s[16:19], s39 offen offset:0 sc1\n"
        "v_mfma_f32_16x16x32_bf16 v[104:107], a[216:219], a[208:211], v[104:107]\n"
        "v_cvt_pk_bf16_f32 v137, v102, v103\n"
        "v_cvt_pk_bf16_f32 v237, v238, v239\n"
        "v_mfma_f32_16x16x32_bf16 v[104:107], a[220:223], a[212:215], v[104:107]\n"
        "s_waitcnt lgkmcnt(5)\n"
        "buffer_store_dwordx2 v[112:113], v117, s[16:19], s39 offen offset:384 sc1\n"
        "v_mfma_f32_16x16x32_bf16 v[96:99], a[72:75], a[224:227], 0\n"
        "s_waitcnt lgkmcnt(4)\n"
        "ds_write_b32 v246, v236 offset:768\n"
        "v_mfma_f32_16x16x32_bf16 v[96:99], a[76:79], a[228:231], v[96:99]\n"
        "s_waitcnt lgkmcnt(4)\n"
        "ds_write_b32 v246, v237 offset:772\n"
        "v_mfma_f32_16x16x32_bf16 v[96:99], a[80:83], a[232:235], v[96:99]\n"
        "s_waitcnt lgkmcnt(4)\n"
        "ds_read_b128 v[162:165], v251 offset:0\n"
        "v_cvt_pk_bf16_f32 v114, v221, v225\n"
        "v_mfma_f32_16x16x32_bf16 v[96:99], a[84:87], a[236:239], v[96:99]\n"
        "s_waitcnt lgkmcnt(4)\n"
        "v_cvt_pk_bf16_f32 v144, v104, v105\n"
        "v_cvt_pk_bf16_f32 v145, v106, v107\n"
        "v_mfma_f32_16x16x32_bf16 v[96:99], a[88:91], a[240:243], v[96:99]\n"
        "s_waitcnt lgkmcnt(3)\n"
        "buffer_store_dword v114, v250, s[16:19], s39 offen offset:384 sc1\n"
        "v_mfma_f32_16x16x32_bf16 v[96:99], a[92:95], a[244:247], v[96:99]\n"
        "v_cvt_pk_bf16_f32 v108, v202, v206\n"
        "v_cvt_pk_bf16_f32 v109, v214, v218\n"
        "v_mfma_f32_16x16x32_bf16 v[100:103], a[116:119], a[244:247], 0\n"
        "v_cvt_pk_bf16_f32 v110, v222, v226\n"
        "v_add_u32 v245, s31, v245\n"
        "v_mfma_f32_16x16x32_bf16 v[100:103], a[112:115], a[240:243], v[100:103]\n"
        "v_add_u32 v246, s31, v246\n"
        "v_mfma_f32_16x16x32_bf16 v[100:103], a[108:111], a[236:239], v[100:103]\n"
        "v_cvt_pk_bf16_f32 v172, v96, v97\n"
        "v_cvt_pk_bf16_f32 v173, v98, v99\n"
        "v_mfma_f32_16x16x32_bf16 v[100:103], a[104:107], a[232:235], v[100:103]\n"
        "buffer_store_dwordx2 v[108:109], v117, s[16:19], s39 offen offset:768 sc1\n"
        "v_mfma_f32_16x16x32_bf16 v[100:103], a[100:103], a[228:231], v[100:103]\n"
        "v_cvt_pk_bf16_f32 v112, v203, v207\n"
        "v_cvt_pk_bf16_f32 v113, v215, v219\n"
        "v_mfma_f32_16x16x32_bf16 v[100:103], a[96:99], a[224:227], v[100:103]\n"
        "v_mfma_f32_16x16x32_bf16 v[104:107], a[120:123], a[224:227], 0\n"
        "v_mfma_f32_16x16x32_bf16 v[104:107], a[124:127], a[228:231], v[104:107]\n"
        "v_mfma_f32_16x16x32_bf16 v[104:107], a[128:131], a[232:235], v[104:107]\n"
        "buffer_store_dword v110, v250, s[16:19], s39 offen offset:768 sc1\n"
        "v_mfma_f32_16x16x32_bf16 v[104:107], a[132:135], a[236:239], v[104:107]\n"
        "v_cvt_pk_bf16_f32 v114, v223, v227\n"
        "v_mfma_f32_16x16x32_bf16 v[104:107], a[136:139], a[240:243], v[104:107]\n"
        "v_mfma_f32_16x16x32_bf16 v[104:107], a[140:143], a[244:247], v[104:107]\n"
        "v_cvt_pk_bf16_f32 v174, v100, v101\n"
        "v_cvt_pk_bf16_f32 v175, v102, v103\n"
        "v_mfma_f32_16x16x32_bf16 v[96:99], a[20:23], a[244:247], 0\n"
        "v_mfma_f32_16x16x32_bf16 v[96:99], a[16:19], a[240:243], v[96:99]\n"
        "buffer_store_dwordx2 v[112:113], v117, s[16:19], s39 offen offset:1152 sc1\n"
        "v_mfma_f32_16x16x32_bf16 v[96:99], a[12:15], a[236:239], v[96:99]\n"
        "v_mfma_f32_16x16x32_bf16 v[96:99], a[8:11], a[232:235], v[96:99]\n"
        "v_mfma_f32_16x16x32_bf16 v[96:99], a[4:7], a[228:231], v[96:99]\n"
        "v_cvt_pk_bf16_f32 v190, v104, v105\n"
        "v_cvt_pk_bf16_f32 v191, v106, v107\n"
        "v_mfma_f32_16x16x32_bf16 v[96:99], a[0:3], a[224:227], v[96:99]\n"
        "ds_read_b128 v[182:185], v251 offset:512\n"
        "v_mfma_f32_16x16x32_bf16 v[64:67], v[152:155], v[156:159], v[0:3]\n"
        "s_waitcnt lgkmcnt(1)\n"
        "buffer_store_dword v114, v250, s[16:19], s39 offen offset:1152 sc1\n"
        "v_mfma_f32_16x16x32_bf16 v[64:67], v[168:171], v[160:163], v[64:67]\n"
        "v_mfma_f32_16x16x32_bf16 v[68:71], v[172:175], v[156:159], v[4:7]\n"
        "v_mfma_f32_16x16x32_bf16 v[68:71], v[188:191], v[164:167], v[68:71]\n"
        "v_mfma_f32_16x16x32_bf16 v[100:103], a[24:27], a[224:227], 0\n"
        "v_mfma_f32_16x16x32_bf16 v[100:103], a[28:31], a[228:231], v[100:103]\n"
        "v_mul_f32 v96, s50, v96\n"
        "v_mul_f32 v97, s50, v97\n"
        "v_mfma_f32_16x16x32_bf16 v[100:103], a[32:35], a[232:235], v[100:103]\n"
        "s_add_u32 m0, s41, 0\n"
        "v_mul_f32 v98, s50, v98\n"
        "v_mul_f32 v99, s50, v99\n"
        "v_mfma_f32_16x16x32_bf16 v[100:103], a[36:39], a[236:239], v[100:103]\n"
        "v_cvt_pk_bf16_f32 v176, v96, v97\n"
        "v_cvt_pk_bf16_f32 v177, v98, v99\n"
        "v_mfma_f32_16x16x32_bf16 v[100:103], a[40:43], a[240:243], v[100:103]\n"
        "buffer_load_dwordx4 v242, s[8:11], s47 offen lds\n"
        "v_mfma_f32_16x16x32_bf16 v[100:103], a[44:47], a[244:247], v[100:103]\n"
        "v_exp_f32 v64, v64\n"
        "v_mfma_f32_16x16x32_bf16 v[104:107], a[68:71], a[244:247], 0\n"
        "v_exp_f32 v65, v65\n"
        "v_mfma_f32_16x16x32_bf16 v[104:107], a[64:67], a[240:243], v[104:107]\n"
        "s_add_u32 m0, s41, 1024\n"
        "v_exp_f32 v66, v66\n"
        "v_mfma_f32_16x16x32_bf16 v[104:107], a[60:63], a[236:239], v[104:107]\n"
        "v_exp_f32 v67, v67\n"
        "v_mfma_f32_16x16x32_bf16 v[104:107], a[56:59], a[232:235], v[104:107]\n"
        "buffer_load_dwordx4 v243, s[8:11], s47 offen lds\n"
        "v_mfma_f32_16x16x32_bf16 v[104:107], a[52:55], a[228:231], v[104:107]\n"
        "v_mul_f32 v100, s50, v100\n"
        "v_mul_f32 v101, s50, v101\n"
        "v_mfma_f32_16x16x32_bf16 v[104:107], a[48:51], a[224:227], v[104:107]\n"
        "v_mul_f32 v102, s50, v102\n"
        "v_mul_f32 v103, s50, v103\n"
        "v_mfma_f32_16x16x32_bf16 v[96:99], a[224:227], a[144:147], 0\n"
        "s_add_u32 m0, s41, 2048\n"
        "v_cvt_pk_bf16_f32 v178, v100, v101\n"
        "v_cvt_pk_bf16_f32 v179, v102, v103\n"
        "v_mfma_f32_16x16x32_bf16 v[96:99], a[228:231], a[148:151], v[96:99]\n"
        "ds_read_b128 a[248:251], v240 offset:12288\n"
        "v_mul_f32 v104, s50, v104\n"
        "v_mfma_f32_16x16x32_bf16 v[96:99], a[232:235], a[152:155], v[96:99]\n"
        "buffer_load_dwordx4 v244, s[8:11], s47 offen lds\n"
        "v_mfma_f32_16x16x32_bf16 v[96:99], a[236:239], a[156:159], v[96:99]\n"
        "v_mul_f32 v105, s50, v105\n"
        "v_mul_f32 v106, s50, v106\n"
        "v_mfma_f32_16x16x32_bf16 v[96:99], a[240:243], a[160:163], v[96:99]\n"
        "v_mul_f32 v107, s50, v107\n"
        "v_cvt_pk_bf16_f32 v180, v104, v105\n"
        "v_mfma_f32_16x16x32_bf16 v[96:99], a[244:247], a[164:167], v[96:99]\n"
        "s_add_u32 m0, s41, 12288\n"
        "ds_read_b128 a[216:219], v241 offset:12288\n"
        "v_cvt_pk_bf16_f32 v181, v106, v107\n"
        "v_mfma_f32_16x16x32_bf16 v[72:75], v[152:155], v[176:179], v[16:19]\n"
        "s_waitcnt lgkmcnt(2)\n"
        "ds_read_b128 a[220:223], v240 offset:12416\n"
        "v_mov_b64 v[186:187], v[180:181]\n"
        "v_mfma_f32_16x16x32_bf16 v[72:75], v[168:171], v[180:183], v[72:75]\n"
        "buffer_load_dwordx4 v242, s[8:11], s35 offen lds\n"
        "v_mfma_f32_16x16x32_bf16 v[76:79], v[172:175], v[176:179], v[20:23]\n"
        "v_cvt_pk_bf16_f32 v130, v96, v97\n"
        "v_cvt_pk_bf16_f32 v131, v98, v99\n"
        "v_mfma_f32_16x16x32_bf16 v[76:79], v[188:191], v[184:187], v[76:79]\n"
        "v_exp_f32 v68, v68\n"
        "v_mfma_f32_16x16x32_bf16 v[100:103], a[244:247], a[188:191], 0\n"
        "s_add_u32 m0, s41, 13312\n"
        "v_exp_f32 v69, v69\n"
        "v_mfma_f32_16x16x32_bf16 v[100:103], a[240:243], a[184:187], v[100:103]\n"
        "v_exp_f32 v70, v70\n"
        "v_mfma_f32_16x16x32_bf16 v[100:103], a[236:239], a[180:183], v[100:103]\n"
        "buffer_load_dwordx4 v243, s[8:11], s35 offen lds\n"
        "v_mfma_f32_16x16x32_bf16 v[100:103], a[232:235], a[176:179], v[100:103]\n"
        "v_exp_f32 v71, v71\n"
        "v_mfma_f32_16x16x32_bf16 v[100:103], a[228:231], a[172:175], v[100:103]\n"
        "v_exp_f32 v72, v72\n"
        "v_mfma_f32_16x16x32_bf16 v[100:103], a[224:227], a[168:171], v[100:103]\n"
        "s_add_u32 m0, s41, 14336\n"
        "v_exp_f32 v73, v73\n"
        "v_mfma_f32_16x16x32_bf16 v[104:107], a[224:227], a[192:195], 0\n"
        "ds_read_b128 a[224:227], v241 offset:12416\n"
        "v_cvt_pk_bf16_f32 v64, v64, v65\n"
        "v_mfma_f32_16x16x32_bf16 v[104:107], a[228:231], a[196:199], v[104:107]\n"
        "ds_read_b128 a[228:231], v240 offset:12544\n"
        "v_cvt_pk_bf16_f32 v65, v66, v67\n"
        "v_mfma_f32_16x16x32_bf16 v[104:107], a[232:235], a[200:203], v[104:107]\n"
        "ds_read_b128 a[232:235], v241 offset:12544\n"
        "v_cvt_pk_bf16_f32 v138, v100, v101\n"
        "v_mfma_f32_16x16x32_bf16 v[104:107], a[236:239], a[204:207], v[104:107]\n"
        "buffer_load_dwordx4 v244, s[8:11], s35 offen lds\n"
        "v_mfma_f32_16x16x32_bf16 v[104:107], a[240:243], a[208:211], v[104:107]\n"
        "s_mov_b32 m0, s48\n"
        "v_cvt_pk_bf16_f32 v139, v102, v103\n"
        "v_cvt_pk_bf16_f32 v66, v68, v69\n"
        "v_mfma_f32_16x16x32_bf16 v[104:107], a[244:247], a[212:215], v[104:107]\n"
        "s_waitcnt lgkmcnt(5)\n"
        "v_exp_f32 v74, v74\n"
        "v_mfma_f32_16x16x32_bf16 v[96:99], a[72:75], a[248:251], 0\n"
        "s_waitcnt lgkmcnt(4)\n"
        "buffer_load_dwordx4 v249, s[12:15], s36 offen offset:0 lds\n"
        "v_mfma_f32_16x16x32_bf16 v[96:99], a[76:79], a[216:219], v[96:99]\n"
        "s_waitcnt lgkmcnt(3)\n"
        "ds_read_b128 v[162:165], v252 offset:0\n"
        "v_xor_b32 v116, 32, v249\n"
        "v_mfma_f32_16x16x32_bf16 v[96:99], a[80:83], a[220:223], v[96:99]\n"
        "s_waitcnt lgkmcnt(3)\n"
        "ds_read_b128 v[182:185], v252 offset:512\n"
        "v_cvt_pk_bf16_f32 v146, v104, v105\n"
        "v_mfma_f32_16x16x32_bf16 v[96:99], a[84:87], a[224:227], v[96:99]\n"
        "s_waitcnt lgkmcnt(3)\n"
        "v_cvt_pk_bf16_f32 v147, v106, v107\n"
        "v_cvt_pk_bf16_f32 v67, v70, v71\n"
        "v_mfma_f32_16x16x32_bf16 v[96:99], a[88:91], a[228:231], v[96:99]\n"
        "s_waitcnt lgkmcnt(2)\n"
        "v_exp_f32 v75, v75\n"
        "v_mfma_f32_16x16x32_bf16 v[96:99], a[92:95], a[232:235], v[96:99]\n"
        "buffer_load_dwordx4 v116, s[12:15], s36 offen offset:1024 lds\n"
        "v_mfma_f32_16x16x32_bf16 v[100:103], a[116:119], a[232:235], 0\n"
        "v_exp_f32 v76, v76\n"
        "v_mfma_f32_16x16x32_bf16 v[100:103], a[112:115], a[228:231], v[100:103]\n"
        "v_exp_f32 v77, v77\n"
        "v_mfma_f32_16x16x32_bf16 v[100:103], a[108:111], a[224:227], v[100:103]\n"
        "v_exp_f32 v78, v78\n"
        "v_mfma_f32_16x16x32_bf16 v[100:103], a[104:107], a[220:223], v[100:103]\n"
        "v_cvt_pk_bf16_f32 v192, v96, v97\n"
        "v_cvt_pk_bf16_f32 v193, v98, v99\n"
        "v_mfma_f32_16x16x32_bf16 v[100:103], a[100:103], a[216:219], v[100:103]\n"
        "v_exp_f32 v79, v79\n"
        "v_mfma_f32_16x16x32_bf16 v[100:103], a[96:99], a[248:251], v[100:103]\n"
        "v_cvt_pk_bf16_f32 v72, v72, v73\n"
        "v_cvt_pk_bf16_f32 v73, v74, v75\n"
        "v_mfma_f32_16x16x32_bf16 v[104:107], a[120:123], a[248:251], 0\n"
        "v_cvt_pk_bf16_f32 v74, v76, v77\n"
        "v_cvt_pk_bf16_f32 v75, v78, v79\n"
        "v_mfma_f32_16x16x32_bf16 v[104:107], a[124:127], a[216:219], v[104:107]\n"
        "v_mfma_f32_16x16x32_bf16 v[104:107], a[128:131], a[220:223], v[104:107]\n"
        "v_mfma_f32_16x16x32_bf16 v[104:107], a[132:135], a[224:227], v[104:107]\n"
        "v_cvt_pk_bf16_f32 v194, v100, v101\n"
        "v_cvt_pk_bf16_f32 v195, v102, v103\n"
        "v_mfma_f32_16x16x32_bf16 v[104:107], a[136:139], a[228:231], v[104:107]\n"
        "44:\n"
        "s_cbranch_execz 40b\n"
        "v_mfma_f32_16x16x32_bf16 v[104:107], a[140:143], a[232:235], v[104:107]\n"
        "v_mfma_f32_16x16x32_bf16 v[96:99], a[20:23], a[232:235], 0\n"
        "v_mfma_f32_16x16x32_bf16 v[96:99], a[16:19], a[228:231], v[96:99]\n"
        "v_mfma_f32_16x16x32_bf16 v[96:99], a[12:15], a[224:227], v[96:99]\n"
        "v_mfma_f32_16x16x32_bf16 v[96:99], a[8:11], a[220:223], v[96:99]\n"
        "v_mfma_f32_16x16x32_bf16 v[96:99], a[4:7], a[216:219], v[96:99]\n"
        "ds_read_b128 v[202:205], v251 offset:4096\n"
        "v_mfma_f32_16x16x32_bf16 v[96:99], a[0:3], a[248:251], v[96:99]\n"
        "v_mfma_f32_16x16x32_bf16 v[68:71], v[192:195], v[156:159], v[8:11]\n"
        "s_waitcnt lgkmcnt(2)\n"
        "v_cvt_pk_bf16_f32 v208, v104, v105\n"
        "v_cvt_pk_bf16_f32 v209, v106, v107\n"
        "s_nop 1\n"
        "v_mfma_f32_16x16x32_bf16 v[68:71], v[208:211], v[160:163], v[68:71]\n"
        "v_mfma_f32_16x16x32_bf16 v[76:79], v[192:195], v[176:179], v[24:27]\n"
        "s_waitcnt lgkmcnt(1)\n"
        "v_mfma_f32_16x16x32_bf16 v[76:79], v[208:211], v[180:183], v[76:79]\n"
        "v_mul_f32 v96, s50, v96\n"
        "v_mul_f32 v97, s50, v97\n"
        "v_mfma_f32_16x16x32_bf16 v[100:103], a[24:27], a[248:251], 0\n"
        "v_mul_f32 v98, s50, v98\n"
        "v_mul_f32 v99, s50, v99\n"
        "v_mfma_f32_16x16x32_bf16 v[100:103], a[28:31], a[216:219], v[100:103]\n"
        "v_cvt_pk_bf16_f32 v196, v96, v97\n"
        "v_cvt_pk_bf16_f32 v197, v98, v99\n"
        "v_mfma_f32_16x16x32_bf16 v[100:103], a[32:35], a[220:223], v[100:103]\n"
        "v_exp_f32 v68, v68\n"
        "v_mfma_f32_16x16x32_bf16 v[100:103], a[36:39], a[224:227], v[100:103]\n"
        "v_exp_f32 v69, v69\n"
        "v_mfma_f32_16x16x32_bf16 v[100:103], a[40:43], a[228:231], v[100:103]\n"
        "v_exp_f32 v70, v70\n"
        "v_mfma_f32_16x16x32_bf16 v[100:103], a[44:47], a[232:235], v[100:103]\n"
        "v_exp_f32 v71, v71\n"
        "v_mfma_f32_16x16x32_bf16 v[104:107], a[68:71], a[232:235], 0\n"
        "v_exp_f32 v76, v76\n"
        "v_mfma_f32_16x16x32_bf16 v[104:107], a[64:67], a[228:231], v[104:107]\n"
        "v_exp_f32 v77, v77\n"
        "v_mfma_f32_16x16x32_bf16 v[104:107], a[60:63], a[224:227], v[104:107]\n"
        "v_exp_f32 v78, v78\n"
        "v_mfma_f32_16x16x32_bf16 v[104:107], a[56:59], a[220:223], v[104:107]\n"
        "v_mul_f32 v100, s50, v100\n"
        "v_mul_f32 v101, s50, v101\n"
        "v_mfma_f32_16x16x32_bf16 v[104:107], a[52:55], a[216:219], v[104:107]\n"
        "v_mul_f32 v102, s50, v102\n"
        "v_mul_f32 v103, s50, v103\n"
        "v_mfma_f32_16x16x32_bf16 v[104:107], a[48:51], a[248:251], v[104:107]\n"
        "v_cvt_pk_bf16_f32 v198, v100, v101\n"
        "v_cvt_pk_bf16_f32 v199, v102, v103\n"
        "v_mfma_f32_16x16x32_bf16 v[96:99], a[248:251], a[144:147], 0\n"
        "ds_read_b128 a[236:239], v240 offset:13824\n"
        "v_cvt_pk_bf16_f32 v68, v68, v69\n"
        "v_mfma_f32_16x16x32_bf16 v[96:99], a[216:219], a[148:151], v[96:99]\n"
        "ds_read_b128 a[240:243], v241 offset:13824\n"
        "v_cvt_pk_bf16_f32 v69, v70, v71\n"
        "v_mfma_f32_16x16x32_bf16 v[96:99], a[220:223], a[152:155], v[96:99]\n"
        "v_mul_f32 v104, s50, v104\n"
        "v_mul_f32 v105, s50, v105\n"
        "v_mfma_f32_16x16x32_bf16 v[96:99], a[224:227], a[156:159], v[96:99]\n"
        "v_mul_f32 v106, s50, v106\n"
        "v_mul_f32 v107, s50, v107\n"
        "v_mfma_f32_16x16x32_bf16 v[96:99], a[228:231], a[160:163], v[96:99]\n"
        "v_cvt_pk_bf16_f32 v200, v104, v105\n"
        "v_cvt_pk_bf16_f32 v201, v106, v107\n"
        "v_mfma_f32_16x16x32_bf16 v[96:99], a[232:235], a[164:167], v[96:99]\n"
        "ds_read_b128 a[244:247], v240 offset:13952\n"
        "v_mov_b64 v[206:207], v[200:201]\n"
        "v_mfma_f32_16x16x32_bf16 v[80:83], v[152:155], v[196:199], v[32:35]\n"
        "s_waitcnt lgkmcnt(3)\n"
        "v_exp_f32 v79, v79\n"
        "v_mfma_f32_16x16x32_bf16 v[80:83], v[168:171], v[200:203], v[80:83]\n"
        "v_cvt_pk_bf16_f32 v76, v76, v77\n"
        "v_cvt_pk_bf16_f32 v77, v78, v79\n"
        "v_mfma_f32_16x16x32_bf16 v[84:87], v[172:175], v[196:199], v[36:39]\n"
        "v_cvt_pk_bf16_f32 v132, v96, v97\n"
        "v_cvt_pk_bf16_f32 v133, v98, v99\n"
        "v_mfma_f32_16x16x32_bf16 v[84:87], v[188:191], v[204:207], v[84:87]\n"
        "ds_read_b128 v[202:205], v252 offset:4096\n"
        "v_mfma_f32_16x16x32_bf16 v[100:103], a[232:235], a[188:191], 0\n"
        "v_exp_f32 v80, v80\n"
        "v_mfma_f32_16x16x32_bf16 v[100:103], a[228:231], a[184:187], v[100:103]\n"
        "v_exp_f32 v81, v81\n"
        "v_mfma_f32_16x16x32_bf16 v[100:103], a[224:227], a[180:183], v[100:103]\n"
        "v_exp_f32 v82, v82\n"
        "v_mfma_f32_16x16x32_bf16 v[100:103], a[220:223], a[176:179], v[100:103]\n"
        "v_exp_f32 v83, v83\n"
        "v_mfma_f32_16x16x32_bf16 v[100:103], a[216:219], a[172:175], v[100:103]\n"
        "v_exp_f32 v84, v84\n"
        "v_mfma_f32_16x16x32_bf16 v[100:103], a[248:251], a[168:171], v[100:103]\n"
        "v_exp_f32 v85, v85\n"
        "v_mfma_f32_16x16x32_bf16 v[104:107], a[248:251], a[192:195], 0\n"
        "ds_read_b128 a[248:251], v241 offset:13952\n"
        "v_cvt_pk_bf16_f32 v80, v80, v81\n"
        "v_mfma_f32_16x16x32_bf16 v[104:107], a[216:219], a[196:199], v[104:107]\n"
        "ds_read_b128 a[216:219], v240 offset:14080\n"
        "v_cvt_pk_bf16_f32 v81, v82, v83\n"
        "v_mfma_f32_16x16x32_bf16 v[104:107], a[220:223], a[200:203], v[104:107]\n"
        "ds_read_b128 a[220:223], v241 offset:14080\n"
        "v_cvt_pk_bf16_f32 v140, v100, v101\n"
        "v_mfma_f32_16x16x32_bf16 v[104:107], a[224:227], a[204:207], v[104:107]\n"
        "v_cvt_pk_bf16_f32 v141, v102, v103\n"
        "v_cvt_pk_bf16_f32 v82, v84, v85\n"
        "v_mfma_f32_16x16x32_bf16 v[104:107], a[228:231], a[208:211], v[104:107]\n"
        "v_exp_f32 v86, v86\n"
        "v_mfma_f32_16x16x32_bf16 v[104:107], a[232:235], a[212:215], v[104:107]\n"
        "s_waitcnt lgkmcnt(6)\n"
        "v_exp_f32 v87, v87\n"
        "v_mfma_f32_16x16x32_bf16 v[96:99], a[72:75], a[236:239], 0\n"
        "s_waitcnt lgkmcnt(5)\n"
        "v_cvt_pk_bf16_f32 v83, v86, v87\n"
        "v_mfma_f32_16x16x32_bf16 v[96:99], a[76:79], a[240:243], v[96:99]\n"
        "s_waitcnt lgkmcnt(4)\n"
        "v_mfma_f32_16x16x32_bf16 v[96:99], a[80:83], a[244:247], v[96:99]\n"
        "s_waitcnt lgkmcnt(2)\n"
        "v_mfma_f32_16x16x32_bf16 v[96:99], a[84:87], a[248:251], v[96:99]\n"
        "s_waitcnt lgkmcnt(1)\n"
        "v_mfma_f32_16x16x32_bf16 v[96:99], a[88:91], a[216:219], v[96:99]\n"
        "s_waitcnt lgkmcnt(0)\n"
        "v_mfma_f32_16x16x32_bf16 v[96:99], a[92:95], a[220:223], v[96:99]\n"
        "v_cvt_pk_bf16_f32 v148, v104, v105\n"
        "v_cvt_pk_bf16_f32 v149, v106, v107\n"
        "v_mfma_f32_16x16x32_bf16 v[84:87], v[192:195], v[196:199], v[40:43]\n"
        "v_mfma_f32_16x16x32_bf16 v[84:87], v[208:211], v[200:203], v[84:87]\n"
        "v_mfma_f32_16x16x32_bf16 v[100:103], a[116:119], a[220:223], 0\n"
        "v_add_u32 v240, s45, v240\n"
        "v_add_u32 v241, s45, v241\n"
        "v_mfma_f32_16x16x32_bf16 v[100:103], a[112:115], a[216:219], v[100:103]\n"
        "v_cvt_pk_bf16_f32 v212, v96, v97\n"
        "v_cvt_pk_bf16_f32 v213, v98, v99\n"
        "v_mfma_f32_16x16x32_bf16 v[100:103], a[108:111], a[248:251], v[100:103]\n"
        "v_mfma_f32_16x16x32_bf16 v[100:103], a[104:107], a[244:247], v[100:103]\n"
        "v_exp_f32 v84, v84\n"
        "v_mfma_f32_16x16x32_bf16 v[100:103], a[100:103], a[240:243], v[100:103]\n"
        "v_exp_f32 v85, v85\n"
        "v_mfma_f32_16x16x32_bf16 v[100:103], a[96:99], a[236:239], v[100:103]\n"
        "v_exp_f32 v86, v86\n"
        "v_mfma_f32_16x16x32_bf16 v[104:107], a[120:123], a[236:239], 0\n"
        "v_exp_f32 v87, v87\n"
        "v_mfma_f32_16x16x32_bf16 v[104:107], a[124:127], a[240:243], v[104:107]\n"
        "v_cvt_pk_bf16_f32 v84, v84, v85\n"
        "v_cvt_pk_bf16_f32 v85, v86, v87\n"
        "v_mfma_f32_16x16x32_bf16 v[104:107], a[128:131], a[244:247], v[104:107]\n"
        "v_mfma_f32_16x16x32_bf16 v[104:107], a[132:135], a[248:251], v[104:107]\n"
        "v_cvt_pk_bf16_f32 v214, v100, v101\n"
        "v_cvt_pk_bf16_f32 v215, v102, v103\n"
        "v_mfma_f32_16x16x32_bf16 v[104:107], a[136:139], a[216:219], v[104:107]\n"
        "s_barrier\n"
        "v_mfma_f32_16x16x32_bf16 v[104:107], a[140:143], a[220:223], v[104:107]\n"
        "v_mfma_f32_16x16x32_bf16 v[96:99], a[20:23], a[220:223], 0\n"
        "v_mfma_f32_16x16x32_bf16 v[96:99], a[16:19], a[216:219], v[96:99]\n"
        "v_mfma_f32_16x16x32_bf16 v[96:99], a[12:15], a[248:251], v[96:99]\n"
        "s_cmp_eq_u32 s37, 100663296\n"
        "v_mfma_f32_16x16x32_bf16 v[96:99], a[8:11], a[244:247], v[96:99]\n"
        "s_cselect_b32 s46, 0, s37\n"
        "v_mfma_f32_16x16x32_bf16 v[96:99], a[4:7], a[240:243], v[96:99]\n"
        "s_add_u32 s38, s46, s43\n"
        "v_cvt_pk_bf16_f32 v230, v104, v105\n"
        "v_cvt_pk_bf16_f32 v231, v106, v107\n"
        "v_mfma_f32_16x16x32_bf16 v[96:99], a[0:3], a[236:239], v[96:99]\n"
        "s_add_u32 s39, s46, s44\n"
        "v_mfma_f32_16x16x32_bf16 v[108:111], v[212:215], v[156:159], v[12:15]\n"
        "s_add_u32 s37, s37, 6291456\n"
        "ds_read_b128 v[222:225], v251 offset:4608\n"
        "v_mfma_f32_16x16x32_bf16 v[108:111], v[228:231], v[164:167], v[108:111]\n"
        "v_mfma_f32_16x16x32_bf16 v[112:115], v[212:215], v[176:179], v[28:31]\n"
        "v_mfma_f32_16x16x32_bf16 v[112:115], v[228:231], v[184:187], v[112:115]\n"
        "v_mfma_f32_16x16x32_bf16 v[116:119], v[212:215], v[196:199], v[44:47]\n"
        "v_mul_f32 v96, s50, v96\n"
        "v_mul_f32 v97, s50, v97\n"
        "v_mfma_f32_16x16x32_bf16 v[116:119], v[228:231], v[204:207], v[116:119]\n"
        "v_mul_f32 v98, s50, v98\n"
        "v_mul_f32 v99, s50, v99\n"
        "v_mfma_f32_16x16x32_bf16 v[100:103], a[24:27], a[236:239], 0\n"
        "v_cvt_pk_bf16_f32 v216, v96, v97\n"
        "v_cvt_pk_bf16_f32 v217, v98, v99\n"
        "v_mfma_f32_16x16x32_bf16 v[100:103], a[28:31], a[240:243], v[100:103]\n"
        "v_exp_f32 v108, v108\n"
        "v_mfma_f32_16x16x32_bf16 v[100:103], a[32:35], a[244:247], v[100:103]\n"
        "v_exp_f32 v109, v109\n"
        "v_mfma_f32_16x16x32_bf16 v[100:103], a[36:39], a[248:251], v[100:103]\n"
        "v_exp_f32 v110, v110\n"
        "v_mfma_f32_16x16x32_bf16 v[100:103], a[40:43], a[216:219], v[100:103]\n"
        "v_exp_f32 v111, v111\n"
        "v_mfma_f32_16x16x32_bf16 v[100:103], a[44:47], a[220:223], v[100:103]\n"
        "v_exp_f32 v112, v112\n"
        "v_mfma_f32_16x16x32_bf16 v[104:107], a[68:71], a[220:223], 0\n"
        "v_exp_f32 v113, v113\n"
        "v_mfma_f32_16x16x32_bf16 v[104:107], a[64:67], a[216:219], v[104:107]\n"
        "v_exp_f32 v114, v114\n"
        "v_mfma_f32_16x16x32_bf16 v[104:107], a[60:63], a[248:251], v[104:107]\n"
        "v_exp_f32 v115, v115\n"
        "v_mfma_f32_16x16x32_bf16 v[104:107], a[56:59], a[244:247], v[104:107]\n"
        "v_mul_f32 v100, s50, v100\n"
        "v_mul_f32 v101, s50, v101\n"
        "v_mfma_f32_16x16x32_bf16 v[104:107], a[52:55], a[240:243], v[104:107]\n"
        "v_mul_f32 v102, s50, v102\n"
        "v_mul_f32 v103, s50, v103\n"
        "v_mfma_f32_16x16x32_bf16 v[104:107], a[48:51], a[236:239], v[104:107]\n"
        "v_cvt_pk_bf16_f32 v218, v100, v101\n"
        "v_cvt_pk_bf16_f32 v219, v102, v103\n"
        "v_mfma_f32_16x16x32_bf16 v[96:99], a[236:239], a[144:147], 0\n"
        "v_exp_f32 v116, v116\n"
        "v_mfma_f32_16x16x32_bf16 v[96:99], a[240:243], a[148:151], v[96:99]\n"
        "v_exp_f32 v117, v117\n"
        "v_mfma_f32_16x16x32_bf16 v[96:99], a[244:247], a[152:155], v[96:99]\n"
        "v_exp_f32 v118, v118\n"
        "v_mfma_f32_16x16x32_bf16 v[96:99], a[248:251], a[156:159], v[96:99]\n"
        "v_mul_f32 v104, s50, v104\n"
        "v_mul_f32 v105, s50, v105\n"
        "v_mfma_f32_16x16x32_bf16 v[96:99], a[216:219], a[160:163], v[96:99]\n"
        "v_mul_f32 v106, s50, v106\n"
        "v_mul_f32 v107, s50, v107\n"
        "v_mfma_f32_16x16x32_bf16 v[96:99], a[220:223], a[164:167], v[96:99]\n"
        "v_cvt_pk_bf16_f32 v220, v104, v105\n"
        "v_cvt_pk_bf16_f32 v221, v106, v107\n"
        "v_mfma_f32_16x16x32_bf16 v[88:91], v[152:155], v[216:219], v[48:51]\n"
        "s_waitcnt lgkmcnt(0)\n"
        "ds_read_b128 v[156:159], v247 offset:0\n"
        "v_mov_b64 v[226:227], v[220:221]\n"
        "v_mfma_f32_16x16x32_bf16 v[88:91], v[168:171], v[220:223], v[88:91]\n"
        "ds_read_b128 v[164:167], v247 offset:4096\n"
        "v_cvt_pk_bf16_f32 v70, v108, v109\n"
        "v_mfma_f32_16x16x32_bf16 v[92:95], v[172:175], v[216:219], v[52:55]\n"
        "s_add_u32 s47, s47, 6291456\n"
        "ds_read_b128 v[160:163], v247 offset:256\n"
        "v_cvt_pk_bf16_f32 v134, v96, v97\n"
        "v_mfma_f32_16x16x32_bf16 v[92:95], v[188:191], v[224:227], v[92:95]\n"
        "s_add_u32 s36, s36, 2097152\n"
        "ds_read_b128 v[222:225], v252 offset:4608\n"
        "v_cvt_pk_bf16_f32 v135, v98, v99\n"
        "v_mfma_f32_16x16x32_bf16 v[100:103], a[220:223], a[188:191], 0\n"
        "s_xor_b32 s48, s48, 8192\n"
        "ds_read_b128 v[152:155], v247 offset:4352\n"
        "v_cvt_pk_bf16_f32 v71, v110, v111\n"
        "v_mfma_f32_16x16x32_bf16 v[100:103], a[216:219], a[184:187], v[100:103]\n"
        "v_exp_f32 v119, v119\n"
        "v_mfma_f32_16x16x32_bf16 v[100:103], a[248:251], a[180:183], v[100:103]\n"
        "v_exp_f32 v88, v88\n"
        "v_mfma_f32_16x16x32_bf16 v[100:103], a[244:247], a[176:179], v[100:103]\n"
        "v_exp_f32 v89, v89\n"
        "v_mfma_f32_16x16x32_bf16 v[100:103], a[240:243], a[172:175], v[100:103]\n"
        "v_exp_f32 v90, v90\n"
        "v_mfma_f32_16x16x32_bf16 v[100:103], a[236:239], a[168:171], v[100:103]\n"
        "v_exp_f32 v91, v91\n"
        "v_mfma_f32_16x16x32_bf16 v[104:107], a[236:239], a[192:195], 0\n"
        "ds_read_b128 a[236:239], v248 offset:0\n"
        "v_cvt_pk_bf16_f32 v78, v112, v113\n"
        "v_mfma_f32_16x16x32_bf16 v[104:107], a[240:243], a[196:199], v[104:107]\n"
        "ds_read_b128 a[240:243], v248 offset:6144\n"
        "v_cvt_pk_bf16_f32 v79, v114, v115\n"
        "v_mfma_f32_16x16x32_bf16 v[104:107], a[244:247], a[200:203], v[104:107]\n"
        "v_cvt_pk_bf16_f32 v142, v100, v101\n"
        "v_cvt_pk_bf16_f32 v143, v102, v103\n"
        "v_mfma_f32_16x16x32_bf16 v[104:107], a[248:251], a[204:207], v[104:107]\n"
        "v_exp_f32 v92, v92\n"
        "v_mfma_f32_16x16x32_bf16 v[104:107], a[216:219], a[208:211], v[104:107]\n"
        "v_exp_f32 v93, v93\n"
        "v_mfma_f32_16x16x32_bf16 v[104:107], a[220:223], a[212:215], v[104:107]\n"
        "ds_read_b128 a[244:247], v254 offset:16\n"
        "v_cvt_pk_bf16_f32 v86, v116, v117\n"
        "v_mfma_f32_16x16x32_bf16 v[108:111], v[192:195], v[216:219], v[56:59]\n"
        "s_waitcnt lgkmcnt(4)\n"
        "ds_read_b128 a[248:251], v254 offset:6160\n"
        "v_mov_b64 v[116:117], 0\n"
        "v_mfma_f32_16x16x32_bf16 v[108:111], v[208:211], v[220:223], v[108:111]\n"
        "v_exp_f32 v94, v94\n"
        "v_mfma_f32_16x16x32_bf16 v[112:115], v[212:215], v[216:219], v[60:63]\n"
        "v_cvt_pk_bf16_f32 v150, v104, v105\n"
        "v_cvt_pk_bf16_f32 v151, v106, v107\n"
        "v_mfma_f32_16x16x32_bf16 v[112:115], v[228:231], v[224:227], v[112:115]\n"
        "s_waitcnt lgkmcnt(3)\n"
        "ds_read_b128 a[216:219], v248 offset:32\n"
        "v_cvt_pk_bf16_f32 v87, v118, v119\n"
        "v_mfma_f32_16x16x32_bf16 v[172:175], v[156:159], a[236:239], 0\n"
        "s_waitcnt lgkmcnt(3)\n"
        "ds_read_b128 a[220:223], v248 offset:6176\n"
        "v_cvt_pk_bf16_f32 v88, v88, v89\n"
        "v_mfma_f32_16x16x32_bf16 v[172:175], v[164:167], a[240:243], v[172:175]\n"
        "v_exp_f32 v95, v95\n"
        "v_mfma_f32_16x16x32_bf16 v[200:203], v[152:155], a[240:243], 0\n"
        "v_exp_f32 v108, v108\n"
        "v_mfma_f32_16x16x32_bf16 v[200:203], v[160:163], a[236:239], v[200:203]\n"
        "ds_read_b128 a[224:227], v254 offset:48\n"
        "v_cvt_pk_bf16_f32 v89, v90, v91\n"
        "v_smfmac_f32_16x16x64_bf16 v[116:119], a[252:255], v[64:71], v255\n"
        "ds_read_b128 a[228:231], v254 offset:6192\n"
        "v_cvt_pk_bf16_f32 v90, v92, v93\n"
        "v_mfma_f32_16x16x32_bf16 v[120:123], v[128:131], v[64:67], 0\n"
        "v_exp_f32 v109, v109\n"
        "v_mfma_f32_16x16x32_bf16 v[120:123], v[132:135], v[68:71], v[120:123]\n"
        "v_exp_f32 v110, v110\n"
        "v_mfma_f32_16x16x32_bf16 v[124:127], v[140:143], v[68:71], 0\n"
        "ds_read_b128 a[232:235], v248 offset:64\n"
        "v_add_f32 v116, v116, v117\n"
        "v_mfma_f32_16x16x32_bf16 v[124:127], v[136:139], v[64:67], v[124:127]\n"
        "ds_read_b128 a[236:239], v248 offset:6208\n"
        "v_cvt_pk_bf16_f32 v91, v94, v95\n"
        "v_mfma_f32_16x16x32_bf16 v[96:99], v[144:147], v[64:67], 0\n"
        "v_rcp_f32 v117, v116\n"
        "v_mfma_f32_16x16x32_bf16 v[96:99], v[148:151], v[68:71], v[96:99]\n"
        "s_waitcnt lgkmcnt(7)\n"
        "v_exp_f32 v111, v111\n"
        "v_mfma_f32_16x16x32_bf16 v[204:207], v[160:163], a[244:247], 0\n"
        "s_waitcnt lgkmcnt(6)\n"
        "v_exp_f32 v112, v112\n"
        "v_mfma_f32_16x16x32_bf16 v[204:207], v[152:155], a[248:251], v[204:207]\n"
        "v_exp_f32 v113, v113\n"
        "v_mfma_f32_16x16x32_bf16 v[176:179], v[164:167], a[248:251], 0\n"
        "v_exp_f32 v114, v114\n"
        "v_mfma_f32_16x16x32_bf16 v[176:179], v[156:159], a[244:247], v[176:179]\n"
        "s_waitcnt lgkmcnt(5)\n"
        "v_mul_f32 v120, v117, v120\n"
        "v_mul_f32 v121, v117, v121\n"
        "v_mfma_f32_16x16x32_bf16 v[180:183], v[156:159], a[216:219], 0\n"
        "s_waitcnt lgkmcnt(4)\n"
        "v_mov_b64 v[100:101], 0\n"
        "v_mul_f32 v122, v117, v122\n"
        "v_mfma_f32_16x16x32_bf16 v[180:183], v[164:167], a[220:223], v[180:183]\n"
        "ds_read_b128 v[232:235], v247 offset:8192\n"
        "v_mul_f32 v123, v117, v123\n"
        "v_mfma_f32_16x16x32_bf16 v[212:215], v[152:155], a[220:223], 0\n"
        "ds_read_b128 v[64:67], v247 offset:12288\n"
        "v_cvt_pk_bf16_f32 v92, v108, v109\n"
        "v_mfma_f32_16x16x32_bf16 v[212:215], v[160:163], a[216:219], v[212:215]\n"
        "s_waitcnt lgkmcnt(5)\n"
        "ds_read_b128 a[240:243], v254 offset:80\n"
        "v_cvt_pk_bf16_f32 v120, v120, v121\n"
        "v_mfma_f32_16x16x32_bf16 v[216:219], v[160:163], a[224:227], 0\n"
        "s_waitcnt lgkmcnt(5)\n"
        "ds_read_b128 a[244:247], v254 offset:6224\n"
        "v_cvt_pk_bf16_f32 v121, v122, v123\n"
        "v_mfma_f32_16x16x32_bf16 v[216:219], v[152:155], a[228:231], v[216:219]\n"
        "ds_write_b32 v245, v120 offset:0\n"
        "v_mfma_f32_16x16x32_bf16 v[184:187], v[164:167], a[228:231], 0\n"
        "v_cvt_pk_bf16_f32 v93, v110, v111\n"
        "v_mul_f32 v124, v117, v124\n"
        "v_mfma_f32_16x16x32_bf16 v[184:187], v[156:159], a[224:227], v[184:187]\n"
        "s_waitcnt lgkmcnt(6)\n"
        "ds_write_b32 v245, v121 offset:4\n"
        "v_mfma_f32_16x16x32_bf16 v[192:195], v[156:159], a[232:235], 0\n"
        "s_waitcnt lgkmcnt(6)\n"
        "v_mul_f32 v125, v117, v125\n"
        "v_maximum3_f32 v253, v253, v116, v117\n"
        "v_mfma_f32_16x16x32_bf16 v[192:195], v[164:167], a[236:239], v[192:195]\n"
        "v_mul_f32 v126, v117, v126\n"
        "v_mul_f32 v127, v117, v127\n"
        "v_mfma_f32_16x16x32_bf16 v[220:223], v[152:155], a[236:239], 0\n"
        "v_cvt_pk_bf16_f32 v94, v112, v113\n"
        "v_cvt_pk_bf16_f32 v124, v124, v125\n"
        "v_mfma_f32_16x16x32_bf16 v[220:223], v[160:163], a[232:235], v[220:223]\n"
        "v_exp_f32 v115, v115\n"
        "v_smfmac_f32_16x16x64_bf16 v[100:103], a[252:255], v[72:79], v255\n"
        "v_cvt_pk_bf16_f32 v125, v126, v127\n"
        "v_cvt_pk_bf16_f32 v95, v114, v115\n"
        "v_mfma_f32_16x16x32_bf16 v[104:107], v[128:131], v[72:75], 0\n"
        "ds_read_b128 v[236:239], v247 offset:8448\n"
        "ds_read_b128 a[248:251], v248 offset:36864\n"
        "v_mfma_f32_16x16x32_bf16 v[104:107], v[132:135], v[76:79], v[104:107]\n"
        "ds_read_b128 v[68:71], v247 offset:12544\n"
        "ds_read_b128 a[216:219], v248 offset:43008\n"
        "v_mfma_f32_16x16x32_bf16 v[108:111], v[140:143], v[76:79], 0\n"
        "ds_write_b32 v245, v124 offset:8\n"
        "v_mfma_f32_16x16x32_bf16 v[108:111], v[136:139], v[72:75], v[108:111]\n"
        "ds_write_b32 v245, v125 offset:12\n"
        "v_mfma_f32_16x16x32_bf16 v[112:115], v[144:147], v[72:75], 0\n"
        "ds_read_b128 a[220:223], v254 offset:36880\n"
        "v_mul_f32 v96, v117, v96\n"
        "v_mfma_f32_16x16x32_bf16 v[112:115], v[148:151], v[76:79], v[112:115]\n"
        "s_waitcnt lgkmcnt(10)\n"
        "ds_read_b128 a[224:227], v254 offset:43024\n"
        "v_mul_f32 v97, v117, v97\n"
        "v_mfma_f32_16x16x32_bf16 v[224:227], v[160:163], a[240:243], 0\n"
        "s_waitcnt lgkmcnt(10)\n"
        "v_mul_f32 v98, v117, v98\n"
        "v_mul_f32 v99, v117, v99\n"
        "v_mfma_f32_16x16x32_bf16 v[224:227], v[152:155], a[244:247], v[224:227]\n"
        "ds_read_b128 a[228:231], v248 offset:36896\n"
        "v_cvt_pk_bf16_f32 v96, v96, v97\n"
        "v_mfma_f32_16x16x32_bf16 v[196:199], v[164:167], a[244:247], 0\n"
        "ds_read_b128 a[232:235], v248 offset:43040\n"
        "v_cvt_pk_bf16_f32 v97, v98, v99\n"
        "v_mfma_f32_16x16x32_bf16 v[196:199], v[156:159], a[240:243], v[196:199]\n"
        "s_waitcnt lgkmcnt(8)\n"
        "ds_write_b32 v246, v96 offset:0\n"
        "v_mfma_f32_16x16x32_bf16 v[172:175], v[232:235], a[248:251], v[172:175]\n"
        "s_waitcnt lgkmcnt(7)\n"
        "ds_write_b32 v246, v97 offset:4\n"
        "v_mfma_f32_16x16x32_bf16 v[172:175], v[64:67], a[216:219], v[172:175]\n"
        "ds_read_b128 a[236:239], v254 offset:36912\n"
        "v_add_f32 v100, v100, v101\n"
        "v_mfma_f32_16x16x32_bf16 v[200:203], v[68:71], a[216:219], v[200:203]\n"
        "v_rcp_f32 v101, v100\n"
        "v_mfma_f32_16x16x32_bf16 v[200:203], v[236:239], a[248:251], v[200:203]\n"
        "s_waitcnt lgkmcnt(6)\n"
        "ds_read_b128 a[240:243], v254 offset:43056\n"
        "v_mul_f32 v104, v101, v104\n"
        "v_mfma_f32_16x16x32_bf16 v[204:207], v[236:239], a[220:223], v[204:207]\n"
        "s_waitcnt lgkmcnt(6)\n"
        "v_mul_f32 v105, v101, v105\n"
        "v_mul_f32 v106, v101, v106\n"
        "v_mfma_f32_16x16x32_bf16 v[204:207], v[68:71], a[224:227], v[204:207]\n"
        "v_mul_f32 v107, v101, v107\n"
        "v_cvt_pk_bf16_f32 v104, v104, v105\n"
        "v_mfma_f32_16x16x32_bf16 v[176:179], v[64:67], a[224:227], v[176:179]\n"
        "v_cvt_pk_bf16_f32 v105, v106, v107\n"
        "v_mov_b64 v[116:117], 0\n"
        "v_mfma_f32_16x16x32_bf16 v[176:179], v[232:235], a[220:223], v[176:179]\n"
        "s_waitcnt lgkmcnt(5)\n"
        "ds_write_b32 v245, v104 offset:256\n"
        "v_mfma_f32_16x16x32_bf16 v[180:183], v[232:235], a[228:231], v[180:183]\n"
        "s_waitcnt lgkmcnt(5)\n"
        "ds_write_b32 v245, v105 offset:260\n"
        "v_mfma_f32_16x16x32_bf16 v[180:183], v[64:67], a[232:235], v[180:183]\n"
        "v_mul_f32 v108, v101, v108\n"
        "v_mul_f32 v109, v101, v109\n"
        "v_mfma_f32_16x16x32_bf16 v[212:215], v[68:71], a[232:235], v[212:215]\n"
        "v_maximum3_f32 v253, v253, v100, v101\n"
        "v_mul_f32 v110, v101, v110\n"
        "v_mfma_f32_16x16x32_bf16 v[212:215], v[236:239], a[228:231], v[212:215]\n"
        "s_waitcnt lgkmcnt(3)\n"
        "v_mul_f32 v111, v101, v111\n"
        "v_cvt_pk_bf16_f32 v108, v108, v109\n"
        "v_mfma_f32_16x16x32_bf16 v[216:219], v[236:239], a[236:239], v[216:219]\n"
        "s_waitcnt lgkmcnt(2)\n"
        "v_cvt_pk_bf16_f32 v109, v110, v111\n"
        "v_mul_f32 v112, v101, v112\n"
        "v_mfma_f32_16x16x32_bf16 v[216:219], v[68:71], a[240:243], v[216:219]\n"
        "ds_read_b128 a[244:247], v248 offset:36928\n"
        "v_mul_f32 v113, v101, v113\n"
        "v_mfma_f32_16x16x32_bf16 v[184:187], v[64:67], a[240:243], v[184:187]\n"
        "ds_read_b128 a[248:251], v248 offset:43072\n"
        "v_mul_f32 v114, v101, v114\n"
        "v_mfma_f32_16x16x32_bf16 v[184:187], v[232:235], a[236:239], v[184:187]\n"
        "ds_write_b32 v245, v108 offset:264\n"
        "v_smfmac_f32_16x16x64_bf16 v[116:119], a[252:255], v[80:87], v255\n"
        "ds_write_b32 v245, v109 offset:268\n"
        "v_mfma_f32_16x16x32_bf16 v[120:123], v[128:131], v[80:83], 0\n"
        "ds_read_b128 a[216:219], v254 offset:36944\n"
        "v_mul_f32 v115, v101, v115\n"
        "v_mfma_f32_16x16x32_bf16 v[120:123], v[132:135], v[84:87], v[120:123]\n"
        "ds_read_b128 a[220:223], v254 offset:43088\n"
        "v_cvt_pk_bf16_f32 v112, v112, v113\n"
        "v_mfma_f32_16x16x32_bf16 v[124:127], v[140:143], v[84:87], 0\n"
        "ds_read_b128 v[156:159], v247 offset:16384\n"
        "v_cvt_pk_bf16_f32 v113, v114, v115\n"
        "v_mfma_f32_16x16x32_bf16 v[124:127], v[136:139], v[80:83], v[124:127]\n"
        "ds_read_b128 v[164:167], v247 offset:20480\n"
        "v_add_f32 v116, v116, v117\n"
        "v_mfma_f32_16x16x32_bf16 v[96:99], v[144:147], v[80:83], 0\n"
        "ds_read_b128 v[160:163], v247 offset:16640\n"
        "ds_read_b128 a[224:227], v248 offset:12288\n"
        "v_mfma_f32_16x16x32_bf16 v[96:99], v[148:151], v[84:87], v[96:99]\n"
        "s_waitcnt lgkmcnt(9)\n"
        "ds_read_b128 v[152:155], v247 offset:20736\n"
        "ds_read_b128 a[228:231], v248 offset:49152\n"
        "v_mfma_f32_16x16x32_bf16 v[192:195], v[232:235], a[244:247], v[192:195]\n"
        "s_waitcnt lgkmcnt(10)\n"
        "ds_write_b32 v246, v112 offset:256\n"
        "v_mfma_f32_16x16x32_bf16 v[192:195], v[64:67], a[248:251], v[192:195]\n"
        "ds_write_b32 v246, v113 offset:260\n"
        "v_mfma_f32_16x16x32_bf16 v[220:223], v[68:71], a[248:251], v[220:223]\n"
        "ds_read_b128 a[232:235], v254 offset:12304\n"
        "v_add_u32 v247, s31, v247\n"
        "v_mfma_f32_16x16x32_bf16 v[220:223], v[236:239], a[244:247], v[220:223]\n"
        "s_waitcnt lgkmcnt(10)\n"
        "ds_read_b128 a[236:239], v254 offset:49168\n"
        "v_mfma_f32_16x16x32_bf16 v[224:227], v[236:239], a[216:219], v[224:227]\n"
        "s_waitcnt lgkmcnt(10)\n"
        "v_rcp_f32 v117, v116\n"
        "v_mfma_f32_16x16x32_bf16 v[224:227], v[68:71], a[220:223], v[224:227]\n"
        "v_mul_f32 v120, v117, v120\n"
        "v_mul_f32 v121, v117, v121\n"
        "v_mfma_f32_16x16x32_bf16 v[196:199], v[64:67], a[220:223], v[196:199]\n"
        "ds_read_b128 a[240:243], v248 offset:12320\n"
        "v_mul_f32 v122, v117, v122\n"
        "v_mfma_f32_16x16x32_bf16 v[196:199], v[232:235], a[216:219], v[196:199]\n"
        "s_waitcnt lgkmcnt(7)\n"
        "ds_read_b128 a[244:247], v248 offset:49184\n"
        "v_mul_f32 v123, v117, v123\n"
        "v_mfma_f32_16x16x32_bf16 v[172:175], v[156:159], a[224:227], v[172:175]\n"
        "s_waitcnt lgkmcnt(6)\n"
        "ds_read_b128 a[248:251], v254 offset:12336\n"
        "v_cvt_pk_bf16_f32 v120, v120, v121\n"
        "v_mfma_f32_16x16x32_bf16 v[172:175], v[164:167], a[228:231], v[172:175]\n"
        "ds_read_b128 a[216:219], v254 offset:49200\n"
        "v_cvt_pk_bf16_f32 v121, v122, v123\n"
        "v_mfma_f32_16x16x32_bf16 v[200:203], v[152:155], a[228:231], v[200:203]\n"
        "ds_write_b32 v245, v120 offset:512\n"
        "v_mfma_f32_16x16x32_bf16 v[200:203], v[160:163], a[224:227], v[200:203]\n"
        "s_waitcnt lgkmcnt(6)\n"
        "ds_write_b32 v245, v121 offset:516\n"
        "v_mfma_f32_16x16x32_bf16 v[204:207], v[160:163], a[232:235], v[204:207]\n"
        "s_waitcnt lgkmcnt(6)\n"
        "v_mul_f32 v124, v117, v124\n"
        "v_mul_f32 v125, v117, v125\n"
        "v_mfma_f32_16x16x32_bf16 v[204:207], v[152:155], a[236:239], v[204:207]\n"
        "v_maximum3_f32 v253, v253, v116, v117\n"
        "v_mul_f32 v126, v117, v126\n"
        "v_mfma_f32_16x16x32_bf16 v[176:179], v[164:167], a[236:239], v[176:179]\n"
        "ds_read_b128 a[220:223], v248 offset:12352\n"
        "v_mul_f32 v127, v117, v127\n"
        "v_mfma_f32_16x16x32_bf16 v[176:179], v[156:159], a[232:235], v[176:179]\n"
        "s_waitcnt lgkmcnt(6)\n"
        "ds_read_b128 a[224:227], v248 offset:49216\n"
        "v_cvt_pk_bf16_f32 v124, v124, v125\n"
        "v_mfma_f32_16x16x32_bf16 v[180:183], v[156:159], a[240:243], v[180:183]\n"
        "s_waitcnt lgkmcnt(6)\n"
        "v_cvt_pk_bf16_f32 v125, v126, v127\n"
        "v_mul_f32 v96, v117, v96\n"
        "v_mfma_f32_16x16x32_bf16 v[180:183], v[164:167], a[244:247], v[180:183]\n"
        "ds_write_b32 v245, v124 offset:520\n"
        "v_mfma_f32_16x16x32_bf16 v[212:215], v[152:155], a[244:247], v[212:215]\n"
        "ds_read_b128 a[228:231], v254 offset:12368\n"
        "v_mul_f32 v97, v117, v97\n"
        "v_mfma_f32_16x16x32_bf16 v[212:215], v[160:163], a[240:243], v[212:215]\n"
        "s_waitcnt lgkmcnt(7)\n"
        "ds_read_b128 a[232:235], v254 offset:49232\n"
        "v_mul_f32 v98, v117, v98\n"
        "v_mfma_f32_16x16x32_bf16 v[216:219], v[160:163], a[248:251], v[216:219]\n"
        "s_waitcnt lgkmcnt(7)\n"
        "ds_write_b32 v245, v125 offset:524\n"
        "v_mfma_f32_16x16x32_bf16 v[216:219], v[152:155], a[216:219], v[216:219]\n"
        "v_mul_f32 v99, v117, v99\n"
        "v_cvt_pk_bf16_f32 v96, v96, v97\n"
        "v_mfma_f32_16x16x32_bf16 v[184:187], v[164:167], a[216:219], v[184:187]\n"
        "v_cvt_pk_bf16_f32 v97, v98, v99\n"
        "v_mov_b64 v[80:81], 0\n"
        "v_mfma_f32_16x16x32_bf16 v[184:187], v[156:159], a[248:251], v[184:187]\n"
        "s_waitcnt lgkmcnt(5)\n"
        "s_waitcnt vmcnt(0)\n"
        "s_barrier\n"
        "ds_write_b32 v246, v96 offset:512\n"
        "v_mfma_f32_16x16x32_bf16 v[192:195], v[156:159], a[220:223], v[192:195]\n"
        "s_waitcnt lgkmcnt(5)\n"
        "ds_write_b32 v246, v97 offset:516\n"
        "v_mfma_f32_16x16x32_bf16 v[192:195], v[164:167], a[224:227], v[192:195]\n"
        "ds_read_b128 a[236:239], v240 offset:0\n"
        "v_mfma_f32_16x16x32_bf16 v[220:223], v[152:155], a[224:227], v[220:223]\n"
        "ds_read_b128 a[240:243], v241 offset:0\n"
        "v_mfma_f32_16x16x32_bf16 v[220:223], v[160:163], a[220:223], v[220:223]\n"
        "s_waitcnt lgkmcnt(6)\n"
        "ds_read_b128 a[244:247], v240 offset:128\n"
        "v_mfma_f32_16x16x32_bf16 v[224:227], v[160:163], a[228:231], v[224:227]\n"
        "s_waitcnt lgkmcnt(6)\n"
        "ds_read_b128 a[248:251], v241 offset:128\n"
        "v_mfma_f32_16x16x32_bf16 v[224:227], v[152:155], a[232:235], v[224:227]\n"
        "ds_read_b128 a[216:219], v240 offset:256\n"
        "v_mfma_f32_16x16x32_bf16 v[196:199], v[164:167], a[232:235], v[196:199]\n"
        "ds_read_b128 a[220:223], v241 offset:256\n"
        "v_mfma_f32_16x16x32_bf16 v[196:199], v[156:159], a[228:231], v[196:199]\n"
        "v_smfmac_f32_16x16x64_bf16 v[80:83], a[252:255], v[88:95], v255\n"
        "v_mfma_f32_16x16x32_bf16 v[84:87], v[128:131], v[88:91], 0\n"
        "v_mfma_f32_16x16x32_bf16 v[84:87], v[132:135], v[92:95], v[84:87]\n"
        "v_mfma_f32_16x16x32_bf16 v[232:235], v[140:143], v[92:95], 0\n"
        "v_mfma_f32_16x16x32_bf16 v[232:235], v[136:139], v[88:91], v[232:235]\n"
        "s_sub_u32 s27, s27, 1\n"
        "v_mfma_f32_16x16x32_bf16 v[236:239], v[144:147], v[88:91], 0\n"
        "s_cmp_lg_u32 s27, 0\n"
        "v_mfma_f32_16x16x32_bf16 v[236:239], v[148:151], v[92:95], v[236:239]\n"
        "s_cbranch_scc1 10b\n"
        "20:\n"
        "s_waitcnt lgkmcnt(0)\n"
        "ds_read_b128 a[0:3], v248 offset:0\n"
        "ds_read_b128 a[4:7], v248 offset:6144\n"
        "ds_read_b128 a[8:11], v248 offset:36864\n"
        "s_and_b32 s46, s42, 1\n"
        "s_lshl_b32 s31, s46, 8\n"
        "s_mul_i32 s45, s46, 786432\n"
        "s_sub_u32 s37, s37, s45\n"
        "s_cmp_eq_u32 s46, 0\n"
        "s_cselect_b32 s43, s43, s44\n"
        "s_add_u32 s28, s37, 786432\n"
        "s_cmp_eq_u32 s28, 100663296\n"
        "s_cselect_b32 s28, 0, s28\n"
        "s_lshl_b32 s29, s42, 8\n"
        "s_add_u32 s29, s29, 147456\n"
        "s_mov_b32 s30, 147456\n"
        "v_subrev_u32 v247, s31, v247\n"
        "ds_read_b128 v[128:131], v247 offset:0\n"
        "ds_read_b128 v[132:135], v247 offset:4096\n"
        "ds_read_b128 v[136:139], v247 offset:8192\n"
        "ds_read_b128 v[140:143], v247 offset:12288\n"
        "ds_read_b128 a[12:15], v248 offset:43008\n"
        "ds_read_b128 v[144:147], v247 offset:16384\n"
        "ds_read_b128 a[16:19], v248 offset:12288\n"
        "ds_read_b128 v[148:151], v247 offset:20480\n"
        "ds_read_b128 a[20:23], v248 offset:49152\n"
        "ds_read_b128 a[44:47], v254 offset:49168\n"
        "ds_read_b128 a[40:43], v254 offset:12304\n"
        "ds_read_b128 a[36:39], v254 offset:43024\n"
        "v_add_f32 v80, v80, v81\n"
        "v_rcp_f32 v81, v80\n"
        "v_mbcnt_lo_u32_b32 v240, -1, 0\n"
        "v_mul_f32 v84, v81, v84\n"
        "v_mul_f32 v85, v81, v85\n"
        "v_mul_f32 v86, v81, v86\n"
        "v_mul_f32 v87, v81, v87\n"
        "v_cvt_pk_bf16_f32 v84, v84, v85\n"
        "v_cvt_pk_bf16_f32 v85, v86, v87\n"
        "v_mul_f32 v232, v81, v232\n"
        "v_mul_f32 v233, v81, v233\n"
        "v_maximum3_f32 v253, v253, v80, v81\n"
        "v_mul_f32 v234, v81, v234\n"
        "v_mul_f32 v235, v81, v235\n"
        "v_cvt_pk_bf16_f32 v86, v232, v233\n"
        "v_cvt_pk_bf16_f32 v87, v234, v235\n"
        "ds_write_b128 v245, v[84:87] offset:768\n"
        "v_mul_f32 v236, v81, v236\n"
        "v_mul_f32 v237, v81, v237\n"
        "v_mul_f32 v238, v81, v238\n"
        "v_mul_f32 v239, v81, v239\n"
        "v_cvt_pk_bf16_f32 v236, v236, v237\n"
        "v_cvt_pk_bf16_f32 v237, v238, v239\n"
        "ds_write_b64 v246, v[236:237] offset:768\n"
        "v_mbcnt_hi_u32_b32 v240, -1, v240\n"
        "v_lshl_add_u32 v241, v240, 4, s30\n"
        "v_lshl_add_u32 v240, v240, 2, s29\n"
        "v_and_b32 v117, 60, v250\n"
        "ds_write_b32 v240, v253\n"
        "s_waitcnt lgkmcnt(14)\n"
        "v_mfma_f32_16x16x32_bf16 v[0:3], v[128:131], a[0:3], 0\n"
        "45:\n"
        "s_cbranch_execz 40b\n"
        "s_waitcnt lgkmcnt(13)\n"
        "ds_read_b128 a[32:35], v254 offset:36880\n"
        "v_add3_u32 v117, v250, v117, s26\n"
        "v_mfma_f32_16x16x32_bf16 v[0:3], v[132:135], a[4:7], v[0:3]\n"
        "s_waitcnt lgkmcnt(13)\n"
        "ds_read_b128 a[28:31], v254 offset:6160\n"
        "v_cvt_pk_bf16_f32 v108, v172, v176\n"
        "v_mfma_f32_16x16x32_bf16 v[0:3], v[136:139], a[8:11], v[0:3]\n"
        "s_waitcnt lgkmcnt(12)\n"
        "ds_read_b128 a[24:27], v254 offset:16\n"
        "v_cvt_pk_bf16_f32 v109, v180, v184\n"
        "v_mfma_f32_16x16x32_bf16 v[0:3], v[140:143], a[12:15], v[0:3]\n"
        "s_waitcnt lgkmcnt(11)\n"
        "ds_read_b128 a[48:51], v248 offset:32\n"
        "v_cvt_pk_bf16_f32 v110, v192, v196\n"
        "v_mfma_f32_16x16x32_bf16 v[0:3], v[144:147], a[16:19], v[0:3]\n"
        "s_waitcnt lgkmcnt(10)\n"
        "ds_read_b128 a[52:55], v248 offset:6176\n"
        "v_cvt_pk_bf16_f32 v112, v173, v177\n"
        "v_mfma_f32_16x16x32_bf16 v[0:3], v[148:151], a[20:23], v[0:3]\n"
        "s_waitcnt lgkmcnt(10)\n"
        "ds_read_b128 a[56:59], v248 offset:36896\n"
        "v_cvt_pk_bf16_f32 v113, v181, v185\n"
        "v_mfma_f32_16x16x32_bf16 v[4:7], v[148:151], a[44:47], 0\n"
        "s_waitcnt lgkmcnt(10)\n"
        "ds_read_b128 a[60:63], v248 offset:43040\n"
        "v_cvt_pk_bf16_f32 v114, v193, v197\n"
        "v_mfma_f32_16x16x32_bf16 v[4:7], v[144:147], a[40:43], v[4:7]\n"
        "s_waitcnt lgkmcnt(10)\n"
        "ds_read_b128 a[64:67], v248 offset:12320\n"
        "v_mfma_f32_16x16x32_bf16 v[4:7], v[140:143], a[36:39], v[4:7]\n"
        "s_waitcnt lgkmcnt(7)\n"
        "s_waitcnt lgkmcnt(8)\n"
        "s_barrier\n"
        "ds_read_b128 a[68:71], v248 offset:49184\n"
        "v_mfma_f32_16x16x32_bf16 v[4:7], v[136:139], a[32:35], v[4:7]\n"
        "s_waitcnt lgkmcnt(7)\n"
        "ds_read_b128 a[92:95], v254 offset:49200\n"
        "v_mfma_f32_16x16x32_bf16 v[4:7], v[132:135], a[28:31], v[4:7]\n"
        "s_waitcnt lgkmcnt(7)\n"
        "ds_read_b128 a[88:91], v254 offset:12336\n"
        "v_mfma_f32_16x16x32_bf16 v[4:7], v[128:131], a[24:27], v[4:7]\n"
        "s_waitcnt lgkmcnt(7)\n"
        "ds_read_b128 a[84:87], v254 offset:43056\n"
        "v_mfma_f32_16x16x32_bf16 v[8:11], v[128:131], a[48:51], 0\n"
        "s_waitcnt lgkmcnt(7)\n"
        "ds_read_b128 a[80:83], v254 offset:36912\n"
        "v_mfma_f32_16x16x32_bf16 v[8:11], v[132:135], a[52:55], v[8:11]\n"
        "s_waitcnt lgkmcnt(7)\n"
        "ds_read_b128 a[76:79], v254 offset:6192\n"
        "v_mfma_f32_16x16x32_bf16 v[8:11], v[136:139], a[56:59], v[8:11]\n"
        "s_waitcnt lgkmcnt(7)\n"
        "ds_read_b128 a[72:75], v254 offset:48\n"
        "v_mfma_f32_16x16x32_bf16 v[8:11], v[140:143], a[60:63], v[8:11]\n"
        "s_waitcnt lgkmcnt(7)\n"
        "ds_read_b128 a[96:99], v248 offset:64\n"
        "v_cvt_pk_bf16_f32 v192, v0, v4\n"
        "v_mfma_f32_16x16x32_bf16 v[8:11], v[144:147], a[64:67], v[8:11]\n"
        "s_waitcnt lgkmcnt(7)\n"
        "ds_read_b128 a[100:103], v248 offset:6208\n"
        "v_cvt_pk_bf16_f32 v196, v1, v5\n"
        "v_mfma_f32_16x16x32_bf16 v[8:11], v[148:151], a[68:71], v[8:11]\n"
        "s_waitcnt lgkmcnt(7)\n"
        "ds_read_b128 a[104:107], v248 offset:36928\n"
        "v_cvt_pk_bf16_f32 v180, v2, v6\n"
        "v_mfma_f32_16x16x32_bf16 v[12:15], v[148:151], a[92:95], 0\n"
        "s_waitcnt lgkmcnt(7)\n"
        "ds_read_b128 a[108:111], v248 offset:43072\n"
        "v_cvt_pk_bf16_f32 v184, v3, v7\n"
        "v_mfma_f32_16x16x32_bf16 v[12:15], v[144:147], a[88:91], v[12:15]\n"
        "s_waitcnt lgkmcnt(7)\n"
        "ds_read_b128 a[112:115], v248 offset:12352\n"
        "v_mfma_f32_16x16x32_bf16 v[12:15], v[140:143], a[84:87], v[12:15]\n"
        "s_waitcnt lgkmcnt(7)\n"
        "ds_read_b128 a[116:119], v248 offset:49216\n"
        "v_mfma_f32_16x16x32_bf16 v[12:15], v[136:139], a[80:83], v[12:15]\n"
        "s_waitcnt lgkmcnt(7)\n"
        "ds_read_b128 a[140:143], v254 offset:49232\n"
        "v_mfma_f32_16x16x32_bf16 v[12:15], v[132:135], a[76:79], v[12:15]\n"
        "s_waitcnt lgkmcnt(7)\n"
        "ds_read_b128 a[136:139], v254 offset:12368\n"
        "v_mfma_f32_16x16x32_bf16 v[12:15], v[128:131], a[72:75], v[12:15]\n"
        "s_waitcnt lgkmcnt(7)\n"
        "ds_read_b128 a[132:135], v254 offset:43088\n"
        "v_mfma_f32_16x16x32_bf16 v[16:19], v[128:131], a[96:99], 0\n"
        "s_waitcnt lgkmcnt(7)\n"
        "ds_read_b128 a[128:131], v254 offset:36944\n"
        "v_mfma_f32_16x16x32_bf16 v[16:19], v[132:135], a[100:103], v[16:19]\n"
        "s_waitcnt lgkmcnt(7)\n"
        "ds_read_b128 a[124:127], v254 offset:6224\n"
        "v_mfma_f32_16x16x32_bf16 v[16:19], v[136:139], a[104:107], v[16:19]\n"
        "s_waitcnt lgkmcnt(7)\n"
        "ds_read_b128 a[120:123], v254 offset:80\n"
        "ds_read_b128 v[64:67], v247 offset:512\n"
        "v_mfma_f32_16x16x32_bf16 v[16:19], v[140:143], a[108:111], v[16:19]\n"
        "s_waitcnt lgkmcnt(8)\n"
        "ds_read_b128 v[68:71], v247 offset:4608\n"
        "v_cvt_pk_bf16_f32 v193, v8, v12\n"
        "v_mfma_f32_16x16x32_bf16 v[16:19], v[144:147], a[112:115], v[16:19]\n"
        "s_waitcnt lgkmcnt(8)\n"
        "ds_read_b128 v[72:75], v247 offset:8704\n"
        "v_cvt_pk_bf16_f32 v197, v9, v13\n"
        "v_mfma_f32_16x16x32_bf16 v[16:19], v[148:151], a[116:119], v[16:19]\n"
        "s_waitcnt lgkmcnt(8)\n"
        "ds_read_b128 v[76:79], v247 offset:12800\n"
        "v_cvt_pk_bf16_f32 v181, v10, v14\n"
        "v_mfma_f32_16x16x32_bf16 v[20:23], v[148:151], a[140:143], 0\n"
        "s_waitcnt lgkmcnt(8)\n"
        "ds_read_b128 v[80:83], v247 offset:16896\n"
        "v_cvt_pk_bf16_f32 v185, v11, v15\n"
        "v_mfma_f32_16x16x32_bf16 v[20:23], v[144:147], a[136:139], v[20:23]\n"
        "s_waitcnt lgkmcnt(8)\n"
        "ds_read_b128 v[84:87], v247 offset:20992\n"
        "v_mfma_f32_16x16x32_bf16 v[20:23], v[140:143], a[132:135], v[20:23]\n"
        "s_waitcnt lgkmcnt(8)\n"
        "buffer_store_dwordx2 v[108:109], v117, s[16:19], s38 offen offset:0 sc1\n"
        "v_mfma_f32_16x16x32_bf16 v[20:23], v[136:139], a[128:131], v[20:23]\n"
        "s_waitcnt lgkmcnt(7)\n"
        "buffer_store_dword v110, v250, s[16:19], s38 offen offset:0 sc1\n"
        "v_mfma_f32_16x16x32_bf16 v[20:23], v[132:135], a[124:127], v[20:23]\n"
        "s_waitcnt lgkmcnt(6)\n"
        "buffer_store_dwordx2 v[112:113], v117, s[16:19], s38 offen offset:384 sc1\n"
        "v_mfma_f32_16x16x32_bf16 v[20:23], v[128:131], a[120:123], v[20:23]\n"
        "s_waitcnt lgkmcnt(5)\n"
        "buffer_store_dword v114, v250, s[16:19], s38 offen offset:384 sc1\n"
        "v_mfma_f32_16x16x32_bf16 v[24:27], v[64:67], a[0:3], 0\n"
        "s_waitcnt lgkmcnt(4)\n"
        "v_cvt_pk_bf16_f32 v108, v174, v178\n"
        "v_cvt_pk_bf16_f32 v109, v182, v186\n"
        "v_mfma_f32_16x16x32_bf16 v[24:27], v[68:71], a[4:7], v[24:27]\n"
        "s_waitcnt lgkmcnt(3)\n"
        "v_cvt_pk_bf16_f32 v110, v194, v198\n"
        "v_cvt_pk_bf16_f32 v112, v175, v179\n"
        "v_mfma_f32_16x16x32_bf16 v[24:27], v[72:75], a[8:11], v[24:27]\n"
        "s_waitcnt lgkmcnt(2)\n"
        "buffer_store_dwordx2 v[108:109], v117, s[16:19], s38 offen offset:768 sc1\n"
        "v_mfma_f32_16x16x32_bf16 v[24:27], v[76:79], a[12:15], v[24:27]\n"
        "s_waitcnt lgkmcnt(1)\n"
        "buffer_store_dword v110, v250, s[16:19], s38 offen offset:768 sc1\n"
        "v_mfma_f32_16x16x32_bf16 v[24:27], v[80:83], a[16:19], v[24:27]\n"
        "s_waitcnt lgkmcnt(0)\n"
        "v_cvt_pk_bf16_f32 v113, v183, v187\n"
        "v_cvt_pk_bf16_f32 v114, v195, v199\n"
        "v_mfma_f32_16x16x32_bf16 v[24:27], v[84:87], a[20:23], v[24:27]\n"
        "buffer_store_dwordx2 v[112:113], v117, s[16:19], s38 offen offset:1152 sc1\n"
        "v_mfma_f32_16x16x32_bf16 v[28:31], v[84:87], a[44:47], 0\n"
        "buffer_store_dword v114, v250, s[16:19], s38 offen offset:1152 sc1\n"
        "v_mfma_f32_16x16x32_bf16 v[28:31], v[80:83], a[40:43], v[28:31]\n"
        "v_cvt_pk_bf16_f32 v108, v200, v204\n"
        "v_cvt_pk_bf16_f32 v109, v212, v216\n"
        "v_mfma_f32_16x16x32_bf16 v[28:31], v[76:79], a[36:39], v[28:31]\n"
        "s_add_u32 s38, s37, s43\n"
        "ds_read_b128 v[96:99], v241\n"
        "v_cvt_pk_bf16_f32 v110, v220, v224\n"
        "v_mfma_f32_16x16x32_bf16 v[28:31], v[72:75], a[32:35], v[28:31]\n"
        "buffer_store_dwordx2 v[108:109], v117, s[16:19], s39 offen offset:0 sc1\n"
        "v_mfma_f32_16x16x32_bf16 v[28:31], v[68:71], a[28:31], v[28:31]\n"
        "buffer_store_dword v110, v250, s[16:19], s39 offen offset:0 sc1\n"
        "v_mfma_f32_16x16x32_bf16 v[28:31], v[64:67], a[24:27], v[28:31]\n"
        "v_cvt_pk_bf16_f32 v112, v201, v205\n"
        "v_cvt_pk_bf16_f32 v113, v213, v217\n"
        "v_mfma_f32_16x16x32_bf16 v[32:35], v[64:67], a[48:51], 0\n"
        "v_cvt_pk_bf16_f32 v114, v221, v225\n"
        "v_cvt_pk_bf16_f32 v108, v202, v206\n"
        "v_mfma_f32_16x16x32_bf16 v[32:35], v[68:71], a[52:55], v[32:35]\n"
        "buffer_store_dwordx2 v[112:113], v117, s[16:19], s39 offen offset:384 sc1\n"
        "v_mfma_f32_16x16x32_bf16 v[32:35], v[72:75], a[56:59], v[32:35]\n"
        "buffer_store_dword v114, v250, s[16:19], s39 offen offset:384 sc1\n"
        "v_mfma_f32_16x16x32_bf16 v[32:35], v[76:79], a[60:63], v[32:35]\n"
        "v_cvt_pk_bf16_f32 v109, v214, v218\n"
        "v_cvt_pk_bf16_f32 v110, v222, v226\n"
        "v_mfma_f32_16x16x32_bf16 v[32:35], v[80:83], a[64:67], v[32:35]\n"
        "buffer_store_dwordx2 v[108:109], v117, s[16:19], s39 offen offset:768 sc1\n"
        "v_mfma_f32_16x16x32_bf16 v[32:35], v[84:87], a[68:71], v[32:35]\n"
        "buffer_store_dword v110, v250, s[16:19], s39 offen offset:768 sc1\n"
        "v_mfma_f32_16x16x32_bf16 v[36:39], v[84:87], a[92:95], 0\n"
        "v_cvt_pk_bf16_f32 v112, v203, v207\n"
        "v_cvt_pk_bf16_f32 v113, v215, v219\n"
        "v_mfma_f32_16x16x32_bf16 v[36:39], v[80:83], a[88:91], v[36:39]\n"
        "v_cvt_pk_bf16_f32 v114, v223, v227\n"
        "v_cvt_pk_bf16_f32 v194, v16, v20\n"
        "v_mfma_f32_16x16x32_bf16 v[36:39], v[76:79], a[84:87], v[36:39]\n"
        "buffer_store_dwordx2 v[112:113], v117, s[16:19], s39 offen offset:1152 sc1\n"
        "v_mfma_f32_16x16x32_bf16 v[36:39], v[72:75], a[80:83], v[36:39]\n"
        "buffer_store_dword v114, v250, s[16:19], s39 offen offset:1152 sc1\n"
        "v_mfma_f32_16x16x32_bf16 v[36:39], v[68:71], a[76:79], v[36:39]\n"
        "buffer_store_dwordx2 v[192:193], v117, s[16:19], s38 offen offset:0 sc1\n"
        "v_mfma_f32_16x16x32_bf16 v[36:39], v[64:67], a[72:75], v[36:39]\n"
        "s_add_u32 s39, s28, s43\n"
        "buffer_store_dword v194, v250, s[16:19], s38 offen offset:0 sc1\n"
        "v_mfma_f32_16x16x32_bf16 v[40:43], v[64:67], a[96:99], 0\n"
        "v_cvt_pk_bf16_f32 v198, v17, v21\n"
        "v_cvt_pk_bf16_f32 v182, v18, v22\n"
        "v_mfma_f32_16x16x32_bf16 v[40:43], v[68:71], a[100:103], v[40:43]\n"
        "buffer_store_dwordx2 v[196:197], v117, s[16:19], s38 offen offset:384 sc1\n"
        "v_mfma_f32_16x16x32_bf16 v[40:43], v[72:75], a[104:107], v[40:43]\n"
        "buffer_store_dword v198, v250, s[16:19], s38 offen offset:384 sc1\n"
        "v_mfma_f32_16x16x32_bf16 v[40:43], v[76:79], a[108:111], v[40:43]\n"
        "buffer_store_dwordx2 v[180:181], v117, s[16:19], s38 offen offset:768 sc1\n"
        "v_mfma_f32_16x16x32_bf16 v[40:43], v[80:83], a[112:115], v[40:43]\n"
        "buffer_store_dword v182, v250, s[16:19], s38 offen offset:768 sc1\n"
        "v_mfma_f32_16x16x32_bf16 v[40:43], v[84:87], a[116:119], v[40:43]\n"
        "v_cvt_pk_bf16_f32 v186, v19, v23\n"
        "s_waitcnt lgkmcnt(0)\n"
        "v_maximum3_f32 v96, v96, v97, v98\n"
        "v_mfma_f32_16x16x32_bf16 v[44:47], v[84:87], a[140:143], 0\n"
        "buffer_store_dwordx2 v[184:185], v117, s[16:19], s38 offen offset:1152 sc1\n"
        "v_mfma_f32_16x16x32_bf16 v[44:47], v[80:83], a[136:139], v[44:47]\n"
        "buffer_store_dword v186, v250, s[16:19], s38 offen offset:1152 sc1\n"
        "v_mfma_f32_16x16x32_bf16 v[44:47], v[76:79], a[132:135], v[44:47]\n"
        "v_maximum3_f32 v96, v96, v99, v99\n"
        "v_cmp_nge_f32 vcc, s25, v96\n"
        "v_mfma_f32_16x16x32_bf16 v[44:47], v[72:75], a[128:131], v[44:47]\n"
        "v_cvt_pk_bf16_f32 v172, v24, v28\n"
        "v_cvt_pk_bf16_f32 v173, v32, v36\n"
        "v_mfma_f32_16x16x32_bf16 v[44:47], v[68:71], a[124:127], v[44:47]\n"
        "v_cvt_pk_bf16_f32 v176, v25, v29\n"
        "v_cvt_pk_bf16_f32 v177, v33, v37\n"
        "v_mfma_f32_16x16x32_bf16 v[44:47], v[64:67], a[120:123], v[44:47]\n"
        "buffer_store_dwordx2 v[172:173], v117, s[16:19], s39 offen offset:0 sc1\n"
        "v_cvt_pk_bf16_f32 v152, v26, v30\n"
        "v_cvt_pk_bf16_f32 v153, v34, v38\n"
        "buffer_store_dwordx2 v[176:177], v117, s[16:19], s39 offen offset:384 sc1\n"
        "v_cvt_pk_bf16_f32 v156, v27, v31\n"
        "v_cvt_pk_bf16_f32 v157, v35, v39\n"
        "buffer_store_dwordx2 v[152:153], v117, s[16:19], s39 offen offset:768 sc1\n"
        "buffer_store_dwordx2 v[156:157], v117, s[16:19], s39 offen offset:1152 sc1\n"
        "v_cvt_pk_bf16_f32 v174, v40, v44\n"
        "buffer_store_dword v174, v250, s[16:19], s39 offen offset:0 sc1\n"
        "v_cvt_pk_bf16_f32 v178, v41, v45\n"
        "buffer_store_dword v178, v250, s[16:19], s39 offen offset:384 sc1\n"
        "v_cvt_pk_bf16_f32 v154, v42, v46\n"
        "buffer_store_dword v154, v250, s[16:19], s39 offen offset:768 sc1\n"
        "v_cvt_pk_bf16_f32 v158, v43, v47\n"
        "buffer_store_dword v158, v250, s[16:19], s39 offen offset:1152 sc1\n"
        "s_cmp_lg_u64 vcc, 0\n"
        "s_cbranch_scc1 30f\n"
        "s_endpgm\n"
        "30:\n"
        : "+{v0}"(tid), "=&{s42}"(w)
        : "{s[0:1]}"(__builtin_amdgcn_kernarg_segment_ptr()), "{s2}"(blockIdx.x), "s"(ldsb)
        : "v1", "v2", "v3", "v4", "v5", "v6", "v7", "v8", "v9", "v10", "v11", "v12", "v13", "v14", "v15", "v16", "v17", "v18", "v19", "v20", "v21", "v22", "v23", "v24", "v25", "v26", "v27", "v28", "v29", "v30", "v31", "v32", "v33", "v34", "v35", "v36", "v37", "v38", "v39", "v40", "v41", "v42", "v43", "v44", "v45", "v46", "v47", "v48", "v49", "v50", "v51", "v52", "v53", "v54", "v55", "v56", "v57", "v58", "v59", "v60", "v61", "v62", "v63", "v64", "v65", "v66", "v67", "v68", "v69", "v70", "v71", "v72", "v73", "v74", "v75", "v76", "v77", "v78", "v79", "v80", "v81", "v82", "v83", "v84", "v85", "v86", "v87", "v88", "v89", "v90", "v91", "v92", "v93", "v94", "v95", "v96", "v97", "v98", "v99", "v100", "v101", "v102", "v103", "v104", "v105", "v106", "v107", "v108", "v109", "v110", "v111", "v112", "v113", "v114", "v115", "v116", "v117", "v118", "v119", "v120", "v121", "v122", "v123", "v124", "v125", "v126", "v127", "v128", "v129", "v130", "v131", "v132", "v133", "v134", "v135", "v136", "v137", "v138", "v139", "v140", "v141", "v142", "v143", "v144", "v145", "v146", "v147", "v148", "v149", "v150", "v151", "v152", "v153", "v154", "v155", "v156", "v157", "v158", "v159", "v160", "v161", "v162", "v163", "v164", "v165", "v166", "v167", "v168", "v169", "v170", "v171", "v172", "v173", "v174", "v175", "v176", "v177", "v178", "v179", "v180", "v181", "v182", "v183", "v184", "v185", "v186", "v187", "v188", "v189", "v190", "v191", "v192", "v193", "v194", "v195", "v196", "v197", "v198", "v199", "v200", "v201", "v202", "v203", "v204", "v205", "v206", "v207", "v208", "v209", "v210", "v211", "v212", "v213", "v214", "v215", "v216", "v217", "v218", "v219", "v220", "v221", "v222", "v223", "v224", "v225", "v226", "v227", "v228", "v229", "v230", "v231", "v232", "v233", "v234", "v235", "v236", "v237", "v238", "v239", "v240", "v241", "v242", "v243", "v244", "v245", "v246", "v247", "v248", "v249", "v250", "v251", "v252", "v253", "v254", "v255", "a0", "a1", "a2", "a3", "a4", "a5", "a6", "a7", "a8", "a9", "a10", "a11", "a12", "a13", "a14", "a15", "a16", "a17", "a18", "a19", "a20", "a21", "a22", "a23", "a24", "a25", "a26", "a27", "a28", "a29", "a30", "a31", "a32", "a33", "a34", "a35", "a36", "a37", "a38", "a39", "a40", "a41", "a42", "a43", "a44", "a45", "a46", "a47", "a48", "a49", "a50", "a51", "a52", "a53", "a54", "a55", "a56", "a57", "a58", "a59", "a60", "a61", "a62", "a63", "a64", "a65", "a66", "a67", "a68", "a69", "a70", "a71", "a72", "a73", "a74", "a75", "a76", "a77", "a78", "a79", "a80", "a81", "a82", "a83", "a84", "a85", "a86", "a87", "a88", "a89", "a90", "a91", "a92", "a93", "a94", "a95", "a96", "a97", "a98", "a99", "a100", "a101", "a102", "a103", "a104", "a105", "a106", "a107", "a108", "a109", "a110", "a111", "a112", "a113", "a114", "a115", "a116", "a117", "a118", "a119", "a120", "a121", "a122", "a123", "a124", "a125", "a126", "a127", "a128", "a129", "a130", "a131", "a132", "a133", "a134", "a135", "a136", "a137", "a138", "a139", "a140", "a141", "a142", "a143", "a144", "a145", "a146", "a147", "a148", "a149", "a150", "a151", "a152", "a153", "a154", "a155", "a156", "a157", "a158", "a159", "a160", "a161", "a162", "a163", "a164", "a165", "a166", "a167", "a168", "a169", "a170", "a171", "a172", "a173", "a174", "a175", "a176", "a177", "a178", "a179", "a180", "a181", "a182", "a183", "a184", "a185", "a186", "a187", "a188", "a189", "a190", "a191", "a192", "a193", "a194", "a195", "a196", "a197", "a198", "a199", "a200", "a201", "a202", "a203", "a204", "a205", "a206", "a207", "a208", "a209", "a210", "a211", "a212", "a213", "a214", "a215", "a216", "a217", "a218", "a219", "a220", "a221", "a222", "a223", "a224", "a225", "a226", "a227", "a228", "a229", "a230", "a231", "a232", "a233", "a234", "a235", "a236", "a237", "a238", "a239", "a240", "a241", "a242", "a243", "a244", "a245", "a246", "a247", "a248", "a249", "a250", "a251", "a252", "a253", "a254", "a255", "s8", "s9", "s10", "s11", "s12", "s13", "s14", "s15", "s16", "s17", "s18", "s19", "s20", "s21", "s22", "s23", "s24", "s25", "s26", "s27", "s28", "s29", "s30", "s31", "s34", "s35", "s36", "s37", "s38", "s39", "s40", "s41", "s43", "s44", "s45", "s46", "s47", "s48", "s49", "s50", "s51", "s52", "s53", "s54", "s55", "s56", "s57", "s58", "s59", "s60", "s61", "s62", "s63", "s64", "s65", "s66", "s67", "vcc", "scc", "memory");
    {
        unsigned l2;
        asm volatile("s_waitcnt vmcnt(0) lgkmcnt(0)\n s_barrier\n v_mbcnt_lo_u32_b32 %0, -1, 0\n v_mbcnt_hi_u32_b32 %0, -1, %0\n s_nop 1"
                     : "=v"(l2) :: "memory");
        guarded_pass(x, wqkv, wo, rpb, rpi, mask, out, ldsb, l2, w);
    }
}


extern "C" void kernel_launch(void* const* d_in, const int* in_sizes, int n_in, void* d_out, int out_size, void* d_ws,
                              size_t ws_size, hipStream_t stream) {
    win_attn_kernel<<<256, 256, 0, stream>>>((const char*)d_in[0], (const char*)d_in[1], (const char*)d_in[2],
                                             (const char*)d_in[3], (const char*)d_in[4], (const char*)d_in[5],
                                             (char*)d_out);

}
```

```cpp
#include <hip/hip_runtime.h>
#include <hip/hip_bf16.h>

typedef unsigned int u32x4 __attribute__((ext_vector_type(4)));

__device__ __forceinline__ void guarded_pass(const char* x, const char* wqkv, const char* wo, const char* rpb, const char* rpi,
                                             const char* mask, char* out, unsigned ldsb, const unsigned lane, const unsigned w) {
    const unsigned wx = blockIdx.x & 63u, wy0 = blockIdx.x >> 6;
    unsigned dm[3], wsrc[3];
#pragma unroll
    for (int p = 0; p < 3; ++p) {
        unsigned gch = 64u * p + lane, t = gch / 24u, c1 = gch % 24u;
        dm[p] = ((8u * wx + 4u + t) & 511u) * 384u + ((c1 ^ (((t >> 1) & 1u) | (w << 1))) << 4);
        wsrc[p] = t * 1152u + ((c1 ^ (2u * (((t >> 1) & 1u) | ((w & 1u) << 1)))) << 4) - 1024u * p;
    }
    u32x4 srdx = {(unsigned)(size_t)x, (unsigned)((size_t)x >> 32) & 0xffffu, 100663296u, 0x20000u};
    u32x4 srdw = {(unsigned)(size_t)wqkv, (unsigned)((size_t)wqkv >> 32) & 0xffffu, 221184u, 0x20000u};
    unsigned ya = (8u * wy0 + 36u + w) * 196608u;
    unsigned wv = w;
    unsigned w3 = w * 3072u, w9 = w * 9216u;
    __builtin_amdgcn_sched_barrier(0);
    asm volatile(
        "s_nop 4\n"
        "s_add_u32 m0, s40, 0\n"
        "s_add_u32 s64, s65, 0\n"
        "buffer_load_dwordx4 v128, s[20:23], s64 offen lds\n"
        "buffer_load_dwordx4 v129, s[20:23], s64 offen offset:1024 lds\n"
        "buffer_load_dwordx4 v130, s[20:23], s64 offen offset:2048 lds\n"
        "s_add_u32 m0, s40, 12288\n"
        "s_add_u32 s64, s65, 36864\n"
        "buffer_load_dwordx4 v128, s[20:23], s64 offen lds\n"
        "buffer_load_dwordx4 v129, s[20:23], s64 offen offset:1024 lds\n"
        "buffer_load_dwordx4 v130, s[20:23], s64 offen offset:2048 lds\n"
        "s_add_u32 m0, s40, 24576\n"
        "s_add_u32 s64, s65, 73728\n"
        "buffer_load_dwordx4 v128, s[20:23], s64 offen lds\n"
        "buffer_load_dwordx4 v129, s[20:23], s64 offen offset:1024 lds\n"
        "buffer_load_dwordx4 v130, s[20:23], s64 offen offset:2048 lds\n"
        :: "{v242}"(dm[0]), "{v243}"(dm[1]), "{v244}"(dm[2]), "{v128}"(wsrc[0]), "{v129}"(wsrc[1]), "{v130}"(wsrc[2]), "{s[8:11]}"(srdx), "{s[20:23]}"(srdw), "{s47}"(ya), "{s40}"(w3), "{s65}"(w9), "s"(ldsb)
        : "s64", "scc", "memory");
    __builtin_amdgcn_sched_barrier(0);

    const unsigned rho = lane & 15u, g = lane >> 4;
    const unsigned cg = w >> 1, T = w & 1u;
    const unsigned ql = 8u * (rho >> 2) + (rho & 3u);
    unsigned xr[2], mr[2];
#pragma unroll
    for (int j = 0; j < 2; ++j) {
        xr[j] = 73728u + ql * 384u + (((4u * j + g) ^ (rho >> 1)) << 4);
        mr[j] = 147456u + 8192u + ql * 128u + (((4u * j + g) ^ (rho >> 1)) << 4);
    }
    unsigned ow = 122880u + (4u * w + g) * 1024u + rho * 16u;
    unsigned ow2 = 122880u + (16u + 2u * w + (g >> 1)) * 1024u + rho * 16u + (g & 1u) * 8u;
    unsigned ord = 122880u + g * 1024u + (32u * T + rho) * 16u;
    __builtin_amdgcn_sched_barrier(0);
    asm volatile(
        "s_nop 4\n"
        "s_add_u32 m0, s40, 36864\n"
        "s_add_u32 s64, s65, 110592\n"
        "buffer_load_dwordx4 v128, s[20:23], s64 offen lds\n"
        "buffer_load_dwordx4 v129, s[20:23], s64 offen offset:1024 lds\n"
        "buffer_load_dwordx4 v130, s[20:23], s64 offen offset:2048 lds\n"
        :: "{v242}"(dm[0]), "{v243}"(dm[1]), "{v244}"(dm[2]), "{v128}"(wsrc[0]), "{v129}"(wsrc[1]), "{v130}"(wsrc[2]), "{s[8:11]}"(srdx), "{s[20:23]}"(srdw), "{s47}"(ya), "{s40}"(w3), "{s65}"(w9), "s"(ldsb)
        : "s64", "scc", "memory");
    __builtin_amdgcn_sched_barrier(0);
    unsigned wor = 0u + cg * 18432u + g * 1536u + rho * 96u + (g & 1u) * 16u;
    unsigned wor2 = 0u + cg * 18432u + g * 1536u + rho * 96u - (g & 1u) * 16u;
    unsigned ml = 147456u + w * 2048u + lane * 16u;
    unsigned ms = (lane >> 3) * 128u + (((lane & 7u) ^ (((lane >> 4) & 1u) | ((w & 1u) << 2))) << 4);
    unsigned st = g * 196608u + cg * 192u + rho * 12u;
    unsigned l16 = lane * 16u;
    unsigned id0 = (rho == 4u * g ? 0x3f80u : 0u) | (rho == 4u * g + 1u ? 0x3f800000u : 0u);
    unsigned id1 = (rho == 4u * g + 2u ? 0x3f80u : 0u) | (rho == 4u * g + 3u ? 0x3f800000u : 0u);
    unsigned x0 = (8u * wx + 4u) * 384u, x1 = ((8u * wx + 8u) & 511u) * 384u;
    u32x4 srdm = {(unsigned)(size_t)mask, (unsigned)((size_t)mask >> 32) & 0xffffu, 33554432u, 0x20000u};
    u32x4 srdo = {(unsigned)(size_t)out, (unsigned)((size_t)out >> 32) & 0xffffu, 100663296u, 0x20000u};
    u32x4 srdwo = {(unsigned)(size_t)wo, (unsigned)((size_t)wo >> 32) & 0xffffu, 73728u, 0x20000u};
    u32x4 srdrp = {(unsigned)(size_t)rpi, (unsigned)((size_t)rpi >> 32) & 0xffffu, 16384u, 0x20000u};
    u32x4 srdrb = {(unsigned)(size_t)rpb, (unsigned)((size_t)rpb >> 32) & 0xffffu, 1800u, 0x20000u};
    __builtin_amdgcn_sched_barrier(0);
    asm volatile(
        "s_nop 4\n"
        "s_add_u32 m0, s40, 49152\n"
        "s_add_u32 s64, s65, 147456\n"
        "buffer_load_dwordx4 v128, s[20:23], s64 offen lds\n"
        "buffer_load_dwordx4 v129, s[20:23], s64 offen offset:1024 lds\n"
        "buffer_load_dwordx4 v130, s[20:23], s64 offen offset:2048 lds\n"
        :: "{v242}"(dm[0]), "{v243}"(dm[1]), "{v244}"(dm[2]), "{v128}"(wsrc[0]), "{v129}"(wsrc[1]), "{v130}"(wsrc[2]), "{s[8:11]}"(srdx), "{s[20:23]}"(srdw), "{s47}"(ya), "{s40}"(w3), "{s65}"(w9), "s"(ldsb)
        : "s64", "scc", "memory");
    __builtin_amdgcn_sched_barrier(0);
    const unsigned q4 = (lane >> 2) & 3u, p4 = lane & 3u;
    unsigned wrd[3];
#pragma unroll
    for (int p = 0; p < 3; ++p) {
        unsigned c = 6u * w + 2u * p + (p4 >> 1);
        wrd[p] = (8u * g + q4) * 384u + ((c ^ (2u * ((q4 >> 1) | ((g & 1u) << 1)))) << 4) + (p4 & 1u) * 8u;
    }
    unsigned od = cg * 18432u + g * 1536u + (((48u * T + rho) ^ (g & 1u)) << 4);
    unsigned rbs = lane * 8u + 2u * w, rbw = 151552u + w * 1024u + lane * 4u;
    const unsigned pfi = ((blockIdx.x >> 3) & 31u) * 4u + w;
    unsigned pf0 = pfi * 2048u + lane * 16u, pf1 = pfi * 1024u + lane * 16u;
    __builtin_amdgcn_sched_barrier(0);
    asm volatile(
        "s_nop 4\n"
        "s_add_u32 m0, s40, 61440\n"
        "s_add_u32 s64, s65, 184320\n"
        "buffer_load_dwordx4 v128, s[20:23], s64 offen lds\n"
        "buffer_load_dwordx4 v129, s[20:23], s64 offen offset:1024 lds\n"
        "buffer_load_dwordx4 v130, s[20:23], s64 offen offset:2048 lds\n"
        :: "{v242}"(dm[0]), "{v243}"(dm[1]), "{v244}"(dm[2]), "{v128}"(wsrc[0]), "{v129}"(wsrc[1]), "{v130}"(wsrc[2]), "{s[8:11]}"(srdx), "{s[20:23]}"(srdw), "{s47}"(ya), "{s40}"(w3), "{s65}"(w9), "s"(ldsb)
        : "s64", "scc", "memory");
    __builtin_amdgcn_sched_barrier(0);
    unsigned rs = g * 256u + ((rho ^ (g | ((w & 1u) << 3))) << 4);
    unsigned rr = 135168u + ql * 256u + (((2u * g) ^ rho) << 4);
    unsigned oa = (4u * g + q4) * 384u + 96u * w + p4 * 8u;
    unsigned ob = (48u * (g >> 1) + 8u * (g & 1u) + q4) * 384u + 96u * w + p4 * 8u;
    unsigned mk = (blockIdx.x + 256u) * 8192u + w * 2048u;
    unsigned raw = (8u * wy0 + 4u + 4u * T) * 196608u;
    asm volatile(
        "s_nop 4\n"
        "s_mul_i32 s40, s42, 3072\n"
        "s_mul_i32 s65, s42, 9216\n"
        "s_lshl_b32 s48, s42, 11\n"
        "s_add_u32 s48, s48, 147456\n"
        "s_mov_b32 s24, 0x3fb8aa3b\n"
        "s_mov_b32 s25, 0xbfb8aa3b\n"
        "s_mov_b32 s26, 0xff7fff7f\n"
        "s_mov_b32 s49, 0x3e13cd3a\n"
        "s_mov_b32 s50, 0x3e13cd3a\n"
        "s_mov_b32 s51, 0x3e13cd3a\n"
        "s_mov_b32 s27, 14\n"
        "s_mov_b32 s28, 73728\n"
        "s_mov_b32 s29, 122880\n"
        "s_mov_b32 s30, 98304\n"
        "s_mov_b32 s38, 0x80000000\n"
        "s_mov_b32 s39, 0x80000000\n"
        "s_lshl_b32 s67, s42, 10\n"
        "s_add_u32 s67, s67, 151552\n"
        "v_mov_b32 v188, v170\n"
        "v_mov_b32 v189, v171\n"
        "v_mov_b32 v210, v170\n"
        "v_mov_b32 v211, v171\n"
        "v_mov_b32 v228, v170\n"
        "v_mov_b32 v229, v171\n"
        "v_accvgpr_write_b32 a252, v140\n"
        "v_add_u32 v136, 98304, v131\n"
        "v_accvgpr_write_b32 a253, v141\n"
        "v_add_u32 v137, 98304, v132\n"
        "v_accvgpr_write_b32 a254, v147\n"
        "v_add_u32 v138, 98304, v133\n"
        "v_mov_b32 v150, v240\n"
        "v_mov_b32 v151, v241\n"
        "v_xor_b32 v253, 64, v139\n"
        "v_xor_b32 v232, 0, v144\n"
        "v_xor_b32 v233, 16, v144\n"
        "v_xor_b32 v234, 128, v144\n"
        "v_xor_b32 v235, 144, v144\n"
        "s_lshl_b32 s46, s42, 12\n"
        "buffer_load_dwordx4 v[64:67], v148, s[20:23], 0 offen\n"
        "buffer_load_dwordx4 v[68:71], v148, s[20:23], 0 offen offset:1024\n"
        "buffer_load_dwordx4 v[72:75], v149, s[52:55], 0 offen\n"
        "s_waitcnt vmcnt(12)\n"
        "s_barrier\n"
        "s_add_u32 m0, s40, 73728\n"
        "ds_read_b64_tr_b16 v[76:77], v131 offset:0\n"
        "ds_read_b64_tr_b16 v[78:79], v131 offset:1536\n"
        "ds_read_b64_tr_b16 v[80:81], v131 offset:12288\n"
        "ds_read_b64_tr_b16 v[82:83], v131 offset:13824\n"
        "ds_read_b64_tr_b16 v[84:85], v131 offset:24576\n"
        "ds_read_b64_tr_b16 v[86:87], v131 offset:26112\n"
        "ds_read_b64_tr_b16 v[88:89], v132 offset:0\n"
        "ds_read_b64_tr_b16 v[90:91], v132 offset:1536\n"
        "ds_read_b64_tr_b16 v[92:93], v132 offset:12288\n"
        "ds_read_b64_tr_b16 v[94:95], v132 offset:13824\n"
        "ds_read_b64_tr_b16 v[104:105], v132 offset:24576\n"
        "ds_read_b64_tr_b16 v[106:107], v132 offset:26112\n"
        "ds_read_b64_tr_b16 v[108:109], v133 offset:0\n"
        "ds_read_b64_tr_b16 v[110:111], v133 offset:1536\n"
        "ds_read_b64_tr_b16 v[116:117], v133 offset:12288\n"
        "ds_read_b64_tr_b16 v[118:119], v133 offset:13824\n"
        "ds_read_b64_tr_b16 v[120:121], v133 offset:24576\n"
        "ds_read_b64_tr_b16 v[122:123], v133 offset:26112\n"
        "s_sub_u32 s64, s47, 6291456\n"
        "buffer_load_dwordx4 v242, s[8:11], s64 offen lds\n"
        "s_add_u32 m0, s40, 74752\n"
        "s_sub_u32 s64, s47, 6291456\n"
        "buffer_load_dwordx4 v243, s[8:11], s64 offen lds\n"
        "s_add_u32 m0, s40, 75776\n"
        "s_sub_u32 s64, s47, 6291456\n"
        "buffer_load_dwordx4 v244, s[8:11], s64 offen lds\n"
        "s_add_u32 m0, s40, 86016\n"
        "s_sub_u32 s64, s47, 5505024\n"
        "buffer_load_dwordx4 v242, s[8:11], s64 offen lds\n"
        "s_add_u32 m0, s40, 87040\n"
        "s_sub_u32 s64, s47, 5505024\n"
        "buffer_load_dwordx4 v243, s[8:11], s64 offen lds\n"
        "s_add_u32 m0, s40, 88064\n"
        "s_sub_u32 s64, s47, 5505024\n"
        "buffer_load_dwordx4 v244, s[8:11], s64 offen lds\n"
        "s_waitcnt lgkmcnt(15)\n"
        "v_lshlrev_b32 v0, 16, v76\n"
        "v_lshlrev_b32 v2, 16, v77\n"
        "v_lshlrev_b32 v4, 16, v78\n"
        "v_lshlrev_b32 v6, 16, v79\n"
        "v_and_b32 v1, 0xffff0000, v76\n"
        "v_and_b32 v3, 0xffff0000, v77\n"
        "v_and_b32 v5, 0xffff0000, v78\n"
        "v_and_b32 v7, 0xffff0000, v79\n"
        "v_pk_mul_f32 v[0:1], v[0:1], s[50:51]\n"
        "v_pk_mul_f32 v[2:3], v[2:3], s[50:51]\n"
        "v_pk_mul_f32 v[4:5], v[4:5], s[50:51]\n"
        "v_pk_mul_f32 v[6:7], v[6:7], s[50:51]\n"
        "v_cvt_pk_bf16_f32 v0, v0, v1\n"
        "v_cvt_pk_bf16_f32 v2, v2, v3\n"
        "v_cvt_pk_bf16_f32 v4, v4, v5\n"
        "v_cvt_pk_bf16_f32 v6, v6, v7\n"
        "v_accvgpr_write_b32 a0, v0\n"
        "v_accvgpr_write_b32 a1, v2\n"
        "v_accvgpr_write_b32 a2, v4\n"
        "v_accvgpr_write_b32 a3, v6\n"
        "v_lshlrev_b32 v8, 16, v80\n"
        "v_lshlrev_b32 v10, 16, v81\n"
        "s_waitcnt lgkmcnt(14)\n"
        "v_lshlrev_b32 v12, 16, v82\n"
        "v_lshlrev_b32 v14, 16, v83\n"
        "v_and_b32 v9, 0xffff0000, v80\n"
        "v_and_b32 v11, 0xffff0000, v81\n"
        "v_and_b32 v13, 0xffff0000, v82\n"
        "v_and_b32 v15, 0xffff0000, v83\n"
        "v_pk_mul_f32 v[8:9], v[8:9], s[50:51]\n"
        "v_pk_mul_f32 v[10:11], v[10:11], s[50:51]\n"
        "v_pk_mul_f32 v[12:13], v[12:13], s[50:51]\n"
        "v_pk_mul_f32 v[14:15], v[14:15], s[50:51]\n"
        "v_cvt_pk_bf16_f32 v8, v8, v9\n"
        "v_cvt_pk_bf16_f32 v10, v10, v11\n"
        "v_cvt_pk_bf16_f32 v12, v12, v13\n"
        "v_cvt_pk_bf16_f32 v14, v14, v15\n"
        "v_accvgpr_write_b32 a4, v8\n"
        "v_accvgpr_write_b32 a5, v10\n"
        "v_accvgpr_write_b32 a6, v12\n"
        "v_accvgpr_write_b32 a7, v14\n"
        "s_waitcnt lgkmcnt(13)\n"
        "v_lshlrev_b32 v0, 16, v84\n"
        "v_lshlrev_b32 v2, 16, v85\n"
        "s_waitcnt lgkmcnt(12)\n"
        "v_lshlrev_b32 v4, 16, v86\n"
        "v_lshlrev_b32 v6, 16, v87\n"
        "v_and_b32 v1, 0xffff0000, v84\n"
        "v_and_b32 v3, 0xffff0000, v85\n"
        "v_and_b32 v5, 0xffff0000, v86\n"
        "v_and_b32 v7, 0xffff0000, v87\n"
        "v_pk_mul_f32 v[0:1], v[0:1], s[50:51]\n"
        "v_pk_mul_f32 v[2:3], v[2:3], s[50:51]\n"
        "v_pk_mul_f32 v[4:5], v[4:5], s[50:51]\n"
        "v_pk_mul_f32 v[6:7], v[6:7], s[50:51]\n"
        "v_cvt_pk_bf16_f32 v0, v0, v1\n"
        "v_cvt_pk_bf16_f32 v2, v2, v3\n"
        "v_cvt_pk_bf16_f32 v4, v4, v5\n"
        "v_cvt_pk_bf16_f32 v6, v6, v7\n"
        "v_accvgpr_write_b32 a8, v0\n"
        "v_accvgpr_write_b32 a9, v2\n"
        "v_accvgpr_write_b32 a10, v4\n"
        "v_accvgpr_write_b32 a11, v6\n"
        "s_waitcnt lgkmcnt(11)\n"
        "v_lshlrev_b32 v8, 16, v88\n"
        "v_lshlrev_b32 v10, 16, v89\n"
        "s_waitcnt lgkmcnt(10)\n"
        "v_lshlrev_b32 v12, 16, v90\n"
        "v_lshlrev_b32 v14, 16, v91\n"
        "buffer_load_ushort v236, v145, s[60:63], 0 offen offset:0\n"
        "buffer_load_ushort v237, v145, s[60:63], 0 offen offset:512\n"
        "buffer_load_ushort v238, v145, s[60:63], 0 offen offset:1024\n"
        "buffer_load_ushort v239, v145, s[60:63], 0 offen offset:1536\n"
        "v_and_b32 v9, 0xffff0000, v88\n"
        "v_and_b32 v11, 0xffff0000, v89\n"
        "v_and_b32 v13, 0xffff0000, v90\n"
        "v_and_b32 v15, 0xffff0000, v91\n"
        "v_pk_mul_f32 v[8:9], v[8:9], s[50:51]\n"
        "v_pk_mul_f32 v[10:11], v[10:11], s[50:51]\n"
        "v_pk_mul_f32 v[12:13], v[12:13], s[50:51]\n"
        "v_pk_mul_f32 v[14:15], v[14:15], s[50:51]\n"
        "s_add_u32 m0, s46, 135168\n"
        "v_cvt_pk_bf16_f32 v8, v8, v9\n"
        "v_cvt_pk_bf16_f32 v10, v10, v11\n"
        "v_cvt_pk_bf16_f32 v12, v12, v13\n"
        "v_cvt_pk_bf16_f32 v14, v14, v15\n"
        "s_mov_b32 s64, s46\n"
        "v_accvgpr_write_b32 a24, v8\n"
        "v_accvgpr_write_b32 a25, v10\n"
        "v_accvgpr_write_b32 a26, v12\n"
        "v_accvgpr_write_b32 a27, v14\n"
        "buffer_load_dwordx4 v139, s[56:59], s64 offen lds\n"
        "s_waitcnt lgkmcnt(9)\n"
        "v_lshlrev_b32 v0, 16, v92\n"
        "v_lshlrev_b32 v2, 16, v93\n"
        "s_waitcnt lgkmcnt(8)\n"
        "v_lshlrev_b32 v4, 16, v94\n"
        "v_lshlrev_b32 v6, 16, v95\n"
        "s_waitcnt lgkmcnt(0)\n"
        "s_waitcnt vmcnt(14)\n"
        "s_barrier\n"
        "v_and_b32 v1, 0xffff0000, v92\n"
        "v_and_b32 v3, 0xffff0000, v93\n"
        "v_and_b32 v5, 0xffff0000, v94\n"
        "v_and_b32 v7, 0xffff0000, v95\n"
        "ds_read_b64_tr_b16 v[152:153], v131 offset:36864\n"
        "ds_read_b64_tr_b16 v[154:155], v131 offset:38400\n"
        "ds_read_b64_tr_b16 v[172:173], v131 offset:49152\n"
        "ds_read_b64_tr_b16 v[174:175], v131 offset:50688\n"
        "ds_read_b64_tr_b16 v[192:193], v131 offset:61440\n"
        "ds_read_b64_tr_b16 v[194:195], v131 offset:62976\n"
        "ds_read_b64_tr_b16 v[212:213], v132 offset:36864\n"
        "ds_read_b64_tr_b16 v[214:215], v132 offset:38400\n"
        "ds_read_b64_tr_b16 v[156:157], v132 offset:49152\n"
        "ds_read_b64_tr_b16 v[158:159], v132 offset:50688\n"
        "ds_read_b64_tr_b16 v[176:177], v132 offset:61440\n"
        "ds_read_b64_tr_b16 v[178:179], v132 offset:62976\n"
        "ds_read_b64_tr_b16 v[196:197], v133 offset:36864\n"
        "ds_read_b64_tr_b16 v[198:199], v133 offset:38400\n"
        "ds_read_b64_tr_b16 v[216:217], v133 offset:49152\n"
        "ds_read_b64_tr_b16 v[218:219], v133 offset:50688\n"
        "ds_read_b64_tr_b16 v[124:125], v133 offset:61440\n"
        "ds_read_b64_tr_b16 v[126:127], v133 offset:62976\n"
        "v_pk_mul_f32 v[0:1], v[0:1], s[50:51]\n"
        "v_pk_mul_f32 v[2:3], v[2:3], s[50:51]\n"
        "v_pk_mul_f32 v[4:5], v[4:5], s[50:51]\n"
        "v_pk_mul_f32 v[6:7], v[6:7], s[50:51]\n"
        "v_cvt_pk_bf16_f32 v0, v0, v1\n"
        "v_cvt_pk_bf16_f32 v2, v2, v3\n"
        "v_cvt_pk_bf16_f32 v4, v4, v5\n"
        "v_cvt_pk_bf16_f32 v6, v6, v7\n"
        "buffer_load_dwordx4 v139, s[56:59], s64 offen offset:1024 lds\n"
        "v_accvgpr_write_b32 a28, v0\n"
        "v_accvgpr_write_b32 a29, v2\n"
        "v_accvgpr_write_b32 a30, v4\n"
        "v_accvgpr_write_b32 a31, v6\n"
        "v_lshlrev_b32 v8, 16, v104\n"
        "v_lshlrev_b32 v10, 16, v105\n"
        "v_lshlrev_b32 v12, 16, v106\n"
        "v_lshlrev_b32 v14, 16, v107\n"
        "v_and_b32 v9, 0xffff0000, v104\n"
        "v_and_b32 v11, 0xffff0000, v105\n"
        "v_and_b32 v13, 0xffff0000, v106\n"
        "v_and_b32 v15, 0xffff0000, v107\n"
        "v_pk_mul_f32 v[8:9], v[8:9], s[50:51]\n"
        "v_pk_mul_f32 v[10:11], v[10:11], s[50:51]\n"
        "v_pk_mul_f32 v[12:13], v[12:13], s[50:51]\n"
        "v_pk_mul_f32 v[14:15], v[14:15], s[50:51]\n"
        "buffer_load_dwordx4 v253, s[56:59], s64 offen offset:2048 lds\n"
        "v_cvt_pk_bf16_f32 v8, v8, v9\n"
        "v_cvt_pk_bf16_f32 v10, v10, v11\n"
        "v_cvt_pk_bf16_f32 v12, v12, v13\n"
        "v_cvt_pk_bf16_f32 v14, v14, v15\n"
        "v_accvgpr_write_b32 a32, v8\n"
        "v_accvgpr_write_b32 a33, v10\n"
        "v_accvgpr_write_b32 a34, v12\n"
        "v_accvgpr_write_b32 a35, v14\n"
        "v_lshlrev_b32 v0, 16, v108\n"
        "v_lshlrev_b32 v2, 16, v109\n"
        "v_lshlrev_b32 v4, 16, v110\n"
        "v_lshlrev_b32 v6, 16, v111\n"
        "v_and_b32 v1, 0xffff0000, v108\n"
        "v_and_b32 v3, 0xffff0000, v109\n"
        "v_and_b32 v5, 0xffff0000, v110\n"
        "v_and_b32 v7, 0xffff0000, v111\n"
        "buffer_load_dwordx4 v253, s[56:59], s64 offen offset:3072 lds\n"
        "v_pk_mul_f32 v[0:1], v[0:1], s[50:51]\n"
        "v_pk_mul_f32 v[2:3], v[2:3], s[50:51]\n"
        "v_pk_mul_f32 v[4:5], v[4:5], s[50:51]\n"
        "v_pk_mul_f32 v[6:7], v[6:7], s[50:51]\n"
        "v_cvt_pk_bf16_f32 v0, v0, v1\n"
        "v_cvt_pk_bf16_f32 v2, v2, v3\n"
        "v_cvt_pk_bf16_f32 v4, v4, v5\n"
        "v_cvt_pk_bf16_f32 v6, v6, v7\n"
        "v_accvgpr_write_b32 a48, v0\n"
        "v_accvgpr_write_b32 a49, v2\n"
        "v_accvgpr_write_b32 a50, v4\n"
        "v_accvgpr_write_b32 a51, v6\n"
        "v_lshlrev_b32 v8, 16, v116\n"
        "v_lshlrev_b32 v10, 16, v117\n"
        "v_lshlrev_b32 v12, 16, v118\n"
        "v_lshlrev_b32 v14, 16, v119\n"
        "v_and_b32 v9, 0xffff0000, v116\n"
        "v_and_b32 v11, 0xffff0000, v117\n"
        "v_and_b32 v13, 0xffff0000, v118\n"
        "v_and_b32 v15, 0xffff0000, v119\n"
        "v_pk_mul_f32 v[8:9], v[8:9], s[50:51]\n"
        "v_pk_mul_f32 v[10:11], v[10:11], s[50:51]\n"
        "v_pk_mul_f32 v[12:13], v[12:13], s[50:51]\n"
        "v_pk_mul_f32 v[14:15], v[14:15], s[50:51]\n"
        "v_cvt_pk_bf16_f32 v8, v8, v9\n"
        "v_cvt_pk_bf16_f32 v10, v10, v11\n"
        "v_cvt_pk_bf16_f32 v12, v12, v13\n"
        "v_cvt_pk_bf16_f32 v14, v14, v15\n"
        "v_accvgpr_write_b32 a52, v8\n"
        "v_accvgpr_write_b32 a53, v10\n"
        "v_accvgpr_write_b32 a54, v12\n"
        "v_accvgpr_write_b32 a55, v14\n"
        "v_lshlrev_b32 v0, 16, v120\n"
        "v_lshlrev_b32 v2, 16, v121\n"
        "v_lshlrev_b32 v4, 16, v122\n"
        "v_lshlrev_b32 v6, 16, v123\n"
        "v_and_b32 v1, 0xffff0000, v120\n"
        "v_and_b32 v3, 0xffff0000, v121\n"
        "v_and_b32 v5, 0xffff0000, v122\n"
        "v_and_b32 v7, 0xffff0000, v123\n"
        "v_pk_mul_f32 v[0:1], v[0:1], s[50:51]\n"
        "v_pk_mul_f32 v[2:3], v[2:3], s[50:51]\n"
        "v_pk_mul_f32 v[4:5], v[4:5], s[50:51]\n"
        "v_pk_mul_f32 v[6:7], v[6:7], s[50:51]\n"
        "v_cvt_pk_bf16_f32 v0, v0, v1\n"
        "v_cvt_pk_bf16_f32 v2, v2, v3\n"
        "v_cvt_pk_bf16_f32 v4, v4, v5\n"
        "v_cvt_pk_bf16_f32 v6, v6, v7\n"
        "v_accvgpr_write_b32 a56, v0\n"
        "v_accvgpr_write_b32 a57, v2\n"
        "v_accvgpr_write_b32 a58, v4\n"
        "v_accvgpr_write_b32 a59, v6\n"
        "s_waitcnt lgkmcnt(15)\n"
        "v_lshlrev_b32 v8, 16, v152\n"
        "v_lshlrev_b32 v10, 16, v153\n"
        "v_lshlrev_b32 v12, 16, v154\n"
        "v_lshlrev_b32 v14, 16, v155\n"
        "v_and_b32 v9, 0xffff0000, v152\n"
        "v_and_b32 v11, 0xffff0000, v153\n"
        "v_and_b32 v13, 0xffff0000, v154\n"
        "v_and_b32 v15, 0xffff0000, v155\n"
        "v_pk_mul_f32 v[8:9], v[8:9], s[50:51]\n"
        "v_pk_mul_f32 v[10:11], v[10:11], s[50:51]\n"
        "v_pk_mul_f32 v[12:13], v[12:13], s[50:51]\n"
        "v_pk_mul_f32 v[14:15], v[14:15], s[50:51]\n"
        "v_cvt_pk_bf16_f32 v8, v8, v9\n"
        "v_cvt_pk_bf16_f32 v10, v10, v11\n"
        "v_cvt_pk_bf16_f32 v12, v12, v13\n"
        "v_cvt_pk_bf16_f32 v14, v14, v15\n"
        "v_accvgpr_write_b32 a12, v8\n"
        "v_accvgpr_write_b32 a13, v10\n"
        "v_accvgpr_write_b32 a14, v12\n"
        "v_accvgpr_write_b32 a15, v14\n"
        "v_lshlrev_b32 v0, 16, v172\n"
        "v_lshlrev_b32 v2, 16, v173\n"
        "s_waitcnt lgkmcnt(14)\n"
        "v_lshlrev_b32 v4, 16, v174\n"
        "v_lshlrev_b32 v6, 16, v175\n"
        "v_and_b32 v1, 0xffff0000, v172\n"
        "v_and_b32 v3, 0xffff0000, v173\n"
        "v_and_b32 v5, 0xffff0000, v174\n"
        "v_and_b32 v7, 0xffff0000, v175\n"
        "v_pk_mul_f32 v[0:1], v[0:1], s[50:51]\n"
        "v_pk_mul_f32 v[2:3], v[2:3], s[50:51]\n"
        "v_pk_mul_f32 v[4:5], v[4:5], s[50:51]\n"
        "v_pk_mul_f32 v[6:7], v[6:7], s[50:51]\n"
        "v_cvt_pk_bf16_f32 v0, v0, v1\n"
        "v_cvt_pk_bf16_f32 v2, v2, v3\n"
        "v_cvt_pk_bf16_f32 v4, v4, v5\n"
        "v_cvt_pk_bf16_f32 v6, v6, v7\n"
        "v_accvgpr_write_b32 a16, v0\n"
        "v_accvgpr_write_b32 a17, v2\n"
        "v_accvgpr_write_b32 a18, v4\n"
        "v_accvgpr_write_b32 a19, v6\n"
        "s_waitcnt lgkmcnt(13)\n"
        "v_lshlrev_b32 v8, 16, v192\n"
        "v_lshlrev_b32 v10, 16, v193\n"
        "s_waitcnt lgkmcnt(12)\n"
        "v_lshlrev_b32 v12, 16, v194\n"
        "v_lshlrev_b32 v14, 16, v195\n"
        "v_and_b32 v9, 0xffff0000, v192\n"
        "v_and_b32 v11, 0xffff0000, v193\n"
        "v_and_b32 v13, 0xffff0000, v194\n"
        "v_and_b32 v15, 0xffff0000, v195\n"
        "v_pk_mul_f32 v[8:9], v[8:9], s[50:51]\n"
        "v_pk_mul_f32 v[10:11], v[10:11], s[50:51]\n"
        "v_pk_mul_f32 v[12:13], v[12:13], s[50:51]\n"
        "v_pk_mul_f32 v[14:15], v[14:15], s[50:51]\n"
        "v_cvt_pk_bf16_f32 v8, v8, v9\n"
        "v_cvt_pk_bf16_f32 v10, v10, v11\n"
        "v_cvt_pk_bf16_f32 v12, v12, v13\n"
        "v_cvt_pk_bf16_f32 v14, v14, v15\n"
        "v_accvgpr_write_b32 a20, v8\n"
        "v_accvgpr_write_b32 a21, v10\n"
        "v_accvgpr_write_b32 a22, v12\n"
        "v_accvgpr_write_b32 a23, v14\n"
        "s_waitcnt lgkmcnt(11)\n"
        "v_lshlrev_b32 v0, 16, v212\n"
        "v_lshlrev_b32 v2, 16, v213\n"
        "s_waitcnt lgkmcnt(10)\n"
        "v_lshlrev_b32 v4, 16, v214\n"
        "v_lshlrev_b32 v6, 16, v215\n"
        "v_and_b32 v1, 0xffff0000, v212\n"
        "v_and_b32 v3, 0xffff0000, v213\n"
        "v_and_b32 v5, 0xffff0000, v214\n"
        "v_and_b32 v7, 0xffff0000, v215\n"
        "v_pk_mul_f32 v[0:1], v[0:1], s[50:51]\n"
        "v_pk_mul_f32 v[2:3], v[2:3], s[50:51]\n"
        "v_pk_mul_f32 v[4:5], v[4:5], s[50:51]\n"
        "v_pk_mul_f32 v[6:7], v[6:7], s[50:51]\n"
        "v_cvt_pk_bf16_f32 v0, v0, v1\n"
        "v_cvt_pk_bf16_f32 v2, v2, v3\n"
        "v_cvt_pk_bf16_f32 v4, v4, v5\n"
        "v_cvt_pk_bf16_f32 v6, v6, v7\n"
        "s_add_u32 m0, s40, 98304\n"
        "v_accvgpr_write_b32 a36, v0\n"
        "v_accvgpr_write_b32 a37, v2\n"
        "v_accvgpr_write_b32 a38, v4\n"
        "v_accvgpr_write_b32 a39, v6\n"
        "s_add_u32 s64, s65, 384\n"
        "s_waitcnt lgkmcnt(9)\n"
        "v_lshlrev_b32 v8, 16, v156\n"
        "v_lshlrev_b32 v10, 16, v157\n"
        "s_waitcnt lgkmcnt(8)\n"
        "v_lshlrev_b32 v12, 16, v158\n"
        "v_lshlrev_b32 v14, 16, v159\n"
        "buffer_load_dwordx4 v128, s[20:23], s64 offen lds\n"
        "v_and_b32 v9, 0xffff0000, v156\n"
        "v_and_b32 v11, 0xffff0000, v157\n"
        "v_and_b32 v13, 0xffff0000, v158\n"
        "v_and_b32 v15, 0xffff0000, v159\n"
        "v_pk_mul_f32 v[8:9], v[8:9], s[50:51]\n"
        "v_pk_mul_f32 v[10:11], v[10:11], s[50:51]\n"
        "v_pk_mul_f32 v[12:13], v[12:13], s[50:51]\n"
        "v_pk_mul_f32 v[14:15], v[14:15], s[50:51]\n"
        "v_cvt_pk_bf16_f32 v8, v8, v9\n"
        "v_cvt_pk_bf16_f32 v10, v10, v11\n"
        "v_cvt_pk_bf16_f32 v12, v12, v13\n"
        "v_cvt_pk_bf16_f32 v14, v14, v15\n"
        "v_accvgpr_write_b32 a40, v8\n"
        "v_accvgpr_write_b32 a41, v10\n"
        "v_accvgpr_write_b32 a42, v12\n"
        "v_accvgpr_write_b32 a43, v14\n"
        "buffer_load_dwordx4 v129, s[20:23], s64 offen offset:1024 lds\n"
        "s_waitcnt lgkmcnt(7)\n"
        "v_lshlrev_b32 v0, 16, v176\n"
        "v_lshlrev_b32 v2, 16, v177\n"
        "s_waitcnt lgkmcnt(6)\n"
        "v_lshlrev_b32 v4, 16, v178\n"
        "v_lshlrev_b32 v6, 16, v179\n"
        "v_and_b32 v1, 0xffff0000, v176\n"
        "v_and_b32 v3, 0xffff0000, v177\n"
        "v_and_b32 v5, 0xffff0000, v178\n"
        "v_and_b32 v7, 0xffff0000, v179\n"
        "v_pk_mul_f32 v[0:1], v[0:1], s[50:51]\n"
        "v_pk_mul_f32 v[2:3], v[2:3], s[50:51]\n"
        "v_pk_mul_f32 v[4:5], v[4:5], s[50:51]\n"
        "v_pk_mul_f32 v[6:7], v[6:7], s[50:51]\n"
        "v_cvt_pk_bf16_f32 v0, v0, v1\n"
        "v_cvt_pk_bf16_f32 v2, v2, v3\n"
        "v_cvt_pk_bf16_f32 v4, v4, v5\n"
        "v_cvt_pk_bf16_f32 v6, v6, v7\n"
        "buffer_load_dwordx4 v130, s[20:23], s64 offen offset:2048 lds\n"
        "v_accvgpr_write_b32 a44, v0\n"
        "v_accvgpr_write_b32 a45, v2\n"
        "v_accvgpr_write_b32 a46, v4\n"
        "v_accvgpr_write_b32 a47, v6\n"
        "s_waitcnt lgkmcnt(5)\n"
        "v_lshlrev_b32 v8, 16, v196\n"
        "v_lshlrev_b32 v10, 16, v197\n"
        "s_waitcnt lgkmcnt(4)\n"
        "v_lshlrev_b32 v12, 16, v198\n"
        "v_lshlrev_b32 v14, 16, v199\n"
        "s_add_u32 m0, s40, 110592\n"
        "v_and_b32 v9, 0xffff0000, v196\n"
        "v_and_b32 v11, 0xffff0000, v197\n"
        "v_and_b32 v13, 0xffff0000, v198\n"
        "v_and_b32 v15, 0xffff0000, v199\n"
        "s_add_u32 s64, s65, 37248\n"
        "v_pk_mul_f32 v[8:9], v[8:9], s[50:51]\n"
        "v_pk_mul_f32 v[10:11], v[10:11], s[50:51]\n"
        "v_pk_mul_f32 v[12:13], v[12:13], s[50:51]\n"
        "v_pk_mul_f32 v[14:15], v[14:15], s[50:51]\n"
        "buffer_load_dwordx4 v128, s[20:23], s64 offen lds\n"
        "v_cvt_pk_bf16_f32 v8, v8, v9\n"
        "v_cvt_pk_bf16_f32 v10, v10, v11\n"
        "v_cvt_pk_bf16_f32 v12, v12, v13\n"
        "v_cvt_pk_bf16_f32 v14, v14, v15\n"
        "v_accvgpr_write_b32 a60, v8\n"
        "v_accvgpr_write_b32 a61, v10\n"
        "v_accvgpr_write_b32 a62, v12\n"
        "v_accvgpr_write_b32 a63, v14\n"
        "s_waitcnt lgkmcnt(3)\n"
        "v_lshlrev_b32 v0, 16, v216\n"
        "v_lshlrev_b32 v2, 16, v217\n"
        "s_waitcnt lgkmcnt(2)\n"
        "v_lshlrev_b32 v4, 16, v218\n"
        "v_lshlrev_b32 v6, 16, v219\n"
        "v_and_b32 v1, 0xffff0000, v216\n"
        "v_and_b32 v3, 0xffff0000, v217\n"
        "v_and_b32 v5, 0xffff0000, v218\n"
        "v_and_b32 v7, 0xffff0000, v219\n"
        "buffer_load_dwordx4 v129, s[20:23], s64 offen offset:1024 lds\n"
        "v_pk_mul_f32 v[0:1], v[0:1], s[50:51]\n"
        "v_pk_mul_f32 v[2:3], v[2:3], s[50:51]\n"
        "v_pk_mul_f32 v[4:5], v[4:5], s[50:51]\n"
        "v_pk_mul_f32 v[6:7], v[6:7], s[50:51]\n"
        "v_cvt_pk_bf16_f32 v0, v0, v1\n"
        "v_cvt_pk_bf16_f32 v2, v2, v3\n"
        "v_cvt_pk_bf16_f32 v4, v4, v5\n"
        "v_cvt_pk_bf16_f32 v6, v6, v7\n"
        "v_accvgpr_write_b32 a64, v0\n"
        "v_accvgpr_write_b32 a65, v2\n"
        "v_accvgpr_write_b32 a66, v4\n"
        "v_accvgpr_write_b32 a67, v6\n"
        "s_waitcnt lgkmcnt(1)\n"
        "v_lshlrev_b32 v8, 16, v124\n"
        "v_lshlrev_b32 v10, 16, v125\n"
        "s_waitcnt lgkmcnt(0)\n"
        "v_lshlrev_b32 v12, 16, v126\n"
        "v_lshlrev_b32 v14, 16, v127\n"
        "buffer_load_dwordx4 v130, s[20:23], s64 offen offset:2048 lds\n"
        "v_and_b32 v9, 0xffff0000, v124\n"
        "v_and_b32 v11, 0xffff0000, v125\n"
        "v_and_b32 v13, 0xffff0000, v126\n"
        "v_and_b32 v15, 0xffff0000, v127\n"
        "v_pk_mul_f32 v[8:9], v[8:9], s[50:51]\n"
        "v_pk_mul_f32 v[10:11], v[10:11], s[50:51]\n"
        "v_pk_mul_f32 v[12:13], v[12:13], s[50:51]\n"
        "v_pk_mul_f32 v[14:15], v[14:15], s[50:51]\n"
        "s_add_u32 m0, s40, 122880\n"
        "v_cvt_pk_bf16_f32 v8, v8, v9\n"
        "v_cvt_pk_bf16_f32 v10, v10, v11\n"
        "v_cvt_pk_bf16_f32 v12, v12, v13\n"
        "v_cvt_pk_bf16_f32 v14, v14, v15\n"
        "s_add_u32 s64, s65, 74112\n"
        "v_accvgpr_write_b32 a68, v8\n"
        "v_accvgpr_write_b32 a69, v10\n"
        "v_accvgpr_write_b32 a70, v12\n"
        "v_accvgpr_write_b32 a71, v14\n"
        "buffer_load_dwordx4 v128, s[20:23], s64 offen lds\n"
        "buffer_load_dwordx4 v129, s[20:23], s64 offen offset:1024 lds\n"
        "buffer_load_dwordx4 v130, s[20:23], s64 offen offset:2048 lds\n"
        "s_waitcnt vmcnt(20)\n"
        "s_barrier\n"
        "ds_read_b128 a[224:227], v150 offset:0\n"
        "s_waitcnt lgkmcnt(0)\n"
        "v_mfma_f32_16x16x32_bf16 v[96:99], a[0:3], a[224:227], 0\n"
        "ds_read_b128 a[228:231], v151 offset:0\n"
        "ds_read_b128 a[232:235], v150 offset:128\n"
        "s_add_u32 m0, s40, 0\n"
        "s_add_u32 s64, s65, 110976\n"
        "s_waitcnt lgkmcnt(1)\n"
        "v_mfma_f32_16x16x32_bf16 v[96:99], a[4:7], a[228:231], v[96:99]\n"
        "s_waitcnt lgkmcnt(0)\n"
        "ds_read_b128 a[236:239], v151 offset:128\n"
        "ds_read_b128 a[240:243], v150 offset:256\n"
        "v_mfma_f32_16x16x32_bf16 v[96:99], a[8:11], a[232:235], v[96:99]\n"
        "s_waitcnt lgkmcnt(1)\n"
        "ds_read_b128 a[244:247], v151 offset:256\n"
        "buffer_load_dwordx4 v128, s[20:23], s64 offen lds\n"
        "v_mfma_f32_16x16x32_bf16 v[96:99], a[12:15], a[236:239], v[96:99]\n"
        "s_waitcnt lgkmcnt(1)\n"
        "ds_read_b128 a[248:251], v150 offset:1536\n"
        "ds_read_b128 a[216:219], v151 offset:1536\n"
        "v_mfma_f32_16x16x32_bf16 v[96:99], a[16:19], a[240:243], v[96:99]\n"
        "s_waitcnt lgkmcnt(2)\n"
        "ds_read_b128 a[220:223], v150 offset:1664\n"
        "v_mfma_f32_16x16x32_bf16 v[96:99], a[20:23], a[244:247], v[96:99]\n"
        "buffer_load_dwordx4 v129, s[20:23], s64 offen offset:1024 lds\n"
        "v_mfma_f32_16x16x32_bf16 v[100:103], a[24:27], a[224:227], 0\n"
        "v_mfma_f32_16x16x32_bf16 v[100:103], a[28:31], a[228:231], v[100:103]\n"
        "v_mfma_f32_16x16x32_bf16 v[100:103], a[32:35], a[232:235], v[100:103]\n"
        "v_mfma_f32_16x16x32_bf16 v[100:103], a[36:39], a[236:239], v[100:103]\n"
        "buffer_load_dwordx4 v130, s[20:23], s64 offen offset:2048 lds\n"
        "v_mfma_f32_16x16x32_bf16 v[100:103], a[40:43], a[240:243], v[100:103]\n"
        "s_waitcnt vmcnt(19)\n"
        "v_lshlrev_b32 v236, 16, v236\n"
        "v_cvt_pk_bf16_f32 v156, v96, v97\n"
        "v_mfma_f32_16x16x32_bf16 v[100:103], a[44:47], a[244:247], v[100:103]\n"
        "s_add_u32 m0, s40, 12288\n"
        "ds_write_b32 v146, v236 offset:0\n"
        "v_mfma_f32_16x16x32_bf16 v[112:115], a[48:51], a[224:227], 0\n"
        "ds_read_b128 a[224:227], v151 offset:1664\n"
        "s_add_u32 s64, s65, 147840\n"
        "v_cvt_pk_bf16_f32 v157, v98, v99\n"
        "s_waitcnt vmcnt(18)\n"
        "v_lshlrev_b32 v237, 16, v237\n"
        "v_mfma_f32_16x16x32_bf16 v[112:115], a[52:55], a[228:231], v[112:115]\n"
        "ds_read_b128 a[228:231], v150 offset:1792\n"
        "buffer_load_dwordx4 v128, s[20:23], s64 offen lds\n"
        "v_cvt_pk_bf16_f32 v158, v100, v101\n"
        "v_mfma_f32_16x16x32_bf16 v[112:115], a[56:59], a[232:235], v[112:115]\n"
        "ds_read_b128 a[232:235], v151 offset:1792\n"
        "ds_write_b32 v146, v237 offset:256\n"
        "v_mfma_f32_16x16x32_bf16 v[112:115], a[60:63], a[236:239], v[112:115]\n"
        "v_cvt_pk_bf16_f32 v159, v102, v103\n"
        "s_waitcnt vmcnt(18)\n"
        "v_lshlrev_b32 v238, 16, v238\n"
        "s_waitcnt vmcnt(17)\n"
        "v_lshlrev_b32 v239, 16, v239\n"
        "v_mfma_f32_16x16x32_bf16 v[112:115], a[64:67], a[240:243], v[112:115]\n"
        "v_mfma_f32_16x16x32_bf16 v[112:115], a[68:71], a[244:247], v[112:115]\n"
        "s_waitcnt lgkmcnt(7)\n"
        "ds_write_b32 v146, v238 offset:512\n"
        "buffer_load_dwordx4 v129, s[20:23], s64 offen offset:1024 lds\n"
        "v_mfma_f32_16x16x32_bf16 v[96:99], a[0:3], a[248:251], 0\n"
        "s_waitcnt lgkmcnt(7)\n"
        "ds_write_b32 v146, v239 offset:768\n"
        "s_add_u32 s35, s47, 786432\n"
        "s_sub_u32 s46, s35, 100663296\n"
        "s_cmp_lt_u32 s46, 786432\n"
        "s_waitcnt vmcnt(5)\n"
        "s_barrier\n"
        "v_mfma_f32_16x16x32_bf16 v[96:99], a[4:7], a[216:219], v[96:99]\n"
        "s_waitcnt lgkmcnt(7)\n"
        "s_cselect_b32 s35, s46, s35\n"
        "s_add_u32 s41, s30, s40\n"
        "ds_read_b64_tr_b16 a[72:73], v136 offset:0\n"
        "ds_read_b64_tr_b16 a[74:75], v136 offset:1536\n"
        "v_mfma_f32_16x16x32_bf16 v[96:99], a[8:11], a[220:223], v[96:99]\n"
        "s_waitcnt lgkmcnt(7)\n"
        "ds_read_b64_tr_b16 a[76:77], v136 offset:12288\n"
        "ds_read_b64_tr_b16 a[78:79], v136 offset:13824\n"
        "v_cvt_pk_bf16_f32 v160, v112, v113\n"
        "v_mfma_f32_16x16x32_bf16 v[96:99], a[12:15], a[224:227], v[96:99]\n"
        "s_waitcnt lgkmcnt(8)\n"
        "buffer_load_dwordx4 v130, s[20:23], s64 offen offset:2048 lds\n"
        "ds_read_b64_tr_b16 a[80:81], v136 offset:24576\n"
        "v_cvt_pk_bf16_f32 v161, v114, v115\n"
        "v_mfma_f32_16x16x32_bf16 v[96:99], a[16:19], a[228:231], v[96:99]\n"
        "s_waitcnt lgkmcnt(8)\n"
        "ds_read_b64_tr_b16 a[82:83], v136 offset:26112\n"
        "ds_read_b64_tr_b16 a[96:97], v137 offset:0\n"
        "v_cvt_pk_bf16_f32 v166, v112, v113\n"
        "v_mfma_f32_16x16x32_bf16 v[96:99], a[20:23], a[232:235], v[96:99]\n"
        "s_add_u32 m0, s40, 24576\n"
        "ds_read_b64_tr_b16 a[98:99], v137 offset:1536\n"
        "ds_read_b128 a[236:239], v150 offset:12288\n"
        "v_cvt_pk_bf16_f32 v167, v114, v115\n"
        "v_mfma_f32_16x16x32_bf16 v[100:103], a[24:27], a[248:251], 0\n"
        "s_add_u32 s64, s65, 184704\n"
        "ds_read_b64_tr_b16 a[100:101], v137 offset:12288\n"
        "ds_read_b128 a[240:243], v151 offset:12288\n"
        "v_cvt_pk_bf16_f32 v176, v96, v97\n"
        "v_mfma_f32_16x16x32_bf16 v[100:103], a[28:31], a[216:219], v[100:103]\n"
        "buffer_load_dwordx4 v128, s[20:23], s64 offen lds\n"
        "ds_read_b64_tr_b16 a[102:103], v137 offset:13824\n"
        "v_cvt_pk_bf16_f32 v177, v98, v99\n"
        "v_mfma_f32_16x16x32_bf16 v[100:103], a[32:35], a[220:223], v[100:103]\n"
        "ds_read_b128 v[0:3], v232 offset:0\n"
        "ds_read_b128 a[244:247], v150 offset:12416\n"
        "v_mfma_f32_16x16x32_bf16 v[100:103], a[36:39], a[224:227], v[100:103]\n"
        "ds_read_b64_tr_b16 a[104:105], v137 offset:24576\n"
        "ds_read_b64_tr_b16 a[106:107], v137 offset:26112\n"
        "v_mfma_f32_16x16x32_bf16 v[100:103], a[40:43], a[228:231], v[100:103]\n"
        "ds_read_b64_tr_b16 a[120:121], v138 offset:0\n"
        "ds_read_b64_tr_b16 a[122:123], v138 offset:1536\n"
        "s_waitcnt lgkmcnt(5)\n"
        "v_lshl_add_u32 v0, v0, 2, s67\n"
        "v_mfma_f32_16x16x32_bf16 v[100:103], a[44:47], a[232:235], v[100:103]\n"
        "buffer_load_dwordx4 v129, s[20:23], s64 offen offset:1024 lds\n"
        "ds_read_b64_tr_b16 a[124:125], v138 offset:12288\n"
        "v_lshl_add_u32 v1, v1, 2, s67\n"
        "v_mfma_f32_16x16x32_bf16 v[112:115], a[48:51], a[248:251], 0\n"
        "ds_read_b128 a[248:251], v151 offset:12416\n"
        "ds_read_b128 v[16:19], v232 offset:1024\n"
        "v_lshl_add_u32 v2, v2, 2, s67\n"
        "v_mfma_f32_16x16x32_bf16 v[112:115], a[52:55], a[216:219], v[112:115]\n"
        "ds_read_b128 a[216:219], v150 offset:12544\n"
        "ds_read_b64_tr_b16 a[126:127], v138 offset:13824\n"
        "v_cvt_pk_bf16_f32 v178, v100, v101\n"
        "v_mfma_f32_16x16x32_bf16 v[112:115], a[56:59], a[220:223], v[112:115]\n"
        "ds_read_b128 a[220:223], v151 offset:12544\n"
        "ds_read_b64_tr_b16 a[128:129], v138 offset:24576\n"
        "v_cvt_pk_bf16_f32 v179, v102, v103\n"
        "v_mfma_f32_16x16x32_bf16 v[112:115], a[60:63], a[224:227], v[112:115]\n"
        "buffer_load_dwordx4 v130, s[20:23], s64 offen offset:2048 lds\n"
        "ds_read_b64_tr_b16 a[130:131], v138 offset:26112\n"
        "v_lshl_add_u32 v3, v3, 2, s67\n"
        "v_mfma_f32_16x16x32_bf16 v[112:115], a[64:67], a[228:231], v[112:115]\n"
        "ds_read_b128 v[32:35], v232 offset:8192\n"
        "ds_read_b128 v[48:51], v232 offset:9216\n"
        "s_waitcnt lgkmcnt(7)\n"
        "v_lshl_add_u32 v16, v16, 2, s67\n"
        "v_mfma_f32_16x16x32_bf16 v[112:115], a[68:71], a[232:235], v[112:115]\n"
        "ds_read_b128 a[224:227], v150 offset:13824\n"
        "ds_read_b128 a[228:231], v151 offset:13824\n"
        "v_xor_b32 v253, 32, v249\n"
        "v_mfma_f32_16x16x32_bf16 v[96:99], a[0:3], a[236:239], 0\n"
        "ds_read_b128 a[232:235], v150 offset:13952\n"
        "ds_read_b32 v0, v0\n"
        "v_lshl_add_u32 v17, v17, 2, s67\n"
        "v_mfma_f32_16x16x32_bf16 v[96:99], a[4:7], a[240:243], v[96:99]\n"
        "s_xor_b32 m0, s48, 8192\n"
        "ds_read_b32 v1, v1\n"
        "ds_read_b32 v2, v2\n"
        "v_cvt_pk_bf16_f32 v180, v112, v113\n"
        "v_mfma_f32_16x16x32_bf16 v[96:99], a[8:11], a[244:247], v[96:99]\n"
        "s_sub_u32 s64, s36, 2097152\n"
        "ds_read_b128 v[4:7], v233 offset:0\n"
        "ds_read_b32 v3, v3\n"
        "v_cvt_pk_bf16_f32 v181, v114, v115\n"
        "v_mfma_f32_16x16x32_bf16 v[96:99], a[12:15], a[248:251], v[96:99]\n"
        "s_waitcnt lgkmcnt(14)\n"
        "buffer_load_dwordx4 v249, s[12:15], s64 offen lds\n"
        "ds_read_b32 v16, v16\n"
        "v_cvt_pk_bf16_f32 v186, v112, v113\n"
        "v_mfma_f32_16x16x32_bf16 v[96:99], a[16:19], a[216:219], v[96:99]\n"
        "s_waitcnt lgkmcnt(13)\n"
        "ds_read_b32 v17, v17\n"
        "v_cvt_pk_bf16_f32 v187, v114, v115\n"
        "v_lshl_add_u32 v18, v18, 2, s67\n"
        "v_mfma_f32_16x16x32_bf16 v[96:99], a[20:23], a[220:223], v[96:99]\n"
        "ds_read_b32 v18, v18\n"
        "v_lshl_add_u32 v19, v19, 2, s67\n"
        "s_waitcnt lgkmcnt(12)\n"
        "v_lshl_add_u32 v32, v32, 2, s67\n"
        "v_mfma_f32_16x16x32_bf16 v[100:103], a[24:27], a[236:239], 0\n"
        "s_barrier\n"
        "ds_read_b32 v19, v19\n"
        "ds_read_b32 v32, v32\n"
        "v_lshl_add_u32 v33, v33, 2, s67\n"
        "v_mfma_f32_16x16x32_bf16 v[100:103], a[28:31], a[240:243], v[100:103]\n"
        "buffer_load_dwordx4 v253, s[12:15], s64 offen offset:1024 lds\n"
        "ds_read_b128 v[20:23], v233 offset:1024\n"
        "v_cvt_pk_bf16_f32 v196, v96, v97\n"
        "v_mfma_f32_16x16x32_bf16 v[100:103], a[32:35], a[244:247], v[100:103]\n"
        "ds_read_b32 v33, v33\n"
        "v_cvt_pk_bf16_f32 v197, v98, v99\n"
        "v_lshl_add_u32 v34, v34, 2, s67\n"
        "v_mfma_f32_16x16x32_bf16 v[100:103], a[36:39], a[248:251], v[100:103]\n"
        "ds_read_b32 v34, v34\n"
        "v_lshl_add_u32 v35, v35, 2, s67\n"
        "s_waitcnt lgkmcnt(15)\n"
        "v_lshl_add_u32 v48, v48, 2, s67\n"
        "v_mfma_f32_16x16x32_bf16 v[100:103], a[40:43], a[216:219], v[100:103]\n"
        "s_add_u32 m0, s41, 0\n"
        "ds_read_b32 v35, v35\n"
        "ds_read_b32 v48, v48\n"
        "v_lshl_add_u32 v49, v49, 2, s67\n"
        "v_mfma_f32_16x16x32_bf16 v[100:103], a[44:47], a[220:223], v[100:103]\n"
        "buffer_load_dwordx4 v242, s[8:11], s47 offen lds\n"
        "ds_read_b32 v49, v49\n"
        "v_mfma_f32_16x16x32_bf16 v[112:115], a[48:51], a[236:239], 0\n"
        "ds_read_b128 a[236:239], v151 offset:13952\n"
        "ds_read_b128 v[36:39], v233 offset:8192\n"
        "v_lshl_add_u32 v50, v50, 2, s67\n"
        "v_mfma_f32_16x16x32_bf16 v[112:115], a[52:55], a[240:243], v[112:115]\n"
        "ds_read_b128 a[240:243], v150 offset:14080\n"
        "ds_read_b32 v50, v50\n"
        "v_cvt_pk_bf16_f32 v198, v100, v101\n"
        "v_mfma_f32_16x16x32_bf16 v[112:115], a[56:59], a[244:247], v[112:115]\n"
        "ds_read_b128 a[244:247], v151 offset:14080\n"
        "v_cvt_pk_bf16_f32 v199, v102, v103\n"
        "v_lshl_add_u32 v51, v51, 2, s67\n"
        "v_mfma_f32_16x16x32_bf16 v[112:115], a[60:63], a[248:251], v[112:115]\n"
        "s_add_u32 m0, s41, 1024\n"
        "ds_read_b128 a[248:251], v150 offset:0\n"
        "ds_read_b32 v51, v51\n"
        "s_waitcnt lgkmcnt(15)\n"
        "v_lshl_add_u32 v4, v4, 2, s67\n"
        "v_mfma_f32_16x16x32_bf16 v[112:115], a[64:67], a[216:219], v[112:115]\n"
        "buffer_load_dwordx4 v243, s[8:11], s47 offen lds\n"
        "ds_read_b128 a[216:219], v151 offset:0\n"
        "v_mfma_f32_16x16x32_bf16 v[112:115], a[68:71], a[220:223], v[112:115]\n"
        "ds_read_b128 v[52:55], v233 offset:9216\n"
        "ds_read_b128 a[220:223], v150 offset:128\n"
        "v_lshl_add_u32 v5, v5, 2, s67\n"
        "v_mfma_f32_16x16x32_bf16 v[96:99], a[0:3], a[224:227], 0\n"
        "ds_read_b32 v4, v4\n"
        "ds_read_b32 v5, v5\n"
        "v_lshl_add_u32 v6, v6, 2, s67\n"
        "v_mfma_f32_16x16x32_bf16 v[96:99], a[4:7], a[228:231], v[96:99]\n"
        "ds_read_b32 v6, v6\n"
        "v_cvt_pk_bf16_f32 v200, v112, v113\n"
        "v_cvt_pk_bf16_f32 v201, v114, v115\n"
        "v_mfma_f32_16x16x32_bf16 v[96:99], a[8:11], a[232:235], v[96:99]\n"
        "s_waitcnt lgkmcnt(12)\n"
        "s_add_u32 m0, s41, 2048\n"
        "v_cvt_pk_bf16_f32 v206, v112, v113\n"
        "v_cvt_pk_bf16_f32 v207, v114, v115\n"
        "v_lshl_add_u32 v7, v7, 2, s67\n"
        "v_mfma_f32_16x16x32_bf16 v[96:99], a[12:15], a[236:239], v[96:99]\n"
        "s_waitcnt lgkmcnt(10)\n"
        "buffer_load_dwordx4 v244, s[8:11], s47 offen lds\n"
        "ds_read_b32 v7, v7\n"
        "v_mfma_f32_16x16x32_bf16 v[96:99], a[16:19], a[240:243], v[96:99]\n"
        "s_waitcnt lgkmcnt(9)\n"
        "ds_read_b128 v[8:11], v234 offset:0\n"
        "v_lshl_add_u32 v20, v20, 2, s67\n"
        "v_lshl_add_u32 v21, v21, 2, s67\n"
        "v_mfma_f32_16x16x32_bf16 v[96:99], a[20:23], a[244:247], v[96:99]\n"
        "ds_read_b32 v20, v20\n"
        "ds_read_b32 v21, v21\n"
        "v_lshl_add_u32 v22, v22, 2, s67\n"
        "v_mfma_f32_16x16x32_bf16 v[100:103], a[24:27], a[224:227], 0\n"
        "s_waitcnt vmcnt(5)\n"
        "s_barrier\n"
        "ds_read_b32 v22, v22\n"
        "v_lshl_add_u32 v23, v23, 2, s67\n"
        "v_lshl_add_u32 v36, v36, 2, s67\n"
        "v_mfma_f32_16x16x32_bf16 v[100:103], a[28:31], a[228:231], v[100:103]\n"
        "s_add_u32 m0, s41, 12288\n"
        "ds_read_b64_tr_b16 a[84:85], v131 offset:0\n"
        "ds_read_b64_tr_b16 a[86:87], v131 offset:1536\n"
        "ds_read_b64_tr_b16 a[88:89], v131 offset:12288\n"
        "v_mfma_f32_16x16x32_bf16 v[100:103], a[32:35], a[232:235], v[100:103]\n"
        "ds_read_b64_tr_b16 a[90:91], v131 offset:13824\n"
        "ds_read_b64_tr_b16 a[92:93], v131 offset:24576\n"
        "ds_read_b64_tr_b16 a[94:95], v131 offset:26112\n"
        "s_add_u32 s47, s47, 6291456\n"
        "v_mfma_f32_16x16x32_bf16 v[100:103], a[36:39], a[236:239], v[100:103]\n"
        "ds_read_b64_tr_b16 a[108:109], v132 offset:0\n"
        "ds_read_b64_tr_b16 a[110:111], v132 offset:1536\n"
        "ds_read_b64_tr_b16 a[112:113], v132 offset:12288\n"
        "s_sub_u32 s31, s30, s28\n"
        "v_mfma_f32_16x16x32_bf16 v[100:103], a[40:43], a[240:243], v[100:103]\n"
        "ds_read_b64_tr_b16 a[114:115], v132 offset:13824\n"
        "ds_read_b64_tr_b16 a[116:117], v132 offset:24576\n"
        "ds_read_b64_tr_b16 a[118:119], v132 offset:26112\n"
        "s_sub_u32 s45, s28, s29\n"
        "v_mfma_f32_16x16x32_bf16 v[100:103], a[44:47], a[244:247], v[100:103]\n"
        "ds_read_b64_tr_b16 a[132:133], v133 offset:0\n"
        "ds_read_b64_tr_b16 a[134:135], v133 offset:1536\n"
        "ds_read_b64_tr_b16 a[136:137], v133 offset:12288\n"
        "s_add_u32 s28, s28, s31\n"
        "v_mfma_f32_16x16x32_bf16 v[112:115], a[48:51], a[224:227], 0\n"
        "ds_read_b64_tr_b16 a[138:139], v133 offset:13824\n"
        "ds_read_b64_tr_b16 a[140:141], v133 offset:24576\n"
        "ds_read_b64_tr_b16 a[142:143], v133 offset:26112\n"
        "s_mov_b32 s30, s29\n"
        "v_mfma_f32_16x16x32_bf16 v[112:115], a[52:55], a[228:231], v[112:115]\n"
        "buffer_load_dwordx4 v242, s[8:11], s35 offen lds\n"
        "ds_read_b128 a[224:227], v151 offset:128\n"
        "s_add_u32 m0, s41, 13312\n"
        "v_mfma_f32_16x16x32_bf16 v[112:115], a[56:59], a[232:235], v[112:115]\n"
        "ds_read_b128 a[228:231], v150 offset:256\n"
        "buffer_load_dwordx4 v243, s[8:11], s35 offen lds\n"
        "s_add_u32 s29, s29, s45\n"
        "v_mfma_f32_16x16x32_bf16 v[112:115], a[60:63], a[236:239], v[112:115]\n"
        "ds_read_b128 a[232:235], v151 offset:256\n"
        "ds_read_b128 v[24:27], v234 offset:1024\n"
        "v_cvt_pk_bf16_f32 v216, v96, v97\n"
        "v_mfma_f32_16x16x32_bf16 v[112:115], a[64:67], a[240:243], v[112:115]\n"
        "ds_read_b32 v23, v23\n"
        "ds_read_b128 v[40:43], v234 offset:8192\n"
        "v_cvt_pk_bf16_f32 v217, v98, v99\n"
        "v_mfma_f32_16x16x32_bf16 v[112:115], a[68:71], a[244:247], v[112:115]\n"
        "s_waitcnt lgkmcnt(15)\n"
        "s_add_u32 m0, s41, 14336\n"
        "ds_read_b32 v36, v36\n"
        "ds_read_b128 a[236:239], v150 offset:1536\n"
        "v_cvt_pk_bf16_f32 v218, v100, v101\n"
        "v_mfma_f32_16x16x32_bf16 v[96:99], a[72:75], a[248:251], 0\n"
        "s_waitcnt lgkmcnt(15)\n"
        "buffer_load_dwordx4 v244, s[8:11], s35 offen lds\n"
        "ds_read_b128 a[240:243], v151 offset:1536\n"
        "v_mfma_f32_16x16x32_bf16 v[96:99], a[76:79], a[216:219], v[96:99]\n"
        "s_waitcnt lgkmcnt(15)\n"
        "ds_read_b128 a[244:247], v150 offset:1664\n"
        "ds_read_b128 v[56:59], v234 offset:9216\n"
        "v_cvt_pk_bf16_f32 v219, v102, v103\n"
        "v_mfma_f32_16x16x32_bf16 v[96:99], a[80:83], a[220:223], v[96:99]\n"
        "s_waitcnt lgkmcnt(10)\n"
        "v_cvt_pk_bf16_f32 v220, v112, v113\n"
        "v_cvt_pk_bf16_f32 v221, v114, v115\n"
        "v_cvt_pk_bf16_f32 v226, v112, v113\n"
        "v_mfma_f32_16x16x32_bf16 v[96:99], a[84:87], a[224:227], v[96:99]\n"
        "s_waitcnt lgkmcnt(9)\n"
        "s_add_u32 m0, s40, 36864\n"
        "v_cvt_pk_bf16_f32 v227, v114, v115\n"
        "v_lshl_add_u32 v37, v37, 2, s67\n"
        "v_lshl_add_u32 v38, v38, 2, s67\n"
        "v_mfma_f32_16x16x32_bf16 v[96:99], a[88:91], a[228:231], v[96:99]\n"
        "s_waitcnt lgkmcnt(8)\n"
        "s_add_u32 s64, s65, 768\n"
        "ds_read_b32 v37, v37\n"
        "ds_read_b32 v38, v38\n"
        "v_lshl_add_u32 v39, v39, 2, s67\n"
        "v_mfma_f32_16x16x32_bf16 v[96:99], a[92:95], a[232:235], v[96:99]\n"
        "buffer_load_dwordx4 v128, s[20:23], s64 offen lds\n"
        "ds_read_b32 v39, v39\n"
        "v_lshl_add_u32 v52, v52, 2, s67\n"
        "v_mfma_f32_16x16x32_bf16 v[100:103], a[96:99], a[248:251], 0\n"
        "ds_read_b32 v52, v52\n"
        "ds_read_b128 v[12:15], v235 offset:0\n"
        "v_lshl_add_u32 v53, v53, 2, s67\n"
        "v_mfma_f32_16x16x32_bf16 v[100:103], a[100:103], a[216:219], v[100:103]\n"
        "ds_read_b32 v53, v53\n"
        "v_cvt_pk_bf16_f32 v152, v96, v97\n"
        "v_cvt_pk_bf16_f32 v153, v98, v99\n"
        "v_mfma_f32_16x16x32_bf16 v[100:103], a[104:107], a[220:223], v[100:103]\n"
        "v_lshl_add_u32 v54, v54, 2, s67\n"
        "v_lshl_add_u32 v55, v55, 2, s67\n"
        "v_lshl_add_u32 v8, v8, 2, s67\n"
        "v_mfma_f32_16x16x32_bf16 v[100:103], a[108:111], a[224:227], v[100:103]\n"
        "ds_read_b32 v54, v54\n"
        "ds_read_b32 v55, v55\n"
        "v_lshl_add_u32 v9, v9, 2, s67\n"
        "v_mfma_f32_16x16x32_bf16 v[100:103], a[112:115], a[228:231], v[100:103]\n"
        "buffer_load_dwordx4 v129, s[20:23], s64 offen offset:1024 lds\n"
        "ds_read_b32 v8, v8\n"
        "v_lshl_add_u32 v10, v10, 2, s67\n"
        "v_mfma_f32_16x16x32_bf16 v[100:103], a[116:119], a[232:235], v[100:103]\n"
        "ds_read_b32 v9, v9\n"
        "ds_read_b32 v10, v10\n"
        "v_lshl_add_u32 v11, v11, 2, s67\n"
        "v_mfma_f32_16x16x32_bf16 v[112:115], a[120:123], a[248:251], 0\n"
        "ds_read_b128 a[248:251], v151 offset:1664\n"
        "ds_read_b32 v11, v11\n"
        "s_waitcnt lgkmcnt(15)\n"
        "v_lshl_add_u32 v24, v24, 2, s67\n"
        "v_mfma_f32_16x16x32_bf16 v[112:115], a[124:127], a[216:219], v[112:115]\n"
        "ds_read_b128 a[216:219], v150 offset:1792\n"
        "ds_read_b128 v[28:31], v235 offset:1024\n"
        "v_cvt_pk_bf16_f32 v154, v100, v101\n"
        "v_mfma_f32_16x16x32_bf16 v[112:115], a[128:131], a[220:223], v[112:115]\n"
        "ds_read_b128 a[220:223], v151 offset:1792\n"
        "ds_read_b32 v24, v24\n"
        "v_cvt_pk_bf16_f32 v155, v102, v103\n"
        "v_mfma_f32_16x16x32_bf16 v[112:115], a[132:135], a[224:227], v[112:115]\n"
        "buffer_load_dwordx4 v130, s[20:23], s64 offen offset:2048 lds\n"
        "ds_read_b128 a[224:227], v150 offset:12288\n"
        "v_lshl_add_u32 v25, v25, 2, s67\n"
        "v_mfma_f32_16x16x32_bf16 v[112:115], a[136:139], a[228:231], v[112:115]\n"
        "ds_read_b32 v25, v25\n"
        "ds_read_b128 v[44:47], v235 offset:8192\n"
        "v_lshl_add_u32 v26, v26, 2, s67\n"
        "v_mfma_f32_16x16x32_bf16 v[112:115], a[140:143], a[232:235], v[112:115]\n"
        "ds_read_b32 v26, v26\n"
        "ds_read_b128 a[228:231], v151 offset:12288\n"
        "v_lshl_add_u32 v27, v27, 2, s67\n"
        "v_mfma_f32_16x16x32_bf16 v[96:99], a[72:75], a[236:239], 0\n"
        "s_add_u32 m0, s40, 49152\n"
        "ds_read_b32 v27, v27\n"
        "ds_read_b128 a[232:235], v150 offset:12416\n"
        "v_lshl_add_u32 v40, v40, 2, s67\n"
        "v_mfma_f32_16x16x32_bf16 v[96:99], a[76:79], a[240:243], v[96:99]\n"
        "s_waitcnt lgkmcnt(15)\n"
        "s_add_u32 s64, s65, 37632\n"
        "ds_read_b32 v40, v40\n"
        "v_cvt_pk_bf16_f32 v168, v112, v113\n"
        "v_cvt_pk_bf16_f32 v169, v114, v115\n"
        "v_mfma_f32_16x16x32_bf16 v[96:99], a[80:83], a[244:247], v[96:99]\n"
        "s_waitcnt lgkmcnt(13)\n"
        "buffer_load_dwordx4 v128, s[20:23], s64 offen lds\n"
        "v_lshl_add_u32 v41, v41, 2, s67\n"
        "v_lshl_add_u32 v42, v42, 2, s67\n"
        "v_mfma_f32_16x16x32_bf16 v[96:99], a[84:87], a[248:251], v[96:99]\n"
        "s_waitcnt lgkmcnt(11)\n"
        "ds_read_b32 v41, v41\n"
        "ds_read_b32 v42, v42\n"
        "v_lshl_add_u32 v43, v43, 2, s67\n"
        "v_mfma_f32_16x16x32_bf16 v[96:99], a[88:91], a[216:219], v[96:99]\n"
        "s_waitcnt lgkmcnt(11)\n"
        "ds_read_b32 v43, v43\n"
        "ds_read_b128 v[60:63], v235 offset:9216\n"
        "v_lshl_add_u32 v56, v56, 2, s67\n"
        "v_mfma_f32_16x16x32_bf16 v[96:99], a[92:95], a[220:223], v[96:99]\n"
        "ds_read_b32 v56, v56\n"
        "v_lshl_add_u32 v57, v57, 2, s67\n"
        "v_lshl_add_u32 v58, v58, 2, s67\n"
        "v_mfma_f32_16x16x32_bf16 v[100:103], a[96:99], a[236:239], 0\n"
        "ds_read_b32 v57, v57\n"
        "ds_read_b32 v58, v58\n"
        "v_lshl_add_u32 v59, v59, 2, s67\n"
        "v_mfma_f32_16x16x32_bf16 v[100:103], a[100:103], a[240:243], v[100:103]\n"
        "buffer_load_dwordx4 v129, s[20:23], s64 offen offset:1024 lds\n"
        "ds_read_b32 v59, v59\n"
        "v_cvt_pk_bf16_f32 v172, v96, v97\n"
        "v_mfma_f32_16x16x32_bf16 v[100:103], a[104:107], a[244:247], v[100:103]\n"
        "v_cvt_pk_bf16_f32 v173, v98, v99\n"
        "v_lshl_add_u32 v12, v12, 2, s67\n"
        "v_lshl_add_u32 v13, v13, 2, s67\n"
        "v_mfma_f32_16x16x32_bf16 v[100:103], a[108:111], a[248:251], v[100:103]\n"
        "ds_read_b32 v12, v12\n"
        "ds_read_b32 v13, v13\n"
        "v_lshl_add_u32 v14, v14, 2, s67\n"
        "v_mfma_f32_16x16x32_bf16 v[100:103], a[112:115], a[216:219], v[100:103]\n"
        "ds_read_b32 v14, v14\n"
        "v_lshl_add_u32 v15, v15, 2, s67\n"
        "v_lshl_add_u32 v28, v28, 2, s67\n"
        "v_mfma_f32_16x16x32_bf16 v[100:103], a[116:119], a[220:223], v[100:103]\n"
        "ds_read_b32 v15, v15\n"
        "ds_read_b32 v28, v28\n"
        "v_lshl_add_u32 v29, v29, 2, s67\n"
        "v_mfma_f32_16x16x32_bf16 v[112:115], a[120:123], a[236:239], 0\n"
        "ds_read_b128 a[236:239], v151 offset:12416\n"
        "buffer_load_dwordx4 v130, s[20:23], s64 offen offset:2048 lds\n"
        "v_lshl_add_u32 v30, v30, 2, s67\n"
        "v_mfma_f32_16x16x32_bf16 v[112:115], a[124:127], a[240:243], v[112:115]\n"
        "ds_read_b128 a[240:243], v150 offset:12544\n"
        "ds_read_b32 v29, v29\n"
        "v_cvt_pk_bf16_f32 v174, v100, v101\n"
        "v_mfma_f32_16x16x32_bf16 v[112:115], a[128:131], a[244:247], v[112:115]\n"
        "ds_read_b128 a[244:247], v151 offset:12544\n"
        "ds_read_b32 v30, v30\n"
        "v_cvt_pk_bf16_f32 v175, v102, v103\n"
        "v_mfma_f32_16x16x32_bf16 v[112:115], a[132:135], a[248:251], v[112:115]\n"
        "s_add_u32 m0, s40, 61440\n"
        "ds_read_b128 a[248:251], v150 offset:13824\n"
        "v_lshl_add_u32 v31, v31, 2, s67\n"
        "s_waitcnt lgkmcnt(15)\n"
        "v_lshl_add_u32 v44, v44, 2, s67\n"
        "v_mfma_f32_16x16x32_bf16 v[112:115], a[136:139], a[216:219], v[112:115]\n"
        "s_add_u32 s64, s65, 74496\n"
        "ds_read_b32 v31, v31\n"
        "ds_read_b128 a[216:219], v151 offset:13824\n"
        "v_lshl_add_u32 v45, v45, 2, s67\n"
        "v_mfma_f32_16x16x32_bf16 v[112:115], a[140:143], a[220:223], v[112:115]\n"
        "buffer_load_dwordx4 v128, s[20:23], s64 offen lds\n"
        "ds_read_b128 a[220:223], v150 offset:13952\n"
        "v_lshl_add_u32 v46, v46, 2, s67\n"
        "v_mfma_f32_16x16x32_bf16 v[96:99], a[72:75], a[224:227], 0\n"
        "ds_read_b32 v44, v44\n"
        "ds_read_b32 v45, v45\n"
        "v_lshl_add_u32 v47, v47, 2, s67\n"
        "v_mfma_f32_16x16x32_bf16 v[96:99], a[76:79], a[228:231], v[96:99]\n"
        "ds_read_b32 v46, v46\n"
        "ds_read_b32 v47, v47\n"
        "v_cvt_pk_bf16_f32 v190, v112, v113\n"
        "v_mfma_f32_16x16x32_bf16 v[96:99], a[80:83], a[232:235], v[96:99]\n"
        "s_waitcnt lgkmcnt(12)\n"
        "v_cvt_pk_bf16_f32 v191, v114, v115\n"
        "v_lshl_add_u32 v60, v60, 2, s67\n"
        "v_lshl_add_u32 v61, v61, 2, s67\n"
        "v_mfma_f32_16x16x32_bf16 v[96:99], a[84:87], a[236:239], v[96:99]\n"
        "s_waitcnt lgkmcnt(11)\n"
        "ds_read_b32 v60, v60\n"
        "ds_read_b32 v61, v61\n"
        "v_lshl_add_u32 v62, v62, 2, s67\n"
        "v_mfma_f32_16x16x32_bf16 v[96:99], a[88:91], a[240:243], v[96:99]\n"
        "s_waitcnt lgkmcnt(11)\n"
        "buffer_load_dwordx4 v129, s[20:23], s64 offen offset:1024 lds\n"
        "ds_read_b32 v62, v62\n"
        "v_lshl_add_u32 v63, v63, 2, s67\n"
        "v_mfma_f32_16x16x32_bf16 v[96:99], a[92:95], a[244:247], v[96:99]\n"
        "ds_read_b32 v63, v63\n"
        "v_add_u32 v240, s31, v240\n"
        "v_add_u32 v241, s31, v241\n"
        "v_mfma_f32_16x16x32_bf16 v[100:103], a[96:99], a[224:227], 0\n"
        "v_add_u32 v245, s45, v245\n"
        "v_add_u32 v246, s45, v246\n"
        "v_add_u32 v247, s45, v247\n"
        "v_mfma_f32_16x16x32_bf16 v[100:103], a[100:103], a[228:231], v[100:103]\n"
        "v_cvt_pk_bf16_f32 v192, v96, v97\n"
        "v_cvt_pk_bf16_f32 v193, v98, v99\n"
        "v_mfma_f32_16x16x32_bf16 v[100:103], a[104:107], a[232:235], v[100:103]\n"
        "v_mfma_f32_16x16x32_bf16 v[100:103], a[108:111], a[236:239], v[100:103]\n"
        "buffer_load_dwordx4 v130, s[20:23], s64 offen offset:2048 lds\n"
        "v_mfma_f32_16x16x32_bf16 v[100:103], a[112:115], a[240:243], v[100:103]\n"
        "v_mfma_f32_16x16x32_bf16 v[100:103], a[116:119], a[244:247], v[100:103]\n"
        "v_mfma_f32_16x16x32_bf16 v[112:115], a[120:123], a[224:227], 0\n"
        "ds_read_b128 a[224:227], v151 offset:13952\n"
        "v_mfma_f32_16x16x32_bf16 v[112:115], a[124:127], a[228:231], v[112:115]\n"
        "ds_read_b128 a[228:231], v150 offset:14080\n"
        "v_mfma_f32_16x16x32_bf16 v[112:115], a[128:131], a[232:235], v[112:115]\n"
        "ds_read_b128 a[232:235], v151 offset:14080\n"
        "v_mfma_f32_16x16x32_bf16 v[112:115], a[132:135], a[236:239], v[112:115]\n"
        "v_mfma_f32_16x16x32_bf16 v[112:115], a[136:139], a[240:243], v[112:115]\n"
        "v_cvt_pk_bf16_f32 v194, v100, v101\n"
        "v_cvt_pk_bf16_f32 v195, v102, v103\n"
        "v_mfma_f32_16x16x32_bf16 v[112:115], a[140:143], a[244:247], v[112:115]\n"
        "s_waitcnt lgkmcnt(14)\n"
        "s_waitcnt vmcnt(15)\n"
        "s_barrier\n"
        "ds_read_b128 v[162:165], v251 offset:0\n"
        "ds_read_b128 v[182:185], v251 offset:512\n"
        "v_mfma_f32_16x16x32_bf16 v[96:99], a[72:75], a[248:251], 0\n"
        "s_waitcnt lgkmcnt(14)\n"
        "s_add_u32 m0, s40, 0\n"
        "v_mfma_f32_16x16x32_bf16 v[96:99], a[76:79], a[216:219], v[96:99]\n"
        "s_waitcnt lgkmcnt(13)\n"
        "s_add_u32 s64, s65, 111360\n"
        "v_mfma_f32_16x16x32_bf16 v[96:99], a[80:83], a[220:223], v[96:99]\n"
        "s_waitcnt lgkmcnt(4)\n"
        "buffer_load_dwordx4 v128, s[20:23], s64 offen lds\n"
        "ds_read_b128 v[202:205], v251 offset:4096\n"
        "v_cvt_pk_bf16_f32 v208, v112, v113\n"
        "v_mfma_f32_16x16x32_bf16 v[96:99], a[84:87], a[224:227], v[96:99]\n"
        "s_waitcnt lgkmcnt(4)\n"
        "v_cvt_pk_bf16_f32 v209, v114, v115\n"
        "v_mfma_f32_16x16x32_bf16 v[96:99], a[88:91], a[228:231], v[96:99]\n"
        "s_waitcnt lgkmcnt(3)\n"
        "v_mfma_f32_16x16x32_bf16 v[96:99], a[92:95], a[232:235], v[96:99]\n"
        "v_mfma_f32_16x16x32_bf16 v[100:103], a[96:99], a[248:251], 0\n"
        "ds_read_b128 v[222:225], v251 offset:4608\n"
        "v_mfma_f32_16x16x32_bf16 v[100:103], a[100:103], a[216:219], v[100:103]\n"
        "buffer_load_dwordx4 v129, s[20:23], s64 offen offset:1024 lds\n"
        "v_mfma_f32_16x16x32_bf16 v[100:103], a[104:107], a[220:223], v[100:103]\n"
        "s_waitcnt lgkmcnt(3)\n"
        "v_pk_min_u16 v162, v162, s26\n"
        "v_pk_min_u16 v163, v163, s26\n"
        "v_pk_min_u16 v164, v164, s26\n"
        "v_mfma_f32_16x16x32_bf16 v[100:103], a[108:111], a[224:227], v[100:103]\n"
        "v_cvt_pk_bf16_f32 v212, v96, v97\n"
        "v_cvt_pk_bf16_f32 v213, v98, v99\n"
        "v_pk_min_u16 v165, v165, s26\n"
        "v_mfma_f32_16x16x32_bf16 v[100:103], a[112:115], a[228:231], v[100:103]\n"
        "v_mfma_f32_16x16x32_bf16 v[100:103], a[116:119], a[232:235], v[100:103]\n"
        "v_mfma_f32_16x16x32_bf16 v[112:115], a[120:123], a[248:251], 0\n"
        "buffer_load_dwordx4 v130, s[20:23], s64 offen offset:2048 lds\n"
        "s_waitcnt lgkmcnt(2)\n"
        "v_pk_min_u16 v182, v182, s26\n"
        "v_pk_min_u16 v183, v183, s26\n"
        "v_mfma_f32_16x16x32_bf16 v[112:115], a[124:127], a[216:219], v[112:115]\n"
        "v_pk_min_u16 v184, v184, s26\n"
        "v_pk_min_u16 v185, v185, s26\n"
        "v_mfma_f32_16x16x32_bf16 v[112:115], a[128:131], a[220:223], v[112:115]\n"
        "v_cvt_pk_bf16_f32 v214, v100, v101\n"
        "v_cvt_pk_bf16_f32 v215, v102, v103\n"
        "v_mfma_f32_16x16x32_bf16 v[112:115], a[132:135], a[224:227], v[112:115]\n"
        "s_add_u32 m0, s40, 12288\n"
        "v_mfma_f32_16x16x32_bf16 v[112:115], a[136:139], a[228:231], v[112:115]\n"
        "s_add_u32 s64, s65, 148224\n"
        "s_waitcnt lgkmcnt(1)\n"
        "v_pk_min_u16 v202, v202, s26\n"
        "v_pk_min_u16 v203, v203, s26\n"
        "v_pk_min_u16 v204, v204, s26\n"
        "v_mfma_f32_16x16x32_bf16 v[112:115], a[140:143], a[232:235], v[112:115]\n"
        "buffer_load_dwordx4 v128, s[20:23], s64 offen lds\n"
        "v_pk_min_u16 v205, v205, s26\n"
        "v_mfma_f32_16x16x32_bf16 v[64:67], v[152:155], v[156:159], v[0:3]\n"
        "v_mfma_f32_16x16x32_bf16 v[64:67], v[168:171], v[160:163], v[64:67]\n"
        "v_mfma_f32_16x16x32_bf16 v[68:71], v[172:175], v[156:159], v[4:7]\n"
        "s_waitcnt lgkmcnt(0)\n"
        "v_pk_min_u16 v222, v222, s26\n"
        "v_pk_min_u16 v223, v223, s26\n"
        "v_pk_min_u16 v224, v224, s26\n"
        "v_mfma_f32_16x16x32_bf16 v[68:71], v[188:191], v[164:167], v[68:71]\n"
        "ds_read_b128 v[162:165], v252 offset:0\n"
        "v_cvt_pk_bf16_f32 v230, v112, v113\n"
        "v_cvt_pk_bf16_f32 v231, v114, v115\n"
        "v_mfma_f32_16x16x32_bf16 v[80:83], v[152:155], v[176:179], v[16:19]\n"
        "buffer_load_dwordx4 v129, s[20:23], s64 offen offset:1024 lds\n"
        "v_pk_min_u16 v225, v225, s26\n"
        "v_max3_f32 v232, v64, v65, v66\n"
        "v_mfma_f32_16x16x32_bf16 v[80:83], v[168:171], v[180:183], v[80:83]\n"
        "v_max3_f32 v232, v232, v67, v68\n"
        "v_max3_f32 v232, v232, v69, v70\n"
        "v_mfma_f32_16x16x32_bf16 v[84:87], v[172:175], v[176:179], v[20:23]\n"
        "v_mfma_f32_16x16x32_bf16 v[84:87], v[188:191], v[184:187], v[84:87]\n"
        "ds_read_b128 v[182:185], v252 offset:512\n"
        "v_mfma_f32_16x16x32_bf16 v[96:99], v[152:155], v[196:199], v[32:35]\n"
        "v_mfma_f32_16x16x32_bf16 v[96:99], v[168:171], v[200:203], v[96:99]\n"
        "buffer_load_dwordx4 v130, s[20:23], s64 offen offset:2048 lds\n"
        "v_max3_f32 v233, v80, v81, v82\n"
        "v_mfma_f32_16x16x32_bf16 v[100:103], v[172:175], v[196:199], v[36:39]\n"
        "v_mfma_f32_16x16x32_bf16 v[100:103], v[188:191], v[204:207], v[100:103]\n"
        "ds_read_b128 v[202:205], v252 offset:4096\n"
        "s_waitcnt vmcnt(15)\n"
        "s_barrier\n"
        "v_max3_f32 v233, v233, v83, v84\n"
        "v_max3_f32 v233, v233, v85, v86\n"
        "v_mfma_f32_16x16x32_bf16 v[112:115], v[152:155], v[216:219], v[48:51]\n"
        "s_add_u32 m0, s40, 24576\n"
        "ds_read_b128 a[236:239], v240 offset:0\n"
        "s_waitcnt lgkmcnt(3)\n"
        "v_pk_min_u16 v162, v162, s26\n"
        "v_pk_min_u16 v163, v163, s26\n"
        "v_mfma_f32_16x16x32_bf16 v[112:115], v[168:171], v[220:223], v[112:115]\n"
        "s_add_u32 s64, s65, 185088\n"
        "ds_read_b128 a[240:243], v241 offset:0\n"
        "v_pk_min_u16 v164, v164, s26\n"
        "v_pk_min_u16 v165, v165, s26\n"
        "v_mfma_f32_16x16x32_bf16 v[116:119], v[172:175], v[216:219], v[52:55]\n"
        "buffer_load_dwordx4 v128, s[20:23], s64 offen lds\n"
        "ds_read_b128 a[244:247], v240 offset:128\n"
        "v_max3_f32 v234, v96, v97, v98\n"
        "v_mfma_f32_16x16x32_bf16 v[116:119], v[188:191], v[224:227], v[116:119]\n"
        "ds_read_b128 v[222:225], v252 offset:4608\n"
        "ds_read_b128 a[248:251], v241 offset:128\n"
        "v_max3_f32 v234, v234, v99, v100\n"
        "v_mfma_f32_16x16x32_bf16 v[72:75], v[192:195], v[156:159], v[8:11]\n"
        "ds_read_b128 a[216:219], v240 offset:256\n"
        "s_waitcnt lgkmcnt(7)\n"
        "v_pk_min_u16 v182, v182, s26\n"
        "v_pk_min_u16 v183, v183, s26\n"
        "v_mfma_f32_16x16x32_bf16 v[72:75], v[208:211], v[160:163], v[72:75]\n"
        "ds_read_b128 a[220:223], v241 offset:256\n"
        "v_pk_min_u16 v184, v184, s26\n"
        "v_pk_min_u16 v185, v185, s26\n"
        "v_mfma_f32_16x16x32_bf16 v[76:79], v[212:215], v[156:159], v[12:15]\n"
        "v_max3_f32 v234, v234, v101, v102\n"
        "v_max3_f32 v235, v112, v113, v114\n"
        "v_max3_f32 v235, v235, v115, v116\n"
        "v_mfma_f32_16x16x32_bf16 v[76:79], v[228:231], v[164:167], v[76:79]\n"
        "buffer_load_dwordx4 v129, s[20:23], s64 offen offset:1024 lds\n"
        "v_max3_f32 v232, v232, v71, v72\n"
        "v_max3_f32 v232, v232, v73, v74\n"
        "v_mfma_f32_16x16x32_bf16 v[88:91], v[192:195], v[176:179], v[24:27]\n"
        "s_waitcnt lgkmcnt(7)\n"
        "v_pk_min_u16 v202, v202, s26\n"
        "v_pk_min_u16 v203, v203, s26\n"
        "v_pk_min_u16 v204, v204, s26\n"
        "v_mfma_f32_16x16x32_bf16 v[88:91], v[208:211], v[180:183], v[88:91]\n"
        "v_pk_min_u16 v205, v205, s26\n"
        "v_max3_f32 v232, v232, v75, v76\n"
        "v_max3_f32 v232, v232, v77, v78\n"
        "v_mfma_f32_16x16x32_bf16 v[92:95], v[212:215], v[176:179], v[28:31]\n"
        "v_max_f32 v232, v232, v79\n"
        "v_max3_f32 v235, v235, v117, v118\n"
        "v_xor_b32 v251, 0x2000, v251\n"
        "v_mfma_f32_16x16x32_bf16 v[92:95], v[228:231], v[184:187], v[92:95]\n"
        "v_max3_f32 v233, v233, v87, v88\n"
        "v_max3_f32 v233, v233, v89, v90\n"
        "v_xor_b32 v252, 0x2000, v252\n"
        "v_mfma_f32_16x16x32_bf16 v[104:107], v[192:195], v[196:199], v[40:43]\n"
        "buffer_load_dwordx4 v130, s[20:23], s64 offen offset:2048 lds\n"
        "s_waitcnt lgkmcnt(3)\n"
        "v_pk_min_u16 v222, v222, s26\n"
        "v_pk_min_u16 v223, v223, s26\n"
        "v_mfma_f32_16x16x32_bf16 v[104:107], v[208:211], v[200:203], v[104:107]\n"
        "v_pk_min_u16 v224, v224, s26\n"
        "v_pk_min_u16 v225, v225, s26\n"
        "v_max3_f32 v233, v233, v91, v92\n"
        "v_mfma_f32_16x16x32_bf16 v[108:111], v[212:215], v[196:199], v[44:47]\n"
        "s_waitcnt vmcnt(9)\n"
        "s_barrier\n"
        "v_max3_f32 v233, v233, v93, v94\n"
        "v_max_f32 v233, v233, v95\n"
        "v_mfma_f32_16x16x32_bf16 v[108:111], v[228:231], v[204:207], v[108:111]\n"
        "ds_read_b64_tr_b16 a[144:145], v131 offset:36864\n"
        "ds_read_b64_tr_b16 a[146:147], v131 offset:38400\n"
        "v_max3_f32 v234, v234, v103, v104\n"
        "v_mfma_f32_16x16x32_bf16 v[120:123], v[192:195], v[216:219], v[56:59]\n"
        "s_mov_b32 m0, s48\n"
        "s_xor_b32 s48, s48, 8192\n"
        "ds_read_b64_tr_b16 a[148:149], v131 offset:49152\n"
        "ds_read_b64_tr_b16 a[150:151], v131 offset:50688\n"
        "v_mfma_f32_16x16x32_bf16 v[120:123], v[208:211], v[220:223], v[120:123]\n"
        "buffer_load_dwordx4 v249, s[12:15], s36 offen offset:0 lds\n"
        "ds_read_b64_tr_b16 a[152:153], v131 offset:61440\n"
        "v_mfma_f32_16x16x32_bf16 v[124:127], v[212:215], v[216:219], v[60:63]\n"
        "ds_read_b64_tr_b16 a[154:155], v131 offset:62976\n"
        "ds_read_b64_tr_b16 a[168:169], v132 offset:36864\n"
        "v_xor_b32 v253, 32, v249\n"
        "v_mfma_f32_16x16x32_bf16 v[124:127], v[228:231], v[224:227], v[124:127]\n"
        "ds_read_b64_tr_b16 a[170:171], v132 offset:38400\n"
        "ds_read_b64_tr_b16 a[172:173], v132 offset:49152\n"
        "v_max3_f32 v234, v234, v105, v106\n"
        "v_mfma_f32_16x16x32_bf16 v[212:215], a[72:75], a[236:239], 0\n"
        "ds_read_b64_tr_b16 a[174:175], v132 offset:50688\n"
        "ds_read_b64_tr_b16 a[176:177], v132 offset:61440\n"
        "v_max3_f32 v234, v234, v107, v108\n"
        "v_mfma_f32_16x16x32_bf16 v[212:215], a[76:79], a[240:243], v[212:215]\n"
        "ds_read_b64_tr_b16 a[178:179], v132 offset:62976\n"
        "ds_read_b64_tr_b16 a[192:193], v133 offset:36864\n"
        "v_max3_f32 v234, v234, v109, v110\n"
        "v_mfma_f32_16x16x32_bf16 v[212:215], a[80:83], a[244:247], v[212:215]\n"
        "s_waitcnt lgkmcnt(15)\n"
        "buffer_load_dwordx4 v253, s[12:15], s36 offen offset:1024 lds\n"
        "s_add_u32 s36, s36, 2097152\n"
        "ds_read_b64_tr_b16 a[194:195], v133 offset:38400\n"
        "v_mfma_f32_16x16x32_bf16 v[212:215], a[84:87], a[248:251], v[212:215]\n"
        "s_waitcnt lgkmcnt(15)\n"
        "ds_read_b64_tr_b16 a[196:197], v133 offset:49152\n"
        "ds_read_b64_tr_b16 a[198:199], v133 offset:50688\n"
        "v_max_f32 v234, v234, v111\n"
        "v_mfma_f32_16x16x32_bf16 v[212:215], a[88:91], a[216:219], v[212:215]\n"
        "s_waitcnt lgkmcnt(15)\n"
        "ds_read_b64_tr_b16 a[200:201], v133 offset:61440\n"
        "ds_read_b64_tr_b16 a[202:203], v133 offset:62976\n"
        "v_max3_f32 v235, v235, v119, v120\n"
        "v_mfma_f32_16x16x32_bf16 v[212:215], a[92:95], a[220:223], v[212:215]\n"
        "v_permlane32_swap_b32 v232, v233\n"
        "v_max_f32 v232, v232, v233\n"
        "v_mfma_f32_16x16x32_bf16 v[216:219], a[96:99], a[236:239], 0\n"
        "v_mov_b32 v233, v232\n"
        "v_max3_f32 v235, v235, v121, v122\n"
        "v_max3_f32 v235, v235, v123, v124\n"
        "v_mfma_f32_16x16x32_bf16 v[216:219], a[100:103], a[240:243], v[216:219]\n"
        "v_permlane16_swap_b32 v232, v233\n"
        "v_max_f32 v232, v232, v233\n"
        "v_mfma_f32_16x16x32_bf16 v[216:219], a[104:107], a[244:247], v[216:219]\n"
        "v_mul_f32 v236, s25, v232\n"
        "v_mul_f32 v237, s25, v232\n"
        "v_max3_f32 v235, v235, v125, v126\n"
        "v_mfma_f32_16x16x32_bf16 v[216:219], a[108:111], a[248:251], v[216:219]\n"
        "v_permlane32_swap_b32 v236, v237\n"
        "v_max_f32 v235, v235, v127\n"
        "v_mfma_f32_16x16x32_bf16 v[216:219], a[112:115], a[216:219], v[216:219]\n"
        "v_cvt_pk_bf16_f32 v152, v212, v213\n"
        "v_permlane32_swap_b32 v234, v235\n"
        "v_mfma_f32_16x16x32_bf16 v[216:219], a[116:119], a[220:223], v[216:219]\n"
        "v_max_f32 v234, v234, v235\n"
        "v_mov_b32 v235, v234\n"
        "v_cvt_pk_bf16_f32 v153, v214, v215\n"
        "v_mfma_f32_16x16x32_bf16 v[162:165], a[120:123], a[236:239], 0\n"
        "v_permlane16_swap_b32 v234, v235\n"
        "v_max_f32 v234, v234, v235\n"
        "v_mfma_f32_16x16x32_bf16 v[162:165], a[124:127], a[240:243], v[162:165]\n"
        "v_mul_f32 v238, s25, v234\n"
        "v_mul_f32 v239, s25, v234\n"
        "v_cvt_pk_bf16_f32 v154, v216, v217\n"
        "v_mfma_f32_16x16x32_bf16 v[162:165], a[128:131], a[244:247], v[162:165]\n"
        "ds_read_b128 a[224:227], v240 offset:1536\n"
        "v_permlane32_swap_b32 v238, v239\n"
        "v_mfma_f32_16x16x32_bf16 v[162:165], a[132:135], a[248:251], v[162:165]\n"
        "ds_read_b128 a[228:231], v241 offset:1536\n"
        "v_cvt_pk_bf16_f32 v155, v218, v219\n"
        "v_fma_f32 v64, v64, s24, v236\n"
        "v_mfma_f32_16x16x32_bf16 v[162:165], a[136:139], a[216:219], v[162:165]\n"
        "ds_read_b128 a[232:235], v240 offset:1664\n"
        "v_exp_f32 v64, v64\n"
        "v_mfma_f32_16x16x32_bf16 v[162:165], a[140:143], a[220:223], v[162:165]\n"
        "v_fma_f32 v65, v65, s24, v236\n"
        "v_exp_f32 v65, v65\n"
        "v_mfma_f32_16x16x32_bf16 v[212:215], a[0:3], a[236:239], 0\n"
        "v_cvt_pk_bf16_f32 v64, v64, v65\n"
        "v_fma_f32 v66, v66, s24, v236\n"
        "v_fma_f32 v67, v67, s24, v236\n"
        "v_mfma_f32_16x16x32_bf16 v[212:215], a[4:7], a[240:243], v[212:215]\n"
        "v_exp_f32 v66, v66\n"
        "v_cvt_pk_bf16_f32 v168, v162, v163\n"
        "v_mfma_f32_16x16x32_bf16 v[212:215], a[8:11], a[244:247], v[212:215]\n"
        "v_exp_f32 v67, v67\n"
        "v_cvt_pk_bf16_f32 v169, v164, v165\n"
        "v_mfma_f32_16x16x32_bf16 v[212:215], a[12:15], a[248:251], v[212:215]\n"
        "v_cvt_pk_bf16_f32 v65, v66, v67\n"
        "v_fma_f32 v68, v68, s24, v236\n"
        "v_fma_f32 v69, v69, s24, v236\n"
        "v_mfma_f32_16x16x32_bf16 v[212:215], a[16:19], a[216:219], v[212:215]\n"
        "v_exp_f32 v68, v68\n"
        "v_fma_f32 v70, v70, s24, v236\n"
        "v_mfma_f32_16x16x32_bf16 v[212:215], a[20:23], a[220:223], v[212:215]\n"
        "v_exp_f32 v69, v69\n"
        "v_fma_f32 v71, v71, s24, v236\n"
        "v_mfma_f32_16x16x32_bf16 v[216:219], a[24:27], a[236:239], 0\n"
        "s_waitcnt lgkmcnt(3)\n"
        "s_waitcnt vmcnt(2)\n"
        "s_barrier\n"
        "v_exp_f32 v70, v70\n"
        "v_cvt_pk_bf16_f32 v66, v68, v69\n"
        "v_mfma_f32_16x16x32_bf16 v[216:219], a[28:31], a[240:243], v[216:219]\n"
        "ds_read_b64_tr_b16 a[156:157], v131 offset:0\n"
        "s_add_u32 m0, s65, 36864\n"
        "ds_read_b64_tr_b16 a[158:159], v131 offset:1536\n"
        "v_cvt_pk_bf16_f32 v156, v212, v213\n"
        "v_mfma_f32_16x16x32_bf16 v[216:219], a[32:35], a[244:247], v[216:219]\n"
        "s_add_u32 s64, s65, 36864\n"
        "ds_read_b64_tr_b16 a[160:161], v131 offset:12288\n"
        "ds_read_b64_tr_b16 a[162:163], v131 offset:13824\n"
        "v_cvt_pk_bf16_f32 v157, v214, v215\n"
        "v_mfma_f32_16x16x32_bf16 v[216:219], a[36:39], a[248:251], v[216:219]\n"
        "buffer_load_dwordx4 v142, s[52:55], s64 offen lds\n"
        "ds_read_b64_tr_b16 a[164:165], v131 offset:24576\n"
        "v_fma_f32 v72, v72, s24, v236\n"
        "v_mfma_f32_16x16x32_bf16 v[216:219], a[40:43], a[216:219], v[216:219]\n"
        "ds_read_b64_tr_b16 a[166:167], v131 offset:26112\n"
        "ds_read_b64_tr_b16 a[180:181], v132 offset:0\n"
        "v_fma_f32 v73, v73, s24, v236\n"
        "v_mfma_f32_16x16x32_bf16 v[216:219], a[44:47], a[220:223], v[216:219]\n"
        "ds_read_b64_tr_b16 a[182:183], v132 offset:1536\n"
        "ds_read_b64_tr_b16 a[184:185], v132 offset:12288\n"
        "v_fma_f32 v74, v74, s24, v236\n"
        "v_mfma_f32_16x16x32_bf16 v[162:165], a[48:51], a[236:239], 0\n"
        "ds_read_b128 a[236:239], v241 offset:1664\n"
        "ds_read_b64_tr_b16 a[186:187], v132 offset:13824\n"
        "v_fma_f32 v75, v75, s24, v236\n"
        "v_mfma_f32_16x16x32_bf16 v[162:165], a[52:55], a[240:243], v[162:165]\n"
        "ds_read_b128 a[240:243], v240 offset:1792\n"
        "ds_read_b64_tr_b16 a[188:189], v132 offset:24576\n"
        "v_cvt_pk_bf16_f32 v158, v216, v217\n"
        "v_mfma_f32_16x16x32_bf16 v[162:165], a[56:59], a[244:247], v[162:165]\n"
        "ds_read_b128 a[244:247], v241 offset:1792\n"
        "buffer_load_dwordx4 v142, s[52:55], s64 offen offset:1024 lds\n"
        "v_cvt_pk_bf16_f32 v159, v218, v219\n"
        "v_mfma_f32_16x16x32_bf16 v[162:165], a[60:63], a[248:251], v[162:165]\n"
        "ds_read_b64_tr_b16 a[190:191], v132 offset:26112\n"
        "ds_read_b64_tr_b16 a[204:205], v133 offset:0\n"
        "v_fma_f32 v76, v76, s24, v236\n"
        "v_mfma_f32_16x16x32_bf16 v[162:165], a[64:67], a[216:219], v[162:165]\n"
        "ds_read_b64_tr_b16 a[206:207], v133 offset:1536\n"
        "ds_read_b64_tr_b16 a[208:209], v133 offset:12288\n"
        "v_fma_f32 v77, v77, s24, v236\n"
        "v_mfma_f32_16x16x32_bf16 v[162:165], a[68:71], a[220:223], v[162:165]\n"
        "s_waitcnt lgkmcnt(15)\n"
        "ds_read_b64_tr_b16 a[210:211], v133 offset:13824\n"
        "ds_read_b64_tr_b16 a[212:213], v133 offset:24576\n"
        "v_fma_f32 v78, v78, s24, v236\n"
        "v_mfma_f32_16x16x32_bf16 v[212:215], a[72:75], a[224:227], 0\n"
        "s_waitcnt lgkmcnt(15)\n"
        "ds_read_b64_tr_b16 a[214:215], v133 offset:26112\n"
        "v_exp_f32 v71, v71\n"
        "v_mfma_f32_16x16x32_bf16 v[212:215], a[76:79], a[228:231], v[212:215]\n"
        "s_waitcnt lgkmcnt(15)\n"
        "buffer_load_dwordx4 v142, s[52:55], s64 offen offset:2048 lds\n"
        "v_exp_f32 v72, v72\n"
        "v_mfma_f32_16x16x32_bf16 v[212:215], a[80:83], a[232:235], v[212:215]\n"
        "s_waitcnt lgkmcnt(11)\n"
        "v_exp_f32 v73, v73\n"
        "v_cvt_pk_bf16_f32 v160, v162, v163\n"
        "v_mfma_f32_16x16x32_bf16 v[212:215], a[84:87], a[236:239], v[212:215]\n"
        "s_waitcnt lgkmcnt(9)\n"
        "v_exp_f32 v74, v74\n"
        "v_cvt_pk_bf16_f32 v161, v164, v165\n"
        "v_mfma_f32_16x16x32_bf16 v[212:215], a[88:91], a[240:243], v[212:215]\n"
        "s_waitcnt lgkmcnt(7)\n"
        "s_add_u32 m0, s65, 39936\n"
        "v_cvt_pk_bf16_f32 v166, v162, v163\n"
        "v_cvt_pk_bf16_f32 v167, v164, v165\n"
        "v_cvt_pk_bf16_f32 v67, v70, v71\n"
        "v_mfma_f32_16x16x32_bf16 v[212:215], a[92:95], a[244:247], v[212:215]\n"
        "s_add_u32 s64, s65, 39936\n"
        "v_exp_f32 v75, v75\n"
        "v_cvt_pk_bf16_f32 v68, v72, v73\n"
        "v_mfma_f32_16x16x32_bf16 v[216:219], a[96:99], a[224:227], 0\n"
        "buffer_load_dwordx4 v142, s[52:55], s64 offen lds\n"
        "v_exp_f32 v76, v76\n"
        "v_mfma_f32_16x16x32_bf16 v[216:219], a[100:103], a[228:231], v[216:219]\n"
        "v_exp_f32 v77, v77\n"
        "v_cvt_pk_bf16_f32 v172, v212, v213\n"
        "v_mfma_f32_16x16x32_bf16 v[216:219], a[104:107], a[232:235], v[216:219]\n"
        "v_exp_f32 v78, v78\n"
        "v_cvt_pk_bf16_f32 v173, v214, v215\n"
        "v_mfma_f32_16x16x32_bf16 v[216:219], a[108:111], a[236:239], v[216:219]\n"
        "v_cvt_pk_bf16_f32 v69, v74, v75\n"
        "v_cvt_pk_bf16_f32 v70, v76, v77\n"
        "v_fma_f32 v79, v79, s24, v236\n"
        "v_mfma_f32_16x16x32_bf16 v[216:219], a[112:115], a[240:243], v[216:219]\n"
        "s_waitcnt lgkmcnt(0)\n"
        "s_barrier\n"
        "v_exp_f32 v79, v79\n"
        "v_fma_f32 v80, v80, s24, v237\n"
        "v_mfma_f32_16x16x32_bf16 v[216:219], a[116:119], a[244:247], v[216:219]\n"
        "buffer_load_dwordx4 v142, s[52:55], s64 offen offset:1024 lds\n"
        "v_exp_f32 v80, v80\n"
        "v_mfma_f32_16x16x32_bf16 v[162:165], a[120:123], a[224:227], 0\n"
        "v_cvt_pk_bf16_f32 v71, v78, v79\n"
        "v_fma_f32 v81, v81, s24, v237\n"
        "v_fma_f32 v82, v82, s24, v237\n"
        "v_mfma_f32_16x16x32_bf16 v[162:165], a[124:127], a[228:231], v[162:165]\n"
        "v_exp_f32 v81, v81\n"
        "v_cvt_pk_bf16_f32 v174, v216, v217\n"
        "v_mfma_f32_16x16x32_bf16 v[162:165], a[128:131], a[232:235], v[162:165]\n"
        "ds_read_b128 a[248:251], v240 offset:12288\n"
        "v_exp_f32 v82, v82\n"
        "v_mfma_f32_16x16x32_bf16 v[162:165], a[132:135], a[236:239], v[162:165]\n"
        "ds_read_b128 a[216:219], v241 offset:12288\n"
        "v_cvt_pk_bf16_f32 v175, v218, v219\n"
        "v_cvt_pk_bf16_f32 v80, v80, v81\n"
        "v_mfma_f32_16x16x32_bf16 v[162:165], a[136:139], a[240:243], v[162:165]\n"
        "buffer_load_dwordx4 v142, s[52:55], s64 offen offset:2048 lds\n"
        "ds_read_b128 a[220:223], v240 offset:12416\n"
        "v_fma_f32 v83, v83, s24, v237\n"
        "v_mfma_f32_16x16x32_bf16 v[162:165], a[140:143], a[244:247], v[162:165]\n"
        "v_exp_f32 v83, v83\n"
        "v_fma_f32 v84, v84, s24, v237\n"
        "v_mfma_f32_16x16x32_bf16 v[212:215], a[0:3], a[224:227], 0\n"
        "v_exp_f32 v84, v84\n"
        "v_cvt_pk_bf16_f32 v81, v82, v83\n"
        "v_mfma_f32_16x16x32_bf16 v[212:215], a[4:7], a[228:231], v[212:215]\n"
        "s_add_u32 m0, s65, 43008\n"
        "v_fma_f32 v85, v85, s24, v237\n"
        "v_exp_f32 v85, v85\n"
        "v_mfma_f32_16x16x32_bf16 v[212:215], a[8:11], a[232:235], v[212:215]\n"
        "s_add_u32 s64, s65, 43008\n"
        "v_cvt_pk_bf16_f32 v190, v162, v163\n"
        "v_cvt_pk_bf16_f32 v191, v164, v165\n"
        "v_cvt_pk_bf16_f32 v82, v84, v85\n"
        "v_mfma_f32_16x16x32_bf16 v[212:215], a[12:15], a[236:239], v[212:215]\n"
        "buffer_load_dwordx4 v142, s[52:55], s64 offen lds\n"
        "v_fma_f32 v86, v86, s24, v237\n"
        "v_fma_f32 v87, v87, s24, v237\n"
        "v_mfma_f32_16x16x32_bf16 v[212:215], a[16:19], a[240:243], v[212:215]\n"
        "v_exp_f32 v86, v86\n"
        "v_fma_f32 v88, v88, s24, v237\n"
        "v_mfma_f32_16x16x32_bf16 v[212:215], a[20:23], a[244:247], v[212:215]\n"
        "v_exp_f32 v87, v87\n"
        "v_fma_f32 v89, v89, s24, v237\n"
        "v_mfma_f32_16x16x32_bf16 v[216:219], a[24:27], a[224:227], 0\n"
        "v_exp_f32 v88, v88\n"
        "v_cvt_pk_bf16_f32 v83, v86, v87\n"
        "v_mfma_f32_16x16x32_bf16 v[216:219], a[28:31], a[228:231], v[216:219]\n"
        "v_exp_f32 v89, v89\n"
        "v_fma_f32 v90, v90, s24, v237\n"
        "v_mfma_f32_16x16x32_bf16 v[216:219], a[32:35], a[232:235], v[216:219]\n"
        "buffer_load_dwordx4 v142, s[52:55], s64 offen offset:1024 lds\n"
        "v_exp_f32 v90, v90\n"
        "v_mfma_f32_16x16x32_bf16 v[216:219], a[36:39], a[236:239], v[216:219]\n"
        "v_cvt_pk_bf16_f32 v176, v212, v213\n"
        "v_cvt_pk_bf16_f32 v177, v214, v215\n"
        "v_cvt_pk_bf16_f32 v84, v88, v89\n"
        "v_mfma_f32_16x16x32_bf16 v[216:219], a[40:43], a[240:243], v[216:219]\n"
        "v_fma_f32 v91, v91, s24, v237\n"
        "v_exp_f32 v91, v91\n"
        "v_mfma_f32_16x16x32_bf16 v[216:219], a[44:47], a[244:247], v[216:219]\n"
        "v_cvt_pk_bf16_f32 v85, v90, v91\n"
        "v_fma_f32 v92, v92, s24, v237\n"
        "v_fma_f32 v93, v93, s24, v237\n"
        "v_mfma_f32_16x16x32_bf16 v[162:165], a[48:51], a[224:227], 0\n"
        "ds_read_b128 a[224:227], v241 offset:12416\n"
        "v_exp_f32 v92, v92\n"
        "v_mfma_f32_16x16x32_bf16 v[162:165], a[52:55], a[228:231], v[162:165]\n"
        "ds_read_b128 a[228:231], v240 offset:12544\n"
        "buffer_load_dwordx4 v142, s[52:55], s64 offen offset:2048 lds\n"
        "v_cvt_pk_bf16_f32 v178, v216, v217\n"
        "v_mfma_f32_16x16x32_bf16 v[162:165], a[56:59], a[232:235], v[162:165]\n"
        "ds_read_b128 a[232:235], v241 offset:12544\n"
        "v_exp_f32 v93, v93\n"
        "v_mfma_f32_16x16x32_bf16 v[162:165], a[60:63], a[236:239], v[162:165]\n"
        "v_cvt_pk_bf16_f32 v179, v218, v219\n"
        "v_cvt_pk_bf16_f32 v86, v92, v93\n"
        "v_fma_f32 v94, v94, s24, v237\n"
        "v_mfma_f32_16x16x32_bf16 v[162:165], a[64:67], a[240:243], v[162:165]\n"
        "s_add_u32 m0, s65, 0\n"
        "v_exp_f32 v94, v94\n"
        "v_fma_f32 v95, v95, s24, v237\n"
        "v_mfma_f32_16x16x32_bf16 v[162:165], a[68:71], a[244:247], v[162:165]\n"
        "s_waitcnt lgkmcnt(5)\n"
        "s_add_u32 s64, s65, 0\n"
        "v_exp_f32 v95, v95\n"
        "v_fma_f32 v96, v96, s24, v238\n"
        "v_mfma_f32_16x16x32_bf16 v[72:75], a[72:75], a[248:251], 0\n"
        "s_waitcnt lgkmcnt(4)\n"
        "buffer_load_dwordx4 v142, s[52:55], s64 offen lds\n"
        "v_exp_f32 v96, v96\n"
        "v_mfma_f32_16x16x32_bf16 v[72:75], a[76:79], a[216:219], v[72:75]\n"
        "s_waitcnt lgkmcnt(3)\n"
        "v_cvt_pk_bf16_f32 v87, v94, v95\n"
        "v_cvt_pk_bf16_f32 v180, v162, v163\n"
        "v_cvt_pk_bf16_f32 v181, v164, v165\n"
        "v_mfma_f32_16x16x32_bf16 v[72:75], a[80:83], a[220:223], v[72:75]\n"
        "s_waitcnt lgkmcnt(2)\n"
        "v_cvt_pk_bf16_f32 v186, v162, v163\n"
        "v_cvt_pk_bf16_f32 v187, v164, v165\n"
        "v_fma_f32 v97, v97, s24, v238\n"
        "v_mfma_f32_16x16x32_bf16 v[72:75], a[84:87], a[224:227], v[72:75]\n"
        "s_waitcnt lgkmcnt(1)\n"
        "v_exp_f32 v97, v97\n"
        "v_fma_f32 v98, v98, s24, v238\n"
        "v_mfma_f32_16x16x32_bf16 v[72:75], a[88:91], a[228:231], v[72:75]\n"
        "s_waitcnt lgkmcnt(0)\n"
        "v_exp_f32 v98, v98\n"
        "v_accvgpr_read_b32 v232, a252\n"
        "v_mfma_f32_16x16x32_bf16 v[72:75], a[92:95], a[232:235], v[72:75]\n"
        "buffer_load_dwordx4 v142, s[52:55], s64 offen offset:1024 lds\n"
        "v_accvgpr_read_b32 v233, a253\n"
        "v_accvgpr_read_b32 v234, a254\n"
        "v_mfma_f32_16x16x32_bf16 v[76:79], a[96:99], a[248:251], 0\n"
        "v_mov_b32 v235, 0x3f803f80\n"
        "v_accvgpr_write_b32 a252, v235\n"
        "v_accvgpr_write_b32 a253, v235\n"
        "v_mfma_f32_16x16x32_bf16 v[76:79], a[100:103], a[216:219], v[76:79]\n"
        "v_cvt_pk_bf16_f32 v192, v72, v73\n"
        "v_cvt_pk_bf16_f32 v193, v74, v75\n"
        "v_accvgpr_write_b32 a254, v235\n"
        "v_mfma_f32_16x16x32_bf16 v[76:79], a[104:107], a[220:223], v[76:79]\n"
        "v_accvgpr_write_b32 a255, v235\n"
        "v_cvt_pk_bf16_f32 v96, v96, v97\n"
        "v_fma_f32 v99, v99, s24, v238\n"
        "v_mfma_f32_16x16x32_bf16 v[76:79], a[108:111], a[224:227], v[76:79]\n"
        "v_exp_f32 v99, v99\n"
        "v_fma_f32 v100, v100, s24, v238\n"
        "v_mfma_f32_16x16x32_bf16 v[76:79], a[112:115], a[228:231], v[76:79]\n"
        "buffer_load_dwordx4 v142, s[52:55], s64 offen offset:2048 lds\n"
        "v_exp_f32 v100, v100\n"
        "v_mfma_f32_16x16x32_bf16 v[76:79], a[116:119], a[232:235], v[76:79]\n"
        "v_cvt_pk_bf16_f32 v97, v98, v99\n"
        "v_fma_f32 v101, v101, s24, v238\n"
        "v_fma_f32 v102, v102, s24, v238\n"
        "v_mfma_f32_16x16x32_bf16 v[162:165], a[120:123], a[248:251], 0\n"
        "ds_read_b128 a[236:239], v150 offset:0\n"
        "v_exp_f32 v101, v101\n"
        "v_mfma_f32_16x16x32_bf16 v[162:165], a[124:127], a[216:219], v[162:165]\n"
        "s_add_u32 m0, s65, 3072\n"
        "ds_read_b128 a[240:243], v151 offset:0\n"
        "v_exp_f32 v102, v102\n"
        "v_mfma_f32_16x16x32_bf16 v[162:165], a[128:131], a[220:223], v[162:165]\n"
        "s_add_u32 s64, s65, 3072\n"
        "s_waitcnt vmcnt(3)\n"
        "s_barrier\n"
        "ds_read_b64_tr_b16 v[212:213], v232 offset:36864\n"
        "ds_read_b128 a[244:247], v150 offset:128\n"
        "v_mfma_f32_16x16x32_bf16 v[162:165], a[132:135], a[224:227], v[162:165]\n"
        "buffer_load_dwordx4 v142, s[52:55], s64 offen lds\n"
        "v_cvt_pk_bf16_f32 v194, v76, v77\n"
        "v_cvt_pk_bf16_f32 v195, v78, v79\n"
        "v_mfma_f32_16x16x32_bf16 v[162:165], a[136:139], a[228:231], v[162:165]\n"
        "ds_read_b64_tr_b16 v[214:215], v232 offset:43008\n"
        "v_cvt_pk_bf16_f32 v98, v100, v101\n"
        "v_fma_f32 v103, v103, s24, v238\n"
        "v_mfma_f32_16x16x32_bf16 v[162:165], a[140:143], a[232:235], v[162:165]\n"
        "v_exp_f32 v103, v103\n"
        "v_fma_f32 v104, v104, s24, v238\n"
        "v_mfma_f32_16x16x32_bf16 v[72:75], a[0:3], a[248:251], 0\n"
        "ds_read_b64_tr_b16 v[216:217], v232 offset:55296\n"
        "v_exp_f32 v104, v104\n"
        "v_mfma_f32_16x16x32_bf16 v[72:75], a[4:7], a[216:219], v[72:75]\n"
        "v_cvt_pk_bf16_f32 v99, v102, v103\n"
        "v_fma_f32 v105, v105, s24, v238\n"
        "v_cvt_pk_bf16_f32 v208, v162, v163\n"
        "v_mfma_f32_16x16x32_bf16 v[72:75], a[8:11], a[220:223], v[72:75]\n"
        "buffer_load_dwordx4 v142, s[52:55], s64 offen offset:1024 lds\n"
        "ds_read_b64_tr_b16 v[218:219], v232 offset:61440\n"
        "v_cvt_pk_bf16_f32 v209, v164, v165\n"
        "v_mfma_f32_16x16x32_bf16 v[72:75], a[12:15], a[224:227], v[72:75]\n"
        "v_exp_f32 v105, v105\n"
        "v_fma_f32 v106, v106, s24, v238\n"
        "v_mfma_f32_16x16x32_bf16 v[72:75], a[16:19], a[228:231], v[72:75]\n"
        "ds_read_b64_tr_b16 v[220:221], v233 offset:49152\n"
        "v_exp_f32 v106, v106\n"
        "v_mfma_f32_16x16x32_bf16 v[72:75], a[20:23], a[232:235], v[72:75]\n"
        "v_cvt_pk_bf16_f32 v100, v104, v105\n"
        "v_fma_f32 v107, v107, s24, v238\n"
        "v_fma_f32 v108, v108, s24, v238\n"
        "v_mfma_f32_16x16x32_bf16 v[76:79], a[24:27], a[248:251], 0\n"
        "ds_read_b64_tr_b16 v[222:223], v233 offset:50688\n"
        "v_exp_f32 v107, v107\n"
        "v_mfma_f32_16x16x32_bf16 v[76:79], a[28:31], a[216:219], v[76:79]\n"
        "buffer_load_dwordx4 v142, s[52:55], s64 offen offset:2048 lds\n"
        "v_exp_f32 v108, v108\n"
        "v_mfma_f32_16x16x32_bf16 v[76:79], a[32:35], a[220:223], v[76:79]\n"
        "ds_read_b64_tr_b16 v[224:225], v232 offset:36896\n"
        "v_cvt_pk_bf16_f32 v196, v72, v73\n"
        "v_cvt_pk_bf16_f32 v197, v74, v75\n"
        "v_mfma_f32_16x16x32_bf16 v[76:79], a[36:39], a[224:227], v[76:79]\n"
        "v_cvt_pk_bf16_f32 v101, v106, v107\n"
        "v_fma_f32 v109, v109, s24, v238\n"
        "v_fma_f32 v110, v110, s24, v238\n"
        "v_mfma_f32_16x16x32_bf16 v[76:79], a[40:43], a[228:231], v[76:79]\n"
        "s_add_u32 m0, s65, 6144\n"
        "ds_read_b64_tr_b16 v[226:227], v232 offset:43040\n"
        "v_exp_f32 v109, v109\n"
        "v_mfma_f32_16x16x32_bf16 v[76:79], a[44:47], a[232:235], v[76:79]\n"
        "s_add_u32 s64, s65, 6144\n"
        "v_exp_f32 v110, v110\n"
        "v_cvt_pk_bf16_f32 v102, v108, v109\n"
        "v_mfma_f32_16x16x32_bf16 v[162:165], a[48:51], a[248:251], 0\n"
        "ds_read_b128 a[248:251], v151 offset:128\n"
        "buffer_load_dwordx4 v142, s[52:55], s64 offen lds\n"
        "v_fma_f32 v111, v111, s24, v238\n"
        "v_mfma_f32_16x16x32_bf16 v[162:165], a[52:55], a[216:219], v[162:165]\n"
        "ds_read_b128 a[216:219], v150 offset:256\n"
        "ds_read_b64_tr_b16 v[182:183], v232 offset:55328\n"
        "v_cvt_pk_bf16_f32 v198, v76, v77\n"
        "v_mfma_f32_16x16x32_bf16 v[162:165], a[56:59], a[220:223], v[162:165]\n"
        "ds_read_b128 a[220:223], v151 offset:256\n"
        "ds_read_b64_tr_b16 v[184:185], v232 offset:61472\n"
        "s_sub_u32 s31, s30, s28\n"
        "v_cvt_pk_bf16_f32 v199, v78, v79\n"
        "v_mfma_f32_16x16x32_bf16 v[162:165], a[60:63], a[224:227], v[162:165]\n"
        "s_sub_u32 s45, s28, s29\n"
        "v_exp_f32 v111, v111\n"
        "v_fma_f32 v112, v112, s24, v239\n"
        "v_mfma_f32_16x16x32_bf16 v[162:165], a[64:67], a[228:231], v[162:165]\n"
        "ds_read_b64_tr_b16 v[202:203], v233 offset:49184\n"
        "ds_read_b128 a[228:231], v150 offset:1536\n"
        "s_add_u32 s28, s28, s31\n"
        "v_cvt_pk_bf16_f32 v103, v110, v111\n"
        "v_mfma_f32_16x16x32_bf16 v[162:165], a[68:71], a[232:235], v[162:165]\n"
        "s_waitcnt lgkmcnt(15)\n"
        "buffer_load_dwordx4 v142, s[52:55], s64 offen offset:1024 lds\n"
        "ds_read_b128 a[232:235], v151 offset:1536\n"
        "v_fma_f32 v113, v113, s24, v239\n"
        "v_mfma_f32_16x16x32_bf16 v[72:75], a[236:239], a[144:147], 0\n"
        "s_waitcnt lgkmcnt(15)\n"
        "ds_read_b64_tr_b16 v[204:205], v233 offset:50720\n"
        "v_exp_f32 v112, v112\n"
        "v_mfma_f32_16x16x32_bf16 v[72:75], a[240:243], a[148:151], v[72:75]\n"
        "s_waitcnt lgkmcnt(15)\n"
        "v_exp_f32 v113, v113\n"
        "v_cvt_pk_bf16_f32 v200, v162, v163\n"
        "v_mfma_f32_16x16x32_bf16 v[72:75], a[244:247], a[152:155], v[72:75]\n"
        "s_waitcnt lgkmcnt(8)\n"
        "ds_read_b64_tr_b16 v[88:89], v232 offset:36928\n"
        "v_cvt_pk_bf16_f32 v201, v164, v165\n"
        "v_cvt_pk_bf16_f32 v206, v162, v163\n"
        "v_mfma_f32_16x16x32_bf16 v[72:75], a[248:251], a[156:159], v[72:75]\n"
        "s_waitcnt lgkmcnt(8)\n"
        "v_cvt_pk_bf16_f32 v207, v164, v165\n"
        "v_cvt_pk_bf16_f32 v112, v112, v113\n"
        "v_fma_f32 v114, v114, s24, v239\n"
        "v_mfma_f32_16x16x32_bf16 v[72:75], a[216:219], a[160:163], v[72:75]\n"
        "s_waitcnt lgkmcnt(6)\n"
        "buffer_load_dwordx4 v142, s[52:55], s64 offen offset:2048 lds\n"
        "s_add_u32 s41, s30, s40\n"
        "s_add_u32 s35, s47, 786432\n"
        "s_sub_u32 s46, s35, 100663296\n"
        "s_cmp_lt_u32 s46, 786432\n"
        "v_mfma_f32_16x16x32_bf16 v[72:75], a[220:223], a[164:167], v[72:75]\n"
        "s_cselect_b32 s35, s46, s35\n"
        "ds_read_b64_tr_b16 v[90:91], v232 offset:43072\n"
        "v_exp_f32 v114, v114\n"
        "v_mfma_f32_16x16x32_bf16 v[76:79], a[236:239], a[168:171], 0\n"
        "ds_read_b64_tr_b16 v[92:93], v232 offset:55360\n"
        "v_fma_f32 v115, v115, s24, v239\n"
        "v_fma_f32 v116, v116, s24, v239\n"
        "v_mfma_f32_16x16x32_bf16 v[76:79], a[240:243], a[172:175], v[76:79]\n"
        "v_exp_f32 v115, v115\n"
        "v_cvt_pk_bf16_f32 v128, v72, v73\n"
        "v_mfma_f32_16x16x32_bf16 v[76:79], a[244:247], a[176:179], v[76:79]\n"
        "ds_read_b64_tr_b16 v[94:95], v232 offset:61504\n"
        "v_exp_f32 v116, v116\n"
        "v_mfma_f32_16x16x32_bf16 v[76:79], a[248:251], a[180:183], v[76:79]\n"
        "s_mov_b32 m0, s48\n"
        "v_cvt_pk_bf16_f32 v129, v74, v75\n"
        "v_cvt_pk_bf16_f32 v113, v114, v115\n"
        "v_fma_f32 v117, v117, s24, v239\n"
        "v_mfma_f32_16x16x32_bf16 v[76:79], a[216:219], a[184:187], v[76:79]\n"
        "buffer_load_dwordx4 v249, s[12:15], s36 offen offset:0 lds\n"
        "ds_read_b64_tr_b16 v[104:105], v233 offset:49216\n"
        "v_mfma_f32_16x16x32_bf16 v[76:79], a[220:223], a[188:191], v[76:79]\n"
        "v_exp_f32 v117, v117\n"
        "v_fma_f32 v118, v118, s24, v239\n"
        "v_mfma_f32_16x16x32_bf16 v[162:165], a[236:239], a[192:195], 0\n"
        "ds_read_b128 a[236:239], v150 offset:1664\n"
        "ds_read_b64_tr_b16 v[106:107], v233 offset:50752\n"
        "v_cvt_pk_bf16_f32 v114, v116, v117\n"
        "v_mfma_f32_16x16x32_bf16 v[162:165], a[240:243], a[196:199], v[162:165]\n"
        "ds_read_b128 a[240:243], v151 offset:1664\n"
        "ds_read_b128 a[224:227], v151 offset:12288\n"
        "v_xor_b32 v253, 32, v249\n"
        "v_mfma_f32_16x16x32_bf16 v[162:165], a[244:247], a[200:203], v[162:165]\n"
        "ds_read_b128 a[244:247], v150 offset:1792\n"
        "v_exp_f32 v118, v118\n"
        "v_mfma_f32_16x16x32_bf16 v[162:165], a[248:251], a[204:207], v[162:165]\n"
        "ds_read_b128 a[248:251], v151 offset:1792\n"
        "buffer_load_dwordx4 v253, s[12:15], s36 offen offset:1024 lds\n"
        "s_mov_b32 s30, s29\n"
        "v_mfma_f32_16x16x32_bf16 v[162:165], a[216:219], a[208:211], v[162:165]\n"
        "s_waitcnt lgkmcnt(4)\n"
        "s_barrier\n"
        "ds_write_b128 v234, v[212:215] offset:36864\n"
        "v_mfma_f32_16x16x32_bf16 v[162:165], a[220:223], a[212:215], v[162:165]\n"
        "s_add_u32 s29, s29, s45\n"
        "ds_read_b128 a[220:223], v150 offset:12288\n"
        "v_cvt_pk_bf16_f32 v136, v76, v77\n"
        "v_mfma_f32_16x16x32_bf16 v[72:75], a[228:231], a[144:147], 0\n"
        "v_cvt_pk_bf16_f32 v137, v78, v79\n"
        "v_fma_f32 v119, v119, s24, v239\n"
        "v_mfma_f32_16x16x32_bf16 v[72:75], a[232:235], a[148:151], v[72:75]\n"
        "s_add_u32 m0, s41, 0\n"
        "v_exp_f32 v119, v119\n"
        "v_mfma_f32_16x16x32_bf16 v[72:75], a[236:239], a[152:155], v[72:75]\n"
        "s_waitcnt lgkmcnt(5)\n"
        "buffer_load_dwordx4 v242, s[8:11], s47 offen lds\n"
        "v_mfma_f32_16x16x32_bf16 v[72:75], a[240:243], a[156:159], v[72:75]\n"
        "s_waitcnt lgkmcnt(3)\n"
        "ds_write_b128 v234, v[216:219] offset:43008\n"
        "v_mfma_f32_16x16x32_bf16 v[72:75], a[244:247], a[160:163], v[72:75]\n"
        "s_waitcnt lgkmcnt(3)\n"
        "v_cvt_pk_bf16_f32 v144, v162, v163\n"
        "v_cvt_pk_bf16_f32 v145, v164, v165\n"
        "v_mfma_f32_16x16x32_bf16 v[72:75], a[248:251], a[164:167], v[72:75]\n"
        "v_cvt_pk_bf16_f32 v115, v118, v119\n"
        "v_fma_f32 v120, v120, s24, v239\n"
        "v_mfma_f32_16x16x32_bf16 v[76:79], a[228:231], a[168:171], 0\n"
        "ds_write_b128 v234, v[220:223] offset:49152\n"
        "v_mfma_f32_16x16x32_bf16 v[76:79], a[232:235], a[172:175], v[76:79]\n"
        "v_exp_f32 v120, v120\n"
        "v_mfma_f32_16x16x32_bf16 v[76:79], a[236:239], a[176:179], v[76:79]\n"
        "v_fma_f32 v121, v121, s24, v239\n"
        "v_cvt_pk_bf16_f32 v130, v72, v73\n"
        "v_mfma_f32_16x16x32_bf16 v[76:79], a[240:243], a[180:183], v[76:79]\n"
        "ds_write_b128 v234, v[224:227] offset:37120\n"
        "v_mfma_f32_16x16x32_bf16 v[76:79], a[244:247], a[184:187], v[76:79]\n"
        "v_cvt_pk_bf16_f32 v131, v74, v75\n"
        "v_fma_f32 v122, v122, s24, v239\n"
        "v_mfma_f32_16x16x32_bf16 v[76:79], a[248:251], a[188:191], v[76:79]\n"
        "v_exp_f32 v121, v121\n"
        "v_mfma_f32_16x16x32_bf16 v[162:165], a[228:231], a[192:195], 0\n"
        "ds_read_b128 a[228:231], v150 offset:12416\n"
        "v_cvt_pk_bf16_f32 v116, v120, v121\n"
        "v_mfma_f32_16x16x32_bf16 v[162:165], a[232:235], a[196:199], v[162:165]\n"
        "ds_read_b128 a[232:235], v151 offset:12416\n"
        "ds_read_b128 a[216:219], v151 offset:13824\n"
        "v_mfma_f32_16x16x32_bf16 v[162:165], a[236:239], a[200:203], v[162:165]\n"
        "ds_write_b128 v234, v[182:185] offset:43264\n"
        "v_mfma_f32_16x16x32_bf16 v[162:165], a[240:243], a[204:207], v[162:165]\n"
        "ds_read_b128 a[236:239], v150 offset:12544\n"
        "ds_read_b128 a[240:243], v151 offset:12544\n"
        "v_mfma_f32_16x16x32_bf16 v[162:165], a[244:247], a[208:211], v[162:165]\n"
        "v_cvt_pk_bf16_f32 v138, v76, v77\n"
        "v_cvt_pk_bf16_f32 v139, v78, v79\n"
        "v_mfma_f32_16x16x32_bf16 v[162:165], a[248:251], a[212:215], v[162:165]\n"
        "s_waitcnt lgkmcnt(9)\n"
        "ds_write_b128 v234, v[202:205] offset:49408\n"
        "v_mfma_f32_16x16x32_bf16 v[72:75], a[220:223], a[144:147], 0\n"
        "ds_read_b128 a[248:251], v150 offset:13824\n"
        "v_fma_f32 v123, v123, s24, v239\n"
        "v_mfma_f32_16x16x32_bf16 v[72:75], a[224:227], a[148:151], v[72:75]\n"
        "s_waitcnt lgkmcnt(7)\n"
        "v_exp_f32 v122, v122\n"
        "v_mfma_f32_16x16x32_bf16 v[72:75], a[228:231], a[152:155], v[72:75]\n"
        "s_waitcnt lgkmcnt(6)\n"
        "ds_write_b128 v234, v[88:91] offset:37376\n"
        "v_mfma_f32_16x16x32_bf16 v[72:75], a[232:235], a[156:159], v[72:75]\n"
        "s_waitcnt lgkmcnt(4)\n"
        "v_exp_f32 v123, v123\n"
        "v_mfma_f32_16x16x32_bf16 v[72:75], a[236:239], a[160:163], v[72:75]\n"
        "s_waitcnt lgkmcnt(3)\n"
        "v_cvt_pk_bf16_f32 v146, v162, v163\n"
        "v_cvt_pk_bf16_f32 v147, v164, v165\n"
        "v_mfma_f32_16x16x32_bf16 v[72:75], a[240:243], a[164:167], v[72:75]\n"
        "ds_write_b128 v234, v[92:95] offset:43520\n"
        "v_mfma_f32_16x16x32_bf16 v[76:79], a[220:223], a[168:171], 0\n"
        "v_cvt_pk_bf16_f32 v117, v122, v123\n"
        "v_fma_f32 v124, v124, s24, v239\n"
        "v_mfma_f32_16x16x32_bf16 v[76:79], a[224:227], a[172:175], v[76:79]\n"
        "v_fma_f32 v125, v125, s24, v239\n"
        "v_fma_f32 v126, v126, s24, v239\n"
        "v_mfma_f32_16x16x32_bf16 v[76:79], a[228:231], a[176:179], v[76:79]\n"
        "ds_write_b128 v234, v[104:107] offset:49664\n"
        "s_waitcnt vmcnt(3)\n"
        "s_barrier\n"
        "v_mfma_f32_16x16x32_bf16 v[76:79], a[232:235], a[180:183], v[76:79]\n"
        "ds_read_b64_tr_b16 v[108:109], v232 offset:0\n"
        "v_fma_f32 v127, v127, s24, v239\n"
        "v_mfma_f32_16x16x32_bf16 v[76:79], a[236:239], a[184:187], v[76:79]\n"
        "ds_read_b64_tr_b16 v[110:111], v232 offset:6144\n"
        "v_cvt_pk_bf16_f32 v132, v72, v73\n"
        "v_mfma_f32_16x16x32_bf16 v[76:79], a[240:243], a[188:191], v[76:79]\n"
        "v_exp_f32 v124, v124\n"
        "v_mfma_f32_16x16x32_bf16 v[162:165], a[220:223], a[192:195], 0\n"
        "ds_read_b128 a[220:223], v150 offset:13952\n"
        "ds_read_b64_tr_b16 v[120:121], v232 offset:18432\n"
        "v_mfma_f32_16x16x32_bf16 v[162:165], a[224:227], a[196:199], v[162:165]\n"
        "ds_read_b128 a[224:227], v151 offset:13952\n"
        "v_cvt_pk_bf16_f32 v133, v74, v75\n"
        "v_mfma_f32_16x16x32_bf16 v[162:165], a[228:231], a[200:203], v[162:165]\n"
        "ds_read_b128 a[228:231], v150 offset:14080\n"
        "ds_read_b64_tr_b16 v[122:123], v232 offset:24576\n"
        "v_mfma_f32_16x16x32_bf16 v[162:165], a[232:235], a[204:207], v[162:165]\n"
        "ds_read_b128 a[232:235], v151 offset:14080\n"
        "v_cvt_pk_bf16_f32 v140, v76, v77\n"
        "v_mfma_f32_16x16x32_bf16 v[162:165], a[236:239], a[208:211], v[162:165]\n"
        "ds_read_b128 a[236:239], v240 offset:13824\n"
        "ds_read_b128 a[244:247], v240 offset:13952\n"
        "v_mfma_f32_16x16x32_bf16 v[162:165], a[240:243], a[212:215], v[162:165]\n"
        "s_waitcnt lgkmcnt(13)\n"
        "ds_read_b128 a[240:243], v241 offset:13824\n"
        "v_cvt_pk_bf16_f32 v141, v78, v79\n"
        "v_mfma_f32_16x16x32_bf16 v[72:75], a[248:251], a[144:147], 0\n"
        "v_exp_f32 v125, v125\n"
        "v_mfma_f32_16x16x32_bf16 v[72:75], a[216:219], a[148:151], v[72:75]\n"
        "s_waitcnt lgkmcnt(8)\n"
        "v_cvt_pk_bf16_f32 v118, v124, v125\n"
        "v_mfma_f32_16x16x32_bf16 v[72:75], a[220:223], a[152:155], v[72:75]\n"
        "s_waitcnt lgkmcnt(6)\n"
        "ds_read_b64_tr_b16 v[124:125], v233 offset:12288\n"
        "ds_read_b64_tr_b16 v[222:223], v232 offset:32\n"
        "v_mfma_f32_16x16x32_bf16 v[72:75], a[224:227], a[156:159], v[72:75]\n"
        "s_waitcnt lgkmcnt(7)\n"
        "v_exp_f32 v126, v126\n"
        "v_mfma_f32_16x16x32_bf16 v[72:75], a[228:231], a[160:163], v[72:75]\n"
        "s_waitcnt lgkmcnt(5)\n"
        "ds_read_b64_tr_b16 v[224:225], v232 offset:6176\n"
        "v_cvt_pk_bf16_f32 v148, v162, v163\n"
        "v_mfma_f32_16x16x32_bf16 v[72:75], a[232:235], a[164:167], v[72:75]\n"
        "v_exp_f32 v127, v127\n"
        "v_mfma_f32_16x16x32_bf16 v[76:79], a[248:251], a[168:171], 0\n"
        "ds_read_b64_tr_b16 v[182:183], v232 offset:18464\n"
        "v_cvt_pk_bf16_f32 v119, v126, v127\n"
        "v_mfma_f32_16x16x32_bf16 v[76:79], a[216:219], a[172:175], v[76:79]\n"
        "ds_read_b64_tr_b16 v[126:127], v233 offset:13824\n"
        "v_cvt_pk_bf16_f32 v149, v164, v165\n"
        "v_mfma_f32_16x16x32_bf16 v[76:79], a[220:223], a[176:179], v[76:79]\n"
        "ds_read_b64_tr_b16 v[184:185], v232 offset:24608\n"
        "v_cvt_pk_bf16_f32 v134, v72, v73\n"
        "v_mfma_f32_16x16x32_bf16 v[76:79], a[224:227], a[180:183], v[76:79]\n"
        "v_cvt_pk_bf16_f32 v135, v74, v75\n"
        "v_mfma_f32_16x16x32_bf16 v[76:79], a[228:231], a[184:187], v[76:79]\n"
        "ds_read_b64_tr_b16 v[202:203], v233 offset:12320\n"
        "v_mfma_f32_16x16x32_bf16 v[76:79], a[232:235], a[188:191], v[76:79]\n"
        "v_mfma_f32_16x16x32_bf16 v[162:165], a[248:251], a[192:195], 0\n"
        "ds_read_b128 a[248:251], v241 offset:13952\n"
        "ds_read_b64_tr_b16 v[204:205], v233 offset:13856\n"
        "v_mfma_f32_16x16x32_bf16 v[162:165], a[216:219], a[196:199], v[162:165]\n"
        "ds_read_b128 a[216:219], v240 offset:14080\n"
        "v_mfma_f32_16x16x32_bf16 v[162:165], a[220:223], a[200:203], v[162:165]\n"
        "ds_read_b128 a[220:223], v241 offset:14080\n"
        "ds_read_b64_tr_b16 v[88:89], v232 offset:64\n"
        "v_mfma_f32_16x16x32_bf16 v[162:165], a[224:227], a[204:207], v[162:165]\n"
        "v_cvt_pk_bf16_f32 v142, v76, v77\n"
        "v_cvt_pk_bf16_f32 v143, v78, v79\n"
        "v_mfma_f32_16x16x32_bf16 v[162:165], a[228:231], a[208:211], v[162:165]\n"
        "ds_read_b64_tr_b16 v[90:91], v232 offset:6208\n"
        "v_mfma_f32_16x16x32_bf16 v[162:165], a[232:235], a[212:215], v[162:165]\n"
        "s_waitcnt lgkmcnt(15)\n"
        "v_mfma_f32_16x16x32_bf16 v[72:75], a[72:75], a[236:239], 0\n"
        "s_waitcnt lgkmcnt(13)\n"
        "ds_read_b64_tr_b16 v[92:93], v232 offset:18496\n"
        "v_mfma_f32_16x16x32_bf16 v[72:75], a[76:79], a[240:243], v[72:75]\n"
        "v_mfma_f32_16x16x32_bf16 v[72:75], a[80:83], a[244:247], v[72:75]\n"
        "s_waitcnt lgkmcnt(6)\n"
        "ds_read_b64_tr_b16 v[94:95], v232 offset:24640\n"
        "v_mfma_f32_16x16x32_bf16 v[72:75], a[84:87], a[248:251], v[72:75]\n"
        "s_waitcnt lgkmcnt(5)\n"
        "v_mfma_f32_16x16x32_bf16 v[72:75], a[88:91], a[216:219], v[72:75]\n"
        "s_waitcnt lgkmcnt(4)\n"
        "ds_read_b64_tr_b16 v[104:105], v233 offset:12352\n"
        "v_cvt_pk_bf16_f32 v150, v162, v163\n"
        "v_mfma_f32_16x16x32_bf16 v[72:75], a[92:95], a[220:223], v[72:75]\n"
        "v_cvt_pk_bf16_f32 v151, v164, v165\n"
        "v_mfma_f32_16x16x32_bf16 v[76:79], a[96:99], a[236:239], 0\n"
        "s_add_u32 m0, s41, 1024\n"
        "ds_read_b64_tr_b16 v[106:107], v233 offset:13888\n"
        "v_mfma_f32_16x16x32_bf16 v[76:79], a[100:103], a[240:243], v[76:79]\n"
        "v_mfma_f32_16x16x32_bf16 v[76:79], a[104:107], a[244:247], v[76:79]\n"
        "buffer_load_dwordx4 v243, s[8:11], s47 offen lds\n"
        "s_waitcnt lgkmcnt(0)\n"
        "s_barrier\n"
        "v_mfma_f32_16x16x32_bf16 v[76:79], a[108:111], a[248:251], v[76:79]\n"
        "ds_write_b128 v234, v[108:111] offset:0\n"
        "v_mfma_f32_16x16x32_bf16 v[76:79], a[112:115], a[216:219], v[76:79]\n"
        "v_cvt_pk_bf16_f32 v212, v72, v73\n"
        "v_cvt_pk_bf16_f32 v213, v74, v75\n"
        "v_mfma_f32_16x16x32_bf16 v[76:79], a[116:119], a[220:223], v[76:79]\n"
        "v_mfma_f32_16x16x32_bf16 v[162:165], a[120:123], a[236:239], 0\n"
        "ds_write_b128 v234, v[120:123] offset:6144\n"
        "v_mfma_f32_16x16x32_bf16 v[162:165], a[124:127], a[240:243], v[162:165]\n"
        "v_mfma_f32_16x16x32_bf16 v[162:165], a[128:131], a[244:247], v[162:165]\n"
        "v_mfma_f32_16x16x32_bf16 v[162:165], a[132:135], a[248:251], v[162:165]\n"
        "ds_write_b128 v234, v[124:127] offset:12288\n"
        "v_mfma_f32_16x16x32_bf16 v[162:165], a[136:139], a[216:219], v[162:165]\n"
        "v_mfma_f32_16x16x32_bf16 v[162:165], a[140:143], a[220:223], v[162:165]\n"
        "v_cvt_pk_bf16_f32 v214, v76, v77\n"
        "v_cvt_pk_bf16_f32 v215, v78, v79\n"
        "v_mfma_f32_16x16x32_bf16 v[72:75], a[0:3], a[236:239], 0\n"
        "ds_write_b128 v234, v[222:225] offset:256\n"
        "v_mfma_f32_16x16x32_bf16 v[72:75], a[4:7], a[240:243], v[72:75]\n"
        "v_mfma_f32_16x16x32_bf16 v[72:75], a[8:11], a[244:247], v[72:75]\n"
        "s_add_u32 m0, s41, 2048\n"
        "v_mfma_f32_16x16x32_bf16 v[72:75], a[12:15], a[248:251], v[72:75]\n"
        "ds_write_b128 v234, v[182:185] offset:6400\n"
        "v_mfma_f32_16x16x32_bf16 v[72:75], a[16:19], a[216:219], v[72:75]\n"
        "buffer_load_dwordx4 v244, s[8:11], s47 offen lds\n"
        "v_mfma_f32_16x16x32_bf16 v[72:75], a[20:23], a[220:223], v[72:75]\n"
        "v_cvt_pk_bf16_f32 v230, v162, v163\n"
        "v_cvt_pk_bf16_f32 v231, v164, v165\n"
        "v_mfma_f32_16x16x32_bf16 v[76:79], a[24:27], a[236:239], 0\n"
        "ds_write_b128 v234, v[202:205] offset:12544\n"
        "v_mfma_f32_16x16x32_bf16 v[76:79], a[28:31], a[240:243], v[76:79]\n"
        "v_mfma_f32_16x16x32_bf16 v[76:79], a[32:35], a[244:247], v[76:79]\n"
        "v_mfma_f32_16x16x32_bf16 v[76:79], a[36:39], a[248:251], v[76:79]\n"
        "ds_write_b128 v234, v[88:91] offset:512\n"
        "v_mfma_f32_16x16x32_bf16 v[76:79], a[40:43], a[216:219], v[76:79]\n"
        "v_cvt_pk_bf16_f32 v216, v72, v73\n"
        "v_cvt_pk_bf16_f32 v217, v74, v75\n"
        "v_mfma_f32_16x16x32_bf16 v[76:79], a[44:47], a[220:223], v[76:79]\n"
        "v_mfma_f32_16x16x32_bf16 v[162:165], a[48:51], a[236:239], 0\n"
        "ds_write_b128 v234, v[92:95] offset:6656\n"
        "v_mfma_f32_16x16x32_bf16 v[162:165], a[52:55], a[240:243], v[162:165]\n"
        "v_mfma_f32_16x16x32_bf16 v[162:165], a[56:59], a[244:247], v[162:165]\n"
        "v_mfma_f32_16x16x32_bf16 v[162:165], a[60:63], a[248:251], v[162:165]\n"
        "ds_write_b128 v234, v[104:107] offset:12800\n"
        "v_mfma_f32_16x16x32_bf16 v[162:165], a[64:67], a[216:219], v[162:165]\n"
        "s_add_u32 m0, s41, 12288\n"
        "v_cvt_pk_bf16_f32 v218, v76, v77\n"
        "v_cvt_pk_bf16_f32 v219, v78, v79\n"
        "v_mfma_f32_16x16x32_bf16 v[162:165], a[68:71], a[220:223], v[162:165]\n"
        "v_mfma_f32_16x16x32_bf16 v[88:91], a[252:255], v[64:67], 0\n"
        "buffer_load_dwordx4 v242, s[8:11], s35 offen lds\n"
        "v_mfma_f32_16x16x32_bf16 v[88:91], a[252:255], v[68:71], v[88:91]\n"
        "v_mfma_f32_16x16x32_bf16 v[92:95], v[132:135], v[68:71], 0\n"
        "v_mfma_f32_16x16x32_bf16 v[92:95], v[128:131], v[64:67], v[92:95]\n"
        "v_mfma_f32_16x16x32_bf16 v[182:185], v[136:139], v[64:67], 0\n"
        "v_mfma_f32_16x16x32_bf16 v[182:185], v[140:143], v[68:71], v[182:185]\n"
        "v_mfma_f32_16x16x32_bf16 v[202:205], v[148:151], v[68:71], 0\n"
        "s_cmp_eq_u32 s37, 100663296\n"
        "v_cvt_pk_bf16_f32 v220, v162, v163\n"
        "v_cvt_pk_bf16_f32 v221, v164, v165\n"
        "v_mfma_f32_16x16x32_bf16 v[202:205], v[144:147], v[64:67], v[202:205]\n"
        "s_cselect_b32 s46, 0, s37\n"
        "v_cvt_pk_bf16_f32 v226, v162, v163\n"
        "v_cvt_pk_bf16_f32 v227, v164, v165\n"
        "v_mfma_f32_16x16x32_bf16 v[72:75], a[252:255], v[80:83], 0\n"
        "s_add_u32 s38, s46, s43\n"
        "v_rcp_f32 v88, v88\n"
        "v_mfma_f32_16x16x32_bf16 v[72:75], a[252:255], v[84:87], v[72:75]\n"
        "s_add_u32 s39, s46, s44\n"
        "v_mul_f32 v92, v88, v92\n"
        "v_mul_f32 v93, v88, v93\n"
        "v_mfma_f32_16x16x32_bf16 v[76:79], v[132:135], v[84:87], 0\n"
        "s_add_u32 s37, s37, 6291456\n"
        "v_mul_f32 v94, v88, v94\n"
        "v_mul_f32 v95, v88, v95\n"
        "v_mfma_f32_16x16x32_bf16 v[76:79], v[128:131], v[80:83], v[76:79]\n"
        "v_rcp_f32 v72, v72\n"
        "v_mfma_f32_16x16x32_bf16 v[104:107], v[136:139], v[80:83], 0\n"
        "s_add_u32 m0, s41, 13312\n"
        "v_cvt_pk_bf16_f32 v92, v92, v93\n"
        "v_cvt_pk_bf16_f32 v93, v94, v95\n"
        "v_mfma_f32_16x16x32_bf16 v[104:107], v[140:143], v[84:87], v[104:107]\n"
        "v_mul_f32 v182, v88, v182\n"
        "v_mul_f32 v183, v88, v183\n"
        "v_mfma_f32_16x16x32_bf16 v[108:111], v[148:151], v[84:87], 0\n"
        "buffer_load_dwordx4 v243, s[8:11], s35 offen lds\n"
        "v_mfma_f32_16x16x32_bf16 v[108:111], v[144:147], v[80:83], v[108:111]\n"
        "v_mul_f32 v184, v88, v184\n"
        "v_mul_f32 v185, v88, v185\n"
        "v_mfma_f32_16x16x32_bf16 v[64:67], a[252:255], v[96:99], 0\n"
        "v_cvt_pk_bf16_f32 v94, v182, v183\n"
        "v_cvt_pk_bf16_f32 v95, v184, v185\n"
        "v_mfma_f32_16x16x32_bf16 v[64:67], a[252:255], v[100:103], v[64:67]\n"
        "ds_write_b128 v245, v[92:95] offset:0\n"
        "v_mfma_f32_16x16x32_bf16 v[68:71], v[132:135], v[100:103], 0\n"
        "v_mul_f32 v202, v88, v202\n"
        "v_mul_f32 v203, v88, v203\n"
        "v_mfma_f32_16x16x32_bf16 v[68:71], v[128:131], v[96:99], v[68:71]\n"
        "v_mul_f32 v204, v88, v204\n"
        "v_mul_f32 v205, v88, v205\n"
        "v_mfma_f32_16x16x32_bf16 v[120:123], v[136:139], v[96:99], 0\n"
        "v_cvt_pk_bf16_f32 v202, v202, v203\n"
        "v_cvt_pk_bf16_f32 v203, v204, v205\n"
        "v_mfma_f32_16x16x32_bf16 v[120:123], v[140:143], v[100:103], v[120:123]\n"
        "v_mul_f32 v76, v72, v76\n"
        "v_mul_f32 v77, v72, v77\n"
        "v_mfma_f32_16x16x32_bf16 v[124:127], v[148:151], v[100:103], 0\n"
        "ds_write_b64 v246, v[202:203] offset:0\n"
        "v_mfma_f32_16x16x32_bf16 v[124:127], v[144:147], v[96:99], v[124:127]\n"
        "v_rcp_f32 v64, v64\n"
        "v_mfma_f32_16x16x32_bf16 v[80:83], a[252:255], v[112:115], 0\n"
        "v_mul_f32 v78, v72, v78\n"
        "v_mul_f32 v79, v72, v79\n"
        "v_mfma_f32_16x16x32_bf16 v[80:83], a[252:255], v[116:119], v[80:83]\n"
        "v_cvt_pk_bf16_f32 v76, v76, v77\n"
        "v_cvt_pk_bf16_f32 v77, v78, v79\n"
        "v_mfma_f32_16x16x32_bf16 v[84:87], v[132:135], v[116:119], 0\n"
        "s_add_u32 m0, s41, 14336\n"
        "v_mul_f32 v104, v72, v104\n"
        "v_mul_f32 v105, v72, v105\n"
        "v_mfma_f32_16x16x32_bf16 v[84:87], v[128:131], v[112:115], v[84:87]\n"
        "v_mul_f32 v106, v72, v106\n"
        "v_mul_f32 v107, v72, v107\n"
        "v_mfma_f32_16x16x32_bf16 v[222:225], v[136:139], v[112:115], 0\n"
        "buffer_load_dwordx4 v244, s[8:11], s35 offen lds\n"
        "v_mfma_f32_16x16x32_bf16 v[222:225], v[140:143], v[116:119], v[222:225]\n"
        "ds_read_b128 a[224:227], v240 offset:0\n"
        "ds_read_b128 a[228:231], v241 offset:0\n"
        "v_mfma_f32_16x16x32_bf16 v[162:165], v[148:151], v[116:119], 0\n"
        "ds_read_b128 a[232:235], v240 offset:128\n"
        "v_cvt_pk_bf16_f32 v78, v104, v105\n"
        "v_mfma_f32_16x16x32_bf16 v[162:165], v[144:147], v[112:115], v[162:165]\n"
        "v_cvt_pk_bf16_f32 v79, v106, v107\n"
        "v_mul_f32 v108, v72, v108\n"
        "v_mfma_f32_16x16x32_bf16 v[96:99], a[236:239], a[144:147], 0\n"
        "ds_write_b128 v245, v[76:79] offset:256\n"
        "v_mfma_f32_16x16x32_bf16 v[96:99], a[240:243], a[148:151], v[96:99]\n"
        "v_mul_f32 v109, v72, v109\n"
        "v_mul_f32 v110, v72, v110\n"
        "v_mfma_f32_16x16x32_bf16 v[96:99], a[244:247], a[152:155], v[96:99]\n"
        "v_mul_f32 v111, v72, v111\n"
        "v_cvt_pk_bf16_f32 v108, v108, v109\n"
        "v_mfma_f32_16x16x32_bf16 v[96:99], a[248:251], a[156:159], v[96:99]\n"
        "v_cvt_pk_bf16_f32 v109, v110, v111\n"
        "v_mul_f32 v68, v64, v68\n"
        "v_mfma_f32_16x16x32_bf16 v[96:99], a[216:219], a[160:163], v[96:99]\n"
        "v_rcp_f32 v80, v80\n"
        "v_mfma_f32_16x16x32_bf16 v[96:99], a[220:223], a[164:167], v[96:99]\n"
        "ds_write_b64 v246, v[108:109] offset:256\n"
        "v_mfma_f32_16x16x32_bf16 v[100:103], a[236:239], a[168:171], 0\n"
        "v_mul_f32 v69, v64, v69\n"
        "v_mul_f32 v70, v64, v70\n"
        "v_mfma_f32_16x16x32_bf16 v[100:103], a[240:243], a[172:175], v[100:103]\n"
        "v_mul_f32 v71, v64, v71\n"
        "v_cvt_pk_bf16_f32 v68, v68, v69\n"
        "v_mfma_f32_16x16x32_bf16 v[100:103], a[244:247], a[176:179], v[100:103]\n"
        "v_cvt_pk_bf16_f32 v134, v96, v97\n"
        "v_cvt_pk_bf16_f32 v135, v98, v99\n"
        "v_mfma_f32_16x16x32_bf16 v[100:103], a[248:251], a[180:183], v[100:103]\n"
        "v_cvt_pk_bf16_f32 v69, v70, v71\n"
        "v_mul_f32 v120, v64, v120\n"
        "v_mfma_f32_16x16x32_bf16 v[100:103], a[216:219], a[184:187], v[100:103]\n"
        "v_mul_f32 v121, v64, v121\n"
        "v_mul_f32 v122, v64, v122\n"
        "v_mfma_f32_16x16x32_bf16 v[100:103], a[220:223], a[188:191], v[100:103]\n"
        "v_mul_f32 v123, v64, v123\n"
        "v_cvt_pk_bf16_f32 v70, v120, v121\n"
        "v_mfma_f32_16x16x32_bf16 v[112:115], a[236:239], a[192:195], 0\n"
        "ds_read_b128 a[236:239], v241 offset:128\n"
        "v_cvt_pk_bf16_f32 v71, v122, v123\n"
        "v_mfma_f32_16x16x32_bf16 v[112:115], a[240:243], a[196:199], v[112:115]\n"
        "ds_read_b128 a[240:243], v240 offset:256\n"
        "v_mul_f32 v124, v64, v124\n"
        "v_mfma_f32_16x16x32_bf16 v[112:115], a[244:247], a[200:203], v[112:115]\n"
        "ds_read_b128 a[244:247], v241 offset:256\n"
        "v_cvt_pk_bf16_f32 v142, v100, v101\n"
        "v_mfma_f32_16x16x32_bf16 v[112:115], a[248:251], a[204:207], v[112:115]\n"
        "ds_read_b128 a[248:251], v240 offset:1536\n"
        "v_cvt_pk_bf16_f32 v143, v102, v103\n"
        "v_mfma_f32_16x16x32_bf16 v[112:115], a[216:219], a[208:211], v[112:115]\n"
        "ds_read_b128 a[216:219], v241 offset:1536\n"
        "v_mul_f32 v125, v64, v125\n"
        "v_mfma_f32_16x16x32_bf16 v[112:115], a[220:223], a[212:215], v[112:115]\n"
        "s_waitcnt lgkmcnt(9)\n"
        "ds_read_b128 a[220:223], v240 offset:1664\n"
        "v_mul_f32 v126, v64, v126\n"
        "v_mfma_f32_16x16x32_bf16 v[96:99], a[224:227], a[144:147], 0\n"
        "s_waitcnt lgkmcnt(9)\n"
        "ds_write_b128 v245, v[68:71] offset:512\n"
        "v_mfma_f32_16x16x32_bf16 v[96:99], a[228:231], a[148:151], v[96:99]\n"
        "s_waitcnt lgkmcnt(9)\n"
        "v_mul_f32 v127, v64, v127\n"
        "v_cvt_pk_bf16_f32 v124, v124, v125\n"
        "v_mfma_f32_16x16x32_bf16 v[96:99], a[232:235], a[152:155], v[96:99]\n"
        "s_waitcnt lgkmcnt(6)\n"
        "v_cvt_pk_bf16_f32 v150, v112, v113\n"
        "v_cvt_pk_bf16_f32 v151, v114, v115\n"
        "v_mfma_f32_16x16x32_bf16 v[96:99], a[236:239], a[156:159], v[96:99]\n"
        "s_waitcnt lgkmcnt(5)\n"
        "v_cvt_pk_bf16_f32 v125, v126, v127\n"
        "v_mul_f32 v84, v80, v84\n"
        "v_mfma_f32_16x16x32_bf16 v[96:99], a[240:243], a[160:163], v[96:99]\n"
        "s_waitcnt lgkmcnt(4)\n"
        "v_mul_f32 v85, v80, v85\n"
        "v_mul_f32 v86, v80, v86\n"
        "v_mfma_f32_16x16x32_bf16 v[96:99], a[244:247], a[164:167], v[96:99]\n"
        "ds_write_b64 v246, v[124:125] offset:512\n"
        "v_mfma_f32_16x16x32_bf16 v[100:103], a[224:227], a[168:171], 0\n"
        "v_mul_f32 v87, v80, v87\n"
        "v_cvt_pk_bf16_f32 v84, v84, v85\n"
        "v_mfma_f32_16x16x32_bf16 v[100:103], a[228:231], a[172:175], v[100:103]\n"
        "v_cvt_pk_bf16_f32 v85, v86, v87\n"
        "v_mul_f32 v222, v80, v222\n"
        "v_mfma_f32_16x16x32_bf16 v[100:103], a[232:235], a[176:179], v[100:103]\n"
        "v_cvt_pk_bf16_f32 v128, v96, v97\n"
        "v_cvt_pk_bf16_f32 v129, v98, v99\n"
        "v_mfma_f32_16x16x32_bf16 v[100:103], a[236:239], a[180:183], v[100:103]\n"
        "v_mul_f32 v223, v80, v223\n"
        "v_mul_f32 v224, v80, v224\n"
        "v_mfma_f32_16x16x32_bf16 v[100:103], a[240:243], a[184:187], v[100:103]\n"
        "v_mul_f32 v225, v80, v225\n"
        "v_cvt_pk_bf16_f32 v86, v222, v223\n"
        "v_mfma_f32_16x16x32_bf16 v[100:103], a[244:247], a[188:191], v[100:103]\n"
        "v_cvt_pk_bf16_f32 v87, v224, v225\n"
        "v_mul_f32 v162, v80, v162\n"
        "v_mfma_f32_16x16x32_bf16 v[112:115], a[224:227], a[192:195], 0\n"
        "ds_read_b128 a[224:227], v241 offset:1664\n"
        "v_mul_f32 v163, v80, v163\n"
        "v_mfma_f32_16x16x32_bf16 v[112:115], a[228:231], a[196:199], v[112:115]\n"
        "ds_read_b128 a[228:231], v240 offset:1792\n"
        "v_mul_f32 v164, v80, v164\n"
        "v_mfma_f32_16x16x32_bf16 v[112:115], a[232:235], a[200:203], v[112:115]\n"
        "ds_read_b128 a[232:235], v241 offset:1792\n"
        "v_cvt_pk_bf16_f32 v136, v100, v101\n"
        "v_mfma_f32_16x16x32_bf16 v[112:115], a[236:239], a[204:207], v[112:115]\n"
        "ds_read_b128 a[236:239], v240 offset:12288\n"
        "v_cvt_pk_bf16_f32 v137, v102, v103\n"
        "v_mfma_f32_16x16x32_bf16 v[112:115], a[240:243], a[208:211], v[112:115]\n"
        "ds_read_b128 a[240:243], v241 offset:12288\n"
        "v_mul_f32 v165, v80, v165\n"
        "v_mfma_f32_16x16x32_bf16 v[112:115], a[244:247], a[212:215], v[112:115]\n"
        "s_waitcnt lgkmcnt(9)\n"
        "ds_read_b128 a[244:247], v240 offset:12416\n"
        "v_cvt_pk_bf16_f32 v162, v162, v163\n"
        "v_mfma_f32_16x16x32_bf16 v[96:99], a[248:251], a[144:147], 0\n"
        "s_waitcnt lgkmcnt(9)\n"
        "ds_write_b128 v245, v[84:87] offset:768\n"
        "v_mfma_f32_16x16x32_bf16 v[96:99], a[216:219], a[148:151], v[96:99]\n"
        "s_waitcnt lgkmcnt(9)\n"
        "v_cvt_pk_bf16_f32 v163, v164, v165\n"
        "v_mfma_f32_16x16x32_bf16 v[96:99], a[220:223], a[152:155], v[96:99]\n"
        "s_waitcnt lgkmcnt(6)\n"
        "v_mfma_f32_16x16x32_bf16 v[96:99], a[224:227], a[156:159], v[96:99]\n"
        "s_waitcnt lgkmcnt(5)\n"
        "v_cvt_pk_bf16_f32 v144, v112, v113\n"
        "v_cvt_pk_bf16_f32 v145, v114, v115\n"
        "v_mfma_f32_16x16x32_bf16 v[96:99], a[228:231], a[160:163], v[96:99]\n"
        "s_waitcnt lgkmcnt(4)\n"
        "v_mfma_f32_16x16x32_bf16 v[96:99], a[232:235], a[164:167], v[96:99]\n"
        "ds_write_b64 v246, v[162:163] offset:768\n"
        "v_mfma_f32_16x16x32_bf16 v[100:103], a[248:251], a[168:171], 0\n"
        "v_mfma_f32_16x16x32_bf16 v[100:103], a[216:219], a[172:175], v[100:103]\n"
        "v_mfma_f32_16x16x32_bf16 v[100:103], a[220:223], a[176:179], v[100:103]\n"
        "v_mfma_f32_16x16x32_bf16 v[100:103], a[224:227], a[180:183], v[100:103]\n"
        "v_mfma_f32_16x16x32_bf16 v[100:103], a[228:231], a[184:187], v[100:103]\n"
        "v_mfma_f32_16x16x32_bf16 v[100:103], a[232:235], a[188:191], v[100:103]\n"
        "v_mfma_f32_16x16x32_bf16 v[112:115], a[248:251], a[192:195], 0\n"
        "ds_read_b128 a[248:251], v241 offset:12416\n"
        "ds_read_b128 v[162:165], v251 offset:0\n"
        "v_mfma_f32_16x16x32_bf16 v[112:115], a[216:219], a[196:199], v[112:115]\n"
        "ds_read_b128 a[216:219], v240 offset:12544\n"
        "v_cvt_pk_bf16_f32 v130, v96, v97\n"
        "v_mfma_f32_16x16x32_bf16 v[112:115], a[220:223], a[200:203], v[112:115]\n"
        "ds_read_b128 a[220:223], v241 offset:12544\n"
        "v_cvt_pk_bf16_f32 v131, v98, v99\n"
        "v_mfma_f32_16x16x32_bf16 v[112:115], a[224:227], a[204:207], v[112:115]\n"
        "v_cvt_pk_bf16_f32 v138, v100, v101\n"
        "v_cvt_pk_bf16_f32 v139, v102, v103\n"
        "v_mfma_f32_16x16x32_bf16 v[112:115], a[228:231], a[208:211], v[112:115]\n"
        "ds_read_b128 v[182:185], v251 offset:512\n"
        "v_mfma_f32_16x16x32_bf16 v[112:115], a[232:235], a[212:215], v[112:115]\n"
        "s_waitcnt lgkmcnt(9)\n"
        "v_mfma_f32_16x16x32_bf16 v[96:99], a[236:239], a[144:147], 0\n"
        "s_waitcnt lgkmcnt(8)\n"
        "v_mfma_f32_16x16x32_bf16 v[96:99], a[240:243], a[148:151], v[96:99]\n"
        "s_waitcnt lgkmcnt(7)\n"
        "v_mfma_f32_16x16x32_bf16 v[96:99], a[244:247], a[152:155], v[96:99]\n"
        "s_waitcnt lgkmcnt(4)\n"
        "ds_read_b128 v[202:205], v251 offset:4096\n"
        "v_mfma_f32_16x16x32_bf16 v[96:99], a[248:251], a[156:159], v[96:99]\n"
        "s_waitcnt lgkmcnt(3)\n"
        "v_mfma_f32_16x16x32_bf16 v[96:99], a[216:219], a[160:163], v[96:99]\n"
        "s_waitcnt lgkmcnt(2)\n"
        "v_mfma_f32_16x16x32_bf16 v[96:99], a[220:223], a[164:167], v[96:99]\n"
        "s_add_u32 s47, s47, 6291456\n"
        "v_cvt_pk_bf16_f32 v146, v112, v113\n"
        "v_cvt_pk_bf16_f32 v147, v114, v115\n"
        "v_mfma_f32_16x16x32_bf16 v[100:103], a[236:239], a[168:171], 0\n"
        "ds_read_b128 v[222:225], v251 offset:4608\n"
        "s_add_u32 s36, s36, 2097152\n"
        "v_mfma_f32_16x16x32_bf16 v[100:103], a[240:243], a[172:175], v[100:103]\n"
        "s_xor_b32 s48, s48, 8192\n"
        "v_cvt_pk_bf16_f32 v132, v96, v97\n"
        "v_cvt_pk_bf16_f32 v133, v98, v99\n"
        "v_mfma_f32_16x16x32_bf16 v[100:103], a[244:247], a[176:179], v[100:103]\n"
        "v_pk_min_u16 v162, v162, s26\n"
        "v_pk_min_u16 v163, v163, s26\n"
        "v_mfma_f32_16x16x32_bf16 v[100:103], a[248:251], a[180:183], v[100:103]\n"
        "v_pk_min_u16 v164, v164, s26\n"
        "v_pk_min_u16 v165, v165, s26\n"
        "v_mfma_f32_16x16x32_bf16 v[100:103], a[216:219], a[184:187], v[100:103]\n"
        "v_add_u32 v240, s31, v240\n"
        "v_add_u32 v241, s31, v241\n"
        "v_mfma_f32_16x16x32_bf16 v[100:103], a[220:223], a[188:191], v[100:103]\n"
        "v_mfma_f32_16x16x32_bf16 v[112:115], a[236:239], a[192:195], 0\n"
        "s_waitcnt lgkmcnt(2)\n"
        "v_pk_min_u16 v182, v182, s26\n"
        "v_pk_min_u16 v183, v183, s26\n"
        "v_mfma_f32_16x16x32_bf16 v[112:115], a[240:243], a[196:199], v[112:115]\n"
        "v_pk_min_u16 v184, v184, s26\n"
        "v_pk_min_u16 v185, v185, s26\n"
        "v_mfma_f32_16x16x32_bf16 v[112:115], a[244:247], a[200:203], v[112:115]\n"
        "v_mfma_f32_16x16x32_bf16 v[112:115], a[248:251], a[204:207], v[112:115]\n"
        "v_cvt_pk_bf16_f32 v140, v100, v101\n"
        "v_cvt_pk_bf16_f32 v141, v102, v103\n"
        "v_mfma_f32_16x16x32_bf16 v[112:115], a[216:219], a[208:211], v[112:115]\n"
        "s_waitcnt lgkmcnt(1)\n"
        "v_pk_min_u16 v202, v202, s26\n"
        "v_pk_min_u16 v203, v203, s26\n"
        "v_mfma_f32_16x16x32_bf16 v[112:115], a[220:223], a[212:215], v[112:115]\n"
        "v_pk_min_u16 v204, v204, s26\n"
        "v_pk_min_u16 v205, v205, s26\n"
        "v_mfma_f32_16x16x32_bf16 v[64:67], v[152:155], v[156:159], v[0:3]\n"
        "v_mfma_f32_16x16x32_bf16 v[64:67], v[168:171], v[160:163], v[64:67]\n"
        "v_mfma_f32_16x16x32_bf16 v[68:71], v[172:175], v[156:159], v[4:7]\n"
        "s_waitcnt lgkmcnt(0)\n"
        "v_pk_min_u16 v222, v222, s26\n"
        "v_pk_min_u16 v223, v223, s26\n"
        "v_mfma_f32_16x16x32_bf16 v[68:71], v[188:191], v[164:167], v[68:71]\n"
        "ds_read_b128 v[162:165], v252 offset:0\n"
        "v_cvt_pk_bf16_f32 v148, v112, v113\n"
        "v_mfma_f32_16x16x32_bf16 v[80:83], v[152:155], v[176:179], v[16:19]\n"
        "v_cvt_pk_bf16_f32 v149, v114, v115\n"
        "v_pk_min_u16 v224, v224, s26\n"
        "v_mfma_f32_16x16x32_bf16 v[80:83], v[168:171], v[180:183], v[80:83]\n"
        "v_pk_min_u16 v225, v225, s26\n"
        "v_max3_f32 v232, v64, v65, v66\n"
        "v_mfma_f32_16x16x32_bf16 v[84:87], v[172:175], v[176:179], v[20:23]\n"
        "v_max3_f32 v232, v232, v67, v68\n"
        "v_max3_f32 v232, v232, v69, v70\n"
        "v_mfma_f32_16x16x32_bf16 v[84:87], v[188:191], v[184:187], v[84:87]\n"
        "ds_read_b128 v[182:185], v252 offset:512\n"
        "v_mfma_f32_16x16x32_bf16 v[96:99], v[152:155], v[196:199], v[32:35]\n"
        "v_max3_f32 v233, v80, v81, v82\n"
        "v_mfma_f32_16x16x32_bf16 v[96:99], v[168:171], v[200:203], v[96:99]\n"
        "v_mfma_f32_16x16x32_bf16 v[100:103], v[172:175], v[196:199], v[36:39]\n"
        "v_mfma_f32_16x16x32_bf16 v[100:103], v[188:191], v[204:207], v[100:103]\n"
        "ds_read_b128 v[202:205], v252 offset:4096\n"
        "v_mfma_f32_16x16x32_bf16 v[112:115], v[152:155], v[216:219], v[48:51]\n"
        "s_waitcnt lgkmcnt(2)\n"
        "v_pk_min_u16 v162, v162, s26\n"
        "v_pk_min_u16 v163, v163, s26\n"
        "v_mfma_f32_16x16x32_bf16 v[112:115], v[168:171], v[220:223], v[112:115]\n"
        "v_pk_min_u16 v164, v164, s26\n"
        "v_pk_min_u16 v165, v165, s26\n"
        "v_mfma_f32_16x16x32_bf16 v[116:119], v[172:175], v[216:219], v[52:55]\n"
        "v_max3_f32 v233, v233, v83, v84\n"
        "v_max3_f32 v233, v233, v85, v86\n"
        "v_mfma_f32_16x16x32_bf16 v[116:119], v[188:191], v[224:227], v[116:119]\n"
        "ds_read_b128 v[222:225], v252 offset:4608\n"
        "v_max3_f32 v234, v96, v97, v98\n"
        "v_mfma_f32_16x16x32_bf16 v[72:75], v[192:195], v[156:159], v[8:11]\n"
        "s_waitcnt lgkmcnt(2)\n"
        "v_pk_min_u16 v182, v182, s26\n"
        "v_pk_min_u16 v183, v183, s26\n"
        "v_mfma_f32_16x16x32_bf16 v[72:75], v[208:211], v[160:163], v[72:75]\n"
        "v_pk_min_u16 v184, v184, s26\n"
        "v_pk_min_u16 v185, v185, s26\n"
        "v_mfma_f32_16x16x32_bf16 v[76:79], v[212:215], v[156:159], v[12:15]\n"
        "v_max3_f32 v234, v234, v99, v100\n"
        "v_max3_f32 v234, v234, v101, v102\n"
        "v_mfma_f32_16x16x32_bf16 v[76:79], v[228:231], v[164:167], v[76:79]\n"
        "v_max3_f32 v235, v112, v113, v114\n"
        "v_max3_f32 v235, v235, v115, v116\n"
        "v_mfma_f32_16x16x32_bf16 v[88:91], v[192:195], v[176:179], v[24:27]\n"
        "s_barrier\n"
        "ds_read_b128 v[152:155], v247 offset:0\n"
        "ds_read_b128 v[160:163], v247 offset:4096\n"
        "v_mfma_f32_16x16x32_bf16 v[88:91], v[208:211], v[180:183], v[88:91]\n"
        "ds_read_b128 v[156:159], v247 offset:256\n"
        "ds_read_b128 v[164:167], v247 offset:4352\n"
        "v_mfma_f32_16x16x32_bf16 v[92:95], v[212:215], v[176:179], v[28:31]\n"
        "ds_read_b128 a[216:219], v248 offset:0\n"
        "ds_read_b128 a[220:223], v248 offset:6144\n"
        "v_mfma_f32_16x16x32_bf16 v[92:95], v[228:231], v[184:187], v[92:95]\n"
        "s_waitcnt lgkmcnt(7)\n"
        "v_pk_min_u16 v202, v202, s26\n"
        "v_pk_min_u16 v203, v203, s26\n"
        "v_mfma_f32_16x16x32_bf16 v[104:107], v[192:195], v[196:199], v[40:43]\n"
        "ds_read_b128 a[224:227], v254 offset:16\n"
        "ds_read_b128 a[228:231], v254 offset:6160\n"
        "v_mfma_f32_16x16x32_bf16 v[104:107], v[208:211], v[200:203], v[104:107]\n"
        "v_pk_min_u16 v204, v204, s26\n"
        "v_pk_min_u16 v205, v205, s26\n"
        "v_mfma_f32_16x16x32_bf16 v[108:111], v[212:215], v[196:199], v[44:47]\n"
        "s_waitcnt lgkmcnt(8)\n"
        "v_pk_min_u16 v222, v222, s26\n"
        "v_pk_min_u16 v223, v223, s26\n"
        "v_mfma_f32_16x16x32_bf16 v[108:111], v[228:231], v[204:207], v[108:111]\n"
        "v_pk_min_u16 v224, v224, s26\n"
        "v_pk_min_u16 v225, v225, s26\n"
        "v_mfma_f32_16x16x32_bf16 v[120:123], v[192:195], v[216:219], v[56:59]\n"
        "ds_read_b128 a[232:235], v248 offset:32\n"
        "v_max3_f32 v232, v232, v71, v72\n"
        "v_mfma_f32_16x16x32_bf16 v[120:123], v[208:211], v[220:223], v[120:123]\n"
        "v_max3_f32 v232, v232, v73, v74\n"
        "v_max3_f32 v232, v232, v75, v76\n"
        "v_mfma_f32_16x16x32_bf16 v[124:127], v[212:215], v[216:219], v[60:63]\n"
        "v_max3_f32 v232, v232, v77, v78\n"
        "v_max_f32 v232, v232, v79\n"
        "v_mfma_f32_16x16x32_bf16 v[124:127], v[228:231], v[224:227], v[124:127]\n"
        "s_waitcnt lgkmcnt(4)\n"
        "v_max3_f32 v233, v233, v87, v88\n"
        "v_max3_f32 v233, v233, v89, v90\n"
        "v_mfma_f32_16x16x32_bf16 v[172:175], v[152:155], a[216:219], 0\n"
        "s_waitcnt lgkmcnt(3)\n"
        "v_max3_f32 v233, v233, v91, v92\n"
        "v_max3_f32 v233, v233, v93, v94\n"
        "v_mfma_f32_16x16x32_bf16 v[172:175], v[160:163], a[220:223], v[172:175]\n"
        "v_max_f32 v233, v233, v95\n"
        "v_max3_f32 v234, v234, v103, v104\n"
        "v_mfma_f32_16x16x32_bf16 v[180:183], v[164:167], a[220:223], 0\n"
        "ds_read_b128 a[220:223], v248 offset:6176\n"
        "ds_read_b128 a[236:239], v247 offset:8192\n"
        "v_mfma_f32_16x16x32_bf16 v[180:183], v[156:159], a[216:219], v[180:183]\n"
        "s_waitcnt lgkmcnt(4)\n"
        "ds_read_b128 a[216:219], v254 offset:48\n"
        "ds_read_b128 a[244:247], v247 offset:12288\n"
        "v_mfma_f32_16x16x32_bf16 v[184:187], v[156:159], a[224:227], 0\n"
        "s_waitcnt lgkmcnt(5)\n"
        "ds_read_b128 a[240:243], v247 offset:8448\n"
        "ds_read_b128 a[248:251], v247 offset:12544\n"
        "v_mfma_f32_16x16x32_bf16 v[184:187], v[164:167], a[228:231], v[184:187]\n"
        "v_permlane32_swap_b32 v232, v233\n"
        "v_mfma_f32_16x16x32_bf16 v[176:179], v[160:163], a[228:231], 0\n"
        "ds_read_b128 a[228:231], v254 offset:6192\n"
        "v_max_f32 v232, v232, v233\n"
        "v_mfma_f32_16x16x32_bf16 v[176:179], v[152:155], a[224:227], v[176:179]\n"
        "s_waitcnt lgkmcnt(7)\n"
        "ds_read_b128 a[224:227], v248 offset:64\n"
        "v_mov_b32 v233, v232\n"
        "v_mfma_f32_16x16x32_bf16 v[212:215], v[152:155], a[232:235], 0\n"
        "s_waitcnt lgkmcnt(7)\n"
        "v_max3_f32 v234, v234, v105, v106\n"
        "v_max3_f32 v234, v234, v107, v108\n"
        "v_mfma_f32_16x16x32_bf16 v[212:215], v[160:163], a[220:223], v[212:215]\n"
        "v_permlane16_swap_b32 v232, v233\n"
        "v_mfma_f32_16x16x32_bf16 v[192:195], v[164:167], a[220:223], 0\n"
        "ds_read_b128 a[220:223], v248 offset:6208\n"
        "v_max_f32 v232, v232, v233\n"
        "v_mfma_f32_16x16x32_bf16 v[192:195], v[156:159], a[232:235], v[192:195]\n"
        "s_waitcnt lgkmcnt(6)\n"
        "ds_read_b128 a[232:235], v254 offset:80\n"
        "v_mul_f32 v236, s25, v232\n"
        "v_mfma_f32_16x16x32_bf16 v[196:199], v[156:159], a[216:219], 0\n"
        "s_waitcnt lgkmcnt(3)\n"
        "v_mul_f32 v237, s25, v232\n"
        "v_max3_f32 v234, v234, v109, v110\n"
        "v_mfma_f32_16x16x32_bf16 v[196:199], v[164:167], a[228:231], v[196:199]\n"
        "v_permlane32_swap_b32 v236, v237\n"
        "v_mfma_f32_16x16x32_bf16 v[216:219], v[160:163], a[228:231], 0\n"
        "ds_read_b128 a[228:231], v254 offset:6224\n"
        "v_max_f32 v234, v234, v111\n"
        "v_mfma_f32_16x16x32_bf16 v[216:219], v[152:155], a[216:219], v[216:219]\n"
        "s_waitcnt lgkmcnt(3)\n"
        "ds_read_b128 a[216:219], v248 offset:36864\n"
        "v_max3_f32 v235, v235, v117, v118\n"
        "v_mfma_f32_16x16x32_bf16 v[220:223], v[152:155], a[224:227], 0\n"
        "s_waitcnt lgkmcnt(3)\n"
        "v_max3_f32 v235, v235, v119, v120\n"
        "v_max3_f32 v235, v235, v121, v122\n"
        "v_mfma_f32_16x16x32_bf16 v[220:223], v[160:163], a[220:223], v[220:223]\n"
        "v_max3_f32 v235, v235, v123, v124\n"
        "v_max3_f32 v235, v235, v125, v126\n"
        "v_mfma_f32_16x16x32_bf16 v[200:203], v[164:167], a[220:223], 0\n"
        "ds_read_b128 a[220:223], v248 offset:43008\n"
        "v_max_f32 v235, v235, v127\n"
        "v_mfma_f32_16x16x32_bf16 v[200:203], v[156:159], a[224:227], v[200:203]\n"
        "s_waitcnt lgkmcnt(3)\n"
        "ds_read_b128 a[224:227], v254 offset:36880\n"
        "v_fma_f32 v64, v64, s24, v236\n"
        "v_mfma_f32_16x16x32_bf16 v[204:207], v[156:159], a[232:235], 0\n"
        "s_waitcnt lgkmcnt(3)\n"
        "v_permlane32_swap_b32 v234, v235\n"
        "v_mfma_f32_16x16x32_bf16 v[204:207], v[164:167], a[228:231], v[204:207]\n"
        "v_max_f32 v234, v234, v235\n"
        "v_mov_b32 v235, v234\n"
        "v_mfma_f32_16x16x32_bf16 v[224:227], v[160:163], a[228:231], 0\n"
        "ds_read_b128 a[228:231], v254 offset:43024\n"
        "v_fma_f32 v65, v65, s24, v236\n"
        "v_mfma_f32_16x16x32_bf16 v[224:227], v[152:155], a[232:235], v[224:227]\n"
        "s_waitcnt lgkmcnt(3)\n"
        "ds_read_b128 a[232:235], v248 offset:36896\n"
        "v_fma_f32 v66, v66, s24, v236\n"
        "v_mfma_f32_16x16x32_bf16 v[172:175], a[236:239], a[216:219], v[172:175]\n"
        "s_waitcnt lgkmcnt(3)\n"
        "v_permlane16_swap_b32 v234, v235\n"
        "v_mfma_f32_16x16x32_bf16 v[172:175], a[244:247], a[220:223], v[172:175]\n"
        "v_max_f32 v234, v234, v235\n"
        "v_mul_f32 v238, s25, v234\n"
        "v_mfma_f32_16x16x32_bf16 v[180:183], a[248:251], a[220:223], v[180:183]\n"
        "ds_read_b128 a[220:223], v248 offset:43040\n"
        "ds_read_b128 v[152:155], v247 offset:16384\n"
        "v_mfma_f32_16x16x32_bf16 v[180:183], a[240:243], a[216:219], v[180:183]\n"
        "s_waitcnt lgkmcnt(4)\n"
        "ds_read_b128 a[216:219], v254 offset:36912\n"
        "ds_read_b128 v[160:163], v247 offset:20480\n"
        "v_mfma_f32_16x16x32_bf16 v[184:187], a[240:243], a[224:227], v[184:187]\n"
        "s_waitcnt lgkmcnt(5)\n"
        "ds_read_b128 v[156:159], v247 offset:16640\n"
        "ds_read_b128 v[164:167], v247 offset:20736\n"
        "v_mfma_f32_16x16x32_bf16 v[184:187], a[248:251], a[228:231], v[184:187]\n"
        "v_mul_f32 v239, s25, v234\n"
        "v_fma_f32 v67, v67, s24, v236\n"
        "v_mfma_f32_16x16x32_bf16 v[176:179], a[244:247], a[228:231], v[176:179]\n"
        "ds_read_b128 a[228:231], v254 offset:43056\n"
        "v_fma_f32 v68, v68, s24, v236\n"
        "v_mfma_f32_16x16x32_bf16 v[176:179], a[236:239], a[224:227], v[176:179]\n"
        "s_waitcnt lgkmcnt(7)\n"
        "ds_read_b128 a[224:227], v248 offset:36928\n"
        "v_fma_f32 v69, v69, s24, v236\n"
        "v_mfma_f32_16x16x32_bf16 v[212:215], a[236:239], a[232:235], v[212:215]\n"
        "s_waitcnt lgkmcnt(7)\n"
        "v_permlane32_swap_b32 v238, v239\n"
        "v_mfma_f32_16x16x32_bf16 v[212:215], a[244:247], a[220:223], v[212:215]\n"
        "v_exp_f32 v64, v64\n"
        "v_mfma_f32_16x16x32_bf16 v[192:195], a[248:251], a[220:223], v[192:195]\n"
        "ds_read_b128 a[220:223], v248 offset:43072\n"
        "v_fma_f32 v70, v70, s24, v236\n"
        "v_mfma_f32_16x16x32_bf16 v[192:195], a[240:243], a[232:235], v[192:195]\n"
        "s_waitcnt lgkmcnt(6)\n"
        "ds_read_b128 a[232:235], v254 offset:36944\n"
        "v_fma_f32 v71, v71, s24, v236\n"
        "v_mfma_f32_16x16x32_bf16 v[196:199], a[240:243], a[216:219], v[196:199]\n"
        "s_waitcnt lgkmcnt(3)\n"
        "v_exp_f32 v65, v65\n"
        "v_mfma_f32_16x16x32_bf16 v[196:199], a[248:251], a[228:231], v[196:199]\n"
        "v_exp_f32 v66, v66\n"
        "v_mfma_f32_16x16x32_bf16 v[216:219], a[244:247], a[228:231], v[216:219]\n"
        "ds_read_b128 a[228:231], v254 offset:43088\n"
        "v_cvt_pk_bf16_f32 v64, v64, v65\n"
        "v_mfma_f32_16x16x32_bf16 v[216:219], a[236:239], a[216:219], v[216:219]\n"
        "s_waitcnt lgkmcnt(3)\n"
        "ds_read_b128 a[216:219], v248 offset:12288\n"
        "v_fma_f32 v72, v72, s24, v236\n"
        "v_mfma_f32_16x16x32_bf16 v[220:223], a[236:239], a[224:227], v[220:223]\n"
        "s_waitcnt lgkmcnt(3)\n"
        "v_exp_f32 v67, v67\n"
        "v_mfma_f32_16x16x32_bf16 v[220:223], a[244:247], a[220:223], v[220:223]\n"
        "v_exp_f32 v68, v68\n"
        "v_mfma_f32_16x16x32_bf16 v[200:203], a[248:251], a[220:223], v[200:203]\n"
        "ds_read_b128 a[220:223], v248 offset:49152\n"
        "v_cvt_pk_bf16_f32 v65, v66, v67\n"
        "v_mfma_f32_16x16x32_bf16 v[200:203], a[240:243], a[224:227], v[200:203]\n"
        "s_waitcnt lgkmcnt(3)\n"
        "ds_read_b128 a[224:227], v254 offset:12304\n"
        "v_fma_f32 v73, v73, s24, v236\n"
        "v_mfma_f32_16x16x32_bf16 v[204:207], a[240:243], a[232:235], v[204:207]\n"
        "s_waitcnt lgkmcnt(3)\n"
        "v_exp_f32 v69, v69\n"
        "v_mfma_f32_16x16x32_bf16 v[204:207], a[248:251], a[228:231], v[204:207]\n"
        "v_exp_f32 v70, v70\n"
        "v_mfma_f32_16x16x32_bf16 v[224:227], a[244:247], a[228:231], v[224:227]\n"
        "ds_read_b128 a[228:231], v254 offset:49168\n"
        "v_cvt_pk_bf16_f32 v66, v68, v69\n"
        "v_mfma_f32_16x16x32_bf16 v[224:227], a[236:239], a[232:235], v[224:227]\n"
        "s_waitcnt lgkmcnt(3)\n"
        "ds_read_b128 a[232:235], v248 offset:12320\n"
        "v_fma_f32 v74, v74, s24, v236\n"
        "v_mfma_f32_16x16x32_bf16 v[172:175], v[152:155], a[216:219], v[172:175]\n"
        "s_waitcnt lgkmcnt(3)\n"
        "v_exp_f32 v71, v71\n"
        "v_mfma_f32_16x16x32_bf16 v[172:175], v[160:163], a[220:223], v[172:175]\n"
        "v_exp_f32 v72, v72\n"
        "v_mfma_f32_16x16x32_bf16 v[180:183], v[164:167], a[220:223], v[180:183]\n"
        "ds_read_b128 a[220:223], v248 offset:49184\n"
        "v_cvt_pk_bf16_f32 v67, v70, v71\n"
        "v_mfma_f32_16x16x32_bf16 v[180:183], v[156:159], a[216:219], v[180:183]\n"
        "s_waitcnt lgkmcnt(3)\n"
        "ds_read_b128 a[216:219], v254 offset:12336\n"
        "v_fma_f32 v75, v75, s24, v236\n"
        "v_mfma_f32_16x16x32_bf16 v[184:187], v[156:159], a[224:227], v[184:187]\n"
        "s_waitcnt lgkmcnt(3)\n"
        "v_exp_f32 v73, v73\n"
        "v_mfma_f32_16x16x32_bf16 v[184:187], v[164:167], a[228:231], v[184:187]\n"
        "v_cvt_pk_bf16_f32 v68, v72, v73\n"
        "v_xor_b32 v251, 0x2000, v251\n"
        "v_mfma_f32_16x16x32_bf16 v[176:179], v[160:163], a[228:231], v[176:179]\n"
        "ds_read_b128 a[228:231], v254 offset:49200\n"
        "v_xor_b32 v252, 0x2000, v252\n"
        "v_mfma_f32_16x16x32_bf16 v[176:179], v[152:155], a[224:227], v[176:179]\n"
        "s_waitcnt lgkmcnt(3)\n"
        "ds_read_b128 a[224:227], v248 offset:12352\n"
        "v_add_u32 v245, s45, v245\n"
        "v_mfma_f32_16x16x32_bf16 v[212:215], v[152:155], a[232:235], v[212:215]\n"
        "s_waitcnt lgkmcnt(3)\n"
        "s_waitcnt vmcnt(0)\n"
        "s_barrier\n"
        "v_exp_f32 v74, v74\n"
        "v_mfma_f32_16x16x32_bf16 v[212:215], v[160:163], a[220:223], v[212:215]\n"
        "v_exp_f32 v75, v75\n"
        "v_mfma_f32_16x16x32_bf16 v[192:195], v[164:167], a[220:223], v[192:195]\n"
        "ds_read_b128 a[220:223], v248 offset:49216\n"
        "ds_read_b128 a[236:239], v240 offset:0\n"
        "v_mfma_f32_16x16x32_bf16 v[192:195], v[156:159], a[232:235], v[192:195]\n"
        "s_waitcnt lgkmcnt(4)\n"
        "ds_read_b128 a[232:235], v254 offset:12368\n"
        "ds_read_b128 a[240:243], v241 offset:0\n"
        "v_mfma_f32_16x16x32_bf16 v[196:199], v[156:159], a[216:219], v[196:199]\n"
        "s_waitcnt lgkmcnt(5)\n"
        "ds_read_b128 a[244:247], v240 offset:128\n"
        "v_cvt_pk_bf16_f32 v69, v74, v75\n"
        "v_mfma_f32_16x16x32_bf16 v[196:199], v[164:167], a[228:231], v[196:199]\n"
        "ds_read_b128 a[248:251], v241 offset:128\n"
        "v_add_u32 v246, s45, v246\n"
        "v_mfma_f32_16x16x32_bf16 v[216:219], v[160:163], a[228:231], v[216:219]\n"
        "ds_read_b128 a[228:231], v254 offset:49232\n"
        "v_add_u32 v247, s45, v247\n"
        "v_mfma_f32_16x16x32_bf16 v[216:219], v[152:155], a[216:219], v[216:219]\n"
        "s_waitcnt lgkmcnt(7)\n"
        "ds_read_b128 a[216:219], v240 offset:256\n"
        "v_mfma_f32_16x16x32_bf16 v[220:223], v[152:155], a[224:227], v[220:223]\n"
        "s_waitcnt lgkmcnt(7)\n"
        "v_mfma_f32_16x16x32_bf16 v[220:223], v[160:163], a[220:223], v[220:223]\n"
        "v_mfma_f32_16x16x32_bf16 v[200:203], v[164:167], a[220:223], v[200:203]\n"
        "ds_read_b128 a[220:223], v241 offset:256\n"
        "v_mfma_f32_16x16x32_bf16 v[200:203], v[156:159], a[224:227], v[200:203]\n"
        "s_waitcnt lgkmcnt(6)\n"
        "v_mfma_f32_16x16x32_bf16 v[204:207], v[156:159], a[232:235], v[204:207]\n"
        "s_waitcnt lgkmcnt(2)\n"
        "v_mfma_f32_16x16x32_bf16 v[204:207], v[164:167], a[228:231], v[204:207]\n"
        "v_mfma_f32_16x16x32_bf16 v[224:227], v[160:163], a[228:231], v[224:227]\n"
        "v_mfma_f32_16x16x32_bf16 v[224:227], v[152:155], a[232:235], v[224:227]\n"
        "s_waitcnt lgkmcnt(0)\n"
        ".p2align 6\n"
        "10:\n"
        "v_mfma_f32_16x16x32_bf16 v[162:165], a[72:75], a[236:239], 0\n"
        "v_fma_f32 v76, v76, s24, v236\n"
        "v_fma_f32 v77, v77, s24, v236\n"
        "v_mfma_f32_16x16x32_bf16 v[162:165], a[76:79], a[240:243], v[162:165]\n"
        "s_waitcnt lgkmcnt(3)\n"
        "s_add_u32 s41, s30, s40\n"
        "v_cvt_pk_bf16_f32 v232, v172, v176\n"
        "v_cvt_pk_bf16_f32 v233, v212, v216\n"
        "v_mfma_f32_16x16x32_bf16 v[162:165], a[80:83], a[244:247], v[162:165]\n"
        "s_waitcnt lgkmcnt(2)\n"
        "v_cvt_pk_bf16_f32 v234, v220, v224\n"
        "v_fma_f32 v78, v78, s24, v236\n"
        "v_mfma_f32_16x16x32_bf16 v[162:165], a[84:87], a[248:251], v[162:165]\n"
        "s_waitcnt lgkmcnt(1)\n"
        "buffer_store_dwordx3 v[232:234], v250, s[16:19], s38 offen offset:0 sc1\n"
        "v_mfma_f32_16x16x32_bf16 v[162:165], a[88:91], a[216:219], v[162:165]\n"
        "s_waitcnt lgkmcnt(0)\n"
        "v_exp_f32 v76, v76\n"
        "v_mfma_f32_16x16x32_bf16 v[162:165], a[92:95], a[220:223], v[162:165]\n"
        "v_exp_f32 v77, v77\n"
        "v_mfma_f32_16x16x32_bf16 v[72:75], a[116:119], a[220:223], 0\n"
        "v_fma_f32 v79, v79, s24, v236\n"
        "v_cvt_pk_bf16_f32 v70, v76, v77\n"
        "v_mfma_f32_16x16x32_bf16 v[72:75], a[112:115], a[216:219], v[72:75]\n"
        "v_exp_f32 v78, v78\n"
        "v_mfma_f32_16x16x32_bf16 v[72:75], a[108:111], a[248:251], v[72:75]\n"
        "v_exp_f32 v79, v79\n"
        "v_mfma_f32_16x16x32_bf16 v[72:75], a[104:107], a[244:247], v[72:75]\n"
        "v_cvt_pk_bf16_f32 v71, v78, v79\n"
        "v_cvt_pk_bf16_f32 v232, v173, v177\n"
        "v_mfma_f32_16x16x32_bf16 v[72:75], a[100:103], a[240:243], v[72:75]\n"
        "v_cvt_pk_bf16_f32 v233, v213, v217\n"
        "v_cvt_pk_bf16_f32 v234, v221, v225\n"
        "v_mfma_f32_16x16x32_bf16 v[72:75], a[96:99], a[236:239], v[72:75]\n"
        "buffer_store_dwordx3 v[232:234], v250, s[16:19], s38 offen offset:384 sc1\n"
        "v_mfma_f32_16x16x32_bf16 v[76:79], a[120:123], a[236:239], 0\n"
        "s_add_u32 s35, s47, 786432\n"
        "v_cvt_pk_bf16_f32 v152, v162, v163\n"
        "v_cvt_pk_bf16_f32 v153, v164, v165\n"
        "v_mfma_f32_16x16x32_bf16 v[76:79], a[124:127], a[240:243], v[76:79]\n"
        "s_sub_u32 s46, s35, 100663296\n"
        "v_fma_f32 v80, v80, s24, v237\n"
        "v_cvt_pk_bf16_f32 v154, v72, v73\n"
        "v_mfma_f32_16x16x32_bf16 v[76:79], a[128:131], a[244:247], v[76:79]\n"
        "s_cmp_lt_u32 s46, 786432\n"
        "v_exp_f32 v80, v80\n"
        "v_mfma_f32_16x16x32_bf16 v[76:79], a[132:135], a[248:251], v[76:79]\n"
        "s_cselect_b32 s35, s46, s35\n"
        "v_cvt_pk_bf16_f32 v155, v74, v75\n"
        "v_cvt_pk_bf16_f32 v232, v174, v178\n"
        "v_mfma_f32_16x16x32_bf16 v[76:79], a[136:139], a[216:219], v[76:79]\n"
        "v_cvt_pk_bf16_f32 v233, v214, v218\n"
        "v_cvt_pk_bf16_f32 v234, v222, v226\n"
        "v_mfma_f32_16x16x32_bf16 v[76:79], a[140:143], a[220:223], v[76:79]\n"
        "v_fma_f32 v81, v81, s24, v237\n"
        "v_fma_f32 v82, v82, s24, v237\n"
        "v_mfma_f32_16x16x32_bf16 v[162:165], a[20:23], a[220:223], 0\n"
        "v_exp_f32 v81, v81\n"
        "v_mfma_f32_16x16x32_bf16 v[162:165], a[16:19], a[216:219], v[162:165]\n"
        "v_exp_f32 v82, v82\n"
        "v_mfma_f32_16x16x32_bf16 v[162:165], a[12:15], a[248:251], v[162:165]\n"
        "buffer_store_dwordx3 v[232:234], v250, s[16:19], s38 offen offset:768 sc1\n"
        "v_mfma_f32_16x16x32_bf16 v[162:165], a[8:11], a[244:247], v[162:165]\n"
        "v_cvt_pk_bf16_f32 v168, v76, v77\n"
        "v_cvt_pk_bf16_f32 v169, v78, v79\n"
        "v_mfma_f32_16x16x32_bf16 v[162:165], a[4:7], a[240:243], v[162:165]\n"
        "v_cvt_pk_bf16_f32 v80, v80, v81\n"
        "v_fma_f32 v83, v83, s24, v237\n"
        "v_mfma_f32_16x16x32_bf16 v[162:165], a[0:3], a[236:239], v[162:165]\n"
        "v_exp_f32 v83, v83\n"
        "v_mfma_f32_16x16x32_bf16 v[72:75], a[24:27], a[236:239], 0\n"
        "v_cvt_pk_bf16_f32 v232, v175, v179\n"
        "v_cvt_pk_bf16_f32 v233, v215, v219\n"
        "v_mfma_f32_16x16x32_bf16 v[72:75], a[28:31], a[240:243], v[72:75]\n"
        "v_cvt_pk_bf16_f32 v234, v223, v227\n"
        "v_cvt_pk_bf16_f32 v81, v82, v83\n"
        "v_mfma_f32_16x16x32_bf16 v[72:75], a[32:35], a[244:247], v[72:75]\n"
        "v_cvt_pk_bf16_f32 v156, v162, v163\n"
        "v_cvt_pk_bf16_f32 v157, v164, v165\n"
        "v_mfma_f32_16x16x32_bf16 v[72:75], a[36:39], a[248:251], v[72:75]\n"
        "v_fma_f32 v84, v84, s24, v237\n"
        "v_fma_f32 v85, v85, s24, v237\n"
        "v_mfma_f32_16x16x32_bf16 v[72:75], a[40:43], a[216:219], v[72:75]\n"
        "v_exp_f32 v84, v84\n"
        "v_mfma_f32_16x16x32_bf16 v[72:75], a[44:47], a[220:223], v[72:75]\n"
        "buffer_store_dwordx3 v[232:234], v250, s[16:19], s38 offen offset:1152 sc1\n"
        "v_mfma_f32_16x16x32_bf16 v[76:79], a[68:71], a[220:223], 0\n"
        "v_exp_f32 v85, v85\n"
        "v_mfma_f32_16x16x32_bf16 v[76:79], a[64:67], a[216:219], v[76:79]\n"
        "v_cvt_pk_bf16_f32 v82, v84, v85\n"
        "v_fma_f32 v86, v86, s24, v237\n"
        "v_mfma_f32_16x16x32_bf16 v[76:79], a[60:63], a[248:251], v[76:79]\n"
        "ds_read_b128 a[224:227], v240 offset:1536\n"
        "v_cvt_pk_bf16_f32 v158, v72, v73\n"
        "v_mfma_f32_16x16x32_bf16 v[76:79], a[56:59], a[244:247], v[76:79]\n"
        "ds_read_b128 a[228:231], v241 offset:1536\n"
        "v_cvt_pk_bf16_f32 v159, v74, v75\n"
        "v_mfma_f32_16x16x32_bf16 v[76:79], a[52:55], a[240:243], v[76:79]\n"
        "ds_read_b128 a[232:235], v240 offset:1664\n"
        "v_cvt_pk_bf16_f32 v232, v180, v184\n"
        "v_mfma_f32_16x16x32_bf16 v[76:79], a[48:51], a[236:239], v[76:79]\n"
        "v_cvt_pk_bf16_f32 v233, v192, v196\n"
        "v_cvt_pk_bf16_f32 v234, v200, v204\n"
        "v_mfma_f32_16x16x32_bf16 v[162:165], a[236:239], a[144:147], 0\n"
        "v_exp_f32 v86, v86\n"
        "v_mfma_f32_16x16x32_bf16 v[162:165], a[240:243], a[148:151], v[162:165]\n"
        "v_fma_f32 v87, v87, s24, v237\n"
        "v_fma_f32 v88, v88, s24, v237\n"
        "v_mfma_f32_16x16x32_bf16 v[162:165], a[244:247], a[152:155], v[162:165]\n"
        "buffer_store_dwordx3 v[232:234], v250, s[16:19], s39 offen offset:0 sc1\n"
        "v_mfma_f32_16x16x32_bf16 v[162:165], a[248:251], a[156:159], v[162:165]\n"
        "v_exp_f32 v87, v87\n"
        "v_mfma_f32_16x16x32_bf16 v[162:165], a[216:219], a[160:163], v[162:165]\n"
        "v_cvt_pk_bf16_f32 v160, v76, v77\n"
        "v_cvt_pk_bf16_f32 v161, v78, v79\n"
        "v_mfma_f32_16x16x32_bf16 v[162:165], a[220:223], a[164:167], v[162:165]\n"
        "v_cvt_pk_bf16_f32 v166, v76, v77\n"
        "v_cvt_pk_bf16_f32 v167, v78, v79\n"
        "v_mfma_f32_16x16x32_bf16 v[72:75], a[220:223], a[188:191], 0\n"
        "v_exp_f32 v88, v88\n"
        "v_mfma_f32_16x16x32_bf16 v[72:75], a[216:219], a[184:187], v[72:75]\n"
        "v_cvt_pk_bf16_f32 v232, v181, v185\n"
        "v_cvt_pk_bf16_f32 v233, v193, v197\n"
        "v_mfma_f32_16x16x32_bf16 v[72:75], a[248:251], a[180:183], v[72:75]\n"
        "v_cvt_pk_bf16_f32 v234, v201, v205\n"
        "v_cvt_pk_bf16_f32 v212, v162, v163\n"
        "v_mfma_f32_16x16x32_bf16 v[72:75], a[244:247], a[176:179], v[72:75]\n"
        "v_cvt_pk_bf16_f32 v213, v164, v165\n"
        "v_cvt_pk_bf16_f32 v83, v86, v87\n"
        "v_mfma_f32_16x16x32_bf16 v[72:75], a[240:243], a[172:175], v[72:75]\n"
        "v_fma_f32 v89, v89, s24, v237\n"
        "v_fma_f32 v90, v90, s24, v237\n"
        "v_mfma_f32_16x16x32_bf16 v[72:75], a[236:239], a[168:171], v[72:75]\n"
        "buffer_store_dwordx3 v[232:234], v250, s[16:19], s39 offen offset:384 sc1\n"
        "v_mfma_f32_16x16x32_bf16 v[76:79], a[236:239], a[192:195], 0\n"
        "ds_read_b128 a[236:239], v241 offset:1664\n"
        "v_fma_f32 v91, v91, s24, v237\n"
        "v_mfma_f32_16x16x32_bf16 v[76:79], a[240:243], a[196:199], v[76:79]\n"
        "ds_read_b128 a[240:243], v240 offset:1792\n"
        "v_fma_f32 v92, v92, s24, v237\n"
        "v_mfma_f32_16x16x32_bf16 v[76:79], a[244:247], a[200:203], v[76:79]\n"
        "ds_read_b128 a[244:247], v241 offset:1792\n"
        "v_cvt_pk_bf16_f32 v214, v72, v73\n"
        "v_mfma_f32_16x16x32_bf16 v[76:79], a[248:251], a[204:207], v[76:79]\n"
        "v_exp_f32 v89, v89\n"
        "v_mfma_f32_16x16x32_bf16 v[76:79], a[216:219], a[208:211], v[76:79]\n"
        "v_cvt_pk_bf16_f32 v215, v74, v75\n"
        "v_cvt_pk_bf16_f32 v232, v182, v186\n"
        "v_mfma_f32_16x16x32_bf16 v[76:79], a[220:223], a[212:215], v[76:79]\n"
        "s_waitcnt lgkmcnt(5)\n"
        "v_cvt_pk_bf16_f32 v233, v194, v198\n"
        "v_cvt_pk_bf16_f32 v234, v202, v206\n"
        "v_mfma_f32_16x16x32_bf16 v[162:165], a[72:75], a[224:227], 0\n"
        "s_waitcnt lgkmcnt(4)\n"
        "v_exp_f32 v90, v90\n"
        "v_mfma_f32_16x16x32_bf16 v[162:165], a[76:79], a[228:231], v[162:165]\n"
        "s_waitcnt lgkmcnt(3)\n"
        "v_exp_f32 v91, v91\n"
        "v_mfma_f32_16x16x32_bf16 v[162:165], a[80:83], a[232:235], v[162:165]\n"
        "s_waitcnt lgkmcnt(2)\n"
        "buffer_store_dwordx3 v[232:234], v250, s[16:19], s39 offen offset:768 sc1\n"
        "v_mfma_f32_16x16x32_bf16 v[162:165], a[84:87], a[236:239], v[162:165]\n"
        "s_waitcnt lgkmcnt(1)\n"
        "v_exp_f32 v92, v92\n"
        "v_mfma_f32_16x16x32_bf16 v[162:165], a[88:91], a[240:243], v[162:165]\n"
        "s_waitcnt lgkmcnt(0)\n"
        "v_cvt_pk_bf16_f32 v216, v76, v77\n"
        "v_cvt_pk_bf16_f32 v217, v78, v79\n"
        "v_mfma_f32_16x16x32_bf16 v[162:165], a[92:95], a[244:247], v[162:165]\n"
        "v_cvt_pk_bf16_f32 v232, v183, v187\n"
        "v_cvt_pk_bf16_f32 v233, v195, v199\n"
        "v_mfma_f32_16x16x32_bf16 v[72:75], a[116:119], a[244:247], 0\n"
        "v_cvt_pk_bf16_f32 v234, v203, v207\n"
        "v_cvt_pk_bf16_f32 v84, v88, v89\n"
        "v_mfma_f32_16x16x32_bf16 v[72:75], a[112:115], a[240:243], v[72:75]\n"
        "v_cvt_pk_bf16_f32 v85, v90, v91\n"
        "v_fma_f32 v93, v93, s24, v237\n"
        "v_mfma_f32_16x16x32_bf16 v[72:75], a[108:111], a[236:239], v[72:75]\n"
        "v_exp_f32 v93, v93\n"
        "v_mfma_f32_16x16x32_bf16 v[72:75], a[104:107], a[232:235], v[72:75]\n"
        "v_cvt_pk_bf16_f32 v172, v162, v163\n"
        "v_cvt_pk_bf16_f32 v173, v164, v165\n"
        "v_mfma_f32_16x16x32_bf16 v[72:75], a[100:103], a[228:231], v[72:75]\n"
        "v_cvt_pk_bf16_f32 v86, v92, v93\n"
        "v_fma_f32 v94, v94, s24, v237\n"
        "v_mfma_f32_16x16x32_bf16 v[72:75], a[96:99], a[224:227], v[72:75]\n"
        "buffer_store_dwordx3 v[232:234], v250, s[16:19], s39 offen offset:1152 sc1\n"
        "v_mfma_f32_16x16x32_bf16 v[76:79], a[120:123], a[224:227], 0\n"
        "v_exp_f32 v94, v94\n"
        "v_mfma_f32_16x16x32_bf16 v[76:79], a[124:127], a[228:231], v[76:79]\n"
        "v_fma_f32 v95, v95, s24, v237\n"
        "v_fma_f32 v96, v96, s24, v238\n"
        "v_mfma_f32_16x16x32_bf16 v[76:79], a[128:131], a[232:235], v[76:79]\n"
        "v_exp_f32 v95, v95\n"
        "v_mfma_f32_16x16x32_bf16 v[76:79], a[132:135], a[236:239], v[76:79]\n"
        "v_exp_f32 v96, v96\n"
        "v_mfma_f32_16x16x32_bf16 v[76:79], a[136:139], a[240:243], v[76:79]\n"
        "v_cvt_pk_bf16_f32 v174, v72, v73\n"
        "v_cvt_pk_bf16_f32 v175, v74, v75\n"
        "v_mfma_f32_16x16x32_bf16 v[76:79], a[140:143], a[244:247], v[76:79]\n"
        "v_cvt_pk_bf16_f32 v87, v94, v95\n"
        "v_fma_f32 v97, v97, s24, v238\n"
        "v_mfma_f32_16x16x32_bf16 v[162:165], a[20:23], a[244:247], 0\n"
        "s_mov_b32 m0, s48\n"
        "v_exp_f32 v97, v97\n"
        "v_mfma_f32_16x16x32_bf16 v[162:165], a[16:19], a[240:243], v[162:165]\n"
        "v_cvt_pk_bf16_f32 v96, v96, v97\n"
        "v_fma_f32 v98, v98, s24, v238\n"
        "v_mfma_f32_16x16x32_bf16 v[162:165], a[12:15], a[236:239], v[162:165]\n"
        "buffer_load_dwordx4 v249, s[12:15], s36 offen offset:0 lds\n"
        "v_mfma_f32_16x16x32_bf16 v[162:165], a[8:11], a[232:235], v[162:165]\n"
        "v_exp_f32 v98, v98\n"
        "v_mfma_f32_16x16x32_bf16 v[162:165], a[4:7], a[228:231], v[162:165]\n"
        "v_cvt_pk_bf16_f32 v190, v76, v77\n"
        "v_cvt_pk_bf16_f32 v191, v78, v79\n"
        "v_mfma_f32_16x16x32_bf16 v[162:165], a[0:3], a[224:227], v[162:165]\n"
        "v_fma_f32 v99, v99, s24, v238\n"
        "v_fma_f32 v100, v100, s24, v238\n"
        "v_mfma_f32_16x16x32_bf16 v[72:75], a[24:27], a[224:227], 0\n"
        "v_exp_f32 v99, v99\n"
        "v_mfma_f32_16x16x32_bf16 v[72:75], a[28:31], a[228:231], v[72:75]\n"
        "v_exp_f32 v100, v100\n"
        "v_mfma_f32_16x16x32_bf16 v[72:75], a[32:35], a[232:235], v[72:75]\n"
        "v_cvt_pk_bf16_f32 v97, v98, v99\n"
        "v_cvt_pk_bf16_f32 v176, v162, v163\n"
        "v_mfma_f32_16x16x32_bf16 v[72:75], a[36:39], a[236:239], v[72:75]\n"
        "v_cvt_pk_bf16_f32 v177, v164, v165\n"
        "v_fma_f32 v101, v101, s24, v238\n"
        "v_mfma_f32_16x16x32_bf16 v[72:75], a[40:43], a[240:243], v[72:75]\n"
        "v_exp_f32 v101, v101\n"
        "v_mfma_f32_16x16x32_bf16 v[72:75], a[44:47], a[244:247], v[72:75]\n"
        "v_cvt_pk_bf16_f32 v98, v100, v101\n"
        "v_fma_f32 v102, v102, s24, v238\n"
        "v_mfma_f32_16x16x32_bf16 v[76:79], a[68:71], a[244:247], 0\n"
        "v_xor_b32 v253, 32, v249\n"
        "v_fma_f32 v103, v103, s24, v238\n"
        "v_mfma_f32_16x16x32_bf16 v[76:79], a[64:67], a[240:243], v[76:79]\n"
        "v_exp_f32 v102, v102\n"
        "v_mfma_f32_16x16x32_bf16 v[76:79], a[60:63], a[236:239], v[76:79]\n"
        "ds_read_b128 a[248:251], v240 offset:12288\n"
        "v_cvt_pk_bf16_f32 v178, v72, v73\n"
        "v_mfma_f32_16x16x32_bf16 v[76:79], a[56:59], a[232:235], v[76:79]\n"
        "ds_read_b128 a[216:219], v241 offset:12288\n"
        "v_cvt_pk_bf16_f32 v179, v74, v75\n"
        "v_mfma_f32_16x16x32_bf16 v[76:79], a[52:55], a[228:231], v[76:79]\n"
        "buffer_load_dwordx4 v253, s[12:15], s36 offen offset:1024 lds\n"
        "v_mfma_f32_16x16x32_bf16 v[76:79], a[48:51], a[224:227], v[76:79]\n"
        "ds_read_b128 a[220:223], v240 offset:12416\n"
        "v_fma_f32 v104, v104, s24, v238\n"
        "v_mfma_f32_16x16x32_bf16 v[162:165], a[224:227], a[144:147], 0\n"
        "v_exp_f32 v103, v103\n"
        "v_mfma_f32_16x16x32_bf16 v[162:165], a[228:231], a[148:151], v[162:165]\n"
        "v_exp_f32 v104, v104\n"
        "v_mfma_f32_16x16x32_bf16 v[162:165], a[232:235], a[152:155], v[162:165]\n"
        "v_cvt_pk_bf16_f32 v99, v102, v103\n"
        "v_cvt_pk_bf16_f32 v180, v76, v77\n"
        "v_mfma_f32_16x16x32_bf16 v[162:165], a[236:239], a[156:159], v[162:165]\n"
        "v_cvt_pk_bf16_f32 v181, v78, v79\n"
        "v_cvt_pk_bf16_f32 v186, v76, v77\n"
        "v_mfma_f32_16x16x32_bf16 v[162:165], a[240:243], a[160:163], v[162:165]\n"
        "v_cvt_pk_bf16_f32 v187, v78, v79\n"
        "v_fma_f32 v105, v105, s24, v238\n"
        "v_mfma_f32_16x16x32_bf16 v[162:165], a[244:247], a[164:167], v[162:165]\n"
        "v_exp_f32 v105, v105\n"
        "v_mfma_f32_16x16x32_bf16 v[72:75], a[244:247], a[188:191], 0\n"
        "v_cvt_pk_bf16_f32 v100, v104, v105\n"
        "v_fma_f32 v106, v106, s24, v238\n"
        "v_mfma_f32_16x16x32_bf16 v[72:75], a[240:243], a[184:187], v[72:75]\n"
        "v_exp_f32 v106, v106\n"
        "v_mfma_f32_16x16x32_bf16 v[72:75], a[236:239], a[180:183], v[72:75]\n"
        "v_fma_f32 v107, v107, s24, v238\n"
        "v_cvt_pk_bf16_f32 v218, v162, v163\n"
        "v_mfma_f32_16x16x32_bf16 v[72:75], a[232:235], a[176:179], v[72:75]\n"
        "v_exp_f32 v107, v107\n"
        "v_mfma_f32_16x16x32_bf16 v[72:75], a[228:231], a[172:175], v[72:75]\n"
        "s_add_u32 m0, s41, 0\n"
        "v_cvt_pk_bf16_f32 v219, v164, v165\n"
        "v_cvt_pk_bf16_f32 v101, v106, v107\n"
        "v_mfma_f32_16x16x32_bf16 v[72:75], a[224:227], a[168:171], v[72:75]\n"
        "v_fma_f32 v108, v108, s24, v238\n"
        "v_fma_f32 v109, v109, s24, v238\n"
        "v_mfma_f32_16x16x32_bf16 v[76:79], a[224:227], a[192:195], 0\n"
        "ds_read_b128 a[224:227], v241 offset:12416\n"
        "v_fma_f32 v110, v110, s24, v238\n"
        "v_mfma_f32_16x16x32_bf16 v[76:79], a[228:231], a[196:199], v[76:79]\n"
        "ds_read_b128 a[228:231], v240 offset:12544\n"
        "v_fma_f32 v111, v111, s24, v238\n"
        "v_mfma_f32_16x16x32_bf16 v[76:79], a[232:235], a[200:203], v[76:79]\n"
        "buffer_load_dwordx4 v242, s[8:11], s47 offen lds\n"
        "s_sub_u32 s31, s30, s28\n"
        "v_mfma_f32_16x16x32_bf16 v[76:79], a[236:239], a[204:207], v[76:79]\n"
        "ds_read_b128 a[232:235], v241 offset:12544\n"
        "s_sub_u32 s45, s28, s29\n"
        "v_cvt_pk_bf16_f32 v220, v72, v73\n"
        "v_mfma_f32_16x16x32_bf16 v[76:79], a[240:243], a[208:211], v[76:79]\n"
        "s_add_u32 s28, s28, s31\n"
        "v_cvt_pk_bf16_f32 v221, v74, v75\n"
        "v_fma_f32 v112, v112, s24, v239\n"
        "v_mfma_f32_16x16x32_bf16 v[76:79], a[244:247], a[212:215], v[76:79]\n"
        "s_waitcnt lgkmcnt(5)\n"
        "v_exp_f32 v108, v108\n"
        "v_mfma_f32_16x16x32_bf16 v[162:165], a[72:75], a[248:251], 0\n"
        "s_waitcnt lgkmcnt(4)\n"
        "v_exp_f32 v109, v109\n"
        "v_mfma_f32_16x16x32_bf16 v[162:165], a[76:79], a[216:219], v[162:165]\n"
        "s_waitcnt lgkmcnt(3)\n"
        "v_exp_f32 v110, v110\n"
        "v_mfma_f32_16x16x32_bf16 v[162:165], a[80:83], a[220:223], v[162:165]\n"
        "s_waitcnt lgkmcnt(2)\n"
        "v_exp_f32 v111, v111\n"
        "v_mfma_f32_16x16x32_bf16 v[162:165], a[84:87], a[224:227], v[162:165]\n"
        "s_waitcnt lgkmcnt(1)\n"
        "v_exp_f32 v112, v112\n"
        "v_mfma_f32_16x16x32_bf16 v[162:165], a[88:91], a[228:231], v[162:165]\n"
        "s_waitcnt lgkmcnt(0)\n"
        "v_cvt_pk_bf16_f32 v222, v76, v77\n"
        "v_cvt_pk_bf16_f32 v223, v78, v79\n"
        "v_mfma_f32_16x16x32_bf16 v[162:165], a[92:95], a[232:235], v[162:165]\n"
        "s_mov_b32 s30, s29\n"
        "v_cvt_pk_bf16_f32 v102, v108, v109\n"
        "v_cvt_pk_bf16_f32 v103, v110, v111\n"
        "v_mfma_f32_16x16x32_bf16 v[72:75], a[116:119], a[232:235], 0\n"
        "s_add_u32 m0, s41, 1024\n"
        "v_fma_f32 v113, v113, s24, v239\n"
        "v_fma_f32 v114, v114, s24, v239\n"
        "v_mfma_f32_16x16x32_bf16 v[72:75], a[112:115], a[228:231], v[72:75]\n"
        "s_add_u32 s29, s29, s45\n"
        "v_exp_f32 v113, v113\n"
        "v_mfma_f32_16x16x32_bf16 v[72:75], a[108:111], a[224:227], v[72:75]\n"
        "buffer_load_dwordx4 v243, s[8:11], s47 offen lds\n"
        "v_mfma_f32_16x16x32_bf16 v[72:75], a[104:107], a[220:223], v[72:75]\n"
        "v_exp_f32 v114, v114\n"
        "v_mfma_f32_16x16x32_bf16 v[72:75], a[100:103], a[216:219], v[72:75]\n"
        "v_cvt_pk_bf16_f32 v192, v162, v163\n"
        "v_cvt_pk_bf16_f32 v193, v164, v165\n"
        "v_mfma_f32_16x16x32_bf16 v[72:75], a[96:99], a[248:251], v[72:75]\n"
        "v_cvt_pk_bf16_f32 v112, v112, v113\n"
        "v_fma_f32 v115, v115, s24, v239\n"
        "v_mfma_f32_16x16x32_bf16 v[76:79], a[120:123], a[248:251], 0\n"
        "v_exp_f32 v115, v115\n"
        "v_mfma_f32_16x16x32_bf16 v[76:79], a[124:127], a[216:219], v[76:79]\n"
        "v_cvt_pk_bf16_f32 v113, v114, v115\n"
        "v_fma_f32 v116, v116, s24, v239\n"
        "v_mfma_f32_16x16x32_bf16 v[76:79], a[128:131], a[220:223], v[76:79]\n"
        "v_exp_f32 v116, v116\n"
        "v_mfma_f32_16x16x32_bf16 v[76:79], a[132:135], a[224:227], v[76:79]\n"
        "v_cvt_pk_bf16_f32 v194, v72, v73\n"
        "v_cvt_pk_bf16_f32 v195, v74, v75\n"
        "v_mfma_f32_16x16x32_bf16 v[76:79], a[136:139], a[228:231], v[76:79]\n"
        "v_fma_f32 v117, v117, s24, v239\n"
        "v_fma_f32 v118, v118, s24, v239\n"
        "v_mfma_f32_16x16x32_bf16 v[76:79], a[140:143], a[232:235], v[76:79]\n"
        "v_exp_f32 v117, v117\n"
        "v_mfma_f32_16x16x32_bf16 v[162:165], a[20:23], a[232:235], 0\n"
        "v_exp_f32 v118, v118\n"
        "v_mfma_f32_16x16x32_bf16 v[162:165], a[16:19], a[228:231], v[162:165]\n"
        "v_cvt_pk_bf16_f32 v114, v116, v117\n"
        "v_fma_f32 v119, v119, s24, v239\n"
        "v_mfma_f32_16x16x32_bf16 v[162:165], a[12:15], a[224:227], v[162:165]\n"
        "s_add_u32 m0, s41, 2048\n"
        "v_exp_f32 v119, v119\n"
        "v_mfma_f32_16x16x32_bf16 v[162:165], a[8:11], a[220:223], v[162:165]\n"
        "v_cvt_pk_bf16_f32 v208, v76, v77\n"
        "v_cvt_pk_bf16_f32 v209, v78, v79\n"
        "v_mfma_f32_16x16x32_bf16 v[162:165], a[4:7], a[216:219], v[162:165]\n"
        "buffer_load_dwordx4 v244, s[8:11], s47 offen lds\n"
        "v_mfma_f32_16x16x32_bf16 v[162:165], a[0:3], a[248:251], v[162:165]\n"
        "v_cvt_pk_bf16_f32 v115, v118, v119\n"
        "v_fma_f32 v120, v120, s24, v239\n"
        "v_mfma_f32_16x16x32_bf16 v[72:75], a[24:27], a[248:251], 0\n"
        "v_exp_f32 v120, v120\n"
        "v_mfma_f32_16x16x32_bf16 v[72:75], a[28:31], a[216:219], v[72:75]\n"
        "v_fma_f32 v121, v121, s24, v239\n"
        "v_fma_f32 v122, v122, s24, v239\n"
        "v_mfma_f32_16x16x32_bf16 v[72:75], a[32:35], a[220:223], v[72:75]\n"
        "v_exp_f32 v121, v121\n"
        "v_mfma_f32_16x16x32_bf16 v[72:75], a[36:39], a[224:227], v[72:75]\n"
        "v_exp_f32 v122, v122\n"
        "v_mfma_f32_16x16x32_bf16 v[72:75], a[40:43], a[228:231], v[72:75]\n"
        "v_cvt_pk_bf16_f32 v196, v162, v163\n"
        "v_cvt_pk_bf16_f32 v197, v164, v165\n"
        "v_mfma_f32_16x16x32_bf16 v[72:75], a[44:47], a[232:235], v[72:75]\n"
        "v_cvt_pk_bf16_f32 v116, v120, v121\n"
        "v_fma_f32 v123, v123, s24, v239\n"
        "v_mfma_f32_16x16x32_bf16 v[76:79], a[68:71], a[232:235], 0\n"
        "v_exp_f32 v123, v123\n"
        "v_mfma_f32_16x16x32_bf16 v[76:79], a[64:67], a[228:231], v[76:79]\n"
        "v_cvt_pk_bf16_f32 v117, v122, v123\n"
        "v_fma_f32 v124, v124, s24, v239\n"
        "v_mfma_f32_16x16x32_bf16 v[76:79], a[60:63], a[224:227], v[76:79]\n"
        "v_exp_f32 v124, v124\n"
        "v_mfma_f32_16x16x32_bf16 v[76:79], a[56:59], a[220:223], v[76:79]\n"
        "v_cvt_pk_bf16_f32 v198, v72, v73\n"
        "v_cvt_pk_bf16_f32 v199, v74, v75\n"
        "v_mfma_f32_16x16x32_bf16 v[76:79], a[52:55], a[216:219], v[76:79]\n"
        "s_add_u32 m0, s41, 12288\n"
        "v_fma_f32 v125, v125, s24, v239\n"
        "v_fma_f32 v126, v126, s24, v239\n"
        "v_mfma_f32_16x16x32_bf16 v[76:79], a[48:51], a[248:251], v[76:79]\n"
        "v_exp_f32 v125, v125\n"
        "v_mfma_f32_16x16x32_bf16 v[182:185], a[252:255], v[64:67], 0\n"
        "buffer_load_dwordx4 v242, s[8:11], s35 offen lds\n"
        "v_mfma_f32_16x16x32_bf16 v[182:185], a[252:255], v[68:71], v[182:185]\n"
        "v_exp_f32 v126, v126\n"
        "v_mfma_f32_16x16x32_bf16 v[202:205], v[132:135], v[68:71], 0\n"
        "v_cvt_pk_bf16_f32 v118, v124, v125\n"
        "v_fma_f32 v127, v127, s24, v239\n"
        "v_mfma_f32_16x16x32_bf16 v[202:205], v[128:131], v[64:67], v[202:205]\n"
        "v_exp_f32 v127, v127\n"
        "v_mfma_f32_16x16x32_bf16 v[88:91], v[136:139], v[64:67], 0\n"
        "v_cvt_pk_bf16_f32 v200, v76, v77\n"
        "v_cvt_pk_bf16_f32 v201, v78, v79\n"
        "v_mfma_f32_16x16x32_bf16 v[88:91], v[140:143], v[68:71], v[88:91]\n"
        "v_cvt_pk_bf16_f32 v206, v76, v77\n"
        "v_cvt_pk_bf16_f32 v207, v78, v79\n"
        "v_mfma_f32_16x16x32_bf16 v[92:95], v[148:151], v[68:71], 0\n"
        "s_cmp_eq_u32 s37, 100663296\n"
        "v_rcp_f32 v182, v182\n"
        "v_mfma_f32_16x16x32_bf16 v[92:95], v[144:147], v[64:67], v[92:95]\n"
        "s_cselect_b32 s46, 0, s37\n"
        "v_cvt_pk_bf16_f32 v119, v126, v127\n"
        "v_mul_f32 v202, v182, v202\n"
        "v_mfma_f32_16x16x32_bf16 v[64:67], a[252:255], v[80:83], 0\n"
        "s_add_u32 s38, s46, s43\n"
        "v_mul_f32 v203, v182, v203\n"
        "v_mul_f32 v204, v182, v204\n"
        "v_mfma_f32_16x16x32_bf16 v[64:67], a[252:255], v[84:87], v[64:67]\n"
        "s_add_u32 s39, s46, s44\n"
        "v_mul_f32 v205, v182, v205\n"
        "v_cvt_pk_bf16_f32 v202, v202, v203\n"
        "v_mfma_f32_16x16x32_bf16 v[68:71], v[132:135], v[84:87], 0\n"
        "s_add_u32 s37, s37, 6291456\n"
        "v_cvt_pk_bf16_f32 v203, v204, v205\n"
        "v_mul_f32 v88, v182, v88\n"
        "v_mfma_f32_16x16x32_bf16 v[68:71], v[128:131], v[80:83], v[68:71]\n"
        "v_rcp_f32 v64, v64\n"
        "v_mfma_f32_16x16x32_bf16 v[72:75], v[136:139], v[80:83], 0\n"
        "s_add_u32 m0, s41, 13312\n"
        "v_mul_f32 v89, v182, v89\n"
        "v_mul_f32 v90, v182, v90\n"
        "v_mfma_f32_16x16x32_bf16 v[72:75], v[140:143], v[84:87], v[72:75]\n"
        "v_mul_f32 v91, v182, v91\n"
        "v_cvt_pk_bf16_f32 v204, v88, v89\n"
        "v_mfma_f32_16x16x32_bf16 v[162:165], v[148:151], v[84:87], 0\n"
        "buffer_load_dwordx4 v243, s[8:11], s35 offen lds\n"
        "v_mfma_f32_16x16x32_bf16 v[162:165], v[144:147], v[80:83], v[162:165]\n"
        "v_cvt_pk_bf16_f32 v205, v90, v91\n"
        "v_mul_f32 v92, v182, v92\n"
        "v_mfma_f32_16x16x32_bf16 v[80:83], a[252:255], v[96:99], 0\n"
        "ds_write_b128 v245, v[202:205] offset:0\n"
        "v_mfma_f32_16x16x32_bf16 v[80:83], a[252:255], v[100:103], v[80:83]\n"
        "v_mul_f32 v93, v182, v93\n"
        "v_mul_f32 v94, v182, v94\n"
        "v_mfma_f32_16x16x32_bf16 v[84:87], v[132:135], v[100:103], 0\n"
        "v_mul_f32 v95, v182, v95\n"
        "v_cvt_pk_bf16_f32 v92, v92, v93\n"
        "v_mfma_f32_16x16x32_bf16 v[84:87], v[128:131], v[96:99], v[84:87]\n"
        "v_cvt_pk_bf16_f32 v93, v94, v95\n"
        "v_mul_f32 v68, v64, v68\n"
        "v_mfma_f32_16x16x32_bf16 v[76:79], v[136:139], v[96:99], 0\n"
        "v_rcp_f32 v80, v80\n"
        "v_mfma_f32_16x16x32_bf16 v[76:79], v[140:143], v[100:103], v[76:79]\n"
        "ds_write_b64 v246, v[92:93] offset:0\n"
        "v_mfma_f32_16x16x32_bf16 v[104:107], v[148:151], v[100:103], 0\n"
        "v_mul_f32 v69, v64, v69\n"
        "v_mul_f32 v70, v64, v70\n"
        "v_mfma_f32_16x16x32_bf16 v[104:107], v[144:147], v[96:99], v[104:107]\n"
        "v_mul_f32 v71, v64, v71\n"
        "v_cvt_pk_bf16_f32 v68, v68, v69\n"
        "v_mfma_f32_16x16x32_bf16 v[96:99], a[252:255], v[112:115], 0\n"
        "v_cvt_pk_bf16_f32 v69, v70, v71\n"
        "v_mul_f32 v72, v64, v72\n"
        "v_mfma_f32_16x16x32_bf16 v[96:99], a[252:255], v[116:119], v[96:99]\n"
        "v_mul_f32 v73, v64, v73\n"
        "v_mul_f32 v74, v64, v74\n"
        "v_mfma_f32_16x16x32_bf16 v[108:111], v[132:135], v[116:119], 0\n"
        "s_add_u32 m0, s41, 14336\n"
        "v_mul_f32 v75, v64, v75\n"
        "v_cvt_pk_bf16_f32 v70, v72, v73\n"
        "v_mfma_f32_16x16x32_bf16 v[108:111], v[128:131], v[112:115], v[108:111]\n"
        "v_mov_b32 v128, v212\n"
        "v_mov_b32 v129, v213\n"
        "v_mfma_f32_16x16x32_bf16 v[120:123], v[136:139], v[112:115], 0\n"
        "buffer_load_dwordx4 v244, s[8:11], s35 offen lds\n"
        "v_mfma_f32_16x16x32_bf16 v[120:123], v[140:143], v[116:119], v[120:123]\n"
        "ds_read_b128 a[236:239], v240 offset:13824\n"
        "v_mov_b32 v136, v214\n"
        "v_mfma_f32_16x16x32_bf16 v[124:127], v[148:151], v[116:119], 0\n"
        "ds_read_b128 a[240:243], v241 offset:13824\n"
        "v_mov_b32 v137, v215\n"
        "v_mfma_f32_16x16x32_bf16 v[124:127], v[144:147], v[112:115], v[124:127]\n"
        "ds_read_b128 a[244:247], v240 offset:13952\n"
        "v_mov_b32 v144, v216\n"
        "v_mfma_f32_16x16x32_bf16 v[100:103], a[248:251], a[144:147], 0\n"
        "v_mov_b32 v145, v217\n"
        "v_cvt_pk_bf16_f32 v71, v74, v75\n"
        "v_mfma_f32_16x16x32_bf16 v[100:103], a[216:219], a[148:151], v[100:103]\n"
        "ds_write_b128 v245, v[68:71] offset:256\n"
        "v_mfma_f32_16x16x32_bf16 v[100:103], a[220:223], a[152:155], v[100:103]\n"
        "v_mov_b32 v130, v218\n"
        "v_mov_b32 v131, v219\n"
        "v_mfma_f32_16x16x32_bf16 v[100:103], a[224:227], a[156:159], v[100:103]\n"
        "v_mov_b32 v138, v220\n"
        "v_mov_b32 v139, v221\n"
        "v_mfma_f32_16x16x32_bf16 v[100:103], a[228:231], a[160:163], v[100:103]\n"
        "v_mul_f32 v162, v64, v162\n"
        "v_mul_f32 v163, v64, v163\n"
        "v_mfma_f32_16x16x32_bf16 v[100:103], a[232:235], a[164:167], v[100:103]\n"
        "v_mul_f32 v164, v64, v164\n"
        "v_mul_f32 v165, v64, v165\n"
        "v_mfma_f32_16x16x32_bf16 v[112:115], a[232:235], a[188:191], 0\n"
        "v_mov_b32 v146, v222\n"
        "v_mov_b32 v147, v223\n"
        "v_mfma_f32_16x16x32_bf16 v[112:115], a[228:231], a[184:187], v[112:115]\n"
        "v_cvt_pk_bf16_f32 v162, v162, v163\n"
        "v_cvt_pk_bf16_f32 v163, v164, v165\n"
        "v_mfma_f32_16x16x32_bf16 v[112:115], a[224:227], a[180:183], v[112:115]\n"
        "ds_write_b64 v246, v[162:163] offset:256\n"
        "v_mfma_f32_16x16x32_bf16 v[112:115], a[220:223], a[176:179], v[112:115]\n"
        "v_cvt_pk_bf16_f32 v132, v100, v101\n"
        "v_cvt_pk_bf16_f32 v133, v102, v103\n"
        "v_mfma_f32_16x16x32_bf16 v[112:115], a[216:219], a[172:175], v[112:115]\n"
        "v_mul_f32 v84, v80, v84\n"
        "v_mul_f32 v85, v80, v85\n"
        "v_mfma_f32_16x16x32_bf16 v[112:115], a[248:251], a[168:171], v[112:115]\n"
        "v_rcp_f32 v96, v96\n"
        "v_mfma_f32_16x16x32_bf16 v[116:119], a[248:251], a[192:195], 0\n"
        "ds_read_b128 a[248:251], v241 offset:13952\n"
        "v_mul_f32 v86, v80, v86\n"
        "v_mfma_f32_16x16x32_bf16 v[116:119], a[216:219], a[196:199], v[116:119]\n"
        "ds_read_b128 a[216:219], v240 offset:14080\n"
        "v_mul_f32 v87, v80, v87\n"
        "v_mfma_f32_16x16x32_bf16 v[116:119], a[220:223], a[200:203], v[116:119]\n"
        "ds_read_b128 a[220:223], v241 offset:14080\n"
        "v_cvt_pk_bf16_f32 v140, v112, v113\n"
        "v_mfma_f32_16x16x32_bf16 v[116:119], a[224:227], a[204:207], v[116:119]\n"
        "v_cvt_pk_bf16_f32 v141, v114, v115\n"
        "v_cvt_pk_bf16_f32 v84, v84, v85\n"
        "v_mfma_f32_16x16x32_bf16 v[116:119], a[228:231], a[208:211], v[116:119]\n"
        "v_cvt_pk_bf16_f32 v85, v86, v87\n"
        "v_mul_f32 v76, v80, v76\n"
        "v_mfma_f32_16x16x32_bf16 v[116:119], a[232:235], a[212:215], v[116:119]\n"
        "s_waitcnt lgkmcnt(7)\n"
        "v_mul_f32 v77, v80, v77\n"
        "v_mul_f32 v78, v80, v78\n"
        "v_mfma_f32_16x16x32_bf16 v[100:103], a[236:239], a[144:147], 0\n"
        "s_waitcnt lgkmcnt(6)\n"
        "v_mul_f32 v79, v80, v79\n"
        "v_cvt_pk_bf16_f32 v86, v76, v77\n"
        "v_mfma_f32_16x16x32_bf16 v[100:103], a[240:243], a[148:151], v[100:103]\n"
        "s_waitcnt lgkmcnt(5)\n"
        "v_cvt_pk_bf16_f32 v87, v78, v79\n"
        "v_mul_f32 v104, v80, v104\n"
        "v_mfma_f32_16x16x32_bf16 v[100:103], a[244:247], a[152:155], v[100:103]\n"
        "s_waitcnt lgkmcnt(2)\n"
        "ds_write_b128 v245, v[84:87] offset:512\n"
        "v_mfma_f32_16x16x32_bf16 v[100:103], a[248:251], a[156:159], v[100:103]\n"
        "s_waitcnt lgkmcnt(2)\n"
        "v_cvt_pk_bf16_f32 v148, v116, v117\n"
        "v_cvt_pk_bf16_f32 v149, v118, v119\n"
        "v_mfma_f32_16x16x32_bf16 v[100:103], a[216:219], a[160:163], v[100:103]\n"
        "s_waitcnt lgkmcnt(1)\n"
        "v_mul_f32 v105, v80, v105\n"
        "v_mul_f32 v106, v80, v106\n"
        "v_mfma_f32_16x16x32_bf16 v[100:103], a[220:223], a[164:167], v[100:103]\n"
        "v_mul_f32 v107, v80, v107\n"
        "v_cvt_pk_bf16_f32 v104, v104, v105\n"
        "v_mfma_f32_16x16x32_bf16 v[112:115], a[220:223], a[188:191], 0\n"
        "v_cvt_pk_bf16_f32 v105, v106, v107\n"
        "v_mul_f32 v108, v96, v108\n"
        "v_mfma_f32_16x16x32_bf16 v[112:115], a[216:219], a[184:187], v[112:115]\n"
        "ds_write_b64 v246, v[104:105] offset:512\n"
        "v_mfma_f32_16x16x32_bf16 v[112:115], a[248:251], a[180:183], v[112:115]\n"
        "v_cvt_pk_bf16_f32 v134, v100, v101\n"
        "v_cvt_pk_bf16_f32 v135, v102, v103\n"
        "v_mfma_f32_16x16x32_bf16 v[112:115], a[244:247], a[176:179], v[112:115]\n"
        "v_mul_f32 v109, v96, v109\n"
        "v_mul_f32 v110, v96, v110\n"
        "v_mfma_f32_16x16x32_bf16 v[112:115], a[240:243], a[172:175], v[112:115]\n"
        "v_mul_f32 v111, v96, v111\n"
        "v_cvt_pk_bf16_f32 v108, v108, v109\n"
        "v_mfma_f32_16x16x32_bf16 v[112:115], a[236:239], a[168:171], v[112:115]\n"
        "v_cvt_pk_bf16_f32 v109, v110, v111\n"
        "v_mul_f32 v120, v96, v120\n"
        "v_mfma_f32_16x16x32_bf16 v[116:119], a[236:239], a[192:195], 0\n"
        "v_mul_f32 v121, v96, v121\n"
        "v_mul_f32 v122, v96, v122\n"
        "v_mfma_f32_16x16x32_bf16 v[116:119], a[240:243], a[196:199], v[116:119]\n"
        "v_mul_f32 v123, v96, v123\n"
        "v_cvt_pk_bf16_f32 v110, v120, v121\n"
        "v_mfma_f32_16x16x32_bf16 v[116:119], a[244:247], a[200:203], v[116:119]\n"
        "v_cvt_pk_bf16_f32 v142, v112, v113\n"
        "v_cvt_pk_bf16_f32 v143, v114, v115\n"
        "v_mfma_f32_16x16x32_bf16 v[116:119], a[248:251], a[204:207], v[116:119]\n"
        "v_cvt_pk_bf16_f32 v111, v122, v123\n"
        "v_mul_f32 v124, v96, v124\n"
        "v_mfma_f32_16x16x32_bf16 v[116:119], a[216:219], a[208:211], v[116:119]\n"
        "ds_write_b128 v245, v[108:111] offset:768\n"
        "v_mfma_f32_16x16x32_bf16 v[116:119], a[220:223], a[212:215], v[116:119]\n"
        "v_mul_f32 v125, v96, v125\n"
        "v_mul_f32 v126, v96, v126\n"
        "v_mfma_f32_16x16x32_bf16 v[100:103], a[20:23], a[220:223], 0\n"
        "v_mul_f32 v127, v96, v127\n"
        "v_cvt_pk_bf16_f32 v124, v124, v125\n"
        "v_mfma_f32_16x16x32_bf16 v[100:103], a[16:19], a[216:219], v[100:103]\n"
        "v_cvt_pk_bf16_f32 v125, v126, v127\n"
        "v_mfma_f32_16x16x32_bf16 v[100:103], a[12:15], a[248:251], v[100:103]\n"
        "v_cvt_pk_bf16_f32 v150, v116, v117\n"
        "v_cvt_pk_bf16_f32 v151, v118, v119\n"
        "v_mfma_f32_16x16x32_bf16 v[100:103], a[8:11], a[244:247], v[100:103]\n"
        "ds_write_b64 v246, v[124:125] offset:768\n"
        "v_mfma_f32_16x16x32_bf16 v[100:103], a[4:7], a[240:243], v[100:103]\n"
        "v_mfma_f32_16x16x32_bf16 v[100:103], a[0:3], a[236:239], v[100:103]\n"
        "v_mfma_f32_16x16x32_bf16 v[112:115], a[24:27], a[236:239], 0\n"
        "v_mfma_f32_16x16x32_bf16 v[112:115], a[28:31], a[240:243], v[112:115]\n"
        "v_mfma_f32_16x16x32_bf16 v[112:115], a[32:35], a[244:247], v[112:115]\n"
        "v_mfma_f32_16x16x32_bf16 v[112:115], a[36:39], a[248:251], v[112:115]\n"
        "v_mfma_f32_16x16x32_bf16 v[112:115], a[40:43], a[216:219], v[112:115]\n"
        "v_mfma_f32_16x16x32_bf16 v[112:115], a[44:47], a[220:223], v[112:115]\n"
        "v_mfma_f32_16x16x32_bf16 v[116:119], a[68:71], a[220:223], 0\n"
        "ds_read_b128 v[162:165], v251 offset:0\n"
        "v_cvt_pk_bf16_f32 v216, v100, v101\n"
        "v_mfma_f32_16x16x32_bf16 v[116:119], a[64:67], a[216:219], v[116:119]\n"
        "v_cvt_pk_bf16_f32 v217, v102, v103\n"
        "v_mfma_f32_16x16x32_bf16 v[116:119], a[60:63], a[248:251], v[116:119]\n"
        "v_mfma_f32_16x16x32_bf16 v[116:119], a[56:59], a[244:247], v[116:119]\n"
        "v_mfma_f32_16x16x32_bf16 v[116:119], a[52:55], a[240:243], v[116:119]\n"
        "ds_read_b128 v[182:185], v251 offset:512\n"
        "v_cvt_pk_bf16_f32 v218, v112, v113\n"
        "v_mfma_f32_16x16x32_bf16 v[116:119], a[48:51], a[236:239], v[116:119]\n"
        "v_cvt_pk_bf16_f32 v219, v114, v115\n"
        "v_mfma_f32_16x16x32_bf16 v[100:103], a[72:75], a[236:239], 0\n"
        "s_add_u32 s47, s47, 6291456\n"
        "v_mfma_f32_16x16x32_bf16 v[100:103], a[76:79], a[240:243], v[100:103]\n"
        "s_add_u32 s36, s36, 2097152\n"
        "v_mfma_f32_16x16x32_bf16 v[100:103], a[80:83], a[244:247], v[100:103]\n"
        "ds_read_b128 v[202:205], v251 offset:4096\n"
        "s_xor_b32 s48, s48, 8192\n"
        "v_cvt_pk_bf16_f32 v220, v116, v117\n"
        "v_mfma_f32_16x16x32_bf16 v[100:103], a[84:87], a[248:251], v[100:103]\n"
        "v_cvt_pk_bf16_f32 v221, v118, v119\n"
        "v_cvt_pk_bf16_f32 v226, v116, v117\n"
        "v_mfma_f32_16x16x32_bf16 v[100:103], a[88:91], a[216:219], v[100:103]\n"
        "v_cvt_pk_bf16_f32 v227, v118, v119\n"
        "v_mfma_f32_16x16x32_bf16 v[100:103], a[92:95], a[220:223], v[100:103]\n"
        "v_mfma_f32_16x16x32_bf16 v[112:115], a[116:119], a[220:223], 0\n"
        "ds_read_b128 v[222:225], v251 offset:4608\n"
        "v_mfma_f32_16x16x32_bf16 v[112:115], a[112:115], a[216:219], v[112:115]\n"
        "v_mfma_f32_16x16x32_bf16 v[112:115], a[108:111], a[248:251], v[112:115]\n"
        "s_waitcnt lgkmcnt(3)\n"
        "v_pk_min_u16 v162, v162, s26\n"
        "v_pk_min_u16 v163, v163, s26\n"
        "v_mfma_f32_16x16x32_bf16 v[112:115], a[104:107], a[244:247], v[112:115]\n"
        "v_pk_min_u16 v164, v164, s26\n"
        "v_cvt_pk_bf16_f32 v212, v100, v101\n"
        "v_mfma_f32_16x16x32_bf16 v[112:115], a[100:103], a[240:243], v[112:115]\n"
        "v_cvt_pk_bf16_f32 v213, v102, v103\n"
        "v_pk_min_u16 v165, v165, s26\n"
        "v_mfma_f32_16x16x32_bf16 v[112:115], a[96:99], a[236:239], v[112:115]\n"
        "v_add_u32 v240, s31, v240\n"
        "v_add_u32 v241, s31, v241\n"
        "v_mfma_f32_16x16x32_bf16 v[116:119], a[120:123], a[236:239], 0\n"
        "s_waitcnt lgkmcnt(2)\n"
        "v_pk_min_u16 v182, v182, s26\n"
        "v_pk_min_u16 v183, v183, s26\n"
        "v_mfma_f32_16x16x32_bf16 v[116:119], a[124:127], a[240:243], v[116:119]\n"
        "v_pk_min_u16 v184, v184, s26\n"
        "v_pk_min_u16 v185, v185, s26\n"
        "v_mfma_f32_16x16x32_bf16 v[116:119], a[128:131], a[244:247], v[116:119]\n"
        "v_cvt_pk_bf16_f32 v214, v112, v113\n"
        "v_cvt_pk_bf16_f32 v215, v114, v115\n"
        "v_mfma_f32_16x16x32_bf16 v[116:119], a[132:135], a[248:251], v[116:119]\n"
        "v_mfma_f32_16x16x32_bf16 v[116:119], a[136:139], a[216:219], v[116:119]\n"
        "s_waitcnt lgkmcnt(1)\n"
        "v_pk_min_u16 v202, v202, s26\n"
        "v_pk_min_u16 v203, v203, s26\n"
        "v_mfma_f32_16x16x32_bf16 v[116:119], a[140:143], a[220:223], v[116:119]\n"
        "v_pk_min_u16 v204, v204, s26\n"
        "v_pk_min_u16 v205, v205, s26\n"
        "v_mfma_f32_16x16x32_bf16 v[64:67], v[152:155], v[156:159], v[0:3]\n"
        "v_mfma_f32_16x16x32_bf16 v[64:67], v[168:171], v[160:163], v[64:67]\n"
        "v_mfma_f32_16x16x32_bf16 v[68:71], v[172:175], v[156:159], v[4:7]\n"
        "s_waitcnt lgkmcnt(0)\n"
        "v_pk_min_u16 v222, v222, s26\n"
        "v_pk_min_u16 v223, v223, s26\n"
        "v_mfma_f32_16x16x32_bf16 v[68:71], v[188:191], v[164:167], v[68:71]\n"
        "ds_read_b128 v[162:165], v252 offset:0\n"
        "v_cvt_pk_bf16_f32 v230, v116, v117\n"
        "v_mfma_f32_16x16x32_bf16 v[80:83], v[152:155], v[176:179], v[16:19]\n"
        "v_cvt_pk_bf16_f32 v231, v118, v119\n"
        "v_pk_min_u16 v224, v224, s26\n"
        "v_mfma_f32_16x16x32_bf16 v[80:83], v[168:171], v[180:183], v[80:83]\n"
        "v_pk_min_u16 v225, v225, s26\n"
        "v_max3_f32 v232, v64, v65, v66\n"
        "v_mfma_f32_16x16x32_bf16 v[84:87], v[172:175], v[176:179], v[20:23]\n"
        "v_max3_f32 v232, v232, v67, v68\n"
        "v_max3_f32 v232, v232, v69, v70\n"
        "v_mfma_f32_16x16x32_bf16 v[84:87], v[188:191], v[184:187], v[84:87]\n"
        "ds_read_b128 v[182:185], v252 offset:512\n"
        "v_mfma_f32_16x16x32_bf16 v[96:99], v[152:155], v[196:199], v[32:35]\n"
        "v_max3_f32 v233, v80, v81, v82\n"
        "v_mfma_f32_16x16x32_bf16 v[96:99], v[168:171], v[200:203], v[96:99]\n"
        "v_mfma_f32_16x16x32_bf16 v[100:103], v[172:175], v[196:199], v[36:39]\n"
        "v_mfma_f32_16x16x32_bf16 v[100:103], v[188:191], v[204:207], v[100:103]\n"
        "ds_read_b128 v[202:205], v252 offset:4096\n"
        "v_mfma_f32_16x16x32_bf16 v[112:115], v[152:155], v[216:219], v[48:51]\n"
        "s_waitcnt lgkmcnt(2)\n"
        "v_pk_min_u16 v162, v162, s26\n"
        "v_pk_min_u16 v163, v163, s26\n"
        "v_mfma_f32_16x16x32_bf16 v[112:115], v[168:171], v[220:223], v[112:115]\n"
        "v_pk_min_u16 v164, v164, s26\n"
        "v_pk_min_u16 v165, v165, s26\n"
        "v_mfma_f32_16x16x32_bf16 v[116:119], v[172:175], v[216:219], v[52:55]\n"
        "v_max3_f32 v233, v233, v83, v84\n"
        "v_max3_f32 v233, v233, v85, v86\n"
        "v_mfma_f32_16x16x32_bf16 v[116:119], v[188:191], v[224:227], v[116:119]\n"
        "ds_read_b128 v[222:225], v252 offset:4608\n"
        "v_max3_f32 v234, v96, v97, v98\n"
        "v_mfma_f32_16x16x32_bf16 v[72:75], v[192:195], v[156:159], v[8:11]\n"
        "s_waitcnt lgkmcnt(2)\n"
        "v_pk_min_u16 v182, v182, s26\n"
        "v_pk_min_u16 v183, v183, s26\n"
        "v_mfma_f32_16x16x32_bf16 v[72:75], v[208:211], v[160:163], v[72:75]\n"
        "v_pk_min_u16 v184, v184, s26\n"
        "v_pk_min_u16 v185, v185, s26\n"
        "v_mfma_f32_16x16x32_bf16 v[76:79], v[212:215], v[156:159], v[12:15]\n"
        "v_max3_f32 v234, v234, v99, v100\n"
        "v_max3_f32 v234, v234, v101, v102\n"
        "v_mfma_f32_16x16x32_bf16 v[76:79], v[228:231], v[164:167], v[76:79]\n"
        "v_max3_f32 v235, v112, v113, v114\n"
        "v_max3_f32 v235, v235, v115, v116\n"
        "v_mfma_f32_16x16x32_bf16 v[88:91], v[192:195], v[176:179], v[24:27]\n"
        "s_barrier\n"
        "ds_read_b128 v[152:155], v247 offset:0\n"
        "s_waitcnt lgkmcnt(2)\n"
        "v_pk_min_u16 v202, v202, s26\n"
        "v_mfma_f32_16x16x32_bf16 v[88:91], v[208:211], v[180:183], v[88:91]\n"
        "ds_read_b128 v[160:163], v247 offset:4096\n"
        "v_pk_min_u16 v203, v203, s26\n"
        "v_mfma_f32_16x16x32_bf16 v[92:95], v[212:215], v[176:179], v[28:31]\n"
        "ds_read_b128 v[156:159], v247 offset:256\n"
        "v_pk_min_u16 v204, v204, s26\n"
        "v_mfma_f32_16x16x32_bf16 v[92:95], v[228:231], v[184:187], v[92:95]\n"
        "ds_read_b128 v[164:167], v247 offset:4352\n"
        "v_pk_min_u16 v205, v205, s26\n"
        "v_mfma_f32_16x16x32_bf16 v[104:107], v[192:195], v[196:199], v[40:43]\n"
        "ds_read_b128 a[216:219], v248 offset:0\n"
        "s_waitcnt lgkmcnt(5)\n"
        "v_pk_min_u16 v222, v222, s26\n"
        "v_mfma_f32_16x16x32_bf16 v[104:107], v[208:211], v[200:203], v[104:107]\n"
        "ds_read_b128 a[220:223], v248 offset:6144\n"
        "v_pk_min_u16 v223, v223, s26\n"
        "v_mfma_f32_16x16x32_bf16 v[108:111], v[212:215], v[196:199], v[44:47]\n"
        "ds_read_b128 a[224:227], v254 offset:16\n"
        "v_pk_min_u16 v224, v224, s26\n"
        "v_mfma_f32_16x16x32_bf16 v[108:111], v[228:231], v[204:207], v[108:111]\n"
        "ds_read_b128 a[228:231], v254 offset:6160\n"
        "v_pk_min_u16 v225, v225, s26\n"
        "v_mfma_f32_16x16x32_bf16 v[120:123], v[192:195], v[216:219], v[56:59]\n"
        "ds_read_b128 a[232:235], v248 offset:32\n"
        "v_max3_f32 v232, v232, v71, v72\n"
        "v_mfma_f32_16x16x32_bf16 v[120:123], v[208:211], v[220:223], v[120:123]\n"
        "v_max3_f32 v232, v232, v73, v74\n"
        "v_max3_f32 v232, v232, v75, v76\n"
        "v_mfma_f32_16x16x32_bf16 v[124:127], v[212:215], v[216:219], v[60:63]\n"
        "v_max3_f32 v232, v232, v77, v78\n"
        "v_max_f32 v232, v232, v79\n"
        "v_mfma_f32_16x16x32_bf16 v[124:127], v[228:231], v[224:227], v[124:127]\n"
        "s_waitcnt lgkmcnt(4)\n"
        "v_max3_f32 v233, v233, v87, v88\n"
        "v_max3_f32 v233, v233, v89, v90\n"
        "v_mfma_f32_16x16x32_bf16 v[172:175], v[152:155], a[216:219], 0\n"
        "s_waitcnt lgkmcnt(3)\n"
        "v_max3_f32 v233, v233, v91, v92\n"
        "v_max3_f32 v233, v233, v93, v94\n"
        "v_mfma_f32_16x16x32_bf16 v[172:175], v[160:163], a[220:223], v[172:175]\n"
        "v_max_f32 v233, v233, v95\n"
        "v_max3_f32 v234, v234, v103, v104\n"
        "v_mfma_f32_16x16x32_bf16 v[180:183], v[164:167], a[220:223], 0\n"
        "ds_read_b128 a[220:223], v248 offset:6176\n"
        "v_max3_f32 v234, v234, v105, v106\n"
        "v_mfma_f32_16x16x32_bf16 v[180:183], v[156:159], a[216:219], v[180:183]\n"
        "s_waitcnt lgkmcnt(3)\n"
        "ds_read_b128 a[216:219], v254 offset:48\n"
        "v_max3_f32 v234, v234, v107, v108\n"
        "v_mfma_f32_16x16x32_bf16 v[184:187], v[156:159], a[224:227], 0\n"
        "s_waitcnt lgkmcnt(3)\n"
        "ds_read_b128 a[236:239], v247 offset:8192\n"
        "v_max3_f32 v234, v234, v109, v110\n"
        "v_mfma_f32_16x16x32_bf16 v[184:187], v[164:167], a[228:231], v[184:187]\n"
        "ds_read_b128 a[244:247], v247 offset:12288\n"
        "v_max_f32 v234, v234, v111\n"
        "v_mfma_f32_16x16x32_bf16 v[176:179], v[160:163], a[228:231], 0\n"
        "ds_read_b128 a[228:231], v254 offset:6192\n"
        "v_max3_f32 v235, v235, v117, v118\n"
        "v_mfma_f32_16x16x32_bf16 v[176:179], v[152:155], a[224:227], v[176:179]\n"
        "s_waitcnt lgkmcnt(5)\n"
        "ds_read_b128 a[224:227], v248 offset:64\n"
        "v_max3_f32 v235, v235, v119, v120\n"
        "v_mfma_f32_16x16x32_bf16 v[212:215], v[152:155], a[232:235], 0\n"
        "s_waitcnt lgkmcnt(5)\n"
        "ds_read_b128 a[240:243], v247 offset:8448\n"
        "v_max3_f32 v235, v235, v121, v122\n"
        "v_mfma_f32_16x16x32_bf16 v[212:215], v[160:163], a[220:223], v[212:215]\n"
        "ds_read_b128 a[248:251], v247 offset:12544\n"
        "v_max3_f32 v235, v235, v123, v124\n"
        "v_mfma_f32_16x16x32_bf16 v[192:195], v[164:167], a[220:223], 0\n"
        "ds_read_b128 a[220:223], v248 offset:6208\n"
        "v_max3_f32 v235, v235, v125, v126\n"
        "v_mfma_f32_16x16x32_bf16 v[192:195], v[156:159], a[232:235], v[192:195]\n"
        "s_waitcnt lgkmcnt(7)\n"
        "ds_read_b128 a[232:235], v254 offset:80\n"
        "v_max_f32 v235, v235, v127\n"
        "v_mfma_f32_16x16x32_bf16 v[196:199], v[156:159], a[216:219], 0\n"
        "s_waitcnt lgkmcnt(5)\n"
        "v_permlane32_swap_b32 v232, v233\n"
        "v_mfma_f32_16x16x32_bf16 v[196:199], v[164:167], a[228:231], v[196:199]\n"
        "v_max_f32 v232, v232, v233\n"
        "v_mov_b32 v233, v232\n"
        "v_mfma_f32_16x16x32_bf16 v[216:219], v[160:163], a[228:231], 0\n"
        "ds_read_b128 a[228:231], v254 offset:6224\n"
        "v_xor_b32 v251, 0x2000, v251\n"
        "v_mfma_f32_16x16x32_bf16 v[216:219], v[152:155], a[216:219], v[216:219]\n"
        "s_waitcnt lgkmcnt(5)\n"
        "ds_read_b128 a[216:219], v248 offset:36864\n"
        "v_xor_b32 v252, 0x2000, v252\n"
        "v_mfma_f32_16x16x32_bf16 v[220:223], v[152:155], a[224:227], 0\n"
        "s_waitcnt lgkmcnt(3)\n"
        "v_permlane16_swap_b32 v232, v233\n"
        "v_mfma_f32_16x16x32_bf16 v[220:223], v[160:163], a[220:223], v[220:223]\n"
        "v_max_f32 v232, v232, v233\n"
        "v_mul_f32 v236, s25, v232\n"
        "v_mfma_f32_16x16x32_bf16 v[200:203], v[164:167], a[220:223], 0\n"
        "ds_read_b128 a[220:223], v248 offset:43008\n"
        "v_mul_f32 v237, s25, v232\n"
        "v_mfma_f32_16x16x32_bf16 v[200:203], v[156:159], a[224:227], v[200:203]\n"
        "s_waitcnt lgkmcnt(3)\n"
        "ds_read_b128 a[224:227], v254 offset:36880\n"
        "v_add_u32 v245, s45, v245\n"
        "v_mfma_f32_16x16x32_bf16 v[204:207], v[156:159], a[232:235], 0\n"
        "s_waitcnt lgkmcnt(3)\n"
        "v_permlane32_swap_b32 v236, v237\n"
        "v_mfma_f32_16x16x32_bf16 v[204:207], v[164:167], a[228:231], v[204:207]\n"
        "v_permlane32_swap_b32 v234, v235\n"
        "v_mfma_f32_16x16x32_bf16 v[224:227], v[160:163], a[228:231], 0\n"
        "ds_read_b128 a[228:231], v254 offset:43024\n"
        "v_max_f32 v234, v234, v235\n"
        "v_mfma_f32_16x16x32_bf16 v[224:227], v[152:155], a[232:235], v[224:227]\n"
        "s_waitcnt lgkmcnt(3)\n"
        "ds_read_b128 a[232:235], v248 offset:36896\n"
        "v_mov_b32 v235, v234\n"
        "v_mfma_f32_16x16x32_bf16 v[172:175], a[236:239], a[216:219], v[172:175]\n"
        "s_waitcnt lgkmcnt(3)\n"
        "v_fma_f32 v64, v64, s24, v236\n"
        "v_fma_f32 v65, v65, s24, v236\n"
        "v_mfma_f32_16x16x32_bf16 v[172:175], a[244:247], a[220:223], v[172:175]\n"
        "v_permlane16_swap_b32 v234, v235\n"
        "v_mfma_f32_16x16x32_bf16 v[180:183], a[248:251], a[220:223], v[180:183]\n"
        "ds_read_b128 a[220:223], v248 offset:43040\n"
        "v_max_f32 v234, v234, v235\n"
        "v_mfma_f32_16x16x32_bf16 v[180:183], a[240:243], a[216:219], v[180:183]\n"
        "s_waitcnt lgkmcnt(3)\n"
        "ds_read_b128 a[216:219], v254 offset:36912\n"
        "v_mul_f32 v238, s25, v234\n"
        "v_mfma_f32_16x16x32_bf16 v[184:187], a[240:243], a[224:227], v[184:187]\n"
        "s_waitcnt lgkmcnt(3)\n"
        "ds_read_b128 v[152:155], v247 offset:16384\n"
        "v_mul_f32 v239, s25, v234\n"
        "v_mfma_f32_16x16x32_bf16 v[184:187], a[248:251], a[228:231], v[184:187]\n"
        "ds_read_b128 v[160:163], v247 offset:20480\n"
        "v_fma_f32 v66, v66, s24, v236\n"
        "v_mfma_f32_16x16x32_bf16 v[176:179], a[244:247], a[228:231], v[176:179]\n"
        "ds_read_b128 a[228:231], v254 offset:43056\n"
        "v_fma_f32 v67, v67, s24, v236\n"
        "v_mfma_f32_16x16x32_bf16 v[176:179], a[236:239], a[224:227], v[176:179]\n"
        "s_waitcnt lgkmcnt(5)\n"
        "ds_read_b128 a[224:227], v248 offset:36928\n"
        "v_fma_f32 v68, v68, s24, v236\n"
        "v_mfma_f32_16x16x32_bf16 v[212:215], a[236:239], a[232:235], v[212:215]\n"
        "s_waitcnt lgkmcnt(5)\n"
        "ds_read_b128 v[156:159], v247 offset:16640\n"
        "v_fma_f32 v69, v69, s24, v236\n"
        "v_mfma_f32_16x16x32_bf16 v[212:215], a[244:247], a[220:223], v[212:215]\n"
        "ds_read_b128 v[164:167], v247 offset:20736\n"
        "v_fma_f32 v70, v70, s24, v236\n"
        "v_mfma_f32_16x16x32_bf16 v[192:195], a[248:251], a[220:223], v[192:195]\n"
        "ds_read_b128 a[220:223], v248 offset:43072\n"
        "v_fma_f32 v71, v71, s24, v236\n"
        "v_mfma_f32_16x16x32_bf16 v[192:195], a[240:243], a[232:235], v[192:195]\n"
        "s_waitcnt lgkmcnt(7)\n"
        "ds_read_b128 a[232:235], v254 offset:36944\n"
        "v_fma_f32 v72, v72, s24, v236\n"
        "v_mfma_f32_16x16x32_bf16 v[196:199], a[240:243], a[216:219], v[196:199]\n"
        "s_waitcnt lgkmcnt(5)\n"
        "v_permlane32_swap_b32 v238, v239\n"
        "v_mfma_f32_16x16x32_bf16 v[196:199], a[248:251], a[228:231], v[196:199]\n"
        "v_exp_f32 v64, v64\n"
        "v_mfma_f32_16x16x32_bf16 v[216:219], a[244:247], a[228:231], v[216:219]\n"
        "ds_read_b128 a[228:231], v254 offset:43088\n"
        "v_fma_f32 v73, v73, s24, v236\n"
        "v_mfma_f32_16x16x32_bf16 v[216:219], a[236:239], a[216:219], v[216:219]\n"
        "s_waitcnt lgkmcnt(5)\n"
        "ds_read_b128 a[216:219], v248 offset:12288\n"
        "v_fma_f32 v74, v74, s24, v236\n"
        "v_mfma_f32_16x16x32_bf16 v[220:223], a[236:239], a[224:227], v[220:223]\n"
        "s_waitcnt lgkmcnt(3)\n"
        "v_exp_f32 v65, v65\n"
        "v_mfma_f32_16x16x32_bf16 v[220:223], a[244:247], a[220:223], v[220:223]\n"
        "v_exp_f32 v66, v66\n"
        "v_mfma_f32_16x16x32_bf16 v[200:203], a[248:251], a[220:223], v[200:203]\n"
        "ds_read_b128 a[220:223], v248 offset:49152\n"
        "v_cvt_pk_bf16_f32 v64, v64, v65\n"
        "v_mfma_f32_16x16x32_bf16 v[200:203], a[240:243], a[224:227], v[200:203]\n"
        "s_waitcnt lgkmcnt(3)\n"
        "ds_read_b128 a[224:227], v254 offset:12304\n"
        "v_fma_f32 v75, v75, s24, v236\n"
        "v_mfma_f32_16x16x32_bf16 v[204:207], a[240:243], a[232:235], v[204:207]\n"
        "s_waitcnt lgkmcnt(3)\n"
        "v_exp_f32 v67, v67\n"
        "v_mfma_f32_16x16x32_bf16 v[204:207], a[248:251], a[228:231], v[204:207]\n"
        "v_exp_f32 v68, v68\n"
        "v_mfma_f32_16x16x32_bf16 v[224:227], a[244:247], a[228:231], v[224:227]\n"
        "ds_read_b128 a[228:231], v254 offset:49168\n"
        "v_cvt_pk_bf16_f32 v65, v66, v67\n"
        "v_mfma_f32_16x16x32_bf16 v[224:227], a[236:239], a[232:235], v[224:227]\n"
        "s_waitcnt lgkmcnt(3)\n"
        "ds_read_b128 a[232:235], v248 offset:12320\n"
        "v_add_u32 v246, s45, v246\n"
        "v_mfma_f32_16x16x32_bf16 v[172:175], v[152:155], a[216:219], v[172:175]\n"
        "s_waitcnt lgkmcnt(3)\n"
        "v_exp_f32 v69, v69\n"
        "v_mfma_f32_16x16x32_bf16 v[172:175], v[160:163], a[220:223], v[172:175]\n"
        "v_cvt_pk_bf16_f32 v66, v68, v69\n"
        "v_add_u32 v247, s45, v247\n"
        "v_mfma_f32_16x16x32_bf16 v[180:183], v[164:167], a[220:223], v[180:183]\n"
        "ds_read_b128 a[220:223], v248 offset:49184\n"
        "v_mfma_f32_16x16x32_bf16 v[180:183], v[156:159], a[216:219], v[180:183]\n"
        "s_waitcnt lgkmcnt(3)\n"
        "ds_read_b128 a[216:219], v254 offset:12336\n"
        "v_mfma_f32_16x16x32_bf16 v[184:187], v[156:159], a[224:227], v[184:187]\n"
        "s_waitcnt lgkmcnt(3)\n"
        "v_exp_f32 v70, v70\n"
        "v_mfma_f32_16x16x32_bf16 v[184:187], v[164:167], a[228:231], v[184:187]\n"
        "v_exp_f32 v71, v71\n"
        "v_mfma_f32_16x16x32_bf16 v[176:179], v[160:163], a[228:231], v[176:179]\n"
        "ds_read_b128 a[228:231], v254 offset:49200\n"
        "v_cvt_pk_bf16_f32 v67, v70, v71\n"
        "v_mfma_f32_16x16x32_bf16 v[176:179], v[152:155], a[224:227], v[176:179]\n"
        "s_waitcnt lgkmcnt(3)\n"
        "ds_read_b128 a[224:227], v248 offset:12352\n"
        "v_mfma_f32_16x16x32_bf16 v[212:215], v[152:155], a[232:235], v[212:215]\n"
        "s_waitcnt lgkmcnt(3)\n"
        "s_waitcnt vmcnt(0)\n"
        "s_barrier\n"
        "v_exp_f32 v72, v72\n"
        "v_mfma_f32_16x16x32_bf16 v[212:215], v[160:163], a[220:223], v[212:215]\n"
        "v_exp_f32 v73, v73\n"
        "v_mfma_f32_16x16x32_bf16 v[192:195], v[164:167], a[220:223], v[192:195]\n"
        "ds_read_b128 a[220:223], v248 offset:49216\n"
        "v_cvt_pk_bf16_f32 v68, v72, v73\n"
        "v_mfma_f32_16x16x32_bf16 v[192:195], v[156:159], a[232:235], v[192:195]\n"
        "s_waitcnt lgkmcnt(3)\n"
        "ds_read_b128 a[232:235], v254 offset:12368\n"
        "v_mfma_f32_16x16x32_bf16 v[196:199], v[156:159], a[216:219], v[196:199]\n"
        "s_waitcnt lgkmcnt(3)\n"
        "ds_read_b128 a[236:239], v240 offset:0\n"
        "v_mfma_f32_16x16x32_bf16 v[196:199], v[164:167], a[228:231], v[196:199]\n"
        "ds_read_b128 a[240:243], v241 offset:0\n"
        "v_mfma_f32_16x16x32_bf16 v[216:219], v[160:163], a[228:231], v[216:219]\n"
        "ds_read_b128 a[228:231], v254 offset:49232\n"
        "v_mfma_f32_16x16x32_bf16 v[216:219], v[152:155], a[216:219], v[216:219]\n"
        "s_waitcnt lgkmcnt(5)\n"
        "ds_read_b128 a[244:247], v240 offset:128\n"
        "v_mfma_f32_16x16x32_bf16 v[220:223], v[152:155], a[224:227], v[220:223]\n"
        "s_waitcnt lgkmcnt(5)\n"
        "ds_read_b128 a[248:251], v241 offset:128\n"
        "v_mfma_f32_16x16x32_bf16 v[220:223], v[160:163], a[220:223], v[220:223]\n"
        "ds_read_b128 a[216:219], v240 offset:256\n"
        "v_mfma_f32_16x16x32_bf16 v[200:203], v[164:167], a[220:223], v[200:203]\n"
        "ds_read_b128 a[220:223], v241 offset:256\n"
        "v_mfma_f32_16x16x32_bf16 v[200:203], v[156:159], a[224:227], v[200:203]\n"
        "s_waitcnt lgkmcnt(7)\n"
        "v_exp_f32 v74, v74\n"
        "v_mfma_f32_16x16x32_bf16 v[204:207], v[156:159], a[232:235], v[204:207]\n"
        "s_waitcnt lgkmcnt(4)\n"
        "v_exp_f32 v75, v75\n"
        "v_mfma_f32_16x16x32_bf16 v[204:207], v[164:167], a[228:231], v[204:207]\n"
        "s_sub_u32 s27, s27, 1\n"
        "v_cvt_pk_bf16_f32 v69, v74, v75\n"
        "v_mfma_f32_16x16x32_bf16 v[224:227], v[160:163], a[228:231], v[224:227]\n"
        "s_cmp_lg_u32 s27, 0\n"
        "v_mfma_f32_16x16x32_bf16 v[224:227], v[152:155], a[232:235], v[224:227]\n"
        "s_cbranch_scc1 10b\n"
        "20:\n"
        "s_waitcnt lgkmcnt(0)\n"
        "s_nop 7\n"
        "v_mfma_f32_16x16x32_bf16 v[48:51], a[252:255], v[64:67], 0\n"
        "s_and_b32 s46, s42, 1\n"
        "s_lshl_b32 s31, s46, 8\n"
        "s_mul_i32 s45, s46, 786432\n"
        "s_sub_u32 s37, s37, s45\n"
        "s_cmp_eq_u32 s46, 0\n"
        "s_cselect_b32 s43, s43, s44\n"
        "s_add_u32 s28, s37, 786432\n"
        "s_cmp_eq_u32 s28, 100663296\n"
        "s_cselect_b32 s28, 0, s28\n"
        "v_fma_f32 v76, v76, s24, v236\n"
        "v_exp_f32 v76, v76\n"
        "v_fma_f32 v77, v77, s24, v236\n"
        "v_exp_f32 v77, v77\n"
        "v_fma_f32 v78, v78, s24, v236\n"
        "v_cvt_pk_bf16_f32 v70, v76, v77\n"
        "v_exp_f32 v78, v78\n"
        "v_fma_f32 v79, v79, s24, v236\n"
        "v_exp_f32 v79, v79\n"
        "v_subrev_u32 v247, s31, v247\n"
        "v_cvt_pk_bf16_f32 v71, v78, v79\n"
        "s_nop 1\n"
        "v_mfma_f32_16x16x32_bf16 v[48:51], a[252:255], v[68:71], v[48:51]\n"
        "v_cvt_pk_bf16_f32 v232, v172, v176\n"
        "v_cvt_pk_bf16_f32 v233, v212, v216\n"
        "v_cvt_pk_bf16_f32 v234, v220, v224\n"
        "v_fma_f32 v80, v80, s24, v237\n"
        "v_exp_f32 v80, v80\n"
        "v_mfma_f32_16x16x32_bf16 v[52:55], v[132:135], v[68:71], 0\n"
        "buffer_store_dwordx3 v[232:234], v250, s[16:19], s38 offen offset:0 sc1\n"
        "ds_read_b128 a[216:219], v248 offset:0\n"
        "v_mfma_f32_16x16x32_bf16 v[52:55], v[128:131], v[64:67], v[52:55]\n"
        "ds_read_b128 a[220:223], v248 offset:6144\n"
        "v_cvt_pk_bf16_f32 v232, v173, v177\n"
        "v_cvt_pk_bf16_f32 v233, v213, v217\n"
        "v_cvt_pk_bf16_f32 v234, v221, v225\n"
        "v_fma_f32 v81, v81, s24, v237\n"
        "v_fma_f32 v82, v82, s24, v237\n"
        "v_mfma_f32_16x16x32_bf16 v[56:59], v[136:139], v[64:67], 0\n"
        "buffer_store_dwordx3 v[232:234], v250, s[16:19], s38 offen offset:384 sc1\n"
        "ds_read_b128 a[224:227], v248 offset:36864\n"
        "v_mfma_f32_16x16x32_bf16 v[56:59], v[140:143], v[68:71], v[56:59]\n"
        "ds_read_b128 a[228:231], v248 offset:43008\n"
        "v_cvt_pk_bf16_f32 v232, v174, v178\n"
        "v_cvt_pk_bf16_f32 v233, v214, v218\n"
        "v_cvt_pk_bf16_f32 v234, v222, v226\n"
        "v_exp_f32 v81, v81\n"
        "v_mfma_f32_16x16x32_bf16 v[60:63], v[148:151], v[68:71], 0\n"
        "buffer_store_dwordx3 v[232:234], v250, s[16:19], s38 offen offset:768 sc1\n"
        "ds_read_b128 a[232:235], v248 offset:12288\n"
        "v_mfma_f32_16x16x32_bf16 v[60:63], v[144:147], v[64:67], v[60:63]\n"
        "ds_read_b128 a[236:239], v248 offset:49152\n"
        "v_cvt_pk_bf16_f32 v232, v175, v179\n"
        "v_cvt_pk_bf16_f32 v233, v215, v219\n"
        "v_cvt_pk_bf16_f32 v234, v223, v227\n"
        "v_cvt_pk_bf16_f32 v80, v80, v81\n"
        "v_fma_f32 v83, v83, s24, v237\n"
        "v_exp_f32 v82, v82\n"
        "v_exp_f32 v83, v83\n"
        "v_fma_f32 v84, v84, s24, v237\n"
        "v_cvt_pk_bf16_f32 v81, v82, v83\n"
        "v_exp_f32 v84, v84\n"
        "v_fma_f32 v85, v85, s24, v237\n"
        "v_exp_f32 v85, v85\n"
        "v_fma_f32 v86, v86, s24, v237\n"
        "v_cvt_pk_bf16_f32 v82, v84, v85\n"
        "v_exp_f32 v86, v86\n"
        "v_fma_f32 v87, v87, s24, v237\n"
        "v_exp_f32 v87, v87\n"
        "v_fma_f32 v88, v88, s24, v237\n"
        "v_cvt_pk_bf16_f32 v83, v86, v87\n"
        "s_nop 1\n"
        "v_mfma_f32_16x16x32_bf16 v[168:171], a[252:255], v[80:83], 0\n"
        "buffer_store_dwordx3 v[232:234], v250, s[16:19], s38 offen offset:1152 sc1\n"
        "ds_read_b128 a[240:243], v254 offset:16\n"
        "v_exp_f32 v88, v88\n"
        "v_fma_f32 v89, v89, s24, v237\n"
        "v_exp_f32 v89, v89\n"
        "v_fma_f32 v90, v90, s24, v237\n"
        "v_cvt_pk_bf16_f32 v84, v88, v89\n"
        "v_exp_f32 v90, v90\n"
        "v_fma_f32 v91, v91, s24, v237\n"
        "v_exp_f32 v91, v91\n"
        "v_fma_f32 v92, v92, s24, v237\n"
        "v_cvt_pk_bf16_f32 v85, v90, v91\n"
        "v_exp_f32 v92, v92\n"
        "v_fma_f32 v93, v93, s24, v237\n"
        "v_exp_f32 v93, v93\n"
        "v_fma_f32 v94, v94, s24, v237\n"
        "v_cvt_pk_bf16_f32 v86, v92, v93\n"
        "v_exp_f32 v94, v94\n"
        "v_fma_f32 v95, v95, s24, v237\n"
        "v_exp_f32 v95, v95\n"
        "v_cvt_pk_bf16_f32 v232, v180, v184\n"
        "v_cvt_pk_bf16_f32 v87, v94, v95\n"
        "s_nop 1\n"
        "v_mfma_f32_16x16x32_bf16 v[168:171], a[252:255], v[84:87], v[168:171]\n"
        "ds_read_b128 a[244:247], v254 offset:6160\n"
        "v_cvt_pk_bf16_f32 v233, v192, v196\n"
        "v_cvt_pk_bf16_f32 v234, v200, v204\n"
        "v_rcp_f32 v48, v48\n"
        "v_fma_f32 v96, v96, s24, v238\n"
        "v_mfma_f32_16x16x32_bf16 v[188:191], v[132:135], v[84:87], 0\n"
        "buffer_store_dwordx3 v[232:234], v250, s[16:19], s39 offen offset:0 sc1\n"
        "ds_read_b128 a[248:251], v254 offset:36880\n"
        "v_mfma_f32_16x16x32_bf16 v[188:191], v[128:131], v[80:83], v[188:191]\n"
        "v_cvt_pk_bf16_f32 v232, v181, v185\n"
        "v_cvt_pk_bf16_f32 v233, v193, v197\n"
        "v_cvt_pk_bf16_f32 v234, v201, v205\n"
        "v_mul_f32 v52, v48, v52\n"
        "v_mul_f32 v53, v48, v53\n"
        "v_mul_f32 v54, v48, v54\n"
        "v_mfma_f32_16x16x32_bf16 v[208:211], v[136:139], v[80:83], 0\n"
        "buffer_store_dwordx3 v[232:234], v250, s[16:19], s39 offen offset:384 sc1\n"
        "v_mul_f32 v55, v48, v55\n"
        "v_mfma_f32_16x16x32_bf16 v[208:211], v[140:143], v[84:87], v[208:211]\n"
        "v_cvt_pk_bf16_f32 v232, v182, v186\n"
        "v_cvt_pk_bf16_f32 v233, v194, v198\n"
        "v_cvt_pk_bf16_f32 v234, v202, v206\n"
        "v_cvt_pk_bf16_f32 v52, v52, v53\n"
        "v_cvt_pk_bf16_f32 v53, v54, v55\n"
        "v_mul_f32 v56, v48, v56\n"
        "v_mfma_f32_16x16x32_bf16 v[228:231], v[148:151], v[84:87], 0\n"
        "buffer_store_dwordx3 v[232:234], v250, s[16:19], s39 offen offset:768 sc1\n"
        "v_mul_f32 v57, v48, v57\n"
        "v_mfma_f32_16x16x32_bf16 v[228:231], v[144:147], v[80:83], v[228:231]\n"
        "v_cvt_pk_bf16_f32 v232, v183, v187\n"
        "v_cvt_pk_bf16_f32 v233, v195, v199\n"
        "v_cvt_pk_bf16_f32 v234, v203, v207\n"
        "v_mul_f32 v58, v48, v58\n"
        "v_mul_f32 v59, v48, v59\n"
        "v_cvt_pk_bf16_f32 v54, v56, v57\n"
        "v_cvt_pk_bf16_f32 v55, v58, v59\n"
        "v_mul_f32 v60, v48, v60\n"
        "v_mul_f32 v61, v48, v61\n"
        "v_mul_f32 v62, v48, v62\n"
        "v_mul_f32 v63, v48, v63\n"
        "v_cvt_pk_bf16_f32 v60, v60, v61\n"
        "v_cvt_pk_bf16_f32 v61, v62, v63\n"
        "ds_write_b128 v245, v[52:55] offset:0\n"
        "ds_write_b64 v246, v[60:61] offset:0\n"
        "v_rcp_f32 v168, v168\n"
        "v_exp_f32 v96, v96\n"
        "v_mul_f32 v188, v168, v188\n"
        "v_mul_f32 v189, v168, v189\n"
        "v_mul_f32 v190, v168, v190\n"
        "v_mul_f32 v191, v168, v191\n"
        "v_cvt_pk_bf16_f32 v188, v188, v189\n"
        "v_cvt_pk_bf16_f32 v189, v190, v191\n"
        "v_mul_f32 v208, v168, v208\n"
        "v_mul_f32 v209, v168, v209\n"
        "v_mul_f32 v210, v168, v210\n"
        "v_mul_f32 v211, v168, v211\n"
        "v_cvt_pk_bf16_f32 v190, v208, v209\n"
        "v_cvt_pk_bf16_f32 v191, v210, v211\n"
        "v_mul_f32 v228, v168, v228\n"
        "v_mul_f32 v229, v168, v229\n"
        "v_mul_f32 v230, v168, v230\n"
        "v_mul_f32 v231, v168, v231\n"
        "v_cvt_pk_bf16_f32 v228, v228, v229\n"
        "v_cvt_pk_bf16_f32 v229, v230, v231\n"
        "ds_write_b128 v245, v[188:191] offset:256\n"
        "ds_write_b64 v246, v[228:229] offset:256\n"
        "s_waitcnt lgkmcnt(0)\n"
        "s_barrier\n"
        "ds_read_b128 v[152:155], v247 offset:0\n"
        "s_waitcnt lgkmcnt(0)\n"
        "v_mfma_f32_16x16x32_bf16 v[0:3], v[152:155], a[216:219], 0\n"
        "buffer_store_dwordx3 v[232:234], v250, s[16:19], s39 offen offset:1152 sc1\n"
        "ds_read_b128 v[156:159], v247 offset:4096\n"
        "s_waitcnt lgkmcnt(0)\n"
        "v_mfma_f32_16x16x32_bf16 v[0:3], v[156:159], a[220:223], v[0:3]\n"
        "ds_read_b128 v[172:175], v247 offset:8192\n"
        "ds_read_b128 v[176:179], v247 offset:12288\n"
        "ds_read_b128 v[212:215], v247 offset:16384\n"
        "ds_read_b128 v[216:219], v247 offset:20480\n"
        "s_waitcnt lgkmcnt(3)\n"
        "v_mfma_f32_16x16x32_bf16 v[0:3], v[172:175], a[224:227], v[0:3]\n"
        "s_waitcnt lgkmcnt(2)\n"
        "ds_read_b128 a[216:219], v254 offset:43024\n"
        "ds_read_b128 a[220:223], v254 offset:12304\n"
        "v_fma_f32 v97, v97, s24, v238\n"
        "v_fma_f32 v98, v98, s24, v238\n"
        "v_mfma_f32_16x16x32_bf16 v[0:3], v[176:179], a[228:231], v[0:3]\n"
        "s_waitcnt lgkmcnt(3)\n"
        "ds_read_b128 a[224:227], v254 offset:49168\n"
        "ds_read_b128 a[228:231], v248 offset:32\n"
        "v_exp_f32 v97, v97\n"
        "v_mfma_f32_16x16x32_bf16 v[0:3], v[212:215], a[232:235], v[0:3]\n"
        "s_waitcnt lgkmcnt(4)\n"
        "ds_read_b128 a[232:235], v248 offset:6176\n"
        "v_exp_f32 v98, v98\n"
        "v_cvt_pk_bf16_f32 v96, v96, v97\n"
        "v_mfma_f32_16x16x32_bf16 v[0:3], v[216:219], a[236:239], v[0:3]\n"
        "ds_read_b128 a[236:239], v248 offset:36896\n"
        "v_fma_f32 v99, v99, s24, v238\n"
        "v_exp_f32 v99, v99\n"
        "v_mfma_f32_16x16x32_bf16 v[4:7], v[152:155], a[240:243], 0\n"
        "ds_read_b128 a[240:243], v248 offset:43040\n"
        "v_cvt_pk_bf16_f32 v97, v98, v99\n"
        "v_fma_f32 v100, v100, s24, v238\n"
        "v_fma_f32 v101, v101, s24, v238\n"
        "v_mfma_f32_16x16x32_bf16 v[4:7], v[156:159], a[244:247], v[4:7]\n"
        "ds_read_b128 a[244:247], v248 offset:12320\n"
        "v_exp_f32 v100, v100\n"
        "v_fma_f32 v102, v102, s24, v238\n"
        "v_mfma_f32_16x16x32_bf16 v[4:7], v[172:175], a[248:251], v[4:7]\n"
        "s_waitcnt lgkmcnt(7)\n"
        "ds_read_b128 a[248:251], v248 offset:49184\n"
        "v_exp_f32 v101, v101\n"
        "v_fma_f32 v103, v103, s24, v238\n"
        "v_mfma_f32_16x16x32_bf16 v[4:7], v[176:179], a[216:219], v[4:7]\n"
        "s_waitcnt lgkmcnt(7)\n"
        "v_cvt_pk_bf16_f32 v98, v100, v101\n"
        "v_exp_f32 v102, v102\n"
        "v_fma_f32 v104, v104, s24, v238\n"
        "v_mfma_f32_16x16x32_bf16 v[4:7], v[212:215], a[220:223], v[4:7]\n"
        "s_waitcnt lgkmcnt(6)\n"
        "v_exp_f32 v103, v103\n"
        "v_exp_f32 v104, v104\n"
        "v_mfma_f32_16x16x32_bf16 v[4:7], v[216:219], a[224:227], v[4:7]\n"
        "s_waitcnt lgkmcnt(5)\n"
        "v_cvt_pk_bf16_f32 v99, v102, v103\n"
        "v_fma_f32 v105, v105, s24, v238\n"
        "v_exp_f32 v105, v105\n"
        "v_mfma_f32_16x16x32_bf16 v[8:11], v[152:155], a[228:231], 0\n"
        "s_waitcnt lgkmcnt(4)\n"
        "ds_read_b128 a[216:219], v254 offset:48\n"
        "v_cvt_pk_bf16_f32 v100, v104, v105\n"
        "v_fma_f32 v106, v106, s24, v238\n"
        "v_fma_f32 v107, v107, s24, v238\n"
        "v_mfma_f32_16x16x32_bf16 v[8:11], v[156:159], a[232:235], v[8:11]\n"
        "s_waitcnt lgkmcnt(4)\n"
        "ds_read_b128 a[220:223], v254 offset:6192\n"
        "v_exp_f32 v106, v106\n"
        "v_fma_f32 v108, v108, s24, v238\n"
        "v_mfma_f32_16x16x32_bf16 v[8:11], v[172:175], a[236:239], v[8:11]\n"
        "s_waitcnt lgkmcnt(4)\n"
        "ds_read_b128 a[224:227], v254 offset:36912\n"
        "v_exp_f32 v107, v107\n"
        "v_fma_f32 v109, v109, s24, v238\n"
        "v_mfma_f32_16x16x32_bf16 v[8:11], v[176:179], a[240:243], v[8:11]\n"
        "s_waitcnt lgkmcnt(4)\n"
        "ds_read_b128 a[228:231], v254 offset:43056\n"
        "v_cvt_pk_bf16_f32 v101, v106, v107\n"
        "v_exp_f32 v108, v108\n"
        "v_mfma_f32_16x16x32_bf16 v[8:11], v[212:215], a[244:247], v[8:11]\n"
        "s_waitcnt lgkmcnt(4)\n"
        "ds_read_b128 a[232:235], v254 offset:12336\n"
        "v_exp_f32 v109, v109\n"
        "v_fma_f32 v110, v110, s24, v238\n"
        "v_mfma_f32_16x16x32_bf16 v[8:11], v[216:219], a[248:251], v[8:11]\n"
        "ds_read_b128 a[236:239], v254 offset:49200\n"
        "s_add_u32 s38, s37, s43\n"
        "v_cvt_pk_bf16_f32 v102, v108, v109\n"
        "v_exp_f32 v110, v110\n"
        "v_mfma_f32_16x16x32_bf16 v[64:67], a[252:255], v[96:99], 0\n"
        "ds_read_b128 a[240:243], v248 offset:64\n"
        "s_add_u32 s39, s28, s43\n"
        "v_fma_f32 v111, v111, s24, v238\n"
        "v_exp_f32 v111, v111\n"
        "v_fma_f32 v112, v112, s24, v239\n"
        "v_cvt_pk_bf16_f32 v103, v110, v111\n"
        "s_nop 1\n"
        "v_mfma_f32_16x16x32_bf16 v[64:67], a[252:255], v[100:103], v[64:67]\n"
        "ds_read_b128 a[244:247], v248 offset:6208\n"
        "v_exp_f32 v112, v112\n"
        "v_fma_f32 v113, v113, s24, v239\n"
        "v_mfma_f32_16x16x32_bf16 v[68:71], v[132:135], v[100:103], 0\n"
        "ds_read_b128 a[248:251], v248 offset:36928\n"
        "v_exp_f32 v113, v113\n"
        "v_fma_f32 v114, v114, s24, v239\n"
        "v_mfma_f32_16x16x32_bf16 v[68:71], v[128:131], v[96:99], v[68:71]\n"
        "v_rcp_f32 v64, v64\n"
        "v_exp_f32 v114, v114\n"
        "v_mfma_f32_16x16x32_bf16 v[72:75], v[136:139], v[96:99], 0\n"
        "v_cvt_pk_bf16_f32 v112, v112, v113\n"
        "v_fma_f32 v115, v115, s24, v239\n"
        "v_exp_f32 v115, v115\n"
        "v_mfma_f32_16x16x32_bf16 v[72:75], v[140:143], v[100:103], v[72:75]\n"
        "v_cvt_pk_bf16_f32 v113, v114, v115\n"
        "v_mul_f32 v68, v64, v68\n"
        "v_mul_f32 v69, v64, v69\n"
        "v_mul_f32 v70, v64, v70\n"
        "v_mfma_f32_16x16x32_bf16 v[76:79], v[148:151], v[100:103], 0\n"
        "v_mul_f32 v71, v64, v71\n"
        "v_cvt_pk_bf16_f32 v68, v68, v69\n"
        "v_cvt_pk_bf16_f32 v69, v70, v71\n"
        "v_mul_f32 v72, v64, v72\n"
        "v_mfma_f32_16x16x32_bf16 v[76:79], v[144:147], v[96:99], v[76:79]\n"
        "s_waitcnt lgkmcnt(8)\n"
        "v_mul_f32 v73, v64, v73\n"
        "v_mul_f32 v74, v64, v74\n"
        "v_mul_f32 v75, v64, v75\n"
        "v_cvt_pk_bf16_f32 v70, v72, v73\n"
        "v_mfma_f32_16x16x32_bf16 v[12:15], v[152:155], a[216:219], 0\n"
        "s_waitcnt lgkmcnt(7)\n"
        "ds_read_b128 a[216:219], v248 offset:43072\n"
        "v_cvt_pk_bf16_f32 v71, v74, v75\n"
        "v_fma_f32 v116, v116, s24, v239\n"
        "v_mul_f32 v76, v64, v76\n"
        "v_mfma_f32_16x16x32_bf16 v[12:15], v[156:159], a[220:223], v[12:15]\n"
        "s_waitcnt lgkmcnt(7)\n"
        "ds_read_b128 a[220:223], v248 offset:12352\n"
        "v_mul_f32 v77, v64, v77\n"
        "v_mul_f32 v78, v64, v78\n"
        "v_mul_f32 v79, v64, v79\n"
        "v_mfma_f32_16x16x32_bf16 v[12:15], v[172:175], a[224:227], v[12:15]\n"
        "s_waitcnt lgkmcnt(7)\n"
        "ds_write_b128 v245, v[68:71] offset:512\n"
        "v_mfma_f32_16x16x32_bf16 v[12:15], v[176:179], a[228:231], v[12:15]\n"
        "s_waitcnt lgkmcnt(7)\n"
        "ds_read_b128 a[224:227], v248 offset:49216\n"
        "ds_read_b128 a[228:231], v254 offset:80\n"
        "v_cvt_pk_bf16_f32 v76, v76, v77\n"
        "v_cvt_pk_bf16_f32 v77, v78, v79\n"
        "v_mfma_f32_16x16x32_bf16 v[12:15], v[212:215], a[232:235], v[12:15]\n"
        "s_waitcnt lgkmcnt(8)\n"
        "ds_read_b128 a[232:235], v254 offset:6224\n"
        "v_exp_f32 v116, v116\n"
        "v_fma_f32 v117, v117, s24, v239\n"
        "v_mfma_f32_16x16x32_bf16 v[12:15], v[216:219], a[236:239], v[12:15]\n"
        "s_waitcnt lgkmcnt(8)\n"
        "ds_write_b64 v246, v[76:77] offset:512\n"
        "ds_read_b128 a[236:239], v254 offset:36944\n"
        "v_mfma_f32_16x16x32_bf16 v[16:19], v[152:155], a[240:243], 0\n"
        "s_waitcnt lgkmcnt(9)\n"
        "ds_read_b128 a[240:243], v254 offset:43088\n"
        "v_exp_f32 v117, v117\n"
        "v_fma_f32 v118, v118, s24, v239\n"
        "v_mfma_f32_16x16x32_bf16 v[16:19], v[156:159], a[244:247], v[16:19]\n"
        "s_waitcnt lgkmcnt(9)\n"
        "ds_read_b128 a[244:247], v254 offset:12368\n"
        "v_exp_f32 v118, v118\n"
        "v_cvt_pk_bf16_f32 v114, v116, v117\n"
        "v_mfma_f32_16x16x32_bf16 v[16:19], v[172:175], a[248:251], v[16:19]\n"
        "s_waitcnt lgkmcnt(9)\n"
        "ds_read_b128 a[248:251], v254 offset:49232\n"
        "v_fma_f32 v119, v119, s24, v239\n"
        "v_exp_f32 v119, v119\n"
        "v_mfma_f32_16x16x32_bf16 v[16:19], v[176:179], a[216:219], v[16:19]\n"
        "s_waitcnt lgkmcnt(9)\n"
        "v_cvt_pk_bf16_f32 v115, v118, v119\n"
        "v_fma_f32 v120, v120, s24, v239\n"
        "v_exp_f32 v120, v120\n"
        "v_mfma_f32_16x16x32_bf16 v[16:19], v[212:215], a[220:223], v[16:19]\n"
        "s_waitcnt lgkmcnt(7)\n"
        "v_fma_f32 v121, v121, s24, v239\n"
        "v_exp_f32 v121, v121\n"
        "v_fma_f32 v122, v122, s24, v239\n"
        "v_mfma_f32_16x16x32_bf16 v[16:19], v[216:219], a[224:227], v[16:19]\n"
        "s_waitcnt lgkmcnt(6)\n"
        "v_cvt_pk_bf16_f32 v116, v120, v121\n"
        "v_exp_f32 v122, v122\n"
        "v_fma_f32 v123, v123, s24, v239\n"
        "v_mfma_f32_16x16x32_bf16 v[20:23], v[152:155], a[228:231], 0\n"
        "s_waitcnt lgkmcnt(5)\n"
        "ds_read_b128 a[216:219], v248 offset:0\n"
        "v_exp_f32 v123, v123\n"
        "v_fma_f32 v124, v124, s24, v239\n"
        "v_mfma_f32_16x16x32_bf16 v[20:23], v[156:159], a[232:235], v[20:23]\n"
        "s_waitcnt lgkmcnt(4)\n"
        "ds_read_b128 a[220:223], v248 offset:6144\n"
        "v_cvt_pk_bf16_f32 v117, v122, v123\n"
        "v_exp_f32 v124, v124\n"
        "v_mfma_f32_16x16x32_bf16 v[20:23], v[172:175], a[236:239], v[20:23]\n"
        "s_waitcnt lgkmcnt(4)\n"
        "ds_read_b128 a[224:227], v248 offset:36864\n"
        "v_fma_f32 v125, v125, s24, v239\n"
        "v_exp_f32 v125, v125\n"
        "v_mfma_f32_16x16x32_bf16 v[20:23], v[176:179], a[240:243], v[20:23]\n"
        "s_waitcnt lgkmcnt(4)\n"
        "ds_read_b128 a[228:231], v248 offset:43008\n"
        "v_cvt_pk_bf16_f32 v118, v124, v125\n"
        "v_fma_f32 v126, v126, s24, v239\n"
        "v_fma_f32 v127, v127, s24, v239\n"
        "v_mfma_f32_16x16x32_bf16 v[20:23], v[212:215], a[244:247], v[20:23]\n"
        "s_waitcnt lgkmcnt(4)\n"
        "ds_read_b128 a[232:235], v248 offset:12288\n"
        "v_exp_f32 v126, v126\n"
        "v_cvt_pk_bf16_f32 v192, v0, v4\n"
        "v_mfma_f32_16x16x32_bf16 v[20:23], v[216:219], a[248:251], v[20:23]\n"
        "ds_read_b128 a[236:239], v248 offset:49152\n"
        "v_exp_f32 v127, v127\n"
        "v_cvt_pk_bf16_f32 v193, v8, v12\n"
        "v_mfma_f32_16x16x32_bf16 v[80:83], a[252:255], v[112:115], 0\n"
        "ds_read_b128 a[240:243], v254 offset:16\n"
        "v_cvt_pk_bf16_f32 v119, v126, v127\n"
        "v_cvt_pk_bf16_f32 v196, v1, v5\n"
        "v_cvt_pk_bf16_f32 v197, v9, v13\n"
        "v_mfma_f32_16x16x32_bf16 v[80:83], a[252:255], v[116:119], v[80:83]\n"
        "ds_read_b128 a[244:247], v254 offset:6160\n"
        "v_cvt_pk_bf16_f32 v194, v16, v20\n"
        "v_cvt_pk_bf16_f32 v198, v17, v21\n"
        "v_cvt_pk_bf16_f32 v200, v2, v6\n"
        "v_mfma_f32_16x16x32_bf16 v[84:87], v[132:135], v[116:119], 0\n"
        "buffer_store_dwordx3 v[192:194], v250, s[16:19], s38 offen offset:0 sc1\n"
        "v_mfma_f32_16x16x32_bf16 v[84:87], v[128:131], v[112:115], v[84:87]\n"
        "ds_read_b128 a[248:251], v254 offset:36880\n"
        "v_rcp_f32 v80, v80\n"
        "v_cvt_pk_bf16_f32 v201, v10, v14\n"
        "v_mfma_f32_16x16x32_bf16 v[88:91], v[136:139], v[112:115], 0\n"
        "v_cvt_pk_bf16_f32 v202, v18, v22\n"
        "v_cvt_pk_bf16_f32 v204, v3, v7\n"
        "v_cvt_pk_bf16_f32 v205, v11, v15\n"
        "v_cvt_pk_bf16_f32 v206, v19, v23\n"
        "v_mfma_f32_16x16x32_bf16 v[88:91], v[140:143], v[116:119], v[88:91]\n"
        "buffer_store_dwordx3 v[196:198], v250, s[16:19], s38 offen offset:384 sc1\n"
        "v_mfma_f32_16x16x32_bf16 v[92:95], v[148:151], v[116:119], 0\n"
        "v_mul_f32 v84, v80, v84\n"
        "v_mul_f32 v85, v80, v85\n"
        "v_mul_f32 v86, v80, v86\n"
        "v_mul_f32 v87, v80, v87\n"
        "v_mfma_f32_16x16x32_bf16 v[92:95], v[144:147], v[112:115], v[92:95]\n"
        "v_cvt_pk_bf16_f32 v84, v84, v85\n"
        "v_cvt_pk_bf16_f32 v85, v86, v87\n"
        "v_mul_f32 v88, v80, v88\n"
        "v_mul_f32 v89, v80, v89\n"
        "v_mul_f32 v90, v80, v90\n"
        "v_mul_f32 v91, v80, v91\n"
        "v_cvt_pk_bf16_f32 v86, v88, v89\n"
        "v_cvt_pk_bf16_f32 v87, v90, v91\n"
        "v_mul_f32 v92, v80, v92\n"
        "v_mul_f32 v93, v80, v93\n"
        "v_mul_f32 v94, v80, v94\n"
        "v_mul_f32 v95, v80, v95\n"
        "v_cvt_pk_bf16_f32 v92, v92, v93\n"
        "v_cvt_pk_bf16_f32 v93, v94, v95\n"
        "ds_write_b128 v245, v[84:87] offset:768\n"
        "ds_write_b64 v246, v[92:93] offset:768\n"
        "s_waitcnt lgkmcnt(0)\n"
        "s_barrier\n"
        "ds_read_b128 v[128:131], v247 offset:512\n"
        "s_waitcnt lgkmcnt(0)\n"
        "v_mfma_f32_16x16x32_bf16 v[24:27], v[128:131], a[216:219], 0\n"
        "ds_read_b128 v[132:135], v247 offset:4608\n"
        "ds_read_b128 v[136:139], v247 offset:8704\n"
        "s_waitcnt lgkmcnt(1)\n"
        "v_mfma_f32_16x16x32_bf16 v[24:27], v[132:135], a[220:223], v[24:27]\n"
        "s_waitcnt lgkmcnt(0)\n"
        "ds_read_b128 v[140:143], v247 offset:12800\n"
        "ds_read_b128 v[144:147], v247 offset:16896\n"
        "v_mfma_f32_16x16x32_bf16 v[24:27], v[136:139], a[224:227], v[24:27]\n"
        "s_waitcnt lgkmcnt(1)\n"
        "ds_read_b128 v[148:151], v247 offset:20992\n"
        "ds_read_b128 a[216:219], v254 offset:43024\n"
        "v_mfma_f32_16x16x32_bf16 v[24:27], v[140:143], a[228:231], v[24:27]\n"
        "s_waitcnt lgkmcnt(2)\n"
        "buffer_store_dwordx3 v[200:202], v250, s[16:19], s38 offen offset:768 sc1\n"
        "v_mfma_f32_16x16x32_bf16 v[24:27], v[144:147], a[232:235], v[24:27]\n"
        "s_waitcnt lgkmcnt(1)\n"
        "ds_read_b128 a[220:223], v254 offset:12304\n"
        "ds_read_b128 a[224:227], v254 offset:49168\n"
        "v_mfma_f32_16x16x32_bf16 v[24:27], v[148:151], a[236:239], v[24:27]\n"
        "ds_read_b128 a[228:231], v248 offset:32\n"
        "ds_read_b128 a[232:235], v248 offset:6176\n"
        "v_mfma_f32_16x16x32_bf16 v[28:31], v[128:131], a[240:243], 0\n"
        "buffer_store_dwordx3 v[204:206], v250, s[16:19], s38 offen offset:1152 sc1\n"
        "v_mfma_f32_16x16x32_bf16 v[28:31], v[132:135], a[244:247], v[28:31]\n"
        "ds_read_b128 a[236:239], v248 offset:36896\n"
        "ds_read_b128 a[240:243], v248 offset:43040\n"
        "v_mfma_f32_16x16x32_bf16 v[28:31], v[136:139], a[248:251], v[28:31]\n"
        "s_waitcnt lgkmcnt(6)\n"
        "ds_read_b128 a[244:247], v248 offset:12320\n"
        "ds_read_b128 a[248:251], v248 offset:49184\n"
        "v_mfma_f32_16x16x32_bf16 v[28:31], v[140:143], a[216:219], v[28:31]\n"
        "s_waitcnt lgkmcnt(7)\n"
        "ds_read_b128 a[216:219], v254 offset:48\n"
        "v_mfma_f32_16x16x32_bf16 v[28:31], v[144:147], a[220:223], v[28:31]\n"
        "s_waitcnt lgkmcnt(7)\n"
        "ds_read_b128 a[220:223], v254 offset:6192\n"
        "v_mfma_f32_16x16x32_bf16 v[28:31], v[148:151], a[224:227], v[28:31]\n"
        "s_waitcnt lgkmcnt(7)\n"
        "ds_read_b128 a[224:227], v254 offset:36912\n"
        "v_mfma_f32_16x16x32_bf16 v[32:35], v[128:131], a[228:231], 0\n"
        "s_waitcnt lgkmcnt(7)\n"
        "ds_read_b128 a[228:231], v254 offset:43056\n"
        "v_mfma_f32_16x16x32_bf16 v[32:35], v[132:135], a[232:235], v[32:35]\n"
        "s_waitcnt lgkmcnt(7)\n"
        "ds_read_b128 a[232:235], v254 offset:12336\n"
        "v_mfma_f32_16x16x32_bf16 v[32:35], v[136:139], a[236:239], v[32:35]\n"
        "s_waitcnt lgkmcnt(7)\n"
        "ds_read_b128 a[236:239], v254 offset:49200\n"
        "v_mfma_f32_16x16x32_bf16 v[32:35], v[140:143], a[240:243], v[32:35]\n"
        "s_waitcnt lgkmcnt(7)\n"
        "ds_read_b128 a[240:243], v248 offset:64\n"
        "v_cvt_pk_bf16_f32 v180, v24, v28\n"
        "v_mfma_f32_16x16x32_bf16 v[32:35], v[144:147], a[244:247], v[32:35]\n"
        "s_waitcnt lgkmcnt(7)\n"
        "ds_read_b128 a[244:247], v248 offset:6208\n"
        "v_cvt_pk_bf16_f32 v184, v25, v29\n"
        "v_mfma_f32_16x16x32_bf16 v[32:35], v[148:151], a[248:251], v[32:35]\n"
        "s_waitcnt lgkmcnt(7)\n"
        "ds_read_b128 a[248:251], v248 offset:36928\n"
        "v_cvt_pk_bf16_f32 v220, v26, v30\n"
        "v_mfma_f32_16x16x32_bf16 v[36:39], v[128:131], a[216:219], 0\n"
        "s_waitcnt lgkmcnt(7)\n"
        "ds_read_b128 a[216:219], v248 offset:43072\n"
        "v_cvt_pk_bf16_f32 v224, v27, v31\n"
        "v_mfma_f32_16x16x32_bf16 v[36:39], v[132:135], a[220:223], v[36:39]\n"
        "s_waitcnt lgkmcnt(7)\n"
        "ds_read_b128 a[220:223], v248 offset:12352\n"
        "v_mfma_f32_16x16x32_bf16 v[36:39], v[136:139], a[224:227], v[36:39]\n"
        "s_waitcnt lgkmcnt(7)\n"
        "ds_read_b128 a[224:227], v248 offset:49216\n"
        "v_mfma_f32_16x16x32_bf16 v[36:39], v[140:143], a[228:231], v[36:39]\n"
        "s_waitcnt lgkmcnt(7)\n"
        "ds_read_b128 a[228:231], v254 offset:80\n"
        "v_mfma_f32_16x16x32_bf16 v[36:39], v[144:147], a[232:235], v[36:39]\n"
        "s_waitcnt lgkmcnt(7)\n"
        "ds_read_b128 a[232:235], v254 offset:6224\n"
        "v_mfma_f32_16x16x32_bf16 v[36:39], v[148:151], a[236:239], v[36:39]\n"
        "s_waitcnt lgkmcnt(7)\n"
        "ds_read_b128 a[236:239], v254 offset:36944\n"
        "v_mfma_f32_16x16x32_bf16 v[40:43], v[128:131], a[240:243], 0\n"
        "s_waitcnt lgkmcnt(7)\n"
        "ds_read_b128 a[240:243], v254 offset:43088\n"
        "v_mfma_f32_16x16x32_bf16 v[40:43], v[132:135], a[244:247], v[40:43]\n"
        "s_waitcnt lgkmcnt(7)\n"
        "ds_read_b128 a[244:247], v254 offset:12368\n"
        "v_mfma_f32_16x16x32_bf16 v[40:43], v[136:139], a[248:251], v[40:43]\n"
        "s_waitcnt lgkmcnt(7)\n"
        "ds_read_b128 a[248:251], v254 offset:49232\n"
        "v_mfma_f32_16x16x32_bf16 v[40:43], v[140:143], a[216:219], v[40:43]\n"
        "s_waitcnt lgkmcnt(7)\n"
        "v_cvt_pk_bf16_f32 v181, v32, v36\n"
        "v_cvt_pk_bf16_f32 v185, v33, v37\n"
        "v_mfma_f32_16x16x32_bf16 v[40:43], v[144:147], a[220:223], v[40:43]\n"
        "s_waitcnt lgkmcnt(6)\n"
        "v_cvt_pk_bf16_f32 v221, v34, v38\n"
        "v_cvt_pk_bf16_f32 v225, v35, v39\n"
        "v_mfma_f32_16x16x32_bf16 v[40:43], v[148:151], a[224:227], v[40:43]\n"
        "s_waitcnt lgkmcnt(5)\n"
        "v_mfma_f32_16x16x32_bf16 v[44:47], v[128:131], a[228:231], 0\n"
        "s_waitcnt lgkmcnt(4)\n"
        "v_mfma_f32_16x16x32_bf16 v[44:47], v[132:135], a[232:235], v[44:47]\n"
        "s_waitcnt lgkmcnt(3)\n"
        "v_mfma_f32_16x16x32_bf16 v[44:47], v[136:139], a[236:239], v[44:47]\n"
        "s_waitcnt lgkmcnt(2)\n"
        "v_mfma_f32_16x16x32_bf16 v[44:47], v[140:143], a[240:243], v[44:47]\n"
        "s_waitcnt lgkmcnt(1)\n"
        "v_mfma_f32_16x16x32_bf16 v[44:47], v[144:147], a[244:247], v[44:47]\n"
        "s_waitcnt lgkmcnt(0)\n"
        "v_mfma_f32_16x16x32_bf16 v[44:47], v[148:151], a[248:251], v[44:47]\n"
        "s_nop 7\n"
        "v_cvt_pk_bf16_f32 v182, v40, v44\n"
        "buffer_store_dwordx3 v[180:182], v250, s[16:19], s39 offen offset:0 sc1\n"
        "v_cvt_pk_bf16_f32 v186, v41, v45\n"
        "buffer_store_dwordx3 v[184:186], v250, s[16:19], s39 offen offset:384 sc1\n"
        "v_cvt_pk_bf16_f32 v222, v42, v46\n"
        "buffer_store_dwordx3 v[220:222], v250, s[16:19], s39 offen offset:768 sc1\n"
        "v_cvt_pk_bf16_f32 v226, v43, v47\n"
        "buffer_store_dwordx3 v[224:226], v250, s[16:19], s39 offen offset:1152 sc1\n"
        : "+{v240}"(xr[0]), "+{v241}"(xr[1]), "+{v251}"(mr[0]), "+{v252}"(mr[1]), "+{v242}"(dm[0]), "+{v243}"(dm[1]), "+{v244}"(dm[2]), "+{v245}"(ow), "+{v246}"(ow2), "+{v247}"(ord), "+{v248}"(wor), "+{v249}"(ms), "+{v250}"(st), "+{v142}"(l16), "+{v170}"(id0), "+{v171}"(id1), "+{v139}"(rs), "+{v144}"(rr), "+{v140}"(oa), "+{v141}"(ob), "+{v147}"(od), "+{v145}"(rbs), "+{v146}"(rbw), "+{v254}"(wor2), "+{v255}"(ml), "+{v148}"(pf0), "+{v149}"(pf1), "+{v128}"(wsrc[0]), "+{v129}"(wsrc[1]), "+{v130}"(wsrc[2]), "+{v131}"(wrd[0]), "+{v132}"(wrd[1]), "+{v133}"(wrd[2]), "+{s47}"(ya), "+{s36}"(mk), "+{s37}"(raw), "+{s43}"(x0), "+{s44}"(x1)
        : "{s[8:11]}"(srdx), "{s[12:15]}"(srdm), "{s[16:19]}"(srdo), "{s[20:23]}"(srdw), "{s[52:55]}"(srdwo), "{s[56:59]}"(srdrp), "{s[60:63]}"(srdrb), "{s42}"(wv), "s"(ldsb)
        : "v0", "v1", "v2", "v3", "v4", "v5", "v6", "v7", "v8", "v9", "v10", "v11", "v12", "v13", "v14", "v15", "v16", "v17", "v18", "v19", "v20", "v21", "v22", "v23", "v24", "v25", "v26", "v27", "v28", "v29", "v30", "v31", "v32", "v33", "v34", "v35", "v36", "v37", "v38", "v39", "v40", "v41", "v42", "v43", "v44", "v45", "v46", "v47", "v48", "v49", "v50", "v51", "v52", "v53", "v54", "v55", "v56", "v57", "v58", "v59", "v60", "v61", "v62", "v63", "v64", "v65", "v66", "v67", "v68", "v69", "v70", "v71", "v72", "v73", "v74", "v75", "v76", "v77", "v78", "v79", "v80", "v81", "v82", "v83", "v84", "v85", "v86", "v87", "v88", "v89", "v90", "v91", "v92", "v93", "v94", "v95", "v96", "v97", "v98", "v99", "v100", "v101", "v102", "v103", "v104", "v105", "v106", "v107", "v108", "v109", "v110", "v111", "v112", "v113", "v114", "v115", "v116", "v117", "v118", "v119", "v120", "v121", "v122", "v123", "v124", "v125", "v126", "v127", "v134", "v135", "v136", "v137", "v138", "v143", "v150", "v151", "v152", "v153", "v154", "v155", "v156", "v157", "v158", "v159", "v160", "v161", "v162", "v163", "v164", "v165", "v166", "v167", "v168", "v169", "v172", "v173", "v174", "v175", "v176", "v177", "v178", "v179", "v180", "v181", "v182", "v183", "v184", "v185", "v186", "v187", "v188", "v189", "v190", "v191", "v192", "v193", "v194", "v195", "v196", "v197", "v198", "v199", "v200", "v201", "v202", "v203", "v204", "v205", "v206", "v207", "v208", "v209", "v210", "v211", "v212", "v213", "v214", "v215", "v216", "v217", "v218", "v219", "v220", "v221", "v222", "v223", "v224", "v225", "v226", "v227", "v228", "v229", "v230", "v231", "v232", "v233", "v234", "v235", "v236", "v237", "v238", "v239", "v253", "a0", "a1", "a2", "a3", "a4", "a5", "a6", "a7", "a8", "a9", "a10", "a11", "a12", "a13", "a14", "a15", "a16", "a17", "a18", "a19", "a20", "a21", "a22", "a23", "a24", "a25", "a26", "a27", "a28", "a29", "a30", "a31", "a32", "a33", "a34", "a35", "a36", "a37", "a38", "a39", "a40", "a41", "a42", "a43", "a44", "a45", "a46", "a47", "a48", "a49", "a50", "a51", "a52", "a53", "a54", "a55", "a56", "a57", "a58", "a59", "a60", "a61", "a62", "a63", "a64", "a65", "a66", "a67", "a68", "a69", "a70", "a71", "a72", "a73", "a74", "a75", "a76", "a77", "a78", "a79", "a80", "a81", "a82", "a83", "a84", "a85", "a86", "a87", "a88", "a89", "a90", "a91", "a92", "a93", "a94", "a95", "a96", "a97", "a98", "a99", "a100", "a101", "a102", "a103", "a104", "a105", "a106", "a107", "a108", "a109", "a110", "a111", "a112", "a113", "a114", "a115", "a116", "a117", "a118", "a119", "a120", "a121", "a122", "a123", "a124", "a125", "a126", "a127", "a128", "a129", "a130", "a131", "a132", "a133", "a134", "a135", "a136", "a137", "a138", "a139", "a140", "a141", "a142", "a143", "a144", "a145", "a146", "a147", "a148", "a149", "a150", "a151", "a152", "a153", "a154", "a155", "a156", "a157", "a158", "a159", "a160", "a161", "a162", "a163", "a164", "a165", "a166", "a167", "a168", "a169", "a170", "a171", "a172", "a173", "a174", "a175", "a176", "a177", "a178", "a179", "a180", "a181", "a182", "a183", "a184", "a185", "a186", "a187", "a188", "a189", "a190", "a191", "a192", "a193", "a194", "a195", "a196", "a197", "a198", "a199", "a200", "a201", "a202", "a203", "a204", "a205", "a206", "a207", "a208", "a209", "a210", "a211", "a212", "a213", "a214", "a215", "a216", "a217", "a218", "a219", "a220", "a221", "a222", "a223", "a224", "a225", "a226", "a227", "a228", "a229", "a230", "a231", "a232", "a233", "a234", "a235", "a236", "a237", "a238", "a239", "a240", "a241", "a242", "a243", "a244", "a245", "a246", "a247", "a248", "a249", "a250", "a251", "a252", "a253", "a254", "a255", "s24", "s25", "s26", "s27", "s28", "s29", "s30", "s31", "s34", "s35", "s38", "s39", "s40", "s41", "s45", "s46", "s48", "s49", "s50", "s51", "s64", "s65", "s66", "s67", "vcc", "scc", "memory");
}


__global__ __launch_bounds__(256, 1) __attribute__((aligned(4096))) void win_attn_kernel(const char* __restrict__ x, const char* __restrict__ wqkv,
                                                          const char* __restrict__ wo, const char* __restrict__ rpb,
                                                          const char* __restrict__ rpi, const char* __restrict__ mask,
                                                          char* __restrict__ out) {
    __shared__ __attribute__((aligned(16))) char smem[163840];
    unsigned tid = threadIdx.x, w;
    const unsigned ldsb = (unsigned)(size_t)smem;
    asm volatile(
        "s_load_dwordx2 s[8:9], s[0:1], 0\n"
        "s_load_dwordx2 s[20:21], s[0:1], 8\n"
        "s_load_dwordx2 s[52:53], s[0:1], 16\n"
        "s_load_dwordx2 s[60:61], s[0:1], 24\n"
        "s_load_dwordx2 s[56:57], s[0:1], 32\n"
        "s_load_dwordx2 s[12:13], s[0:1], 40\n"
        "s_load_dwordx2 s[16:17], s[0:1], 48\n"
        "v_readfirstlane_b32 s42, v0\n"
        "v_and_b32 v1, 63, v0\n"
        "v_lshlrev_b32 v145, 3, v0\n"
        "s_lshr_b32 s42, s42, 6\n"
        "s_bfe_u32 s41, s2, 0x50003\n"
        "s_lshl2_add_u32 s41, s41, s42\n"
        "s_lshl_b32 s49, s41, 8\n"
        "v_and_b32 v10, 1, v0\n"
        "v_lshl_add_u32 v10, v10, 7, s49\n"
        "v_min_u32 v10, 32000, v10\n"
        "s_getpc_b64 s[38:39]\n"
        "global_load_dword v66, v10, s[38:39]\n"
        "v_bfe_u32 v2, v0, 4, 2\n"
        "s_and_b32 s51, s42, 1\n"
        "s_lshl_b32 s66, s51, 3\n"
        "v_xor_b32 v11, v1, v2\n"
        "v_xor_b32 v11, s66, v11\n"
        "v_lshlrev_b32 v139, 4, v11\n"
        "v_xor_b32 v144, 64, v139\n"
        "s_lshl_b32 s34, s42, 12\n"
        "s_mul_i32 s40, s42, 3072\n"
        "s_and_b32 s31, s2, 63\n"
        "s_lshl_b32 s31, s31, 3\n"
        "s_add_u32 s31, s31, 4\n"
        "s_lshr_b32 s45, s2, 6\n"
        "s_lshl_b32 s45, s45, 3\n"
        "s_add_u32 s47, s45, s42\n"
        "s_add_u32 s47, s47, 68\n"
        "s_mul_i32 s47, s47, 196608\n"
        "s_lshl_b32 s46, s42, 1\n"
        "s_lshl_b32 s49, s41, 11\n"
        "v_lshl_add_u32 v148, v1, 7, s49\n"
        "v_lshl_or_b32 v148, v2, 28, v148\n"
        "s_mov_b32 s62, 1800\n"
        "s_mov_b32 s63, 0x20000\n"
        "s_mov_b32 s58, 16384\n"
        "s_mov_b32 s59, 0x20000\n"
        "s_mov_b32 s22, 221184\n"
        "s_mov_b32 s23, 0x20000\n"
        "s_mov_b32 s10, 100663296\n"
        "s_mov_b32 s11, 0x20000\n"
        "v_mul_u32_u24 v15, 171, v1\n"
        "v_lshrrev_b32 v15, 12, v15\n"
        "v_mul_u32_u24 v13, 24, v15\n"
        "v_sub_u32 v18, v1, v13\n"
        "v_bfe_u32 v21, v15, 1, 1\n"
        "v_add_u32 v13, s31, v15\n"
        "v_and_b32 v13, 0x1ff, v13\n"
        "v_mul_u32_u24 v13, 384, v13\n"
        "v_bitop3_b32 v14, v21, v18, s46 bitop3:0x36\n"
        "v_lshl_add_u32 v242, v14, 4, v13\n"
        "v_add_u32 v12, 64, v1\n"
        "v_mul_u32_u24 v16, 171, v12\n"
        "v_lshrrev_b32 v16, 12, v16\n"
        "v_mul_u32_u24 v13, 24, v16\n"
        "v_sub_u32 v19, v12, v13\n"
        "v_bfe_u32 v22, v16, 1, 1\n"
        "v_add_u32 v13, s31, v16\n"
        "v_and_b32 v13, 0x1ff, v13\n"
        "v_mul_u32_u24 v13, 384, v13\n"
        "v_bitop3_b32 v14, v22, v19, s46 bitop3:0x36\n"
        "v_lshl_add_u32 v243, v14, 4, v13\n"
        "v_add_u32 v12, 128, v1\n"
        "v_mul_u32_u24 v17, 171, v12\n"
        "v_lshrrev_b32 v17, 12, v17\n"
        "v_mul_u32_u24 v13, 24, v17\n"
        "v_sub_u32 v20, v12, v13\n"
        "v_bfe_u32 v23, v17, 1, 1\n"
        "v_add_u32 v13, s31, v17\n"
        "v_and_b32 v13, 0x1ff, v13\n"
        "v_mul_u32_u24 v13, 384, v13\n"
        "v_bitop3_b32 v14, v23, v20, s46 bitop3:0x36\n"
        "v_lshl_add_u32 v244, v14, 4, v13\n"
        "s_mov_b32 s14, 33554432\n"
        "s_mov_b32 s15, 0x20000\n"
        "v_bfe_u32 v4, v0, 4, 1\n"
        "s_lshl_b32 s66, s51, 2\n"
        "v_bitop3_b32 v11, v4, v1, s66 bitop3:0x36\n"
        "v_lshlrev_b32 v249, 4, v11\n"
        "s_waitcnt lgkmcnt(0)\n"
        "buffer_load_dword v64, v148, s[20:23], 0 offen\n"
        "buffer_load_dwordx2 v[28:29], v145, s[60:63], 0 offen\n"
        "s_add_u32 m0, s34, 122880\n"
        "s_mov_b32 s64, s34\n"
        "buffer_load_dwordx4 v139, s[56:59], s64 offen lds\n"
        "v_xor_b32 v149, 32, v249\n"
        "s_lshl_b32 s48, s42, 11\n"
        "s_lshl_b32 s36, s2, 13\n"
        "s_add_u32 s36, s36, s48\n"
        "s_add_u32 s36, s36, 4194304\n"
        "s_add_u32 s48, s48, 147456\n"
        "s_mov_b32 s54, 73728\n"
        "s_mov_b32 s55, 0x20000\n"
        "s_mov_b32 s18, 100663296\n"
        "s_mov_b32 s19, 0x20000\n"
        "s_mul_i32 s65, s42, 9216\n"
        "v_lshl_or_b32 v13, v21, 1, s66\n"
        "buffer_load_dwordx4 v139, s[56:59], s64 offen offset:1024 lds\n"
        "v_xor_b32 v13, v13, v18\n"
        "v_mul_u32_u24 v14, 1152, v15\n"
        "v_lshl_add_u32 v128, v13, 4, v14\n"
        "v_lshl_or_b32 v13, v22, 1, s66\n"
        "v_xor_b32 v13, v13, v19\n"
        "v_mul_u32_u24 v14, 1152, v16\n"
        "v_lshl_add_u32 v129, v13, 4, v14\n"
        "v_subrev_u32 v129, 1024, v129\n"
        "v_lshl_or_b32 v13, v23, 1, s66\n"
        "v_xor_b32 v13, v13, v20\n"
        "v_mul_u32_u24 v14, 1152, v17\n"
        "v_lshl_add_u32 v130, v13, 4, v14\n"
        "buffer_load_dwordx4 v144, s[56:59], s64 offen offset:2048 lds\n"
        "v_subrev_u32 v130, 2048, v130\n"
        "v_bfe_u32 v6, v0, 2, 2\n"
        "v_and_b32 v7, 3, v0\n"
        "v_lshlrev_b32 v150, 4, v1\n"
        "v_lshl_add_u32 v8, v6, 3, v7\n"
        "v_and_b32 v9, 0xf0, v150\n"
        "v_lshlrev_b32 v146, 2, v0\n"
        "v_add_u32 v146, 139264, v146\n"
        "v_lshlrev_b32 v10, 5, v2\n"
        "v_xor_b32 v10, v10, v9\n"
        "v_lshl_add_u32 v10, v8, 8, v10\n"
        "v_add_u32 v140, 122880, v10\n"
        "buffer_load_dwordx4 v144, s[56:59], s64 offen offset:3072 lds\n"
        "v_xor_b32 v141, 16, v140\n"
        "v_xor_b32 v142, 128, v140\n"
        "v_xor_b32 v143, 144, v140\n"
        "s_lshl_b32 s67, s42, 10\n"
        "s_add_u32 s67, s67, 139264\n"
        "v_bfe_u32 v10, v0, 1, 3\n"
        "v_xor_b32 v10, v10, v2\n"
        "v_lshlrev_b32 v10, 4, v10\n"
        "v_mul_u32_u24 v11, 384, v8\n"
        "v_add_u32 v11, 73728, v11\n"
        "v_add_u32 v240, v10, v11\n"
        "v_xor_b32 v241, 64, v240\n"
        "s_add_u32 m0, s40, 73728\n"
        "s_sub_u32 s64, s47, 12582912\n"
        "buffer_load_dwordx4 v242, s[8:11], s64 offen nt lds\n"
        "v_lshl_add_u32 v10, v8, 7, v10\n"
        "v_add_u32 v251, 147456, v10\n"
        "v_xor_b32 v252, 64, v251\n"
        "v_lshl_add_u32 v10, v2, 3, v6\n"
        "v_mul_u32_u24 v10, 384, v10\n"
        "v_and_b32 v11, 1, v0\n"
        "v_lshl_add_u32 v10, v11, 3, v10\n"
        "v_and_b32 v12, 2, v6\n"
        "v_lshl_or_b32 v12, v4, 2, v12\n"
        "s_mul_i32 s35, s42, 6\n"
        "v_bfe_u32 v13, v0, 1, 1\n"
        "v_add_u32 v13, s35, v13\n"
        "s_add_u32 m0, s40, 74752\n"
        "s_sub_u32 s64, s47, 12582912\n"
        "buffer_load_dwordx4 v243, s[8:11], s64 offen nt lds\n"
        "v_xor_b32 v14, v13, v12\n"
        "v_lshl_add_u32 v131, v14, 4, v10\n"
        "v_add_u32 v136, 98304, v131\n"
        "v_add_u32 v13, 2, v13\n"
        "v_xor_b32 v14, v13, v12\n"
        "v_lshl_add_u32 v132, v14, 4, v10\n"
        "v_add_u32 v137, 98304, v132\n"
        "v_add_u32 v13, 2, v13\n"
        "v_xor_b32 v14, v13, v12\n"
        "v_lshl_add_u32 v133, v14, 4, v10\n"
        "v_add_u32 v138, 98304, v133\n"
        "v_lshlrev_b32 v10, 4, v11\n"
        "s_add_u32 m0, s40, 75776\n"
        "s_sub_u32 s64, s47, 12582912\n"
        "buffer_load_dwordx4 v244, s[8:11], s64 offen nt lds\n"
        "v_mov_b32 v12, 0x3fb9\n"
        "v_cmp_eq_u32 vcc, v6, v2\n"
        "v_lshlrev_b32 v10, v10, v12\n"
        "v_bfe_i32 v13, v0, 0, 1\n"
        "v_cndmask_b32 v10, 0, v10, vcc\n"
        "v_cmp_gt_u32 vcc, 2, v7\n"
        "v_and_b32 v13, 0xaaaaaaaa, v13\n"
        "v_or_b32 v255, 0x44444444, v13\n"
        "v_cndmask_b32 v170, 0, v10, vcc\n"
        "v_xor_b32 v171, v170, v10\n"
        "v_mov_b64 v[188:189], v[170:171]\n"
        "v_mov_b64 v[210:211], v[170:171]\n"
        "s_add_u32 m0, s40, 86016\n"
        "s_sub_u32 s64, s47, 11796480\n"
        "buffer_load_dwordx4 v242, s[8:11], s64 offen nt lds\n"
        "v_mov_b64 v[228:229], v[170:171]\n"
        "v_bfe_u32 v5, v0, 5, 1\n"
        "v_and_b32 v3, 15, v0\n"
        "v_lshl_add_u32 v10, v2, 10, v9\n"
        "s_add_u32 s49, s34, 73728\n"
        "v_add_u32 v245, s49, v10\n"
        "s_lshl_b32 s49, s51, 9\n"
        "s_add_u32 s49, s49, 73728\n"
        "v_add_u32 v247, s49, v10\n"
        "v_lshl_add_u32 v10, v5, 10, v9\n"
        "v_lshl_add_u32 v10, v4, 3, v10\n"
        "s_lshl_b32 s49, s42, 11\n"
        "s_add_u32 m0, s40, 87040\n"
        "s_sub_u32 s64, s47, 11796480\n"
        "buffer_load_dwordx4 v243, s[8:11], s64 offen nt lds\n"
        "s_add_u32 s49, s49, 90112\n"
        "v_add_u32 v246, s49, v10\n"
        "s_lshr_b32 s35, s42, 1\n"
        "s_mul_i32 s49, s35, 18432\n"
        "v_mul_u32_u24 v12, 1536, v2\n"
        "v_add_u32 v12, s49, v12\n"
        "v_mad_u32_u24 v13, v9, 6, v12\n"
        "v_lshl_add_u32 v248, v4, 4, v13\n"
        "v_mad_i32_i24 v254, v4, -16, v13\n"
        "v_mul_u32_u24 v10, 196608, v2\n"
        "v_lshl_add_u32 v10, v3, 2, v10\n"
        "s_lshl_b32 s49, s35, 6\n"
        "s_add_u32 m0, s40, 88064\n"
        "s_sub_u32 s64, s47, 11796480\n"
        "buffer_load_dwordx4 v244, s[8:11], s64 offen nt lds\n"
        "s_add_u32 s49, s49, 128\n"
        "v_add_u32 v250, s49, v10\n"
        "s_mul_i32 s26, s35, 192\n"
        "s_sub_u32 s26, s26, 128\n"
        "s_mul_i32 s43, s31, 384\n"
        "s_add_u32 s44, s31, 4\n"
        "s_and_b32 s44, s44, 511\n"
        "s_mul_i32 s44, s44, 384\n"
        "s_add_u32 s37, s45, s66\n"
        "s_add_u32 s37, s37, 4\n"
        "s_mul_i32 s37, s37, 196608\n"
        "s_mul_i32 s51, s51, 36864\n"
        "s_mov_b32 m0, s48\n"
        "s_sub_u32 s64, s36, 4194304\n"
        "buffer_load_dwordx4 v249, s[12:15], s64 offen nt lds\n"
        "s_mul_i32 s66, s35, 18432\n"
        "s_add_u32 s66, s66, s51\n"
        "v_add_u32 v12, s51, v12\n"
        "v_bfrev_b32 v14, v0\n"
        "v_bfe_u32 v10, v14, 28, 2\n"
        "v_mad_u32_u24 v10, v10, 6, v7\n"
        "v_bfe_u32 v13, v0, 1, 3\n"
        "v_mad_u32_u24 v13, v13, 6, v11\n"
        "v_add_u32 v13, 4, v13\n"
        "v_xor_b32 v10, v10, v4\n"
        "v_xor_b32 v13, v13, v4\n"
        "v_lshl_add_u32 v236, v10, 4, v12\n"
        "buffer_load_dwordx4 v149, s[12:15], s64 offen offset:1024 nt lds\n"
        "v_lshl_add_u32 v237, v13, 4, v12\n"
        "v_lshrrev_b32 v10, 2, v1\n"
        "v_mul_u32_u24 v10, 192, v10\n"
        "v_add_u32 v10, s66, v10\n"
        "v_lshlrev_b32 v13, 5, v4\n"
        "v_add_u32 v12, v10, v13\n"
        "v_sub_u32 v13, v10, v13\n"
        "v_lshrrev_b32 v14, 30, v14\n"
        "v_lshl_add_u32 v234, v14, 3, v12\n"
        "v_lshl_add_u32 v235, v14, 3, v13\n"
        "v_lshl_add_u32 v238, v7, 3, v12\n"
        "v_lshl_add_u32 v239, v7, 3, v13\n"
        "s_lshl_b32 s46, s35, 8\n"
        "s_mul_i32 s35, s35, -192\n"
        "s_movk_i32 s49, 1536\n"
        "v_mul_u32_u24 v10, 171, v1\n"
        "v_lshrrev_b32 v10, 11, v10\n"
        "v_mad_i32_i24 v13, v10, -12, v1\n"
        "v_lshrrev_b32 v13, 3, v13\n"
        "v_mul_u32_u24 v14, 192, v10\n"
        "v_add_u32 v14, s46, v14\n"
        "v_mad_i32_i24 v14, v13, s35, v14\n"
        "v_and_b32 v13, 4, v10\n"
        "v_lshlrev_b32 v13, 3, v13\n"
        "v_xad_u32 v124, v150, v13, v14\n"
        "v_lshrrev_b32 v13, 2, v10\n"
        "v_and_or_b32 v13, v10, 8, v13\n"
        "v_mad_u32_u24 v127, v13, s49, v124\n"
        "v_add_u32 v12, 64, v1\n"
        "v_mul_u32_u24 v10, 171, v12\n"
        "v_lshrrev_b32 v10, 11, v10\n"
        "v_mad_i32_i24 v13, v10, -12, v12\n"
        "v_lshrrev_b32 v13, 3, v13\n"
        "v_mul_u32_u24 v14, 192, v10\n"
        "v_add_u32 v14, s46, v14\n"
        "v_mad_i32_i24 v14, v13, s35, v14\n"
        "v_and_b32 v13, 4, v10\n"
        "v_lshlrev_b32 v13, 3, v13\n"
        "v_xad_u32 v125, v150, v13, v14\n"
        "v_lshrrev_b32 v13, 2, v10\n"
        "v_and_or_b32 v13, v10, 8, v13\n"
        "v_mad_u32_u24 v232, v13, s49, v125\n"
        "v_add_u32 v12, 128, v1\n"
        "v_mul_u32_u24 v10, 171, v12\n"
        "v_lshrrev_b32 v10, 11, v10\n"
        "v_mad_i32_i24 v13, v10, -12, v12\n"
        "v_lshrrev_b32 v13, 3, v13\n"
        "v_mul_u32_u24 v14, 192, v10\n"
        "v_add_u32 v14, s46, v14\n"
        "v_mad_i32_i24 v14, v13, s35, v14\n"
        "v_and_b32 v13, 4, v10\n"
        "v_lshlrev_b32 v13, 3, v13\n"
        "v_xad_u32 v126, v150, v13, v14\n"
        "v_lshrrev_b32 v13, 2, v10\n"
        "v_and_or_b32 v13, v10, 8, v13\n"
        "v_mad_u32_u24 v233, v13, s49, v126\n"
        "s_mov_b32 s24, 0x3fb8aa3b\n"
        "s_mov_b32 s25, 0x4f800000\n"
        "s_mov_b32 s50, 0x3e553b95\n"
        "s_mov_b32 s27, 13\n"
        "s_mov_b32 s28, 98304\n"
        "s_mov_b32 s30, 73728\n"
        "s_mov_b32 s29, 122880\n"
        "v_mov_b32 v253, 0x3f803f80\n"
        "s_nop 0\n"
        "v_accvgpr_write_b32 a252, v253\n"
        "v_accvgpr_write_b32 a253, v253\n"
        "v_accvgpr_write_b32 a254, v253\n"
        "v_accvgpr_write_b32 a255, v253\n"
        "s_waitcnt vmcnt(12)\n"
        "v_lshlrev_b32 v32, 16, v28\n"
        "v_and_b32 v33, 0xffff0000, v28\n"
        "v_lshlrev_b32 v34, 16, v29\n"
        "v_and_b32 v35, 0xffff0000, v29\n"
        "v_mul_f32 v32, s24, v32\n"
        "v_mul_f32 v33, s24, v33\n"
        "v_mul_f32 v34, s24, v34\n"
        "v_mul_f32 v35, s24, v35\n"
        "ds_write_b32 v146, v32 offset:0\n"
        "ds_write_b32 v146, v33 offset:1024\n"
        "ds_write_b32 v146, v34 offset:2048\n"
        "ds_write_b32 v146, v35 offset:3072\n"
        "s_waitcnt lgkmcnt(0)\n"
        "s_waitcnt vmcnt(8)\n"
        "s_barrier\n"
        "ds_read_b128 v[0:3], v140 offset:0\n"
        "ds_read_b128 v[4:7], v141 offset:0\n"
        "ds_read_b128 v[8:11], v142 offset:0\n"
        "ds_read_b128 v[12:15], v143 offset:0\n"
        "ds_read_b128 v[16:19], v140 offset:1024\n"
        "ds_read_b128 v[20:23], v141 offset:1024\n"
        "ds_read_b128 v[24:27], v142 offset:1024\n"
        "ds_read_b128 v[28:31], v143 offset:1024\n"
        "ds_read_b128 v[32:35], v140 offset:8192\n"
        "ds_read_b128 v[36:39], v141 offset:8192\n"
        "ds_read_b128 v[40:43], v142 offset:8192\n"
        "ds_read_b128 v[44:47], v143 offset:8192\n"
        "ds_read_b128 v[48:51], v140 offset:9216\n"
        "ds_read_b128 v[52:55], v141 offset:9216\n"
        "ds_read_b128 v[56:59], v142 offset:9216\n"
        "ds_read_b128 v[60:63], v143 offset:9216\n"
        "s_add_u32 m0, s40, 0\n"
        "s_add_u32 s64, s65, 0\n"
        "buffer_load_dwordx4 v128, s[20:23], s64 offen lds\n"
        "s_waitcnt lgkmcnt(15)\n"
        "v_lshl_add_u32 v0, v0, 2, s67\n"
        "v_lshl_add_u32 v1, v1, 2, s67\n"
        "v_lshl_add_u32 v2, v2, 2, s67\n"
        "v_lshl_add_u32 v3, v3, 2, s67\n"
        "s_waitcnt lgkmcnt(14)\n"
        "v_lshl_add_u32 v4, v4, 2, s67\n"
        "v_lshl_add_u32 v5, v5, 2, s67\n"
        "v_lshl_add_u32 v6, v6, 2, s67\n"
        "v_lshl_add_u32 v7, v7, 2, s67\n"
        "ds_read_b32 v0, v0\n"
        "ds_read_b32 v1, v1\n"
        "ds_read_b32 v2, v2\n"
        "ds_read_b32 v3, v3\n"
        "ds_read_b32 v4, v4\n"
        "ds_read_b32 v5, v5\n"
        "ds_read_b32 v6, v6\n"
        "ds_read_b32 v7, v7\n"
        "buffer_load_dwordx4 v129, s[20:23], s64 offen offset:1024 lds\n"
        "s_waitcnt lgkmcnt(15)\n"
        "v_lshl_add_u32 v8, v8, 2, s67\n"
        "v_lshl_add_u32 v9, v9, 2, s67\n"
        "v_lshl_add_u32 v10, v10, 2, s67\n"
        "v_lshl_add_u32 v11, v11, 2, s67\n"
        "v_lshl_add_u32 v12, v12, 2, s67\n"
        "v_lshl_add_u32 v13, v13, 2, s67\n"
        "v_lshl_add_u32 v14, v14, 2, s67\n"
        "v_lshl_add_u32 v15, v15, 2, s67\n"
        "ds_read_b32 v8, v8\n"
        "ds_read_b32 v9, v9\n"
        "ds_read_b32 v10, v10\n"
        "ds_read_b32 v11, v11\n"
        "ds_read_b32 v12, v12\n"
        "ds_read_b32 v13, v13\n"
        "ds_read_b32 v14, v14\n"
        "ds_read_b32 v15, v15\n"
        "buffer_load_dwordx4 v130, s[20:23], s64 offen offset:2048 lds\n"
        "v_lshl_add_u32 v16, v16, 2, s67\n"
        "v_lshl_add_u32 v17, v17, 2, s67\n"
        "v_lshl_add_u32 v18, v18, 2, s67\n"
        "v_lshl_add_u32 v19, v19, 2, s67\n"
        "v_lshl_add_u32 v20, v20, 2, s67\n"
        "v_lshl_add_u32 v21, v21, 2, s67\n"
        "v_lshl_add_u32 v22, v22, 2, s67\n"
        "v_lshl_add_u32 v23, v23, 2, s67\n"
        "ds_read_b32 v16, v16\n"
        "ds_read_b32 v17, v17\n"
        "ds_read_b32 v18, v18\n"
        "ds_read_b32 v19, v19\n"
        "ds_read_b32 v20, v20\n"
        "ds_read_b32 v21, v21\n"
        "ds_read_b32 v22, v22\n"
        "ds_read_b32 v23, v23\n"
        "s_add_u32 m0, s40, 12288\n"
        "s_add_u32 s64, s65, 36864\n"
        "buffer_load_dwordx4 v128, s[20:23], s64 offen lds\n"
        "v_lshl_add_u32 v24, v24, 2, s67\n"
        "v_lshl_add_u32 v25, v25, 2, s67\n"
        "v_lshl_add_u32 v26, v26, 2, s67\n"
        "v_lshl_add_u32 v27, v27, 2, s67\n"
        "v_lshl_add_u32 v28, v28, 2, s67\n"
        "v_lshl_add_u32 v29, v29, 2, s67\n"
        "v_lshl_add_u32 v30, v30, 2, s67\n"
        "v_lshl_add_u32 v31, v31, 2, s67\n"
        "ds_read_b32 v24, v24\n"
        "ds_read_b32 v25, v25\n"
        "ds_read_b32 v26, v26\n"
        "ds_read_b32 v27, v27\n"
        "ds_read_b32 v28, v28\n"
        "ds_read_b32 v29, v29\n"
        "ds_read_b32 v30, v30\n"
        "ds_read_b32 v31, v31\n"
        "buffer_load_dwordx4 v129, s[20:23], s64 offen offset:1024 lds\n"
        "buffer_load_dwordx4 v130, s[20:23], s64 offen offset:2048 lds\n"
        "s_mov_b64 exec, 0\n"
        "s_cmp_eq_u32 s42, 0\n"
        "s_cbranch_scc1 41f\n"
        "s_cmp_eq_u32 s42, 1\n"
        "s_cbranch_scc1 42f\n"
        "s_cmp_eq_u32 s42, 2\n"
        "s_cbranch_scc1 43f\n"
        "s_branch 40f\n"
        ".fill 364, 4, 0xbf800000\n"
        "40:\n"
        "s_mov_b64 exec, -1\n"
        "s_add_u32 m0, s40, 24576\n"
        "s_add_u32 s64, s65, 73728\n"
        "buffer_load_dwordx4 v128, s[20:23], s64 offen lds\n"
        "v_lshl_add_u32 v32, v32, 2, s67\n"
        "v_lshl_add_u32 v33, v33, 2, s67\n"
        "v_lshl_add_u32 v34, v34, 2, s67\n"
        "v_lshl_add_u32 v35, v35, 2, s67\n"
        "s_waitcnt lgkmcnt(15)\n"
        "v_lshl_add_u32 v36, v36, 2, s67\n"
        "v_lshl_add_u32 v37, v37, 2, s67\n"
        "v_lshl_add_u32 v38, v38, 2, s67\n"
        "v_lshl_add_u32 v39, v39, 2, s67\n"
        "ds_read_b32 v32, v32\n"
        "ds_read_b32 v33, v33\n"
        "ds_read_b32 v34, v34\n"
        "ds_read_b32 v35, v35\n"
        "ds_read_b32 v36, v36\n"
        "ds_read_b32 v37, v37\n"
        "ds_read_b32 v38, v38\n"
        "ds_read_b32 v39, v39\n"
        "buffer_load_dwordx4 v129, s[20:23], s64 offen offset:1024 lds\n"
        "v_lshl_add_u32 v40, v40, 2, s67\n"
        "v_lshl_add_u32 v41, v41, 2, s67\n"
        "v_lshl_add_u32 v42, v42, 2, s67\n"
        "v_lshl_add_u32 v43, v43, 2, s67\n"
        "v_lshl_add_u32 v44, v44, 2, s67\n"
        "v_lshl_add_u32 v45, v45, 2, s67\n"
        "v_lshl_add_u32 v46, v46, 2, s67\n"
        "v_lshl_add_u32 v47, v47, 2, s67\n"
        "ds_read_b32 v40, v40\n"
        "ds_read_b32 v41, v41\n"
        "ds_read_b32 v42, v42\n"
        "ds_read_b32 v43, v43\n"
        "ds_read_b32 v44, v44\n"
        "ds_read_b32 v45, v45\n"
        "ds_read_b32 v46, v46\n"
        "ds_read_b32 v47, v47\n"
        "buffer_load_dwordx4 v130, s[20:23], s64 offen offset:2048 lds\n"
        "v_lshl_add_u32 v48, v48, 2, s67\n"
        "v_lshl_add_u32 v49, v49, 2, s67\n"
        "v_lshl_add_u32 v50, v50, 2, s67\n"
        "v_lshl_add_u32 v51, v51, 2, s67\n"
        "v_lshl_add_u32 v52, v52, 2, s67\n"
        "v_lshl_add_u32 v53, v53, 2, s67\n"
        "v_lshl_add_u32 v54, v54, 2, s67\n"
        "v_lshl_add_u32 v55, v55, 2, s67\n"
        "ds_read_b32 v48, v48\n"
        "ds_read_b32 v49, v49\n"
        "ds_read_b32 v50, v50\n"
        "ds_read_b32 v51, v51\n"
        "ds_read_b32 v52, v52\n"
        "ds_read_b32 v53, v53\n"
        "ds_read_b32 v54, v54\n"
        "ds_read_b32 v55, v55\n"
        "s_add_u32 m0, s40, 36864\n"
        "s_add_u32 s64, s65, 110592\n"
        "buffer_load_dwordx4 v128, s[20:23], s64 offen lds\n"
        "v_lshl_add_u32 v56, v56, 2, s67\n"
        "v_lshl_add_u32 v57, v57, 2, s67\n"
        "v_lshl_add_u32 v58, v58, 2, s67\n"
        "v_lshl_add_u32 v59, v59, 2, s67\n"
        "v_lshl_add_u32 v60, v60, 2, s67\n"
        "v_lshl_add_u32 v61, v61, 2, s67\n"
        "v_lshl_add_u32 v62, v62, 2, s67\n"
        "v_lshl_add_u32 v63, v63, 2, s67\n"
        "ds_read_b32 v56, v56\n"
        "ds_read_b32 v57, v57\n"
        "ds_read_b32 v58, v58\n"
        "ds_read_b32 v59, v59\n"
        "ds_read_b32 v60, v60\n"
        "ds_read_b32 v61, v61\n"
        "ds_read_b32 v62, v62\n"
        "ds_read_b32 v63, v63\n"
        "buffer_load_dwordx4 v129, s[20:23], s64 offen offset:1024 lds\n"
        "buffer_load_dwordx4 v130, s[20:23], s64 offen offset:2048 lds\n"
        "s_add_u32 m0, s40, 49152\n"
        "s_add_u32 s64, s65, 147456\n"
        "buffer_load_dwordx4 v128, s[20:23], s64 offen lds\n"
        "buffer_load_dwordx4 v129, s[20:23], s64 offen offset:1024 lds\n"
        "buffer_load_dwordx4 v130, s[20:23], s64 offen offset:2048 lds\n"
        "s_add_u32 m0, s40, 61440\n"
        "s_add_u32 s64, s65, 184320\n"
        "buffer_load_dwordx4 v128, s[20:23], s64 offen lds\n"
        "buffer_load_dwordx4 v129, s[20:23], s64 offen offset:1024 lds\n"
        "buffer_load_dwordx4 v130, s[20:23], s64 offen offset:2048 lds\n"
        "s_waitcnt vmcnt(12)\n"
        "s_barrier\n"
        "ds_read_b64_tr_b16 a[0:1], v131 offset:0\n"
        "ds_read_b128 a[144:147], v240 offset:0\n"
        "ds_read_b64_tr_b16 a[2:3], v131 offset:1536\n"
        "ds_read_b64_tr_b16 a[4:5], v131 offset:12288\n"
        "ds_read_b128 a[148:151], v241 offset:0\n"
        "ds_read_b64_tr_b16 a[6:7], v131 offset:13824\n"
        "ds_read_b64_tr_b16 a[24:25], v132 offset:0\n"
        "ds_read_b64_tr_b16 a[26:27], v132 offset:1536\n"
        "ds_read_b64_tr_b16 a[28:29], v132 offset:12288\n"
        "ds_read_b64_tr_b16 a[30:31], v132 offset:13824\n"
        "ds_read_b64_tr_b16 a[48:49], v133 offset:0\n"
        "ds_read_b64_tr_b16 a[50:51], v133 offset:1536\n"
        "ds_read_b64_tr_b16 a[52:53], v133 offset:12288\n"
        "ds_read_b64_tr_b16 a[54:55], v133 offset:13824\n"
        "ds_read_b128 a[160:163], v240 offset:1536\n"
        "ds_read_b128 a[164:167], v241 offset:1536\n"
        "s_waitcnt lgkmcnt(13)\n"
        "v_mfma_f32_16x16x32_bf16 v[96:99], a[0:3], a[144:147], 0\n"
        "s_waitcnt lgkmcnt(10)\n"
        "s_add_u32 m0, s40, 98304\n"
        "s_add_u32 s64, s65, 384\n"
        "ds_read_b128 a[176:179], v240 offset:12288\n"
        "v_mfma_f32_16x16x32_bf16 v[96:99], a[4:7], a[148:151], v[96:99]\n"
        "s_waitcnt lgkmcnt(7)\n"
        "buffer_load_dwordx4 v128, s[20:23], s64 offen lds\n"
        "v_mfma_f32_16x16x32_bf16 v[100:103], a[28:31], a[148:151], 0\n"
        "ds_read_b128 a[180:183], v241 offset:12288\n"
        "v_mfma_f32_16x16x32_bf16 v[100:103], a[24:27], a[144:147], v[100:103]\n"
        "s_waitcnt lgkmcnt(6)\n"
        "ds_read_b128 a[192:195], v240 offset:13824\n"
        "v_mfma_f32_16x16x32_bf16 v[104:107], a[48:51], a[144:147], 0\n"
        "s_waitcnt lgkmcnt(5)\n"
        "ds_read_b128 a[196:199], v241 offset:13824\n"
        "v_mfma_f32_16x16x32_bf16 v[104:107], a[52:55], a[148:151], v[104:107]\n"
        "s_waitcnt lgkmcnt(4)\n"
        "buffer_load_dwordx4 v129, s[20:23], s64 offen offset:1024 lds\n"
        "v_mfma_f32_16x16x32_bf16 v[108:111], a[4:7], a[164:167], 0\n"
        "ds_read_b128 a[152:155], v240 offset:128\n"
        "v_mfma_f32_16x16x32_bf16 v[108:111], a[0:3], a[160:163], v[108:111]\n"
        "ds_read_b128 a[156:159], v241 offset:128\n"
        "v_mfma_f32_16x16x32_bf16 v[112:115], a[24:27], a[160:163], 0\n"
        "ds_read_b128 a[168:171], v240 offset:1664\n"
        "v_mfma_f32_16x16x32_bf16 v[112:115], a[28:31], a[164:167], v[112:115]\n"
        "buffer_load_dwordx4 v130, s[20:23], s64 offen offset:2048 lds\n"
        "v_mfma_f32_16x16x32_bf16 v[116:119], a[52:55], a[164:167], 0\n"
        "s_add_u32 m0, s40, 110592\n"
        "ds_read_b128 a[172:175], v241 offset:1664\n"
        "v_mfma_f32_16x16x32_bf16 v[116:119], a[48:51], a[160:163], v[116:119]\n"
        "s_waitcnt lgkmcnt(7)\n"
        "s_add_u32 s64, s65, 37248\n"
        "v_mfma_f32_16x16x32_bf16 v[120:123], a[0:3], a[176:179], 0\n"
        "s_waitcnt lgkmcnt(6)\n"
        "s_waitcnt vmcnt(9)\n"
        "s_barrier\n"
        "ds_read_b128 a[184:187], v240 offset:12416\n"
        "v_mfma_f32_16x16x32_bf16 v[120:123], a[4:7], a[180:183], v[120:123]\n"
        "buffer_load_dwordx4 v128, s[20:23], s64 offen lds\n"
        "v_mfma_f32_16x16x32_bf16 v[92:95], a[28:31], a[180:183], 0\n"
        "ds_read_b64_tr_b16 a[8:9], v131 offset:24576\n"
        "ds_read_b64_tr_b16 a[10:11], v131 offset:26112\n"
        "v_mfma_f32_16x16x32_bf16 v[92:95], a[24:27], a[176:179], v[92:95]\n"
        "ds_read_b64_tr_b16 a[12:13], v131 offset:36864\n"
        "v_mfma_f32_16x16x32_bf16 v[152:155], a[48:51], a[176:179], 0\n"
        "ds_read_b64_tr_b16 a[14:15], v131 offset:38400\n"
        "v_mfma_f32_16x16x32_bf16 v[152:155], a[52:55], a[180:183], v[152:155]\n"
        "s_waitcnt lgkmcnt(9)\n"
        "buffer_load_dwordx4 v129, s[20:23], s64 offen offset:1024 lds\n"
        "v_mfma_f32_16x16x32_bf16 v[172:175], a[4:7], a[196:199], 0\n"
        "ds_read_b64_tr_b16 a[32:33], v132 offset:24576\n"
        "ds_read_b64_tr_b16 a[34:35], v132 offset:26112\n"
        "v_mfma_f32_16x16x32_bf16 v[172:175], a[0:3], a[192:195], v[172:175]\n"
        "ds_read_b64_tr_b16 a[36:37], v132 offset:36864\n"
        "v_mfma_f32_16x16x32_bf16 v[192:195], a[24:27], a[192:195], 0\n"
        "ds_read_b64_tr_b16 a[38:39], v132 offset:38400\n"
        "v_mfma_f32_16x16x32_bf16 v[192:195], a[28:31], a[196:199], v[192:195]\n"
        "buffer_load_dwordx4 v130, s[20:23], s64 offen offset:2048 lds\n"
        "v_mfma_f32_16x16x32_bf16 v[212:215], a[52:55], a[196:199], 0\n"
        "s_add_u32 m0, s40, 0\n"
        "ds_read_b64_tr_b16 a[56:57], v133 offset:24576\n"
        "ds_read_b64_tr_b16 a[58:59], v133 offset:26112\n"
        "v_mfma_f32_16x16x32_bf16 v[212:215], a[48:51], a[192:195], v[212:215]\n"
        "s_waitcnt lgkmcnt(8)\n"
        "s_add_u32 s64, s65, 74112\n"
        "ds_read_b64_tr_b16 a[60:61], v133 offset:36864\n"
        "v_mfma_f32_16x16x32_bf16 v[96:99], a[8:11], a[152:155], v[96:99]\n"
        "s_waitcnt lgkmcnt(7)\n"
        "ds_read_b64_tr_b16 a[62:63], v133 offset:38400\n"
        "v_mfma_f32_16x16x32_bf16 v[96:99], a[12:15], a[156:159], v[96:99]\n"
        "s_waitcnt lgkmcnt(4)\n"
        "buffer_load_dwordx4 v128, s[20:23], s64 offen lds\n"
        "v_mfma_f32_16x16x32_bf16 v[100:103], a[36:39], a[156:159], v[100:103]\n"
        "ds_read_b128 a[188:191], v241 offset:12416\n"
        "v_mfma_f32_16x16x32_bf16 v[100:103], a[32:35], a[152:155], v[100:103]\n"
        "s_waitcnt lgkmcnt(3)\n"
        "ds_read_b128 a[200:203], v240 offset:13952\n"
        "v_mfma_f32_16x16x32_bf16 v[104:107], a[56:59], a[152:155], v[104:107]\n"
        "s_waitcnt lgkmcnt(2)\n"
        "ds_read_b128 a[204:207], v241 offset:13952\n"
        "v_mfma_f32_16x16x32_bf16 v[104:107], a[60:63], a[156:159], v[104:107]\n"
        "buffer_load_dwordx4 v129, s[20:23], s64 offen offset:1024 lds\n"
        "v_mfma_f32_16x16x32_bf16 v[108:111], a[12:15], a[172:175], v[108:111]\n"
        "ds_read_b128 a[216:219], v240 offset:256\n"
        "v_mfma_f32_16x16x32_bf16 v[108:111], a[8:11], a[168:171], v[108:111]\n"
        "ds_read_b128 a[220:223], v241 offset:256\n"
        "v_mfma_f32_16x16x32_bf16 v[112:115], a[32:35], a[168:171], v[112:115]\n"
        "ds_read_b128 a[224:227], v240 offset:1792\n"
        "v_mfma_f32_16x16x32_bf16 v[112:115], a[36:39], a[172:175], v[112:115]\n"
        "buffer_load_dwordx4 v130, s[20:23], s64 offen offset:2048 lds\n"
        "v_mfma_f32_16x16x32_bf16 v[116:119], a[60:63], a[172:175], v[116:119]\n"
        "s_waitcnt vmcnt(9)\n"
        "s_barrier\n"
        "ds_read_b128 a[228:231], v241 offset:1792\n"
        "v_mfma_f32_16x16x32_bf16 v[116:119], a[56:59], a[168:171], v[116:119]\n"
        "s_add_u32 m0, s40, 12288\n"
        "ds_read_b64_tr_b16 a[16:17], v131 offset:49152\n"
        "v_mfma_f32_16x16x32_bf16 v[120:123], a[8:11], a[184:187], v[120:123]\n"
        "s_waitcnt lgkmcnt(7)\n"
        "s_add_u32 s64, s65, 110976\n"
        "ds_read_b64_tr_b16 a[18:19], v131 offset:50688\n"
        "v_mfma_f32_16x16x32_bf16 v[120:123], a[12:15], a[188:191], v[120:123]\n"
        "buffer_load_dwordx4 v128, s[20:23], s64 offen lds\n"
        "v_mfma_f32_16x16x32_bf16 v[92:95], a[36:39], a[188:191], v[92:95]\n"
        "ds_read_b64_tr_b16 a[20:21], v131 offset:61440\n"
        "ds_read_b64_tr_b16 a[22:23], v131 offset:62976\n"
        "v_mfma_f32_16x16x32_bf16 v[92:95], a[32:35], a[184:187], v[92:95]\n"
        "ds_read_b64_tr_b16 a[40:41], v132 offset:49152\n"
        "v_mfma_f32_16x16x32_bf16 v[152:155], a[56:59], a[184:187], v[152:155]\n"
        "ds_read_b64_tr_b16 a[42:43], v132 offset:50688\n"
        "v_mfma_f32_16x16x32_bf16 v[152:155], a[60:63], a[188:191], v[152:155]\n"
        "s_waitcnt lgkmcnt(10)\n"
        "buffer_load_dwordx4 v129, s[20:23], s64 offen offset:1024 lds\n"
        "v_mfma_f32_16x16x32_bf16 v[172:175], a[12:15], a[204:207], v[172:175]\n"
        "ds_read_b64_tr_b16 a[44:45], v132 offset:61440\n"
        "ds_read_b64_tr_b16 a[46:47], v132 offset:62976\n"
        "v_mfma_f32_16x16x32_bf16 v[172:175], a[8:11], a[200:203], v[172:175]\n"
        "ds_read_b64_tr_b16 a[64:65], v133 offset:49152\n"
        "v_mfma_f32_16x16x32_bf16 v[192:195], a[32:35], a[200:203], v[192:195]\n"
        "ds_read_b64_tr_b16 a[66:67], v133 offset:50688\n"
        "v_mfma_f32_16x16x32_bf16 v[192:195], a[36:39], a[204:207], v[192:195]\n"
        "buffer_load_dwordx4 v130, s[20:23], s64 offen offset:2048 lds\n"
        "v_mfma_f32_16x16x32_bf16 v[212:215], a[60:63], a[204:207], v[212:215]\n"
        "s_add_u32 m0, s40, 24576\n"
        "ds_read_b64_tr_b16 a[68:69], v133 offset:61440\n"
        "ds_read_b64_tr_b16 a[70:71], v133 offset:62976\n"
        "v_mfma_f32_16x16x32_bf16 v[212:215], a[56:59], a[200:203], v[212:215]\n"
        "s_waitcnt lgkmcnt(10)\n"
        "s_add_u32 s64, s65, 147840\n"
        "ds_read_b128 a[232:235], v240 offset:12544\n"
        "v_mfma_f32_16x16x32_bf16 v[96:99], a[16:19], a[216:219], v[96:99]\n"
        "s_waitcnt lgkmcnt(9)\n"
        "ds_read_b128 a[236:239], v241 offset:12544\n"
        "v_mfma_f32_16x16x32_bf16 v[96:99], a[20:23], a[220:223], v[96:99]\n"
        "s_waitcnt lgkmcnt(6)\n"
        "buffer_load_dwordx4 v128, s[20:23], s64 offen lds\n"
        "v_mfma_f32_16x16x32_bf16 v[100:103], a[44:47], a[220:223], v[100:103]\n"
        "ds_read_b128 a[240:243], v240 offset:14080\n"
        "v_mfma_f32_16x16x32_bf16 v[100:103], a[40:43], a[216:219], v[100:103]\n"
        "s_waitcnt lgkmcnt(5)\n"
        "ds_read_b128 a[244:247], v241 offset:14080\n"
        "v_mfma_f32_16x16x32_bf16 v[104:107], a[64:67], a[216:219], v[104:107]\n"
        "s_waitcnt lgkmcnt(4)\n"
        "v_mfma_f32_16x16x32_bf16 v[104:107], a[68:71], a[220:223], v[104:107]\n"
        "buffer_load_dwordx4 v129, s[20:23], s64 offen offset:1024 lds\n"
        "v_mfma_f32_16x16x32_bf16 v[108:111], a[20:23], a[228:231], v[108:111]\n"
        "v_mul_f32 v96, s50, v96\n"
        "v_mul_f32 v97, s50, v97\n"
        "v_mfma_f32_16x16x32_bf16 v[108:111], a[16:19], a[224:227], v[108:111]\n"
        "v_mul_f32 v98, s50, v98\n"
        "v_mul_f32 v99, s50, v99\n"
        "v_mfma_f32_16x16x32_bf16 v[112:115], a[40:43], a[224:227], v[112:115]\n"
        "v_cvt_pk_bf16_f32 v156, v96, v97\n"
        "v_cvt_pk_bf16_f32 v157, v98, v99\n"
        "v_mfma_f32_16x16x32_bf16 v[112:115], a[44:47], a[228:231], v[112:115]\n"
        "s_waitcnt vmcnt(8)\n"
        "s_barrier\n"
        "buffer_load_dwordx4 v130, s[20:23], s64 offen offset:2048 lds\n"
        "v_mfma_f32_16x16x32_bf16 v[116:119], a[68:71], a[228:231], v[116:119]\n"
        "s_add_u32 m0, s40, 36864\n"
        "ds_read_b64_tr_b16 a[72:73], v136 offset:0\n"
        "v_mul_f32 v100, s50, v100\n"
        "v_mfma_f32_16x16x32_bf16 v[116:119], a[64:67], a[224:227], v[116:119]\n"
        "s_waitcnt lgkmcnt(4)\n"
        "s_add_u32 s64, s65, 184704\n"
        "ds_read_b64_tr_b16 a[74:75], v136 offset:1536\n"
        "v_mul_f32 v101, s50, v101\n"
        "v_mfma_f32_16x16x32_bf16 v[120:123], a[16:19], a[232:235], v[120:123]\n"
        "s_waitcnt lgkmcnt(4)\n"
        "ds_read_b64_tr_b16 a[76:77], v136 offset:12288\n"
        "v_mul_f32 v102, s50, v102\n"
        "v_mfma_f32_16x16x32_bf16 v[120:123], a[20:23], a[236:239], v[120:123]\n"
        "buffer_load_dwordx4 v128, s[20:23], s64 offen lds\n"
        "v_mfma_f32_16x16x32_bf16 v[92:95], a[44:47], a[236:239], v[92:95]\n"
        "ds_read_b64_tr_b16 a[78:79], v136 offset:13824\n"
        "ds_read_b64_tr_b16 a[96:97], v137 offset:0\n"
        "v_mfma_f32_16x16x32_bf16 v[92:95], a[40:43], a[232:235], v[92:95]\n"
        "ds_read_b64_tr_b16 a[98:99], v137 offset:1536\n"
        "v_mul_f32 v103, s50, v103\n"
        "v_mfma_f32_16x16x32_bf16 v[152:155], a[64:67], a[232:235], v[152:155]\n"
        "ds_read_b64_tr_b16 a[100:101], v137 offset:12288\n"
        "v_cvt_pk_bf16_f32 v158, v100, v101\n"
        "v_mfma_f32_16x16x32_bf16 v[152:155], a[68:71], a[236:239], v[152:155]\n"
        "s_waitcnt lgkmcnt(7)\n"
        "buffer_load_dwordx4 v129, s[20:23], s64 offen offset:1024 lds\n"
        "v_mfma_f32_16x16x32_bf16 v[172:175], a[20:23], a[244:247], v[172:175]\n"
        "ds_read_b64_tr_b16 a[102:103], v137 offset:13824\n"
        "ds_read_b64_tr_b16 a[120:121], v138 offset:0\n"
        "v_mfma_f32_16x16x32_bf16 v[172:175], a[16:19], a[240:243], v[172:175]\n"
        "ds_read_b64_tr_b16 a[122:123], v138 offset:1536\n"
        "v_cvt_pk_bf16_f32 v159, v102, v103\n"
        "v_mfma_f32_16x16x32_bf16 v[192:195], a[40:43], a[240:243], v[192:195]\n"
        "ds_read_b64_tr_b16 a[124:125], v138 offset:12288\n"
        "v_mul_f32 v104, s50, v104\n"
        "v_mfma_f32_16x16x32_bf16 v[192:195], a[44:47], a[244:247], v[192:195]\n"
        "buffer_load_dwordx4 v130, s[20:23], s64 offen offset:2048 lds\n"
        "v_mfma_f32_16x16x32_bf16 v[212:215], a[68:71], a[244:247], v[212:215]\n"
        "s_add_u32 m0, s40, 49152\n"
        "ds_read_b64_tr_b16 a[126:127], v138 offset:13824\n"
        "v_mul_f32 v105, s50, v105\n"
        "v_mfma_f32_16x16x32_bf16 v[212:215], a[64:67], a[240:243], v[212:215]\n"
        "s_waitcnt lgkmcnt(10)\n"
        "s_add_u32 s64, s65, 768\n"
        "v_mul_f32 v106, s50, v106\n"
        "v_mul_f32 v107, s50, v107\n"
        "v_mfma_f32_16x16x32_bf16 v[64:67], a[72:75], a[144:147], 0\n"
        "s_waitcnt lgkmcnt(8)\n"
        "v_cvt_pk_bf16_f32 v160, v104, v105\n"
        "v_cvt_pk_bf16_f32 v161, v106, v107\n"
        "v_mfma_f32_16x16x32_bf16 v[64:67], a[76:79], a[148:151], v[64:67]\n"
        "s_waitcnt lgkmcnt(4)\n"
        "buffer_load_dwordx4 v128, s[20:23], s64 offen lds\n"
        "v_mfma_f32_16x16x32_bf16 v[68:71], a[100:103], a[148:151], 0\n"
        "v_mul_f32 v108, s50, v108\n"
        "v_mul_f32 v109, s50, v109\n"
        "v_mfma_f32_16x16x32_bf16 v[68:71], a[96:99], a[144:147], v[68:71]\n"
        "s_waitcnt lgkmcnt(2)\n"
        "v_mov_b64 v[166:167], v[160:161]\n"
        "v_mul_f32 v110, s50, v110\n"
        "v_mfma_f32_16x16x32_bf16 v[72:75], a[120:123], a[144:147], 0\n"
        "s_waitcnt lgkmcnt(0)\n"
        "v_mul_f32 v111, s50, v111\n"
        "v_cvt_pk_bf16_f32 v176, v108, v109\n"
        "v_mfma_f32_16x16x32_bf16 v[72:75], a[124:127], a[148:151], v[72:75]\n"
        "buffer_load_dwordx4 v129, s[20:23], s64 offen offset:1024 lds\n"
        "v_mfma_f32_16x16x32_bf16 v[76:79], a[76:79], a[164:167], 0\n"
        "v_cvt_pk_bf16_f32 v177, v110, v111\n"
        "v_mul_f32 v112, s50, v112\n"
        "v_mfma_f32_16x16x32_bf16 v[76:79], a[72:75], a[160:163], v[76:79]\n"
        "v_mul_f32 v113, s50, v113\n"
        "v_mul_f32 v114, s50, v114\n"
        "v_mfma_f32_16x16x32_bf16 v[80:83], a[96:99], a[160:163], 0\n"
        "s_waitcnt vmcnt(8)\n"
        "s_barrier\n"
        "v_mul_f32 v115, s50, v115\n"
        "v_cvt_pk_bf16_f32 v178, v112, v113\n"
        "v_mfma_f32_16x16x32_bf16 v[80:83], a[100:103], a[164:167], v[80:83]\n"
        "buffer_load_dwordx4 v130, s[20:23], s64 offen offset:2048 lds\n"
        "v_mfma_f32_16x16x32_bf16 v[84:87], a[124:127], a[164:167], 0\n"
        "s_add_u32 m0, s40, 61440\n"
        "ds_read_b64_tr_b16 a[80:81], v131 offset:0\n"
        "ds_read_b64_tr_b16 a[82:83], v131 offset:1536\n"
        "v_mfma_f32_16x16x32_bf16 v[84:87], a[120:123], a[160:163], v[84:87]\n"
        "s_add_u32 s64, s65, 37632\n"
        "ds_read_b64_tr_b16 a[84:85], v131 offset:12288\n"
        "v_cvt_pk_bf16_f32 v179, v114, v115\n"
        "v_mfma_f32_16x16x32_bf16 v[88:91], a[72:75], a[176:179], 0\n"
        "ds_read_b64_tr_b16 a[86:87], v131 offset:13824\n"
        "v_mul_f32 v116, s50, v116\n"
        "v_mfma_f32_16x16x32_bf16 v[88:91], a[76:79], a[180:183], v[88:91]\n"
        "buffer_load_dwordx4 v128, s[20:23], s64 offen lds\n"
        "v_mfma_f32_16x16x32_bf16 v[108:111], a[100:103], a[180:183], 0\n"
        "ds_read_b64_tr_b16 a[104:105], v132 offset:0\n"
        "ds_read_b64_tr_b16 a[106:107], v132 offset:1536\n"
        "v_mfma_f32_16x16x32_bf16 v[108:111], a[96:99], a[176:179], v[108:111]\n"
        "ds_read_b64_tr_b16 a[108:109], v132 offset:12288\n"
        "v_mul_f32 v117, s50, v117\n"
        "v_mfma_f32_16x16x32_bf16 v[112:115], a[120:123], a[176:179], 0\n"
        "ds_read_b64_tr_b16 a[110:111], v132 offset:13824\n"
        "v_mul_f32 v118, s50, v118\n"
        "v_mfma_f32_16x16x32_bf16 v[112:115], a[124:127], a[180:183], v[112:115]\n"
        "buffer_load_dwordx4 v129, s[20:23], s64 offen offset:1024 lds\n"
        "v_mfma_f32_16x16x32_bf16 v[96:99], a[76:79], a[196:199], 0\n"
        "ds_read_b64_tr_b16 a[128:129], v133 offset:0\n"
        "ds_read_b64_tr_b16 a[130:131], v133 offset:1536\n"
        "v_mfma_f32_16x16x32_bf16 v[96:99], a[72:75], a[192:195], v[96:99]\n"
        "ds_read_b64_tr_b16 a[132:133], v133 offset:12288\n"
        "v_mul_f32 v119, s50, v119\n"
        "v_mfma_f32_16x16x32_bf16 v[100:103], a[96:99], a[192:195], 0\n"
        "ds_read_b64_tr_b16 a[134:135], v133 offset:13824\n"
        "v_cvt_pk_bf16_f32 v180, v116, v117\n"
        "v_mfma_f32_16x16x32_bf16 v[100:103], a[100:103], a[196:199], v[100:103]\n"
        "buffer_load_dwordx4 v130, s[20:23], s64 offen offset:2048 lds\n"
        "v_mfma_f32_16x16x32_bf16 v[104:107], a[124:127], a[196:199], 0\n"
        "v_cvt_pk_bf16_f32 v181, v118, v119\n"
        "v_mul_f32 v120, s50, v120\n"
        "v_mfma_f32_16x16x32_bf16 v[104:107], a[120:123], a[192:195], v[104:107]\n"
        "s_waitcnt lgkmcnt(10)\n"
        "v_mul_f32 v121, s50, v121\n"
        "v_mov_b64 v[186:187], v[180:181]\n"
        "v_mfma_f32_16x16x32_bf16 v[64:67], a[80:83], a[152:155], v[64:67]\n"
        "s_waitcnt lgkmcnt(8)\n"
        "v_mul_f32 v122, s50, v122\n"
        "v_mul_f32 v123, s50, v123\n"
        "v_mfma_f32_16x16x32_bf16 v[64:67], a[84:87], a[156:159], v[64:67]\n"
        "s_waitcnt lgkmcnt(4)\n"
        "v_cvt_pk_bf16_f32 v196, v120, v121\n"
        "v_cvt_pk_bf16_f32 v197, v122, v123\n"
        "v_mfma_f32_16x16x32_bf16 v[68:71], a[108:111], a[156:159], v[68:71]\n"
        "v_mul_f32 v92, s50, v92\n"
        "v_mul_f32 v93, s50, v93\n"
        "v_mfma_f32_16x16x32_bf16 v[68:71], a[104:107], a[152:155], v[68:71]\n"
        "s_waitcnt lgkmcnt(2)\n"
        "v_mul_f32 v94, s50, v94\n"
        "v_mul_f32 v95, s50, v95\n"
        "v_mfma_f32_16x16x32_bf16 v[72:75], a[128:131], a[152:155], v[72:75]\n"
        "s_waitcnt lgkmcnt(0)\n"
        "v_cvt_pk_bf16_f32 v198, v92, v93\n"
        "v_cvt_pk_bf16_f32 v199, v94, v95\n"
        "v_mfma_f32_16x16x32_bf16 v[72:75], a[132:135], a[156:159], v[72:75]\n"
        "v_mul_f32 v152, s50, v152\n"
        "v_mul_f32 v153, s50, v153\n"
        "v_mfma_f32_16x16x32_bf16 v[76:79], a[84:87], a[172:175], v[76:79]\n"
        "v_mul_f32 v154, s50, v154\n"
        "v_mul_f32 v155, s50, v155\n"
        "v_mfma_f32_16x16x32_bf16 v[76:79], a[80:83], a[168:171], v[76:79]\n"
        "v_cvt_pk_bf16_f32 v200, v152, v153\n"
        "v_cvt_pk_bf16_f32 v201, v154, v155\n"
        "v_mfma_f32_16x16x32_bf16 v[80:83], a[104:107], a[168:171], v[80:83]\n"
        "s_waitcnt vmcnt(6)\n"
        "s_barrier\n"
        "s_add_u32 m0, s40, 0\n"
        "v_mul_f32 v172, s50, v172\n"
        "v_mfma_f32_16x16x32_bf16 v[80:83], a[108:111], a[172:175], v[80:83]\n"
        "s_add_u32 s64, s65, 74496\n"
        "ds_read_b64_tr_b16 a[88:89], v131 offset:24576\n"
        "v_mul_f32 v173, s50, v173\n"
        "v_mfma_f32_16x16x32_bf16 v[84:87], a[132:135], a[172:175], v[84:87]\n"
        "ds_read_b64_tr_b16 a[90:91], v131 offset:26112\n"
        "v_mov_b64 v[206:207], v[200:201]\n"
        "v_mfma_f32_16x16x32_bf16 v[84:87], a[128:131], a[168:171], v[84:87]\n"
        "buffer_load_dwordx4 v128, s[20:23], s64 offen lds\n"
        "v_mfma_f32_16x16x32_bf16 v[88:91], a[80:83], a[184:187], v[88:91]\n"
        "ds_read_b64_tr_b16 a[92:93], v131 offset:36864\n"
        "ds_read_b64_tr_b16 a[94:95], v131 offset:38400\n"
        "v_mfma_f32_16x16x32_bf16 v[88:91], a[84:87], a[188:191], v[88:91]\n"
        "ds_read_b64_tr_b16 a[112:113], v132 offset:24576\n"
        "v_mul_f32 v174, s50, v174\n"
        "v_mfma_f32_16x16x32_bf16 v[108:111], a[108:111], a[188:191], v[108:111]\n"
        "ds_read_b64_tr_b16 a[114:115], v132 offset:26112\n"
        "v_mul_f32 v175, s50, v175\n"
        "v_mfma_f32_16x16x32_bf16 v[108:111], a[104:107], a[184:187], v[108:111]\n"
        "buffer_load_dwordx4 v129, s[20:23], s64 offen offset:1024 lds\n"
        "v_mfma_f32_16x16x32_bf16 v[112:115], a[128:131], a[184:187], v[112:115]\n"
        "ds_read_b64_tr_b16 a[116:117], v132 offset:36864\n"
        "ds_read_b64_tr_b16 a[118:119], v132 offset:38400\n"
        "v_mfma_f32_16x16x32_bf16 v[112:115], a[132:135], a[188:191], v[112:115]\n"
        "ds_read_b64_tr_b16 a[136:137], v133 offset:24576\n"
        "v_cvt_pk_bf16_f32 v216, v172, v173\n"
        "v_mfma_f32_16x16x32_bf16 v[96:99], a[84:87], a[204:207], v[96:99]\n"
        "ds_read_b64_tr_b16 a[138:139], v133 offset:26112\n"
        "v_cvt_pk_bf16_f32 v217, v174, v175\n"
        "v_mfma_f32_16x16x32_bf16 v[96:99], a[80:83], a[200:203], v[96:99]\n"
        "buffer_load_dwordx4 v130, s[20:23], s64 offen offset:2048 lds\n"
        "v_mfma_f32_16x16x32_bf16 v[100:103], a[104:107], a[200:203], v[100:103]\n"
        "s_add_u32 m0, s40, 12288\n"
        "ds_read_b64_tr_b16 a[140:141], v133 offset:36864\n"
        "ds_read_b64_tr_b16 a[142:143], v133 offset:38400\n"
        "v_mfma_f32_16x16x32_bf16 v[100:103], a[108:111], a[204:207], v[100:103]\n"
        "s_add_u32 s64, s65, 111360\n"
        "v_mul_f32 v192, s50, v192\n"
        "v_mul_f32 v193, s50, v193\n"
        "v_mfma_f32_16x16x32_bf16 v[104:107], a[132:135], a[204:207], v[104:107]\n"
        "buffer_load_dwordx4 v128, s[20:23], s64 offen lds\n"
        "v_mfma_f32_16x16x32_bf16 v[104:107], a[128:131], a[200:203], v[104:107]\n"
        "s_waitcnt lgkmcnt(10)\n"
        "v_mul_f32 v194, s50, v194\n"
        "v_mul_f32 v195, s50, v195\n"
        "v_mfma_f32_16x16x32_bf16 v[64:67], a[88:91], a[216:219], v[64:67]\n"
        "s_waitcnt lgkmcnt(8)\n"
        "v_cvt_pk_bf16_f32 v218, v192, v193\n"
        "v_cvt_pk_bf16_f32 v219, v194, v195\n"
        "v_mfma_f32_16x16x32_bf16 v[64:67], a[92:95], a[220:223], v[64:67]\n"
        "s_waitcnt lgkmcnt(4)\n"
        "v_mul_f32 v212, s50, v212\n"
        "v_mul_f32 v213, s50, v213\n"
        "v_mfma_f32_16x16x32_bf16 v[68:71], a[116:119], a[220:223], v[68:71]\n"
        "buffer_load_dwordx4 v129, s[20:23], s64 offen offset:1024 lds\n"
        "v_mfma_f32_16x16x32_bf16 v[68:71], a[112:115], a[216:219], v[68:71]\n"
        "s_waitcnt lgkmcnt(2)\n"
        "v_mul_f32 v214, s50, v214\n"
        "v_mul_f32 v215, s50, v215\n"
        "v_mfma_f32_16x16x32_bf16 v[72:75], a[136:139], a[216:219], v[72:75]\n"
        "s_waitcnt lgkmcnt(0)\n"
        "v_cvt_pk_bf16_f32 v220, v212, v213\n"
        "v_cvt_pk_bf16_f32 v221, v214, v215\n"
        "v_mfma_f32_16x16x32_bf16 v[72:75], a[140:143], a[220:223], v[72:75]\n"
        "v_mov_b64 v[226:227], v[220:221]\n"
        "v_cvt_pk_bf16_f32 v152, v64, v65\n"
        "v_mfma_f32_16x16x32_bf16 v[76:79], a[92:95], a[228:231], v[76:79]\n"
        "buffer_load_dwordx4 v130, s[20:23], s64 offen offset:2048 lds\n"
        "v_mfma_f32_16x16x32_bf16 v[76:79], a[88:91], a[224:227], v[76:79]\n"
        "v_cvt_pk_bf16_f32 v153, v66, v67\n"
        "v_cvt_pk_bf16_f32 v154, v68, v69\n"
        "v_mfma_f32_16x16x32_bf16 v[80:83], a[112:115], a[224:227], v[80:83]\n"
        "v_cvt_pk_bf16_f32 v155, v70, v71\n"
        "v_cvt_pk_bf16_f32 v168, v72, v73\n"
        "v_mfma_f32_16x16x32_bf16 v[80:83], a[116:119], a[228:231], v[80:83]\n"
        "ds_read_b128 v[162:165], v251 offset:0\n"
        "v_cvt_pk_bf16_f32 v169, v74, v75\n"
        "v_mfma_f32_16x16x32_bf16 v[84:87], a[140:143], a[228:231], v[84:87]\n"
        "v_cvt_pk_bf16_f32 v172, v76, v77\n"
        "v_cvt_pk_bf16_f32 v173, v78, v79\n"
        "v_mfma_f32_16x16x32_bf16 v[84:87], a[136:139], a[224:227], v[84:87]\n"
        "ds_read_b128 v[182:185], v251 offset:512\n"
        "v_mfma_f32_16x16x32_bf16 v[88:91], a[88:91], a[232:235], v[88:91]\n"
        "s_waitcnt vmcnt(6)\n"
        "s_barrier\n"
        "s_add_u32 m0, s40, 24576\n"
        "v_cvt_pk_bf16_f32 v174, v80, v81\n"
        "v_mfma_f32_16x16x32_bf16 v[88:91], a[92:95], a[236:239], v[88:91]\n"
        "s_add_u32 s64, s65, 148224\n"
        "ds_read_b64_tr_b16 a[144:145], v131 offset:49152\n"
        "v_cvt_pk_bf16_f32 v175, v82, v83\n"
        "v_mfma_f32_16x16x32_bf16 v[108:111], a[116:119], a[236:239], v[108:111]\n"
        "ds_read_b64_tr_b16 a[146:147], v131 offset:50688\n"
        "v_cvt_pk_bf16_f32 v190, v84, v85\n"
        "v_mfma_f32_16x16x32_bf16 v[108:111], a[112:115], a[232:235], v[108:111]\n"
        "buffer_load_dwordx4 v128, s[20:23], s64 offen lds\n"
        "v_mfma_f32_16x16x32_bf16 v[112:115], a[136:139], a[232:235], v[112:115]\n"
        "ds_read_b64_tr_b16 a[148:149], v131 offset:61440\n"
        "ds_read_b64_tr_b16 a[150:151], v131 offset:62976\n"
        "v_mfma_f32_16x16x32_bf16 v[112:115], a[140:143], a[236:239], v[112:115]\n"
        "ds_read_b64_tr_b16 a[168:169], v132 offset:49152\n"
        "v_cvt_pk_bf16_f32 v191, v86, v87\n"
        "v_mfma_f32_16x16x32_bf16 v[96:99], a[92:95], a[244:247], v[96:99]\n"
        "ds_read_b64_tr_b16 a[170:171], v132 offset:50688\n"
        "v_cvt_pk_bf16_f32 v192, v88, v89\n"
        "v_mfma_f32_16x16x32_bf16 v[96:99], a[88:91], a[240:243], v[96:99]\n"
        "buffer_load_dwordx4 v129, s[20:23], s64 offen offset:1024 lds\n"
        "v_mfma_f32_16x16x32_bf16 v[100:103], a[112:115], a[240:243], v[100:103]\n"
        "ds_read_b64_tr_b16 a[172:173], v132 offset:61440\n"
        "ds_read_b64_tr_b16 a[174:175], v132 offset:62976\n"
        "v_mfma_f32_16x16x32_bf16 v[100:103], a[116:119], a[244:247], v[100:103]\n"
        "ds_read_b64_tr_b16 a[192:193], v133 offset:49152\n"
        "v_cvt_pk_bf16_f32 v193, v90, v91\n"
        "v_mfma_f32_16x16x32_bf16 v[104:107], a[140:143], a[244:247], v[104:107]\n"
        "41:\n"
        "s_cbranch_execz 44f\n"
        "ds_read_b64_tr_b16 a[194:195], v133 offset:50688\n"
        "ds_read_b128 v[202:205], v251 offset:4096\n"
        "v_mfma_f32_16x16x32_bf16 v[104:107], a[136:139], a[240:243], v[104:107]\n"
        "buffer_load_dwordx4 v130, s[20:23], s64 offen offset:2048 lds\n"
        "v_mfma_f32_16x16x32_bf16 v[64:67], v[152:155], v[156:159], v[0:3]\n"
        "s_waitcnt lgkmcnt(12)\n"
        "s_add_u32 m0, s40, 36864\n"
        "ds_read_b64_tr_b16 a[196:197], v133 offset:61440\n"
        "ds_read_b64_tr_b16 a[198:199], v133 offset:62976\n"
        "v_mfma_f32_16x16x32_bf16 v[64:67], v[168:171], v[160:163], v[64:67]\n"
        "s_add_u32 s64, s65, 185088\n"
        "v_cvt_pk_bf16_f32 v194, v108, v109\n"
        "v_cvt_pk_bf16_f32 v195, v110, v111\n"
        "v_mfma_f32_16x16x32_bf16 v[68:71], v[172:175], v[156:159], v[4:7]\n"
        "buffer_load_dwordx4 v128, s[20:23], s64 offen lds\n"
        "v_mfma_f32_16x16x32_bf16 v[68:71], v[188:191], v[164:167], v[68:71]\n"
        "ds_read_b128 v[222:225], v251 offset:4608\n"
        "v_cvt_pk_bf16_f32 v208, v112, v113\n"
        "v_mfma_f32_16x16x32_bf16 v[72:75], v[152:155], v[176:179], v[16:19]\n"
        "s_waitcnt lgkmcnt(14)\n"
        "v_cvt_pk_bf16_f32 v209, v114, v115\n"
        "v_cvt_pk_bf16_f32 v212, v96, v97\n"
        "v_mfma_f32_16x16x32_bf16 v[72:75], v[168:171], v[180:183], v[72:75]\n"
        "v_cvt_pk_bf16_f32 v213, v98, v99\n"
        "v_cvt_pk_bf16_f32 v214, v100, v101\n"
        "v_mfma_f32_16x16x32_bf16 v[76:79], v[172:175], v[176:179], v[20:23]\n"
        "buffer_load_dwordx4 v129, s[20:23], s64 offen offset:1024 lds\n"
        "v_mfma_f32_16x16x32_bf16 v[76:79], v[188:191], v[184:187], v[76:79]\n"
        "ds_read_b128 v[162:165], v252 offset:0\n"
        "v_cvt_pk_bf16_f32 v215, v102, v103\n"
        "v_mfma_f32_16x16x32_bf16 v[80:83], v[152:155], v[196:199], v[32:35]\n"
        "s_waitcnt lgkmcnt(4)\n"
        "ds_read_b128 v[182:185], v252 offset:512\n"
        "v_cvt_pk_bf16_f32 v230, v104, v105\n"
        "v_mfma_f32_16x16x32_bf16 v[80:83], v[168:171], v[200:203], v[80:83]\n"
        "ds_read_b128 a[248:251], v240 offset:0\n"
        "v_cvt_pk_bf16_f32 v231, v106, v107\n"
        "v_mfma_f32_16x16x32_bf16 v[84:87], v[172:175], v[196:199], v[36:39]\n"
        "buffer_load_dwordx4 v130, s[20:23], s64 offen offset:2048 lds\n"
        "v_mfma_f32_16x16x32_bf16 v[84:87], v[188:191], v[204:207], v[84:87]\n"
        "s_add_u32 m0, s40, 98304\n"
        "ds_read_b128 v[202:205], v252 offset:4096\n"
        "v_mfma_f32_16x16x32_bf16 v[88:91], v[152:155], v[216:219], v[48:51]\n"
        "s_waitcnt lgkmcnt(4)\n"
        "s_waitcnt lgkmcnt(5)\n"
        "s_waitcnt vmcnt(6)\n"
        "s_barrier\n"
        "v_exp_f32 v64, v64\n"
        "v_mfma_f32_16x16x32_bf16 v[88:91], v[168:171], v[220:223], v[88:91]\n"
        "ds_read_b64_tr_b16 a[152:153], v131 offset:0\n"
        "ds_read_b128 a[216:219], v241 offset:0\n"
        "v_mfma_f32_16x16x32_bf16 v[92:95], v[172:175], v[216:219], v[52:55]\n"
        "ds_read_b64_tr_b16 a[154:155], v131 offset:1536\n"
        "ds_read_b128 a[220:223], v240 offset:128\n"
        "v_mfma_f32_16x16x32_bf16 v[92:95], v[188:191], v[224:227], v[92:95]\n"
        "buffer_load_dword v148, s[52:55], 0 offen lds\n"
        "v_mfma_f32_16x16x32_bf16 v[108:111], v[212:215], v[156:159], v[12:15]\n"
        "s_waitcnt lgkmcnt(7)\n"
        "s_add_u32 m0, s40, 98304\n"
        "s_sub_u32 s64, s47, 6291456\n"
        "ds_read_b64_tr_b16 a[156:157], v131 offset:12288\n"
        "v_mfma_f32_16x16x32_bf16 v[108:111], v[228:231], v[164:167], v[108:111]\n"
        "ds_read_b128 a[224:227], v241 offset:128\n"
        "ds_read_b64_tr_b16 a[158:159], v131 offset:13824\n"
        "v_mfma_f32_16x16x32_bf16 v[112:115], v[212:215], v[176:179], v[28:31]\n"
        "s_waitcnt lgkmcnt(9)\n"
        "buffer_load_dwordx4 v242, s[8:11], s64 offen lds\n"
        "v_mfma_f32_16x16x32_bf16 v[112:115], v[228:231], v[184:187], v[112:115]\n"
        "s_add_u32 m0, s40, 99328\n"
        "s_sub_u32 s64, s47, 6291456\n"
        "ds_read_b64_tr_b16 a[176:177], v132 offset:0\n"
        "v_mfma_f32_16x16x32_bf16 v[116:119], v[212:215], v[196:199], v[44:47]\n"
        "s_waitcnt lgkmcnt(8)\n"
        "ds_read_b64_tr_b16 a[178:179], v132 offset:1536\n"
        "ds_read_b64_tr_b16 a[180:181], v132 offset:12288\n"
        "v_mfma_f32_16x16x32_bf16 v[116:119], v[228:231], v[204:207], v[116:119]\n"
        "ds_read_b64_tr_b16 a[182:183], v132 offset:13824\n"
        "ds_read_b64_tr_b16 a[200:201], v133 offset:0\n"
        "v_mfma_f32_16x16x32_bf16 v[120:123], a[248:251], a[144:147], 0\n"
        "s_waitcnt lgkmcnt(10)\n"
        "buffer_load_dwordx4 v243, s[8:11], s64 offen lds\n"
        "v_mfma_f32_16x16x32_bf16 v[120:123], a[216:219], a[148:151], v[120:123]\n"
        "s_waitcnt lgkmcnt(8)\n"
        "s_add_u32 m0, s40, 100352\n"
        "s_sub_u32 s64, s47, 6291456\n"
        "ds_read_b64_tr_b16 a[202:203], v133 offset:1536\n"
        "v_mfma_f32_16x16x32_bf16 v[120:123], a[220:223], a[152:155], v[120:123]\n"
        "s_waitcnt lgkmcnt(6)\n"
        "ds_read_b64_tr_b16 a[204:205], v133 offset:12288\n"
        "ds_read_b64_tr_b16 a[206:207], v133 offset:13824\n"
        "v_mfma_f32_16x16x32_bf16 v[120:123], a[224:227], a[156:159], v[120:123]\n"
        "s_waitcnt lgkmcnt(4)\n"
        "buffer_load_dwordx4 v244, s[8:11], s64 offen lds\n"
        "v_mfma_f32_16x16x32_bf16 v[96:99], a[224:227], a[180:183], 0\n"
        "s_add_u32 m0, s40, 110592\n"
        "ds_read_b128 a[228:231], v240 offset:1536\n"
        "v_mfma_f32_16x16x32_bf16 v[96:99], a[220:223], a[176:179], v[96:99]\n"
        "s_sub_u32 s64, s47, 5505024\n"
        "ds_read_b128 a[232:235], v241 offset:1536\n"
        "v_mfma_f32_16x16x32_bf16 v[96:99], a[216:219], a[172:175], v[96:99]\n"
        "buffer_load_dwordx4 v242, s[8:11], s64 offen lds\n"
        "v_mfma_f32_16x16x32_bf16 v[96:99], a[248:251], a[168:171], v[96:99]\n"
        "s_add_u32 m0, s40, 111616\n"
        "ds_read_b128 a[236:239], v240 offset:1664\n"
        "ds_read_b128 a[240:243], v241 offset:1664\n"
        "v_mfma_f32_16x16x32_bf16 v[100:103], a[248:251], a[192:195], 0\n"
        "s_sub_u32 s64, s47, 5505024\n"
        "v_exp_f32 v65, v65\n"
        "v_mfma_f32_16x16x32_bf16 v[100:103], a[216:219], a[196:199], v[100:103]\n"
        "s_waitcnt lgkmcnt(6)\n"
        "v_exp_f32 v66, v66\n"
        "v_mfma_f32_16x16x32_bf16 v[100:103], a[220:223], a[200:203], v[100:103]\n"
        "s_waitcnt lgkmcnt(4)\n"
        "buffer_load_dwordx4 v243, s[8:11], s64 offen lds\n"
        "v_mfma_f32_16x16x32_bf16 v[100:103], a[224:227], a[204:207], v[100:103]\n"
        "s_add_u32 m0, s40, 112640\n"
        "ds_read_b128 a[244:247], v240 offset:12288\n"
        "v_cvt_pk_bf16_f32 v64, v64, v65\n"
        "v_exp_f32 v68, v68\n"
        "v_exp_f32 v67, v67\n"
        "v_exp_f32 v69, v69\n"
        "v_cvt_pk_bf16_f32 v65, v66, v67\n"
        "v_exp_f32 v70, v70\n"
        "v_cvt_pk_bf16_f32 v66, v68, v69\n"
        "v_exp_f32 v71, v71\n"
        "v_exp_f32 v72, v72\n"
        "v_cvt_pk_bf16_f32 v67, v70, v71\n"
        "v_mfma_f32_16x16x32_bf16 v[68:71], v[192:195], v[156:159], v[8:11]\n"
        "s_sub_u32 s64, s47, 5505024\n"
        "v_exp_f32 v73, v73\n"
        "v_mfma_f32_16x16x32_bf16 v[68:71], v[208:211], v[160:163], v[68:71]\n"
        "s_waitcnt lgkmcnt(1)\n"
        "v_exp_f32 v74, v74\n"
        "v_mfma_f32_16x16x32_bf16 v[104:107], a[240:243], a[156:159], 0\n"
        "buffer_load_dwordx4 v244, s[8:11], s64 offen lds\n"
        "v_mfma_f32_16x16x32_bf16 v[104:107], a[236:239], a[152:155], v[104:107]\n"
        "s_xor_b32 m0, s48, 8192\n"
        "ds_read_b128 a[248:251], v241 offset:12288\n"
        "v_cvt_pk_bf16_f32 v72, v72, v73\n"
        "v_mfma_f32_16x16x32_bf16 v[104:107], a[232:235], a[148:151], v[104:107]\n"
        "s_sub_u32 s64, s36, 2097152\n"
        "v_exp_f32 v75, v75\n"
        "v_mfma_f32_16x16x32_bf16 v[104:107], a[228:231], a[144:147], v[104:107]\n"
        "ds_read_b128 a[216:219], v240 offset:12416\n"
        "v_cvt_pk_bf16_f32 v73, v74, v75\n"
        "v_mfma_f32_16x16x32_bf16 v[156:159], a[228:231], a[168:171], 0\n"
        "s_waitcnt vmcnt(7)\n"
        "s_barrier\n"
        "buffer_load_dwordx4 v249, s[12:15], s64 offen lds\n"
        "v_mfma_f32_16x16x32_bf16 v[156:159], a[232:235], a[172:175], v[156:159]\n"
        "ds_read_b64_tr_b16 a[160:161], v131 offset:24576\n"
        "v_mfma_f32_16x16x32_bf16 v[156:159], a[236:239], a[176:179], v[156:159]\n"
        "ds_read_b64_tr_b16 a[162:163], v131 offset:26112\n"
        "v_mfma_f32_16x16x32_bf16 v[156:159], a[240:243], a[180:183], v[156:159]\n"
        "ds_read_b64_tr_b16 a[164:165], v131 offset:36864\n"
        "ds_read_b128 a[220:223], v241 offset:12416\n"
        "v_mfma_f32_16x16x32_bf16 v[160:163], a[240:243], a[204:207], 0\n"
        "ds_read_b64_tr_b16 a[166:167], v131 offset:38400\n"
        "v_mfma_f32_16x16x32_bf16 v[160:163], a[236:239], a[200:203], v[160:163]\n"
        "buffer_load_dwordx4 v149, s[12:15], s64 offen offset:1024 lds\n"
        "v_mfma_f32_16x16x32_bf16 v[160:163], a[232:235], a[196:199], v[160:163]\n"
        "ds_read_b64_tr_b16 a[184:185], v132 offset:24576\n"
        "ds_read_b64_tr_b16 a[186:187], v132 offset:26112\n"
        "v_mfma_f32_16x16x32_bf16 v[160:163], a[228:231], a[192:195], v[160:163]\n"
        "ds_read_b64_tr_b16 a[188:189], v132 offset:36864\n"
        "v_exp_f32 v76, v76\n"
        "v_exp_f32 v77, v77\n"
        "v_exp_f32 v78, v78\n"
        "v_cvt_pk_bf16_f32 v74, v76, v77\n"
        "v_exp_f32 v79, v79\n"
        "v_exp_f32 v80, v80\n"
        "v_cvt_pk_bf16_f32 v75, v78, v79\n"
        "v_mfma_f32_16x16x32_bf16 v[76:79], v[192:195], v[176:179], v[24:27]\n"
        "ds_read_b64_tr_b16 a[190:191], v132 offset:38400\n"
        "v_mfma_f32_16x16x32_bf16 v[76:79], v[208:211], v[180:183], v[76:79]\n"
        "s_waitcnt lgkmcnt(11)\n"
        "ds_read_b64_tr_b16 a[208:209], v133 offset:24576\n"
        "v_mfma_f32_16x16x32_bf16 v[164:167], a[244:247], a[144:147], 0\n"
        "s_waitcnt lgkmcnt(11)\n"
        "ds_read_b64_tr_b16 a[210:211], v133 offset:26112\n"
        "v_mfma_f32_16x16x32_bf16 v[164:167], a[248:251], a[148:151], v[164:167]\n"
        "s_waitcnt lgkmcnt(11)\n"
        "ds_read_b64_tr_b16 a[212:213], v133 offset:36864\n"
        "v_mfma_f32_16x16x32_bf16 v[164:167], a[216:219], a[152:155], v[164:167]\n"
        "s_waitcnt lgkmcnt(8)\n"
        "ds_read_b64_tr_b16 a[214:215], v133 offset:38400\n"
        "v_mfma_f32_16x16x32_bf16 v[164:167], a[220:223], a[156:159], v[164:167]\n"
        "v_exp_f32 v81, v81\n"
        "v_mfma_f32_16x16x32_bf16 v[176:179], a[220:223], a[180:183], 0\n"
        "ds_read_b128 a[224:227], v240 offset:13824\n"
        "v_cvt_pk_bf16_f32 v80, v80, v81\n"
        "v_mfma_f32_16x16x32_bf16 v[176:179], a[216:219], a[176:179], v[176:179]\n"
        "ds_read_b128 a[228:231], v241 offset:13824\n"
        "v_mfma_f32_16x16x32_bf16 v[176:179], a[248:251], a[172:175], v[176:179]\n"
        "ds_read_b128 a[232:235], v240 offset:13952\n"
        "v_mfma_f32_16x16x32_bf16 v[176:179], a[244:247], a[168:171], v[176:179]\n"
        "ds_read_b128 a[236:239], v241 offset:13952\n"
        "v_mfma_f32_16x16x32_bf16 v[180:183], a[244:247], a[192:195], 0\n"
        "v_exp_f32 v82, v82\n"
        "v_mfma_f32_16x16x32_bf16 v[180:183], a[248:251], a[196:199], v[180:183]\n"
        "v_exp_f32 v83, v83\n"
        "v_mfma_f32_16x16x32_bf16 v[180:183], a[216:219], a[200:203], v[180:183]\n"
        "ds_read_b128 v[222:225], v252 offset:4608\n"
        "v_cvt_pk_bf16_f32 v81, v82, v83\n"
        "v_mfma_f32_16x16x32_bf16 v[180:183], a[220:223], a[204:207], v[180:183]\n"
        "v_exp_f32 v84, v84\n"
        "v_exp_f32 v85, v85\n"
        "v_exp_f32 v86, v86\n"
        "v_cvt_pk_bf16_f32 v82, v84, v85\n"
        "v_exp_f32 v87, v87\n"
        "v_exp_f32 v88, v88\n"
        "v_cvt_pk_bf16_f32 v83, v86, v87\n"
        "v_mfma_f32_16x16x32_bf16 v[84:87], v[192:195], v[196:199], v[40:43]\n"
        "v_exp_f32 v89, v89\n"
        "v_mfma_f32_16x16x32_bf16 v[84:87], v[208:211], v[200:203], v[84:87]\n"
        "s_waitcnt lgkmcnt(1)\n"
        "v_exp_f32 v90, v90\n"
        "v_mfma_f32_16x16x32_bf16 v[184:187], a[236:239], a[156:159], 0\n"
        "ds_read_b128 a[240:243], v240 offset:256\n"
        "v_cvt_pk_bf16_f32 v88, v88, v89\n"
        "v_mfma_f32_16x16x32_bf16 v[184:187], a[232:235], a[152:155], v[184:187]\n"
        "v_exp_f32 v91, v91\n"
        "v_mfma_f32_16x16x32_bf16 v[184:187], a[228:231], a[148:151], v[184:187]\n"
        "ds_read_b128 a[244:247], v241 offset:256\n"
        "v_cvt_pk_bf16_f32 v89, v90, v91\n"
        "v_mfma_f32_16x16x32_bf16 v[184:187], a[224:227], a[144:147], v[184:187]\n"
        "v_exp_f32 v92, v92\n"
        "v_mfma_f32_16x16x32_bf16 v[196:199], a[224:227], a[168:171], 0\n"
        "v_exp_f32 v93, v93\n"
        "v_mfma_f32_16x16x32_bf16 v[196:199], a[228:231], a[172:175], v[196:199]\n"
        "v_exp_f32 v94, v94\n"
        "v_mfma_f32_16x16x32_bf16 v[196:199], a[232:235], a[176:179], v[196:199]\n"
        "ds_read_b128 a[248:251], v240 offset:1792\n"
        "v_cvt_pk_bf16_f32 v90, v92, v93\n"
        "v_mfma_f32_16x16x32_bf16 v[196:199], a[236:239], a[180:183], v[196:199]\n"
        "v_exp_f32 v95, v95\n"
        "v_mfma_f32_16x16x32_bf16 v[200:203], a[236:239], a[204:207], 0\n"
        "ds_read_b128 a[216:219], v241 offset:1792\n"
        "v_cvt_pk_bf16_f32 v91, v94, v95\n"
        "v_mfma_f32_16x16x32_bf16 v[200:203], a[232:235], a[200:203], v[200:203]\n"
        "v_exp_f32 v108, v108\n"
        "v_mfma_f32_16x16x32_bf16 v[200:203], a[228:231], a[196:199], v[200:203]\n"
        "v_exp_f32 v109, v109\n"
        "v_mfma_f32_16x16x32_bf16 v[200:203], a[224:227], a[192:195], v[200:203]\n"
        "v_exp_f32 v110, v110\n"
        "v_exp_f32 v111, v111\n"
        "v_exp_f32 v70, v70\n"
        "v_exp_f32 v69, v69\n"
        "v_exp_f32 v68, v68\n"
        "v_exp_f32 v71, v71\n"
        "v_cvt_pk_bf16_f32 v68, v68, v69\n"
        "v_cvt_pk_bf16_f32 v69, v70, v71\n"
        "v_cvt_pk_bf16_f32 v70, v108, v109\n"
        "v_cvt_pk_bf16_f32 v71, v110, v111\n"
        "v_mfma_f32_16x16x32_bf16 v[108:111], v[192:195], v[216:219], v[56:59]\n"
        "s_waitcnt lgkmcnt(4)\n"
        "v_exp_f32 v112, v112\n"
        "v_mfma_f32_16x16x32_bf16 v[108:111], v[208:211], v[220:223], v[108:111]\n"
        "v_exp_f32 v113, v113\n"
        "v_exp_f32 v114, v114\n"
        "v_exp_f32 v115, v115\n"
        "v_exp_f32 v78, v78\n"
        "v_exp_f32 v77, v77\n"
        "v_exp_f32 v76, v76\n"
        "v_exp_f32 v79, v79\n"
        "v_cvt_pk_bf16_f32 v76, v76, v77\n"
        "v_cvt_pk_bf16_f32 v77, v78, v79\n"
        "v_cvt_pk_bf16_f32 v78, v112, v113\n"
        "v_cvt_pk_bf16_f32 v79, v114, v115\n"
        "v_mfma_f32_16x16x32_bf16 v[112:115], v[212:215], v[216:219], v[60:63]\n"
        "v_exp_f32 v116, v116\n"
        "v_mfma_f32_16x16x32_bf16 v[112:115], v[228:231], v[224:227], v[112:115]\n"
        "s_waitcnt lgkmcnt(3)\n"
        "v_exp_f32 v117, v117\n"
        "v_mfma_f32_16x16x32_bf16 v[120:123], a[240:243], a[160:163], v[120:123]\n"
        "s_waitcnt lgkmcnt(2)\n"
        "v_exp_f32 v118, v118\n"
        "v_mfma_f32_16x16x32_bf16 v[120:123], a[244:247], a[164:167], v[120:123]\n"
        "v_exp_f32 v119, v119\n"
        "v_mfma_f32_16x16x32_bf16 v[96:99], a[244:247], a[188:191], v[96:99]\n"
        "ds_read_b128 a[220:223], v240 offset:12544\n"
        "v_mfma_f32_16x16x32_bf16 v[96:99], a[240:243], a[184:187], v[96:99]\n"
        "ds_read_b128 a[224:227], v241 offset:12544\n"
        "v_mfma_f32_16x16x32_bf16 v[100:103], a[240:243], a[208:211], v[100:103]\n"
        "v_exp_f32 v84, v84\n"
        "v_mfma_f32_16x16x32_bf16 v[100:103], a[244:247], a[212:215], v[100:103]\n"
        "s_waitcnt lgkmcnt(2)\n"
        "v_exp_f32 v85, v85\n"
        "v_mfma_f32_16x16x32_bf16 v[104:107], a[216:219], a[164:167], v[104:107]\n"
        "v_exp_f32 v86, v86\n"
        "v_mfma_f32_16x16x32_bf16 v[104:107], a[248:251], a[160:163], v[104:107]\n"
        "v_cvt_pk_bf16_f32 v84, v84, v85\n"
        "v_cvt_pk_bf16_f32 v128, v120, v121\n"
        "v_mfma_f32_16x16x32_bf16 v[156:159], a[248:251], a[184:187], v[156:159]\n"
        "ds_read_b128 a[228:231], v240 offset:14080\n"
        "v_cvt_pk_bf16_f32 v129, v122, v123\n"
        "v_mfma_f32_16x16x32_bf16 v[156:159], a[216:219], a[188:191], v[156:159]\n"
        "ds_read_b128 a[232:235], v241 offset:14080\n"
        "v_cvt_pk_bf16_f32 v136, v96, v97\n"
        "v_mfma_f32_16x16x32_bf16 v[160:163], a[216:219], a[212:215], v[160:163]\n"
        "v_exp_f32 v87, v87\n"
        "v_mfma_f32_16x16x32_bf16 v[160:163], a[248:251], a[208:211], v[160:163]\n"
        "s_waitcnt lgkmcnt(3)\n"
        "v_cvt_pk_bf16_f32 v85, v86, v87\n"
        "v_cvt_pk_bf16_f32 v86, v116, v117\n"
        "v_mfma_f32_16x16x32_bf16 v[164:167], a[220:223], a[160:163], v[164:167]\n"
        "s_waitcnt lgkmcnt(2)\n"
        "v_cvt_pk_bf16_f32 v87, v118, v119\n"
        "v_cvt_pk_bf16_f32 v137, v98, v99\n"
        "v_mfma_f32_16x16x32_bf16 v[164:167], a[224:227], a[164:167], v[164:167]\n"
        "v_exp_f32 v108, v108\n"
        "v_mfma_f32_16x16x32_bf16 v[176:179], a[224:227], a[188:191], v[176:179]\n"
        "v_exp_f32 v109, v109\n"
        "v_mfma_f32_16x16x32_bf16 v[176:179], a[220:223], a[184:187], v[176:179]\n"
        "v_exp_f32 v110, v110\n"
        "v_mfma_f32_16x16x32_bf16 v[180:183], a[220:223], a[208:211], v[180:183]\n"
        "s_waitcnt lgkmcnt(0)\n"
        "s_barrier\n"
        "v_cvt_pk_bf16_f32 v92, v108, v109\n"
        "v_cvt_pk_bf16_f32 v144, v100, v101\n"
        "v_mfma_f32_16x16x32_bf16 v[180:183], a[224:227], a[212:215], v[180:183]\n"
        "v_exp_f32 v111, v111\n"
        "v_mfma_f32_16x16x32_bf16 v[184:187], a[232:235], a[164:167], v[184:187]\n"
        "v_cvt_pk_bf16_f32 v93, v110, v111\n"
        "v_cvt_pk_bf16_f32 v145, v102, v103\n"
        "v_mfma_f32_16x16x32_bf16 v[184:187], a[228:231], a[160:163], v[184:187]\n"
        "v_exp_f32 v112, v112\n"
        "v_mfma_f32_16x16x32_bf16 v[196:199], a[228:231], a[184:187], v[196:199]\n"
        "v_exp_f32 v113, v113\n"
        "v_mfma_f32_16x16x32_bf16 v[196:199], a[232:235], a[188:191], v[196:199]\n"
        "v_exp_f32 v114, v114\n"
        "v_mfma_f32_16x16x32_bf16 v[200:203], a[232:235], a[212:215], v[200:203]\n"
        "v_cvt_pk_bf16_f32 v94, v112, v113\n"
        "v_cvt_pk_bf16_f32 v130, v104, v105\n"
        "v_mfma_f32_16x16x32_bf16 v[200:203], a[228:231], a[208:211], v[200:203]\n"
        "v_exp_f32 v115, v115\n"
        "v_mov_b64 v[120:121], 0\n"
        "s_nop 1\n"
        "v_smfmac_f32_16x16x64_bf16 v[120:123], a[252:255], v[64:71], v255\n"
        "v_cvt_pk_bf16_f32 v95, v114, v115\n"
        "v_cvt_pk_bf16_f32 v131, v106, v107\n"
        "s_nop 1\n"
        "v_mfma_f32_16x16x32_bf16 v[116:119], v[128:131], v[64:67], 0\n"
        "v_cvt_pk_bf16_f32 v138, v156, v157\n"
        "v_cvt_pk_bf16_f32 v139, v158, v159\n"
        "v_cvt_pk_bf16_f32 v132, v164, v165\n"
        "v_cvt_pk_bf16_f32 v133, v166, v167\n"
        "v_cvt_pk_bf16_f32 v134, v184, v185\n"
        "v_cvt_pk_bf16_f32 v135, v186, v187\n"
        "s_nop 1\n"
        "v_mfma_f32_16x16x32_bf16 v[116:119], v[132:135], v[68:71], v[116:119]\n"
        "v_cvt_pk_bf16_f32 v146, v160, v161\n"
        "v_cvt_pk_bf16_f32 v147, v162, v163\n"
        "v_cvt_pk_bf16_f32 v140, v176, v177\n"
        "v_cvt_pk_bf16_f32 v141, v178, v179\n"
        "v_cvt_pk_bf16_f32 v142, v196, v197\n"
        "v_cvt_pk_bf16_f32 v143, v198, v199\n"
        "s_nop 1\n"
        "v_mfma_f32_16x16x32_bf16 v[152:155], v[140:143], v[68:71], 0\n"
        "v_mov_b64 v[172:173], 0\n"
        "v_cvt_pk_bf16_f32 v148, v180, v181\n"
        "v_mfma_f32_16x16x32_bf16 v[152:155], v[136:139], v[64:67], v[152:155]\n"
        "v_cvt_pk_bf16_f32 v149, v182, v183\n"
        "v_cvt_pk_bf16_f32 v150, v200, v201\n"
        "v_mfma_f32_16x16x32_bf16 v[160:163], v[144:147], v[64:67], 0\n"
        "v_cvt_pk_bf16_f32 v151, v202, v203\n"
        "v_mov_b64 v[192:193], 0\n"
        "s_nop 0\n"
        "v_mfma_f32_16x16x32_bf16 v[160:163], v[148:151], v[68:71], v[160:163]\n"
        "v_add_f32 v120, v120, v121\n"
        "v_add_u32 v240, 24576, v240\n"
        "v_smfmac_f32_16x16x64_bf16 v[172:175], a[252:255], v[72:79], v255\n"
        "v_add_u32 v241, 24576, v241\n"
        "v_mfma_f32_16x16x32_bf16 v[176:179], v[128:131], v[72:75], 0\n"
        "v_rcp_f32 v121, v120\n"
        "v_mfma_f32_16x16x32_bf16 v[176:179], v[132:135], v[76:79], v[176:179]\n"
        "v_mov_b64 v[212:213], 0\n"
        "v_mul_f32 v116, v121, v116\n"
        "v_mfma_f32_16x16x32_bf16 v[180:183], v[140:143], v[76:79], 0\n"
        "v_mul_f32 v117, v121, v117\n"
        "v_mul_f32 v118, v121, v118\n"
        "v_mfma_f32_16x16x32_bf16 v[180:183], v[136:139], v[72:75], v[180:183]\n"
        "v_mul_f32 v119, v121, v119\n"
        "v_cvt_pk_bf16_f32 v116, v116, v117\n"
        "v_mfma_f32_16x16x32_bf16 v[184:187], v[144:147], v[72:75], 0\n"
        "v_cvt_pk_bf16_f32 v117, v118, v119\n"
        "v_mul_f32 v152, v121, v152\n"
        "v_mfma_f32_16x16x32_bf16 v[184:187], v[148:151], v[76:79], v[184:187]\n"
        "v_mul_f32 v153, v121, v153\n"
        "v_maximum3_f32 v253, v253, v120, v121\n"
        "v_smfmac_f32_16x16x64_bf16 v[192:195], a[252:255], v[80:87], v255\n"
        "s_waitcnt vmcnt(5)\n"
        "s_barrier\n"
        "ds_read_b128 a[236:239], v240 offset:0\n"
        "v_mul_f32 v154, v121, v154\n"
        "v_mfma_f32_16x16x32_bf16 v[196:199], v[128:131], v[80:83], 0\n"
        "ds_read_b128 a[240:243], v241 offset:0\n"
        "v_mul_f32 v155, v121, v155\n"
        "v_mfma_f32_16x16x32_bf16 v[196:199], v[132:135], v[84:87], v[196:199]\n"
        "v_cvt_pk_bf16_f32 v118, v152, v153\n"
        "v_cvt_pk_bf16_f32 v119, v154, v155\n"
        "v_mfma_f32_16x16x32_bf16 v[200:203], v[140:143], v[84:87], 0\n"
        "ds_read_b128 a[244:247], v240 offset:128\n"
        "v_mul_f32 v160, v121, v160\n"
        "v_mfma_f32_16x16x32_bf16 v[200:203], v[136:139], v[80:83], v[200:203]\n"
        "ds_read_b128 a[248:251], v241 offset:128\n"
        "v_mul_f32 v161, v121, v161\n"
        "v_mfma_f32_16x16x32_bf16 v[204:207], v[144:147], v[80:83], 0\n"
        "ds_read_b128 a[216:219], v240 offset:256\n"
        "v_mul_f32 v162, v121, v162\n"
        "v_mfma_f32_16x16x32_bf16 v[204:207], v[148:151], v[84:87], v[204:207]\n"
        "ds_read_b128 a[220:223], v241 offset:256\n"
        "v_mul_f32 v163, v121, v163\n"
        "v_smfmac_f32_16x16x64_bf16 v[212:215], a[252:255], v[88:95], v255\n"
        "ds_write_b128 v245, v[116:119] offset:0\n"
        "v_mfma_f32_16x16x32_bf16 v[216:219], v[128:131], v[88:91], 0\n"
        "v_cvt_pk_bf16_f32 v160, v160, v161\n"
        "v_cvt_pk_bf16_f32 v161, v162, v163\n"
        "v_mfma_f32_16x16x32_bf16 v[216:219], v[132:135], v[92:95], v[216:219]\n"
        "v_add_f32 v172, v172, v173\n"
        "v_add_f32 v192, v192, v193\n"
        "v_mfma_f32_16x16x32_bf16 v[220:223], v[140:143], v[92:95], 0\n"
        "v_rcp_f32 v173, v172\n"
        "v_mfma_f32_16x16x32_bf16 v[220:223], v[136:139], v[88:91], v[220:223]\n"
        "s_add_u32 m0, s66, 0\n"
        "ds_write_b64 v246, v[160:161] offset:0\n"
        "v_mfma_f32_16x16x32_bf16 v[224:227], v[144:147], v[88:91], 0\n"
        "s_add_u32 s64, s51, 0\n"
        "v_rcp_f32 v193, v192\n"
        "v_mfma_f32_16x16x32_bf16 v[224:227], v[148:151], v[92:95], v[224:227]\n"
        "s_waitcnt lgkmcnt(7)\n"
        "v_mul_f32 v176, v173, v176\n"
        "v_mul_f32 v177, v173, v177\n"
        "v_mfma_f32_16x16x32_bf16 v[96:99], a[72:75], a[236:239], 0\n"
        "s_waitcnt lgkmcnt(6)\n"
        "v_mul_f32 v178, v173, v178\n"
        "v_mul_f32 v179, v173, v179\n"
        "v_mfma_f32_16x16x32_bf16 v[96:99], a[76:79], a[240:243], v[96:99]\n"
        "s_waitcnt lgkmcnt(5)\n"
        "buffer_load_dwordx4 v124, s[52:55], s64 offen lds\n"
        "v_mfma_f32_16x16x32_bf16 v[96:99], a[80:83], a[244:247], v[96:99]\n"
        "s_waitcnt lgkmcnt(4)\n"
        "v_cvt_pk_bf16_f32 v176, v176, v177\n"
        "v_cvt_pk_bf16_f32 v177, v178, v179\n"
        "v_mfma_f32_16x16x32_bf16 v[96:99], a[84:87], a[248:251], v[96:99]\n"
        "s_waitcnt lgkmcnt(3)\n"
        "v_mul_f32 v180, v173, v180\n"
        "v_mul_f32 v181, v173, v181\n"
        "v_mfma_f32_16x16x32_bf16 v[96:99], a[88:91], a[216:219], v[96:99]\n"
        "s_waitcnt lgkmcnt(2)\n"
        "v_maximum3_f32 v253, v253, v172, v173\n"
        "v_mul_f32 v182, v173, v182\n"
        "v_mfma_f32_16x16x32_bf16 v[96:99], a[92:95], a[220:223], v[96:99]\n"
        "s_add_u32 s41, s29, s40\n"
        "v_mul_f32 v183, v173, v183\n"
        "v_cvt_pk_bf16_f32 v178, v180, v181\n"
        "v_mfma_f32_16x16x32_bf16 v[100:103], a[116:119], a[220:223], 0\n"
        "s_add_u32 s35, s47, 786432\n"
        "v_cvt_pk_bf16_f32 v179, v182, v183\n"
        "v_mul_f32 v184, v173, v184\n"
        "v_mfma_f32_16x16x32_bf16 v[100:103], a[112:115], a[216:219], v[100:103]\n"
        "s_sub_u32 s46, s35, 100663296\n"
        "buffer_load_dwordx4 v125, s[52:55], s64 offen offset:1024 lds\n"
        "v_mfma_f32_16x16x32_bf16 v[100:103], a[108:111], a[248:251], v[100:103]\n"
        "s_cmp_lt_u32 s46, 786432\n"
        "v_cvt_pk_bf16_f32 v152, v96, v97\n"
        "v_cvt_pk_bf16_f32 v153, v98, v99\n"
        "v_mfma_f32_16x16x32_bf16 v[100:103], a[104:107], a[244:247], v[100:103]\n"
        "s_cselect_b32 s35, s46, s35\n"
        "ds_write_b128 v245, v[176:179] offset:256\n"
        "v_mfma_f32_16x16x32_bf16 v[100:103], a[100:103], a[240:243], v[100:103]\n"
        "s_sub_u32 s31, s28, s30\n"
        "v_mul_f32 v185, v173, v185\n"
        "v_mul_f32 v186, v173, v186\n"
        "v_mfma_f32_16x16x32_bf16 v[100:103], a[96:99], a[236:239], v[100:103]\n"
        "s_sub_u32 s45, s29, s28\n"
        "v_mul_f32 v187, v173, v187\n"
        "v_cvt_pk_bf16_f32 v184, v184, v185\n"
        "v_mfma_f32_16x16x32_bf16 v[104:107], a[120:123], a[236:239], 0\n"
        "s_mov_b32 s29, s30\n"
        "v_cvt_pk_bf16_f32 v185, v186, v187\n"
        "v_mul_f32 v196, v193, v196\n"
        "v_mfma_f32_16x16x32_bf16 v[104:107], a[124:127], a[240:243], v[104:107]\n"
        "s_mov_b32 s30, s28\n"
        "v_cvt_pk_bf16_f32 v154, v100, v101\n"
        "v_cvt_pk_bf16_f32 v155, v102, v103\n"
        "v_mfma_f32_16x16x32_bf16 v[104:107], a[128:131], a[244:247], v[104:107]\n"
        "s_add_u32 s28, s28, s45\n"
        "buffer_load_dwordx4 v126, s[52:55], s64 offen offset:2048 lds\n"
        "v_mfma_f32_16x16x32_bf16 v[104:107], a[132:135], a[248:251], v[104:107]\n"
        "ds_write_b64 v246, v[184:185] offset:256\n"
        "v_mfma_f32_16x16x32_bf16 v[104:107], a[136:139], a[216:219], v[104:107]\n"
        "v_mul_f32 v197, v193, v197\n"
        "v_mul_f32 v198, v193, v198\n"
        "v_mfma_f32_16x16x32_bf16 v[104:107], a[140:143], a[220:223], v[104:107]\n"
        "v_mul_f32 v199, v193, v199\n"
        "v_cvt_pk_bf16_f32 v196, v196, v197\n"
        "v_mfma_f32_16x16x32_bf16 v[96:99], a[20:23], a[220:223], 0\n"
        "s_add_u32 m0, s66, 3072\n"
        "v_cvt_pk_bf16_f32 v197, v198, v199\n"
        "v_mul_f32 v200, v193, v200\n"
        "v_mfma_f32_16x16x32_bf16 v[96:99], a[16:19], a[216:219], v[96:99]\n"
        "s_add_u32 s64, s51, 6144\n"
        "v_cvt_pk_bf16_f32 v168, v104, v105\n"
        "v_cvt_pk_bf16_f32 v169, v106, v107\n"
        "v_mfma_f32_16x16x32_bf16 v[96:99], a[12:15], a[248:251], v[96:99]\n"
        "v_mul_f32 v201, v193, v201\n"
        "v_maximum3_f32 v253, v253, v192, v193\n"
        "v_mfma_f32_16x16x32_bf16 v[96:99], a[8:11], a[244:247], v[96:99]\n"
        "buffer_load_dwordx4 v124, s[52:55], s64 offen lds\n"
        "v_mfma_f32_16x16x32_bf16 v[96:99], a[4:7], a[240:243], v[96:99]\n"
        "v_mul_f32 v202, v193, v202\n"
        "v_mul_f32 v203, v193, v203\n"
        "v_mfma_f32_16x16x32_bf16 v[96:99], a[0:3], a[236:239], v[96:99]\n"
        "v_cvt_pk_bf16_f32 v198, v200, v201\n"
        "v_cvt_pk_bf16_f32 v199, v202, v203\n"
        "v_mfma_f32_16x16x32_bf16 v[100:103], a[24:27], a[236:239], 0\n"
        "ds_write_b128 v245, v[196:199] offset:512\n"
        "v_mfma_f32_16x16x32_bf16 v[100:103], a[28:31], a[240:243], v[100:103]\n"
        "v_mul_f32 v204, v193, v204\n"
        "v_mul_f32 v205, v193, v205\n"
        "v_mfma_f32_16x16x32_bf16 v[100:103], a[32:35], a[244:247], v[100:103]\n"
        "v_mul_f32 v96, s50, v96\n"
        "v_mul_f32 v97, s50, v97\n"
        "v_mfma_f32_16x16x32_bf16 v[100:103], a[36:39], a[248:251], v[100:103]\n"
        "v_mul_f32 v98, s50, v98\n"
        "v_mul_f32 v99, s50, v99\n"
        "v_mfma_f32_16x16x32_bf16 v[100:103], a[40:43], a[216:219], v[100:103]\n"
        "buffer_load_dwordx4 v125, s[52:55], s64 offen offset:1024 lds\n"
        "v_mfma_f32_16x16x32_bf16 v[100:103], a[44:47], a[220:223], v[100:103]\n"
        "v_cvt_pk_bf16_f32 v156, v96, v97\n"
        "v_cvt_pk_bf16_f32 v157, v98, v99\n"
        "v_mfma_f32_16x16x32_bf16 v[104:107], a[68:71], a[220:223], 0\n"
        "v_mul_f32 v206, v193, v206\n"
        "v_mul_f32 v207, v193, v207\n"
        "v_mfma_f32_16x16x32_bf16 v[104:107], a[64:67], a[216:219], v[104:107]\n"
        "v_cvt_pk_bf16_f32 v204, v204, v205\n"
        "v_cvt_pk_bf16_f32 v205, v206, v207\n"
        "v_mfma_f32_16x16x32_bf16 v[104:107], a[60:63], a[248:251], v[104:107]\n"
        "v_mul_f32 v100, s50, v100\n"
        "v_mul_f32 v101, s50, v101\n"
        "v_mfma_f32_16x16x32_bf16 v[104:107], a[56:59], a[244:247], v[104:107]\n"
        "v_mul_f32 v102, s50, v102\n"
        "v_mul_f32 v103, s50, v103\n"
        "v_mfma_f32_16x16x32_bf16 v[104:107], a[52:55], a[240:243], v[104:107]\n"
        "v_cvt_pk_bf16_f32 v158, v100, v101\n"
        "v_cvt_pk_bf16_f32 v159, v102, v103\n"
        "v_mfma_f32_16x16x32_bf16 v[104:107], a[48:51], a[236:239], v[104:107]\n"
        "buffer_load_dwordx4 v126, s[52:55], s64 offen offset:2048 lds\n"
        "v_mfma_f32_16x16x32_bf16 v[96:99], a[236:239], a[144:147], 0\n"
        "ds_write_b64 v246, v[204:205] offset:512\n"
        "v_mfma_f32_16x16x32_bf16 v[96:99], a[240:243], a[148:151], v[96:99]\n"
        "v_add_f32 v212, v212, v213\n"
        "v_xor_b32 v251, 0x2000, v251\n"
        "v_mfma_f32_16x16x32_bf16 v[96:99], a[244:247], a[152:155], v[96:99]\n"
        "v_rcp_f32 v213, v212\n"
        "v_mfma_f32_16x16x32_bf16 v[96:99], a[248:251], a[156:159], v[96:99]\n"
        "s_add_u32 m0, s66, 6144\n"
        "v_mul_f32 v104, s50, v104\n"
        "v_mul_f32 v105, s50, v105\n"
        "v_mfma_f32_16x16x32_bf16 v[96:99], a[216:219], a[160:163], v[96:99]\n"
        "s_add_u32 s64, s51, 18432\n"
        "v_mul_f32 v106, s50, v106\n"
        "v_mul_f32 v107, s50, v107\n"
        "v_mfma_f32_16x16x32_bf16 v[96:99], a[220:223], a[164:167], v[96:99]\n"
        "v_cvt_pk_bf16_f32 v160, v104, v105\n"
        "v_cvt_pk_bf16_f32 v161, v106, v107\n"
        "v_mfma_f32_16x16x32_bf16 v[100:103], a[220:223], a[188:191], 0\n"
        "buffer_load_dwordx4 v124, s[52:55], s64 offen lds\n"
        "v_mfma_f32_16x16x32_bf16 v[100:103], a[216:219], a[184:187], v[100:103]\n"
        "v_mov_b64 v[166:167], v[160:161]\n"
        "v_mul_f32 v216, v213, v216\n"
        "v_mfma_f32_16x16x32_bf16 v[100:103], a[248:251], a[180:183], v[100:103]\n"
        "ds_read_b128 a[224:227], v240 offset:1536\n"
        "v_mul_f32 v217, v213, v217\n"
        "v_mfma_f32_16x16x32_bf16 v[100:103], a[244:247], a[176:179], v[100:103]\n"
        "ds_read_b128 a[228:231], v241 offset:1536\n"
        "v_mul_f32 v218, v213, v218\n"
        "v_mfma_f32_16x16x32_bf16 v[100:103], a[240:243], a[172:175], v[100:103]\n"
        "ds_read_b128 a[232:235], v240 offset:1664\n"
        "v_mul_f32 v219, v213, v219\n"
        "v_mfma_f32_16x16x32_bf16 v[100:103], a[236:239], a[168:171], v[100:103]\n"
        "v_cvt_pk_bf16_f32 v216, v216, v217\n"
        "v_cvt_pk_bf16_f32 v217, v218, v219\n"
        "v_mfma_f32_16x16x32_bf16 v[104:107], a[236:239], a[192:195], 0\n"
        "ds_read_b128 a[236:239], v241 offset:1664\n"
        "v_cvt_pk_bf16_f32 v128, v96, v97\n"
        "v_mfma_f32_16x16x32_bf16 v[104:107], a[240:243], a[196:199], v[104:107]\n"
        "ds_read_b128 a[240:243], v240 offset:1792\n"
        "v_cvt_pk_bf16_f32 v129, v98, v99\n"
        "v_mfma_f32_16x16x32_bf16 v[104:107], a[244:247], a[200:203], v[104:107]\n"
        "buffer_load_dwordx4 v125, s[52:55], s64 offen offset:1024 lds\n"
        "v_mfma_f32_16x16x32_bf16 v[104:107], a[248:251], a[204:207], v[104:107]\n"
        "ds_read_b128 a[244:247], v241 offset:1792\n"
        "v_mul_f32 v220, v213, v220\n"
        "v_mfma_f32_16x16x32_bf16 v[104:107], a[216:219], a[208:211], v[104:107]\n"
        "v_mul_f32 v221, v213, v221\n"
        "v_maximum3_f32 v253, v253, v212, v213\n"
        "v_mfma_f32_16x16x32_bf16 v[104:107], a[220:223], a[212:215], v[104:107]\n"
        "s_waitcnt lgkmcnt(5)\n"
        "v_mul_f32 v222, v213, v222\n"
        "v_mul_f32 v223, v213, v223\n"
        "v_mfma_f32_16x16x32_bf16 v[96:99], a[72:75], a[224:227], 0\n"
        "s_waitcnt lgkmcnt(4)\n"
        "v_cvt_pk_bf16_f32 v218, v220, v221\n"
        "v_cvt_pk_bf16_f32 v219, v222, v223\n"
        "v_mfma_f32_16x16x32_bf16 v[96:99], a[76:79], a[228:231], v[96:99]\n"
        "42:\n"
        "s_cbranch_execz 45f\n"
        "s_waitcnt lgkmcnt(3)\n"
        "v_xor_b32 v252, 0x2000, v252\n"
        "v_mul_f32 v224, v213, v224\n"
        "v_mfma_f32_16x16x32_bf16 v[96:99], a[80:83], a[232:235], v[96:99]\n"
        "s_waitcnt lgkmcnt(2)\n"
        "buffer_load_dwordx4 v126, s[52:55], s64 offen offset:2048 lds\n"
        "v_mfma_f32_16x16x32_bf16 v[96:99], a[84:87], a[236:239], v[96:99]\n"
        "s_waitcnt lgkmcnt(1)\n"
        "ds_write_b128 v245, v[216:219] offset:768\n"
        "v_mfma_f32_16x16x32_bf16 v[96:99], a[88:91], a[240:243], v[96:99]\n"
        "s_waitcnt lgkmcnt(1)\n"
        "v_mul_f32 v225, v213, v225\n"
        "v_cvt_pk_bf16_f32 v136, v100, v101\n"
        "v_mfma_f32_16x16x32_bf16 v[96:99], a[92:95], a[244:247], v[96:99]\n"
        "v_cvt_pk_bf16_f32 v137, v102, v103\n"
        "v_mul_f32 v226, v213, v226\n"
        "v_mfma_f32_16x16x32_bf16 v[100:103], a[116:119], a[244:247], 0\n"
        "s_waitcnt vmcnt(9)\n"
        "s_barrier\n"
        "v_mul_f32 v227, v213, v227\n"
        "v_cvt_pk_bf16_f32 v224, v224, v225\n"
        "v_mfma_f32_16x16x32_bf16 v[100:103], a[112:115], a[240:243], v[100:103]\n"
        "s_add_u32 m0, s66, 9216\n"
        "v_cvt_pk_bf16_f32 v225, v226, v227\n"
        "v_cvt_pk_bf16_f32 v144, v104, v105\n"
        "v_mfma_f32_16x16x32_bf16 v[100:103], a[108:111], a[236:239], v[100:103]\n"
        "s_add_u32 s64, s51, 24576\n"
        "ds_write_b64 v246, v[224:225] offset:768\n"
        "v_mfma_f32_16x16x32_bf16 v[100:103], a[104:107], a[232:235], v[100:103]\n"
        "v_cvt_pk_bf16_f32 v145, v106, v107\n"
        "v_cvt_pk_bf16_f32 v172, v96, v97\n"
        "v_mfma_f32_16x16x32_bf16 v[100:103], a[100:103], a[228:231], v[100:103]\n"
        "v_cvt_pk_bf16_f32 v173, v98, v99\n"
        "v_add_u32 v245, s31, v245\n"
        "v_mfma_f32_16x16x32_bf16 v[100:103], a[96:99], a[224:227], v[100:103]\n"
        "buffer_load_dwordx4 v124, s[52:55], s64 offen lds\n"
        "v_mfma_f32_16x16x32_bf16 v[104:107], a[120:123], a[224:227], 0\n"
        "s_waitcnt vmcnt(4)\n"
        "ds_read_b64_tr_b16 v[88:89], v234 offset:0\n"
        "ds_read_b64_tr_b16 v[90:91], v234 offset:3072\n"
        "v_mfma_f32_16x16x32_bf16 v[104:107], a[124:127], a[228:231], v[104:107]\n"
        "ds_read_b64_tr_b16 v[92:93], v235 offset:32\n"
        "ds_read_b64_tr_b16 v[94:95], v235 offset:3104\n"
        "v_mfma_f32_16x16x32_bf16 v[104:107], a[128:131], a[232:235], v[104:107]\n"
        "ds_read_b64_tr_b16 v[120:121], v234 offset:64\n"
        "ds_read_b128 v[162:165], v251 offset:0\n"
        "v_mfma_f32_16x16x32_bf16 v[104:107], a[132:135], a[236:239], v[104:107]\n"
        "ds_read_b64_tr_b16 v[122:123], v234 offset:3136\n"
        "v_cvt_pk_bf16_f32 v174, v100, v101\n"
        "v_mfma_f32_16x16x32_bf16 v[104:107], a[136:139], a[240:243], v[104:107]\n"
        "ds_read_b64_tr_b16 v[216:217], v235 offset:96\n"
        "v_cvt_pk_bf16_f32 v175, v102, v103\n"
        "v_mfma_f32_16x16x32_bf16 v[104:107], a[140:143], a[244:247], v[104:107]\n"
        "buffer_load_dwordx4 v125, s[52:55], s64 offen offset:1024 lds\n"
        "v_mfma_f32_16x16x32_bf16 v[96:99], a[20:23], a[244:247], 0\n"
        "ds_read_b64_tr_b16 v[218:219], v235 offset:3168\n"
        "ds_read_b64_tr_b16 v[220:221], v238 offset:128\n"
        "v_mfma_f32_16x16x32_bf16 v[96:99], a[16:19], a[240:243], v[96:99]\n"
        "ds_read_b64_tr_b16 v[222:223], v238 offset:3200\n"
        "v_add_u32 v246, s31, v246\n"
        "v_mfma_f32_16x16x32_bf16 v[96:99], a[12:15], a[236:239], v[96:99]\n"
        "ds_read_b64_tr_b16 v[224:225], v239 offset:160\n"
        "v_cvt_pk_bf16_f32 v190, v104, v105\n"
        "v_mfma_f32_16x16x32_bf16 v[96:99], a[8:11], a[232:235], v[96:99]\n"
        "ds_read_b64_tr_b16 v[226:227], v239 offset:3232\n"
        "v_cvt_pk_bf16_f32 v191, v106, v107\n"
        "v_mfma_f32_16x16x32_bf16 v[96:99], a[4:7], a[228:231], v[96:99]\n"
        "ds_read_b128 v[182:185], v251 offset:512\n"
        "v_mfma_f32_16x16x32_bf16 v[96:99], a[0:3], a[224:227], v[96:99]\n"
        "buffer_load_dwordx4 v126, s[52:55], s64 offen offset:2048 lds\n"
        "v_mfma_f32_16x16x32_bf16 v[64:67], v[152:155], v[156:159], v[0:3]\n"
        "s_waitcnt lgkmcnt(8)\n"
        "s_waitcnt lgkmcnt(12)\n"
        "ds_write_b128 v236, v[88:91] offset:0\n"
        "v_mfma_f32_16x16x32_bf16 v[64:67], v[168:171], v[160:163], v[64:67]\n"
        "v_mfma_f32_16x16x32_bf16 v[68:71], v[172:175], v[156:159], v[4:7]\n"
        "v_mfma_f32_16x16x32_bf16 v[68:71], v[188:191], v[164:167], v[68:71]\n"
        "s_add_u32 m0, s66, 12288\n"
        "v_mfma_f32_16x16x32_bf16 v[100:103], a[24:27], a[224:227], 0\n"
        "s_add_u32 s64, s51, 12288\n"
        "v_mul_f32 v96, s50, v96\n"
        "v_mul_f32 v97, s50, v97\n"
        "v_mfma_f32_16x16x32_bf16 v[100:103], a[28:31], a[228:231], v[100:103]\n"
        "v_mul_f32 v98, s50, v98\n"
        "v_mul_f32 v99, s50, v99\n"
        "v_mfma_f32_16x16x32_bf16 v[100:103], a[32:35], a[232:235], v[100:103]\n"
        "buffer_load_dwordx4 v127, s[52:55], s64 offen lds\n"
        "v_mfma_f32_16x16x32_bf16 v[100:103], a[36:39], a[236:239], v[100:103]\n"
        "v_cvt_pk_bf16_f32 v176, v96, v97\n"
        "v_cvt_pk_bf16_f32 v177, v98, v99\n"
        "v_mfma_f32_16x16x32_bf16 v[100:103], a[40:43], a[240:243], v[100:103]\n"
        "ds_write_b128 v236, v[92:95] offset:384\n"
        "v_mfma_f32_16x16x32_bf16 v[100:103], a[44:47], a[244:247], v[100:103]\n"
        "v_exp_f32 v64, v64\n"
        "v_mfma_f32_16x16x32_bf16 v[104:107], a[68:71], a[244:247], 0\n"
        "v_exp_f32 v65, v65\n"
        "v_mfma_f32_16x16x32_bf16 v[104:107], a[64:67], a[240:243], v[104:107]\n"
        "v_exp_f32 v66, v66\n"
        "v_mfma_f32_16x16x32_bf16 v[104:107], a[60:63], a[236:239], v[104:107]\n"
        "v_exp_f32 v67, v67\n"
        "v_mfma_f32_16x16x32_bf16 v[104:107], a[56:59], a[232:235], v[104:107]\n"
        "buffer_load_dwordx4 v232, s[52:55], s64 offen offset:1024 lds\n"
        "v_mfma_f32_16x16x32_bf16 v[104:107], a[52:55], a[228:231], v[104:107]\n"
        "s_waitcnt lgkmcnt(9)\n"
        "ds_write_b128 v236, v[120:123] offset:768\n"
        "v_mfma_f32_16x16x32_bf16 v[104:107], a[48:51], a[224:227], v[104:107]\n"
        "v_mul_f32 v100, s50, v100\n"
        "v_mul_f32 v101, s50, v101\n"
        "v_mfma_f32_16x16x32_bf16 v[96:99], a[224:227], a[144:147], 0\n"
        "v_mul_f32 v102, s50, v102\n"
        "v_mul_f32 v103, s50, v103\n"
        "v_mfma_f32_16x16x32_bf16 v[96:99], a[228:231], a[148:151], v[96:99]\n"
        "v_cvt_pk_bf16_f32 v178, v100, v101\n"
        "v_cvt_pk_bf16_f32 v179, v102, v103\n"
        "v_mfma_f32_16x16x32_bf16 v[96:99], a[232:235], a[152:155], v[96:99]\n"
        "s_waitcnt lgkmcnt(8)\n"
        "ds_write_b128 v236, v[216:219] offset:1152\n"
        "v_mfma_f32_16x16x32_bf16 v[96:99], a[236:239], a[156:159], v[96:99]\n"
        "v_mul_f32 v104, s50, v104\n"
        "v_mul_f32 v105, s50, v105\n"
        "v_mfma_f32_16x16x32_bf16 v[96:99], a[240:243], a[160:163], v[96:99]\n"
        "v_mul_f32 v106, s50, v106\n"
        "v_mul_f32 v107, s50, v107\n"
        "v_mfma_f32_16x16x32_bf16 v[96:99], a[244:247], a[164:167], v[96:99]\n"
        "v_cvt_pk_bf16_f32 v180, v104, v105\n"
        "v_cvt_pk_bf16_f32 v181, v106, v107\n"
        "v_mfma_f32_16x16x32_bf16 v[72:75], v[152:155], v[176:179], v[16:19]\n"
        "s_waitcnt lgkmcnt(4)\n"
        "buffer_load_dwordx4 v233, s[52:55], s64 offen offset:2048 lds\n"
        "v_mfma_f32_16x16x32_bf16 v[72:75], v[168:171], v[180:183], v[72:75]\n"
        "ds_write_b128 v237, v[220:223] offset:0\n"
        "v_mfma_f32_16x16x32_bf16 v[76:79], v[172:175], v[176:179], v[20:23]\n"
        "s_add_u32 m0, s66, 15360\n"
        "v_mov_b64 v[186:187], v[180:181]\n"
        "v_cvt_pk_bf16_f32 v130, v96, v97\n"
        "s_nop 0\n"
        "v_mfma_f32_16x16x32_bf16 v[76:79], v[188:191], v[184:187], v[76:79]\n"
        "s_add_u32 s64, s51, 13824\n"
        "v_cvt_pk_bf16_f32 v131, v98, v99\n"
        "v_cvt_pk_bf16_f32 v64, v64, v65\n"
        "v_mfma_f32_16x16x32_bf16 v[100:103], a[244:247], a[188:191], 0\n"
        "v_exp_f32 v68, v68\n"
        "v_mfma_f32_16x16x32_bf16 v[100:103], a[240:243], a[184:187], v[100:103]\n"
        "buffer_load_dwordx4 v127, s[52:55], s64 offen lds\n"
        "v_mfma_f32_16x16x32_bf16 v[100:103], a[236:239], a[180:183], v[100:103]\n"
        "ds_read_b128 a[248:251], v240 offset:12288\n"
        "v_cvt_pk_bf16_f32 v65, v66, v67\n"
        "v_mfma_f32_16x16x32_bf16 v[100:103], a[232:235], a[176:179], v[100:103]\n"
        "ds_read_b128 a[216:219], v241 offset:12288\n"
        "v_mfma_f32_16x16x32_bf16 v[100:103], a[228:231], a[172:175], v[100:103]\n"
        "ds_read_b128 a[220:223], v240 offset:12416\n"
        "v_mfma_f32_16x16x32_bf16 v[100:103], a[224:227], a[168:171], v[100:103]\n"
        "ds_write_b128 v237, v[224:227] offset:768\n"
        "v_mfma_f32_16x16x32_bf16 v[104:107], a[224:227], a[192:195], 0\n"
        "v_exp_f32 v69, v69\n"
        "v_mfma_f32_16x16x32_bf16 v[104:107], a[228:231], a[196:199], v[104:107]\n"
        "ds_read_b128 a[224:227], v241 offset:12416\n"
        "v_cvt_pk_bf16_f32 v66, v68, v69\n"
        "v_mfma_f32_16x16x32_bf16 v[104:107], a[232:235], a[200:203], v[104:107]\n"
        "ds_read_b128 a[228:231], v240 offset:12544\n"
        "v_cvt_pk_bf16_f32 v138, v100, v101\n"
        "v_mfma_f32_16x16x32_bf16 v[104:107], a[236:239], a[204:207], v[104:107]\n"
        "ds_read_b128 a[232:235], v241 offset:12544\n"
        "v_cvt_pk_bf16_f32 v139, v102, v103\n"
        "v_mfma_f32_16x16x32_bf16 v[104:107], a[240:243], a[208:211], v[104:107]\n"
        "buffer_load_dwordx4 v232, s[52:55], s64 offen offset:1024 lds\n"
        "v_mfma_f32_16x16x32_bf16 v[104:107], a[244:247], a[212:215], v[104:107]\n"
        "s_waitcnt lgkmcnt(6)\n"
        "v_exp_f32 v70, v70\n"
        "v_mfma_f32_16x16x32_bf16 v[96:99], a[72:75], a[248:251], 0\n"
        "s_waitcnt lgkmcnt(5)\n"
        "v_exp_f32 v71, v71\n"
        "v_mfma_f32_16x16x32_bf16 v[96:99], a[76:79], a[216:219], v[96:99]\n"
        "s_waitcnt lgkmcnt(4)\n"
        "v_exp_f32 v72, v72\n"
        "v_mfma_f32_16x16x32_bf16 v[96:99], a[80:83], a[220:223], v[96:99]\n"
        "s_waitcnt lgkmcnt(2)\n"
        "v_exp_f32 v73, v73\n"
        "v_mfma_f32_16x16x32_bf16 v[96:99], a[84:87], a[224:227], v[96:99]\n"
        "s_waitcnt lgkmcnt(1)\n"
        "buffer_load_dwordx4 v233, s[52:55], s64 offen offset:2048 lds\n"
        "v_mfma_f32_16x16x32_bf16 v[96:99], a[88:91], a[228:231], v[96:99]\n"
        "s_waitcnt lgkmcnt(0)\n"
        "s_waitcnt vmcnt(6)\n"
        "ds_read_b64_tr_b16 v[88:89], v234 offset:6144\n"
        "v_cvt_pk_bf16_f32 v146, v104, v105\n"
        "v_mfma_f32_16x16x32_bf16 v[96:99], a[92:95], a[232:235], v[96:99]\n"
        "ds_read_b64_tr_b16 v[90:91], v234 offset:9216\n"
        "v_cvt_pk_bf16_f32 v147, v106, v107\n"
        "v_mfma_f32_16x16x32_bf16 v[100:103], a[116:119], a[232:235], 0\n"
        "ds_read_b64_tr_b16 v[92:93], v235 offset:6176\n"
        "v_cvt_pk_bf16_f32 v67, v70, v71\n"
        "v_mfma_f32_16x16x32_bf16 v[100:103], a[112:115], a[228:231], v[100:103]\n"
        "ds_read_b64_tr_b16 v[94:95], v235 offset:9248\n"
        "v_cvt_pk_bf16_f32 v72, v72, v73\n"
        "v_mfma_f32_16x16x32_bf16 v[100:103], a[108:111], a[224:227], v[100:103]\n"
        "s_add_u32 m0, s41, 0\n"
        "ds_read_b64_tr_b16 v[120:121], v234 offset:6208\n"
        "v_cvt_pk_bf16_f32 v192, v96, v97\n"
        "v_mfma_f32_16x16x32_bf16 v[100:103], a[104:107], a[220:223], v[100:103]\n"
        "ds_read_b64_tr_b16 v[122:123], v234 offset:9280\n"
        "v_cvt_pk_bf16_f32 v193, v98, v99\n"
        "v_mfma_f32_16x16x32_bf16 v[100:103], a[100:103], a[216:219], v[100:103]\n"
        "ds_read_b64_tr_b16 v[216:217], v235 offset:6240\n"
        "v_mfma_f32_16x16x32_bf16 v[100:103], a[96:99], a[248:251], v[100:103]\n"
        "buffer_load_dwordx4 v242, s[8:11], s47 offen lds\n"
        "v_mfma_f32_16x16x32_bf16 v[104:107], a[120:123], a[248:251], 0\n"
        "ds_read_b64_tr_b16 v[218:219], v235 offset:9312\n"
        "ds_read_b64_tr_b16 v[220:221], v238 offset:6272\n"
        "v_mfma_f32_16x16x32_bf16 v[104:107], a[124:127], a[216:219], v[104:107]\n"
        "s_add_u32 m0, s41, 1024\n"
        "ds_read_b64_tr_b16 v[222:223], v238 offset:9344\n"
        "v_mfma_f32_16x16x32_bf16 v[104:107], a[128:131], a[220:223], v[104:107]\n"
        "ds_read_b64_tr_b16 v[224:225], v239 offset:6304\n"
        "v_cvt_pk_bf16_f32 v194, v100, v101\n"
        "v_mfma_f32_16x16x32_bf16 v[104:107], a[132:135], a[224:227], v[104:107]\n"
        "ds_read_b64_tr_b16 v[226:227], v239 offset:9376\n"
        "v_cvt_pk_bf16_f32 v195, v102, v103\n"
        "v_mfma_f32_16x16x32_bf16 v[104:107], a[136:139], a[228:231], v[104:107]\n"
        "buffer_load_dwordx4 v243, s[8:11], s47 offen lds\n"
        "v_mfma_f32_16x16x32_bf16 v[104:107], a[140:143], a[232:235], v[104:107]\n"
        "s_waitcnt lgkmcnt(10)\n"
        "ds_write_b128 v236, v[88:91] offset:6144\n"
        "v_mfma_f32_16x16x32_bf16 v[96:99], a[20:23], a[232:235], 0\n"
        "s_add_u32 m0, s41, 2048\n"
        "v_exp_f32 v74, v74\n"
        "v_mfma_f32_16x16x32_bf16 v[96:99], a[16:19], a[228:231], v[96:99]\n"
        "v_exp_f32 v75, v75\n"
        "v_mfma_f32_16x16x32_bf16 v[96:99], a[12:15], a[224:227], v[96:99]\n"
        "v_exp_f32 v76, v76\n"
        "v_mfma_f32_16x16x32_bf16 v[96:99], a[8:11], a[220:223], v[96:99]\n"
        "buffer_load_dwordx4 v244, s[8:11], s47 offen lds\n"
        "v_mfma_f32_16x16x32_bf16 v[96:99], a[4:7], a[216:219], v[96:99]\n"
        "s_waitcnt lgkmcnt(9)\n"
        "ds_write_b128 v236, v[92:95] offset:6528\n"
        "v_mfma_f32_16x16x32_bf16 v[96:99], a[0:3], a[248:251], v[96:99]\n"
        "s_add_u32 m0, s41, 12288\n"
        "v_cvt_pk_bf16_f32 v208, v104, v105\n"
        "v_cvt_pk_bf16_f32 v209, v106, v107\n"
        "v_mfma_f32_16x16x32_bf16 v[100:103], a[24:27], a[248:251], 0\n"
        "v_exp_f32 v77, v77\n"
        "v_mfma_f32_16x16x32_bf16 v[100:103], a[28:31], a[216:219], v[100:103]\n"
        "v_exp_f32 v78, v78\n"
        "v_mfma_f32_16x16x32_bf16 v[100:103], a[32:35], a[220:223], v[100:103]\n"
        "buffer_load_dwordx4 v242, s[8:11], s35 offen lds\n"
        "v_mfma_f32_16x16x32_bf16 v[100:103], a[36:39], a[224:227], v[100:103]\n"
        "ds_read_b128 v[162:165], v252 offset:0\n"
        "v_mul_f32 v96, s50, v96\n"
        "v_mfma_f32_16x16x32_bf16 v[100:103], a[40:43], a[228:231], v[100:103]\n"
        "s_add_u32 m0, s41, 13312\n"
        "s_waitcnt lgkmcnt(9)\n"
        "ds_write_b128 v236, v[120:123] offset:6912\n"
        "v_mfma_f32_16x16x32_bf16 v[100:103], a[44:47], a[232:235], v[100:103]\n"
        "v_mul_f32 v97, s50, v97\n"
        "v_mul_f32 v98, s50, v98\n"
        "v_mfma_f32_16x16x32_bf16 v[104:107], a[68:71], a[232:235], 0\n"
        "v_mul_f32 v99, s50, v99\n"
        "v_cvt_pk_bf16_f32 v196, v96, v97\n"
        "v_mfma_f32_16x16x32_bf16 v[104:107], a[64:67], a[228:231], v[104:107]\n"
        "v_cvt_pk_bf16_f32 v197, v98, v99\n"
        "v_cvt_pk_bf16_f32 v73, v74, v75\n"
        "v_mfma_f32_16x16x32_bf16 v[104:107], a[60:63], a[224:227], v[104:107]\n"
        "buffer_load_dwordx4 v243, s[8:11], s35 offen lds\n"
        "v_mfma_f32_16x16x32_bf16 v[104:107], a[56:59], a[220:223], v[104:107]\n"
        "s_add_u32 m0, s41, 14336\n"
        "v_mul_f32 v100, s50, v100\n"
        "v_mul_f32 v101, s50, v101\n"
        "v_mfma_f32_16x16x32_bf16 v[104:107], a[52:55], a[216:219], v[104:107]\n"
        "ds_read_b128 v[182:185], v252 offset:512\n"
        "v_mul_f32 v102, s50, v102\n"
        "v_mfma_f32_16x16x32_bf16 v[104:107], a[48:51], a[248:251], v[104:107]\n"
        "buffer_load_dwordx4 v244, s[8:11], s35 offen lds\n"
        "v_mfma_f32_16x16x32_bf16 v[68:71], v[192:195], v[156:159], v[8:11]\n"
        "s_waitcnt lgkmcnt(2)\n"
        "s_waitcnt lgkmcnt(9)\n"
        "ds_write_b128 v236, v[216:219] offset:7296\n"
        "v_mfma_f32_16x16x32_bf16 v[68:71], v[208:211], v[160:163], v[68:71]\n"
        "v_mul_f32 v103, s50, v103\n"
        "v_cvt_pk_bf16_f32 v198, v100, v101\n"
        "v_mfma_f32_16x16x32_bf16 v[96:99], a[248:251], a[144:147], 0\n"
        "s_mov_b32 m0, s48\n"
        "v_cvt_pk_bf16_f32 v199, v102, v103\n"
        "v_mul_f32 v104, s50, v104\n"
        "v_mfma_f32_16x16x32_bf16 v[96:99], a[216:219], a[148:151], v[96:99]\n"
        "v_mul_f32 v105, s50, v105\n"
        "v_mul_f32 v106, s50, v106\n"
        "v_mfma_f32_16x16x32_bf16 v[96:99], a[220:223], a[152:155], v[96:99]\n"
        "buffer_load_dwordx4 v249, s[12:15], s36 offen offset:0 lds\n"
        "v_mfma_f32_16x16x32_bf16 v[96:99], a[224:227], a[156:159], v[96:99]\n"
        "v_mul_f32 v107, s50, v107\n"
        "v_cvt_pk_bf16_f32 v200, v104, v105\n"
        "v_mfma_f32_16x16x32_bf16 v[96:99], a[228:231], a[160:163], v[96:99]\n"
        "ds_read_b128 v[202:205], v251 offset:4096\n"
        "v_cvt_pk_bf16_f32 v201, v106, v107\n"
        "v_mfma_f32_16x16x32_bf16 v[96:99], a[232:235], a[164:167], v[96:99]\n"
        "ds_write_b128 v237, v[220:223] offset:6144\n"
        "v_cvt_pk_bf16_f32 v74, v76, v77\n"
        "v_exp_f32 v79, v79\n"
        "v_mov_b64 v[206:207], v[200:201]\n"
        "v_cvt_pk_bf16_f32 v75, v78, v79\n"
        "v_mfma_f32_16x16x32_bf16 v[76:79], v[192:195], v[176:179], v[24:27]\n"
        "s_waitcnt lgkmcnt(3)\n"
        "v_xor_b32 v116, 32, v249\n"
        "v_mfma_f32_16x16x32_bf16 v[76:79], v[208:211], v[180:183], v[76:79]\n"
        "v_cvt_pk_bf16_f32 v132, v96, v97\n"
        "v_cvt_pk_bf16_f32 v133, v98, v99\n"
        "v_mfma_f32_16x16x32_bf16 v[100:103], a[232:235], a[188:191], 0\n"
        "v_exp_f32 v68, v68\n"
        "v_mfma_f32_16x16x32_bf16 v[100:103], a[228:231], a[184:187], v[100:103]\n"
        "buffer_load_dwordx4 v116, s[12:15], s36 offen offset:1024 lds\n"
        "v_mfma_f32_16x16x32_bf16 v[100:103], a[224:227], a[180:183], v[100:103]\n"
        "ds_write_b128 v237, v[224:227] offset:6912\n"
        "v_mfma_f32_16x16x32_bf16 v[100:103], a[220:223], a[176:179], v[100:103]\n"
        "v_exp_f32 v69, v69\n"
        "v_mfma_f32_16x16x32_bf16 v[100:103], a[216:219], a[172:175], v[100:103]\n"
        "v_exp_f32 v70, v70\n"
        "v_mfma_f32_16x16x32_bf16 v[100:103], a[248:251], a[168:171], v[100:103]\n"
        "v_exp_f32 v71, v71\n"
        "v_mfma_f32_16x16x32_bf16 v[80:83], v[152:155], v[196:199], v[32:35]\n"
        "s_waitcnt lgkmcnt(2)\n"
        "s_waitcnt vmcnt(8)\n"
        "ds_read_b64_tr_b16 v[220:221], v234 offset:12288\n"
        "ds_read_b64_tr_b16 v[222:223], v234 offset:15360\n"
        "v_mfma_f32_16x16x32_bf16 v[80:83], v[168:171], v[200:203], v[80:83]\n"
        "ds_read_b64_tr_b16 v[224:225], v235 offset:12320\n"
        "ds_read_b128 a[236:239], v240 offset:13824\n"
        "v_mfma_f32_16x16x32_bf16 v[104:107], a[248:251], a[192:195], 0\n"
        "ds_read_b64_tr_b16 v[226:227], v235 offset:15392\n"
        "ds_read_b128 a[240:243], v241 offset:13824\n"
        "v_mfma_f32_16x16x32_bf16 v[104:107], a[216:219], a[196:199], v[104:107]\n"
        "ds_read_b64_tr_b16 v[88:89], v234 offset:12352\n"
        "ds_read_b128 a[244:247], v240 offset:13952\n"
        "v_mfma_f32_16x16x32_bf16 v[104:107], a[220:223], a[200:203], v[104:107]\n"
        "ds_read_b64_tr_b16 v[90:91], v234 offset:15424\n"
        "ds_read_b128 a[248:251], v241 offset:13952\n"
        "v_mfma_f32_16x16x32_bf16 v[104:107], a[224:227], a[204:207], v[104:107]\n"
        "ds_read_b64_tr_b16 v[92:93], v235 offset:12384\n"
        "ds_read_b128 a[216:219], v240 offset:14080\n"
        "v_mfma_f32_16x16x32_bf16 v[104:107], a[228:231], a[208:211], v[104:107]\n"
        "ds_read_b64_tr_b16 v[94:95], v235 offset:15456\n"
        "ds_read_b128 a[220:223], v241 offset:14080\n"
        "v_mfma_f32_16x16x32_bf16 v[104:107], a[232:235], a[212:215], v[104:107]\n"
        "ds_read_b64_tr_b16 v[120:121], v238 offset:12416\n"
        "ds_read_b64_tr_b16 v[122:123], v238 offset:15488\n"
        "v_mfma_f32_16x16x32_bf16 v[84:87], v[172:175], v[196:199], v[36:39]\n"
        "ds_read_b64_tr_b16 v[124:125], v239 offset:12448\n"
        "v_cvt_pk_bf16_f32 v140, v100, v101\n"
        "v_mfma_f32_16x16x32_bf16 v[84:87], v[188:191], v[204:207], v[84:87]\n"
        "s_waitcnt lgkmcnt(13)\n"
        "ds_read_b64_tr_b16 v[126:127], v239 offset:15520\n"
        "v_cvt_pk_bf16_f32 v141, v102, v103\n"
        "v_mfma_f32_16x16x32_bf16 v[96:99], a[72:75], a[236:239], 0\n"
        "s_waitcnt lgkmcnt(12)\n"
        "v_cvt_pk_bf16_f32 v148, v104, v105\n"
        "v_cvt_pk_bf16_f32 v149, v106, v107\n"
        "v_mfma_f32_16x16x32_bf16 v[96:99], a[76:79], a[240:243], v[96:99]\n"
        "s_waitcnt lgkmcnt(10)\n"
        "ds_write_b128 v236, v[220:223] offset:12288\n"
        "v_mfma_f32_16x16x32_bf16 v[96:99], a[80:83], a[244:247], v[96:99]\n"
        "s_waitcnt lgkmcnt(9)\n"
        "v_exp_f32 v76, v76\n"
        "v_mfma_f32_16x16x32_bf16 v[96:99], a[84:87], a[248:251], v[96:99]\n"
        "s_waitcnt lgkmcnt(7)\n"
        "v_exp_f32 v77, v77\n"
        "v_mfma_f32_16x16x32_bf16 v[96:99], a[88:91], a[216:219], v[96:99]\n"
        "s_waitcnt lgkmcnt(5)\n"
        "v_exp_f32 v78, v78\n"
        "v_mfma_f32_16x16x32_bf16 v[96:99], a[92:95], a[220:223], v[96:99]\n"
        "ds_read_b128 v[202:205], v252 offset:4096\n"
        "v_cvt_pk_bf16_f32 v68, v68, v69\n"
        "v_mfma_f32_16x16x32_bf16 v[100:103], a[116:119], a[220:223], 0\n"
        "ds_write_b128 v236, v[224:227] offset:12672\n"
        "v_mfma_f32_16x16x32_bf16 v[100:103], a[112:115], a[216:219], v[100:103]\n"
        "v_exp_f32 v79, v79\n"
        "v_mfma_f32_16x16x32_bf16 v[100:103], a[108:111], a[248:251], v[100:103]\n"
        "v_exp_f32 v80, v80\n"
        "v_mfma_f32_16x16x32_bf16 v[100:103], a[104:107], a[244:247], v[100:103]\n"
        "v_cvt_pk_bf16_f32 v212, v96, v97\n"
        "v_cvt_pk_bf16_f32 v213, v98, v99\n"
        "v_mfma_f32_16x16x32_bf16 v[100:103], a[100:103], a[240:243], v[100:103]\n"
        "v_exp_f32 v81, v81\n"
        "v_mfma_f32_16x16x32_bf16 v[100:103], a[96:99], a[236:239], v[100:103]\n"
        "ds_write_b128 v236, v[88:91] offset:13056\n"
        "v_mfma_f32_16x16x32_bf16 v[104:107], a[120:123], a[236:239], 0\n"
        "v_exp_f32 v82, v82\n"
        "v_mfma_f32_16x16x32_bf16 v[104:107], a[124:127], a[240:243], v[104:107]\n"
        "v_exp_f32 v83, v83\n"
        "v_mfma_f32_16x16x32_bf16 v[104:107], a[128:131], a[244:247], v[104:107]\n"
        "v_exp_f32 v84, v84\n"
        "v_mfma_f32_16x16x32_bf16 v[104:107], a[132:135], a[248:251], v[104:107]\n"
        "v_cvt_pk_bf16_f32 v214, v100, v101\n"
        "v_cvt_pk_bf16_f32 v215, v102, v103\n"
        "v_mfma_f32_16x16x32_bf16 v[104:107], a[136:139], a[216:219], v[104:107]\n"
        "v_exp_f32 v85, v85\n"
        "v_mfma_f32_16x16x32_bf16 v[104:107], a[140:143], a[220:223], v[104:107]\n"
        "v_cvt_pk_bf16_f32 v69, v70, v71\n"
        "v_cvt_pk_bf16_f32 v76, v76, v77\n"
        "v_mfma_f32_16x16x32_bf16 v[96:99], a[20:23], a[220:223], 0\n"
        "ds_write_b128 v236, v[92:95] offset:13440\n"
        "v_mfma_f32_16x16x32_bf16 v[96:99], a[16:19], a[216:219], v[96:99]\n"
        "v_exp_f32 v86, v86\n"
        "v_mfma_f32_16x16x32_bf16 v[96:99], a[12:15], a[248:251], v[96:99]\n"
        "v_exp_f32 v87, v87\n"
        "v_mfma_f32_16x16x32_bf16 v[96:99], a[8:11], a[244:247], v[96:99]\n"
        "v_cvt_pk_bf16_f32 v230, v104, v105\n"
        "v_cvt_pk_bf16_f32 v231, v106, v107\n"
        "v_mfma_f32_16x16x32_bf16 v[96:99], a[4:7], a[240:243], v[96:99]\n"
        "s_cmp_eq_u32 s37, 100663296\n"
        "v_cvt_pk_bf16_f32 v77, v78, v79\n"
        "v_cvt_pk_bf16_f32 v80, v80, v81\n"
        "v_mfma_f32_16x16x32_bf16 v[96:99], a[0:3], a[236:239], v[96:99]\n"
        "s_cselect_b32 s46, 0, s37\n"
        "s_waitcnt lgkmcnt(7)\n"
        "ds_write_b128 v237, v[120:123] offset:12288\n"
        "v_mfma_f32_16x16x32_bf16 v[108:111], v[212:215], v[156:159], v[12:15]\n"
        "s_add_u32 s38, s46, s43\n"
        "v_cvt_pk_bf16_f32 v81, v82, v83\n"
        "v_cvt_pk_bf16_f32 v82, v84, v85\n"
        "v_mfma_f32_16x16x32_bf16 v[108:111], v[228:231], v[164:167], v[108:111]\n"
        "s_add_u32 s39, s46, s44\n"
        "v_mul_f32 v96, s50, v96\n"
        "v_mul_f32 v97, s50, v97\n"
        "v_mfma_f32_16x16x32_bf16 v[112:115], v[212:215], v[176:179], v[28:31]\n"
        "s_add_u32 s37, s37, 6291456\n"
        "v_mul_f32 v98, s50, v98\n"
        "v_mul_f32 v99, s50, v99\n"
        "v_mfma_f32_16x16x32_bf16 v[112:115], v[228:231], v[184:187], v[112:115]\n"
        "v_cvt_pk_bf16_f32 v216, v96, v97\n"
        "v_cvt_pk_bf16_f32 v217, v98, v99\n"
        "v_mfma_f32_16x16x32_bf16 v[116:119], v[212:215], v[196:199], v[44:47]\n"
        "s_waitcnt lgkmcnt(4)\n"
        "s_waitcnt lgkmcnt(6)\n"
        "ds_write_b128 v237, v[124:127] offset:13056\n"
        "v_mfma_f32_16x16x32_bf16 v[116:119], v[228:231], v[204:207], v[116:119]\n"
        "v_exp_f32 v108, v108\n"
        "v_mfma_f32_16x16x32_bf16 v[100:103], a[24:27], a[236:239], 0\n"
        "v_exp_f32 v109, v109\n"
        "v_mfma_f32_16x16x32_bf16 v[100:103], a[28:31], a[240:243], v[100:103]\n"
        "v_exp_f32 v110, v110\n"
        "v_mfma_f32_16x16x32_bf16 v[100:103], a[32:35], a[244:247], v[100:103]\n"
        "ds_read_b128 v[222:225], v251 offset:4608\n"
        "v_cvt_pk_bf16_f32 v83, v86, v87\n"
        "v_mfma_f32_16x16x32_bf16 v[100:103], a[36:39], a[248:251], v[100:103]\n"
        "v_exp_f32 v111, v111\n"
        "v_mfma_f32_16x16x32_bf16 v[100:103], a[40:43], a[216:219], v[100:103]\n"
        "v_add_u32 v240, s45, v240\n"
        "v_add_u32 v241, s45, v241\n"
        "v_mfma_f32_16x16x32_bf16 v[100:103], a[44:47], a[220:223], v[100:103]\n"
        "v_exp_f32 v112, v112\n"
        "v_mfma_f32_16x16x32_bf16 v[84:87], v[192:195], v[196:199], v[40:43]\n"
        "v_exp_f32 v113, v113\n"
        "v_mfma_f32_16x16x32_bf16 v[84:87], v[208:211], v[200:203], v[84:87]\n"
        "v_exp_f32 v114, v114\n"
        "v_mfma_f32_16x16x32_bf16 v[104:107], a[68:71], a[220:223], 0\n"
        "v_exp_f32 v115, v115\n"
        "v_mfma_f32_16x16x32_bf16 v[104:107], a[64:67], a[216:219], v[104:107]\n"
        "v_mul_f32 v100, s50, v100\n"
        "v_mul_f32 v101, s50, v101\n"
        "v_mfma_f32_16x16x32_bf16 v[104:107], a[60:63], a[248:251], v[104:107]\n"
        "v_mul_f32 v102, s50, v102\n"
        "v_mul_f32 v103, s50, v103\n"
        "v_mfma_f32_16x16x32_bf16 v[104:107], a[56:59], a[244:247], v[104:107]\n"
        "v_cvt_pk_bf16_f32 v218, v100, v101\n"
        "v_cvt_pk_bf16_f32 v219, v102, v103\n"
        "v_mfma_f32_16x16x32_bf16 v[104:107], a[52:55], a[240:243], v[104:107]\n"
        "s_waitcnt lgkmcnt(1)\n"
        "s_barrier\n"
        "v_exp_f32 v116, v116\n"
        "v_mfma_f32_16x16x32_bf16 v[104:107], a[48:51], a[236:239], v[104:107]\n"
        "v_exp_f32 v117, v117\n"
        "v_mfma_f32_16x16x32_bf16 v[96:99], a[236:239], a[144:147], 0\n"
        "v_exp_f32 v118, v118\n"
        "v_mfma_f32_16x16x32_bf16 v[96:99], a[240:243], a[148:151], v[96:99]\n"
        "v_exp_f32 v119, v119\n"
        "v_mfma_f32_16x16x32_bf16 v[96:99], a[244:247], a[152:155], v[96:99]\n"
        "v_exp_f32 v84, v84\n"
        "v_mfma_f32_16x16x32_bf16 v[96:99], a[248:251], a[156:159], v[96:99]\n"
        "v_mul_f32 v104, s50, v104\n"
        "v_mul_f32 v105, s50, v105\n"
        "v_mfma_f32_16x16x32_bf16 v[96:99], a[216:219], a[160:163], v[96:99]\n"
        "v_mul_f32 v106, s50, v106\n"
        "v_mul_f32 v107, s50, v107\n"
        "v_mfma_f32_16x16x32_bf16 v[96:99], a[220:223], a[164:167], v[96:99]\n"
        "v_cvt_pk_bf16_f32 v220, v104, v105\n"
        "v_cvt_pk_bf16_f32 v221, v106, v107\n"
        "v_mfma_f32_16x16x32_bf16 v[88:91], v[152:155], v[216:219], v[48:51]\n"
        "s_waitcnt lgkmcnt(0)\n"
        "ds_read_b128 v[156:159], v247 offset:0\n"
        "v_mov_b64 v[226:227], v[220:221]\n"
        "v_mfma_f32_16x16x32_bf16 v[88:91], v[168:171], v[220:223], v[88:91]\n"
        "ds_read_b128 v[164:167], v247 offset:4096\n"
        "v_cvt_pk_bf16_f32 v70, v108, v109\n"
        "v_mfma_f32_16x16x32_bf16 v[92:95], v[172:175], v[216:219], v[52:55]\n"
        "s_add_u32 s47, s47, 6291456\n"
        "ds_read_b128 v[160:163], v247 offset:256\n"
        "v_cvt_pk_bf16_f32 v134, v96, v97\n"
        "v_mfma_f32_16x16x32_bf16 v[92:95], v[188:191], v[224:227], v[92:95]\n"
        "s_add_u32 s36, s36, 2097152\n"
        "ds_read_b128 v[222:225], v252 offset:4608\n"
        "v_cvt_pk_bf16_f32 v135, v98, v99\n"
        "v_mfma_f32_16x16x32_bf16 v[100:103], a[220:223], a[188:191], 0\n"
        "s_xor_b32 s48, s48, 8192\n"
        "ds_read_b128 v[152:155], v247 offset:4352\n"
        "v_cvt_pk_bf16_f32 v71, v110, v111\n"
        "v_mfma_f32_16x16x32_bf16 v[100:103], a[216:219], a[184:187], v[100:103]\n"
        "v_exp_f32 v85, v85\n"
        "v_mfma_f32_16x16x32_bf16 v[100:103], a[248:251], a[180:183], v[100:103]\n"
        "v_exp_f32 v86, v86\n"
        "v_mfma_f32_16x16x32_bf16 v[100:103], a[244:247], a[176:179], v[100:103]\n"
        "v_exp_f32 v87, v87\n"
        "v_mfma_f32_16x16x32_bf16 v[100:103], a[240:243], a[172:175], v[100:103]\n"
        "v_exp_f32 v88, v88\n"
        "v_mfma_f32_16x16x32_bf16 v[100:103], a[236:239], a[168:171], v[100:103]\n"
        "v_exp_f32 v89, v89\n"
        "v_mfma_f32_16x16x32_bf16 v[104:107], a[236:239], a[192:195], 0\n"
        "ds_read_b128 a[236:239], v248 offset:0\n"
        "v_cvt_pk_bf16_f32 v78, v112, v113\n"
        "v_mfma_f32_16x16x32_bf16 v[104:107], a[240:243], a[196:199], v[104:107]\n"
        "ds_read_b128 a[240:243], v248 offset:6144\n"
        "v_cvt_pk_bf16_f32 v79, v114, v115\n"
        "v_mfma_f32_16x16x32_bf16 v[104:107], a[244:247], a[200:203], v[104:107]\n"
        "ds_read_b128 a[244:247], v254 offset:16\n"
        "v_cvt_pk_bf16_f32 v142, v100, v101\n"
        "v_mfma_f32_16x16x32_bf16 v[104:107], a[248:251], a[204:207], v[104:107]\n"
        "ds_read_b128 a[248:251], v254 offset:6160\n"
        "v_cvt_pk_bf16_f32 v143, v102, v103\n"
        "v_mfma_f32_16x16x32_bf16 v[104:107], a[216:219], a[208:211], v[104:107]\n"
        "ds_read_b128 a[216:219], v248 offset:32\n"
        "v_cvt_pk_bf16_f32 v84, v84, v85\n"
        "v_mfma_f32_16x16x32_bf16 v[104:107], a[220:223], a[212:215], v[104:107]\n"
        "ds_read_b128 a[220:223], v248 offset:6176\n"
        "v_cvt_pk_bf16_f32 v85, v86, v87\n"
        "v_mfma_f32_16x16x32_bf16 v[108:111], v[192:195], v[216:219], v[56:59]\n"
        "s_waitcnt lgkmcnt(7)\n"
        "ds_read_b128 a[224:227], v254 offset:48\n"
        "v_cvt_pk_bf16_f32 v86, v116, v117\n"
        "v_mfma_f32_16x16x32_bf16 v[108:111], v[208:211], v[220:223], v[108:111]\n"
        "ds_read_b128 a[228:231], v254 offset:6192\n"
        "v_mov_b64 v[116:117], 0\n"
        "v_mfma_f32_16x16x32_bf16 v[112:115], v[212:215], v[216:219], v[60:63]\n"
        "v_cvt_pk_bf16_f32 v150, v104, v105\n"
        "v_cvt_pk_bf16_f32 v151, v106, v107\n"
        "v_mfma_f32_16x16x32_bf16 v[112:115], v[228:231], v[224:227], v[112:115]\n"
        "s_waitcnt lgkmcnt(7)\n"
        "ds_read_b128 a[232:235], v248 offset:64\n"
        "v_cvt_pk_bf16_f32 v87, v118, v119\n"
        "v_mfma_f32_16x16x32_bf16 v[172:175], v[156:159], a[236:239], 0\n"
        "s_waitcnt lgkmcnt(7)\n"
        "v_exp_f32 v90, v90\n"
        "v_mfma_f32_16x16x32_bf16 v[172:175], v[164:167], a[240:243], v[172:175]\n"
        "v_exp_f32 v91, v91\n"
        "v_mfma_f32_16x16x32_bf16 v[200:203], v[152:155], a[240:243], 0\n"
        "v_exp_f32 v92, v92\n"
        "v_mfma_f32_16x16x32_bf16 v[200:203], v[160:163], a[236:239], v[200:203]\n"
        "ds_read_b128 a[236:239], v248 offset:6208\n"
        "v_cvt_pk_bf16_f32 v88, v88, v89\n"
        "v_smfmac_f32_16x16x64_bf16 v[116:119], a[252:255], v[64:71], v255\n"
        "v_exp_f32 v93, v93\n"
        "v_mfma_f32_16x16x32_bf16 v[120:123], v[128:131], v[64:67], 0\n"
        "v_exp_f32 v94, v94\n"
        "v_mfma_f32_16x16x32_bf16 v[120:123], v[132:135], v[68:71], v[120:123]\n"
        "v_exp_f32 v95, v95\n"
        "v_mfma_f32_16x16x32_bf16 v[124:127], v[140:143], v[68:71], 0\n"
        "v_exp_f32 v108, v108\n"
        "v_mfma_f32_16x16x32_bf16 v[124:127], v[136:139], v[64:67], v[124:127]\n"
        "v_cvt_pk_bf16_f32 v89, v90, v91\n"
        "v_cvt_pk_bf16_f32 v90, v92, v93\n"
        "v_mfma_f32_16x16x32_bf16 v[96:99], v[144:147], v[64:67], 0\n"
        "v_exp_f32 v109, v109\n"
        "v_mfma_f32_16x16x32_bf16 v[96:99], v[148:151], v[68:71], v[96:99]\n"
        "s_waitcnt lgkmcnt(7)\n"
        "ds_read_b128 a[240:243], v254 offset:80\n"
        "v_cvt_pk_bf16_f32 v91, v94, v95\n"
        "v_mfma_f32_16x16x32_bf16 v[204:207], v[160:163], a[244:247], 0\n"
        "s_waitcnt lgkmcnt(7)\n"
        "v_exp_f32 v110, v110\n"
        "v_mfma_f32_16x16x32_bf16 v[204:207], v[152:155], a[248:251], v[204:207]\n"
        "v_exp_f32 v111, v111\n"
        "v_mfma_f32_16x16x32_bf16 v[176:179], v[164:167], a[248:251], 0\n"
        "v_exp_f32 v112, v112\n"
        "v_mfma_f32_16x16x32_bf16 v[176:179], v[156:159], a[244:247], v[176:179]\n"
        "s_waitcnt lgkmcnt(6)\n"
        "ds_read_b128 a[244:247], v254 offset:6224\n"
        "v_add_f32 v116, v116, v117\n"
        "v_mfma_f32_16x16x32_bf16 v[180:183], v[156:159], a[216:219], 0\n"
        "s_waitcnt lgkmcnt(6)\n"
        "ds_read_b128 a[248:251], v248 offset:36864\n"
        "v_mov_b64 v[100:101], 0\n"
        "v_mfma_f32_16x16x32_bf16 v[180:183], v[164:167], a[220:223], v[180:183]\n"
        "v_rcp_f32 v117, v116\n"
        "v_mfma_f32_16x16x32_bf16 v[212:215], v[152:155], a[220:223], 0\n"
        "ds_read_b128 v[232:235], v247 offset:8192\n"
        "v_cvt_pk_bf16_f32 v92, v108, v109\n"
        "v_mfma_f32_16x16x32_bf16 v[212:215], v[160:163], a[216:219], v[212:215]\n"
        "s_waitcnt lgkmcnt(7)\n"
        "v_mul_f32 v120, v117, v120\n"
        "v_mul_f32 v121, v117, v121\n"
        "v_mfma_f32_16x16x32_bf16 v[216:219], v[160:163], a[224:227], 0\n"
        "s_waitcnt lgkmcnt(6)\n"
        "v_mul_f32 v122, v117, v122\n"
        "v_mul_f32 v123, v117, v123\n"
        "v_mfma_f32_16x16x32_bf16 v[216:219], v[152:155], a[228:231], v[216:219]\n"
        "43:\n"
        "s_cbranch_execz 46f\n"
        "v_cvt_pk_bf16_f32 v93, v110, v111\n"
        "v_cvt_pk_bf16_f32 v120, v120, v121\n"
        "v_mfma_f32_16x16x32_bf16 v[184:187], v[164:167], a[228:231], 0\n"
        "ds_read_b128 v[64:67], v247 offset:12288\n"
        "v_cvt_pk_bf16_f32 v121, v122, v123\n"
        "v_mfma_f32_16x16x32_bf16 v[184:187], v[156:159], a[224:227], v[184:187]\n"
        "s_waitcnt lgkmcnt(6)\n"
        "v_exp_f32 v113, v113\n"
        "v_mfma_f32_16x16x32_bf16 v[192:195], v[156:159], a[232:235], 0\n"
        "s_waitcnt lgkmcnt(5)\n"
        "ds_write_b32 v245, v120 offset:0\n"
        "v_mfma_f32_16x16x32_bf16 v[192:195], v[164:167], a[236:239], v[192:195]\n"
        "v_exp_f32 v114, v114\n"
        "v_mfma_f32_16x16x32_bf16 v[220:223], v[152:155], a[236:239], 0\n"
        "ds_write_b32 v245, v121 offset:4\n"
        "v_mfma_f32_16x16x32_bf16 v[220:223], v[160:163], a[232:235], v[220:223]\n"
        "v_cvt_pk_bf16_f32 v94, v112, v113\n"
        "v_mul_f32 v124, v117, v124\n"
        "v_smfmac_f32_16x16x64_bf16 v[100:103], a[252:255], v[72:79], v255\n"
        "v_exp_f32 v115, v115\n"
        "v_mfma_f32_16x16x32_bf16 v[104:107], v[128:131], v[72:75], 0\n"
        "ds_read_b128 v[236:239], v247 offset:8448\n"
        "v_mul_f32 v125, v117, v125\n"
        "v_mfma_f32_16x16x32_bf16 v[104:107], v[132:135], v[76:79], v[104:107]\n"
        "ds_read_b128 v[68:71], v247 offset:12544\n"
        "ds_read_b128 a[216:219], v248 offset:43008\n"
        "v_mfma_f32_16x16x32_bf16 v[108:111], v[140:143], v[76:79], 0\n"
        "v_maximum3_f32 v253, v253, v116, v117\n"
        "v_mul_f32 v126, v117, v126\n"
        "v_mfma_f32_16x16x32_bf16 v[108:111], v[136:139], v[72:75], v[108:111]\n"
        "v_mul_f32 v127, v117, v127\n"
        "v_cvt_pk_bf16_f32 v95, v114, v115\n"
        "v_mfma_f32_16x16x32_bf16 v[112:115], v[144:147], v[72:75], 0\n"
        "ds_read_b128 a[220:223], v254 offset:36880\n"
        "v_cvt_pk_bf16_f32 v124, v124, v125\n"
        "v_mfma_f32_16x16x32_bf16 v[112:115], v[148:151], v[76:79], v[112:115]\n"
        "s_waitcnt lgkmcnt(10)\n"
        "ds_read_b128 a[224:227], v254 offset:43024\n"
        "v_cvt_pk_bf16_f32 v125, v126, v127\n"
        "v_mfma_f32_16x16x32_bf16 v[224:227], v[160:163], a[240:243], 0\n"
        "s_waitcnt lgkmcnt(10)\n"
        "ds_write_b32 v245, v124 offset:8\n"
        "v_mfma_f32_16x16x32_bf16 v[224:227], v[152:155], a[244:247], v[224:227]\n"
        "ds_write_b32 v245, v125 offset:12\n"
        "v_mfma_f32_16x16x32_bf16 v[196:199], v[164:167], a[244:247], 0\n"
        "ds_read_b128 a[228:231], v248 offset:36896\n"
        "v_mul_f32 v96, v117, v96\n"
        "v_mfma_f32_16x16x32_bf16 v[196:199], v[156:159], a[240:243], v[196:199]\n"
        "s_waitcnt lgkmcnt(11)\n"
        "ds_read_b128 a[232:235], v248 offset:43040\n"
        "v_mul_f32 v97, v117, v97\n"
        "v_mfma_f32_16x16x32_bf16 v[172:175], v[232:235], a[248:251], v[172:175]\n"
        "s_waitcnt lgkmcnt(6)\n"
        "v_mul_f32 v98, v117, v98\n"
        "v_mul_f32 v99, v117, v99\n"
        "v_mfma_f32_16x16x32_bf16 v[172:175], v[64:67], a[216:219], v[172:175]\n"
        "v_cvt_pk_bf16_f32 v96, v96, v97\n"
        "v_cvt_pk_bf16_f32 v97, v98, v99\n"
        "v_mfma_f32_16x16x32_bf16 v[200:203], v[68:71], a[216:219], v[200:203]\n"
        "ds_read_b128 a[236:239], v254 offset:36912\n"
        "v_add_f32 v100, v100, v101\n"
        "v_mfma_f32_16x16x32_bf16 v[200:203], v[236:239], a[248:251], v[200:203]\n"
        "s_waitcnt lgkmcnt(6)\n"
        "ds_read_b128 a[240:243], v254 offset:43056\n"
        "v_mfma_f32_16x16x32_bf16 v[204:207], v[236:239], a[220:223], v[204:207]\n"
        "s_waitcnt lgkmcnt(6)\n"
        "ds_write_b32 v246, v96 offset:0\n"
        "v_mfma_f32_16x16x32_bf16 v[204:207], v[68:71], a[224:227], v[204:207]\n"
        "ds_write_b32 v246, v97 offset:4\n"
        "v_mfma_f32_16x16x32_bf16 v[176:179], v[64:67], a[224:227], v[176:179]\n"
        "v_rcp_f32 v101, v100\n"
        "v_mfma_f32_16x16x32_bf16 v[176:179], v[232:235], a[220:223], v[176:179]\n"
        "s_waitcnt lgkmcnt(5)\n"
        "v_mul_f32 v104, v101, v104\n"
        "v_mul_f32 v105, v101, v105\n"
        "v_mfma_f32_16x16x32_bf16 v[180:183], v[232:235], a[228:231], v[180:183]\n"
        "s_waitcnt lgkmcnt(4)\n"
        "v_mov_b64 v[116:117], 0\n"
        "v_mul_f32 v106, v101, v106\n"
        "v_mfma_f32_16x16x32_bf16 v[180:183], v[64:67], a[232:235], v[180:183]\n"
        "v_mul_f32 v107, v101, v107\n"
        "v_cvt_pk_bf16_f32 v104, v104, v105\n"
        "v_mfma_f32_16x16x32_bf16 v[212:215], v[68:71], a[232:235], v[212:215]\n"
        "v_cvt_pk_bf16_f32 v105, v106, v107\n"
        "v_mul_f32 v108, v101, v108\n"
        "v_mfma_f32_16x16x32_bf16 v[212:215], v[236:239], a[228:231], v[212:215]\n"
        "s_waitcnt lgkmcnt(3)\n"
        "ds_write_b32 v245, v104 offset:256\n"
        "v_mfma_f32_16x16x32_bf16 v[216:219], v[236:239], a[236:239], v[216:219]\n"
        "s_waitcnt lgkmcnt(3)\n"
        "ds_write_b32 v245, v105 offset:260\n"
        "v_mfma_f32_16x16x32_bf16 v[216:219], v[68:71], a[240:243], v[216:219]\n"
        "ds_read_b128 a[244:247], v248 offset:36928\n"
        "v_mul_f32 v109, v101, v109\n"
        "v_mfma_f32_16x16x32_bf16 v[184:187], v[64:67], a[240:243], v[184:187]\n"
        "ds_read_b128 a[248:251], v248 offset:43072\n"
        "v_maximum3_f32 v253, v253, v100, v101\n"
        "v_mfma_f32_16x16x32_bf16 v[184:187], v[232:235], a[236:239], v[184:187]\n"
        "v_mul_f32 v110, v101, v110\n"
        "v_mul_f32 v111, v101, v111\n"
        "v_smfmac_f32_16x16x64_bf16 v[116:119], a[252:255], v[80:87], v255\n"
        "v_cvt_pk_bf16_f32 v108, v108, v109\n"
        "v_cvt_pk_bf16_f32 v109, v110, v111\n"
        "v_mfma_f32_16x16x32_bf16 v[120:123], v[128:131], v[80:83], 0\n"
        "ds_read_b128 a[216:219], v254 offset:36944\n"
        "v_mul_f32 v112, v101, v112\n"
        "v_mfma_f32_16x16x32_bf16 v[120:123], v[132:135], v[84:87], v[120:123]\n"
        "ds_read_b128 a[220:223], v254 offset:43088\n"
        "v_mul_f32 v113, v101, v113\n"
        "v_mfma_f32_16x16x32_bf16 v[124:127], v[140:143], v[84:87], 0\n"
        "ds_read_b128 v[156:159], v247 offset:16384\n"
        "v_mul_f32 v114, v101, v114\n"
        "v_mfma_f32_16x16x32_bf16 v[124:127], v[136:139], v[80:83], v[124:127]\n"
        "ds_read_b128 v[164:167], v247 offset:20480\n"
        "v_mul_f32 v115, v101, v115\n"
        "v_mfma_f32_16x16x32_bf16 v[96:99], v[144:147], v[80:83], 0\n"
        "ds_read_b128 v[160:163], v247 offset:16640\n"
        "ds_read_b128 a[224:227], v248 offset:12288\n"
        "v_mfma_f32_16x16x32_bf16 v[96:99], v[148:151], v[84:87], v[96:99]\n"
        "s_waitcnt lgkmcnt(7)\n"
        "ds_read_b128 v[152:155], v247 offset:20736\n"
        "ds_read_b128 a[228:231], v248 offset:49152\n"
        "v_mfma_f32_16x16x32_bf16 v[192:195], v[232:235], a[244:247], v[192:195]\n"
        "s_waitcnt lgkmcnt(8)\n"
        "ds_write_b32 v245, v108 offset:264\n"
        "v_mfma_f32_16x16x32_bf16 v[192:195], v[64:67], a[248:251], v[192:195]\n"
        "ds_write_b32 v245, v109 offset:268\n"
        "v_mfma_f32_16x16x32_bf16 v[220:223], v[68:71], a[248:251], v[220:223]\n"
        "ds_read_b128 a[232:235], v254 offset:12304\n"
        "v_cvt_pk_bf16_f32 v112, v112, v113\n"
        "v_mfma_f32_16x16x32_bf16 v[220:223], v[236:239], a[244:247], v[220:223]\n"
        "s_waitcnt lgkmcnt(10)\n"
        "ds_read_b128 a[236:239], v254 offset:49168\n"
        "v_cvt_pk_bf16_f32 v113, v114, v115\n"
        "v_mfma_f32_16x16x32_bf16 v[224:227], v[236:239], a[216:219], v[224:227]\n"
        "s_waitcnt lgkmcnt(10)\n"
        "ds_write_b32 v246, v112 offset:256\n"
        "v_mfma_f32_16x16x32_bf16 v[224:227], v[68:71], a[220:223], v[224:227]\n"
        "ds_write_b32 v246, v113 offset:260\n"
        "v_mfma_f32_16x16x32_bf16 v[196:199], v[64:67], a[220:223], v[196:199]\n"
        "ds_read_b128 a[240:243], v248 offset:12320\n"
        "v_add_f32 v116, v116, v117\n"
        "v_mfma_f32_16x16x32_bf16 v[196:199], v[232:235], a[216:219], v[196:199]\n"
        "s_waitcnt lgkmcnt(9)\n"
        "ds_read_b128 a[244:247], v248 offset:49184\n"
        "v_add_u32 v247, s31, v247\n"
        "v_mfma_f32_16x16x32_bf16 v[172:175], v[156:159], a[224:227], v[172:175]\n"
        "s_waitcnt lgkmcnt(8)\n"
        "v_rcp_f32 v117, v116\n"
        "v_mfma_f32_16x16x32_bf16 v[172:175], v[164:167], a[228:231], v[172:175]\n"
        "v_mul_f32 v120, v117, v120\n"
        "v_mul_f32 v121, v117, v121\n"
        "v_mfma_f32_16x16x32_bf16 v[200:203], v[152:155], a[228:231], v[200:203]\n"
        "ds_read_b128 a[248:251], v254 offset:12336\n"
        "v_mul_f32 v122, v117, v122\n"
        "v_mfma_f32_16x16x32_bf16 v[200:203], v[160:163], a[224:227], v[200:203]\n"
        "s_waitcnt lgkmcnt(6)\n"
        "ds_read_b128 a[216:219], v254 offset:49200\n"
        "v_mul_f32 v123, v117, v123\n"
        "v_mfma_f32_16x16x32_bf16 v[204:207], v[160:163], a[232:235], v[204:207]\n"
        "s_waitcnt lgkmcnt(6)\n"
        "v_cvt_pk_bf16_f32 v120, v120, v121\n"
        "v_cvt_pk_bf16_f32 v121, v122, v123\n"
        "v_mfma_f32_16x16x32_bf16 v[204:207], v[152:155], a[236:239], v[204:207]\n"
        "ds_write_b32 v245, v120 offset:512\n"
        "v_mfma_f32_16x16x32_bf16 v[176:179], v[164:167], a[236:239], v[176:179]\n"
        "ds_read_b128 a[220:223], v248 offset:12352\n"
        "v_mul_f32 v124, v117, v124\n"
        "v_mfma_f32_16x16x32_bf16 v[176:179], v[156:159], a[232:235], v[176:179]\n"
        "s_waitcnt lgkmcnt(5)\n"
        "ds_read_b128 a[224:227], v248 offset:49216\n"
        "v_mul_f32 v125, v117, v125\n"
        "v_mfma_f32_16x16x32_bf16 v[180:183], v[156:159], a[240:243], v[180:183]\n"
        "s_waitcnt lgkmcnt(5)\n"
        "ds_write_b32 v245, v121 offset:516\n"
        "v_mfma_f32_16x16x32_bf16 v[180:183], v[164:167], a[244:247], v[180:183]\n"
        "v_maximum3_f32 v253, v253, v116, v117\n"
        "v_mul_f32 v126, v117, v126\n"
        "v_mfma_f32_16x16x32_bf16 v[212:215], v[152:155], a[244:247], v[212:215]\n"
        "ds_read_b128 a[228:231], v254 offset:12368\n"
        "v_mul_f32 v127, v117, v127\n"
        "v_mfma_f32_16x16x32_bf16 v[212:215], v[160:163], a[240:243], v[212:215]\n"
        "s_waitcnt lgkmcnt(6)\n"
        "ds_read_b128 a[232:235], v254 offset:49232\n"
        "v_cvt_pk_bf16_f32 v124, v124, v125\n"
        "v_mfma_f32_16x16x32_bf16 v[216:219], v[160:163], a[248:251], v[216:219]\n"
        "s_waitcnt lgkmcnt(6)\n"
        "v_cvt_pk_bf16_f32 v125, v126, v127\n"
        "v_mul_f32 v96, v117, v96\n"
        "v_mfma_f32_16x16x32_bf16 v[216:219], v[152:155], a[216:219], v[216:219]\n"
        "ds_write_b32 v245, v124 offset:520\n"
        "v_mfma_f32_16x16x32_bf16 v[184:187], v[164:167], a[216:219], v[184:187]\n"
        "ds_write_b32 v245, v125 offset:524\n"
        "v_mfma_f32_16x16x32_bf16 v[184:187], v[156:159], a[248:251], v[184:187]\n"
        "s_waitcnt lgkmcnt(6)\n"
        "s_waitcnt vmcnt(0)\n"
        "s_barrier\n"
        "v_mul_f32 v97, v117, v97\n"
        "v_mul_f32 v98, v117, v98\n"
        "v_mfma_f32_16x16x32_bf16 v[192:195], v[156:159], a[220:223], v[192:195]\n"
        "s_waitcnt lgkmcnt(5)\n"
        "v_mul_f32 v99, v117, v99\n"
        "v_cvt_pk_bf16_f32 v96, v96, v97\n"
        "v_mfma_f32_16x16x32_bf16 v[192:195], v[164:167], a[224:227], v[192:195]\n"
        "v_cvt_pk_bf16_f32 v97, v98, v99\n"
        "v_mov_b64 v[80:81], 0\n"
        "v_mfma_f32_16x16x32_bf16 v[220:223], v[152:155], a[224:227], v[220:223]\n"
        "ds_write_b32 v246, v96 offset:512\n"
        "v_mfma_f32_16x16x32_bf16 v[220:223], v[160:163], a[220:223], v[220:223]\n"
        "s_waitcnt lgkmcnt(4)\n"
        "ds_write_b32 v246, v97 offset:516\n"
        "v_mfma_f32_16x16x32_bf16 v[224:227], v[160:163], a[228:231], v[224:227]\n"
        "s_waitcnt lgkmcnt(4)\n"
        "ds_read_b128 a[236:239], v240 offset:0\n"
        "v_mfma_f32_16x16x32_bf16 v[224:227], v[152:155], a[232:235], v[224:227]\n"
        "ds_read_b128 a[240:243], v241 offset:0\n"
        "v_mfma_f32_16x16x32_bf16 v[196:199], v[164:167], a[232:235], v[196:199]\n"
        "ds_read_b128 a[244:247], v240 offset:128\n"
        "v_mfma_f32_16x16x32_bf16 v[196:199], v[156:159], a[228:231], v[196:199]\n"
        "ds_read_b128 a[248:251], v241 offset:128\n"
        "v_smfmac_f32_16x16x64_bf16 v[80:83], a[252:255], v[88:95], v255\n"
        "ds_read_b128 a[216:219], v240 offset:256\n"
        "v_mfma_f32_16x16x32_bf16 v[84:87], v[128:131], v[88:91], 0\n"
        "ds_read_b128 a[220:223], v241 offset:256\n"
        "v_mfma_f32_16x16x32_bf16 v[84:87], v[132:135], v[92:95], v[84:87]\n"
        "v_mfma_f32_16x16x32_bf16 v[232:235], v[140:143], v[92:95], 0\n"
        "v_mfma_f32_16x16x32_bf16 v[232:235], v[136:139], v[88:91], v[232:235]\n"
        "v_mfma_f32_16x16x32_bf16 v[236:239], v[144:147], v[88:91], 0\n"
        "v_mfma_f32_16x16x32_bf16 v[236:239], v[148:151], v[92:95], v[236:239]\n"
        "s_waitcnt lgkmcnt(0)\n"
        ".p2align 6\n"
        "10:\n"
        "s_waitcnt lgkmcnt(5)\n"
        "v_mfma_f32_16x16x32_bf16 v[96:99], a[72:75], a[236:239], 0\n"
        "s_waitcnt lgkmcnt(4)\n"
        "v_and_b32 v117, 60, v250\n"
        "v_add3_u32 v117, v250, v117, s26\n"
        "v_mfma_f32_16x16x32_bf16 v[96:99], a[76:79], a[240:243], v[96:99]\n"
        "s_waitcnt lgkmcnt(3)\n"
        "v_cvt_pk_bf16_f32 v108, v172, v176\n"
        "v_cvt_pk_bf16_f32 v109, v180, v184\n"
        "v_mfma_f32_16x16x32_bf16 v[96:99], a[80:83], a[244:247], v[96:99]\n"
        "s_waitcnt lgkmcnt(2)\n"
        "v_cvt_pk_bf16_f32 v110, v192, v196\n"
        "v_add_f32 v80, v80, v81\n"
        "v_mfma_f32_16x16x32_bf16 v[96:99], a[84:87], a[248:251], v[96:99]\n"
        "s_waitcnt lgkmcnt(1)\n"
        "buffer_store_dwordx2 v[108:109], v117, s[16:19], s38 offen offset:0 sc1\n"
        "v_mfma_f32_16x16x32_bf16 v[96:99], a[88:91], a[216:219], v[96:99]\n"
        "s_waitcnt lgkmcnt(0)\n"
        "v_rcp_f32 v81, v80\n"
        "v_mfma_f32_16x16x32_bf16 v[96:99], a[92:95], a[220:223], v[96:99]\n"
        "s_add_u32 s41, s29, s40\n"
        "v_cvt_pk_bf16_f32 v112, v173, v177\n"
        "v_cvt_pk_bf16_f32 v113, v181, v185\n"
        "v_mfma_f32_16x16x32_bf16 v[100:103], a[116:119], a[220:223], 0\n"
        "s_add_u32 s35, s47, 786432\n"
        "v_mul_f32 v84, v81, v84\n"
        "v_mul_f32 v85, v81, v85\n"
        "v_mfma_f32_16x16x32_bf16 v[100:103], a[112:115], a[216:219], v[100:103]\n"
        "s_sub_u32 s46, s35, 100663296\n"
        "v_cvt_pk_bf16_f32 v152, v96, v97\n"
        "v_cvt_pk_bf16_f32 v153, v98, v99\n"
        "v_mfma_f32_16x16x32_bf16 v[100:103], a[108:111], a[248:251], v[100:103]\n"
        "s_cmp_lt_u32 s46, 786432\n"
        "buffer_store_dword v110, v250, s[16:19], s38 offen offset:0 sc1\n"
        "v_mfma_f32_16x16x32_bf16 v[100:103], a[104:107], a[244:247], v[100:103]\n"
        "s_cselect_b32 s35, s46, s35\n"
        "v_cvt_pk_bf16_f32 v114, v193, v197\n"
        "v_mul_f32 v86, v81, v86\n"
        "v_mfma_f32_16x16x32_bf16 v[100:103], a[100:103], a[240:243], v[100:103]\n"
        "s_sub_u32 s31, s28, s30\n"
        "v_mul_f32 v87, v81, v87\n"
        "v_cvt_pk_bf16_f32 v84, v84, v85\n"
        "v_mfma_f32_16x16x32_bf16 v[100:103], a[96:99], a[236:239], v[100:103]\n"
        "s_sub_u32 s45, s29, s28\n"
        "ds_write_b32 v245, v84 offset:768\n"
        "v_mfma_f32_16x16x32_bf16 v[104:107], a[120:123], a[236:239], 0\n"
        "s_mov_b32 s29, s30\n"
        "v_cvt_pk_bf16_f32 v85, v86, v87\n"
        "v_mul_f32 v232, v81, v232\n"
        "v_mfma_f32_16x16x32_bf16 v[104:107], a[124:127], a[240:243], v[104:107]\n"
        "s_mov_b32 s30, s28\n"
        "buffer_store_dwordx2 v[112:113], v117, s[16:19], s38 offen offset:384 sc1\n"
        "v_mfma_f32_16x16x32_bf16 v[104:107], a[128:131], a[244:247], v[104:107]\n"
        "s_add_u32 s28, s28, s45\n"
        "v_cvt_pk_bf16_f32 v154, v100, v101\n"
        "v_cvt_pk_bf16_f32 v155, v102, v103\n"
        "v_mfma_f32_16x16x32_bf16 v[104:107], a[132:135], a[248:251], v[104:107]\n"
        "ds_write_b32 v245, v85 offset:772\n"
        "v_mfma_f32_16x16x32_bf16 v[104:107], a[136:139], a[216:219], v[104:107]\n"
        "v_cvt_pk_bf16_f32 v108, v174, v178\n"
        "v_cvt_pk_bf16_f32 v109, v182, v186\n"
        "v_mfma_f32_16x16x32_bf16 v[104:107], a[140:143], a[220:223], v[104:107]\n"
        "v_mul_f32 v233, v81, v233\n"
        "v_maximum3_f32 v253, v253, v80, v81\n"
        "v_mfma_f32_16x16x32_bf16 v[96:99], a[20:23], a[220:223], 0\n"
        "buffer_store_dword v114, v250, s[16:19], s38 offen offset:384 sc1\n"
        "v_mfma_f32_16x16x32_bf16 v[96:99], a[16:19], a[216:219], v[96:99]\n"
        "v_cvt_pk_bf16_f32 v110, v194, v198\n"
        "v_mul_f32 v234, v81, v234\n"
        "v_mfma_f32_16x16x32_bf16 v[96:99], a[12:15], a[248:251], v[96:99]\n"
        "v_cvt_pk_bf16_f32 v168, v104, v105\n"
        "v_cvt_pk_bf16_f32 v169, v106, v107\n"
        "v_mfma_f32_16x16x32_bf16 v[96:99], a[8:11], a[244:247], v[96:99]\n"
        "v_mul_f32 v235, v81, v235\n"
        "v_cvt_pk_bf16_f32 v232, v232, v233\n"
        "v_mfma_f32_16x16x32_bf16 v[96:99], a[4:7], a[240:243], v[96:99]\n"
        "ds_write_b32 v245, v232 offset:776\n"
        "v_mfma_f32_16x16x32_bf16 v[96:99], a[0:3], a[236:239], v[96:99]\n"
        "buffer_store_dwordx2 v[108:109], v117, s[16:19], s38 offen offset:768 sc1\n"
        "v_mfma_f32_16x16x32_bf16 v[100:103], a[24:27], a[236:239], 0\n"
        "v_cvt_pk_bf16_f32 v112, v175, v179\n"
        "v_cvt_pk_bf16_f32 v113, v183, v187\n"
        "v_mfma_f32_16x16x32_bf16 v[100:103], a[28:31], a[240:243], v[100:103]\n"
        "v_cvt_pk_bf16_f32 v233, v234, v235\n"
        "v_xor_b32 v251, 0x2000, v251\n"
        "v_mfma_f32_16x16x32_bf16 v[100:103], a[32:35], a[244:247], v[100:103]\n"
        "v_mul_f32 v96, s50, v96\n"
        "v_mul_f32 v97, s50, v97\n"
        "v_mfma_f32_16x16x32_bf16 v[100:103], a[36:39], a[248:251], v[100:103]\n"
        "v_mul_f32 v98, s50, v98\n"
        "v_mul_f32 v99, s50, v99\n"
        "v_mfma_f32_16x16x32_bf16 v[100:103], a[40:43], a[216:219], v[100:103]\n"
        "v_cvt_pk_bf16_f32 v156, v96, v97\n"
        "v_cvt_pk_bf16_f32 v157, v98, v99\n"
        "v_mfma_f32_16x16x32_bf16 v[100:103], a[44:47], a[220:223], v[100:103]\n"
        "buffer_store_dword v110, v250, s[16:19], s38 offen offset:768 sc1\n"
        "v_mfma_f32_16x16x32_bf16 v[104:107], a[68:71], a[220:223], 0\n"
        "ds_write_b32 v245, v233 offset:780\n"
        "v_mfma_f32_16x16x32_bf16 v[104:107], a[64:67], a[216:219], v[104:107]\n"
        "v_cvt_pk_bf16_f32 v114, v195, v199\n"
        "v_xor_b32 v252, 0x2000, v252\n"
        "v_mfma_f32_16x16x32_bf16 v[104:107], a[60:63], a[248:251], v[104:107]\n"
        "ds_read_b128 a[224:227], v240 offset:1536\n"
        "v_mul_f32 v100, s50, v100\n"
        "v_mfma_f32_16x16x32_bf16 v[104:107], a[56:59], a[244:247], v[104:107]\n"
        "v_mul_f32 v101, s50, v101\n"
        "v_mul_f32 v102, s50, v102\n"
        "v_mfma_f32_16x16x32_bf16 v[104:107], a[52:55], a[240:243], v[104:107]\n"
        "v_mul_f32 v103, s50, v103\n"
        "v_cvt_pk_bf16_f32 v158, v100, v101\n"
        "v_mfma_f32_16x16x32_bf16 v[104:107], a[48:51], a[236:239], v[104:107]\n"
        "buffer_store_dwordx2 v[112:113], v117, s[16:19], s38 offen offset:1152 sc1\n"
        "v_mfma_f32_16x16x32_bf16 v[96:99], a[236:239], a[144:147], 0\n"
        "ds_read_b128 a[228:231], v241 offset:1536\n"
        "v_cvt_pk_bf16_f32 v159, v102, v103\n"
        "v_mfma_f32_16x16x32_bf16 v[96:99], a[240:243], a[148:151], v[96:99]\n"
        "ds_read_b128 a[232:235], v240 offset:1664\n"
        "v_cvt_pk_bf16_f32 v108, v200, v204\n"
        "v_mfma_f32_16x16x32_bf16 v[96:99], a[244:247], a[152:155], v[96:99]\n"
        "v_mul_f32 v104, s50, v104\n"
        "v_mul_f32 v105, s50, v105\n"
        "v_mfma_f32_16x16x32_bf16 v[96:99], a[248:251], a[156:159], v[96:99]\n"
        "buffer_store_dword v114, v250, s[16:19], s38 offen offset:1152 sc1\n"
        "v_mfma_f32_16x16x32_bf16 v[96:99], a[216:219], a[160:163], v[96:99]\n"
        "v_mul_f32 v106, s50, v106\n"
        "v_mul_f32 v107, s50, v107\n"
        "v_mfma_f32_16x16x32_bf16 v[96:99], a[220:223], a[164:167], v[96:99]\n"
        "v_cvt_pk_bf16_f32 v160, v104, v105\n"
        "v_cvt_pk_bf16_f32 v161, v106, v107\n"
        "v_mfma_f32_16x16x32_bf16 v[100:103], a[220:223], a[188:191], 0\n"
        "v_mov_b64 v[166:167], v[160:161]\n"
        "v_cvt_pk_bf16_f32 v109, v212, v216\n"
        "v_mfma_f32_16x16x32_bf16 v[100:103], a[216:219], a[184:187], v[100:103]\n"
        "buffer_store_dwordx2 v[108:109], v117, s[16:19], s39 offen offset:0 sc1\n"
        "v_mfma_f32_16x16x32_bf16 v[100:103], a[248:251], a[180:183], v[100:103]\n"
        "v_cvt_pk_bf16_f32 v128, v96, v97\n"
        "v_cvt_pk_bf16_f32 v129, v98, v99\n"
        "v_mfma_f32_16x16x32_bf16 v[100:103], a[244:247], a[176:179], v[100:103]\n"
        "v_cvt_pk_bf16_f32 v110, v220, v224\n"
        "v_cvt_pk_bf16_f32 v112, v201, v205\n"
        "v_mfma_f32_16x16x32_bf16 v[100:103], a[240:243], a[172:175], v[100:103]\n"
        "v_cvt_pk_bf16_f32 v113, v213, v217\n"
        "v_mul_f32 v236, v81, v236\n"
        "v_mfma_f32_16x16x32_bf16 v[100:103], a[236:239], a[168:171], v[100:103]\n"
        "v_mul_f32 v237, v81, v237\n"
        "v_mul_f32 v238, v81, v238\n"
        "v_mfma_f32_16x16x32_bf16 v[104:107], a[236:239], a[192:195], 0\n"
        "ds_read_b128 a[236:239], v241 offset:1664\n"
        "v_mul_f32 v239, v81, v239\n"
        "v_mfma_f32_16x16x32_bf16 v[104:107], a[240:243], a[196:199], v[104:107]\n"
        "ds_read_b128 a[240:243], v240 offset:1792\n"
        "v_cvt_pk_bf16_f32 v236, v236, v237\n"
        "v_mfma_f32_16x16x32_bf16 v[104:107], a[244:247], a[200:203], v[104:107]\n"
        "ds_read_b128 a[244:247], v241 offset:1792\n"
        "v_cvt_pk_bf16_f32 v136, v100, v101\n"
        "v_mfma_f32_16x16x32_bf16 v[104:107], a[248:251], a[204:207], v[104:107]\n"
        "buffer_store_dword v110, v250, s[16:19], s39 offen offset:0 sc1\n"
        "v_mfma_f32_16x16x32_bf16 v[104:107], a[216:219], a[208:211], v[104:107]\n"
        "v_cvt_pk_bf16_f32 v137, v102, v103\n"
        "v_cvt_pk_bf16_f32 v237, v238, v239\n"
        "v_mfma_f32_16x16x32_bf16 v[104:107], a[220:223], a[212:215], v[104:107]\n"
        "s_waitcnt lgkmcnt(5)\n"
        "buffer_store_dwordx2 v[112:113], v117, s[16:19], s39 offen offset:384 sc1\n"
        "v_mfma_f32_16x16x32_bf16 v[96:99], a[72:75], a[224:227], 0\n"
        "s_waitcnt lgkmcnt(4)\n"
        "ds_write_b32 v246, v236 offset:768\n"
        "v_mfma_f32_16x16x32_bf16 v[96:99], a[76:79], a[228:231], v[96:99]\n"
        "s_waitcnt lgkmcnt(4)\n"
        "ds_write_b32 v246, v237 offset:772\n"
        "v_mfma_f32_16x16x32_bf16 v[96:99], a[80:83], a[232:235], v[96:99]\n"
        "s_waitcnt lgkmcnt(4)\n"
        "ds_read_b128 v[162:165], v251 offset:0\n"
        "v_cvt_pk_bf16_f32 v114, v221, v225\n"
        "v_mfma_f32_16x16x32_bf16 v[96:99], a[84:87], a[236:239], v[96:99]\n"
        "s_waitcnt lgkmcnt(4)\n"
        "v_cvt_pk_bf16_f32 v144, v104, v105\n"
        "v_cvt_pk_bf16_f32 v145, v106, v107\n"
        "v_mfma_f32_16x16x32_bf16 v[96:99], a[88:91], a[240:243], v[96:99]\n"
        "s_waitcnt lgkmcnt(3)\n"
        "buffer_store_dword v114, v250, s[16:19], s39 offen offset:384 sc1\n"
        "v_mfma_f32_16x16x32_bf16 v[96:99], a[92:95], a[244:247], v[96:99]\n"
        "v_cvt_pk_bf16_f32 v108, v202, v206\n"
        "v_cvt_pk_bf16_f32 v109, v214, v218\n"
        "v_mfma_f32_16x16x32_bf16 v[100:103], a[116:119], a[244:247], 0\n"
        "v_cvt_pk_bf16_f32 v110, v222, v226\n"
        "v_add_u32 v245, s31, v245\n"
        "v_mfma_f32_16x16x32_bf16 v[100:103], a[112:115], a[240:243], v[100:103]\n"
        "v_add_u32 v246, s31, v246\n"
        "v_mfma_f32_16x16x32_bf16 v[100:103], a[108:111], a[236:239], v[100:103]\n"
        "v_cvt_pk_bf16_f32 v172, v96, v97\n"
        "v_cvt_pk_bf16_f32 v173, v98, v99\n"
        "v_mfma_f32_16x16x32_bf16 v[100:103], a[104:107], a[232:235], v[100:103]\n"
        "buffer_store_dwordx2 v[108:109], v117, s[16:19], s39 offen offset:768 sc1\n"
        "v_mfma_f32_16x16x32_bf16 v[100:103], a[100:103], a[228:231], v[100:103]\n"
        "v_cvt_pk_bf16_f32 v112, v203, v207\n"
        "v_cvt_pk_bf16_f32 v113, v215, v219\n"
        "v_mfma_f32_16x16x32_bf16 v[100:103], a[96:99], a[224:227], v[100:103]\n"
        "s_nop 2\n"
        "v_mfma_f32_16x16x32_bf16 v[104:107], a[120:123], a[224:227], 0\n"
        "s_nop 2\n"
        "v_mfma_f32_16x16x32_bf16 v[104:107], a[124:127], a[228:231], v[104:107]\n"
        "v_cvt_pk_bf16_f32 v174, v100, v101\n"
        "v_cvt_pk_bf16_f32 v175, v102, v103\n"
        "v_mfma_f32_16x16x32_bf16 v[104:107], a[128:131], a[232:235], v[104:107]\n"
        "buffer_store_dword v110, v250, s[16:19], s39 offen offset:768 sc1\n"
        "v_mfma_f32_16x16x32_bf16 v[104:107], a[132:135], a[236:239], v[104:107]\n"
        "v_cvt_pk_bf16_f32 v114, v223, v227\n"
        "v_mfma_f32_16x16x32_bf16 v[104:107], a[136:139], a[240:243], v[104:107]\n"
        "v_mfma_f32_16x16x32_bf16 v[104:107], a[140:143], a[244:247], v[104:107]\n"
        "s_nop 2\n"
        "v_mfma_f32_16x16x32_bf16 v[96:99], a[20:23], a[244:247], 0\n"
        "s_nop 2\n"
        "v_mfma_f32_16x16x32_bf16 v[96:99], a[16:19], a[240:243], v[96:99]\n"
        "buffer_store_dwordx2 v[112:113], v117, s[16:19], s39 offen offset:1152 sc1\n"
        "v_mfma_f32_16x16x32_bf16 v[96:99], a[12:15], a[236:239], v[96:99]\n"
        "v_cvt_pk_bf16_f32 v190, v104, v105\n"
        "v_cvt_pk_bf16_f32 v191, v106, v107\n"
        "v_mfma_f32_16x16x32_bf16 v[96:99], a[8:11], a[232:235], v[96:99]\n"
        "v_mfma_f32_16x16x32_bf16 v[96:99], a[4:7], a[228:231], v[96:99]\n"
        "ds_read_b128 v[182:185], v251 offset:512\n"
        "v_mfma_f32_16x16x32_bf16 v[96:99], a[0:3], a[224:227], v[96:99]\n"
        "s_nop 2\n"
        "v_mfma_f32_16x16x32_bf16 v[64:67], v[152:155], v[156:159], v[0:3]\n"
        "s_waitcnt lgkmcnt(1)\n"
        "buffer_store_dword v114, v250, s[16:19], s39 offen offset:1152 sc1\n"
        "v_mfma_f32_16x16x32_bf16 v[64:67], v[168:171], v[160:163], v[64:67]\n"
        "s_nop 0\n"
        "v_mfma_f32_16x16x32_bf16 v[68:71], v[172:175], v[156:159], v[4:7]\n"
        "v_mul_f32 v96, s50, v96\n"
        "v_mul_f32 v97, s50, v97\n"
        "v_mfma_f32_16x16x32_bf16 v[68:71], v[188:191], v[164:167], v[68:71]\n"
        "v_mul_f32 v98, s50, v98\n"
        "v_mul_f32 v99, s50, v99\n"
        "v_mfma_f32_16x16x32_bf16 v[100:103], a[24:27], a[224:227], 0\n"
        "v_cvt_pk_bf16_f32 v176, v96, v97\n"
        "v_cvt_pk_bf16_f32 v177, v98, v99\n"
        "v_mfma_f32_16x16x32_bf16 v[100:103], a[28:31], a[228:231], v[100:103]\n"
        "v_exp_f32 v64, v64\n"
        "v_mfma_f32_16x16x32_bf16 v[100:103], a[32:35], a[232:235], v[100:103]\n"
        "s_add_u32 m0, s41, 0\n"
        "v_exp_f32 v65, v65\n"
        "v_mfma_f32_16x16x32_bf16 v[100:103], a[36:39], a[236:239], v[100:103]\n"
        "v_exp_f32 v66, v66\n"
        "v_mfma_f32_16x16x32_bf16 v[100:103], a[40:43], a[240:243], v[100:103]\n"
        "buffer_load_dwordx4 v242, s[8:11], s47 offen lds\n"
        "v_mfma_f32_16x16x32_bf16 v[100:103], a[44:47], a[244:247], v[100:103]\n"
        "v_exp_f32 v67, v67\n"
        "v_mfma_f32_16x16x32_bf16 v[104:107], a[68:71], a[244:247], 0\n"
        "v_exp_f32 v68, v68\n"
        "v_mfma_f32_16x16x32_bf16 v[104:107], a[64:67], a[240:243], v[104:107]\n"
        "s_add_u32 m0, s41, 1024\n"
        "v_exp_f32 v69, v69\n"
        "v_mfma_f32_16x16x32_bf16 v[104:107], a[60:63], a[236:239], v[104:107]\n"
        "v_exp_f32 v70, v70\n"
        "v_mfma_f32_16x16x32_bf16 v[104:107], a[56:59], a[232:235], v[104:107]\n"
        "buffer_load_dwordx4 v243, s[8:11], s47 offen lds\n"
        "v_mfma_f32_16x16x32_bf16 v[104:107], a[52:55], a[228:231], v[104:107]\n"
        "v_mul_f32 v100, s50, v100\n"
        "v_mul_f32 v101, s50, v101\n"
        "v_mfma_f32_16x16x32_bf16 v[104:107], a[48:51], a[224:227], v[104:107]\n"
        "v_mul_f32 v102, s50, v102\n"
        "v_mul_f32 v103, s50, v103\n"
        "v_mfma_f32_16x16x32_bf16 v[96:99], a[224:227], a[144:147], 0\n"
        "s_add_u32 m0, s41, 2048\n"
        "v_cvt_pk_bf16_f32 v178, v100, v101\n"
        "v_cvt_pk_bf16_f32 v179, v102, v103\n"
        "v_mfma_f32_16x16x32_bf16 v[96:99], a[228:231], a[148:151], v[96:99]\n"
        "ds_read_b128 a[248:251], v240 offset:12288\n"
        "v_mul_f32 v104, s50, v104\n"
        "v_mfma_f32_16x16x32_bf16 v[96:99], a[232:235], a[152:155], v[96:99]\n"
        "buffer_load_dwordx4 v244, s[8:11], s47 offen lds\n"
        "v_mfma_f32_16x16x32_bf16 v[96:99], a[236:239], a[156:159], v[96:99]\n"
        "v_mul_f32 v105, s50, v105\n"
        "v_mul_f32 v106, s50, v106\n"
        "v_mfma_f32_16x16x32_bf16 v[96:99], a[240:243], a[160:163], v[96:99]\n"
        "v_mul_f32 v107, s50, v107\n"
        "v_cvt_pk_bf16_f32 v180, v104, v105\n"
        "v_mfma_f32_16x16x32_bf16 v[96:99], a[244:247], a[164:167], v[96:99]\n"
        "s_add_u32 m0, s41, 12288\n"
        "ds_read_b128 a[216:219], v241 offset:12288\n"
        "v_cvt_pk_bf16_f32 v181, v106, v107\n"
        "v_mfma_f32_16x16x32_bf16 v[72:75], v[152:155], v[176:179], v[16:19]\n"
        "s_waitcnt lgkmcnt(2)\n"
        "ds_read_b128 a[220:223], v240 offset:12416\n"
        "v_mov_b64 v[186:187], v[180:181]\n"
        "v_mfma_f32_16x16x32_bf16 v[72:75], v[168:171], v[180:183], v[72:75]\n"
        "buffer_load_dwordx4 v242, s[8:11], s35 offen lds\n"
        "v_mfma_f32_16x16x32_bf16 v[76:79], v[172:175], v[176:179], v[20:23]\n"
        "v_cvt_pk_bf16_f32 v130, v96, v97\n"
        "v_cvt_pk_bf16_f32 v131, v98, v99\n"
        "v_mfma_f32_16x16x32_bf16 v[76:79], v[188:191], v[184:187], v[76:79]\n"
        "v_exp_f32 v71, v71\n"
        "v_mfma_f32_16x16x32_bf16 v[100:103], a[244:247], a[188:191], 0\n"
        "s_add_u32 m0, s41, 13312\n"
        "v_exp_f32 v72, v72\n"
        "v_mfma_f32_16x16x32_bf16 v[100:103], a[240:243], a[184:187], v[100:103]\n"
        "v_exp_f32 v73, v73\n"
        "v_mfma_f32_16x16x32_bf16 v[100:103], a[236:239], a[180:183], v[100:103]\n"
        "buffer_load_dwordx4 v243, s[8:11], s35 offen lds\n"
        "v_mfma_f32_16x16x32_bf16 v[100:103], a[232:235], a[176:179], v[100:103]\n"
        "v_exp_f32 v74, v74\n"
        "v_mfma_f32_16x16x32_bf16 v[100:103], a[228:231], a[172:175], v[100:103]\n"
        "v_exp_f32 v75, v75\n"
        "v_mfma_f32_16x16x32_bf16 v[100:103], a[224:227], a[168:171], v[100:103]\n"
        "s_add_u32 m0, s41, 14336\n"
        "v_exp_f32 v76, v76\n"
        "v_mfma_f32_16x16x32_bf16 v[104:107], a[224:227], a[192:195], 0\n"
        "ds_read_b128 a[224:227], v241 offset:12416\n"
        "v_cvt_pk_bf16_f32 v64, v64, v65\n"
        "v_mfma_f32_16x16x32_bf16 v[104:107], a[228:231], a[196:199], v[104:107]\n"
        "ds_read_b128 a[228:231], v240 offset:12544\n"
        "v_cvt_pk_bf16_f32 v65, v66, v67\n"
        "v_mfma_f32_16x16x32_bf16 v[104:107], a[232:235], a[200:203], v[104:107]\n"
        "ds_read_b128 a[232:235], v241 offset:12544\n"
        "v_cvt_pk_bf16_f32 v138, v100, v101\n"
        "v_mfma_f32_16x16x32_bf16 v[104:107], a[236:239], a[204:207], v[104:107]\n"
        "buffer_load_dwordx4 v244, s[8:11], s35 offen lds\n"
        "v_mfma_f32_16x16x32_bf16 v[104:107], a[240:243], a[208:211], v[104:107]\n"
        "s_mov_b32 m0, s48\n"
        "v_cvt_pk_bf16_f32 v139, v102, v103\n"
        "v_cvt_pk_bf16_f32 v66, v68, v69\n"
        "v_mfma_f32_16x16x32_bf16 v[104:107], a[244:247], a[212:215], v[104:107]\n"
        "s_waitcnt lgkmcnt(5)\n"
        "v_exp_f32 v77, v77\n"
        "v_mfma_f32_16x16x32_bf16 v[96:99], a[72:75], a[248:251], 0\n"
        "s_waitcnt lgkmcnt(4)\n"
        "buffer_load_dwordx4 v249, s[12:15], s36 offen offset:0 lds\n"
        "v_mfma_f32_16x16x32_bf16 v[96:99], a[76:79], a[216:219], v[96:99]\n"
        "s_waitcnt lgkmcnt(3)\n"
        "ds_read_b128 v[162:165], v252 offset:0\n"
        "v_xor_b32 v116, 32, v249\n"
        "v_mfma_f32_16x16x32_bf16 v[96:99], a[80:83], a[220:223], v[96:99]\n"
        "s_waitcnt lgkmcnt(3)\n"
        "ds_read_b128 v[182:185], v252 offset:512\n"
        "v_cvt_pk_bf16_f32 v146, v104, v105\n"
        "v_mfma_f32_16x16x32_bf16 v[96:99], a[84:87], a[224:227], v[96:99]\n"
        "s_waitcnt lgkmcnt(3)\n"
        "v_cvt_pk_bf16_f32 v147, v106, v107\n"
        "v_cvt_pk_bf16_f32 v67, v70, v71\n"
        "v_mfma_f32_16x16x32_bf16 v[96:99], a[88:91], a[228:231], v[96:99]\n"
        "s_waitcnt lgkmcnt(2)\n"
        "v_exp_f32 v78, v78\n"
        "v_mfma_f32_16x16x32_bf16 v[96:99], a[92:95], a[232:235], v[96:99]\n"
        "buffer_load_dwordx4 v116, s[12:15], s36 offen offset:1024 lds\n"
        "v_mfma_f32_16x16x32_bf16 v[100:103], a[116:119], a[232:235], 0\n"
        "v_exp_f32 v79, v79\n"
        "v_mfma_f32_16x16x32_bf16 v[100:103], a[112:115], a[228:231], v[100:103]\n"
        "v_cvt_pk_bf16_f32 v72, v72, v73\n"
        "v_cvt_pk_bf16_f32 v73, v74, v75\n"
        "v_mfma_f32_16x16x32_bf16 v[100:103], a[108:111], a[224:227], v[100:103]\n"
        "v_cvt_pk_bf16_f32 v74, v76, v77\n"
        "v_cvt_pk_bf16_f32 v192, v96, v97\n"
        "v_mfma_f32_16x16x32_bf16 v[100:103], a[104:107], a[220:223], v[100:103]\n"
        "v_cvt_pk_bf16_f32 v193, v98, v99\n"
        "v_cvt_pk_bf16_f32 v75, v78, v79\n"
        "v_mfma_f32_16x16x32_bf16 v[100:103], a[100:103], a[216:219], v[100:103]\n"
        "v_mfma_f32_16x16x32_bf16 v[100:103], a[96:99], a[248:251], v[100:103]\n"
        "s_nop 2\n"
        "v_mfma_f32_16x16x32_bf16 v[104:107], a[120:123], a[248:251], 0\n"
        "s_nop 2\n"
        "v_mfma_f32_16x16x32_bf16 v[104:107], a[124:127], a[216:219], v[104:107]\n"
        "v_cvt_pk_bf16_f32 v194, v100, v101\n"
        "v_cvt_pk_bf16_f32 v195, v102, v103\n"
        "v_mfma_f32_16x16x32_bf16 v[104:107], a[128:131], a[220:223], v[104:107]\n"
        "44:\n"
        "s_cbranch_execz 40b\n"
        "v_mfma_f32_16x16x32_bf16 v[104:107], a[132:135], a[224:227], v[104:107]\n"
        "v_mfma_f32_16x16x32_bf16 v[104:107], a[136:139], a[228:231], v[104:107]\n"
        "v_mfma_f32_16x16x32_bf16 v[104:107], a[140:143], a[232:235], v[104:107]\n"
        "s_nop 2\n"
        "v_mfma_f32_16x16x32_bf16 v[96:99], a[20:23], a[232:235], 0\n"
        "s_nop 2\n"
        "v_mfma_f32_16x16x32_bf16 v[96:99], a[16:19], a[228:231], v[96:99]\n"
        "v_cvt_pk_bf16_f32 v208, v104, v105\n"
        "v_cvt_pk_bf16_f32 v209, v106, v107\n"
        "v_mfma_f32_16x16x32_bf16 v[96:99], a[12:15], a[224:227], v[96:99]\n"
        "v_mfma_f32_16x16x32_bf16 v[96:99], a[8:11], a[220:223], v[96:99]\n"
        "v_mfma_f32_16x16x32_bf16 v[96:99], a[4:7], a[216:219], v[96:99]\n"
        "ds_read_b128 v[202:205], v251 offset:4096\n"
        "v_mfma_f32_16x16x32_bf16 v[96:99], a[0:3], a[248:251], v[96:99]\n"
        "s_nop 2\n"
        "v_mfma_f32_16x16x32_bf16 v[68:71], v[192:195], v[156:159], v[8:11]\n"
        "s_waitcnt lgkmcnt(2)\n"
        "s_nop 1\n"
        "v_mfma_f32_16x16x32_bf16 v[68:71], v[208:211], v[160:163], v[68:71]\n"
        "v_mfma_f32_16x16x32_bf16 v[76:79], v[192:195], v[176:179], v[24:27]\n"
        "s_waitcnt lgkmcnt(1)\n"
        "v_mul_f32 v96, s50, v96\n"
        "v_mul_f32 v97, s50, v97\n"
        "v_mfma_f32_16x16x32_bf16 v[76:79], v[208:211], v[180:183], v[76:79]\n"
        "v_mul_f32 v98, s50, v98\n"
        "v_mul_f32 v99, s50, v99\n"
        "v_mfma_f32_16x16x32_bf16 v[100:103], a[24:27], a[248:251], 0\n"
        "v_cvt_pk_bf16_f32 v196, v96, v97\n"
        "v_cvt_pk_bf16_f32 v197, v98, v99\n"
        "v_mfma_f32_16x16x32_bf16 v[100:103], a[28:31], a[216:219], v[100:103]\n"
        "v_exp_f32 v68, v68\n"
        "v_mfma_f32_16x16x32_bf16 v[100:103], a[32:35], a[220:223], v[100:103]\n"
        "v_exp_f32 v69, v69\n"
        "v_mfma_f32_16x16x32_bf16 v[100:103], a[36:39], a[224:227], v[100:103]\n"
        "v_exp_f32 v70, v70\n"
        "v_mfma_f32_16x16x32_bf16 v[100:103], a[40:43], a[228:231], v[100:103]\n"
        "v_exp_f32 v71, v71\n"
        "v_mfma_f32_16x16x32_bf16 v[100:103], a[44:47], a[232:235], v[100:103]\n"
        "v_exp_f32 v76, v76\n"
        "v_mfma_f32_16x16x32_bf16 v[104:107], a[68:71], a[232:235], 0\n"
        "v_exp_f32 v77, v77\n"
        "v_mfma_f32_16x16x32_bf16 v[104:107], a[64:67], a[228:231], v[104:107]\n"
        "v_exp_f32 v78, v78\n"
        "v_mfma_f32_16x16x32_bf16 v[104:107], a[60:63], a[224:227], v[104:107]\n"
        "v_exp_f32 v79, v79\n"
        "v_mfma_f32_16x16x32_bf16 v[104:107], a[56:59], a[220:223], v[104:107]\n"
        "v_mul_f32 v100, s50, v100\n"
        "v_mul_f32 v101, s50, v101\n"
        "v_mfma_f32_16x16x32_bf16 v[104:107], a[52:55], a[216:219], v[104:107]\n"
        "v_mul_f32 v102, s50, v102\n"
        "v_mul_f32 v103, s50, v103\n"
        "v_mfma_f32_16x16x32_bf16 v[104:107], a[48:51], a[248:251], v[104:107]\n"
        "v_cvt_pk_bf16_f32 v198, v100, v101\n"
        "v_cvt_pk_bf16_f32 v199, v102, v103\n"
        "v_mfma_f32_16x16x32_bf16 v[96:99], a[248:251], a[144:147], 0\n"
        "ds_read_b128 a[236:239], v240 offset:13824\n"
        "v_cvt_pk_bf16_f32 v68, v68, v69\n"
        "v_mfma_f32_16x16x32_bf16 v[96:99], a[216:219], a[148:151], v[96:99]\n"
        "ds_read_b128 a[240:243], v241 offset:13824\n"
        "v_cvt_pk_bf16_f32 v69, v70, v71\n"
        "v_mfma_f32_16x16x32_bf16 v[96:99], a[220:223], a[152:155], v[96:99]\n"
        "v_mul_f32 v104, s50, v104\n"
        "v_mul_f32 v105, s50, v105\n"
        "v_mfma_f32_16x16x32_bf16 v[96:99], a[224:227], a[156:159], v[96:99]\n"
        "v_mul_f32 v106, s50, v106\n"
        "v_mul_f32 v107, s50, v107\n"
        "v_mfma_f32_16x16x32_bf16 v[96:99], a[228:231], a[160:163], v[96:99]\n"
        "v_cvt_pk_bf16_f32 v200, v104, v105\n"
        "v_cvt_pk_bf16_f32 v201, v106, v107\n"
        "v_mfma_f32_16x16x32_bf16 v[96:99], a[232:235], a[164:167], v[96:99]\n"
        "ds_read_b128 a[244:247], v240 offset:13952\n"
        "v_mov_b64 v[206:207], v[200:201]\n"
        "v_mfma_f32_16x16x32_bf16 v[80:83], v[152:155], v[196:199], v[32:35]\n"
        "s_waitcnt lgkmcnt(3)\n"
        "v_cvt_pk_bf16_f32 v76, v76, v77\n"
        "v_cvt_pk_bf16_f32 v77, v78, v79\n"
        "v_mfma_f32_16x16x32_bf16 v[80:83], v[168:171], v[200:203], v[80:83]\n"
        "s_nop 0\n"
        "v_mfma_f32_16x16x32_bf16 v[84:87], v[172:175], v[196:199], v[36:39]\n"
        "v_cvt_pk_bf16_f32 v132, v96, v97\n"
        "v_cvt_pk_bf16_f32 v133, v98, v99\n"
        "v_mfma_f32_16x16x32_bf16 v[84:87], v[188:191], v[204:207], v[84:87]\n"
        "ds_read_b128 v[202:205], v252 offset:4096\n"
        "v_mfma_f32_16x16x32_bf16 v[100:103], a[232:235], a[188:191], 0\n"
        "v_mfma_f32_16x16x32_bf16 v[100:103], a[228:231], a[184:187], v[100:103]\n"
        "v_exp_f32 v80, v80\n"
        "v_mfma_f32_16x16x32_bf16 v[100:103], a[224:227], a[180:183], v[100:103]\n"
        "v_exp_f32 v81, v81\n"
        "v_mfma_f32_16x16x32_bf16 v[100:103], a[220:223], a[176:179], v[100:103]\n"
        "v_exp_f32 v82, v82\n"
        "v_mfma_f32_16x16x32_bf16 v[100:103], a[216:219], a[172:175], v[100:103]\n"
        "v_exp_f32 v83, v83\n"
        "v_mfma_f32_16x16x32_bf16 v[100:103], a[248:251], a[168:171], v[100:103]\n"
        "v_exp_f32 v84, v84\n"
        "v_mfma_f32_16x16x32_bf16 v[104:107], a[248:251], a[192:195], 0\n"
        "ds_read_b128 a[248:251], v241 offset:13952\n"
        "v_cvt_pk_bf16_f32 v80, v80, v81\n"
        "v_mfma_f32_16x16x32_bf16 v[104:107], a[216:219], a[196:199], v[104:107]\n"
        "ds_read_b128 a[216:219], v240 offset:14080\n"
        "v_cvt_pk_bf16_f32 v81, v82, v83\n"
        "v_mfma_f32_16x16x32_bf16 v[104:107], a[220:223], a[200:203], v[104:107]\n"
        "ds_read_b128 a[220:223], v241 offset:14080\n"
        "v_cvt_pk_bf16_f32 v140, v100, v101\n"
        "v_mfma_f32_16x16x32_bf16 v[104:107], a[224:227], a[204:207], v[104:107]\n"
        "v_cvt_pk_bf16_f32 v141, v102, v103\n"
        "v_mfma_f32_16x16x32_bf16 v[104:107], a[228:231], a[208:211], v[104:107]\n"
        "v_exp_f32 v85, v85\n"
        "v_mfma_f32_16x16x32_bf16 v[104:107], a[232:235], a[212:215], v[104:107]\n"
        "s_waitcnt lgkmcnt(6)\n"
        "v_exp_f32 v86, v86\n"
        "v_mfma_f32_16x16x32_bf16 v[96:99], a[72:75], a[236:239], 0\n"
        "s_waitcnt lgkmcnt(5)\n"
        "v_cvt_pk_bf16_f32 v82, v84, v85\n"
        "v_mfma_f32_16x16x32_bf16 v[96:99], a[76:79], a[240:243], v[96:99]\n"
        "s_waitcnt lgkmcnt(4)\n"
        "v_exp_f32 v87, v87\n"
        "v_mfma_f32_16x16x32_bf16 v[96:99], a[80:83], a[244:247], v[96:99]\n"
        "s_waitcnt lgkmcnt(2)\n"
        "v_cvt_pk_bf16_f32 v83, v86, v87\n"
        "v_mfma_f32_16x16x32_bf16 v[96:99], a[84:87], a[248:251], v[96:99]\n"
        "s_waitcnt lgkmcnt(1)\n"
        "v_cvt_pk_bf16_f32 v148, v104, v105\n"
        "v_cvt_pk_bf16_f32 v149, v106, v107\n"
        "v_mfma_f32_16x16x32_bf16 v[96:99], a[88:91], a[216:219], v[96:99]\n"
        "s_waitcnt lgkmcnt(0)\n"
        "v_mfma_f32_16x16x32_bf16 v[96:99], a[92:95], a[220:223], v[96:99]\n"
        "s_nop 2\n"
        "v_mfma_f32_16x16x32_bf16 v[84:87], v[192:195], v[196:199], v[40:43]\n"
        "s_nop 1\n"
        "v_mfma_f32_16x16x32_bf16 v[84:87], v[208:211], v[200:203], v[84:87]\n"
        "v_mfma_f32_16x16x32_bf16 v[100:103], a[116:119], a[220:223], 0\n"
        "v_cvt_pk_bf16_f32 v212, v96, v97\n"
        "v_cvt_pk_bf16_f32 v213, v98, v99\n"
        "v_mfma_f32_16x16x32_bf16 v[100:103], a[112:115], a[216:219], v[100:103]\n"
        "v_add_u32 v240, s45, v240\n"
        "v_add_u32 v241, s45, v241\n"
        "v_mfma_f32_16x16x32_bf16 v[100:103], a[108:111], a[248:251], v[100:103]\n"
        "v_mfma_f32_16x16x32_bf16 v[100:103], a[104:107], a[244:247], v[100:103]\n"
        "v_exp_f32 v84, v84\n"
        "v_mfma_f32_16x16x32_bf16 v[100:103], a[100:103], a[240:243], v[100:103]\n"
        "v_exp_f32 v85, v85\n"
        "v_mfma_f32_16x16x32_bf16 v[100:103], a[96:99], a[236:239], v[100:103]\n"
        "v_exp_f32 v86, v86\n"
        "v_mfma_f32_16x16x32_bf16 v[104:107], a[120:123], a[236:239], 0\n"
        "v_exp_f32 v87, v87\n"
        "v_mfma_f32_16x16x32_bf16 v[104:107], a[124:127], a[240:243], v[104:107]\n"
        "v_cvt_pk_bf16_f32 v84, v84, v85\n"
        "v_cvt_pk_bf16_f32 v85, v86, v87\n"
        "v_mfma_f32_16x16x32_bf16 v[104:107], a[128:131], a[244:247], v[104:107]\n"
        "v_mfma_f32_16x16x32_bf16 v[104:107], a[132:135], a[248:251], v[104:107]\n"
        "v_cvt_pk_bf16_f32 v214, v100, v101\n"
        "v_cvt_pk_bf16_f32 v215, v102, v103\n"
        "v_mfma_f32_16x16x32_bf16 v[104:107], a[136:139], a[216:219], v[104:107]\n"
        "v_mfma_f32_16x16x32_bf16 v[104:107], a[140:143], a[220:223], v[104:107]\n"
        "s_nop 2\n"
        "v_mfma_f32_16x16x32_bf16 v[96:99], a[20:23], a[220:223], 0\n"
        "s_nop 2\n"
        "v_mfma_f32_16x16x32_bf16 v[96:99], a[16:19], a[216:219], v[96:99]\n"
        "v_cvt_pk_bf16_f32 v230, v104, v105\n"
        "v_cvt_pk_bf16_f32 v231, v106, v107\n"
        "v_mfma_f32_16x16x32_bf16 v[96:99], a[12:15], a[248:251], v[96:99]\n"
        "s_barrier\n"
        "v_mfma_f32_16x16x32_bf16 v[96:99], a[8:11], a[244:247], v[96:99]\n"
        "s_cmp_eq_u32 s37, 100663296\n"
        "v_mfma_f32_16x16x32_bf16 v[96:99], a[4:7], a[240:243], v[96:99]\n"
        "s_cselect_b32 s46, 0, s37\n"
        "v_mfma_f32_16x16x32_bf16 v[96:99], a[0:3], a[236:239], v[96:99]\n"
        "s_add_u32 s38, s46, s43\n"
        "s_nop 1\n"
        "v_mfma_f32_16x16x32_bf16 v[108:111], v[212:215], v[156:159], v[12:15]\n"
        "s_add_u32 s39, s46, s44\n"
        "ds_read_b128 v[222:225], v251 offset:4608\n"
        "v_mfma_f32_16x16x32_bf16 v[108:111], v[228:231], v[164:167], v[108:111]\n"
        "s_add_u32 s37, s37, 6291456\n"
        "v_mul_f32 v96, s50, v96\n"
        "v_mul_f32 v97, s50, v97\n"
        "v_mfma_f32_16x16x32_bf16 v[112:115], v[212:215], v[176:179], v[28:31]\n"
        "v_mul_f32 v98, s50, v98\n"
        "v_mul_f32 v99, s50, v99\n"
        "v_mfma_f32_16x16x32_bf16 v[112:115], v[228:231], v[184:187], v[112:115]\n"
        "v_cvt_pk_bf16_f32 v216, v96, v97\n"
        "v_cvt_pk_bf16_f32 v217, v98, v99\n"
        "v_mfma_f32_16x16x32_bf16 v[116:119], v[212:215], v[196:199], v[44:47]\n"
        "v_exp_f32 v108, v108\n"
        "v_mfma_f32_16x16x32_bf16 v[116:119], v[228:231], v[204:207], v[116:119]\n"
        "v_exp_f32 v109, v109\n"
        "v_mfma_f32_16x16x32_bf16 v[100:103], a[24:27], a[236:239], 0\n"
        "v_exp_f32 v110, v110\n"
        "v_mfma_f32_16x16x32_bf16 v[100:103], a[28:31], a[240:243], v[100:103]\n"
        "v_exp_f32 v111, v111\n"
        "v_mfma_f32_16x16x32_bf16 v[100:103], a[32:35], a[244:247], v[100:103]\n"
        "v_exp_f32 v112, v112\n"
        "v_mfma_f32_16x16x32_bf16 v[100:103], a[36:39], a[248:251], v[100:103]\n"
        "v_exp_f32 v113, v113\n"
        "v_mfma_f32_16x16x32_bf16 v[100:103], a[40:43], a[216:219], v[100:103]\n"
        "v_exp_f32 v114, v114\n"
        "v_mfma_f32_16x16x32_bf16 v[100:103], a[44:47], a[220:223], v[100:103]\n"
        "v_exp_f32 v115, v115\n"
        "v_mfma_f32_16x16x32_bf16 v[104:107], a[68:71], a[220:223], 0\n"
        "v_exp_f32 v116, v116\n"
        "v_mfma_f32_16x16x32_bf16 v[104:107], a[64:67], a[216:219], v[104:107]\n"
        "v_exp_f32 v117, v117\n"
        "v_mfma_f32_16x16x32_bf16 v[104:107], a[60:63], a[248:251], v[104:107]\n"
        "v_exp_f32 v118, v118\n"
        "v_mfma_f32_16x16x32_bf16 v[104:107], a[56:59], a[244:247], v[104:107]\n"
        "v_mul_f32 v100, s50, v100\n"
        "v_mul_f32 v101, s50, v101\n"
        "v_mfma_f32_16x16x32_bf16 v[104:107], a[52:55], a[240:243], v[104:107]\n"
        "v_mul_f32 v102, s50, v102\n"
        "v_mul_f32 v103, s50, v103\n"
        "v_mfma_f32_16x16x32_bf16 v[104:107], a[48:51], a[236:239], v[104:107]\n"
        "v_cvt_pk_bf16_f32 v218, v100, v101\n"
        "v_cvt_pk_bf16_f32 v219, v102, v103\n"
        "v_mfma_f32_16x16x32_bf16 v[96:99], a[236:239], a[144:147], 0\n"
        "v_exp_f32 v119, v119\n"
        "v_mfma_f32_16x16x32_bf16 v[96:99], a[240:243], a[148:151], v[96:99]\n"
        "v_cvt_pk_bf16_f32 v70, v108, v109\n"
        "v_cvt_pk_bf16_f32 v71, v110, v111\n"
        "v_mfma_f32_16x16x32_bf16 v[96:99], a[244:247], a[152:155], v[96:99]\n"
        "v_mul_f32 v104, s50, v104\n"
        "v_mul_f32 v105, s50, v105\n"
        "v_mfma_f32_16x16x32_bf16 v[96:99], a[248:251], a[156:159], v[96:99]\n"
        "v_mul_f32 v106, s50, v106\n"
        "v_mul_f32 v107, s50, v107\n"
        "v_mfma_f32_16x16x32_bf16 v[96:99], a[216:219], a[160:163], v[96:99]\n"
        "v_cvt_pk_bf16_f32 v220, v104, v105\n"
        "v_cvt_pk_bf16_f32 v221, v106, v107\n"
        "v_mfma_f32_16x16x32_bf16 v[96:99], a[220:223], a[164:167], v[96:99]\n"
        "ds_read_b128 v[156:159], v247 offset:0\n"
        "v_mov_b64 v[226:227], v[220:221]\n"
        "v_mfma_f32_16x16x32_bf16 v[88:91], v[152:155], v[216:219], v[48:51]\n"
        "s_waitcnt lgkmcnt(1)\n"
        "ds_read_b128 v[164:167], v247 offset:4096\n"
        "v_cvt_pk_bf16_f32 v78, v112, v113\n"
        "v_mfma_f32_16x16x32_bf16 v[88:91], v[168:171], v[220:223], v[88:91]\n"
        "ds_read_b128 v[160:163], v247 offset:256\n"
        "v_cvt_pk_bf16_f32 v79, v114, v115\n"
        "v_mfma_f32_16x16x32_bf16 v[92:95], v[172:175], v[216:219], v[52:55]\n"
        "s_add_u32 s47, s47, 6291456\n"
        "ds_read_b128 v[152:155], v247 offset:4352\n"
        "v_cvt_pk_bf16_f32 v134, v96, v97\n"
        "v_mfma_f32_16x16x32_bf16 v[92:95], v[188:191], v[224:227], v[92:95]\n"
        "s_add_u32 s36, s36, 2097152\n"
        "ds_read_b128 v[222:225], v252 offset:4608\n"
        "v_cvt_pk_bf16_f32 v135, v98, v99\n"
        "v_mfma_f32_16x16x32_bf16 v[100:103], a[220:223], a[188:191], 0\n"
        "s_xor_b32 s48, s48, 8192\n"
        "v_exp_f32 v88, v88\n"
        "v_mfma_f32_16x16x32_bf16 v[100:103], a[216:219], a[184:187], v[100:103]\n"
        "v_exp_f32 v89, v89\n"
        "v_mfma_f32_16x16x32_bf16 v[100:103], a[248:251], a[180:183], v[100:103]\n"
        "v_exp_f32 v90, v90\n"
        "v_mfma_f32_16x16x32_bf16 v[100:103], a[244:247], a[176:179], v[100:103]\n"
        "v_exp_f32 v91, v91\n"
        "v_mfma_f32_16x16x32_bf16 v[100:103], a[240:243], a[172:175], v[100:103]\n"
        "v_exp_f32 v92, v92\n"
        "v_mfma_f32_16x16x32_bf16 v[100:103], a[236:239], a[168:171], v[100:103]\n"
        "v_exp_f32 v93, v93\n"
        "v_mfma_f32_16x16x32_bf16 v[104:107], a[236:239], a[192:195], 0\n"
        "ds_read_b128 a[236:239], v248 offset:0\n"
        "v_cvt_pk_bf16_f32 v86, v116, v117\n"
        "v_mfma_f32_16x16x32_bf16 v[104:107], a[240:243], a[196:199], v[104:107]\n"
        "ds_read_b128 a[240:243], v248 offset:6144\n"
        "v_cvt_pk_bf16_f32 v87, v118, v119\n"
        "v_mfma_f32_16x16x32_bf16 v[104:107], a[244:247], a[200:203], v[104:107]\n"
        "ds_read_b128 a[244:247], v254 offset:16\n"
        "v_mov_b64 v[116:117], 0\n"
        "v_mfma_f32_16x16x32_bf16 v[104:107], a[248:251], a[204:207], v[104:107]\n"
        "v_cvt_pk_bf16_f32 v142, v100, v101\n"
        "v_cvt_pk_bf16_f32 v143, v102, v103\n"
        "v_mfma_f32_16x16x32_bf16 v[104:107], a[216:219], a[208:211], v[104:107]\n"
        "ds_read_b128 a[248:251], v254 offset:6160\n"
        "v_cvt_pk_bf16_f32 v88, v88, v89\n"
        "v_mfma_f32_16x16x32_bf16 v[104:107], a[220:223], a[212:215], v[104:107]\n"
        "ds_read_b128 a[216:219], v248 offset:32\n"
        "v_cvt_pk_bf16_f32 v89, v90, v91\n"
        "v_mfma_f32_16x16x32_bf16 v[108:111], v[192:195], v[216:219], v[56:59]\n"
        "s_waitcnt lgkmcnt(5)\n"
        "ds_read_b128 a[220:223], v248 offset:6176\n"
        "v_cvt_pk_bf16_f32 v90, v92, v93\n"
        "v_mfma_f32_16x16x32_bf16 v[108:111], v[208:211], v[220:223], v[108:111]\n"
        "v_exp_f32 v94, v94\n"
        "v_mfma_f32_16x16x32_bf16 v[112:115], v[212:215], v[216:219], v[60:63]\n"
        "ds_read_b128 a[224:227], v254 offset:48\n"
        "v_cvt_pk_bf16_f32 v150, v104, v105\n"
        "v_mfma_f32_16x16x32_bf16 v[112:115], v[228:231], v[224:227], v[112:115]\n"
        "s_waitcnt lgkmcnt(6)\n"
        "ds_read_b128 a[228:231], v254 offset:6192\n"
        "v_cvt_pk_bf16_f32 v151, v106, v107\n"
        "v_mfma_f32_16x16x32_bf16 v[172:175], v[156:159], a[236:239], 0\n"
        "s_waitcnt lgkmcnt(6)\n"
        "v_exp_f32 v95, v95\n"
        "v_mfma_f32_16x16x32_bf16 v[172:175], v[164:167], a[240:243], v[172:175]\n"
        "ds_read_b128 a[232:235], v248 offset:64\n"
        "v_cvt_pk_bf16_f32 v91, v94, v95\n"
        "v_mfma_f32_16x16x32_bf16 v[200:203], v[152:155], a[240:243], 0\n"
        "v_exp_f32 v108, v108\n"
        "v_mfma_f32_16x16x32_bf16 v[200:203], v[160:163], a[236:239], v[200:203]\n"
        "v_exp_f32 v109, v109\n"
        "v_smfmac_f32_16x16x64_bf16 v[116:119], a[252:255], v[64:71], v255\n"
        "ds_read_b128 a[236:239], v248 offset:6208\n"
        "v_cvt_pk_bf16_f32 v92, v108, v109\n"
        "v_mfma_f32_16x16x32_bf16 v[120:123], v[128:131], v[64:67], 0\n"
        "v_exp_f32 v110, v110\n"
        "v_mfma_f32_16x16x32_bf16 v[120:123], v[132:135], v[68:71], v[120:123]\n"
        "v_exp_f32 v111, v111\n"
        "v_mfma_f32_16x16x32_bf16 v[124:127], v[140:143], v[68:71], 0\n"
        "v_exp_f32 v112, v112\n"
        "v_mfma_f32_16x16x32_bf16 v[124:127], v[136:139], v[64:67], v[124:127]\n"
        "v_exp_f32 v113, v113\n"
        "v_mfma_f32_16x16x32_bf16 v[96:99], v[144:147], v[64:67], 0\n"
        "v_exp_f32 v114, v114\n"
        "v_mfma_f32_16x16x32_bf16 v[96:99], v[148:151], v[68:71], v[96:99]\n"
        "s_waitcnt lgkmcnt(7)\n"
        "ds_read_b128 a[240:243], v254 offset:80\n"
        "v_add_f32 v116, v116, v117\n"
        "v_mfma_f32_16x16x32_bf16 v[204:207], v[160:163], a[244:247], 0\n"
        "s_waitcnt lgkmcnt(7)\n"
        "v_rcp_f32 v117, v116\n"
        "v_mfma_f32_16x16x32_bf16 v[204:207], v[152:155], a[248:251], v[204:207]\n"
        "v_exp_f32 v115, v115\n"
        "v_mfma_f32_16x16x32_bf16 v[176:179], v[164:167], a[248:251], 0\n"
        "v_mul_f32 v120, v117, v120\n"
        "v_mul_f32 v121, v117, v121\n"
        "v_mfma_f32_16x16x32_bf16 v[176:179], v[156:159], a[244:247], v[176:179]\n"
        "s_waitcnt lgkmcnt(6)\n"
        "ds_read_b128 a[244:247], v254 offset:6224\n"
        "v_mul_f32 v122, v117, v122\n"
        "v_mfma_f32_16x16x32_bf16 v[180:183], v[156:159], a[216:219], 0\n"
        "s_waitcnt lgkmcnt(6)\n"
        "ds_read_b128 a[248:251], v248 offset:36864\n"
        "v_mov_b64 v[100:101], 0\n"
        "v_mfma_f32_16x16x32_bf16 v[180:183], v[164:167], a[220:223], v[180:183]\n"
        "ds_read_b128 v[232:235], v247 offset:8192\n"
        "v_mul_f32 v123, v117, v123\n"
        "v_mfma_f32_16x16x32_bf16 v[212:215], v[152:155], a[220:223], 0\n"
        "ds_read_b128 v[64:67], v247 offset:12288\n"
        "v_cvt_pk_bf16_f32 v93, v110, v111\n"
        "v_mfma_f32_16x16x32_bf16 v[212:215], v[160:163], a[216:219], v[212:215]\n"
        "s_waitcnt lgkmcnt(8)\n"
        "ds_read_b128 v[236:239], v247 offset:8448\n"
        "v_cvt_pk_bf16_f32 v120, v120, v121\n"
        "v_mfma_f32_16x16x32_bf16 v[216:219], v[160:163], a[224:227], 0\n"
        "s_waitcnt lgkmcnt(8)\n"
        "ds_read_b128 v[68:71], v247 offset:12544\n"
        "v_cvt_pk_bf16_f32 v121, v122, v123\n"
        "v_mfma_f32_16x16x32_bf16 v[216:219], v[152:155], a[228:231], v[216:219]\n"
        "ds_write_b32 v245, v120 offset:0\n"
        "v_mfma_f32_16x16x32_bf16 v[184:187], v[164:167], a[228:231], 0\n"
        "ds_write_b32 v245, v121 offset:4\n"
        "v_mfma_f32_16x16x32_bf16 v[184:187], v[156:159], a[224:227], v[184:187]\n"
        "s_waitcnt lgkmcnt(10)\n"
        "ds_read_b128 a[216:219], v248 offset:43008\n"
        "v_cvt_pk_bf16_f32 v94, v112, v113\n"
        "v_mfma_f32_16x16x32_bf16 v[192:195], v[156:159], a[232:235], 0\n"
        "s_waitcnt lgkmcnt(10)\n"
        "v_mul_f32 v124, v117, v124\n"
        "v_mul_f32 v125, v117, v125\n"
        "v_mfma_f32_16x16x32_bf16 v[192:195], v[164:167], a[236:239], v[192:195]\n"
        "v_maximum3_f32 v253, v253, v116, v117\n"
        "v_mul_f32 v126, v117, v126\n"
        "v_mfma_f32_16x16x32_bf16 v[220:223], v[152:155], a[236:239], 0\n"
        "v_mul_f32 v127, v117, v127\n"
        "v_cvt_pk_bf16_f32 v95, v114, v115\n"
        "v_mfma_f32_16x16x32_bf16 v[220:223], v[160:163], a[232:235], v[220:223]\n"
        "ds_read_b128 a[220:223], v254 offset:36880\n"
        "v_cvt_pk_bf16_f32 v124, v124, v125\n"
        "v_smfmac_f32_16x16x64_bf16 v[100:103], a[252:255], v[72:79], v255\n"
        "ds_read_b128 a[224:227], v254 offset:43024\n"
        "v_cvt_pk_bf16_f32 v125, v126, v127\n"
        "v_mfma_f32_16x16x32_bf16 v[104:107], v[128:131], v[72:75], 0\n"
        "ds_write_b32 v245, v124 offset:8\n"
        "v_mfma_f32_16x16x32_bf16 v[104:107], v[132:135], v[76:79], v[104:107]\n"
        "ds_write_b32 v245, v125 offset:12\n"
        "v_mfma_f32_16x16x32_bf16 v[108:111], v[140:143], v[76:79], 0\n"
        "v_mul_f32 v96, v117, v96\n"
        "v_mul_f32 v97, v117, v97\n"
        "v_mfma_f32_16x16x32_bf16 v[108:111], v[136:139], v[72:75], v[108:111]\n"
        "v_mul_f32 v98, v117, v98\n"
        "v_mul_f32 v99, v117, v99\n"
        "v_mfma_f32_16x16x32_bf16 v[112:115], v[144:147], v[72:75], 0\n"
        "ds_read_b128 a[228:231], v248 offset:36896\n"
        "v_cvt_pk_bf16_f32 v96, v96, v97\n"
        "v_mfma_f32_16x16x32_bf16 v[112:115], v[148:151], v[76:79], v[112:115]\n"
        "s_waitcnt lgkmcnt(14)\n"
        "ds_read_b128 a[232:235], v248 offset:43040\n"
        "v_cvt_pk_bf16_f32 v97, v98, v99\n"
        "v_mfma_f32_16x16x32_bf16 v[224:227], v[160:163], a[240:243], 0\n"
        "s_waitcnt lgkmcnt(14)\n"
        "ds_write_b32 v246, v96 offset:0\n"
        "v_mfma_f32_16x16x32_bf16 v[224:227], v[152:155], a[244:247], v[224:227]\n"
        "ds_write_b32 v246, v97 offset:4\n"
        "v_mfma_f32_16x16x32_bf16 v[196:199], v[164:167], a[244:247], 0\n"
        "ds_read_b128 a[236:239], v254 offset:36912\n"
        "v_add_f32 v100, v100, v101\n"
        "v_mfma_f32_16x16x32_bf16 v[196:199], v[156:159], a[240:243], v[196:199]\n"
        "s_waitcnt lgkmcnt(15)\n"
        "v_rcp_f32 v101, v100\n"
        "v_mfma_f32_16x16x32_bf16 v[172:175], v[232:235], a[248:251], v[172:175]\n"
        "s_waitcnt lgkmcnt(9)\n"
        "ds_read_b128 a[240:243], v254 offset:43056\n"
        "v_mul_f32 v104, v101, v104\n"
        "v_mfma_f32_16x16x32_bf16 v[172:175], v[64:67], a[216:219], v[172:175]\n"
        "ds_read_b128 a[244:247], v248 offset:36928\n"
        "v_mul_f32 v105, v101, v105\n"
        "v_mfma_f32_16x16x32_bf16 v[200:203], v[68:71], a[216:219], v[200:203]\n"
        "v_mul_f32 v106, v101, v106\n"
        "v_mul_f32 v107, v101, v107\n"
        "v_mfma_f32_16x16x32_bf16 v[200:203], v[236:239], a[248:251], v[200:203]\n"
        "s_waitcnt lgkmcnt(10)\n"
        "ds_read_b128 a[248:251], v248 offset:43072\n"
        "v_cvt_pk_bf16_f32 v104, v104, v105\n"
        "v_mfma_f32_16x16x32_bf16 v[204:207], v[236:239], a[220:223], v[204:207]\n"
        "s_waitcnt lgkmcnt(10)\n"
        "ds_read_b128 a[216:219], v254 offset:36944\n"
        "v_mov_b64 v[116:117], 0\n"
        "v_mfma_f32_16x16x32_bf16 v[204:207], v[68:71], a[224:227], v[204:207]\n"
        "v_cvt_pk_bf16_f32 v105, v106, v107\n"
        "v_mul_f32 v108, v101, v108\n"
        "v_mfma_f32_16x16x32_bf16 v[176:179], v[64:67], a[224:227], v[176:179]\n"
        "ds_write_b32 v245, v104 offset:256\n"
        "v_mfma_f32_16x16x32_bf16 v[176:179], v[232:235], a[220:223], v[176:179]\n"
        "s_waitcnt lgkmcnt(9)\n"
        "ds_write_b32 v245, v105 offset:260\n"
        "v_mfma_f32_16x16x32_bf16 v[180:183], v[232:235], a[228:231], v[180:183]\n"
        "s_waitcnt lgkmcnt(9)\n"
        "ds_read_b128 a[220:223], v254 offset:43088\n"
        "v_mul_f32 v109, v101, v109\n"
        "v_mfma_f32_16x16x32_bf16 v[180:183], v[64:67], a[232:235], v[180:183]\n"
        "v_maximum3_f32 v253, v253, v100, v101\n"
        "v_mul_f32 v110, v101, v110\n"
        "v_mfma_f32_16x16x32_bf16 v[212:215], v[68:71], a[232:235], v[212:215]\n"
        "v_mul_f32 v111, v101, v111\n"
        "v_cvt_pk_bf16_f32 v108, v108, v109\n"
        "v_mfma_f32_16x16x32_bf16 v[212:215], v[236:239], a[228:231], v[212:215]\n"
        "s_waitcnt lgkmcnt(7)\n"
        "ds_read_b128 v[156:159], v247 offset:16384\n"
        "v_cvt_pk_bf16_f32 v109, v110, v111\n"
        "v_mfma_f32_16x16x32_bf16 v[216:219], v[236:239], a[236:239], v[216:219]\n"
        "s_waitcnt lgkmcnt(7)\n"
        "ds_write_b32 v245, v108 offset:264\n"
        "v_mfma_f32_16x16x32_bf16 v[216:219], v[68:71], a[240:243], v[216:219]\n"
        "ds_write_b32 v245, v109 offset:268\n"
        "v_mfma_f32_16x16x32_bf16 v[184:187], v[64:67], a[240:243], v[184:187]\n"
        "v_mul_f32 v112, v101, v112\n"
        "v_mul_f32 v113, v101, v113\n"
        "v_mfma_f32_16x16x32_bf16 v[184:187], v[232:235], a[236:239], v[184:187]\n"
        "v_mul_f32 v114, v101, v114\n"
        "v_mul_f32 v115, v101, v115\n"
        "v_smfmac_f32_16x16x64_bf16 v[116:119], a[252:255], v[80:87], v255\n"
        "ds_read_b128 v[164:167], v247 offset:20480\n"
        "v_cvt_pk_bf16_f32 v112, v112, v113\n"
        "v_mfma_f32_16x16x32_bf16 v[120:123], v[128:131], v[80:83], 0\n"
        "ds_read_b128 v[160:163], v247 offset:16640\n"
        "v_cvt_pk_bf16_f32 v113, v114, v115\n"
        "v_mfma_f32_16x16x32_bf16 v[120:123], v[132:135], v[84:87], v[120:123]\n"
        "ds_write_b32 v246, v112 offset:256\n"
        "v_mfma_f32_16x16x32_bf16 v[124:127], v[140:143], v[84:87], 0\n"
        "ds_write_b32 v246, v113 offset:260\n"
        "v_mfma_f32_16x16x32_bf16 v[124:127], v[136:139], v[80:83], v[124:127]\n"
        "ds_read_b128 a[224:227], v248 offset:12288\n"
        "v_add_f32 v116, v116, v117\n"
        "v_mfma_f32_16x16x32_bf16 v[96:99], v[144:147], v[80:83], 0\n"
        "ds_read_b128 v[152:155], v247 offset:20736\n"
        "v_mfma_f32_16x16x32_bf16 v[96:99], v[148:151], v[84:87], v[96:99]\n"
        "s_waitcnt lgkmcnt(14)\n"
        "ds_read_b128 a[228:231], v248 offset:49152\n"
        "v_mfma_f32_16x16x32_bf16 v[192:195], v[232:235], a[244:247], v[192:195]\n"
        "s_waitcnt lgkmcnt(14)\n"
        "v_rcp_f32 v117, v116\n"
        "v_mfma_f32_16x16x32_bf16 v[192:195], v[64:67], a[248:251], v[192:195]\n"
        "ds_read_b128 a[232:235], v254 offset:12304\n"
        "v_mul_f32 v120, v117, v120\n"
        "v_mfma_f32_16x16x32_bf16 v[220:223], v[68:71], a[248:251], v[220:223]\n"
        "ds_read_b128 a[236:239], v254 offset:49168\n"
        "v_mul_f32 v121, v117, v121\n"
        "v_mfma_f32_16x16x32_bf16 v[220:223], v[236:239], a[244:247], v[220:223]\n"
        "s_waitcnt lgkmcnt(15)\n"
        "ds_read_b128 a[240:243], v248 offset:12320\n"
        "v_mul_f32 v122, v117, v122\n"
        "v_mfma_f32_16x16x32_bf16 v[224:227], v[236:239], a[216:219], v[224:227]\n"
        "s_waitcnt lgkmcnt(13)\n"
        "ds_read_b128 a[244:247], v248 offset:49184\n"
        "v_mul_f32 v123, v117, v123\n"
        "v_mfma_f32_16x16x32_bf16 v[224:227], v[68:71], a[220:223], v[224:227]\n"
        "ds_read_b128 a[248:251], v254 offset:12336\n"
        "v_cvt_pk_bf16_f32 v120, v120, v121\n"
        "v_mfma_f32_16x16x32_bf16 v[196:199], v[64:67], a[220:223], v[196:199]\n"
        "ds_read_b128 a[220:223], v248 offset:12352\n"
        "v_cvt_pk_bf16_f32 v121, v122, v123\n"
        "v_mfma_f32_16x16x32_bf16 v[196:199], v[232:235], a[216:219], v[196:199]\n"
        "s_waitcnt lgkmcnt(8)\n"
        "ds_write_b32 v245, v120 offset:512\n"
        "v_mfma_f32_16x16x32_bf16 v[172:175], v[156:159], a[224:227], v[172:175]\n"
        "s_waitcnt lgkmcnt(7)\n"
        "ds_read_b128 a[216:219], v254 offset:49200\n"
        "v_mul_f32 v124, v117, v124\n"
        "v_mfma_f32_16x16x32_bf16 v[172:175], v[164:167], a[228:231], v[172:175]\n"
        "ds_write_b32 v245, v121 offset:516\n"
        "v_mfma_f32_16x16x32_bf16 v[200:203], v[152:155], a[228:231], v[200:203]\n"
        "ds_read_b128 a[228:231], v254 offset:12368\n"
        "v_mul_f32 v125, v117, v125\n"
        "v_mfma_f32_16x16x32_bf16 v[200:203], v[160:163], a[224:227], v[200:203]\n"
        "s_waitcnt lgkmcnt(9)\n"
        "ds_read_b128 a[224:227], v248 offset:49216\n"
        "v_maximum3_f32 v253, v253, v116, v117\n"
        "v_mfma_f32_16x16x32_bf16 v[204:207], v[160:163], a[232:235], v[204:207]\n"
        "s_waitcnt lgkmcnt(9)\n"
        "v_mul_f32 v126, v117, v126\n"
        "v_mul_f32 v127, v117, v127\n"
        "v_mfma_f32_16x16x32_bf16 v[204:207], v[152:155], a[236:239], v[204:207]\n"
        "v_cvt_pk_bf16_f32 v124, v124, v125\n"
        "v_cvt_pk_bf16_f32 v125, v126, v127\n"
        "v_mfma_f32_16x16x32_bf16 v[176:179], v[164:167], a[236:239], v[176:179]\n"
        "ds_write_b32 v245, v124 offset:520\n"
        "v_mfma_f32_16x16x32_bf16 v[176:179], v[156:159], a[232:235], v[176:179]\n"
        "s_waitcnt lgkmcnt(9)\n"
        "ds_write_b32 v245, v125 offset:524\n"
        "v_mfma_f32_16x16x32_bf16 v[180:183], v[156:159], a[240:243], v[180:183]\n"
        "s_waitcnt lgkmcnt(9)\n"
        "ds_read_b128 a[232:235], v254 offset:49232\n"
        "v_mul_f32 v96, v117, v96\n"
        "v_mfma_f32_16x16x32_bf16 v[180:183], v[164:167], a[244:247], v[180:183]\n"
        "v_mul_f32 v97, v117, v97\n"
        "v_mul_f32 v98, v117, v98\n"
        "v_mfma_f32_16x16x32_bf16 v[212:215], v[152:155], a[244:247], v[212:215]\n"
        "v_mul_f32 v99, v117, v99\n"
        "v_cvt_pk_bf16_f32 v96, v96, v97\n"
        "v_mfma_f32_16x16x32_bf16 v[212:215], v[160:163], a[240:243], v[212:215]\n"
        "s_waitcnt lgkmcnt(9)\n"
        "v_cvt_pk_bf16_f32 v97, v98, v99\n"
        "v_add_u32 v247, s31, v247\n"
        "v_mfma_f32_16x16x32_bf16 v[216:219], v[160:163], a[248:251], v[216:219]\n"
        "s_waitcnt lgkmcnt(6)\n"
        "v_mov_b64 v[80:81], 0\n"
        "v_mfma_f32_16x16x32_bf16 v[216:219], v[152:155], a[216:219], v[216:219]\n"
        "ds_write_b32 v246, v96 offset:512\n"
        "v_mfma_f32_16x16x32_bf16 v[184:187], v[164:167], a[216:219], v[184:187]\n"
        "ds_write_b32 v246, v97 offset:516\n"
        "v_mfma_f32_16x16x32_bf16 v[184:187], v[156:159], a[248:251], v[184:187]\n"
        "s_waitcnt vmcnt(0)\n"
        "s_barrier\n"
        "v_mfma_f32_16x16x32_bf16 v[192:195], v[156:159], a[220:223], v[192:195]\n"
        "s_waitcnt lgkmcnt(5)\n"
        "v_mfma_f32_16x16x32_bf16 v[192:195], v[164:167], a[224:227], v[192:195]\n"
        "ds_read_b128 a[236:239], v240 offset:0\n"
        "v_mfma_f32_16x16x32_bf16 v[220:223], v[152:155], a[224:227], v[220:223]\n"
        "ds_read_b128 a[240:243], v241 offset:0\n"
        "v_mfma_f32_16x16x32_bf16 v[220:223], v[160:163], a[220:223], v[220:223]\n"
        "ds_read_b128 a[244:247], v240 offset:128\n"
        "v_mfma_f32_16x16x32_bf16 v[224:227], v[160:163], a[228:231], v[224:227]\n"
        "s_waitcnt lgkmcnt(5)\n"
        "ds_read_b128 a[248:251], v241 offset:128\n"
        "v_mfma_f32_16x16x32_bf16 v[224:227], v[152:155], a[232:235], v[224:227]\n"
        "ds_read_b128 a[216:219], v240 offset:256\n"
        "v_mfma_f32_16x16x32_bf16 v[196:199], v[164:167], a[232:235], v[196:199]\n"
        "ds_read_b128 a[220:223], v241 offset:256\n"
        "v_mfma_f32_16x16x32_bf16 v[196:199], v[156:159], a[228:231], v[196:199]\n"
        "v_smfmac_f32_16x16x64_bf16 v[80:83], a[252:255], v[88:95], v255\n"
        "v_mfma_f32_16x16x32_bf16 v[84:87], v[128:131], v[88:91], 0\n"
        "v_mfma_f32_16x16x32_bf16 v[84:87], v[132:135], v[92:95], v[84:87]\n"
        "v_mfma_f32_16x16x32_bf16 v[232:235], v[140:143], v[92:95], 0\n"
        "v_mfma_f32_16x16x32_bf16 v[232:235], v[136:139], v[88:91], v[232:235]\n"
        "s_sub_u32 s27, s27, 1\n"
        "v_mfma_f32_16x16x32_bf16 v[236:239], v[144:147], v[88:91], 0\n"
        "s_cmp_lg_u32 s27, 0\n"
        "v_mfma_f32_16x16x32_bf16 v[236:239], v[148:151], v[92:95], v[236:239]\n"
        "s_cbranch_scc1 10b\n"
        "20:\n"
        "s_waitcnt lgkmcnt(0)\n"
        "v_mfma_f32_16x16x32_bf16 v[96:99], a[72:75], a[236:239], 0\n"
        "v_and_b32 v242, 60, v250\n"
        "v_add3_u32 v242, v250, v242, s26\n"
        "v_mfma_f32_16x16x32_bf16 v[96:99], a[76:79], a[240:243], v[96:99]\n"
        "v_cvt_pk_bf16_f32 v108, v172, v176\n"
        "v_cvt_pk_bf16_f32 v109, v180, v184\n"
        "v_mfma_f32_16x16x32_bf16 v[96:99], a[80:83], a[244:247], v[96:99]\n"
        "v_cvt_pk_bf16_f32 v110, v192, v196\n"
        "v_add_f32 v80, v80, v81\n"
        "v_mfma_f32_16x16x32_bf16 v[96:99], a[84:87], a[248:251], v[96:99]\n"
        "buffer_store_dwordx2 v[108:109], v242, s[16:19], s38 offen offset:0 sc1\n"
        "v_mfma_f32_16x16x32_bf16 v[96:99], a[88:91], a[216:219], v[96:99]\n"
        "v_rcp_f32 v81, v80\n"
        "v_mfma_f32_16x16x32_bf16 v[96:99], a[92:95], a[220:223], v[96:99]\n"
        "v_cvt_pk_bf16_f32 v112, v173, v177\n"
        "v_cvt_pk_bf16_f32 v113, v181, v185\n"
        "v_mfma_f32_16x16x32_bf16 v[100:103], a[116:119], a[220:223], 0\n"
        "v_mul_f32 v84, v81, v84\n"
        "v_mul_f32 v85, v81, v85\n"
        "v_mfma_f32_16x16x32_bf16 v[100:103], a[112:115], a[216:219], v[100:103]\n"
        "v_mul_f32 v86, v81, v86\n"
        "v_mul_f32 v87, v81, v87\n"
        "v_mfma_f32_16x16x32_bf16 v[100:103], a[108:111], a[248:251], v[100:103]\n"
        "s_sub_u32 s31, s29, s30\n"
        "buffer_store_dword v110, v250, s[16:19], s38 offen offset:0 sc1\n"
        "v_mfma_f32_16x16x32_bf16 v[100:103], a[104:107], a[244:247], v[100:103]\n"
        "v_cvt_pk_bf16_f32 v152, v96, v97\n"
        "v_cvt_pk_bf16_f32 v153, v98, v99\n"
        "v_mfma_f32_16x16x32_bf16 v[100:103], a[100:103], a[240:243], v[100:103]\n"
        "v_cvt_pk_bf16_f32 v114, v193, v197\n"
        "v_cvt_pk_bf16_f32 v84, v84, v85\n"
        "v_mfma_f32_16x16x32_bf16 v[100:103], a[96:99], a[236:239], v[100:103]\n"
        "ds_write_b32 v245, v84 offset:768\n"
        "v_mfma_f32_16x16x32_bf16 v[104:107], a[120:123], a[236:239], 0\n"
        "v_cvt_pk_bf16_f32 v85, v86, v87\n"
        "v_mul_f32 v232, v81, v232\n"
        "v_mfma_f32_16x16x32_bf16 v[104:107], a[124:127], a[240:243], v[104:107]\n"
        "buffer_store_dwordx2 v[112:113], v242, s[16:19], s38 offen offset:384 sc1\n"
        "v_mfma_f32_16x16x32_bf16 v[104:107], a[128:131], a[244:247], v[104:107]\n"
        "45:\n"
        "s_cbranch_execz 40b\n"
        "ds_write_b32 v245, v85 offset:772\n"
        "v_mfma_f32_16x16x32_bf16 v[104:107], a[132:135], a[248:251], v[104:107]\n"
        "v_cvt_pk_bf16_f32 v154, v100, v101\n"
        "v_cvt_pk_bf16_f32 v155, v102, v103\n"
        "v_mfma_f32_16x16x32_bf16 v[104:107], a[136:139], a[216:219], v[104:107]\n"
        "v_cvt_pk_bf16_f32 v108, v174, v178\n"
        "v_cvt_pk_bf16_f32 v109, v182, v186\n"
        "v_mfma_f32_16x16x32_bf16 v[104:107], a[140:143], a[220:223], v[104:107]\n"
        "v_mul_f32 v233, v81, v233\n"
        "v_maximum3_f32 v253, v253, v80, v81\n"
        "v_mfma_f32_16x16x32_bf16 v[96:99], a[20:23], a[220:223], 0\n"
        "buffer_store_dword v114, v250, s[16:19], s38 offen offset:384 sc1\n"
        "v_mfma_f32_16x16x32_bf16 v[96:99], a[16:19], a[216:219], v[96:99]\n"
        "v_cvt_pk_bf16_f32 v110, v194, v198\n"
        "v_mul_f32 v234, v81, v234\n"
        "v_mfma_f32_16x16x32_bf16 v[96:99], a[12:15], a[248:251], v[96:99]\n"
        "v_cvt_pk_bf16_f32 v168, v104, v105\n"
        "v_cvt_pk_bf16_f32 v169, v106, v107\n"
        "v_mfma_f32_16x16x32_bf16 v[96:99], a[8:11], a[244:247], v[96:99]\n"
        "v_mul_f32 v235, v81, v235\n"
        "v_cvt_pk_bf16_f32 v232, v232, v233\n"
        "v_mfma_f32_16x16x32_bf16 v[96:99], a[4:7], a[240:243], v[96:99]\n"
        "ds_write_b32 v245, v232 offset:776\n"
        "v_mfma_f32_16x16x32_bf16 v[96:99], a[0:3], a[236:239], v[96:99]\n"
        "buffer_store_dwordx2 v[108:109], v242, s[16:19], s38 offen offset:768 sc1\n"
        "v_mfma_f32_16x16x32_bf16 v[100:103], a[24:27], a[236:239], 0\n"
        "v_cvt_pk_bf16_f32 v112, v175, v179\n"
        "v_cvt_pk_bf16_f32 v113, v183, v187\n"
        "v_mfma_f32_16x16x32_bf16 v[100:103], a[28:31], a[240:243], v[100:103]\n"
        "v_cvt_pk_bf16_f32 v233, v234, v235\n"
        "v_xor_b32 v251, 0x2000, v251\n"
        "v_mfma_f32_16x16x32_bf16 v[100:103], a[32:35], a[244:247], v[100:103]\n"
        "v_mul_f32 v96, s50, v96\n"
        "v_mul_f32 v97, s50, v97\n"
        "v_mfma_f32_16x16x32_bf16 v[100:103], a[36:39], a[248:251], v[100:103]\n"
        "v_mul_f32 v98, s50, v98\n"
        "v_mul_f32 v99, s50, v99\n"
        "v_mfma_f32_16x16x32_bf16 v[100:103], a[40:43], a[216:219], v[100:103]\n"
        "buffer_store_dword v110, v250, s[16:19], s38 offen offset:768 sc1\n"
        "v_mfma_f32_16x16x32_bf16 v[100:103], a[44:47], a[220:223], v[100:103]\n"
        "v_cvt_pk_bf16_f32 v156, v96, v97\n"
        "v_cvt_pk_bf16_f32 v157, v98, v99\n"
        "v_mfma_f32_16x16x32_bf16 v[104:107], a[68:71], a[220:223], 0\n"
        "ds_write_b32 v245, v233 offset:780\n"
        "v_mfma_f32_16x16x32_bf16 v[104:107], a[64:67], a[216:219], v[104:107]\n"
        "v_cvt_pk_bf16_f32 v114, v195, v199\n"
        "v_xor_b32 v252, 0x2000, v252\n"
        "v_mfma_f32_16x16x32_bf16 v[104:107], a[60:63], a[248:251], v[104:107]\n"
        "v_mul_f32 v100, s50, v100\n"
        "v_mul_f32 v101, s50, v101\n"
        "v_mfma_f32_16x16x32_bf16 v[104:107], a[56:59], a[244:247], v[104:107]\n"
        "buffer_store_dwordx2 v[112:113], v242, s[16:19], s38 offen offset:1152 sc1\n"
        "v_mfma_f32_16x16x32_bf16 v[104:107], a[52:55], a[240:243], v[104:107]\n"
        "v_mul_f32 v102, s50, v102\n"
        "v_mul_f32 v103, s50, v103\n"
        "v_mfma_f32_16x16x32_bf16 v[104:107], a[48:51], a[236:239], v[104:107]\n"
        "v_cvt_pk_bf16_f32 v158, v100, v101\n"
        "v_cvt_pk_bf16_f32 v159, v102, v103\n"
        "v_mfma_f32_16x16x32_bf16 v[96:99], a[236:239], a[144:147], 0\n"
        "ds_read_b128 a[224:227], v240 offset:1536\n"
        "v_cvt_pk_bf16_f32 v108, v200, v204\n"
        "v_mfma_f32_16x16x32_bf16 v[96:99], a[240:243], a[148:151], v[96:99]\n"
        "ds_read_b128 a[228:231], v241 offset:1536\n"
        "v_cvt_pk_bf16_f32 v109, v212, v216\n"
        "v_mfma_f32_16x16x32_bf16 v[96:99], a[244:247], a[152:155], v[96:99]\n"
        "buffer_store_dword v114, v250, s[16:19], s38 offen offset:1152 sc1\n"
        "v_mfma_f32_16x16x32_bf16 v[96:99], a[248:251], a[156:159], v[96:99]\n"
        "v_mul_f32 v104, s50, v104\n"
        "v_mul_f32 v105, s50, v105\n"
        "v_mfma_f32_16x16x32_bf16 v[96:99], a[216:219], a[160:163], v[96:99]\n"
        "v_mul_f32 v106, s50, v106\n"
        "v_mul_f32 v107, s50, v107\n"
        "v_mfma_f32_16x16x32_bf16 v[96:99], a[220:223], a[164:167], v[96:99]\n"
        "v_cvt_pk_bf16_f32 v160, v104, v105\n"
        "v_cvt_pk_bf16_f32 v161, v106, v107\n"
        "v_mfma_f32_16x16x32_bf16 v[100:103], a[220:223], a[188:191], 0\n"
        "ds_read_b128 a[232:235], v240 offset:1664\n"
        "v_mov_b64 v[166:167], v[160:161]\n"
        "v_mfma_f32_16x16x32_bf16 v[100:103], a[216:219], a[184:187], v[100:103]\n"
        "buffer_store_dwordx2 v[108:109], v242, s[16:19], s39 offen offset:0 sc1\n"
        "v_mfma_f32_16x16x32_bf16 v[100:103], a[248:251], a[180:183], v[100:103]\n"
        "v_cvt_pk_bf16_f32 v128, v96, v97\n"
        "v_cvt_pk_bf16_f32 v129, v98, v99\n"
        "v_mfma_f32_16x16x32_bf16 v[100:103], a[244:247], a[176:179], v[100:103]\n"
        "v_cvt_pk_bf16_f32 v110, v220, v224\n"
        "v_cvt_pk_bf16_f32 v112, v201, v205\n"
        "v_mfma_f32_16x16x32_bf16 v[100:103], a[240:243], a[172:175], v[100:103]\n"
        "v_cvt_pk_bf16_f32 v113, v213, v217\n"
        "v_mul_f32 v236, v81, v236\n"
        "v_mfma_f32_16x16x32_bf16 v[100:103], a[236:239], a[168:171], v[100:103]\n"
        "v_mul_f32 v237, v81, v237\n"
        "v_mul_f32 v238, v81, v238\n"
        "v_mfma_f32_16x16x32_bf16 v[104:107], a[236:239], a[192:195], 0\n"
        "buffer_store_dword v110, v250, s[16:19], s39 offen offset:0 sc1\n"
        "v_mfma_f32_16x16x32_bf16 v[104:107], a[240:243], a[196:199], v[104:107]\n"
        "ds_read_b128 a[236:239], v241 offset:1664\n"
        "ds_read_b128 a[240:243], v240 offset:1792\n"
        "v_mfma_f32_16x16x32_bf16 v[104:107], a[244:247], a[200:203], v[104:107]\n"
        "ds_read_b128 a[244:247], v241 offset:1792\n"
        "v_cvt_pk_bf16_f32 v136, v100, v101\n"
        "v_mfma_f32_16x16x32_bf16 v[104:107], a[248:251], a[204:207], v[104:107]\n"
        "v_cvt_pk_bf16_f32 v137, v102, v103\n"
        "v_mul_f32 v239, v81, v239\n"
        "v_mfma_f32_16x16x32_bf16 v[104:107], a[216:219], a[208:211], v[104:107]\n"
        "v_cvt_pk_bf16_f32 v236, v236, v237\n"
        "v_cvt_pk_bf16_f32 v237, v238, v239\n"
        "v_mfma_f32_16x16x32_bf16 v[104:107], a[220:223], a[212:215], v[104:107]\n"
        "s_waitcnt lgkmcnt(5)\n"
        "buffer_store_dwordx2 v[112:113], v242, s[16:19], s39 offen offset:384 sc1\n"
        "v_mfma_f32_16x16x32_bf16 v[96:99], a[72:75], a[224:227], 0\n"
        "s_waitcnt lgkmcnt(4)\n"
        "ds_write_b32 v246, v236 offset:768\n"
        "v_mfma_f32_16x16x32_bf16 v[96:99], a[76:79], a[228:231], v[96:99]\n"
        "s_waitcnt lgkmcnt(4)\n"
        "ds_write_b32 v246, v237 offset:772\n"
        "v_mfma_f32_16x16x32_bf16 v[96:99], a[80:83], a[232:235], v[96:99]\n"
        "s_waitcnt lgkmcnt(4)\n"
        "ds_read_b128 v[162:165], v251 offset:0\n"
        "v_cvt_pk_bf16_f32 v114, v221, v225\n"
        "v_mfma_f32_16x16x32_bf16 v[96:99], a[84:87], a[236:239], v[96:99]\n"
        "s_waitcnt lgkmcnt(4)\n"
        "v_cvt_pk_bf16_f32 v144, v104, v105\n"
        "v_cvt_pk_bf16_f32 v145, v106, v107\n"
        "v_mfma_f32_16x16x32_bf16 v[96:99], a[88:91], a[240:243], v[96:99]\n"
        "s_waitcnt lgkmcnt(3)\n"
        "buffer_store_dword v114, v250, s[16:19], s39 offen offset:384 sc1\n"
        "v_mfma_f32_16x16x32_bf16 v[96:99], a[92:95], a[244:247], v[96:99]\n"
        "v_cvt_pk_bf16_f32 v108, v202, v206\n"
        "v_cvt_pk_bf16_f32 v109, v214, v218\n"
        "v_mfma_f32_16x16x32_bf16 v[100:103], a[116:119], a[244:247], 0\n"
        "v_cvt_pk_bf16_f32 v110, v222, v226\n"
        "v_add_u32 v245, s31, v245\n"
        "v_mfma_f32_16x16x32_bf16 v[100:103], a[112:115], a[240:243], v[100:103]\n"
        "v_add_u32 v246, s31, v246\n"
        "v_mfma_f32_16x16x32_bf16 v[100:103], a[108:111], a[236:239], v[100:103]\n"
        "v_cvt_pk_bf16_f32 v172, v96, v97\n"
        "v_cvt_pk_bf16_f32 v173, v98, v99\n"
        "v_mfma_f32_16x16x32_bf16 v[100:103], a[104:107], a[232:235], v[100:103]\n"
        "buffer_store_dwordx2 v[108:109], v242, s[16:19], s39 offen offset:768 sc1\n"
        "v_mfma_f32_16x16x32_bf16 v[100:103], a[100:103], a[228:231], v[100:103]\n"
        "v_cvt_pk_bf16_f32 v112, v203, v207\n"
        "v_cvt_pk_bf16_f32 v113, v215, v219\n"
        "v_mfma_f32_16x16x32_bf16 v[100:103], a[96:99], a[224:227], v[100:103]\n"
        "s_nop 2\n"
        "v_mfma_f32_16x16x32_bf16 v[104:107], a[120:123], a[224:227], 0\n"
        "s_nop 2\n"
        "v_mfma_f32_16x16x32_bf16 v[104:107], a[124:127], a[228:231], v[104:107]\n"
        "v_cvt_pk_bf16_f32 v174, v100, v101\n"
        "v_cvt_pk_bf16_f32 v175, v102, v103\n"
        "v_mfma_f32_16x16x32_bf16 v[104:107], a[128:131], a[232:235], v[104:107]\n"
        "buffer_store_dword v110, v250, s[16:19], s39 offen offset:768 sc1\n"
        "v_mfma_f32_16x16x32_bf16 v[104:107], a[132:135], a[236:239], v[104:107]\n"
        "v_cvt_pk_bf16_f32 v114, v223, v227\n"
        "v_mfma_f32_16x16x32_bf16 v[104:107], a[136:139], a[240:243], v[104:107]\n"
        "v_mfma_f32_16x16x32_bf16 v[104:107], a[140:143], a[244:247], v[104:107]\n"
        "s_nop 2\n"
        "v_mfma_f32_16x16x32_bf16 v[96:99], a[20:23], a[244:247], 0\n"
        "ds_read_b128 a[248:251], v240 offset:12288\n"
        "s_nop 0\n"
        "v_mfma_f32_16x16x32_bf16 v[96:99], a[16:19], a[240:243], v[96:99]\n"
        "buffer_store_dwordx2 v[112:113], v242, s[16:19], s39 offen offset:1152 sc1\n"
        "v_mfma_f32_16x16x32_bf16 v[96:99], a[12:15], a[236:239], v[96:99]\n"
        "ds_read_b128 a[216:219], v241 offset:12288\n"
        "v_cvt_pk_bf16_f32 v190, v104, v105\n"
        "v_mfma_f32_16x16x32_bf16 v[96:99], a[8:11], a[232:235], v[96:99]\n"
        "ds_read_b128 a[220:223], v240 offset:12416\n"
        "v_cvt_pk_bf16_f32 v191, v106, v107\n"
        "v_mfma_f32_16x16x32_bf16 v[96:99], a[4:7], a[228:231], v[96:99]\n"
        "ds_read_b128 v[182:185], v251 offset:512\n"
        "v_mfma_f32_16x16x32_bf16 v[96:99], a[0:3], a[224:227], v[96:99]\n"
        "s_nop 2\n"
        "v_mfma_f32_16x16x32_bf16 v[0:3], v[152:155], v[156:159], v[0:3]\n"
        "s_waitcnt lgkmcnt(4)\n"
        "buffer_store_dword v114, v250, s[16:19], s39 offen offset:1152 sc1\n"
        "v_mfma_f32_16x16x32_bf16 v[0:3], v[168:171], v[160:163], v[0:3]\n"
        "s_nop 0\n"
        "v_mfma_f32_16x16x32_bf16 v[4:7], v[172:175], v[156:159], v[4:7]\n"
        "v_mul_f32 v96, s50, v96\n"
        "v_mul_f32 v97, s50, v97\n"
        "v_mfma_f32_16x16x32_bf16 v[4:7], v[188:191], v[164:167], v[4:7]\n"
        "v_mul_f32 v98, s50, v98\n"
        "v_mul_f32 v99, s50, v99\n"
        "v_mfma_f32_16x16x32_bf16 v[100:103], a[24:27], a[224:227], 0\n"
        "v_cvt_pk_bf16_f32 v176, v96, v97\n"
        "v_cvt_pk_bf16_f32 v177, v98, v99\n"
        "v_mfma_f32_16x16x32_bf16 v[100:103], a[28:31], a[228:231], v[100:103]\n"
        "v_exp_f32 v0, v0\n"
        "v_mfma_f32_16x16x32_bf16 v[100:103], a[32:35], a[232:235], v[100:103]\n"
        "v_exp_f32 v1, v1\n"
        "v_mfma_f32_16x16x32_bf16 v[100:103], a[36:39], a[236:239], v[100:103]\n"
        "v_exp_f32 v2, v2\n"
        "v_mfma_f32_16x16x32_bf16 v[100:103], a[40:43], a[240:243], v[100:103]\n"
        "v_exp_f32 v3, v3\n"
        "v_mfma_f32_16x16x32_bf16 v[100:103], a[44:47], a[244:247], v[100:103]\n"
        "v_exp_f32 v4, v4\n"
        "v_mfma_f32_16x16x32_bf16 v[104:107], a[48:51], a[224:227], 0\n"
        "ds_read_b128 a[224:227], v241 offset:12416\n"
        "v_cvt_pk_bf16_f32 v64, v0, v1\n"
        "v_mfma_f32_16x16x32_bf16 v[104:107], a[52:55], a[228:231], v[104:107]\n"
        "ds_read_b128 a[228:231], v240 offset:12544\n"
        "v_cvt_pk_bf16_f32 v65, v2, v3\n"
        "v_mfma_f32_16x16x32_bf16 v[104:107], a[56:59], a[232:235], v[104:107]\n"
        "s_barrier\n"
        "ds_read_b128 a[232:235], v241 offset:12544\n"
        "v_mul_f32 v100, s50, v100\n"
        "v_mfma_f32_16x16x32_bf16 v[104:107], a[60:63], a[236:239], v[104:107]\n"
        "v_mul_f32 v101, s50, v101\n"
        "v_mul_f32 v102, s50, v102\n"
        "v_mfma_f32_16x16x32_bf16 v[104:107], a[64:67], a[240:243], v[104:107]\n"
        "v_mul_f32 v103, s50, v103\n"
        "v_cvt_pk_bf16_f32 v178, v100, v101\n"
        "v_mfma_f32_16x16x32_bf16 v[104:107], a[68:71], a[244:247], v[104:107]\n"
        "s_waitcnt lgkmcnt(6)\n"
        "v_cvt_pk_bf16_f32 v179, v102, v103\n"
        "v_mfma_f32_16x16x32_bf16 v[96:99], a[72:75], a[248:251], 0\n"
        "s_waitcnt lgkmcnt(5)\n"
        "v_exp_f32 v5, v5\n"
        "v_mfma_f32_16x16x32_bf16 v[96:99], a[76:79], a[216:219], v[96:99]\n"
        "s_waitcnt lgkmcnt(4)\n"
        "v_exp_f32 v6, v6\n"
        "v_mfma_f32_16x16x32_bf16 v[96:99], a[80:83], a[220:223], v[96:99]\n"
        "s_waitcnt lgkmcnt(2)\n"
        "v_exp_f32 v7, v7\n"
        "v_mfma_f32_16x16x32_bf16 v[96:99], a[84:87], a[224:227], v[96:99]\n"
        "s_waitcnt lgkmcnt(1)\n"
        "v_mul_f32 v104, s50, v104\n"
        "v_mul_f32 v105, s50, v105\n"
        "v_mfma_f32_16x16x32_bf16 v[96:99], a[88:91], a[228:231], v[96:99]\n"
        "s_waitcnt lgkmcnt(0)\n"
        "v_mul_f32 v106, s50, v106\n"
        "v_mul_f32 v107, s50, v107\n"
        "v_mfma_f32_16x16x32_bf16 v[96:99], a[92:95], a[232:235], v[96:99]\n"
        "v_cvt_pk_bf16_f32 v180, v104, v105\n"
        "v_cvt_pk_bf16_f32 v181, v106, v107\n"
        "v_mfma_f32_16x16x32_bf16 v[16:19], v[152:155], v[176:179], v[16:19]\n"
        "ds_read_b128 v[162:165], v252 offset:0\n"
        "v_mov_b64 v[186:187], v[180:181]\n"
        "v_mfma_f32_16x16x32_bf16 v[16:19], v[168:171], v[180:183], v[16:19]\n"
        "s_and_b32 s41, s42, 1\n"
        "v_cvt_pk_bf16_f32 v66, v4, v5\n"
        "v_cvt_pk_bf16_f32 v192, v96, v97\n"
        "v_mfma_f32_16x16x32_bf16 v[20:23], v[172:175], v[176:179], v[20:23]\n"
        "s_lshl_b32 s45, s41, 8\n"
        "v_cvt_pk_bf16_f32 v193, v98, v99\n"
        "v_cvt_pk_bf16_f32 v67, v6, v7\n"
        "v_mfma_f32_16x16x32_bf16 v[20:23], v[188:191], v[184:187], v[20:23]\n"
        "s_sub_u32 s45, s31, s45\n"
        "v_exp_f32 v16, v16\n"
        "v_mfma_f32_16x16x32_bf16 v[100:103], a[116:119], a[232:235], 0\n"
        "s_mul_i32 s48, s41, 786432\n"
        "ds_read_b128 v[182:185], v252 offset:512\n"
        "v_add_u32 v243, s45, v247\n"
        "v_mfma_f32_16x16x32_bf16 v[100:103], a[112:115], a[228:231], v[100:103]\n"
        "s_sub_u32 s48, s37, s48\n"
        "v_exp_f32 v17, v17\n"
        "v_mfma_f32_16x16x32_bf16 v[100:103], a[108:111], a[224:227], v[100:103]\n"
        "s_cmp_eq_u32 s41, 0\n"
        "v_exp_f32 v18, v18\n"
        "v_mfma_f32_16x16x32_bf16 v[100:103], a[104:107], a[220:223], v[100:103]\n"
        "s_cselect_b32 s41, s43, s44\n"
        "v_exp_f32 v19, v19\n"
        "v_mfma_f32_16x16x32_bf16 v[100:103], a[100:103], a[216:219], v[100:103]\n"
        "s_add_u32 s35, s48, 6291456\n"
        "v_exp_f32 v20, v20\n"
        "v_mfma_f32_16x16x32_bf16 v[100:103], a[96:99], a[248:251], v[100:103]\n"
        "s_add_u32 s35, s35, s41\n"
        "v_exp_f32 v21, v21\n"
        "v_mfma_f32_16x16x32_bf16 v[104:107], a[120:123], a[248:251], 0\n"
        "s_add_u32 s36, s48, 7077888\n"
        "v_exp_f32 v22, v22\n"
        "v_mfma_f32_16x16x32_bf16 v[104:107], a[124:127], a[216:219], v[104:107]\n"
        "s_cmp_eq_u32 s36, 100663296\n"
        "v_exp_f32 v23, v23\n"
        "v_mfma_f32_16x16x32_bf16 v[104:107], a[128:131], a[220:223], v[104:107]\n"
        "s_cselect_b32 s36, 0, s36\n"
        "v_cvt_pk_bf16_f32 v194, v100, v101\n"
        "v_cvt_pk_bf16_f32 v195, v102, v103\n"
        "v_mfma_f32_16x16x32_bf16 v[104:107], a[132:135], a[224:227], v[104:107]\n"
        "s_add_u32 s36, s36, s41\n"
        "v_cvt_pk_bf16_f32 v72, v16, v17\n"
        "v_cvt_pk_bf16_f32 v73, v18, v19\n"
        "v_mfma_f32_16x16x32_bf16 v[104:107], a[136:139], a[228:231], v[104:107]\n"
        "s_lshl_b32 s29, s42, 8\n"
        "v_cvt_pk_bf16_f32 v74, v20, v21\n"
        "v_cvt_pk_bf16_f32 v75, v22, v23\n"
        "v_mfma_f32_16x16x32_bf16 v[104:107], a[140:143], a[232:235], v[104:107]\n"
        "s_add_u32 s29, s29, 147456\n"
        "v_mbcnt_lo_u32_b32 v244, -1, 0\n"
        "v_mbcnt_hi_u32_b32 v244, -1, v244\n"
        "v_mfma_f32_16x16x32_bf16 v[96:99], a[20:23], a[232:235], 0\n"
        "s_mov_b32 s47, 147456\n"
        "ds_read_b128 a[236:239], v240 offset:13824\n"
        "v_lshl_add_u32 v249, v244, 4, s47\n"
        "v_mfma_f32_16x16x32_bf16 v[96:99], a[16:19], a[228:231], v[96:99]\n"
        "ds_read_b128 a[240:243], v241 offset:13824\n"
        "v_cvt_pk_bf16_f32 v208, v104, v105\n"
        "v_mfma_f32_16x16x32_bf16 v[96:99], a[12:15], a[224:227], v[96:99]\n"
        "ds_read_b128 a[244:247], v240 offset:13952\n"
        "v_cvt_pk_bf16_f32 v209, v106, v107\n"
        "v_mfma_f32_16x16x32_bf16 v[96:99], a[8:11], a[220:223], v[96:99]\n"
        "v_lshl_add_u32 v244, v244, 2, s29\n"
        "v_mfma_f32_16x16x32_bf16 v[96:99], a[4:7], a[216:219], v[96:99]\n"
        "ds_read_b128 v[202:205], v251 offset:4096\n"
        "v_mfma_f32_16x16x32_bf16 v[96:99], a[0:3], a[248:251], v[96:99]\n"
        "s_nop 2\n"
        "v_mfma_f32_16x16x32_bf16 v[8:11], v[192:195], v[156:159], v[8:11]\n"
        "s_waitcnt lgkmcnt(5)\n"
        "s_nop 1\n"
        "v_mfma_f32_16x16x32_bf16 v[8:11], v[208:211], v[160:163], v[8:11]\n"
        "v_mfma_f32_16x16x32_bf16 v[24:27], v[192:195], v[176:179], v[24:27]\n"
        "s_waitcnt lgkmcnt(4)\n"
        "v_mul_f32 v96, s50, v96\n"
        "v_mul_f32 v97, s50, v97\n"
        "v_mfma_f32_16x16x32_bf16 v[24:27], v[208:211], v[180:183], v[24:27]\n"
        "v_mul_f32 v98, s50, v98\n"
        "v_mul_f32 v99, s50, v99\n"
        "v_mfma_f32_16x16x32_bf16 v[100:103], a[24:27], a[248:251], 0\n"
        "v_cvt_pk_bf16_f32 v196, v96, v97\n"
        "v_cvt_pk_bf16_f32 v197, v98, v99\n"
        "v_mfma_f32_16x16x32_bf16 v[100:103], a[28:31], a[216:219], v[100:103]\n"
        "v_exp_f32 v8, v8\n"
        "v_mfma_f32_16x16x32_bf16 v[100:103], a[32:35], a[220:223], v[100:103]\n"
        "v_exp_f32 v9, v9\n"
        "v_mfma_f32_16x16x32_bf16 v[100:103], a[36:39], a[224:227], v[100:103]\n"
        "v_exp_f32 v10, v10\n"
        "v_mfma_f32_16x16x32_bf16 v[100:103], a[40:43], a[228:231], v[100:103]\n"
        "v_exp_f32 v11, v11\n"
        "v_mfma_f32_16x16x32_bf16 v[100:103], a[44:47], a[232:235], v[100:103]\n"
        "v_exp_f32 v24, v24\n"
        "v_mfma_f32_16x16x32_bf16 v[104:107], a[48:51], a[248:251], 0\n"
        "ds_read_b128 a[248:251], v241 offset:13952\n"
        "v_cvt_pk_bf16_f32 v68, v8, v9\n"
        "v_mfma_f32_16x16x32_bf16 v[104:107], a[52:55], a[216:219], v[104:107]\n"
        "ds_read_b128 a[216:219], v240 offset:14080\n"
        "v_cvt_pk_bf16_f32 v69, v10, v11\n"
        "v_mfma_f32_16x16x32_bf16 v[104:107], a[56:59], a[220:223], v[104:107]\n"
        "ds_read_b128 a[220:223], v241 offset:14080\n"
        "v_mul_f32 v100, s50, v100\n"
        "v_mfma_f32_16x16x32_bf16 v[104:107], a[60:63], a[224:227], v[104:107]\n"
        "v_mul_f32 v101, s50, v101\n"
        "v_mul_f32 v102, s50, v102\n"
        "v_mfma_f32_16x16x32_bf16 v[104:107], a[64:67], a[228:231], v[104:107]\n"
        "v_mul_f32 v103, s50, v103\n"
        "v_cvt_pk_bf16_f32 v198, v100, v101\n"
        "v_mfma_f32_16x16x32_bf16 v[104:107], a[68:71], a[232:235], v[104:107]\n"
        "s_waitcnt lgkmcnt(6)\n"
        "v_cvt_pk_bf16_f32 v199, v102, v103\n"
        "v_mfma_f32_16x16x32_bf16 v[96:99], a[72:75], a[236:239], 0\n"
        "s_waitcnt lgkmcnt(5)\n"
        "v_exp_f32 v25, v25\n"
        "v_mfma_f32_16x16x32_bf16 v[96:99], a[76:79], a[240:243], v[96:99]\n"
        "s_waitcnt lgkmcnt(4)\n"
        "v_exp_f32 v26, v26\n"
        "v_mfma_f32_16x16x32_bf16 v[96:99], a[80:83], a[244:247], v[96:99]\n"
        "s_waitcnt lgkmcnt(2)\n"
        "v_exp_f32 v27, v27\n"
        "v_mfma_f32_16x16x32_bf16 v[96:99], a[84:87], a[248:251], v[96:99]\n"
        "s_waitcnt lgkmcnt(1)\n"
        "v_mul_f32 v104, s50, v104\n"
        "v_mul_f32 v105, s50, v105\n"
        "v_mfma_f32_16x16x32_bf16 v[96:99], a[88:91], a[216:219], v[96:99]\n"
        "s_waitcnt lgkmcnt(0)\n"
        "v_mul_f32 v106, s50, v106\n"
        "v_mul_f32 v107, s50, v107\n"
        "v_mfma_f32_16x16x32_bf16 v[96:99], a[92:95], a[220:223], v[96:99]\n"
        "v_cvt_pk_bf16_f32 v200, v104, v105\n"
        "v_cvt_pk_bf16_f32 v201, v106, v107\n"
        "v_mfma_f32_16x16x32_bf16 v[32:35], v[152:155], v[196:199], v[32:35]\n"
        "v_mov_b64 v[206:207], v[200:201]\n"
        "v_cvt_pk_bf16_f32 v76, v24, v25\n"
        "v_mfma_f32_16x16x32_bf16 v[32:35], v[168:171], v[200:203], v[32:35]\n"
        "v_cvt_pk_bf16_f32 v77, v26, v27\n"
        "v_mfma_f32_16x16x32_bf16 v[36:39], v[172:175], v[196:199], v[36:39]\n"
        "v_cvt_pk_bf16_f32 v212, v96, v97\n"
        "v_cvt_pk_bf16_f32 v213, v98, v99\n"
        "v_mfma_f32_16x16x32_bf16 v[36:39], v[188:191], v[204:207], v[36:39]\n"
        "ds_read_b128 v[202:205], v252 offset:4096\n"
        "v_mfma_f32_16x16x32_bf16 v[100:103], a[116:119], a[220:223], 0\n"
        "v_mfma_f32_16x16x32_bf16 v[100:103], a[112:115], a[216:219], v[100:103]\n"
        "v_exp_f32 v32, v32\n"
        "v_mfma_f32_16x16x32_bf16 v[100:103], a[108:111], a[248:251], v[100:103]\n"
        "s_cmp_eq_u32 s37, 100663296\n"
        "v_exp_f32 v33, v33\n"
        "v_mfma_f32_16x16x32_bf16 v[100:103], a[104:107], a[244:247], v[100:103]\n"
        "s_cselect_b32 s46, 0, s37\n"
        "v_exp_f32 v34, v34\n"
        "v_mfma_f32_16x16x32_bf16 v[100:103], a[100:103], a[240:243], v[100:103]\n"
        "s_add_u32 s38, s46, s43\n"
        "ds_read_b128 a[72:75], v248 offset:0\n"
        "v_cvt_pk_bf16_f32 v80, v32, v33\n"
        "v_mfma_f32_16x16x32_bf16 v[100:103], a[96:99], a[236:239], v[100:103]\n"
        "s_add_u32 s39, s46, s44\n"
        "v_exp_f32 v35, v35\n"
        "v_mfma_f32_16x16x32_bf16 v[40:43], v[192:195], v[196:199], v[40:43]\n"
        "s_waitcnt lgkmcnt(1)\n"
        "ds_read_b128 a[76:79], v248 offset:6144\n"
        "v_cvt_pk_bf16_f32 v81, v34, v35\n"
        "v_mfma_f32_16x16x32_bf16 v[40:43], v[208:211], v[200:203], v[40:43]\n"
        "v_exp_f32 v36, v36\n"
        "v_mfma_f32_16x16x32_bf16 v[104:107], a[120:123], a[236:239], 0\n"
        "ds_read_b128 a[80:83], v248 offset:36864\n"
        "v_cvt_pk_bf16_f32 v214, v100, v101\n"
        "v_mfma_f32_16x16x32_bf16 v[104:107], a[124:127], a[240:243], v[104:107]\n"
        "ds_read_b128 a[84:87], v248 offset:43008\n"
        "v_cvt_pk_bf16_f32 v215, v102, v103\n"
        "v_mfma_f32_16x16x32_bf16 v[104:107], a[128:131], a[244:247], v[104:107]\n"
        "v_exp_f32 v37, v37\n"
        "v_mfma_f32_16x16x32_bf16 v[104:107], a[132:135], a[248:251], v[104:107]\n"
        "v_exp_f32 v38, v38\n"
        "v_mfma_f32_16x16x32_bf16 v[104:107], a[136:139], a[216:219], v[104:107]\n"
        "ds_read_b128 a[88:91], v248 offset:12288\n"
        "v_cvt_pk_bf16_f32 v82, v36, v37\n"
        "v_mfma_f32_16x16x32_bf16 v[104:107], a[140:143], a[220:223], v[104:107]\n"
        "v_exp_f32 v39, v39\n"
        "v_mfma_f32_16x16x32_bf16 v[96:99], a[20:23], a[220:223], 0\n"
        "ds_read_b128 a[92:95], v248 offset:49152\n"
        "v_cvt_pk_bf16_f32 v83, v38, v39\n"
        "v_mfma_f32_16x16x32_bf16 v[96:99], a[16:19], a[216:219], v[96:99]\n"
        "v_exp_f32 v40, v40\n"
        "v_mfma_f32_16x16x32_bf16 v[96:99], a[12:15], a[248:251], v[96:99]\n"
        "v_exp_f32 v41, v41\n"
        "v_mfma_f32_16x16x32_bf16 v[96:99], a[8:11], a[244:247], v[96:99]\n"
        "v_cvt_pk_bf16_f32 v230, v104, v105\n"
        "v_cvt_pk_bf16_f32 v231, v106, v107\n"
        "v_mfma_f32_16x16x32_bf16 v[96:99], a[4:7], a[240:243], v[96:99]\n"
        "v_exp_f32 v42, v42\n"
        "v_mfma_f32_16x16x32_bf16 v[96:99], a[0:3], a[236:239], v[96:99]\n"
        "ds_read_b128 a[96:99], v254 offset:49168\n"
        "v_cvt_pk_bf16_f32 v84, v40, v41\n"
        "v_mfma_f32_16x16x32_bf16 v[12:15], v[212:215], v[156:159], v[12:15]\n"
        "v_exp_f32 v43, v43\n"
        "v_mfma_f32_16x16x32_bf16 v[12:15], v[228:231], v[164:167], v[12:15]\n"
        "ds_read_b128 v[222:225], v251 offset:4608\n"
        "v_cvt_pk_bf16_f32 v85, v42, v43\n"
        "v_mfma_f32_16x16x32_bf16 v[28:31], v[212:215], v[176:179], v[28:31]\n"
        "v_mul_f32 v96, s50, v96\n"
        "v_mul_f32 v97, s50, v97\n"
        "v_mfma_f32_16x16x32_bf16 v[28:31], v[228:231], v[184:187], v[28:31]\n"
        "v_mul_f32 v98, s50, v98\n"
        "v_mul_f32 v99, s50, v99\n"
        "v_mfma_f32_16x16x32_bf16 v[44:47], v[212:215], v[196:199], v[44:47]\n"
        "v_cvt_pk_bf16_f32 v216, v96, v97\n"
        "v_cvt_pk_bf16_f32 v217, v98, v99\n"
        "v_mfma_f32_16x16x32_bf16 v[44:47], v[228:231], v[204:207], v[44:47]\n"
        "v_exp_f32 v12, v12\n"
        "v_mfma_f32_16x16x32_bf16 v[100:103], a[24:27], a[236:239], 0\n"
        "v_exp_f32 v13, v13\n"
        "v_mfma_f32_16x16x32_bf16 v[100:103], a[28:31], a[240:243], v[100:103]\n"
        "v_exp_f32 v14, v14\n"
        "v_mfma_f32_16x16x32_bf16 v[100:103], a[32:35], a[244:247], v[100:103]\n"
        "ds_read_b128 a[100:103], v254 offset:12304\n"
        "v_cvt_pk_bf16_f32 v70, v12, v13\n"
        "v_mfma_f32_16x16x32_bf16 v[100:103], a[36:39], a[248:251], v[100:103]\n"
        "v_exp_f32 v15, v15\n"
        "v_mfma_f32_16x16x32_bf16 v[100:103], a[40:43], a[216:219], v[100:103]\n"
        "ds_read_b128 a[104:107], v254 offset:43024\n"
        "v_cvt_pk_bf16_f32 v71, v14, v15\n"
        "v_mfma_f32_16x16x32_bf16 v[100:103], a[44:47], a[220:223], v[100:103]\n"
        "v_exp_f32 v28, v28\n"
        "v_mfma_f32_16x16x32_bf16 v[104:107], a[68:71], a[220:223], 0\n"
        "v_exp_f32 v29, v29\n"
        "v_mfma_f32_16x16x32_bf16 v[104:107], a[64:67], a[216:219], v[104:107]\n"
        "v_exp_f32 v30, v30\n"
        "v_mfma_f32_16x16x32_bf16 v[104:107], a[60:63], a[248:251], v[104:107]\n"
        "ds_read_b128 a[108:111], v254 offset:36880\n"
        "v_cvt_pk_bf16_f32 v78, v28, v29\n"
        "v_mfma_f32_16x16x32_bf16 v[104:107], a[56:59], a[244:247], v[104:107]\n"
        "v_mul_f32 v100, s50, v100\n"
        "v_mul_f32 v101, s50, v101\n"
        "v_mfma_f32_16x16x32_bf16 v[104:107], a[52:55], a[240:243], v[104:107]\n"
        "v_mul_f32 v102, s50, v102\n"
        "v_mul_f32 v103, s50, v103\n"
        "v_mfma_f32_16x16x32_bf16 v[104:107], a[48:51], a[236:239], v[104:107]\n"
        "v_cvt_pk_bf16_f32 v218, v100, v101\n"
        "v_cvt_pk_bf16_f32 v219, v102, v103\n"
        "v_mfma_f32_16x16x32_bf16 v[96:99], a[236:239], a[144:147], 0\n"
        "v_exp_f32 v31, v31\n"
        "v_mfma_f32_16x16x32_bf16 v[96:99], a[240:243], a[148:151], v[96:99]\n"
        "ds_read_b128 a[112:115], v254 offset:6160\n"
        "v_cvt_pk_bf16_f32 v79, v30, v31\n"
        "v_mfma_f32_16x16x32_bf16 v[96:99], a[244:247], a[152:155], v[96:99]\n"
        "v_mul_f32 v104, s50, v104\n"
        "v_mul_f32 v105, s50, v105\n"
        "v_mfma_f32_16x16x32_bf16 v[96:99], a[248:251], a[156:159], v[96:99]\n"
        "v_mul_f32 v106, s50, v106\n"
        "v_mul_f32 v107, s50, v107\n"
        "v_mfma_f32_16x16x32_bf16 v[96:99], a[216:219], a[160:163], v[96:99]\n"
        "v_cvt_pk_bf16_f32 v220, v104, v105\n"
        "v_cvt_pk_bf16_f32 v221, v106, v107\n"
        "v_mfma_f32_16x16x32_bf16 v[96:99], a[220:223], a[164:167], v[96:99]\n"
        "ds_read_b128 a[224:227], v240 offset:12288\n"
        "v_mov_b64 v[226:227], v[220:221]\n"
        "v_mfma_f32_16x16x32_bf16 v[48:51], v[152:155], v[216:219], v[48:51]\n"
        "s_waitcnt lgkmcnt(5)\n"
        "v_exp_f32 v44, v44\n"
        "v_mfma_f32_16x16x32_bf16 v[48:51], v[168:171], v[220:223], v[48:51]\n"
        "v_exp_f32 v45, v45\n"
        "v_mfma_f32_16x16x32_bf16 v[52:55], v[172:175], v[216:219], v[52:55]\n"
        "v_exp_f32 v46, v46\n"
        "v_mfma_f32_16x16x32_bf16 v[52:55], v[188:191], v[224:227], v[52:55]\n"
        "ds_read_b128 v[222:225], v252 offset:4608\n"
        "v_cvt_pk_bf16_f32 v134, v96, v97\n"
        "v_mfma_f32_16x16x32_bf16 v[100:103], a[220:223], a[188:191], 0\n"
        "ds_read_b128 a[228:231], v241 offset:12288\n"
        "v_cvt_pk_bf16_f32 v135, v98, v99\n"
        "v_mfma_f32_16x16x32_bf16 v[100:103], a[216:219], a[184:187], v[100:103]\n"
        "ds_read_b128 a[232:235], v240 offset:12416\n"
        "v_cvt_pk_bf16_f32 v86, v44, v45\n"
        "v_mfma_f32_16x16x32_bf16 v[100:103], a[248:251], a[180:183], v[100:103]\n"
        "v_exp_f32 v47, v47\n"
        "v_mfma_f32_16x16x32_bf16 v[100:103], a[244:247], a[176:179], v[100:103]\n"
        "ds_read_b128 a[116:119], v254 offset:16\n"
        "v_cvt_pk_bf16_f32 v87, v46, v47\n"
        "v_mfma_f32_16x16x32_bf16 v[100:103], a[240:243], a[172:175], v[100:103]\n"
        "v_exp_f32 v48, v48\n"
        "v_mfma_f32_16x16x32_bf16 v[100:103], a[236:239], a[168:171], v[100:103]\n"
        "v_exp_f32 v49, v49\n"
        "v_mfma_f32_16x16x32_bf16 v[56:59], v[192:195], v[216:219], v[56:59]\n"
        "s_waitcnt lgkmcnt(3)\n"
        "v_exp_f32 v50, v50\n"
        "v_mfma_f32_16x16x32_bf16 v[56:59], v[208:211], v[220:223], v[56:59]\n"
        "ds_read_b128 a[120:123], v248 offset:32\n"
        "v_cvt_pk_bf16_f32 v88, v48, v49\n"
        "v_mfma_f32_16x16x32_bf16 v[60:63], v[212:215], v[216:219], v[60:63]\n"
        "v_exp_f32 v51, v51\n"
        "v_mfma_f32_16x16x32_bf16 v[60:63], v[228:231], v[224:227], v[60:63]\n"
        "v_cvt_pk_bf16_f32 v142, v100, v101\n"
        "v_cvt_pk_bf16_f32 v143, v102, v103\n"
        "v_mfma_f32_16x16x32_bf16 v[104:107], a[236:239], a[192:195], 0\n"
        "ds_read_b128 a[236:239], v241 offset:12416\n"
        "v_cvt_pk_bf16_f32 v89, v50, v51\n"
        "v_mfma_f32_16x16x32_bf16 v[104:107], a[240:243], a[196:199], v[104:107]\n"
        "ds_read_b128 a[240:243], v240 offset:12544\n"
        "v_mfma_f32_16x16x32_bf16 v[104:107], a[244:247], a[200:203], v[104:107]\n"
        "ds_read_b128 a[244:247], v241 offset:12544\n"
        "v_mfma_f32_16x16x32_bf16 v[104:107], a[248:251], a[204:207], v[104:107]\n"
        "v_exp_f32 v52, v52\n"
        "v_mfma_f32_16x16x32_bf16 v[104:107], a[216:219], a[208:211], v[104:107]\n"
        "v_exp_f32 v53, v53\n"
        "v_mfma_f32_16x16x32_bf16 v[104:107], a[220:223], a[212:215], v[104:107]\n"
        "v_exp_f32 v54, v54\n"
        "v_mfma_f32_16x16x32_bf16 v[96:99], a[224:227], a[144:147], 0\n"
        "s_waitcnt lgkmcnt(6)\n"
        "ds_read_b128 a[248:251], v240 offset:1536\n"
        "v_cvt_pk_bf16_f32 v90, v52, v53\n"
        "v_mfma_f32_16x16x32_bf16 v[96:99], a[228:231], a[148:151], v[96:99]\n"
        "s_waitcnt lgkmcnt(6)\n"
        "v_exp_f32 v55, v55\n"
        "v_mfma_f32_16x16x32_bf16 v[96:99], a[232:235], a[152:155], v[96:99]\n"
        "s_waitcnt lgkmcnt(3)\n"
        "ds_read_b128 a[216:219], v241 offset:1536\n"
        "v_cvt_pk_bf16_f32 v150, v104, v105\n"
        "v_mfma_f32_16x16x32_bf16 v[96:99], a[236:239], a[156:159], v[96:99]\n"
        "s_waitcnt lgkmcnt(3)\n"
        "ds_read_b128 a[220:223], v240 offset:1664\n"
        "v_cvt_pk_bf16_f32 v151, v106, v107\n"
        "v_mfma_f32_16x16x32_bf16 v[96:99], a[240:243], a[160:163], v[96:99]\n"
        "s_waitcnt lgkmcnt(3)\n"
        "ds_read_b128 a[124:127], v248 offset:6176\n"
        "v_cvt_pk_bf16_f32 v91, v54, v55\n"
        "v_mfma_f32_16x16x32_bf16 v[96:99], a[244:247], a[164:167], v[96:99]\n"
        "v_exp_f32 v56, v56\n"
        "v_mfma_f32_16x16x32_bf16 v[100:103], a[244:247], a[188:191], 0\n"
        "v_exp_f32 v57, v57\n"
        "v_mfma_f32_16x16x32_bf16 v[100:103], a[240:243], a[184:187], v[100:103]\n"
        "v_exp_f32 v58, v58\n"
        "v_mfma_f32_16x16x32_bf16 v[100:103], a[236:239], a[180:183], v[100:103]\n"
        "ds_read_b128 v[32:35], v247 offset:0\n"
        "v_cvt_pk_bf16_f32 v92, v56, v57\n"
        "v_mfma_f32_16x16x32_bf16 v[100:103], a[232:235], a[176:179], v[100:103]\n"
        "v_cvt_pk_bf16_f32 v132, v96, v97\n"
        "v_cvt_pk_bf16_f32 v133, v98, v99\n"
        "v_mfma_f32_16x16x32_bf16 v[100:103], a[228:231], a[172:175], v[100:103]\n"
        "v_exp_f32 v59, v59\n"
        "v_mfma_f32_16x16x32_bf16 v[100:103], a[224:227], a[168:171], v[100:103]\n"
        "ds_read_b128 v[156:159], v247 offset:4096\n"
        "v_cvt_pk_bf16_f32 v93, v58, v59\n"
        "v_mfma_f32_16x16x32_bf16 v[104:107], a[224:227], a[192:195], 0\n"
        "ds_read_b128 a[224:227], v241 offset:1664\n"
        "v_mfma_f32_16x16x32_bf16 v[104:107], a[228:231], a[196:199], v[104:107]\n"
        "ds_read_b128 a[228:231], v240 offset:1792\n"
        "v_mfma_f32_16x16x32_bf16 v[104:107], a[232:235], a[200:203], v[104:107]\n"
        "ds_read_b128 a[232:235], v241 offset:1792\n"
        "v_cvt_pk_bf16_f32 v140, v100, v101\n"
        "v_mfma_f32_16x16x32_bf16 v[104:107], a[236:239], a[204:207], v[104:107]\n"
        "ds_read_b128 v[160:163], v247 offset:8192\n"
        "v_cvt_pk_bf16_f32 v141, v102, v103\n"
        "v_mfma_f32_16x16x32_bf16 v[104:107], a[240:243], a[208:211], v[104:107]\n"
        "v_exp_f32 v60, v60\n"
        "v_mfma_f32_16x16x32_bf16 v[104:107], a[244:247], a[212:215], v[104:107]\n"
        "s_waitcnt lgkmcnt(9)\n"
        "v_exp_f32 v61, v61\n"
        "v_mfma_f32_16x16x32_bf16 v[96:99], a[248:251], a[144:147], 0\n"
        "s_waitcnt lgkmcnt(8)\n"
        "v_exp_f32 v62, v62\n"
        "v_mfma_f32_16x16x32_bf16 v[96:99], a[216:219], a[148:151], v[96:99]\n"
        "s_waitcnt lgkmcnt(7)\n"
        "ds_read_b128 v[164:167], v247 offset:12288\n"
        "v_cvt_pk_bf16_f32 v94, v60, v61\n"
        "v_mfma_f32_16x16x32_bf16 v[96:99], a[220:223], a[152:155], v[96:99]\n"
        "46:\n"
        "s_cbranch_execz 40b\n"
        "s_waitcnt lgkmcnt(4)\n"
        "v_exp_f32 v63, v63\n"
        "v_mfma_f32_16x16x32_bf16 v[96:99], a[224:227], a[156:159], v[96:99]\n"
        "s_waitcnt lgkmcnt(3)\n"
        "v_cvt_pk_bf16_f32 v148, v104, v105\n"
        "v_cvt_pk_bf16_f32 v149, v106, v107\n"
        "v_mfma_f32_16x16x32_bf16 v[96:99], a[228:231], a[160:163], v[96:99]\n"
        "s_waitcnt lgkmcnt(2)\n"
        "ds_read_b128 v[152:155], v247 offset:16384\n"
        "v_cvt_pk_bf16_f32 v95, v62, v63\n"
        "v_mfma_f32_16x16x32_bf16 v[96:99], a[232:235], a[164:167], v[96:99]\n"
        "ds_read_b128 v[168:171], v247 offset:20480\n"
        "s_nop 0\n"
        "v_mfma_f32_16x16x32_bf16 v[100:103], a[232:235], a[188:191], 0\n"
        "ds_read_b128 a[128:131], v248 offset:36896\n"
        "s_nop 0\n"
        "v_mfma_f32_16x16x32_bf16 v[100:103], a[228:231], a[184:187], v[100:103]\n"
        "ds_read_b128 a[132:135], v248 offset:43040\n"
        "v_mfma_f32_16x16x32_bf16 v[100:103], a[224:227], a[180:183], v[100:103]\n"
        "ds_read_b128 a[136:139], v248 offset:12320\n"
        "v_cvt_pk_bf16_f32 v130, v96, v97\n"
        "v_mfma_f32_16x16x32_bf16 v[100:103], a[220:223], a[176:179], v[100:103]\n"
        "ds_read_b128 a[140:143], v248 offset:49184\n"
        "v_cvt_pk_bf16_f32 v131, v98, v99\n"
        "v_mfma_f32_16x16x32_bf16 v[100:103], a[216:219], a[172:175], v[100:103]\n"
        "ds_read_b128 a[0:3], v254 offset:49200\n"
        "v_mfma_f32_16x16x32_bf16 v[100:103], a[248:251], a[168:171], v[100:103]\n"
        "ds_read_b128 a[4:7], v254 offset:12336\n"
        "v_mfma_f32_16x16x32_bf16 v[104:107], a[248:251], a[192:195], 0\n"
        "ds_read_b128 a[8:11], v254 offset:43056\n"
        "v_mov_b64 v[116:117], 0\n"
        "v_mfma_f32_16x16x32_bf16 v[104:107], a[216:219], a[196:199], v[104:107]\n"
        "ds_read_b128 a[12:15], v254 offset:36912\n"
        "s_nop 0\n"
        "v_mfma_f32_16x16x32_bf16 v[104:107], a[220:223], a[200:203], v[104:107]\n"
        "ds_read_b128 a[16:19], v254 offset:6192\n"
        "v_cvt_pk_bf16_f32 v138, v100, v101\n"
        "v_mfma_f32_16x16x32_bf16 v[104:107], a[224:227], a[204:207], v[104:107]\n"
        "ds_read_b128 a[20:23], v254 offset:48\n"
        "v_cvt_pk_bf16_f32 v139, v102, v103\n"
        "v_mfma_f32_16x16x32_bf16 v[104:107], a[228:231], a[208:211], v[104:107]\n"
        "ds_read_b128 a[24:27], v248 offset:64\n"
        "v_mfma_f32_16x16x32_bf16 v[104:107], a[232:235], a[212:215], v[104:107]\n"
        "ds_read_b128 a[28:31], v248 offset:6208\n"
        "v_mfma_f32_16x16x32_bf16 v[172:175], v[32:35], a[72:75], 0\n"
        "ds_read_b128 a[32:35], v248 offset:36928\n"
        "v_mfma_f32_16x16x32_bf16 v[172:175], v[156:159], a[76:79], v[172:175]\n"
        "s_waitcnt lgkmcnt(15)\n"
        "ds_read_b128 a[36:39], v248 offset:43072\n"
        "v_mfma_f32_16x16x32_bf16 v[172:175], v[160:163], a[80:83], v[172:175]\n"
        "s_waitcnt lgkmcnt(15)\n"
        "ds_read_b128 a[40:43], v248 offset:12352\n"
        "v_mfma_f32_16x16x32_bf16 v[172:175], v[164:167], a[84:87], v[172:175]\n"
        "s_waitcnt lgkmcnt(15)\n"
        "v_cvt_pk_bf16_f32 v146, v104, v105\n"
        "v_cvt_pk_bf16_f32 v147, v106, v107\n"
        "v_mfma_f32_16x16x32_bf16 v[172:175], v[152:155], a[88:91], v[172:175]\n"
        "s_waitcnt lgkmcnt(15)\n"
        "ds_read_b128 a[44:47], v248 offset:49216\n"
        "v_mfma_f32_16x16x32_bf16 v[172:175], v[168:171], a[92:95], v[172:175]\n"
        "ds_read_b128 a[48:51], v254 offset:49232\n"
        "v_smfmac_f32_16x16x64_bf16 v[116:119], a[252:255], v[64:71], v255\n"
        "ds_read_b128 a[52:55], v254 offset:12368\n"
        "v_mfma_f32_16x16x32_bf16 v[120:123], v[128:131], v[64:67], 0\n"
        "ds_read_b128 a[56:59], v254 offset:43088\n"
        "v_mfma_f32_16x16x32_bf16 v[120:123], v[132:135], v[68:71], v[120:123]\n"
        "ds_read_b128 a[60:63], v254 offset:36944\n"
        "v_mfma_f32_16x16x32_bf16 v[124:127], v[140:143], v[68:71], 0\n"
        "ds_read_b128 a[64:67], v254 offset:6224\n"
        "v_mfma_f32_16x16x32_bf16 v[124:127], v[136:139], v[64:67], v[124:127]\n"
        "ds_read_b128 a[68:71], v254 offset:80\n"
        "v_add_f32 v116, v116, v117\n"
        "v_mfma_f32_16x16x32_bf16 v[232:235], v[144:147], v[64:67], 0\n"
        "v_rcp_f32 v117, v116\n"
        "v_mfma_f32_16x16x32_bf16 v[232:235], v[148:151], v[68:71], v[232:235]\n"
        "v_mul_f32 v120, v117, v120\n"
        "v_mul_f32 v121, v117, v121\n"
        "v_mfma_f32_16x16x32_bf16 v[176:179], v[168:171], a[96:99], 0\n"
        "v_mul_f32 v122, v117, v122\n"
        "v_mul_f32 v123, v117, v123\n"
        "v_mfma_f32_16x16x32_bf16 v[176:179], v[152:155], a[100:103], v[176:179]\n"
        "v_cvt_pk_bf16_f32 v120, v120, v121\n"
        "v_cvt_pk_bf16_f32 v121, v122, v123\n"
        "v_mfma_f32_16x16x32_bf16 v[176:179], v[164:167], a[104:107], v[176:179]\n"
        "ds_write_b32 v245, v120 offset:0\n"
        "v_mfma_f32_16x16x32_bf16 v[176:179], v[160:163], a[108:111], v[176:179]\n"
        "ds_write_b32 v245, v121 offset:4\n"
        "v_mfma_f32_16x16x32_bf16 v[176:179], v[156:159], a[112:115], v[176:179]\n"
        "v_mul_f32 v124, v117, v124\n"
        "v_mul_f32 v125, v117, v125\n"
        "v_mfma_f32_16x16x32_bf16 v[176:179], v[32:35], a[116:119], v[176:179]\n"
        "v_maximum3_f32 v253, v253, v116, v117\n"
        "v_mul_f32 v126, v117, v126\n"
        "v_mfma_f32_16x16x32_bf16 v[180:183], v[32:35], a[120:123], 0\n"
        "v_mov_b64 v[100:101], 0\n"
        "v_mul_f32 v127, v117, v127\n"
        "v_mfma_f32_16x16x32_bf16 v[180:183], v[156:159], a[124:127], v[180:183]\n"
        "s_waitcnt lgkmcnt(15)\n"
        "ds_read_b128 v[0:3], v247 offset:256\n"
        "v_cvt_pk_bf16_f32 v124, v124, v125\n"
        "v_mfma_f32_16x16x32_bf16 v[180:183], v[160:163], a[128:131], v[180:183]\n"
        "s_waitcnt lgkmcnt(15)\n"
        "ds_read_b128 v[4:7], v247 offset:4352\n"
        "v_cvt_pk_bf16_f32 v125, v126, v127\n"
        "v_mfma_f32_16x16x32_bf16 v[180:183], v[164:167], a[132:135], v[180:183]\n"
        "s_waitcnt lgkmcnt(15)\n"
        "ds_write_b32 v245, v124 offset:8\n"
        "v_mfma_f32_16x16x32_bf16 v[180:183], v[152:155], a[136:139], v[180:183]\n"
        "s_waitcnt lgkmcnt(15)\n"
        "ds_write_b32 v245, v125 offset:12\n"
        "v_mfma_f32_16x16x32_bf16 v[180:183], v[168:171], a[140:143], v[180:183]\n"
        "s_waitcnt lgkmcnt(15)\n"
        "ds_read_b128 v[8:11], v247 offset:8448\n"
        "v_mul_f32 v232, v117, v232\n"
        "v_mfma_f32_16x16x32_bf16 v[184:187], v[168:171], a[0:3], 0\n"
        "s_waitcnt lgkmcnt(15)\n"
        "ds_read_b128 v[16:19], v247 offset:12544\n"
        "v_mul_f32 v233, v117, v233\n"
        "v_mfma_f32_16x16x32_bf16 v[184:187], v[152:155], a[4:7], v[184:187]\n"
        "s_waitcnt lgkmcnt(15)\n"
        "ds_read_b128 v[20:23], v247 offset:16640\n"
        "v_mul_f32 v234, v117, v234\n"
        "v_mfma_f32_16x16x32_bf16 v[184:187], v[164:167], a[8:11], v[184:187]\n"
        "s_waitcnt lgkmcnt(15)\n"
        "ds_read_b128 v[12:15], v247 offset:20736\n"
        "v_mul_f32 v235, v117, v235\n"
        "v_mfma_f32_16x16x32_bf16 v[184:187], v[160:163], a[12:15], v[184:187]\n"
        "s_waitcnt lgkmcnt(15)\n"
        "v_cvt_pk_bf16_f32 v232, v232, v233\n"
        "v_cvt_pk_bf16_f32 v233, v234, v235\n"
        "v_mfma_f32_16x16x32_bf16 v[184:187], v[156:159], a[16:19], v[184:187]\n"
        "s_waitcnt lgkmcnt(15)\n"
        "ds_write_b32 v246, v232 offset:0\n"
        "v_mfma_f32_16x16x32_bf16 v[184:187], v[32:35], a[20:23], v[184:187]\n"
        "v_cvt_pk_bf16_f32 v188, v172, v176\n"
        "v_cvt_pk_bf16_f32 v208, v173, v177\n"
        "v_smfmac_f32_16x16x64_bf16 v[100:103], a[252:255], v[72:79], v255\n"
        "ds_write_b32 v246, v233 offset:4\n"
        "v_mfma_f32_16x16x32_bf16 v[104:107], v[128:131], v[72:75], 0\n"
        "v_cvt_pk_bf16_f32 v228, v174, v178\n"
        "s_nop 0\n"
        "v_mfma_f32_16x16x32_bf16 v[104:107], v[132:135], v[76:79], v[104:107]\n"
        "v_cvt_pk_bf16_f32 v189, v180, v184\n"
        "v_cvt_pk_bf16_f32 v209, v181, v185\n"
        "v_mfma_f32_16x16x32_bf16 v[108:111], v[140:143], v[76:79], 0\n"
        "buffer_store_dwordx2 v[188:189], v242, s[16:19], s38 offen offset:0 sc1\n"
        "v_mfma_f32_16x16x32_bf16 v[108:111], v[136:139], v[72:75], v[108:111]\n"
        "v_cvt_pk_bf16_f32 v229, v182, v186\n"
        "v_cvt_pk_bf16_f32 v236, v175, v179\n"
        "v_mfma_f32_16x16x32_bf16 v[112:115], v[144:147], v[72:75], 0\n"
        "v_cvt_pk_bf16_f32 v237, v183, v187\n"
        "v_add_f32 v100, v100, v101\n"
        "v_mfma_f32_16x16x32_bf16 v[112:115], v[148:151], v[76:79], v[112:115]\n"
        "s_waitcnt lgkmcnt(15)\n"
        "buffer_store_dwordx2 v[208:209], v242, s[16:19], s38 offen offset:384 sc1\n"
        "v_mfma_f32_16x16x32_bf16 v[192:195], v[32:35], a[24:27], 0\n"
        "s_waitcnt lgkmcnt(15)\n"
        "v_rcp_f32 v101, v100\n"
        "v_mfma_f32_16x16x32_bf16 v[192:195], v[156:159], a[28:31], v[192:195]\n"
        "s_waitcnt lgkmcnt(15)\n"
        "v_mul_f32 v104, v101, v104\n"
        "v_mul_f32 v105, v101, v105\n"
        "v_mfma_f32_16x16x32_bf16 v[192:195], v[160:163], a[32:35], v[192:195]\n"
        "s_waitcnt lgkmcnt(15)\n"
        "buffer_store_dwordx2 v[228:229], v242, s[16:19], s38 offen offset:768 sc1\n"
        "v_mfma_f32_16x16x32_bf16 v[192:195], v[164:167], a[36:39], v[192:195]\n"
        "s_waitcnt lgkmcnt(15)\n"
        "v_mul_f32 v106, v101, v106\n"
        "v_mul_f32 v107, v101, v107\n"
        "v_mfma_f32_16x16x32_bf16 v[192:195], v[152:155], a[40:43], v[192:195]\n"
        "s_waitcnt lgkmcnt(15)\n"
        "v_cvt_pk_bf16_f32 v104, v104, v105\n"
        "v_cvt_pk_bf16_f32 v105, v106, v107\n"
        "v_mfma_f32_16x16x32_bf16 v[192:195], v[168:171], a[44:47], v[192:195]\n"
        "s_waitcnt lgkmcnt(15)\n"
        "buffer_store_dwordx2 v[236:237], v242, s[16:19], s38 offen offset:1152 sc1\n"
        "v_mfma_f32_16x16x32_bf16 v[196:199], v[168:171], a[48:51], 0\n"
        "s_waitcnt lgkmcnt(15)\n"
        "v_mov_b64 v[48:49], 0\n"
        "v_mul_f32 v108, v101, v108\n"
        "v_mfma_f32_16x16x32_bf16 v[196:199], v[152:155], a[52:55], v[196:199]\n"
        "s_waitcnt lgkmcnt(15)\n"
        "ds_write_b32 v245, v104 offset:256\n"
        "v_mfma_f32_16x16x32_bf16 v[196:199], v[164:167], a[56:59], v[196:199]\n"
        "s_waitcnt lgkmcnt(15)\n"
        "ds_write_b32 v245, v105 offset:260\n"
        "v_mfma_f32_16x16x32_bf16 v[196:199], v[160:163], a[60:63], v[196:199]\n"
        "s_waitcnt lgkmcnt(15)\n"
        "v_mul_f32 v109, v101, v109\n"
        "v_maximum3_f32 v253, v253, v100, v101\n"
        "v_mfma_f32_16x16x32_bf16 v[196:199], v[156:159], a[64:67], v[196:199]\n"
        "s_waitcnt lgkmcnt(14)\n"
        "v_mul_f32 v110, v101, v110\n"
        "v_mul_f32 v111, v101, v111\n"
        "v_mfma_f32_16x16x32_bf16 v[196:199], v[32:35], a[68:71], v[196:199]\n"
        "s_waitcnt lgkmcnt(11)\n"
        "v_cvt_pk_bf16_f32 v108, v108, v109\n"
        "v_cvt_pk_bf16_f32 v109, v110, v111\n"
        "v_mfma_f32_16x16x32_bf16 v[200:203], v[0:3], a[72:75], 0\n"
        "s_waitcnt lgkmcnt(10)\n"
        "ds_write_b32 v245, v108 offset:264\n"
        "v_mfma_f32_16x16x32_bf16 v[200:203], v[4:7], a[76:79], v[200:203]\n"
        "s_waitcnt lgkmcnt(8)\n"
        "ds_write_b32 v245, v109 offset:268\n"
        "v_mfma_f32_16x16x32_bf16 v[200:203], v[8:11], a[80:83], v[200:203]\n"
        "s_waitcnt lgkmcnt(8)\n"
        "v_mul_f32 v112, v101, v112\n"
        "v_cvt_pk_bf16_f32 v190, v192, v196\n"
        "v_mfma_f32_16x16x32_bf16 v[200:203], v[16:19], a[84:87], v[200:203]\n"
        "s_waitcnt lgkmcnt(7)\n"
        "buffer_store_dword v190, v250, s[16:19], s38 offen offset:0 sc1\n"
        "v_mfma_f32_16x16x32_bf16 v[200:203], v[20:23], a[88:91], v[200:203]\n"
        "s_waitcnt lgkmcnt(6)\n"
        "v_mul_f32 v113, v101, v113\n"
        "v_cvt_pk_bf16_f32 v210, v193, v197\n"
        "v_mfma_f32_16x16x32_bf16 v[200:203], v[12:15], a[92:95], v[200:203]\n"
        "v_mul_f32 v114, v101, v114\n"
        "v_mul_f32 v115, v101, v115\n"
        "v_smfmac_f32_16x16x64_bf16 v[48:51], a[252:255], v[80:87], v255\n"
        "buffer_store_dword v210, v250, s[16:19], s38 offen offset:384 sc1\n"
        "v_mfma_f32_16x16x32_bf16 v[52:55], v[128:131], v[80:83], 0\n"
        "v_cvt_pk_bf16_f32 v112, v112, v113\n"
        "v_cvt_pk_bf16_f32 v113, v114, v115\n"
        "v_mfma_f32_16x16x32_bf16 v[52:55], v[132:135], v[84:87], v[52:55]\n"
        "ds_write_b32 v246, v112 offset:256\n"
        "v_mfma_f32_16x16x32_bf16 v[56:59], v[140:143], v[84:87], 0\n"
        "v_cvt_pk_bf16_f32 v230, v194, v198\n"
        "v_cvt_pk_bf16_f32 v238, v195, v199\n"
        "v_mfma_f32_16x16x32_bf16 v[56:59], v[136:139], v[80:83], v[56:59]\n"
        "buffer_store_dword v230, v250, s[16:19], s38 offen offset:768 sc1\n"
        "v_mfma_f32_16x16x32_bf16 v[60:63], v[144:147], v[80:83], 0\n"
        "ds_write_b32 v246, v113 offset:260\n"
        "v_mfma_f32_16x16x32_bf16 v[60:63], v[148:151], v[84:87], v[60:63]\n"
        "buffer_store_dword v238, v250, s[16:19], s38 offen offset:1152 sc1\n"
        "v_mfma_f32_16x16x32_bf16 v[204:207], v[12:15], a[96:99], 0\n"
        "v_add_f32 v48, v48, v49\n"
        "v_mfma_f32_16x16x32_bf16 v[204:207], v[20:23], a[100:103], v[204:207]\n"
        "v_rcp_f32 v49, v48\n"
        "v_mfma_f32_16x16x32_bf16 v[204:207], v[16:19], a[104:107], v[204:207]\n"
        "v_mul_f32 v52, v49, v52\n"
        "v_mul_f32 v53, v49, v53\n"
        "v_mfma_f32_16x16x32_bf16 v[204:207], v[8:11], a[108:111], v[204:207]\n"
        "v_mul_f32 v54, v49, v54\n"
        "v_mul_f32 v55, v49, v55\n"
        "v_mfma_f32_16x16x32_bf16 v[204:207], v[4:7], a[112:115], v[204:207]\n"
        "v_cvt_pk_bf16_f32 v52, v52, v53\n"
        "v_cvt_pk_bf16_f32 v53, v54, v55\n"
        "v_mfma_f32_16x16x32_bf16 v[204:207], v[0:3], a[116:119], v[204:207]\n"
        "ds_write_b32 v245, v52 offset:512\n"
        "v_mfma_f32_16x16x32_bf16 v[212:215], v[0:3], a[120:123], 0\n"
        "v_mov_b64 v[64:65], 0\n"
        "v_mul_f32 v56, v49, v56\n"
        "v_mfma_f32_16x16x32_bf16 v[212:215], v[4:7], a[124:127], v[212:215]\n"
        "ds_write_b32 v245, v53 offset:516\n"
        "v_mfma_f32_16x16x32_bf16 v[212:215], v[8:11], a[128:131], v[212:215]\n"
        "v_mul_f32 v57, v49, v57\n"
        "v_maximum3_f32 v253, v253, v48, v49\n"
        "v_mfma_f32_16x16x32_bf16 v[212:215], v[16:19], a[132:135], v[212:215]\n"
        "v_mul_f32 v58, v49, v58\n"
        "v_mul_f32 v59, v49, v59\n"
        "v_mfma_f32_16x16x32_bf16 v[212:215], v[20:23], a[136:139], v[212:215]\n"
        "v_cvt_pk_bf16_f32 v56, v56, v57\n"
        "v_cvt_pk_bf16_f32 v57, v58, v59\n"
        "v_mfma_f32_16x16x32_bf16 v[212:215], v[12:15], a[140:143], v[212:215]\n"
        "ds_write_b32 v245, v56 offset:520\n"
        "v_mfma_f32_16x16x32_bf16 v[216:219], v[12:15], a[0:3], 0\n"
        "ds_write_b32 v245, v57 offset:524\n"
        "v_mfma_f32_16x16x32_bf16 v[216:219], v[20:23], a[4:7], v[216:219]\n"
        "v_mul_f32 v60, v49, v60\n"
        "v_mul_f32 v61, v49, v61\n"
        "v_mfma_f32_16x16x32_bf16 v[216:219], v[16:19], a[8:11], v[216:219]\n"
        "v_mul_f32 v62, v49, v62\n"
        "v_mul_f32 v63, v49, v63\n"
        "v_mfma_f32_16x16x32_bf16 v[216:219], v[8:11], a[12:15], v[216:219]\n"
        "v_cvt_pk_bf16_f32 v60, v60, v61\n"
        "v_cvt_pk_bf16_f32 v61, v62, v63\n"
        "v_mfma_f32_16x16x32_bf16 v[216:219], v[4:7], a[16:19], v[216:219]\n"
        "ds_write_b32 v246, v60 offset:512\n"
        "v_mfma_f32_16x16x32_bf16 v[216:219], v[0:3], a[20:23], v[216:219]\n"
        "v_cvt_pk_bf16_f32 v188, v200, v204\n"
        "v_cvt_pk_bf16_f32 v208, v201, v205\n"
        "v_smfmac_f32_16x16x64_bf16 v[64:67], a[252:255], v[88:95], v255\n"
        "ds_write_b32 v246, v61 offset:516\n"
        "v_mfma_f32_16x16x32_bf16 v[68:71], v[128:131], v[88:91], 0\n"
        "v_cvt_pk_bf16_f32 v228, v202, v206\n"
        "s_nop 0\n"
        "v_mfma_f32_16x16x32_bf16 v[68:71], v[132:135], v[92:95], v[68:71]\n"
        "v_cvt_pk_bf16_f32 v189, v212, v216\n"
        "v_cvt_pk_bf16_f32 v209, v213, v217\n"
        "v_mfma_f32_16x16x32_bf16 v[72:75], v[140:143], v[92:95], 0\n"
        "s_waitcnt lgkmcnt(6)\n"
        "s_barrier\n"
        "buffer_store_dwordx2 v[188:189], v242, s[16:19], s39 offen offset:0 sc1\n"
        "v_mfma_f32_16x16x32_bf16 v[72:75], v[136:139], v[88:91], v[72:75]\n"
        "ds_read_b128 a[168:171], v243 offset:0\n"
        "v_cvt_pk_bf16_f32 v229, v214, v218\n"
        "v_mfma_f32_16x16x32_bf16 v[76:79], v[144:147], v[88:91], 0\n"
        "ds_read_b128 a[172:175], v243 offset:4096\n"
        "v_cvt_pk_bf16_f32 v236, v203, v207\n"
        "v_mfma_f32_16x16x32_bf16 v[76:79], v[148:151], v[92:95], v[76:79]\n"
        "buffer_store_dwordx2 v[208:209], v242, s[16:19], s39 offen offset:384 sc1\n"
        "v_mfma_f32_16x16x32_bf16 v[220:223], v[0:3], a[24:27], 0\n"
        "ds_read_b128 a[176:179], v243 offset:8192\n"
        "v_cvt_pk_bf16_f32 v237, v215, v219\n"
        "v_mfma_f32_16x16x32_bf16 v[220:223], v[4:7], a[28:31], v[220:223]\n"
        "ds_read_b128 a[180:183], v243 offset:12288\n"
        "v_add_f32 v64, v64, v65\n"
        "v_mfma_f32_16x16x32_bf16 v[220:223], v[8:11], a[32:35], v[220:223]\n"
        "buffer_store_dwordx2 v[228:229], v242, s[16:19], s39 offen offset:768 sc1\n"
        "v_mfma_f32_16x16x32_bf16 v[220:223], v[16:19], a[36:39], v[220:223]\n"
        "ds_read_b128 a[184:187], v243 offset:16384\n"
        "v_mfma_f32_16x16x32_bf16 v[220:223], v[20:23], a[40:43], v[220:223]\n"
        "v_rcp_f32 v65, v64\n"
        "v_mfma_f32_16x16x32_bf16 v[220:223], v[12:15], a[44:47], v[220:223]\n"
        "buffer_store_dwordx2 v[236:237], v242, s[16:19], s39 offen offset:1152 sc1\n"
        "v_mfma_f32_16x16x32_bf16 v[224:227], v[12:15], a[48:51], 0\n"
        "ds_read_b128 a[188:191], v243 offset:20480\n"
        "v_mul_f32 v68, v65, v68\n"
        "v_mfma_f32_16x16x32_bf16 v[224:227], v[20:23], a[52:55], v[224:227]\n"
        "v_mul_f32 v69, v65, v69\n"
        "v_mul_f32 v70, v65, v70\n"
        "v_mfma_f32_16x16x32_bf16 v[224:227], v[16:19], a[56:59], v[224:227]\n"
        "v_mul_f32 v71, v65, v71\n"
        "v_cvt_pk_bf16_f32 v68, v68, v69\n"
        "v_mfma_f32_16x16x32_bf16 v[224:227], v[8:11], a[60:63], v[224:227]\n"
        "v_cvt_pk_bf16_f32 v69, v70, v71\n"
        "v_mul_f32 v72, v65, v72\n"
        "v_mfma_f32_16x16x32_bf16 v[224:227], v[4:7], a[64:67], v[224:227]\n"
        "ds_write_b32 v245, v68 offset:768\n"
        "v_mfma_f32_16x16x32_bf16 v[224:227], v[0:3], a[68:71], v[224:227]\n"
        "s_waitcnt lgkmcnt(6)\n"
        "ds_write_b32 v245, v69 offset:772\n"
        "v_mfma_f32_16x16x32_bf16 v[24:27], a[168:171], a[72:75], 0\n"
        "s_waitcnt lgkmcnt(6)\n"
        "v_mul_f32 v73, v65, v73\n"
        "v_maximum3_f32 v253, v253, v64, v65\n"
        "v_mfma_f32_16x16x32_bf16 v[24:27], a[172:175], a[76:79], v[24:27]\n"
        "s_waitcnt lgkmcnt(5)\n"
        "v_mul_f32 v74, v65, v74\n"
        "v_mul_f32 v75, v65, v75\n"
        "v_mfma_f32_16x16x32_bf16 v[24:27], a[176:179], a[80:83], v[24:27]\n"
        "s_waitcnt lgkmcnt(4)\n"
        "v_cvt_pk_bf16_f32 v72, v72, v73\n"
        "v_cvt_pk_bf16_f32 v73, v74, v75\n"
        "v_mfma_f32_16x16x32_bf16 v[24:27], a[180:183], a[84:87], v[24:27]\n"
        "s_waitcnt lgkmcnt(3)\n"
        "v_cvt_pk_bf16_f32 v190, v220, v224\n"
        "v_mul_f32 v76, v65, v76\n"
        "v_mfma_f32_16x16x32_bf16 v[24:27], a[184:187], a[88:91], v[24:27]\n"
        "s_waitcnt lgkmcnt(2)\n"
        "buffer_store_dword v190, v250, s[16:19], s39 offen offset:0 sc1\n"
        "v_mfma_f32_16x16x32_bf16 v[24:27], a[188:191], a[92:95], v[24:27]\n"
        "ds_write_b32 v245, v72 offset:776\n"
        "v_mfma_f32_16x16x32_bf16 v[28:31], a[188:191], a[96:99], 0\n"
        "ds_write_b32 v245, v73 offset:780\n"
        "v_mfma_f32_16x16x32_bf16 v[28:31], a[184:187], a[100:103], v[28:31]\n"
        "v_mul_f32 v77, v65, v77\n"
        "v_cvt_pk_bf16_f32 v210, v221, v225\n"
        "v_mfma_f32_16x16x32_bf16 v[28:31], a[180:183], a[104:107], v[28:31]\n"
        "buffer_store_dword v210, v250, s[16:19], s39 offen offset:384 sc1\n"
        "v_mfma_f32_16x16x32_bf16 v[28:31], a[176:179], a[108:111], v[28:31]\n"
        "v_mul_f32 v78, v65, v78\n"
        "v_mul_f32 v79, v65, v79\n"
        "v_mfma_f32_16x16x32_bf16 v[28:31], a[172:175], a[112:115], v[28:31]\n"
        "v_cvt_pk_bf16_f32 v76, v76, v77\n"
        "v_cvt_pk_bf16_f32 v77, v78, v79\n"
        "v_mfma_f32_16x16x32_bf16 v[28:31], a[168:171], a[116:119], v[28:31]\n"
        "ds_write_b32 v246, v76 offset:768\n"
        "v_mfma_f32_16x16x32_bf16 v[32:35], a[168:171], a[120:123], 0\n"
        "v_cvt_pk_bf16_f32 v230, v222, v226\n"
        "v_cvt_pk_bf16_f32 v238, v223, v227\n"
        "v_mfma_f32_16x16x32_bf16 v[32:35], a[172:175], a[124:127], v[32:35]\n"
        "buffer_store_dword v230, v250, s[16:19], s39 offen offset:768 sc1\n"
        "v_mfma_f32_16x16x32_bf16 v[32:35], a[176:179], a[128:131], v[32:35]\n"
        "buffer_store_dword v238, v250, s[16:19], s39 offen offset:1152 sc1\n"
        "v_mfma_f32_16x16x32_bf16 v[32:35], a[180:183], a[132:135], v[32:35]\n"
        "ds_write_b32 v246, v77 offset:772\n"
        "v_mfma_f32_16x16x32_bf16 v[32:35], a[184:187], a[136:139], v[32:35]\n"
        "ds_write_b32 v244, v253\n"
        "v_mfma_f32_16x16x32_bf16 v[32:35], a[188:191], a[140:143], v[32:35]\n"
        "v_cvt_pk_bf16_f32 v172, v24, v28\n"
        "v_mfma_f32_16x16x32_bf16 v[36:39], a[188:191], a[0:3], 0\n"
        "v_mfma_f32_16x16x32_bf16 v[36:39], a[184:187], a[4:7], v[36:39]\n"
        "v_mfma_f32_16x16x32_bf16 v[36:39], a[180:183], a[8:11], v[36:39]\n"
        "v_cvt_pk_bf16_f32 v176, v25, v29\n"
        "v_mfma_f32_16x16x32_bf16 v[36:39], a[176:179], a[12:15], v[36:39]\n"
        "v_mfma_f32_16x16x32_bf16 v[36:39], a[172:175], a[16:19], v[36:39]\n"
        "v_mfma_f32_16x16x32_bf16 v[36:39], a[168:171], a[20:23], v[36:39]\n"
        "s_waitcnt lgkmcnt(0)\n"
        "s_barrier\n"
        "ds_read_b128 a[192:195], v243 offset:512\n"
        "v_cvt_pk_bf16_f32 v180, v26, v30\n"
        "v_mfma_f32_16x16x32_bf16 v[40:43], a[168:171], a[24:27], 0\n"
        "ds_read_b128 a[196:199], v243 offset:4608\n"
        "s_nop 0\n"
        "v_mfma_f32_16x16x32_bf16 v[40:43], a[172:175], a[28:31], v[40:43]\n"
        "ds_read_b128 a[200:203], v243 offset:8704\n"
        "v_cvt_pk_bf16_f32 v173, v32, v36\n"
        "v_mfma_f32_16x16x32_bf16 v[40:43], a[176:179], a[32:35], v[40:43]\n"
        "ds_read_b128 a[204:207], v243 offset:12800\n"
        "v_cvt_pk_bf16_f32 v177, v33, v37\n"
        "v_mfma_f32_16x16x32_bf16 v[40:43], a[180:183], a[36:39], v[40:43]\n"
        "buffer_store_dwordx2 v[172:173], v242, s[16:19], s35 offen offset:0 sc1\n"
        "v_mfma_f32_16x16x32_bf16 v[40:43], a[184:187], a[40:43], v[40:43]\n"
        "ds_read_b128 a[208:211], v243 offset:16896\n"
        "v_cvt_pk_bf16_f32 v181, v34, v38\n"
        "v_mfma_f32_16x16x32_bf16 v[40:43], a[188:191], a[44:47], v[40:43]\n"
        "ds_read_b128 a[212:215], v243 offset:20992\n"
        "v_cvt_pk_bf16_f32 v184, v27, v31\n"
        "v_mfma_f32_16x16x32_bf16 v[44:47], a[188:191], a[48:51], 0\n"
        "buffer_store_dwordx2 v[176:177], v242, s[16:19], s35 offen offset:384 sc1\n"
        "v_mfma_f32_16x16x32_bf16 v[44:47], a[184:187], a[52:55], v[44:47]\n"
        "ds_read_b128 v[100:103], v249\n"
        "v_cvt_pk_bf16_f32 v185, v35, v39\n"
        "v_mfma_f32_16x16x32_bf16 v[44:47], a[180:183], a[56:59], v[44:47]\n"
        "v_mfma_f32_16x16x32_bf16 v[44:47], a[176:179], a[60:63], v[44:47]\n"
        "buffer_store_dwordx2 v[180:181], v242, s[16:19], s35 offen offset:768 sc1\n"
        "v_mfma_f32_16x16x32_bf16 v[44:47], a[172:175], a[64:67], v[44:47]\n"
        "v_mfma_f32_16x16x32_bf16 v[44:47], a[168:171], a[68:71], v[44:47]\n"
        "s_waitcnt lgkmcnt(6)\n"
        "s_nop 1\n"
        "v_mfma_f32_16x16x32_bf16 v[0:3], a[192:195], a[72:75], 0\n"
        "s_waitcnt lgkmcnt(5)\n"
        "buffer_store_dwordx2 v[184:185], v242, s[16:19], s35 offen offset:1152 sc1\n"
        "v_mfma_f32_16x16x32_bf16 v[0:3], a[196:199], a[76:79], v[0:3]\n"
        "s_waitcnt lgkmcnt(4)\n"
        "s_nop 1\n"
        "v_mfma_f32_16x16x32_bf16 v[0:3], a[200:203], a[80:83], v[0:3]\n"
        "s_waitcnt lgkmcnt(3)\n"
        "v_cvt_pk_bf16_f32 v174, v40, v44\n"
        "v_cvt_pk_bf16_f32 v178, v41, v45\n"
        "v_mfma_f32_16x16x32_bf16 v[0:3], a[204:207], a[84:87], v[0:3]\n"
        "s_waitcnt lgkmcnt(2)\n"
        "buffer_store_dword v174, v250, s[16:19], s35 offen offset:0 sc1\n"
        "v_mfma_f32_16x16x32_bf16 v[0:3], a[208:211], a[88:91], v[0:3]\n"
        "s_waitcnt lgkmcnt(1)\n"
        "v_cvt_pk_bf16_f32 v182, v42, v46\n"
        "v_cvt_pk_bf16_f32 v186, v43, v47\n"
        "v_mfma_f32_16x16x32_bf16 v[0:3], a[212:215], a[92:95], v[0:3]\n"
        "v_mfma_f32_16x16x32_bf16 v[4:7], a[212:215], a[96:99], 0\n"
        "buffer_store_dword v178, v250, s[16:19], s35 offen offset:384 sc1\n"
        "v_mfma_f32_16x16x32_bf16 v[4:7], a[208:211], a[100:103], v[4:7]\n"
        "s_waitcnt lgkmcnt(0)\n"
        "v_maximum3_f32 v100, v100, v101, v102\n"
        "v_maximum3_f32 v100, v100, v103, v103\n"
        "v_mfma_f32_16x16x32_bf16 v[4:7], a[204:207], a[104:107], v[4:7]\n"
        "v_cmp_nge_f32 vcc, s25, v100\n"
        "v_mfma_f32_16x16x32_bf16 v[4:7], a[200:203], a[108:111], v[4:7]\n"
        "buffer_store_dword v182, v250, s[16:19], s35 offen offset:768 sc1\n"
        "v_mfma_f32_16x16x32_bf16 v[4:7], a[196:199], a[112:115], v[4:7]\n"
        "v_mfma_f32_16x16x32_bf16 v[4:7], a[192:195], a[116:119], v[4:7]\n"
        "v_mfma_f32_16x16x32_bf16 v[8:11], a[192:195], a[120:123], 0\n"
        "buffer_store_dword v186, v250, s[16:19], s35 offen offset:1152 sc1\n"
        "v_mfma_f32_16x16x32_bf16 v[8:11], a[196:199], a[124:127], v[8:11]\n"
        "v_mfma_f32_16x16x32_bf16 v[8:11], a[200:203], a[128:131], v[8:11]\n"
        "v_mfma_f32_16x16x32_bf16 v[8:11], a[204:207], a[132:135], v[8:11]\n"
        "v_mfma_f32_16x16x32_bf16 v[8:11], a[208:211], a[136:139], v[8:11]\n"
        "v_mfma_f32_16x16x32_bf16 v[8:11], a[212:215], a[140:143], v[8:11]\n"
        "v_mfma_f32_16x16x32_bf16 v[12:15], a[212:215], a[0:3], 0\n"
        "v_cvt_pk_bf16_f32 v192, v0, v4\n"
        "v_mfma_f32_16x16x32_bf16 v[12:15], a[208:211], a[4:7], v[12:15]\n"
        "v_cvt_pk_bf16_f32 v196, v1, v5\n"
        "v_mfma_f32_16x16x32_bf16 v[12:15], a[204:207], a[8:11], v[12:15]\n"
        "v_mfma_f32_16x16x32_bf16 v[12:15], a[200:203], a[12:15], v[12:15]\n"
        "v_cvt_pk_bf16_f32 v200, v2, v6\n"
        "v_mfma_f32_16x16x32_bf16 v[12:15], a[196:199], a[16:19], v[12:15]\n"
        "v_mfma_f32_16x16x32_bf16 v[12:15], a[192:195], a[20:23], v[12:15]\n"
        "v_cvt_pk_bf16_f32 v204, v3, v7\n"
        "s_nop 0\n"
        "v_mfma_f32_16x16x32_bf16 v[16:19], a[192:195], a[24:27], 0\n"
        "s_nop 2\n"
        "v_mfma_f32_16x16x32_bf16 v[16:19], a[196:199], a[28:31], v[16:19]\n"
        "v_mfma_f32_16x16x32_bf16 v[16:19], a[200:203], a[32:35], v[16:19]\n"
        "v_cvt_pk_bf16_f32 v193, v8, v12\n"
        "v_cvt_pk_bf16_f32 v197, v9, v13\n"
        "v_mfma_f32_16x16x32_bf16 v[16:19], a[204:207], a[36:39], v[16:19]\n"
        "buffer_store_dwordx2 v[192:193], v242, s[16:19], s36 offen offset:0 sc1\n"
        "v_mfma_f32_16x16x32_bf16 v[16:19], a[208:211], a[40:43], v[16:19]\n"
        "v_cvt_pk_bf16_f32 v201, v10, v14\n"
        "v_cvt_pk_bf16_f32 v205, v11, v15\n"
        "v_mfma_f32_16x16x32_bf16 v[16:19], a[212:215], a[44:47], v[16:19]\n"
        "buffer_store_dwordx2 v[196:197], v242, s[16:19], s36 offen offset:384 sc1\n"
        "v_mfma_f32_16x16x32_bf16 v[20:23], a[212:215], a[48:51], 0\n"
        "v_mfma_f32_16x16x32_bf16 v[20:23], a[208:211], a[52:55], v[20:23]\n"
        "buffer_store_dwordx2 v[200:201], v242, s[16:19], s36 offen offset:768 sc1\n"
        "v_mfma_f32_16x16x32_bf16 v[20:23], a[204:207], a[56:59], v[20:23]\n"
        "v_mfma_f32_16x16x32_bf16 v[20:23], a[200:203], a[60:63], v[20:23]\n"
        "buffer_store_dwordx2 v[204:205], v242, s[16:19], s36 offen offset:1152 sc1\n"
        "v_mfma_f32_16x16x32_bf16 v[20:23], a[196:199], a[64:67], v[20:23]\n"
        "v_mfma_f32_16x16x32_bf16 v[20:23], a[192:195], a[68:71], v[20:23]\n"
        "s_nop 2\n"
        "s_nop 4\n"
        "v_cvt_pk_bf16_f32 v194, v16, v20\n"
        "buffer_store_dword v194, v250, s[16:19], s36 offen offset:0 sc1\n"
        "v_cvt_pk_bf16_f32 v198, v17, v21\n"
        "buffer_store_dword v198, v250, s[16:19], s36 offen offset:384 sc1\n"
        "v_cvt_pk_bf16_f32 v202, v18, v22\n"
        "buffer_store_dword v202, v250, s[16:19], s36 offen offset:768 sc1\n"
        "v_cvt_pk_bf16_f32 v206, v19, v23\n"
        "buffer_store_dword v206, v250, s[16:19], s36 offen offset:1152 sc1\n"
        "s_cmp_lg_u64 vcc, 0\n"
        "s_cbranch_scc1 30f\n"
        "s_endpgm\n"
        "30:\n"
        : "+{v0}"(tid), "=&{s42}"(w)
        : "{s[0:1]}"(__builtin_amdgcn_kernarg_segment_ptr()), "{s2}"(blockIdx.x), "s"(ldsb)
        : "v1", "v2", "v3", "v4", "v5", "v6", "v7", "v8", "v9", "v10", "v11", "v12", "v13", "v14", "v15", "v16", "v17", "v18", "v19", "v20", "v21", "v22", "v23", "v24", "v25", "v26", "v27", "v28", "v29", "v30", "v31", "v32", "v33", "v34", "v35", "v36", "v37", "v38", "v39", "v40", "v41", "v42", "v43", "v44", "v45", "v46", "v47", "v48", "v49", "v50", "v51", "v52", "v53", "v54", "v55", "v56", "v57", "v58", "v59", "v60", "v61", "v62", "v63", "v64", "v65", "v66", "v67", "v68", "v69", "v70", "v71", "v72", "v73", "v74", "v75", "v76", "v77", "v78", "v79", "v80", "v81", "v82", "v83", "v84", "v85", "v86", "v87", "v88", "v89", "v90", "v91", "v92", "v93", "v94", "v95", "v96", "v97", "v98", "v99", "v100", "v101", "v102", "v103", "v104", "v105", "v106", "v107", "v108", "v109", "v110", "v111", "v112", "v113", "v114", "v115", "v116", "v117", "v118", "v119", "v120", "v121", "v122", "v123", "v124", "v125", "v126", "v127", "v128", "v129", "v130", "v131", "v132", "v133", "v134", "v135", "v136", "v137", "v138", "v139", "v140", "v141", "v142", "v143", "v144", "v145", "v146", "v147", "v148", "v149", "v150", "v151", "v152", "v153", "v154", "v155", "v156", "v157", "v158", "v159", "v160", "v161", "v162", "v163", "v164", "v165", "v166", "v167", "v168", "v169", "v170", "v171", "v172", "v173", "v174", "v175", "v176", "v177", "v178", "v179", "v180", "v181", "v182", "v183", "v184", "v185", "v186", "v187", "v188", "v189", "v190", "v191", "v192", "v193", "v194", "v195", "v196", "v197", "v198", "v199", "v200", "v201", "v202", "v203", "v204", "v205", "v206", "v207", "v208", "v209", "v210", "v211", "v212", "v213", "v214", "v215", "v216", "v217", "v218", "v219", "v220", "v221", "v222", "v223", "v224", "v225", "v226", "v227", "v228", "v229", "v230", "v231", "v232", "v233", "v234", "v235", "v236", "v237", "v238", "v239", "v240", "v241", "v242", "v243", "v244", "v245", "v246", "v247", "v248", "v249", "v250", "v251", "v252", "v253", "v254", "v255", "a0", "a1", "a2", "a3", "a4", "a5", "a6", "a7", "a8", "a9", "a10", "a11", "a12", "a13", "a14", "a15", "a16", "a17", "a18", "a19", "a20", "a21", "a22", "a23", "a24", "a25", "a26", "a27", "a28", "a29", "a30", "a31", "a32", "a33", "a34", "a35", "a36", "a37", "a38", "a39", "a40", "a41", "a42", "a43", "a44", "a45", "a46", "a47", "a48", "a49", "a50", "a51", "a52", "a53", "a54", "a55", "a56", "a57", "a58", "a59", "a60", "a61", "a62", "a63", "a64", "a65", "a66", "a67", "a68", "a69", "a70", "a71", "a72", "a73", "a74", "a75", "a76", "a77", "a78", "a79", "a80", "a81", "a82", "a83", "a84", "a85", "a86", "a87", "a88", "a89", "a90", "a91", "a92", "a93", "a94", "a95", "a96", "a97", "a98", "a99", "a100", "a101", "a102", "a103", "a104", "a105", "a106", "a107", "a108", "a109", "a110", "a111", "a112", "a113", "a114", "a115", "a116", "a117", "a118", "a119", "a120", "a121", "a122", "a123", "a124", "a125", "a126", "a127", "a128", "a129", "a130", "a131", "a132", "a133", "a134", "a135", "a136", "a137", "a138", "a139", "a140", "a141", "a142", "a143", "a144", "a145", "a146", "a147", "a148", "a149", "a150", "a151", "a152", "a153", "a154", "a155", "a156", "a157", "a158", "a159", "a160", "a161", "a162", "a163", "a164", "a165", "a166", "a167", "a168", "a169", "a170", "a171", "a172", "a173", "a174", "a175", "a176", "a177", "a178", "a179", "a180", "a181", "a182", "a183", "a184", "a185", "a186", "a187", "a188", "a189", "a190", "a191", "a192", "a193", "a194", "a195", "a196", "a197", "a198", "a199", "a200", "a201", "a202", "a203", "a204", "a205", "a206", "a207", "a208", "a209", "a210", "a211", "a212", "a213", "a214", "a215", "a216", "a217", "a218", "a219", "a220", "a221", "a222", "a223", "a224", "a225", "a226", "a227", "a228", "a229", "a230", "a231", "a232", "a233", "a234", "a235", "a236", "a237", "a238", "a239", "a240", "a241", "a242", "a243", "a244", "a245", "a246", "a247", "a248", "a249", "a250", "a251", "a252", "a253", "a254", "a255", "s8", "s9", "s10", "s11", "s12", "s13", "s14", "s15", "s16", "s17", "s18", "s19", "s20", "s21", "s22", "s23", "s24", "s25", "s26", "s27", "s28", "s29", "s30", "s31", "s34", "s35", "s36", "s37", "s38", "s39", "s40", "s41", "s43", "s44", "s45", "s46", "s47", "s48", "s49", "s50", "s51", "s52", "s53", "s54", "s55", "s56", "s57", "s58", "s59", "s60", "s61", "s62", "s63", "s64", "s65", "s66", "s67", "vcc", "scc", "memory");
    {
        unsigned l2;
        asm volatile("s_waitcnt vmcnt(0) lgkmcnt(0)\n s_barrier\n v_mbcnt_lo_u32_b32 %0, -1, 0\n v_mbcnt_hi_u32_b32 %0, -1, %0\n s_nop 1"
                     : "=v"(l2) :: "memory");
        guarded_pass(x, wqkv, wo, rpb, rpi, mask, out, ldsb, l2, w);
    }
}


extern "C" void kernel_launch(void* const* d_in, const int* in_sizes, int n_in, void* d_out, int out_size, void* d_ws,
                              size_t ws_size, hipStream_t stream) {
    win_attn_kernel<<<256, 256, 0, stream>>>((const char*)d_in[0], (const char*)d_in[1], (const char*)d_in[2],
                                             (const char*)d_in[3], (const char*)d_in[4], (const char*)d_in[5],
                                             (char*)d_out);

}
```

```cpp
#include <hip/hip_runtime.h>
#include <hip/hip_bf16.h>

typedef unsigned int u32x4 __attribute__((ext_vector_type(4)));

__device__ __forceinline__ void guarded_pass(const char* x, const char* wqkv, const char* wo, const char* rpb, const char* rpi,
                                             const char* mask, char* out, unsigned ldsb, const unsigned lane, const unsigned w) {
    const unsigned wx = blockIdx.x & 63u, wy0 = blockIdx.x >> 6;
    unsigned dm[3], wsrc[3];
#pragma unroll
    for (int p = 0; p < 3; ++p) {
        unsigned gch = 64u * p + lane, t = gch / 24u, c1 = gch % 24u;
        dm[p] = ((8u * wx + 4u + t) & 511u) * 384u + ((c1 ^ (((t >> 1) & 1u) | (w << 1))) << 4);
        wsrc[p] = t * 1152u + ((c1 ^ (2u * (((t >> 1) & 1u) | ((w & 1u) << 1)))) << 4) - 1024u * p;
    }
    u32x4 srdx = {(unsigned)(size_t)x, (unsigned)((size_t)x >> 32) & 0xffffu, 100663296u, 0x20000u};
    u32x4 srdw = {(unsigned)(size_t)wqkv, (unsigned)((size_t)wqkv >> 32) & 0xffffu, 221184u, 0x20000u};
    unsigned ya = (8u * wy0 + 36u + w) * 196608u;
    unsigned wv = w;
    unsigned w3 = w * 3072u, w9 = w * 9216u;
    __builtin_amdgcn_sched_barrier(0);
    asm volatile(
        "s_nop 4\n"
        "s_add_u32 m0, s40, 0\n"
        "s_add_u32 s64, s65, 0\n"
        "buffer_load_dwordx4 v128, s[20:23], s64 offen lds\n"
        "buffer_load_dwordx4 v129, s[20:23], s64 offen offset:1024 lds\n"
        "buffer_load_dwordx4 v130, s[20:23], s64 offen offset:2048 lds\n"
        "s_add_u32 m0, s40, 12288\n"
        "s_add_u32 s64, s65, 36864\n"
        "buffer_load_dwordx4 v128, s[20:23], s64 offen lds\n"
        "buffer_load_dwordx4 v129, s[20:23], s64 offen offset:1024 lds\n"
        "buffer_load_dwordx4 v130, s[20:23], s64 offen offset:2048 lds\n"
        "s_add_u32 m0, s40, 24576\n"
        "s_add_u32 s64, s65, 73728\n"
        "buffer_load_dwordx4 v128, s[20:23], s64 offen lds\n"
        "buffer_load_dwordx4 v129, s[20:23], s64 offen offset:1024 lds\n"
        "buffer_load_dwordx4 v130, s[20:23], s64 offen offset:2048 lds\n"
        :: "{v242}"(dm[0]), "{v243}"(dm[1]), "{v244}"(dm[2]), "{v128}"(wsrc[0]), "{v129}"(wsrc[1]), "{v130}"(wsrc[2]), "{s[8:11]}"(srdx), "{s[20:23]}"(srdw), "{s47}"(ya), "{s40}"(w3), "{s65}"(w9), "s"(ldsb)
        : "s64", "scc", "memory");
    __builtin_amdgcn_sched_barrier(0);

    const unsigned rho = lane & 15u, g = lane >> 4;
    const unsigned cg = w >> 1, T = w & 1u;
    const unsigned ql = 8u * (rho >> 2) + (rho & 3u);
    unsigned xr[2], mr[2];
#pragma unroll
    for (int j = 0; j < 2; ++j) {
        xr[j] = 73728u + ql * 384u + (((4u * j + g) ^ (rho >> 1)) << 4);
        mr[j] = 147456u + 8192u + ql * 128u + (((4u * j + g) ^ (rho >> 1)) << 4);
    }
    unsigned ow = 122880u + (4u * w + g) * 1024u + rho * 16u;
    unsigned ow2 = 122880u + (16u + 2u * w + (g >> 1)) * 1024u + rho * 16u + (g & 1u) * 8u;
    unsigned ord = 122880u + g * 1024u + (32u * T + rho) * 16u;
    __builtin_amdgcn_sched_barrier(0);
    asm volatile(
        "s_nop 4\n"
        "s_add_u32 m0, s40, 36864\n"
        "s_add_u32 s64, s65, 110592\n"
        "buffer_load_dwordx4 v128, s[20:23], s64 offen lds\n"
        "buffer_load_dwordx4 v129, s[20:23], s64 offen offset:1024 lds\n"
        "buffer_load_dwordx4 v130, s[20:23], s64 offen offset:2048 lds\n"
        :: "{v242}"(dm[0]), "{v243}"(dm[1]), "{v244}"(dm[2]), "{v128}"(wsrc[0]), "{v129}"(wsrc[1]), "{v130}"(wsrc[2]), "{s[8:11]}"(srdx), "{s[20:23]}"(srdw), "{s47}"(ya), "{s40}"(w3), "{s65}"(w9), "s"(ldsb)
        : "s64", "scc", "memory");
    __builtin_amdgcn_sched_barrier(0);
    unsigned wor = 0u + cg * 18432u + g * 1536u + rho * 96u + (g & 1u) * 16u;
    unsigned wor2 = 0u + cg * 18432u + g * 1536u + rho * 96u - (g & 1u) * 16u;
    unsigned ml = 147456u + w * 2048u + lane * 16u;
    unsigned ms = (lane >> 3) * 128u + (((lane & 7u) ^ (((lane >> 4) & 1u) | ((w & 1u) << 2))) << 4);
    unsigned st = g * 196608u + cg * 192u + rho * 12u;
    unsigned l16 = lane * 16u;
    unsigned id0 = (rho == 4u * g ? 0x3f80u : 0u) | (rho == 4u * g + 1u ? 0x3f800000u : 0u);
    unsigned id1 = (rho == 4u * g + 2u ? 0x3f80u : 0u) | (rho == 4u * g + 3u ? 0x3f800000u : 0u);
    unsigned x0 = (8u * wx + 4u) * 384u, x1 = ((8u * wx + 8u) & 511u) * 384u;
    u32x4 srdm = {(unsigned)(size_t)mask, (unsigned)((size_t)mask >> 32) & 0xffffu, 33554432u, 0x20000u};
    u32x4 srdo = {(unsigned)(size_t)out, (unsigned)((size_t)out >> 32) & 0xffffu, 100663296u, 0x20000u};
    u32x4 srdwo = {(unsigned)(size_t)wo, (unsigned)((size_t)wo >> 32) & 0xffffu, 73728u, 0x20000u};
    u32x4 srdrp = {(unsigned)(size_t)rpi, (unsigned)((size_t)rpi >> 32) & 0xffffu, 16384u, 0x20000u};
    u32x4 srdrb = {(unsigned)(size_t)rpb, (unsigned)((size_t)rpb >> 32) & 0xffffu, 1800u, 0x20000u};
    __builtin_amdgcn_sched_barrier(0);
    asm volatile(
        "s_nop 4\n"
        "s_add_u32 m0, s40, 49152\n"
        "s_add_u32 s64, s65, 147456\n"
        "buffer_load_dwordx4 v128, s[20:23], s64 offen lds\n"
        "buffer_load_dwordx4 v129, s[20:23], s64 offen offset:1024 lds\n"
        "buffer_load_dwordx4 v130, s[20:23], s64 offen offset:2048 lds\n"
        :: "{v242}"(dm[0]), "{v243}"(dm[1]), "{v244}"(dm[2]), "{v128}"(wsrc[0]), "{v129}"(wsrc[1]), "{v130}"(wsrc[2]), "{s[8:11]}"(srdx), "{s[20:23]}"(srdw), "{s47}"(ya), "{s40}"(w3), "{s65}"(w9), "s"(ldsb)
        : "s64", "scc", "memory");
    __builtin_amdgcn_sched_barrier(0);
    const unsigned q4 = (lane >> 2) & 3u, p4 = lane & 3u;
    unsigned wrd[3];
#pragma unroll
    for (int p = 0; p < 3; ++p) {
        unsigned c = 6u * w + 2u * p + (p4 >> 1);
        wrd[p] = (8u * g + q4) * 384u + ((c ^ (2u * ((q4 >> 1) | ((g & 1u) << 1)))) << 4) + (p4 & 1u) * 8u;
    }
    unsigned od = cg * 18432u + g * 1536u + (((48u * T + rho) ^ (g & 1u)) << 4);
    unsigned rbs = lane * 8u + 2u * w, rbw = 151552u + w * 1024u + lane * 4u;
    const unsigned pfi = ((blockIdx.x >> 3) & 31u) * 4u + w;
    unsigned pf0 = pfi * 2048u + lane * 16u, pf1 = pfi * 1024u + lane * 16u;
    __builtin_amdgcn_sched_barrier(0);
    asm volatile(
        "s_nop 4\n"
        "s_add_u32 m0, s40, 61440\n"
        "s_add_u32 s64, s65, 184320\n"
        "buffer_load_dwordx4 v128, s[20:23], s64 offen lds\n"
        "buffer_load_dwordx4 v129, s[20:23], s64 offen offset:1024 lds\n"
        "buffer_load_dwordx4 v130, s[20:23], s64 offen offset:2048 lds\n"
        :: "{v242}"(dm[0]), "{v243}"(dm[1]), "{v244}"(dm[2]), "{v128}"(wsrc[0]), "{v129}"(wsrc[1]), "{v130}"(wsrc[2]), "{s[8:11]}"(srdx), "{s[20:23]}"(srdw), "{s47}"(ya), "{s40}"(w3), "{s65}"(w9), "s"(ldsb)
        : "s64", "scc", "memory");
    __builtin_amdgcn_sched_barrier(0);
    unsigned rs = g * 256u + ((rho ^ (g | ((w & 1u) << 3))) << 4);
    unsigned rr = 135168u + ql * 256u + (((2u * g) ^ rho) << 4);
    unsigned oa = (4u * g + q4) * 384u + 96u * w + p4 * 8u;
    unsigned ob = (48u * (g >> 1) + 8u * (g & 1u) + q4) * 384u + 96u * w + p4 * 8u;
    unsigned mk = (blockIdx.x + 256u) * 8192u + w * 2048u;
    unsigned raw = (8u * wy0 + 4u + 4u * T) * 196608u;
    asm volatile(
        "s_nop 4\n"
        "s_mul_i32 s40, s42, 3072\n"
        "s_mul_i32 s65, s42, 9216\n"
        "s_lshl_b32 s48, s42, 11\n"
        "s_add_u32 s48, s48, 147456\n"
        "s_mov_b32 s24, 0x3fb8aa3b\n"
        "s_mov_b32 s25, 0xbfb8aa3b\n"
        "s_mov_b32 s26, 0xff7fff7f\n"
        "s_mov_b32 s49, 0x3e13cd3a\n"
        "s_mov_b32 s50, 0x3e13cd3a\n"
        "s_mov_b32 s51, 0x3e13cd3a\n"
        "s_mov_b32 s27, 14\n"
        "s_mov_b32 s28, 73728\n"
        "s_mov_b32 s29, 122880\n"
        "s_mov_b32 s30, 98304\n"
        "s_mov_b32 s38, 0x80000000\n"
        "s_mov_b32 s39, 0x80000000\n"
        "s_lshl_b32 s67, s42, 10\n"
        "s_add_u32 s67, s67, 151552\n"
        "v_mov_b32 v188, v170\n"
        "v_mov_b32 v189, v171\n"
        "v_mov_b32 v210, v170\n"
        "v_mov_b32 v211, v171\n"
        "v_mov_b32 v228, v170\n"
        "v_mov_b32 v229, v171\n"
        "v_accvgpr_write_b32 a252, v140\n"
        "v_add_u32 v136, 98304, v131\n"
        "v_accvgpr_write_b32 a253, v141\n"
        "v_add_u32 v137, 98304, v132\n"
        "v_accvgpr_write_b32 a254, v147\n"
        "v_add_u32 v138, 98304, v133\n"
        "v_mov_b32 v150, v240\n"
        "v_mov_b32 v151, v241\n"
        "v_xor_b32 v253, 64, v139\n"
        "v_xor_b32 v232, 0, v144\n"
        "v_xor_b32 v233, 16, v144\n"
        "v_xor_b32 v234, 128, v144\n"
        "v_xor_b32 v235, 144, v144\n"
        "s_lshl_b32 s46, s42, 12\n"
        "buffer_load_dwordx4 v[64:67], v148, s[20:23], 0 offen\n"
        "buffer_load_dwordx4 v[68:71], v148, s[20:23], 0 offen offset:1024\n"
        "buffer_load_dwordx4 v[72:75], v149, s[52:55], 0 offen\n"
        "s_waitcnt vmcnt(12)\n"
        "s_barrier\n"
        "s_add_u32 m0, s40, 73728\n"
        "ds_read_b64_tr_b16 v[76:77], v131 offset:0\n"
        "ds_read_b64_tr_b16 v[78:79], v131 offset:1536\n"
        "ds_read_b64_tr_b16 v[80:81], v131 offset:12288\n"
        "ds_read_b64_tr_b16 v[82:83], v131 offset:13824\n"
        "ds_read_b64_tr_b16 v[84:85], v131 offset:24576\n"
        "ds_read_b64_tr_b16 v[86:87], v131 offset:26112\n"
        "ds_read_b64_tr_b16 v[88:89], v132 offset:0\n"
        "ds_read_b64_tr_b16 v[90:91], v132 offset:1536\n"
        "ds_read_b64_tr_b16 v[92:93], v132 offset:12288\n"
        "ds_read_b64_tr_b16 v[94:95], v132 offset:13824\n"
        "ds_read_b64_tr_b16 v[104:105], v132 offset:24576\n"
        "ds_read_b64_tr_b16 v[106:107], v132 offset:26112\n"
        "ds_read_b64_tr_b16 v[108:109], v133 offset:0\n"
        "ds_read_b64_tr_b16 v[110:111], v133 offset:1536\n"
        "ds_read_b64_tr_b16 v[116:117], v133 offset:12288\n"
        "ds_read_b64_tr_b16 v[118:119], v133 offset:13824\n"
        "ds_read_b64_tr_b16 v[120:121], v133 offset:24576\n"
        "ds_read_b64_tr_b16 v[122:123], v133 offset:26112\n"
        "s_sub_u32 s64, s47, 6291456\n"
        "buffer_load_dwordx4 v242, s[8:11], s64 offen lds\n"
        "s_add_u32 m0, s40, 74752\n"
        "s_sub_u32 s64, s47, 6291456\n"
        "buffer_load_dwordx4 v243, s[8:11], s64 offen lds\n"
        "s_add_u32 m0, s40, 75776\n"
        "s_sub_u32 s64, s47, 6291456\n"
        "buffer_load_dwordx4 v244, s[8:11], s64 offen lds\n"
        "s_add_u32 m0, s40, 86016\n"
        "s_sub_u32 s64, s47, 5505024\n"
        "buffer_load_dwordx4 v242, s[8:11], s64 offen lds\n"
        "s_add_u32 m0, s40, 87040\n"
        "s_sub_u32 s64, s47, 5505024\n"
        "buffer_load_dwordx4 v243, s[8:11], s64 offen lds\n"
        "s_add_u32 m0, s40, 88064\n"
        "s_sub_u32 s64, s47, 5505024\n"
        "buffer_load_dwordx4 v244, s[8:11], s64 offen lds\n"
        "s_waitcnt lgkmcnt(15)\n"
        "v_lshlrev_b32 v0, 16, v76\n"
        "v_lshlrev_b32 v2, 16, v77\n"
        "v_lshlrev_b32 v4, 16, v78\n"
        "v_lshlrev_b32 v6, 16, v79\n"
        "v_and_b32 v1, 0xffff0000, v76\n"
        "v_and_b32 v3, 0xffff0000, v77\n"
        "v_and_b32 v5, 0xffff0000, v78\n"
        "v_and_b32 v7, 0xffff0000, v79\n"
        "v_pk_mul_f32 v[0:1], v[0:1], s[50:51]\n"
        "v_pk_mul_f32 v[2:3], v[2:3], s[50:51]\n"
        "v_pk_mul_f32 v[4:5], v[4:5], s[50:51]\n"
        "v_pk_mul_f32 v[6:7], v[6:7], s[50:51]\n"
        "v_cvt_pk_bf16_f32 v0, v0, v1\n"
        "v_cvt_pk_bf16_f32 v2, v2, v3\n"
        "v_cvt_pk_bf16_f32 v4, v4, v5\n"
        "v_cvt_pk_bf16_f32 v6, v6, v7\n"
        "v_accvgpr_write_b32 a0, v0\n"
        "v_accvgpr_write_b32 a1, v2\n"
        "v_accvgpr_write_b32 a2, v4\n"
        "v_accvgpr_write_b32 a3, v6\n"
        "v_lshlrev_b32 v8, 16, v80\n"
        "v_lshlrev_b32 v10, 16, v81\n"
        "s_waitcnt lgkmcnt(14)\n"
        "v_lshlrev_b32 v12, 16, v82\n"
        "v_lshlrev_b32 v14, 16, v83\n"
        "v_and_b32 v9, 0xffff0000, v80\n"
        "v_and_b32 v11, 0xffff0000, v81\n"
        "v_and_b32 v13, 0xffff0000, v82\n"
        "v_and_b32 v15, 0xffff0000, v83\n"
        "v_pk_mul_f32 v[8:9], v[8:9], s[50:51]\n"
        "v_pk_mul_f32 v[10:11], v[10:11], s[50:51]\n"
        "v_pk_mul_f32 v[12:13], v[12:13], s[50:51]\n"
        "v_pk_mul_f32 v[14:15], v[14:15], s[50:51]\n"
        "v_cvt_pk_bf16_f32 v8, v8, v9\n"
        "v_cvt_pk_bf16_f32 v10, v10, v11\n"
        "v_cvt_pk_bf16_f32 v12, v12, v13\n"
        "v_cvt_pk_bf16_f32 v14, v14, v15\n"
        "v_accvgpr_write_b32 a4, v8\n"
        "v_accvgpr_write_b32 a5, v10\n"
        "v_accvgpr_write_b32 a6, v12\n"
        "v_accvgpr_write_b32 a7, v14\n"
        "s_waitcnt lgkmcnt(13)\n"
        "v_lshlrev_b32 v0, 16, v84\n"
        "v_lshlrev_b32 v2, 16, v85\n"
        "s_waitcnt lgkmcnt(12)\n"
        "v_lshlrev_b32 v4, 16, v86\n"
        "v_lshlrev_b32 v6, 16, v87\n"
        "v_and_b32 v1, 0xffff0000, v84\n"
        "v_and_b32 v3, 0xffff0000, v85\n"
        "v_and_b32 v5, 0xffff0000, v86\n"
        "v_and_b32 v7, 0xffff0000, v87\n"
        "v_pk_mul_f32 v[0:1], v[0:1], s[50:51]\n"
        "v_pk_mul_f32 v[2:3], v[2:3], s[50:51]\n"
        "v_pk_mul_f32 v[4:5], v[4:5], s[50:51]\n"
        "v_pk_mul_f32 v[6:7], v[6:7], s[50:51]\n"
        "v_cvt_pk_bf16_f32 v0, v0, v1\n"
        "v_cvt_pk_bf16_f32 v2, v2, v3\n"
        "v_cvt_pk_bf16_f32 v4, v4, v5\n"
        "v_cvt_pk_bf16_f32 v6, v6, v7\n"
        "v_accvgpr_write_b32 a8, v0\n"
        "v_accvgpr_write_b32 a9, v2\n"
        "v_accvgpr_write_b32 a10, v4\n"
        "v_accvgpr_write_b32 a11, v6\n"
        "s_waitcnt lgkmcnt(11)\n"
        "v_lshlrev_b32 v8, 16, v88\n"
        "v_lshlrev_b32 v10, 16, v89\n"
        "s_waitcnt lgkmcnt(10)\n"
        "v_lshlrev_b32 v12, 16, v90\n"
        "v_lshlrev_b32 v14, 16, v91\n"
        "buffer_load_ushort v236, v145, s[60:63], 0 offen offset:0\n"
        "buffer_load_ushort v237, v145, s[60:63], 0 offen offset:512\n"
        "buffer_load_ushort v238, v145, s[60:63], 0 offen offset:1024\n"
        "buffer_load_ushort v239, v145, s[60:63], 0 offen offset:1536\n"
        "v_and_b32 v9, 0xffff0000, v88\n"
        "v_and_b32 v11, 0xffff0000, v89\n"
        "v_and_b32 v13, 0xffff0000, v90\n"
        "v_and_b32 v15, 0xffff0000, v91\n"
        "v_pk_mul_f32 v[8:9], v[8:9], s[50:51]\n"
        "v_pk_mul_f32 v[10:11], v[10:11], s[50:51]\n"
        "v_pk_mul_f32 v[12:13], v[12:13], s[50:51]\n"
        "v_pk_mul_f32 v[14:15], v[14:15], s[50:51]\n"
        "s_add_u32 m0, s46, 135168\n"
        "v_cvt_pk_bf16_f32 v8, v8, v9\n"
        "v_cvt_pk_bf16_f32 v10, v10, v11\n"
        "v_cvt_pk_bf16_f32 v12, v12, v13\n"
        "v_cvt_pk_bf16_f32 v14, v14, v15\n"
        "s_mov_b32 s64, s46\n"
        "v_accvgpr_write_b32 a24, v8\n"
        "v_accvgpr_write_b32 a25, v10\n"
        "v_accvgpr_write_b32 a26, v12\n"
        "v_accvgpr_write_b32 a27, v14\n"
        "buffer_load_dwordx4 v139, s[56:59], s64 offen lds\n"
        "s_waitcnt lgkmcnt(9)\n"
        "v_lshlrev_b32 v0, 16, v92\n"
        "v_lshlrev_b32 v2, 16, v93\n"
        "s_waitcnt lgkmcnt(8)\n"
        "v_lshlrev_b32 v4, 16, v94\n"
        "v_lshlrev_b32 v6, 16, v95\n"
        "s_waitcnt lgkmcnt(0)\n"
        "s_waitcnt vmcnt(14)\n"
        "s_barrier\n"
        "v_and_b32 v1, 0xffff0000, v92\n"
        "v_and_b32 v3, 0xffff0000, v93\n"
        "v_and_b32 v5, 0xffff0000, v94\n"
        "v_and_b32 v7, 0xffff0000, v95\n"
        "ds_read_b64_tr_b16 v[152:153], v131 offset:36864\n"
        "ds_read_b64_tr_b16 v[154:155], v131 offset:38400\n"
        "ds_read_b64_tr_b16 v[172:173], v131 offset:49152\n"
        "ds_read_b64_tr_b16 v[174:175], v131 offset:50688\n"
        "ds_read_b64_tr_b16 v[192:193], v131 offset:61440\n"
        "ds_read_b64_tr_b16 v[194:195], v131 offset:62976\n"
        "ds_read_b64_tr_b16 v[212:213], v132 offset:36864\n"
        "ds_read_b64_tr_b16 v[214:215], v132 offset:38400\n"
        "ds_read_b64_tr_b16 v[156:157], v132 offset:49152\n"
        "ds_read_b64_tr_b16 v[158:159], v132 offset:50688\n"
        "ds_read_b64_tr_b16 v[176:177], v132 offset:61440\n"
        "ds_read_b64_tr_b16 v[178:179], v132 offset:62976\n"
        "ds_read_b64_tr_b16 v[196:197], v133 offset:36864\n"
        "ds_read_b64_tr_b16 v[198:199], v133 offset:38400\n"
        "ds_read_b64_tr_b16 v[216:217], v133 offset:49152\n"
        "ds_read_b64_tr_b16 v[218:219], v133 offset:50688\n"
        "ds_read_b64_tr_b16 v[124:125], v133 offset:61440\n"
        "ds_read_b64_tr_b16 v[126:127], v133 offset:62976\n"
        "v_pk_mul_f32 v[0:1], v[0:1], s[50:51]\n"
        "v_pk_mul_f32 v[2:3], v[2:3], s[50:51]\n"
        "v_pk_mul_f32 v[4:5], v[4:5], s[50:51]\n"
        "v_pk_mul_f32 v[6:7], v[6:7], s[50:51]\n"
        "v_cvt_pk_bf16_f32 v0, v0, v1\n"
        "v_cvt_pk_bf16_f32 v2, v2, v3\n"
        "v_cvt_pk_bf16_f32 v4, v4, v5\n"
        "v_cvt_pk_bf16_f32 v6, v6, v7\n"
        "buffer_load_dwordx4 v139, s[56:59], s64 offen offset:1024 lds\n"
        "v_accvgpr_write_b32 a28, v0\n"
        "v_accvgpr_write_b32 a29, v2\n"
        "v_accvgpr_write_b32 a30, v4\n"
        "v_accvgpr_write_b32 a31, v6\n"
        "v_lshlrev_b32 v8, 16, v104\n"
        "v_lshlrev_b32 v10, 16, v105\n"
        "v_lshlrev_b32 v12, 16, v106\n"
        "v_lshlrev_b32 v14, 16, v107\n"
        "v_and_b32 v9, 0xffff0000, v104\n"
        "v_and_b32 v11, 0xffff0000, v105\n"
        "v_and_b32 v13, 0xffff0000, v106\n"
        "v_and_b32 v15, 0xffff0000, v107\n"
        "v_pk_mul_f32 v[8:9], v[8:9], s[50:51]\n"
        "v_pk_mul_f32 v[10:11], v[10:11], s[50:51]\n"
        "v_pk_mul_f32 v[12:13], v[12:13], s[50:51]\n"
        "v_pk_mul_f32 v[14:15], v[14:15], s[50:51]\n"
        "buffer_load_dwordx4 v253, s[56:59], s64 offen offset:2048 lds\n"
        "v_cvt_pk_bf16_f32 v8, v8, v9\n"
        "v_cvt_pk_bf16_f32 v10, v10, v11\n"
        "v_cvt_pk_bf16_f32 v12, v12, v13\n"
        "v_cvt_pk_bf16_f32 v14, v14, v15\n"
        "v_accvgpr_write_b32 a32, v8\n"
        "v_accvgpr_write_b32 a33, v10\n"
        "v_accvgpr_write_b32 a34, v12\n"
        "v_accvgpr_write_b32 a35, v14\n"
        "v_lshlrev_b32 v0, 16, v108\n"
        "v_lshlrev_b32 v2, 16, v109\n"
        "v_lshlrev_b32 v4, 16, v110\n"
        "v_lshlrev_b32 v6, 16, v111\n"
        "v_and_b32 v1, 0xffff0000, v108\n"
        "v_and_b32 v3, 0xffff0000, v109\n"
        "v_and_b32 v5, 0xffff0000, v110\n"
        "v_and_b32 v7, 0xffff0000, v111\n"
        "buffer_load_dwordx4 v253, s[56:59], s64 offen offset:3072 lds\n"
        "v_pk_mul_f32 v[0:1], v[0:1], s[50:51]\n"
        "v_pk_mul_f32 v[2:3], v[2:3], s[50:51]\n"
        "v_pk_mul_f32 v[4:5], v[4:5], s[50:51]\n"
        "v_pk_mul_f32 v[6:7], v[6:7], s[50:51]\n"
        "v_cvt_pk_bf16_f32 v0, v0, v1\n"
        "v_cvt_pk_bf16_f32 v2, v2, v3\n"
        "v_cvt_pk_bf16_f32 v4, v4, v5\n"
        "v_cvt_pk_bf16_f32 v6, v6, v7\n"
        "v_accvgpr_write_b32 a48, v0\n"
        "v_accvgpr_write_b32 a49, v2\n"
        "v_accvgpr_write_b32 a50, v4\n"
        "v_accvgpr_write_b32 a51, v6\n"
        "v_lshlrev_b32 v8, 16, v116\n"
        "v_lshlrev_b32 v10, 16, v117\n"
        "v_lshlrev_b32 v12, 16, v118\n"
        "v_lshlrev_b32 v14, 16, v119\n"
        "v_and_b32 v9, 0xffff0000, v116\n"
        "v_and_b32 v11, 0xffff0000, v117\n"
        "v_and_b32 v13, 0xffff0000, v118\n"
        "v_and_b32 v15, 0xffff0000, v119\n"
        "v_pk_mul_f32 v[8:9], v[8:9], s[50:51]\n"
        "v_pk_mul_f32 v[10:11], v[10:11], s[50:51]\n"
        "v_pk_mul_f32 v[12:13], v[12:13], s[50:51]\n"
        "v_pk_mul_f32 v[14:15], v[14:15], s[50:51]\n"
        "v_cvt_pk_bf16_f32 v8, v8, v9\n"
        "v_cvt_pk_bf16_f32 v10, v10, v11\n"
        "v_cvt_pk_bf16_f32 v12, v12, v13\n"
        "v_cvt_pk_bf16_f32 v14, v14, v15\n"
        "v_accvgpr_write_b32 a52, v8\n"
        "v_accvgpr_write_b32 a53, v10\n"
        "v_accvgpr_write_b32 a54, v12\n"
        "v_accvgpr_write_b32 a55, v14\n"
        "v_lshlrev_b32 v0, 16, v120\n"
        "v_lshlrev_b32 v2, 16, v121\n"
        "v_lshlrev_b32 v4, 16, v122\n"
        "v_lshlrev_b32 v6, 16, v123\n"
        "v_and_b32 v1, 0xffff0000, v120\n"
        "v_and_b32 v3, 0xffff0000, v121\n"
        "v_and_b32 v5, 0xffff0000, v122\n"
        "v_and_b32 v7, 0xffff0000, v123\n"
        "v_pk_mul_f32 v[0:1], v[0:1], s[50:51]\n"
        "v_pk_mul_f32 v[2:3], v[2:3], s[50:51]\n"
        "v_pk_mul_f32 v[4:5], v[4:5], s[50:51]\n"
        "v_pk_mul_f32 v[6:7], v[6:7], s[50:51]\n"
        "v_cvt_pk_bf16_f32 v0, v0, v1\n"
        "v_cvt_pk_bf16_f32 v2, v2, v3\n"
        "v_cvt_pk_bf16_f32 v4, v4, v5\n"
        "v_cvt_pk_bf16_f32 v6, v6, v7\n"
        "v_accvgpr_write_b32 a56, v0\n"
        "v_accvgpr_write_b32 a57, v2\n"
        "v_accvgpr_write_b32 a58, v4\n"
        "v_accvgpr_write_b32 a59, v6\n"
        "s_waitcnt lgkmcnt(15)\n"
        "v_lshlrev_b32 v8, 16, v152\n"
        "v_lshlrev_b32 v10, 16, v153\n"
        "v_lshlrev_b32 v12, 16, v154\n"
        "v_lshlrev_b32 v14, 16, v155\n"
        "v_and_b32 v9, 0xffff0000, v152\n"
        "v_and_b32 v11, 0xffff0000, v153\n"
        "v_and_b32 v13, 0xffff0000, v154\n"
        "v_and_b32 v15, 0xffff0000, v155\n"
        "v_pk_mul_f32 v[8:9], v[8:9], s[50:51]\n"
        "v_pk_mul_f32 v[10:11], v[10:11], s[50:51]\n"
        "v_pk_mul_f32 v[12:13], v[12:13], s[50:51]\n"
        "v_pk_mul_f32 v[14:15], v[14:15], s[50:51]\n"
        "v_cvt_pk_bf16_f32 v8, v8, v9\n"
        "v_cvt_pk_bf16_f32 v10, v10, v11\n"
        "v_cvt_pk_bf16_f32 v12, v12, v13\n"
        "v_cvt_pk_bf16_f32 v14, v14, v15\n"
        "v_accvgpr_write_b32 a12, v8\n"
        "v_accvgpr_write_b32 a13, v10\n"
        "v_accvgpr_write_b32 a14, v12\n"
        "v_accvgpr_write_b32 a15, v14\n"
        "v_lshlrev_b32 v0, 16, v172\n"
        "v_lshlrev_b32 v2, 16, v173\n"
        "s_waitcnt lgkmcnt(14)\n"
        "v_lshlrev_b32 v4, 16, v174\n"
        "v_lshlrev_b32 v6, 16, v175\n"
        "v_and_b32 v1, 0xffff0000, v172\n"
        "v_and_b32 v3, 0xffff0000, v173\n"
        "v_and_b32 v5, 0xffff0000, v174\n"
        "v_and_b32 v7, 0xffff0000, v175\n"
        "v_pk_mul_f32 v[0:1], v[0:1], s[50:51]\n"
        "v_pk_mul_f32 v[2:3], v[2:3], s[50:51]\n"
        "v_pk_mul_f32 v[4:5], v[4:5], s[50:51]\n"
        "v_pk_mul_f32 v[6:7], v[6:7], s[50:51]\n"
        "v_cvt_pk_bf16_f32 v0, v0, v1\n"
        "v_cvt_pk_bf16_f32 v2, v2, v3\n"
        "v_cvt_pk_bf16_f32 v4, v4, v5\n"
        "v_cvt_pk_bf16_f32 v6, v6, v7\n"
        "v_accvgpr_write_b32 a16, v0\n"
        "v_accvgpr_write_b32 a17, v2\n"
        "v_accvgpr_write_b32 a18, v4\n"
        "v_accvgpr_write_b32 a19, v6\n"
        "s_waitcnt lgkmcnt(13)\n"
        "v_lshlrev_b32 v8, 16, v192\n"
        "v_lshlrev_b32 v10, 16, v193\n"
        "s_waitcnt lgkmcnt(12)\n"
        "v_lshlrev_b32 v12, 16, v194\n"
        "v_lshlrev_b32 v14, 16, v195\n"
        "v_and_b32 v9, 0xffff0000, v192\n"
        "v_and_b32 v11, 0xffff0000, v193\n"
        "v_and_b32 v13, 0xffff0000, v194\n"
        "v_and_b32 v15, 0xffff0000, v195\n"
        "v_pk_mul_f32 v[8:9], v[8:9], s[50:51]\n"
        "v_pk_mul_f32 v[10:11], v[10:11], s[50:51]\n"
        "v_pk_mul_f32 v[12:13], v[12:13], s[50:51]\n"
        "v_pk_mul_f32 v[14:15], v[14:15], s[50:51]\n"
        "v_cvt_pk_bf16_f32 v8, v8, v9\n"
        "v_cvt_pk_bf16_f32 v10, v10, v11\n"
        "v_cvt_pk_bf16_f32 v12, v12, v13\n"
        "v_cvt_pk_bf16_f32 v14, v14, v15\n"
        "v_accvgpr_write_b32 a20, v8\n"
        "v_accvgpr_write_b32 a21, v10\n"
        "v_accvgpr_write_b32 a22, v12\n"
        "v_accvgpr_write_b32 a23, v14\n"
        "s_waitcnt lgkmcnt(11)\n"
        "v_lshlrev_b32 v0, 16, v212\n"
        "v_lshlrev_b32 v2, 16, v213\n"
        "s_waitcnt lgkmcnt(10)\n"
        "v_lshlrev_b32 v4, 16, v214\n"
        "v_lshlrev_b32 v6, 16, v215\n"
        "v_and_b32 v1, 0xffff0000, v212\n"
        "v_and_b32 v3, 0xffff0000, v213\n"
        "v_and_b32 v5, 0xffff0000, v214\n"
        "v_and_b32 v7, 0xffff0000, v215\n"
        "v_pk_mul_f32 v[0:1], v[0:1], s[50:51]\n"
        "v_pk_mul_f32 v[2:3], v[2:3], s[50:51]\n"
        "v_pk_mul_f32 v[4:5], v[4:5], s[50:51]\n"
        "v_pk_mul_f32 v[6:7], v[6:7], s[50:51]\n"
        "v_cvt_pk_bf16_f32 v0, v0, v1\n"
        "v_cvt_pk_bf16_f32 v2, v2, v3\n"
        "v_cvt_pk_bf16_f32 v4, v4, v5\n"
        "v_cvt_pk_bf16_f32 v6, v6, v7\n"
        "s_add_u32 m0, s40, 98304\n"
        "v_accvgpr_write_b32 a36, v0\n"
        "v_accvgpr_write_b32 a37, v2\n"
        "v_accvgpr_write_b32 a38, v4\n"
        "v_accvgpr_write_b32 a39, v6\n"
        "s_add_u32 s64, s65, 384\n"
        "s_waitcnt lgkmcnt(9)\n"
        "v_lshlrev_b32 v8, 16, v156\n"
        "v_lshlrev_b32 v10, 16, v157\n"
        "s_waitcnt lgkmcnt(8)\n"
        "v_lshlrev_b32 v12, 16, v158\n"
        "v_lshlrev_b32 v14, 16, v159\n"
        "buffer_load_dwordx4 v128, s[20:23], s64 offen lds\n"
        "v_and_b32 v9, 0xffff0000, v156\n"
        "v_and_b32 v11, 0xffff0000, v157\n"
        "v_and_b32 v13, 0xffff0000, v158\n"
        "v_and_b32 v15, 0xffff0000, v159\n"
        "v_pk_mul_f32 v[8:9], v[8:9], s[50:51]\n"
        "v_pk_mul_f32 v[10:11], v[10:11], s[50:51]\n"
        "v_pk_mul_f32 v[12:13], v[12:13], s[50:51]\n"
        "v_pk_mul_f32 v[14:15], v[14:15], s[50:51]\n"
        "v_cvt_pk_bf16_f32 v8, v8, v9\n"
        "v_cvt_pk_bf16_f32 v10, v10, v11\n"
        "v_cvt_pk_bf16_f32 v12, v12, v13\n"
        "v_cvt_pk_bf16_f32 v14, v14, v15\n"
        "v_accvgpr_write_b32 a40, v8\n"
        "v_accvgpr_write_b32 a41, v10\n"
        "v_accvgpr_write_b32 a42, v12\n"
        "v_accvgpr_write_b32 a43, v14\n"
        "buffer_load_dwordx4 v129, s[20:23], s64 offen offset:1024 lds\n"
        "s_waitcnt lgkmcnt(7)\n"
        "v_lshlrev_b32 v0, 16, v176\n"
        "v_lshlrev_b32 v2, 16, v177\n"
        "s_waitcnt lgkmcnt(6)\n"
        "v_lshlrev_b32 v4, 16, v178\n"
        "v_lshlrev_b32 v6, 16, v179\n"
        "v_and_b32 v1, 0xffff0000, v176\n"
        "v_and_b32 v3, 0xffff0000, v177\n"
        "v_and_b32 v5, 0xffff0000, v178\n"
        "v_and_b32 v7, 0xffff0000, v179\n"
        "v_pk_mul_f32 v[0:1], v[0:1], s[50:51]\n"
        "v_pk_mul_f32 v[2:3], v[2:3], s[50:51]\n"
        "v_pk_mul_f32 v[4:5], v[4:5], s[50:51]\n"
        "v_pk_mul_f32 v[6:7], v[6:7], s[50:51]\n"
        "v_cvt_pk_bf16_f32 v0, v0, v1\n"
        "v_cvt_pk_bf16_f32 v2, v2, v3\n"
        "v_cvt_pk_bf16_f32 v4, v4, v5\n"
        "v_cvt_pk_bf16_f32 v6, v6, v7\n"
        "buffer_load_dwordx4 v130, s[20:23], s64 offen offset:2048 lds\n"
        "v_accvgpr_write_b32 a44, v0\n"
        "v_accvgpr_write_b32 a45, v2\n"
        "v_accvgpr_write_b32 a46, v4\n"
        "v_accvgpr_write_b32 a47, v6\n"
        "s_waitcnt lgkmcnt(5)\n"
        "v_lshlrev_b32 v8, 16, v196\n"
        "v_lshlrev_b32 v10, 16, v197\n"
        "s_waitcnt lgkmcnt(4)\n"
        "v_lshlrev_b32 v12, 16, v198\n"
        "v_lshlrev_b32 v14, 16, v199\n"
        "s_add_u32 m0, s40, 110592\n"
        "v_and_b32 v9, 0xffff0000, v196\n"
        "v_and_b32 v11, 0xffff0000, v197\n"
        "v_and_b32 v13, 0xffff0000, v198\n"
        "v_and_b32 v15, 0xffff0000, v199\n"
        "s_add_u32 s64, s65, 37248\n"
        "v_pk_mul_f32 v[8:9], v[8:9], s[50:51]\n"
        "v_pk_mul_f32 v[10:11], v[10:11], s[50:51]\n"
        "v_pk_mul_f32 v[12:13], v[12:13], s[50:51]\n"
        "v_pk_mul_f32 v[14:15], v[14:15], s[50:51]\n"
        "buffer_load_dwordx4 v128, s[20:23], s64 offen lds\n"
        "v_cvt_pk_bf16_f32 v8, v8, v9\n"
        "v_cvt_pk_bf16_f32 v10, v10, v11\n"
        "v_cvt_pk_bf16_f32 v12, v12, v13\n"
        "v_cvt_pk_bf16_f32 v14, v14, v15\n"
        "v_accvgpr_write_b32 a60, v8\n"
        "v_accvgpr_write_b32 a61, v10\n"
        "v_accvgpr_write_b32 a62, v12\n"
        "v_accvgpr_write_b32 a63, v14\n"
        "s_waitcnt lgkmcnt(3)\n"
        "v_lshlrev_b32 v0, 16, v216\n"
        "v_lshlrev_b32 v2, 16, v217\n"
        "s_waitcnt lgkmcnt(2)\n"
        "v_lshlrev_b32 v4, 16, v218\n"
        "v_lshlrev_b32 v6, 16, v219\n"
        "v_and_b32 v1, 0xffff0000, v216\n"
        "v_and_b32 v3, 0xffff0000, v217\n"
        "v_and_b32 v5, 0xffff0000, v218\n"
        "v_and_b32 v7, 0xffff0000, v219\n"
        "buffer_load_dwordx4 v129, s[20:23], s64 offen offset:1024 lds\n"
        "v_pk_mul_f32 v[0:1], v[0:1], s[50:51]\n"
        "v_pk_mul_f32 v[2:3], v[2:3], s[50:51]\n"
        "v_pk_mul_f32 v[4:5], v[4:5], s[50:51]\n"
        "v_pk_mul_f32 v[6:7], v[6:7], s[50:51]\n"
        "v_cvt_pk_bf16_f32 v0, v0, v1\n"
        "v_cvt_pk_bf16_f32 v2, v2, v3\n"
        "v_cvt_pk_bf16_f32 v4, v4, v5\n"
        "v_cvt_pk_bf16_f32 v6, v6, v7\n"
        "v_accvgpr_write_b32 a64, v0\n"
        "v_accvgpr_write_b32 a65, v2\n"
        "v_accvgpr_write_b32 a66, v4\n"
        "v_accvgpr_write_b32 a67, v6\n"
        "s_waitcnt lgkmcnt(1)\n"
        "v_lshlrev_b32 v8, 16, v124\n"
        "v_lshlrev_b32 v10, 16, v125\n"
        "s_waitcnt lgkmcnt(0)\n"
        "v_lshlrev_b32 v12, 16, v126\n"
        "v_lshlrev_b32 v14, 16, v127\n"
        "buffer_load_dwordx4 v130, s[20:23], s64 offen offset:2048 lds\n"
        "v_and_b32 v9, 0xffff0000, v124\n"
        "v_and_b32 v11, 0xffff0000, v125\n"
        "v_and_b32 v13, 0xffff0000, v126\n"
        "v_and_b32 v15, 0xffff0000, v127\n"
        "v_pk_mul_f32 v[8:9], v[8:9], s[50:51]\n"
        "v_pk_mul_f32 v[10:11], v[10:11], s[50:51]\n"
        "v_pk_mul_f32 v[12:13], v[12:13], s[50:51]\n"
        "v_pk_mul_f32 v[14:15], v[14:15], s[50:51]\n"
        "s_add_u32 m0, s40, 122880\n"
        "v_cvt_pk_bf16_f32 v8, v8, v9\n"
        "v_cvt_pk_bf16_f32 v10, v10, v11\n"
        "v_cvt_pk_bf16_f32 v12, v12, v13\n"
        "v_cvt_pk_bf16_f32 v14, v14, v15\n"
        "s_add_u32 s64, s65, 74112\n"
        "v_accvgpr_write_b32 a68, v8\n"
        "v_accvgpr_write_b32 a69, v10\n"
        "v_accvgpr_write_b32 a70, v12\n"
        "v_accvgpr_write_b32 a71, v14\n"
        "buffer_load_dwordx4 v128, s[20:23], s64 offen lds\n"
        "buffer_load_dwordx4 v129, s[20:23], s64 offen offset:1024 lds\n"
        "buffer_load_dwordx4 v130, s[20:23], s64 offen offset:2048 lds\n"
        "s_waitcnt vmcnt(20)\n"
        "s_barrier\n"
        "ds_read_b128 a[224:227], v150 offset:0\n"
        "s_waitcnt lgkmcnt(0)\n"
        "v_mfma_f32_16x16x32_bf16 v[96:99], a[0:3], a[224:227], 0\n"
        "ds_read_b128 a[228:231], v151 offset:0\n"
        "ds_read_b128 a[232:235], v150 offset:128\n"
        "s_add_u32 m0, s40, 0\n"
        "s_add_u32 s64, s65, 110976\n"
        "s_waitcnt lgkmcnt(1)\n"
        "v_mfma_f32_16x16x32_bf16 v[96:99], a[4:7], a[228:231], v[96:99]\n"
        "s_waitcnt lgkmcnt(0)\n"
        "ds_read_b128 a[236:239], v151 offset:128\n"
        "ds_read_b128 a[240:243], v150 offset:256\n"
        "v_mfma_f32_16x16x32_bf16 v[96:99], a[8:11], a[232:235], v[96:99]\n"
        "s_waitcnt lgkmcnt(1)\n"
        "ds_read_b128 a[244:247], v151 offset:256\n"
        "buffer_load_dwordx4 v128, s[20:23], s64 offen lds\n"
        "v_mfma_f32_16x16x32_bf16 v[96:99], a[12:15], a[236:239], v[96:99]\n"
        "s_waitcnt lgkmcnt(1)\n"
        "ds_read_b128 a[248:251], v150 offset:1536\n"
        "ds_read_b128 a[216:219], v151 offset:1536\n"
        "v_mfma_f32_16x16x32_bf16 v[96:99], a[16:19], a[240:243], v[96:99]\n"
        "s_waitcnt lgkmcnt(2)\n"
        "ds_read_b128 a[220:223], v150 offset:1664\n"
        "v_mfma_f32_16x16x32_bf16 v[96:99], a[20:23], a[244:247], v[96:99]\n"
        "buffer_load_dwordx4 v129, s[20:23], s64 offen offset:1024 lds\n"
        "v_mfma_f32_16x16x32_bf16 v[100:103], a[24:27], a[224:227], 0\n"
        "v_mfma_f32_16x16x32_bf16 v[100:103], a[28:31], a[228:231], v[100:103]\n"
        "v_mfma_f32_16x16x32_bf16 v[100:103], a[32:35], a[232:235], v[100:103]\n"
        "v_mfma_f32_16x16x32_bf16 v[100:103], a[36:39], a[236:239], v[100:103]\n"
        "buffer_load_dwordx4 v130, s[20:23], s64 offen offset:2048 lds\n"
        "v_mfma_f32_16x16x32_bf16 v[100:103], a[40:43], a[240:243], v[100:103]\n"
        "s_waitcnt vmcnt(19)\n"
        "v_lshlrev_b32 v236, 16, v236\n"
        "v_cvt_pk_bf16_f32 v156, v96, v97\n"
        "v_mfma_f32_16x16x32_bf16 v[100:103], a[44:47], a[244:247], v[100:103]\n"
        "s_add_u32 m0, s40, 12288\n"
        "ds_write_b32 v146, v236 offset:0\n"
        "v_mfma_f32_16x16x32_bf16 v[112:115], a[48:51], a[224:227], 0\n"
        "ds_read_b128 a[224:227], v151 offset:1664\n"
        "s_add_u32 s64, s65, 147840\n"
        "v_cvt_pk_bf16_f32 v157, v98, v99\n"
        "s_waitcnt vmcnt(18)\n"
        "v_lshlrev_b32 v237, 16, v237\n"
        "v_mfma_f32_16x16x32_bf16 v[112:115], a[52:55], a[228:231], v[112:115]\n"
        "ds_read_b128 a[228:231], v150 offset:1792\n"
        "buffer_load_dwordx4 v128, s[20:23], s64 offen lds\n"
        "v_cvt_pk_bf16_f32 v158, v100, v101\n"
        "v_mfma_f32_16x16x32_bf16 v[112:115], a[56:59], a[232:235], v[112:115]\n"
        "ds_read_b128 a[232:235], v151 offset:1792\n"
        "ds_write_b32 v146, v237 offset:256\n"
        "v_mfma_f32_16x16x32_bf16 v[112:115], a[60:63], a[236:239], v[112:115]\n"
        "v_cvt_pk_bf16_f32 v159, v102, v103\n"
        "s_waitcnt vmcnt(18)\n"
        "v_lshlrev_b32 v238, 16, v238\n"
        "s_waitcnt vmcnt(17)\n"
        "v_lshlrev_b32 v239, 16, v239\n"
        "v_mfma_f32_16x16x32_bf16 v[112:115], a[64:67], a[240:243], v[112:115]\n"
        "v_mfma_f32_16x16x32_bf16 v[112:115], a[68:71], a[244:247], v[112:115]\n"
        "s_waitcnt lgkmcnt(7)\n"
        "ds_write_b32 v146, v238 offset:512\n"
        "buffer_load_dwordx4 v129, s[20:23], s64 offen offset:1024 lds\n"
        "v_mfma_f32_16x16x32_bf16 v[96:99], a[0:3], a[248:251], 0\n"
        "s_waitcnt lgkmcnt(7)\n"
        "ds_write_b32 v146, v239 offset:768\n"
        "s_add_u32 s35, s47, 786432\n"
        "s_sub_u32 s46, s35, 100663296\n"
        "s_cmp_lt_u32 s46, 786432\n"
        "s_waitcnt vmcnt(5)\n"
        "s_barrier\n"
        "v_mfma_f32_16x16x32_bf16 v[96:99], a[4:7], a[216:219], v[96:99]\n"
        "s_waitcnt lgkmcnt(7)\n"
        "s_cselect_b32 s35, s46, s35\n"
        "s_add_u32 s41, s30, s40\n"
        "ds_read_b64_tr_b16 a[72:73], v136 offset:0\n"
        "ds_read_b64_tr_b16 a[74:75], v136 offset:1536\n"
        "v_mfma_f32_16x16x32_bf16 v[96:99], a[8:11], a[220:223], v[96:99]\n"
        "s_waitcnt lgkmcnt(7)\n"
        "ds_read_b64_tr_b16 a[76:77], v136 offset:12288\n"
        "ds_read_b64_tr_b16 a[78:79], v136 offset:13824\n"
        "v_cvt_pk_bf16_f32 v160, v112, v113\n"
        "v_mfma_f32_16x16x32_bf16 v[96:99], a[12:15], a[224:227], v[96:99]\n"
        "s_waitcnt lgkmcnt(8)\n"
        "buffer_load_dwordx4 v130, s[20:23], s64 offen offset:2048 lds\n"
        "ds_read_b64_tr_b16 a[80:81], v136 offset:24576\n"
        "v_cvt_pk_bf16_f32 v161, v114, v115\n"
        "v_mfma_f32_16x16x32_bf16 v[96:99], a[16:19], a[228:231], v[96:99]\n"
        "s_waitcnt lgkmcnt(8)\n"
        "ds_read_b64_tr_b16 a[82:83], v136 offset:26112\n"
        "ds_read_b64_tr_b16 a[96:97], v137 offset:0\n"
        "v_cvt_pk_bf16_f32 v166, v112, v113\n"
        "v_mfma_f32_16x16x32_bf16 v[96:99], a[20:23], a[232:235], v[96:99]\n"
        "s_add_u32 m0, s40, 24576\n"
        "ds_read_b64_tr_b16 a[98:99], v137 offset:1536\n"
        "ds_read_b128 a[236:239], v150 offset:12288\n"
        "v_cvt_pk_bf16_f32 v167, v114, v115\n"
        "v_mfma_f32_16x16x32_bf16 v[100:103], a[24:27], a[248:251], 0\n"
        "s_add_u32 s64, s65, 184704\n"
        "ds_read_b64_tr_b16 a[100:101], v137 offset:12288\n"
        "ds_read_b128 a[240:243], v151 offset:12288\n"
        "v_cvt_pk_bf16_f32 v176, v96, v97\n"
        "v_mfma_f32_16x16x32_bf16 v[100:103], a[28:31], a[216:219], v[100:103]\n"
        "buffer_load_dwordx4 v128, s[20:23], s64 offen lds\n"
        "ds_read_b64_tr_b16 a[102:103], v137 offset:13824\n"
        "v_cvt_pk_bf16_f32 v177, v98, v99\n"
        "v_mfma_f32_16x16x32_bf16 v[100:103], a[32:35], a[220:223], v[100:103]\n"
        "ds_read_b128 v[0:3], v232 offset:0\n"
        "ds_read_b128 a[244:247], v150 offset:12416\n"
        "v_mfma_f32_16x16x32_bf16 v[100:103], a[36:39], a[224:227], v[100:103]\n"
        "ds_read_b64_tr_b16 a[104:105], v137 offset:24576\n"
        "ds_read_b64_tr_b16 a[106:107], v137 offset:26112\n"
        "v_mfma_f32_16x16x32_bf16 v[100:103], a[40:43], a[228:231], v[100:103]\n"
        "ds_read_b64_tr_b16 a[120:121], v138 offset:0\n"
        "ds_read_b64_tr_b16 a[122:123], v138 offset:1536\n"
        "s_waitcnt lgkmcnt(5)\n"
        "v_lshl_add_u32 v0, v0, 2, s67\n"
        "v_mfma_f32_16x16x32_bf16 v[100:103], a[44:47], a[232:235], v[100:103]\n"
        "buffer_load_dwordx4 v129, s[20:23], s64 offen offset:1024 lds\n"
        "ds_read_b64_tr_b16 a[124:125], v138 offset:12288\n"
        "v_lshl_add_u32 v1, v1, 2, s67\n"
        "v_mfma_f32_16x16x32_bf16 v[112:115], a[48:51], a[248:251], 0\n"
        "ds_read_b128 a[248:251], v151 offset:12416\n"
        "ds_read_b128 v[16:19], v232 offset:1024\n"
        "v_lshl_add_u32 v2, v2, 2, s67\n"
        "v_mfma_f32_16x16x32_bf16 v[112:115], a[52:55], a[216:219], v[112:115]\n"
        "ds_read_b128 a[216:219], v150 offset:12544\n"
        "ds_read_b64_tr_b16 a[126:127], v138 offset:13824\n"
        "v_cvt_pk_bf16_f32 v178, v100, v101\n"
        "v_mfma_f32_16x16x32_bf16 v[112:115], a[56:59], a[220:223], v[112:115]\n"
        "ds_read_b128 a[220:223], v151 offset:12544\n"
        "ds_read_b64_tr_b16 a[128:129], v138 offset:24576\n"
        "v_cvt_pk_bf16_f32 v179, v102, v103\n"
        "v_mfma_f32_16x16x32_bf16 v[112:115], a[60:63], a[224:227], v[112:115]\n"
        "buffer_load_dwordx4 v130, s[20:23], s64 offen offset:2048 lds\n"
        "ds_read_b64_tr_b16 a[130:131], v138 offset:26112\n"
        "v_lshl_add_u32 v3, v3, 2, s67\n"
        "v_mfma_f32_16x16x32_bf16 v[112:115], a[64:67], a[228:231], v[112:115]\n"
        "ds_read_b128 v[32:35], v232 offset:8192\n"
        "ds_read_b128 v[48:51], v232 offset:9216\n"
        "s_waitcnt lgkmcnt(7)\n"
        "v_lshl_add_u32 v16, v16, 2, s67\n"
        "v_mfma_f32_16x16x32_bf16 v[112:115], a[68:71], a[232:235], v[112:115]\n"
        "ds_read_b128 a[224:227], v150 offset:13824\n"
        "ds_read_b128 a[228:231], v151 offset:13824\n"
        "v_xor_b32 v253, 32, v249\n"
        "v_mfma_f32_16x16x32_bf16 v[96:99], a[0:3], a[236:239], 0\n"
        "ds_read_b128 a[232:235], v150 offset:13952\n"
        "ds_read_b32 v0, v0\n"
        "v_lshl_add_u32 v17, v17, 2, s67\n"
        "v_mfma_f32_16x16x32_bf16 v[96:99], a[4:7], a[240:243], v[96:99]\n"
        "s_xor_b32 m0, s48, 8192\n"
        "ds_read_b32 v1, v1\n"
        "ds_read_b32 v2, v2\n"
        "v_cvt_pk_bf16_f32 v180, v112, v113\n"
        "v_mfma_f32_16x16x32_bf16 v[96:99], a[8:11], a[244:247], v[96:99]\n"
        "s_sub_u32 s64, s36, 2097152\n"
        "ds_read_b128 v[4:7], v233 offset:0\n"
        "ds_read_b32 v3, v3\n"
        "v_cvt_pk_bf16_f32 v181, v114, v115\n"
        "v_mfma_f32_16x16x32_bf16 v[96:99], a[12:15], a[248:251], v[96:99]\n"
        "s_waitcnt lgkmcnt(14)\n"
        "buffer_load_dwordx4 v249, s[12:15], s64 offen lds\n"
        "ds_read_b32 v16, v16\n"
        "v_cvt_pk_bf16_f32 v186, v112, v113\n"
        "v_mfma_f32_16x16x32_bf16 v[96:99], a[16:19], a[216:219], v[96:99]\n"
        "s_waitcnt lgkmcnt(13)\n"
        "ds_read_b32 v17, v17\n"
        "v_cvt_pk_bf16_f32 v187, v114, v115\n"
        "v_lshl_add_u32 v18, v18, 2, s67\n"
        "v_mfma_f32_16x16x32_bf16 v[96:99], a[20:23], a[220:223], v[96:99]\n"
        "ds_read_b32 v18, v18\n"
        "v_lshl_add_u32 v19, v19, 2, s67\n"
        "s_waitcnt lgkmcnt(12)\n"
        "v_lshl_add_u32 v32, v32, 2, s67\n"
        "v_mfma_f32_16x16x32_bf16 v[100:103], a[24:27], a[236:239], 0\n"
        "s_barrier\n"
        "ds_read_b32 v19, v19\n"
        "ds_read_b32 v32, v32\n"
        "v_lshl_add_u32 v33, v33, 2, s67\n"
        "v_mfma_f32_16x16x32_bf16 v[100:103], a[28:31], a[240:243], v[100:103]\n"
        "buffer_load_dwordx4 v253, s[12:15], s64 offen offset:1024 lds\n"
        "ds_read_b128 v[20:23], v233 offset:1024\n"
        "v_cvt_pk_bf16_f32 v196, v96, v97\n"
        "v_mfma_f32_16x16x32_bf16 v[100:103], a[32:35], a[244:247], v[100:103]\n"
        "ds_read_b32 v33, v33\n"
        "v_cvt_pk_bf16_f32 v197, v98, v99\n"
        "v_lshl_add_u32 v34, v34, 2, s67\n"
        "v_mfma_f32_16x16x32_bf16 v[100:103], a[36:39], a[248:251], v[100:103]\n"
        "ds_read_b32 v34, v34\n"
        "v_lshl_add_u32 v35, v35, 2, s67\n"
        "s_waitcnt lgkmcnt(15)\n"
        "v_lshl_add_u32 v48, v48, 2, s67\n"
        "v_mfma_f32_16x16x32_bf16 v[100:103], a[40:43], a[216:219], v[100:103]\n"
        "s_add_u32 m0, s41, 0\n"
        "ds_read_b32 v35, v35\n"
        "ds_read_b32 v48, v48\n"
        "v_lshl_add_u32 v49, v49, 2, s67\n"
        "v_mfma_f32_16x16x32_bf16 v[100:103], a[44:47], a[220:223], v[100:103]\n"
        "buffer_load_dwordx4 v242, s[8:11], s47 offen lds\n"
        "ds_read_b32 v49, v49\n"
        "v_mfma_f32_16x16x32_bf16 v[112:115], a[48:51], a[236:239], 0\n"
        "ds_read_b128 a[236:239], v151 offset:13952\n"
        "ds_read_b128 v[36:39], v233 offset:8192\n"
        "v_lshl_add_u32 v50, v50, 2, s67\n"
        "v_mfma_f32_16x16x32_bf16 v[112:115], a[52:55], a[240:243], v[112:115]\n"
        "ds_read_b128 a[240:243], v150 offset:14080\n"
        "ds_read_b32 v50, v50\n"
        "v_cvt_pk_bf16_f32 v198, v100, v101\n"
        "v_mfma_f32_16x16x32_bf16 v[112:115], a[56:59], a[244:247], v[112:115]\n"
        "ds_read_b128 a[244:247], v151 offset:14080\n"
        "v_cvt_pk_bf16_f32 v199, v102, v103\n"
        "v_lshl_add_u32 v51, v51, 2, s67\n"
        "v_mfma_f32_16x16x32_bf16 v[112:115], a[60:63], a[248:251], v[112:115]\n"
        "s_add_u32 m0, s41, 1024\n"
        "ds_read_b128 a[248:251], v150 offset:0\n"
        "ds_read_b32 v51, v51\n"
        "s_waitcnt lgkmcnt(15)\n"
        "v_lshl_add_u32 v4, v4, 2, s67\n"
        "v_mfma_f32_16x16x32_bf16 v[112:115], a[64:67], a[216:219], v[112:115]\n"
        "buffer_load_dwordx4 v243, s[8:11], s47 offen lds\n"
        "ds_read_b128 a[216:219], v151 offset:0\n"
        "v_mfma_f32_16x16x32_bf16 v[112:115], a[68:71], a[220:223], v[112:115]\n"
        "ds_read_b128 v[52:55], v233 offset:9216\n"
        "ds_read_b128 a[220:223], v150 offset:128\n"
        "v_lshl_add_u32 v5, v5, 2, s67\n"
        "v_mfma_f32_16x16x32_bf16 v[96:99], a[0:3], a[224:227], 0\n"
        "ds_read_b32 v4, v4\n"
        "ds_read_b32 v5, v5\n"
        "v_lshl_add_u32 v6, v6, 2, s67\n"
        "v_mfma_f32_16x16x32_bf16 v[96:99], a[4:7], a[228:231], v[96:99]\n"
        "ds_read_b32 v6, v6\n"
        "v_cvt_pk_bf16_f32 v200, v112, v113\n"
        "v_cvt_pk_bf16_f32 v201, v114, v115\n"
        "v_mfma_f32_16x16x32_bf16 v[96:99], a[8:11], a[232:235], v[96:99]\n"
        "s_waitcnt lgkmcnt(12)\n"
        "s_add_u32 m0, s41, 2048\n"
        "v_cvt_pk_bf16_f32 v206, v112, v113\n"
        "v_cvt_pk_bf16_f32 v207, v114, v115\n"
        "v_lshl_add_u32 v7, v7, 2, s67\n"
        "v_mfma_f32_16x16x32_bf16 v[96:99], a[12:15], a[236:239], v[96:99]\n"
        "s_waitcnt lgkmcnt(10)\n"
        "buffer_load_dwordx4 v244, s[8:11], s47 offen lds\n"
        "ds_read_b32 v7, v7\n"
        "v_mfma_f32_16x16x32_bf16 v[96:99], a[16:19], a[240:243], v[96:99]\n"
        "s_waitcnt lgkmcnt(9)\n"
        "ds_read_b128 v[8:11], v234 offset:0\n"
        "v_lshl_add_u32 v20, v20, 2, s67\n"
        "v_lshl_add_u32 v21, v21, 2, s67\n"
        "v_mfma_f32_16x16x32_bf16 v[96:99], a[20:23], a[244:247], v[96:99]\n"
        "ds_read_b32 v20, v20\n"
        "ds_read_b32 v21, v21\n"
        "v_lshl_add_u32 v22, v22, 2, s67\n"
        "v_mfma_f32_16x16x32_bf16 v[100:103], a[24:27], a[224:227], 0\n"
        "s_waitcnt vmcnt(5)\n"
        "s_barrier\n"
        "ds_read_b32 v22, v22\n"
        "v_lshl_add_u32 v23, v23, 2, s67\n"
        "v_lshl_add_u32 v36, v36, 2, s67\n"
        "v_mfma_f32_16x16x32_bf16 v[100:103], a[28:31], a[228:231], v[100:103]\n"
        "s_add_u32 m0, s41, 12288\n"
        "ds_read_b64_tr_b16 a[84:85], v131 offset:0\n"
        "ds_read_b64_tr_b16 a[86:87], v131 offset:1536\n"
        "ds_read_b64_tr_b16 a[88:89], v131 offset:12288\n"
        "v_mfma_f32_16x16x32_bf16 v[100:103], a[32:35], a[232:235], v[100:103]\n"
        "ds_read_b64_tr_b16 a[90:91], v131 offset:13824\n"
        "ds_read_b64_tr_b16 a[92:93], v131 offset:24576\n"
        "ds_read_b64_tr_b16 a[94:95], v131 offset:26112\n"
        "s_add_u32 s47, s47, 6291456\n"
        "v_mfma_f32_16x16x32_bf16 v[100:103], a[36:39], a[236:239], v[100:103]\n"
        "ds_read_b64_tr_b16 a[108:109], v132 offset:0\n"
        "ds_read_b64_tr_b16 a[110:111], v132 offset:1536\n"
        "ds_read_b64_tr_b16 a[112:113], v132 offset:12288\n"
        "s_sub_u32 s31, s30, s28\n"
        "v_mfma_f32_16x16x32_bf16 v[100:103], a[40:43], a[240:243], v[100:103]\n"
        "ds_read_b64_tr_b16 a[114:115], v132 offset:13824\n"
        "ds_read_b64_tr_b16 a[116:117], v132 offset:24576\n"
        "ds_read_b64_tr_b16 a[118:119], v132 offset:26112\n"
        "s_sub_u32 s45, s28, s29\n"
        "v_mfma_f32_16x16x32_bf16 v[100:103], a[44:47], a[244:247], v[100:103]\n"
        "ds_read_b64_tr_b16 a[132:133], v133 offset:0\n"
        "ds_read_b64_tr_b16 a[134:135], v133 offset:1536\n"
        "ds_read_b64_tr_b16 a[136:137], v133 offset:12288\n"
        "s_add_u32 s28, s28, s31\n"
        "v_mfma_f32_16x16x32_bf16 v[112:115], a[48:51], a[224:227], 0\n"
        "ds_read_b64_tr_b16 a[138:139], v133 offset:13824\n"
        "ds_read_b64_tr_b16 a[140:141], v133 offset:24576\n"
        "ds_read_b64_tr_b16 a[142:143], v133 offset:26112\n"
        "s_mov_b32 s30, s29\n"
        "v_mfma_f32_16x16x32_bf16 v[112:115], a[52:55], a[228:231], v[112:115]\n"
        "buffer_load_dwordx4 v242, s[8:11], s35 offen lds\n"
        "ds_read_b128 a[224:227], v151 offset:128\n"
        "s_add_u32 m0, s41, 13312\n"
        "v_mfma_f32_16x16x32_bf16 v[112:115], a[56:59], a[232:235], v[112:115]\n"
        "ds_read_b128 a[228:231], v150 offset:256\n"
        "buffer_load_dwordx4 v243, s[8:11], s35 offen lds\n"
        "s_add_u32 s29, s29, s45\n"
        "v_mfma_f32_16x16x32_bf16 v[112:115], a[60:63], a[236:239], v[112:115]\n"
        "ds_read_b128 a[232:235], v151 offset:256\n"
        "ds_read_b128 v[24:27], v234 offset:1024\n"
        "v_cvt_pk_bf16_f32 v216, v96, v97\n"
        "v_mfma_f32_16x16x32_bf16 v[112:115], a[64:67], a[240:243], v[112:115]\n"
        "ds_read_b32 v23, v23\n"
        "ds_read_b128 v[40:43], v234 offset:8192\n"
        "v_cvt_pk_bf16_f32 v217, v98, v99\n"
        "v_mfma_f32_16x16x32_bf16 v[112:115], a[68:71], a[244:247], v[112:115]\n"
        "s_waitcnt lgkmcnt(15)\n"
        "s_add_u32 m0, s41, 14336\n"
        "ds_read_b32 v36, v36\n"
        "ds_read_b128 a[236:239], v150 offset:1536\n"
        "v_cvt_pk_bf16_f32 v218, v100, v101\n"
        "v_mfma_f32_16x16x32_bf16 v[96:99], a[72:75], a[248:251], 0\n"
        "s_waitcnt lgkmcnt(15)\n"
        "buffer_load_dwordx4 v244, s[8:11], s35 offen lds\n"
        "ds_read_b128 a[240:243], v151 offset:1536\n"
        "v_mfma_f32_16x16x32_bf16 v[96:99], a[76:79], a[216:219], v[96:99]\n"
        "s_waitcnt lgkmcnt(15)\n"
        "ds_read_b128 a[244:247], v150 offset:1664\n"
        "ds_read_b128 v[56:59], v234 offset:9216\n"
        "v_cvt_pk_bf16_f32 v219, v102, v103\n"
        "v_mfma_f32_16x16x32_bf16 v[96:99], a[80:83], a[220:223], v[96:99]\n"
        "s_waitcnt lgkmcnt(10)\n"
        "v_cvt_pk_bf16_f32 v220, v112, v113\n"
        "v_cvt_pk_bf16_f32 v221, v114, v115\n"
        "v_cvt_pk_bf16_f32 v226, v112, v113\n"
        "v_mfma_f32_16x16x32_bf16 v[96:99], a[84:87], a[224:227], v[96:99]\n"
        "s_waitcnt lgkmcnt(9)\n"
        "s_add_u32 m0, s40, 36864\n"
        "v_cvt_pk_bf16_f32 v227, v114, v115\n"
        "v_lshl_add_u32 v37, v37, 2, s67\n"
        "v_lshl_add_u32 v38, v38, 2, s67\n"
        "v_mfma_f32_16x16x32_bf16 v[96:99], a[88:91], a[228:231], v[96:99]\n"
        "s_waitcnt lgkmcnt(8)\n"
        "s_add_u32 s64, s65, 768\n"
        "ds_read_b32 v37, v37\n"
        "ds_read_b32 v38, v38\n"
        "v_lshl_add_u32 v39, v39, 2, s67\n"
        "v_mfma_f32_16x16x32_bf16 v[96:99], a[92:95], a[232:235], v[96:99]\n"
        "buffer_load_dwordx4 v128, s[20:23], s64 offen lds\n"
        "ds_read_b32 v39, v39\n"
        "v_lshl_add_u32 v52, v52, 2, s67\n"
        "v_mfma_f32_16x16x32_bf16 v[100:103], a[96:99], a[248:251], 0\n"
        "ds_read_b32 v52, v52\n"
        "ds_read_b128 v[12:15], v235 offset:0\n"
        "v_lshl_add_u32 v53, v53, 2, s67\n"
        "v_mfma_f32_16x16x32_bf16 v[100:103], a[100:103], a[216:219], v[100:103]\n"
        "ds_read_b32 v53, v53\n"
        "v_cvt_pk_bf16_f32 v152, v96, v97\n"
        "v_cvt_pk_bf16_f32 v153, v98, v99\n"
        "v_mfma_f32_16x16x32_bf16 v[100:103], a[104:107], a[220:223], v[100:103]\n"
        "v_lshl_add_u32 v54, v54, 2, s67\n"
        "v_lshl_add_u32 v55, v55, 2, s67\n"
        "v_lshl_add_u32 v8, v8, 2, s67\n"
        "v_mfma_f32_16x16x32_bf16 v[100:103], a[108:111], a[224:227], v[100:103]\n"
        "ds_read_b32 v54, v54\n"
        "ds_read_b32 v55, v55\n"
        "v_lshl_add_u32 v9, v9, 2, s67\n"
        "v_mfma_f32_16x16x32_bf16 v[100:103], a[112:115], a[228:231], v[100:103]\n"
        "buffer_load_dwordx4 v129, s[20:23], s64 offen offset:1024 lds\n"
        "ds_read_b32 v8, v8\n"
        "v_lshl_add_u32 v10, v10, 2, s67\n"
        "v_mfma_f32_16x16x32_bf16 v[100:103], a[116:119], a[232:235], v[100:103]\n"
        "ds_read_b32 v9, v9\n"
        "ds_read_b32 v10, v10\n"
        "v_lshl_add_u32 v11, v11, 2, s67\n"
        "v_mfma_f32_16x16x32_bf16 v[112:115], a[120:123], a[248:251], 0\n"
        "ds_read_b128 a[248:251], v151 offset:1664\n"
        "ds_read_b32 v11, v11\n"
        "s_waitcnt lgkmcnt(15)\n"
        "v_lshl_add_u32 v24, v24, 2, s67\n"
        "v_mfma_f32_16x16x32_bf16 v[112:115], a[124:127], a[216:219], v[112:115]\n"
        "ds_read_b128 a[216:219], v150 offset:1792\n"
        "ds_read_b128 v[28:31], v235 offset:1024\n"
        "v_cvt_pk_bf16_f32 v154, v100, v101\n"
        "v_mfma_f32_16x16x32_bf16 v[112:115], a[128:131], a[220:223], v[112:115]\n"
        "ds_read_b128 a[220:223], v151 offset:1792\n"
        "ds_read_b32 v24, v24\n"
        "v_cvt_pk_bf16_f32 v155, v102, v103\n"
        "v_mfma_f32_16x16x32_bf16 v[112:115], a[132:135], a[224:227], v[112:115]\n"
        "buffer_load_dwordx4 v130, s[20:23], s64 offen offset:2048 lds\n"
        "ds_read_b128 a[224:227], v150 offset:12288\n"
        "v_lshl_add_u32 v25, v25, 2, s67\n"
        "v_mfma_f32_16x16x32_bf16 v[112:115], a[136:139], a[228:231], v[112:115]\n"
        "ds_read_b32 v25, v25\n"
        "ds_read_b128 v[44:47], v235 offset:8192\n"
        "v_lshl_add_u32 v26, v26, 2, s67\n"
        "v_mfma_f32_16x16x32_bf16 v[112:115], a[140:143], a[232:235], v[112:115]\n"
        "ds_read_b32 v26, v26\n"
        "ds_read_b128 a[228:231], v151 offset:12288\n"
        "v_lshl_add_u32 v27, v27, 2, s67\n"
        "v_mfma_f32_16x16x32_bf16 v[96:99], a[72:75], a[236:239], 0\n"
        "s_add_u32 m0, s40, 49152\n"
        "ds_read_b32 v27, v27\n"
        "ds_read_b128 a[232:235], v150 offset:12416\n"
        "v_lshl_add_u32 v40, v40, 2, s67\n"
        "v_mfma_f32_16x16x32_bf16 v[96:99], a[76:79], a[240:243], v[96:99]\n"
        "s_waitcnt lgkmcnt(15)\n"
        "s_add_u32 s64, s65, 37632\n"
        "ds_read_b32 v40, v40\n"
        "v_cvt_pk_bf16_f32 v168, v112, v113\n"
        "v_cvt_pk_bf16_f32 v169, v114, v115\n"
        "v_mfma_f32_16x16x32_bf16 v[96:99], a[80:83], a[244:247], v[96:99]\n"
        "s_waitcnt lgkmcnt(13)\n"
        "buffer_load_dwordx4 v128, s[20:23], s64 offen lds\n"
        "v_lshl_add_u32 v41, v41, 2, s67\n"
        "v_lshl_add_u32 v42, v42, 2, s67\n"
        "v_mfma_f32_16x16x32_bf16 v[96:99], a[84:87], a[248:251], v[96:99]\n"
        "s_waitcnt lgkmcnt(11)\n"
        "ds_read_b32 v41, v41\n"
        "ds_read_b32 v42, v42\n"
        "v_lshl_add_u32 v43, v43, 2, s67\n"
        "v_mfma_f32_16x16x32_bf16 v[96:99], a[88:91], a[216:219], v[96:99]\n"
        "s_waitcnt lgkmcnt(11)\n"
        "ds_read_b32 v43, v43\n"
        "ds_read_b128 v[60:63], v235 offset:9216\n"
        "v_lshl_add_u32 v56, v56, 2, s67\n"
        "v_mfma_f32_16x16x32_bf16 v[96:99], a[92:95], a[220:223], v[96:99]\n"
        "ds_read_b32 v56, v56\n"
        "v_lshl_add_u32 v57, v57, 2, s67\n"
        "v_lshl_add_u32 v58, v58, 2, s67\n"
        "v_mfma_f32_16x16x32_bf16 v[100:103], a[96:99], a[236:239], 0\n"
        "ds_read_b32 v57, v57\n"
        "ds_read_b32 v58, v58\n"
        "v_lshl_add_u32 v59, v59, 2, s67\n"
        "v_mfma_f32_16x16x32_bf16 v[100:103], a[100:103], a[240:243], v[100:103]\n"
        "buffer_load_dwordx4 v129, s[20:23], s64 offen offset:1024 lds\n"
        "ds_read_b32 v59, v59\n"
        "v_cvt_pk_bf16_f32 v172, v96, v97\n"
        "v_mfma_f32_16x16x32_bf16 v[100:103], a[104:107], a[244:247], v[100:103]\n"
        "v_cvt_pk_bf16_f32 v173, v98, v99\n"
        "v_lshl_add_u32 v12, v12, 2, s67\n"
        "v_lshl_add_u32 v13, v13, 2, s67\n"
        "v_mfma_f32_16x16x32_bf16 v[100:103], a[108:111], a[248:251], v[100:103]\n"
        "ds_read_b32 v12, v12\n"
        "ds_read_b32 v13, v13\n"
        "v_lshl_add_u32 v14, v14, 2, s67\n"
        "v_mfma_f32_16x16x32_bf16 v[100:103], a[112:115], a[216:219], v[100:103]\n"
        "ds_read_b32 v14, v14\n"
        "v_lshl_add_u32 v15, v15, 2, s67\n"
        "v_lshl_add_u32 v28, v28, 2, s67\n"
        "v_mfma_f32_16x16x32_bf16 v[100:103], a[116:119], a[220:223], v[100:103]\n"
        "ds_read_b32 v15, v15\n"
        "ds_read_b32 v28, v28\n"
        "v_lshl_add_u32 v29, v29, 2, s67\n"
        "v_mfma_f32_16x16x32_bf16 v[112:115], a[120:123], a[236:239], 0\n"
        "ds_read_b128 a[236:239], v151 offset:12416\n"
        "buffer_load_dwordx4 v130, s[20:23], s64 offen offset:2048 lds\n"
        "v_lshl_add_u32 v30, v30, 2, s67\n"
        "v_mfma_f32_16x16x32_bf16 v[112:115], a[124:127], a[240:243], v[112:115]\n"
        "ds_read_b128 a[240:243], v150 offset:12544\n"
        "ds_read_b32 v29, v29\n"
        "v_cvt_pk_bf16_f32 v174, v100, v101\n"
        "v_mfma_f32_16x16x32_bf16 v[112:115], a[128:131], a[244:247], v[112:115]\n"
        "ds_read_b128 a[244:247], v151 offset:12544\n"
        "ds_read_b32 v30, v30\n"
        "v_cvt_pk_bf16_f32 v175, v102, v103\n"
        "v_mfma_f32_16x16x32_bf16 v[112:115], a[132:135], a[248:251], v[112:115]\n"
        "s_add_u32 m0, s40, 61440\n"
        "ds_read_b128 a[248:251], v150 offset:13824\n"
        "v_lshl_add_u32 v31, v31, 2, s67\n"
        "s_waitcnt lgkmcnt(15)\n"
        "v_lshl_add_u32 v44, v44, 2, s67\n"
        "v_mfma_f32_16x16x32_bf16 v[112:115], a[136:139], a[216:219], v[112:115]\n"
        "s_add_u32 s64, s65, 74496\n"
        "ds_read_b32 v31, v31\n"
        "ds_read_b128 a[216:219], v151 offset:13824\n"
        "v_lshl_add_u32 v45, v45, 2, s67\n"
        "v_mfma_f32_16x16x32_bf16 v[112:115], a[140:143], a[220:223], v[112:115]\n"
        "buffer_load_dwordx4 v128, s[20:23], s64 offen lds\n"
        "ds_read_b128 a[220:223], v150 offset:13952\n"
        "v_lshl_add_u32 v46, v46, 2, s67\n"
        "v_mfma_f32_16x16x32_bf16 v[96:99], a[72:75], a[224:227], 0\n"
        "ds_read_b32 v44, v44\n"
        "ds_read_b32 v45, v45\n"
        "v_lshl_add_u32 v47, v47, 2, s67\n"
        "v_mfma_f32_16x16x32_bf16 v[96:99], a[76:79], a[228:231], v[96:99]\n"
        "ds_read_b32 v46, v46\n"
        "ds_read_b32 v47, v47\n"
        "v_cvt_pk_bf16_f32 v190, v112, v113\n"
        "v_mfma_f32_16x16x32_bf16 v[96:99], a[80:83], a[232:235], v[96:99]\n"
        "s_waitcnt lgkmcnt(12)\n"
        "v_cvt_pk_bf16_f32 v191, v114, v115\n"
        "v_lshl_add_u32 v60, v60, 2, s67\n"
        "v_lshl_add_u32 v61, v61, 2, s67\n"
        "v_mfma_f32_16x16x32_bf16 v[96:99], a[84:87], a[236:239], v[96:99]\n"
        "s_waitcnt lgkmcnt(11)\n"
        "ds_read_b32 v60, v60\n"
        "ds_read_b32 v61, v61\n"
        "v_lshl_add_u32 v62, v62, 2, s67\n"
        "v_mfma_f32_16x16x32_bf16 v[96:99], a[88:91], a[240:243], v[96:99]\n"
        "s_waitcnt lgkmcnt(11)\n"
        "buffer_load_dwordx4 v129, s[20:23], s64 offen offset:1024 lds\n"
        "ds_read_b32 v62, v62\n"
        "v_lshl_add_u32 v63, v63, 2, s67\n"
        "v_mfma_f32_16x16x32_bf16 v[96:99], a[92:95], a[244:247], v[96:99]\n"
        "ds_read_b32 v63, v63\n"
        "v_add_u32 v240, s31, v240\n"
        "v_add_u32 v241, s31, v241\n"
        "v_mfma_f32_16x16x32_bf16 v[100:103], a[96:99], a[224:227], 0\n"
        "v_add_u32 v245, s45, v245\n"
        "v_add_u32 v246, s45, v246\n"
        "v_add_u32 v247, s45, v247\n"
        "v_mfma_f32_16x16x32_bf16 v[100:103], a[100:103], a[228:231], v[100:103]\n"
        "v_cvt_pk_bf16_f32 v192, v96, v97\n"
        "v_cvt_pk_bf16_f32 v193, v98, v99\n"
        "v_mfma_f32_16x16x32_bf16 v[100:103], a[104:107], a[232:235], v[100:103]\n"
        "v_mfma_f32_16x16x32_bf16 v[100:103], a[108:111], a[236:239], v[100:103]\n"
        "buffer_load_dwordx4 v130, s[20:23], s64 offen offset:2048 lds\n"
        "v_mfma_f32_16x16x32_bf16 v[100:103], a[112:115], a[240:243], v[100:103]\n"
        "v_mfma_f32_16x16x32_bf16 v[100:103], a[116:119], a[244:247], v[100:103]\n"
        "v_mfma_f32_16x16x32_bf16 v[112:115], a[120:123], a[224:227], 0\n"
        "ds_read_b128 a[224:227], v151 offset:13952\n"
        "v_mfma_f32_16x16x32_bf16 v[112:115], a[124:127], a[228:231], v[112:115]\n"
        "ds_read_b128 a[228:231], v150 offset:14080\n"
        "v_mfma_f32_16x16x32_bf16 v[112:115], a[128:131], a[232:235], v[112:115]\n"
        "ds_read_b128 a[232:235], v151 offset:14080\n"
        "v_mfma_f32_16x16x32_bf16 v[112:115], a[132:135], a[236:239], v[112:115]\n"
        "v_mfma_f32_16x16x32_bf16 v[112:115], a[136:139], a[240:243], v[112:115]\n"
        "v_cvt_pk_bf16_f32 v194, v100, v101\n"
        "v_cvt_pk_bf16_f32 v195, v102, v103\n"
        "v_mfma_f32_16x16x32_bf16 v[112:115], a[140:143], a[244:247], v[112:115]\n"
        "s_waitcnt lgkmcnt(14)\n"
        "s_waitcnt vmcnt(15)\n"
        "s_barrier\n"
        "ds_read_b128 v[162:165], v251 offset:0\n"
        "ds_read_b128 v[182:185], v251 offset:512\n"
        "v_mfma_f32_16x16x32_bf16 v[96:99], a[72:75], a[248:251], 0\n"
        "s_waitcnt lgkmcnt(14)\n"
        "s_add_u32 m0, s40, 0\n"
        "v_mfma_f32_16x16x32_bf16 v[96:99], a[76:79], a[216:219], v[96:99]\n"
        "s_waitcnt lgkmcnt(13)\n"
        "s_add_u32 s64, s65, 111360\n"
        "v_mfma_f32_16x16x32_bf16 v[96:99], a[80:83], a[220:223], v[96:99]\n"
        "s_waitcnt lgkmcnt(4)\n"
        "buffer_load_dwordx4 v128, s[20:23], s64 offen lds\n"
        "ds_read_b128 v[202:205], v251 offset:4096\n"
        "v_cvt_pk_bf16_f32 v208, v112, v113\n"
        "v_mfma_f32_16x16x32_bf16 v[96:99], a[84:87], a[224:227], v[96:99]\n"
        "s_waitcnt lgkmcnt(4)\n"
        "v_cvt_pk_bf16_f32 v209, v114, v115\n"
        "v_mfma_f32_16x16x32_bf16 v[96:99], a[88:91], a[228:231], v[96:99]\n"
        "s_waitcnt lgkmcnt(3)\n"
        "v_mfma_f32_16x16x32_bf16 v[96:99], a[92:95], a[232:235], v[96:99]\n"
        "v_mfma_f32_16x16x32_bf16 v[100:103], a[96:99], a[248:251], 0\n"
        "ds_read_b128 v[222:225], v251 offset:4608\n"
        "v_mfma_f32_16x16x32_bf16 v[100:103], a[100:103], a[216:219], v[100:103]\n"
        "buffer_load_dwordx4 v129, s[20:23], s64 offen offset:1024 lds\n"
        "v_mfma_f32_16x16x32_bf16 v[100:103], a[104:107], a[220:223], v[100:103]\n"
        "s_waitcnt lgkmcnt(3)\n"
        "v_pk_min_u16 v162, v162, s26\n"
        "v_pk_min_u16 v163, v163, s26\n"
        "v_pk_min_u16 v164, v164, s26\n"
        "v_mfma_f32_16x16x32_bf16 v[100:103], a[108:111], a[224:227], v[100:103]\n"
        "v_cvt_pk_bf16_f32 v212, v96, v97\n"
        "v_cvt_pk_bf16_f32 v213, v98, v99\n"
        "v_pk_min_u16 v165, v165, s26\n"
        "v_mfma_f32_16x16x32_bf16 v[100:103], a[112:115], a[228:231], v[100:103]\n"
        "v_mfma_f32_16x16x32_bf16 v[100:103], a[116:119], a[232:235], v[100:103]\n"
        "v_mfma_f32_16x16x32_bf16 v[112:115], a[120:123], a[248:251], 0\n"
        "buffer_load_dwordx4 v130, s[20:23], s64 offen offset:2048 lds\n"
        "s_waitcnt lgkmcnt(2)\n"
        "v_pk_min_u16 v182, v182, s26\n"
        "v_pk_min_u16 v183, v183, s26\n"
        "v_mfma_f32_16x16x32_bf16 v[112:115], a[124:127], a[216:219], v[112:115]\n"
        "v_pk_min_u16 v184, v184, s26\n"
        "v_pk_min_u16 v185, v185, s26\n"
        "v_mfma_f32_16x16x32_bf16 v[112:115], a[128:131], a[220:223], v[112:115]\n"
        "v_cvt_pk_bf16_f32 v214, v100, v101\n"
        "v_cvt_pk_bf16_f32 v215, v102, v103\n"
        "v_mfma_f32_16x16x32_bf16 v[112:115], a[132:135], a[224:227], v[112:115]\n"
        "s_add_u32 m0, s40, 12288\n"
        "v_mfma_f32_16x16x32_bf16 v[112:115], a[136:139], a[228:231], v[112:115]\n"
        "s_add_u32 s64, s65, 148224\n"
        "s_waitcnt lgkmcnt(1)\n"
        "v_pk_min_u16 v202, v202, s26\n"
        "v_pk_min_u16 v203, v203, s26\n"
        "v_pk_min_u16 v204, v204, s26\n"
        "v_mfma_f32_16x16x32_bf16 v[112:115], a[140:143], a[232:235], v[112:115]\n"
        "buffer_load_dwordx4 v128, s[20:23], s64 offen lds\n"
        "v_pk_min_u16 v205, v205, s26\n"
        "v_mfma_f32_16x16x32_bf16 v[64:67], v[152:155], v[156:159], v[0:3]\n"
        "v_mfma_f32_16x16x32_bf16 v[64:67], v[168:171], v[160:163], v[64:67]\n"
        "v_mfma_f32_16x16x32_bf16 v[68:71], v[172:175], v[156:159], v[4:7]\n"
        "s_waitcnt lgkmcnt(0)\n"
        "v_pk_min_u16 v222, v222, s26\n"
        "v_pk_min_u16 v223, v223, s26\n"
        "v_pk_min_u16 v224, v224, s26\n"
        "v_mfma_f32_16x16x32_bf16 v[68:71], v[188:191], v[164:167], v[68:71]\n"
        "ds_read_b128 v[162:165], v252 offset:0\n"
        "v_cvt_pk_bf16_f32 v230, v112, v113\n"
        "v_cvt_pk_bf16_f32 v231, v114, v115\n"
        "v_mfma_f32_16x16x32_bf16 v[80:83], v[152:155], v[176:179], v[16:19]\n"
        "buffer_load_dwordx4 v129, s[20:23], s64 offen offset:1024 lds\n"
        "v_pk_min_u16 v225, v225, s26\n"
        "v_max3_f32 v232, v64, v65, v66\n"
        "v_mfma_f32_16x16x32_bf16 v[80:83], v[168:171], v[180:183], v[80:83]\n"
        "v_max3_f32 v232, v232, v67, v68\n"
        "v_max3_f32 v232, v232, v69, v70\n"
        "v_mfma_f32_16x16x32_bf16 v[84:87], v[172:175], v[176:179], v[20:23]\n"
        "v_mfma_f32_16x16x32_bf16 v[84:87], v[188:191], v[184:187], v[84:87]\n"
        "ds_read_b128 v[182:185], v252 offset:512\n"
        "v_mfma_f32_16x16x32_bf16 v[96:99], v[152:155], v[196:199], v[32:35]\n"
        "v_mfma_f32_16x16x32_bf16 v[96:99], v[168:171], v[200:203], v[96:99]\n"
        "buffer_load_dwordx4 v130, s[20:23], s64 offen offset:2048 lds\n"
        "v_max3_f32 v233, v80, v81, v82\n"
        "v_mfma_f32_16x16x32_bf16 v[100:103], v[172:175], v[196:199], v[36:39]\n"
        "v_mfma_f32_16x16x32_bf16 v[100:103], v[188:191], v[204:207], v[100:103]\n"
        "ds_read_b128 v[202:205], v252 offset:4096\n"
        "s_waitcnt vmcnt(15)\n"
        "s_barrier\n"
        "v_max3_f32 v233, v233, v83, v84\n"
        "v_max3_f32 v233, v233, v85, v86\n"
        "v_mfma_f32_16x16x32_bf16 v[112:115], v[152:155], v[216:219], v[48:51]\n"
        "s_add_u32 m0, s40, 24576\n"
        "ds_read_b128 a[236:239], v240 offset:0\n"
        "s_waitcnt lgkmcnt(3)\n"
        "v_pk_min_u16 v162, v162, s26\n"
        "v_pk_min_u16 v163, v163, s26\n"
        "v_mfma_f32_16x16x32_bf16 v[112:115], v[168:171], v[220:223], v[112:115]\n"
        "s_add_u32 s64, s65, 185088\n"
        "ds_read_b128 a[240:243], v241 offset:0\n"
        "v_pk_min_u16 v164, v164, s26\n"
        "v_pk_min_u16 v165, v165, s26\n"
        "v_mfma_f32_16x16x32_bf16 v[116:119], v[172:175], v[216:219], v[52:55]\n"
        "buffer_load_dwordx4 v128, s[20:23], s64 offen lds\n"
        "ds_read_b128 a[244:247], v240 offset:128\n"
        "v_max3_f32 v234, v96, v97, v98\n"
        "v_mfma_f32_16x16x32_bf16 v[116:119], v[188:191], v[224:227], v[116:119]\n"
        "ds_read_b128 v[222:225], v252 offset:4608\n"
        "ds_read_b128 a[248:251], v241 offset:128\n"
        "v_max3_f32 v234, v234, v99, v100\n"
        "v_mfma_f32_16x16x32_bf16 v[72:75], v[192:195], v[156:159], v[8:11]\n"
        "ds_read_b128 a[216:219], v240 offset:256\n"
        "s_waitcnt lgkmcnt(7)\n"
        "v_pk_min_u16 v182, v182, s26\n"
        "v_pk_min_u16 v183, v183, s26\n"
        "v_mfma_f32_16x16x32_bf16 v[72:75], v[208:211], v[160:163], v[72:75]\n"
        "ds_read_b128 a[220:223], v241 offset:256\n"
        "v_pk_min_u16 v184, v184, s26\n"
        "v_pk_min_u16 v185, v185, s26\n"
        "v_mfma_f32_16x16x32_bf16 v[76:79], v[212:215], v[156:159], v[12:15]\n"
        "v_max3_f32 v234, v234, v101, v102\n"
        "v_max3_f32 v235, v112, v113, v114\n"
        "v_max3_f32 v235, v235, v115, v116\n"
        "v_mfma_f32_16x16x32_bf16 v[76:79], v[228:231], v[164:167], v[76:79]\n"
        "buffer_load_dwordx4 v129, s[20:23], s64 offen offset:1024 lds\n"
        "v_max3_f32 v232, v232, v71, v72\n"
        "v_max3_f32 v232, v232, v73, v74\n"
        "v_mfma_f32_16x16x32_bf16 v[88:91], v[192:195], v[176:179], v[24:27]\n"
        "s_waitcnt lgkmcnt(7)\n"
        "v_pk_min_u16 v202, v202, s26\n"
        "v_pk_min_u16 v203, v203, s26\n"
        "v_pk_min_u16 v204, v204, s26\n"
        "v_mfma_f32_16x16x32_bf16 v[88:91], v[208:211], v[180:183], v[88:91]\n"
        "v_pk_min_u16 v205, v205, s26\n"
        "v_max3_f32 v232, v232, v75, v76\n"
        "v_max3_f32 v232, v232, v77, v78\n"
        "v_mfma_f32_16x16x32_bf16 v[92:95], v[212:215], v[176:179], v[28:31]\n"
        "v_max_f32 v232, v232, v79\n"
        "v_max3_f32 v235, v235, v117, v118\n"
        "v_xor_b32 v251, 0x2000, v251\n"
        "v_mfma_f32_16x16x32_bf16 v[92:95], v[228:231], v[184:187], v[92:95]\n"
        "v_max3_f32 v233, v233, v87, v88\n"
        "v_max3_f32 v233, v233, v89, v90\n"
        "v_xor_b32 v252, 0x2000, v252\n"
        "v_mfma_f32_16x16x32_bf16 v[104:107], v[192:195], v[196:199], v[40:43]\n"
        "buffer_load_dwordx4 v130, s[20:23], s64 offen offset:2048 lds\n"
        "s_waitcnt lgkmcnt(3)\n"
        "v_pk_min_u16 v222, v222, s26\n"
        "v_pk_min_u16 v223, v223, s26\n"
        "v_mfma_f32_16x16x32_bf16 v[104:107], v[208:211], v[200:203], v[104:107]\n"
        "v_pk_min_u16 v224, v224, s26\n"
        "v_pk_min_u16 v225, v225, s26\n"
        "v_max3_f32 v233, v233, v91, v92\n"
        "v_mfma_f32_16x16x32_bf16 v[108:111], v[212:215], v[196:199], v[44:47]\n"
        "s_waitcnt vmcnt(9)\n"
        "s_barrier\n"
        "v_max3_f32 v233, v233, v93, v94\n"
        "v_max_f32 v233, v233, v95\n"
        "v_mfma_f32_16x16x32_bf16 v[108:111], v[228:231], v[204:207], v[108:111]\n"
        "ds_read_b64_tr_b16 a[144:145], v131 offset:36864\n"
        "ds_read_b64_tr_b16 a[146:147], v131 offset:38400\n"
        "v_max3_f32 v234, v234, v103, v104\n"
        "v_mfma_f32_16x16x32_bf16 v[120:123], v[192:195], v[216:219], v[56:59]\n"
        "s_mov_b32 m0, s48\n"
        "s_xor_b32 s48, s48, 8192\n"
        "ds_read_b64_tr_b16 a[148:149], v131 offset:49152\n"
        "ds_read_b64_tr_b16 a[150:151], v131 offset:50688\n"
        "v_mfma_f32_16x16x32_bf16 v[120:123], v[208:211], v[220:223], v[120:123]\n"
        "buffer_load_dwordx4 v249, s[12:15], s36 offen offset:0 lds\n"
        "ds_read_b64_tr_b16 a[152:153], v131 offset:61440\n"
        "v_mfma_f32_16x16x32_bf16 v[124:127], v[212:215], v[216:219], v[60:63]\n"
        "ds_read_b64_tr_b16 a[154:155], v131 offset:62976\n"
        "ds_read_b64_tr_b16 a[168:169], v132 offset:36864\n"
        "v_xor_b32 v253, 32, v249\n"
        "v_mfma_f32_16x16x32_bf16 v[124:127], v[228:231], v[224:227], v[124:127]\n"
        "ds_read_b64_tr_b16 a[170:171], v132 offset:38400\n"
        "ds_read_b64_tr_b16 a[172:173], v132 offset:49152\n"
        "v_max3_f32 v234, v234, v105, v106\n"
        "v_mfma_f32_16x16x32_bf16 v[212:215], a[72:75], a[236:239], 0\n"
        "ds_read_b64_tr_b16 a[174:175], v132 offset:50688\n"
        "ds_read_b64_tr_b16 a[176:177], v132 offset:61440\n"
        "v_max3_f32 v234, v234, v107, v108\n"
        "v_mfma_f32_16x16x32_bf16 v[212:215], a[76:79], a[240:243], v[212:215]\n"
        "ds_read_b64_tr_b16 a[178:179], v132 offset:62976\n"
        "ds_read_b64_tr_b16 a[192:193], v133 offset:36864\n"
        "v_max3_f32 v234, v234, v109, v110\n"
        "v_mfma_f32_16x16x32_bf16 v[212:215], a[80:83], a[244:247], v[212:215]\n"
        "s_waitcnt lgkmcnt(15)\n"
        "buffer_load_dwordx4 v253, s[12:15], s36 offen offset:1024 lds\n"
        "s_add_u32 s36, s36, 2097152\n"
        "ds_read_b64_tr_b16 a[194:195], v133 offset:38400\n"
        "v_mfma_f32_16x16x32_bf16 v[212:215], a[84:87], a[248:251], v[212:215]\n"
        "s_waitcnt lgkmcnt(15)\n"
        "ds_read_b64_tr_b16 a[196:197], v133 offset:49152\n"
        "ds_read_b64_tr_b16 a[198:199], v133 offset:50688\n"
        "v_max_f32 v234, v234, v111\n"
        "v_mfma_f32_16x16x32_bf16 v[212:215], a[88:91], a[216:219], v[212:215]\n"
        "s_waitcnt lgkmcnt(15)\n"
        "ds_read_b64_tr_b16 a[200:201], v133 offset:61440\n"
        "ds_read_b64_tr_b16 a[202:203], v133 offset:62976\n"
        "v_max3_f32 v235, v235, v119, v120\n"
        "v_mfma_f32_16x16x32_bf16 v[212:215], a[92:95], a[220:223], v[212:215]\n"
        "v_permlane32_swap_b32 v232, v233\n"
        "v_max_f32 v232, v232, v233\n"
        "v_mfma_f32_16x16x32_bf16 v[216:219], a[96:99], a[236:239], 0\n"
        "v_mov_b32 v233, v232\n"
        "v_max3_f32 v235, v235, v121, v122\n"
        "v_max3_f32 v235, v235, v123, v124\n"
        "v_mfma_f32_16x16x32_bf16 v[216:219], a[100:103], a[240:243], v[216:219]\n"
        "v_permlane16_swap_b32 v232, v233\n"
        "v_max_f32 v232, v232, v233\n"
        "v_mfma_f32_16x16x32_bf16 v[216:219], a[104:107], a[244:247], v[216:219]\n"
        "v_mul_f32 v236, s25, v232\n"
        "v_mul_f32 v237, s25, v232\n"
        "v_max3_f32 v235, v235, v125, v126\n"
        "v_mfma_f32_16x16x32_bf16 v[216:219], a[108:111], a[248:251], v[216:219]\n"
        "v_permlane32_swap_b32 v236, v237\n"
        "v_max_f32 v235, v235, v127\n"
        "v_mfma_f32_16x16x32_bf16 v[216:219], a[112:115], a[216:219], v[216:219]\n"
        "v_cvt_pk_bf16_f32 v152, v212, v213\n"
        "v_permlane32_swap_b32 v234, v235\n"
        "v_mfma_f32_16x16x32_bf16 v[216:219], a[116:119], a[220:223], v[216:219]\n"
        "v_max_f32 v234, v234, v235\n"
        "v_mov_b32 v235, v234\n"
        "v_cvt_pk_bf16_f32 v153, v214, v215\n"
        "v_mfma_f32_16x16x32_bf16 v[162:165], a[120:123], a[236:239], 0\n"
        "v_permlane16_swap_b32 v234, v235\n"
        "v_max_f32 v234, v234, v235\n"
        "v_mfma_f32_16x16x32_bf16 v[162:165], a[124:127], a[240:243], v[162:165]\n"
        "v_mul_f32 v238, s25, v234\n"
        "v_mul_f32 v239, s25, v234\n"
        "v_cvt_pk_bf16_f32 v154, v216, v217\n"
        "v_mfma_f32_16x16x32_bf16 v[162:165], a[128:131], a[244:247], v[162:165]\n"
        "ds_read_b128 a[224:227], v240 offset:1536\n"
        "v_permlane32_swap_b32 v238, v239\n"
        "v_mfma_f32_16x16x32_bf16 v[162:165], a[132:135], a[248:251], v[162:165]\n"
        "ds_read_b128 a[228:231], v241 offset:1536\n"
        "v_cvt_pk_bf16_f32 v155, v218, v219\n"
        "v_fma_f32 v64, v64, s24, v236\n"
        "v_mfma_f32_16x16x32_bf16 v[162:165], a[136:139], a[216:219], v[162:165]\n"
        "ds_read_b128 a[232:235], v240 offset:1664\n"
        "v_exp_f32 v64, v64\n"
        "v_mfma_f32_16x16x32_bf16 v[162:165], a[140:143], a[220:223], v[162:165]\n"
        "v_fma_f32 v65, v65, s24, v236\n"
        "v_exp_f32 v65, v65\n"
        "v_mfma_f32_16x16x32_bf16 v[212:215], a[0:3], a[236:239], 0\n"
        "v_cvt_pk_bf16_f32 v64, v64, v65\n"
        "v_fma_f32 v66, v66, s24, v236\n"
        "v_fma_f32 v67, v67, s24, v236\n"
        "v_mfma_f32_16x16x32_bf16 v[212:215], a[4:7], a[240:243], v[212:215]\n"
        "v_exp_f32 v66, v66\n"
        "v_cvt_pk_bf16_f32 v168, v162, v163\n"
        "v_mfma_f32_16x16x32_bf16 v[212:215], a[8:11], a[244:247], v[212:215]\n"
        "v_exp_f32 v67, v67\n"
        "v_cvt_pk_bf16_f32 v169, v164, v165\n"
        "v_mfma_f32_16x16x32_bf16 v[212:215], a[12:15], a[248:251], v[212:215]\n"
        "v_cvt_pk_bf16_f32 v65, v66, v67\n"
        "v_fma_f32 v68, v68, s24, v236\n"
        "v_fma_f32 v69, v69, s24, v236\n"
        "v_mfma_f32_16x16x32_bf16 v[212:215], a[16:19], a[216:219], v[212:215]\n"
        "v_exp_f32 v68, v68\n"
        "v_fma_f32 v70, v70, s24, v236\n"
        "v_mfma_f32_16x16x32_bf16 v[212:215], a[20:23], a[220:223], v[212:215]\n"
        "v_exp_f32 v69, v69\n"
        "v_fma_f32 v71, v71, s24, v236\n"
        "v_mfma_f32_16x16x32_bf16 v[216:219], a[24:27], a[236:239], 0\n"
        "s_waitcnt lgkmcnt(3)\n"
        "s_waitcnt vmcnt(2)\n"
        "s_barrier\n"
        "v_exp_f32 v70, v70\n"
        "v_cvt_pk_bf16_f32 v66, v68, v69\n"
        "v_mfma_f32_16x16x32_bf16 v[216:219], a[28:31], a[240:243], v[216:219]\n"
        "ds_read_b64_tr_b16 a[156:157], v131 offset:0\n"
        "s_add_u32 m0, s65, 36864\n"
        "ds_read_b64_tr_b16 a[158:159], v131 offset:1536\n"
        "v_cvt_pk_bf16_f32 v156, v212, v213\n"
        "v_mfma_f32_16x16x32_bf16 v[216:219], a[32:35], a[244:247], v[216:219]\n"
        "s_add_u32 s64, s65, 36864\n"
        "ds_read_b64_tr_b16 a[160:161], v131 offset:12288\n"
        "ds_read_b64_tr_b16 a[162:163], v131 offset:13824\n"
        "v_cvt_pk_bf16_f32 v157, v214, v215\n"
        "v_mfma_f32_16x16x32_bf16 v[216:219], a[36:39], a[248:251], v[216:219]\n"
        "buffer_load_dwordx4 v142, s[52:55], s64 offen lds\n"
        "ds_read_b64_tr_b16 a[164:165], v131 offset:24576\n"
        "v_fma_f32 v72, v72, s24, v236\n"
        "v_mfma_f32_16x16x32_bf16 v[216:219], a[40:43], a[216:219], v[216:219]\n"
        "ds_read_b64_tr_b16 a[166:167], v131 offset:26112\n"
        "ds_read_b64_tr_b16 a[180:181], v132 offset:0\n"
        "v_fma_f32 v73, v73, s24, v236\n"
        "v_mfma_f32_16x16x32_bf16 v[216:219], a[44:47], a[220:223], v[216:219]\n"
        "ds_read_b64_tr_b16 a[182:183], v132 offset:1536\n"
        "ds_read_b64_tr_b16 a[184:185], v132 offset:12288\n"
        "v_fma_f32 v74, v74, s24, v236\n"
        "v_mfma_f32_16x16x32_bf16 v[162:165], a[48:51], a[236:239], 0\n"
        "ds_read_b128 a[236:239], v241 offset:1664\n"
        "ds_read_b64_tr_b16 a[186:187], v132 offset:13824\n"
        "v_fma_f32 v75, v75, s24, v236\n"
        "v_mfma_f32_16x16x32_bf16 v[162:165], a[52:55], a[240:243], v[162:165]\n"
        "ds_read_b128 a[240:243], v240 offset:1792\n"
        "ds_read_b64_tr_b16 a[188:189], v132 offset:24576\n"
        "v_cvt_pk_bf16_f32 v158, v216, v217\n"
        "v_mfma_f32_16x16x32_bf16 v[162:165], a[56:59], a[244:247], v[162:165]\n"
        "ds_read_b128 a[244:247], v241 offset:1792\n"
        "buffer_load_dwordx4 v142, s[52:55], s64 offen offset:1024 lds\n"
        "v_cvt_pk_bf16_f32 v159, v218, v219\n"
        "v_mfma_f32_16x16x32_bf16 v[162:165], a[60:63], a[248:251], v[162:165]\n"
        "ds_read_b64_tr_b16 a[190:191], v132 offset:26112\n"
        "ds_read_b64_tr_b16 a[204:205], v133 offset:0\n"
        "v_fma_f32 v76, v76, s24, v236\n"
        "v_mfma_f32_16x16x32_bf16 v[162:165], a[64:67], a[216:219], v[162:165]\n"
        "ds_read_b64_tr_b16 a[206:207], v133 offset:1536\n"
        "ds_read_b64_tr_b16 a[208:209], v133 offset:12288\n"
        "v_fma_f32 v77, v77, s24, v236\n"
        "v_mfma_f32_16x16x32_bf16 v[162:165], a[68:71], a[220:223], v[162:165]\n"
        "s_waitcnt lgkmcnt(15)\n"
        "ds_read_b64_tr_b16 a[210:211], v133 offset:13824\n"
        "ds_read_b64_tr_b16 a[212:213], v133 offset:24576\n"
        "v_fma_f32 v78, v78, s24, v236\n"
        "v_mfma_f32_16x16x32_bf16 v[212:215], a[72:75], a[224:227], 0\n"
        "s_waitcnt lgkmcnt(15)\n"
        "ds_read_b64_tr_b16 a[214:215], v133 offset:26112\n"
        "v_exp_f32 v71, v71\n"
        "v_mfma_f32_16x16x32_bf16 v[212:215], a[76:79], a[228:231], v[212:215]\n"
        "s_waitcnt lgkmcnt(15)\n"
        "buffer_load_dwordx4 v142, s[52:55], s64 offen offset:2048 lds\n"
        "v_exp_f32 v72, v72\n"
        "v_mfma_f32_16x16x32_bf16 v[212:215], a[80:83], a[232:235], v[212:215]\n"
        "s_waitcnt lgkmcnt(11)\n"
        "v_exp_f32 v73, v73\n"
        "v_cvt_pk_bf16_f32 v160, v162, v163\n"
        "v_mfma_f32_16x16x32_bf16 v[212:215], a[84:87], a[236:239], v[212:215]\n"
        "s_waitcnt lgkmcnt(9)\n"
        "v_exp_f32 v74, v74\n"
        "v_cvt_pk_bf16_f32 v161, v164, v165\n"
        "v_mfma_f32_16x16x32_bf16 v[212:215], a[88:91], a[240:243], v[212:215]\n"
        "s_waitcnt lgkmcnt(7)\n"
        "s_add_u32 m0, s65, 39936\n"
        "v_cvt_pk_bf16_f32 v166, v162, v163\n"
        "v_cvt_pk_bf16_f32 v167, v164, v165\n"
        "v_cvt_pk_bf16_f32 v67, v70, v71\n"
        "v_mfma_f32_16x16x32_bf16 v[212:215], a[92:95], a[244:247], v[212:215]\n"
        "s_add_u32 s64, s65, 39936\n"
        "v_exp_f32 v75, v75\n"
        "v_cvt_pk_bf16_f32 v68, v72, v73\n"
        "v_mfma_f32_16x16x32_bf16 v[216:219], a[96:99], a[224:227], 0\n"
        "buffer_load_dwordx4 v142, s[52:55], s64 offen lds\n"
        "v_exp_f32 v76, v76\n"
        "v_mfma_f32_16x16x32_bf16 v[216:219], a[100:103], a[228:231], v[216:219]\n"
        "v_exp_f32 v77, v77\n"
        "v_cvt_pk_bf16_f32 v172, v212, v213\n"
        "v_mfma_f32_16x16x32_bf16 v[216:219], a[104:107], a[232:235], v[216:219]\n"
        "v_exp_f32 v78, v78\n"
        "v_cvt_pk_bf16_f32 v173, v214, v215\n"
        "v_mfma_f32_16x16x32_bf16 v[216:219], a[108:111], a[236:239], v[216:219]\n"
        "v_cvt_pk_bf16_f32 v69, v74, v75\n"
        "v_cvt_pk_bf16_f32 v70, v76, v77\n"
        "v_fma_f32 v79, v79, s24, v236\n"
        "v_mfma_f32_16x16x32_bf16 v[216:219], a[112:115], a[240:243], v[216:219]\n"
        "s_waitcnt lgkmcnt(0)\n"
        "s_barrier\n"
        "v_exp_f32 v79, v79\n"
        "v_fma_f32 v80, v80, s24, v237\n"
        "v_mfma_f32_16x16x32_bf16 v[216:219], a[116:119], a[244:247], v[216:219]\n"
        "buffer_load_dwordx4 v142, s[52:55], s64 offen offset:1024 lds\n"
        "v_exp_f32 v80, v80\n"
        "v_mfma_f32_16x16x32_bf16 v[162:165], a[120:123], a[224:227], 0\n"
        "v_cvt_pk_bf16_f32 v71, v78, v79\n"
        "v_fma_f32 v81, v81, s24, v237\n"
        "v_fma_f32 v82, v82, s24, v237\n"
        "v_mfma_f32_16x16x32_bf16 v[162:165], a[124:127], a[228:231], v[162:165]\n"
        "v_exp_f32 v81, v81\n"
        "v_cvt_pk_bf16_f32 v174, v216, v217\n"
        "v_mfma_f32_16x16x32_bf16 v[162:165], a[128:131], a[232:235], v[162:165]\n"
        "ds_read_b128 a[248:251], v240 offset:12288\n"
        "v_exp_f32 v82, v82\n"
        "v_mfma_f32_16x16x32_bf16 v[162:165], a[132:135], a[236:239], v[162:165]\n"
        "ds_read_b128 a[216:219], v241 offset:12288\n"
        "v_cvt_pk_bf16_f32 v175, v218, v219\n"
        "v_cvt_pk_bf16_f32 v80, v80, v81\n"
        "v_mfma_f32_16x16x32_bf16 v[162:165], a[136:139], a[240:243], v[162:165]\n"
        "buffer_load_dwordx4 v142, s[52:55], s64 offen offset:2048 lds\n"
        "ds_read_b128 a[220:223], v240 offset:12416\n"
        "v_fma_f32 v83, v83, s24, v237\n"
        "v_mfma_f32_16x16x32_bf16 v[162:165], a[140:143], a[244:247], v[162:165]\n"
        "v_exp_f32 v83, v83\n"
        "v_fma_f32 v84, v84, s24, v237\n"
        "v_mfma_f32_16x16x32_bf16 v[212:215], a[0:3], a[224:227], 0\n"
        "v_exp_f32 v84, v84\n"
        "v_cvt_pk_bf16_f32 v81, v82, v83\n"
        "v_mfma_f32_16x16x32_bf16 v[212:215], a[4:7], a[228:231], v[212:215]\n"
        "s_add_u32 m0, s65, 43008\n"
        "v_fma_f32 v85, v85, s24, v237\n"
        "v_exp_f32 v85, v85\n"
        "v_mfma_f32_16x16x32_bf16 v[212:215], a[8:11], a[232:235], v[212:215]\n"
        "s_add_u32 s64, s65, 43008\n"
        "v_cvt_pk_bf16_f32 v190, v162, v163\n"
        "v_cvt_pk_bf16_f32 v191, v164, v165\n"
        "v_cvt_pk_bf16_f32 v82, v84, v85\n"
        "v_mfma_f32_16x16x32_bf16 v[212:215], a[12:15], a[236:239], v[212:215]\n"
        "buffer_load_dwordx4 v142, s[52:55], s64 offen lds\n"
        "v_fma_f32 v86, v86, s24, v237\n"
        "v_fma_f32 v87, v87, s24, v237\n"
        "v_mfma_f32_16x16x32_bf16 v[212:215], a[16:19], a[240:243], v[212:215]\n"
        "v_exp_f32 v86, v86\n"
        "v_fma_f32 v88, v88, s24, v237\n"
        "v_mfma_f32_16x16x32_bf16 v[212:215], a[20:23], a[244:247], v[212:215]\n"
        "v_exp_f32 v87, v87\n"
        "v_fma_f32 v89, v89, s24, v237\n"
        "v_mfma_f32_16x16x32_bf16 v[216:219], a[24:27], a[224:227], 0\n"
        "v_exp_f32 v88, v88\n"
        "v_cvt_pk_bf16_f32 v83, v86, v87\n"
        "v_mfma_f32_16x16x32_bf16 v[216:219], a[28:31], a[228:231], v[216:219]\n"
        "v_exp_f32 v89, v89\n"
        "v_fma_f32 v90, v90, s24, v237\n"
        "v_mfma_f32_16x16x32_bf16 v[216:219], a[32:35], a[232:235], v[216:219]\n"
        "buffer_load_dwordx4 v142, s[52:55], s64 offen offset:1024 lds\n"
        "v_exp_f32 v90, v90\n"
        "v_mfma_f32_16x16x32_bf16 v[216:219], a[36:39], a[236:239], v[216:219]\n"
        "v_cvt_pk_bf16_f32 v176, v212, v213\n"
        "v_cvt_pk_bf16_f32 v177, v214, v215\n"
        "v_cvt_pk_bf16_f32 v84, v88, v89\n"
        "v_mfma_f32_16x16x32_bf16 v[216:219], a[40:43], a[240:243], v[216:219]\n"
        "v_fma_f32 v91, v91, s24, v237\n"
        "v_exp_f32 v91, v91\n"
        "v_mfma_f32_16x16x32_bf16 v[216:219], a[44:47], a[244:247], v[216:219]\n"
        "v_cvt_pk_bf16_f32 v85, v90, v91\n"
        "v_fma_f32 v92, v92, s24, v237\n"
        "v_fma_f32 v93, v93, s24, v237\n"
        "v_mfma_f32_16x16x32_bf16 v[162:165], a[48:51], a[224:227], 0\n"
        "ds_read_b128 a[224:227], v241 offset:12416\n"
        "v_exp_f32 v92, v92\n"
        "v_mfma_f32_16x16x32_bf16 v[162:165], a[52:55], a[228:231], v[162:165]\n"
        "ds_read_b128 a[228:231], v240 offset:12544\n"
        "buffer_load_dwordx4 v142, s[52:55], s64 offen offset:2048 lds\n"
        "v_cvt_pk_bf16_f32 v178, v216, v217\n"
        "v_mfma_f32_16x16x32_bf16 v[162:165], a[56:59], a[232:235], v[162:165]\n"
        "ds_read_b128 a[232:235], v241 offset:12544\n"
        "v_exp_f32 v93, v93\n"
        "v_mfma_f32_16x16x32_bf16 v[162:165], a[60:63], a[236:239], v[162:165]\n"
        "v_cvt_pk_bf16_f32 v179, v218, v219\n"
        "v_cvt_pk_bf16_f32 v86, v92, v93\n"
        "v_fma_f32 v94, v94, s24, v237\n"
        "v_mfma_f32_16x16x32_bf16 v[162:165], a[64:67], a[240:243], v[162:165]\n"
        "s_add_u32 m0, s65, 0\n"
        "v_exp_f32 v94, v94\n"
        "v_fma_f32 v95, v95, s24, v237\n"
        "v_mfma_f32_16x16x32_bf16 v[162:165], a[68:71], a[244:247], v[162:165]\n"
        "s_waitcnt lgkmcnt(5)\n"
        "s_add_u32 s64, s65, 0\n"
        "v_exp_f32 v95, v95\n"
        "v_fma_f32 v96, v96, s24, v238\n"
        "v_mfma_f32_16x16x32_bf16 v[72:75], a[72:75], a[248:251], 0\n"
        "s_waitcnt lgkmcnt(4)\n"
        "buffer_load_dwordx4 v142, s[52:55], s64 offen lds\n"
        "v_exp_f32 v96, v96\n"
        "v_mfma_f32_16x16x32_bf16 v[72:75], a[76:79], a[216:219], v[72:75]\n"
        "s_waitcnt lgkmcnt(3)\n"
        "v_cvt_pk_bf16_f32 v87, v94, v95\n"
        "v_cvt_pk_bf16_f32 v180, v162, v163\n"
        "v_cvt_pk_bf16_f32 v181, v164, v165\n"
        "v_mfma_f32_16x16x32_bf16 v[72:75], a[80:83], a[220:223], v[72:75]\n"
        "s_waitcnt lgkmcnt(2)\n"
        "v_cvt_pk_bf16_f32 v186, v162, v163\n"
        "v_cvt_pk_bf16_f32 v187, v164, v165\n"
        "v_fma_f32 v97, v97, s24, v238\n"
        "v_mfma_f32_16x16x32_bf16 v[72:75], a[84:87], a[224:227], v[72:75]\n"
        "s_waitcnt lgkmcnt(1)\n"
        "v_exp_f32 v97, v97\n"
        "v_fma_f32 v98, v98, s24, v238\n"
        "v_mfma_f32_16x16x32_bf16 v[72:75], a[88:91], a[228:231], v[72:75]\n"
        "s_waitcnt lgkmcnt(0)\n"
        "v_exp_f32 v98, v98\n"
        "v_accvgpr_read_b32 v232, a252\n"
        "v_mfma_f32_16x16x32_bf16 v[72:75], a[92:95], a[232:235], v[72:75]\n"
        "buffer_load_dwordx4 v142, s[52:55], s64 offen offset:1024 lds\n"
        "v_accvgpr_read_b32 v233, a253\n"
        "v_accvgpr_read_b32 v234, a254\n"
        "v_mfma_f32_16x16x32_bf16 v[76:79], a[96:99], a[248:251], 0\n"
        "v_mov_b32 v235, 0x3f803f80\n"
        "v_accvgpr_write_b32 a252, v235\n"
        "v_accvgpr_write_b32 a253, v235\n"
        "v_mfma_f32_16x16x32_bf16 v[76:79], a[100:103], a[216:219], v[76:79]\n"
        "v_cvt_pk_bf16_f32 v192, v72, v73\n"
        "v_cvt_pk_bf16_f32 v193, v74, v75\n"
        "v_accvgpr_write_b32 a254, v235\n"
        "v_mfma_f32_16x16x32_bf16 v[76:79], a[104:107], a[220:223], v[76:79]\n"
        "v_accvgpr_write_b32 a255, v235\n"
        "v_cvt_pk_bf16_f32 v96, v96, v97\n"
        "v_fma_f32 v99, v99, s24, v238\n"
        "v_mfma_f32_16x16x32_bf16 v[76:79], a[108:111], a[224:227], v[76:79]\n"
        "v_exp_f32 v99, v99\n"
        "v_fma_f32 v100, v100, s24, v238\n"
        "v_mfma_f32_16x16x32_bf16 v[76:79], a[112:115], a[228:231], v[76:79]\n"
        "buffer_load_dwordx4 v142, s[52:55], s64 offen offset:2048 lds\n"
        "v_exp_f32 v100, v100\n"
        "v_mfma_f32_16x16x32_bf16 v[76:79], a[116:119], a[232:235], v[76:79]\n"
        "v_cvt_pk_bf16_f32 v97, v98, v99\n"
        "v_fma_f32 v101, v101, s24, v238\n"
        "v_fma_f32 v102, v102, s24, v238\n"
        "v_mfma_f32_16x16x32_bf16 v[162:165], a[120:123], a[248:251], 0\n"
        "ds_read_b128 a[236:239], v150 offset:0\n"
        "v_exp_f32 v101, v101\n"
        "v_mfma_f32_16x16x32_bf16 v[162:165], a[124:127], a[216:219], v[162:165]\n"
        "s_add_u32 m0, s65, 3072\n"
        "ds_read_b128 a[240:243], v151 offset:0\n"
        "v_exp_f32 v102, v102\n"
        "v_mfma_f32_16x16x32_bf16 v[162:165], a[128:131], a[220:223], v[162:165]\n"
        "s_add_u32 s64, s65, 3072\n"
        "s_waitcnt vmcnt(3)\n"
        "s_barrier\n"
        "ds_read_b64_tr_b16 v[212:213], v232 offset:36864\n"
        "ds_read_b128 a[244:247], v150 offset:128\n"
        "v_mfma_f32_16x16x32_bf16 v[162:165], a[132:135], a[224:227], v[162:165]\n"
        "buffer_load_dwordx4 v142, s[52:55], s64 offen lds\n"
        "v_cvt_pk_bf16_f32 v194, v76, v77\n"
        "v_cvt_pk_bf16_f32 v195, v78, v79\n"
        "v_mfma_f32_16x16x32_bf16 v[162:165], a[136:139], a[228:231], v[162:165]\n"
        "ds_read_b64_tr_b16 v[214:215], v232 offset:43008\n"
        "v_cvt_pk_bf16_f32 v98, v100, v101\n"
        "v_fma_f32 v103, v103, s24, v238\n"
        "v_mfma_f32_16x16x32_bf16 v[162:165], a[140:143], a[232:235], v[162:165]\n"
        "v_exp_f32 v103, v103\n"
        "v_fma_f32 v104, v104, s24, v238\n"
        "v_mfma_f32_16x16x32_bf16 v[72:75], a[0:3], a[248:251], 0\n"
        "ds_read_b64_tr_b16 v[216:217], v232 offset:55296\n"
        "v_exp_f32 v104, v104\n"
        "v_mfma_f32_16x16x32_bf16 v[72:75], a[4:7], a[216:219], v[72:75]\n"
        "v_cvt_pk_bf16_f32 v99, v102, v103\n"
        "v_fma_f32 v105, v105, s24, v238\n"
        "v_cvt_pk_bf16_f32 v208, v162, v163\n"
        "v_mfma_f32_16x16x32_bf16 v[72:75], a[8:11], a[220:223], v[72:75]\n"
        "buffer_load_dwordx4 v142, s[52:55], s64 offen offset:1024 lds\n"
        "ds_read_b64_tr_b16 v[218:219], v232 offset:61440\n"
        "v_cvt_pk_bf16_f32 v209, v164, v165\n"
        "v_mfma_f32_16x16x32_bf16 v[72:75], a[12:15], a[224:227], v[72:75]\n"
        "v_exp_f32 v105, v105\n"
        "v_fma_f32 v106, v106, s24, v238\n"
        "v_mfma_f32_16x16x32_bf16 v[72:75], a[16:19], a[228:231], v[72:75]\n"
        "ds_read_b64_tr_b16 v[220:221], v233 offset:49152\n"
        "v_exp_f32 v106, v106\n"
        "v_mfma_f32_16x16x32_bf16 v[72:75], a[20:23], a[232:235], v[72:75]\n"
        "v_cvt_pk_bf16_f32 v100, v104, v105\n"
        "v_fma_f32 v107, v107, s24, v238\n"
        "v_fma_f32 v108, v108, s24, v238\n"
        "v_mfma_f32_16x16x32_bf16 v[76:79], a[24:27], a[248:251], 0\n"
        "ds_read_b64_tr_b16 v[222:223], v233 offset:50688\n"
        "v_exp_f32 v107, v107\n"
        "v_mfma_f32_16x16x32_bf16 v[76:79], a[28:31], a[216:219], v[76:79]\n"
        "buffer_load_dwordx4 v142, s[52:55], s64 offen offset:2048 lds\n"
        "v_exp_f32 v108, v108\n"
        "v_mfma_f32_16x16x32_bf16 v[76:79], a[32:35], a[220:223], v[76:79]\n"
        "ds_read_b64_tr_b16 v[224:225], v232 offset:36896\n"
        "v_cvt_pk_bf16_f32 v196, v72, v73\n"
        "v_cvt_pk_bf16_f32 v197, v74, v75\n"
        "v_mfma_f32_16x16x32_bf16 v[76:79], a[36:39], a[224:227], v[76:79]\n"
        "v_cvt_pk_bf16_f32 v101, v106, v107\n"
        "v_fma_f32 v109, v109, s24, v238\n"
        "v_fma_f32 v110, v110, s24, v238\n"
        "v_mfma_f32_16x16x32_bf16 v[76:79], a[40:43], a[228:231], v[76:79]\n"
        "s_add_u32 m0, s65, 6144\n"
        "ds_read_b64_tr_b16 v[226:227], v232 offset:43040\n"
        "v_exp_f32 v109, v109\n"
        "v_mfma_f32_16x16x32_bf16 v[76:79], a[44:47], a[232:235], v[76:79]\n"
        "s_add_u32 s64, s65, 6144\n"
        "v_exp_f32 v110, v110\n"
        "v_cvt_pk_bf16_f32 v102, v108, v109\n"
        "v_mfma_f32_16x16x32_bf16 v[162:165], a[48:51], a[248:251], 0\n"
        "ds_read_b128 a[248:251], v151 offset:128\n"
        "buffer_load_dwordx4 v142, s[52:55], s64 offen lds\n"
        "v_fma_f32 v111, v111, s24, v238\n"
        "v_mfma_f32_16x16x32_bf16 v[162:165], a[52:55], a[216:219], v[162:165]\n"
        "ds_read_b128 a[216:219], v150 offset:256\n"
        "ds_read_b64_tr_b16 v[182:183], v232 offset:55328\n"
        "v_cvt_pk_bf16_f32 v198, v76, v77\n"
        "v_mfma_f32_16x16x32_bf16 v[162:165], a[56:59], a[220:223], v[162:165]\n"
        "ds_read_b128 a[220:223], v151 offset:256\n"
        "ds_read_b64_tr_b16 v[184:185], v232 offset:61472\n"
        "s_sub_u32 s31, s30, s28\n"
        "v_cvt_pk_bf16_f32 v199, v78, v79\n"
        "v_mfma_f32_16x16x32_bf16 v[162:165], a[60:63], a[224:227], v[162:165]\n"
        "s_sub_u32 s45, s28, s29\n"
        "v_exp_f32 v111, v111\n"
        "v_fma_f32 v112, v112, s24, v239\n"
        "v_mfma_f32_16x16x32_bf16 v[162:165], a[64:67], a[228:231], v[162:165]\n"
        "ds_read_b64_tr_b16 v[202:203], v233 offset:49184\n"
        "ds_read_b128 a[228:231], v150 offset:1536\n"
        "s_add_u32 s28, s28, s31\n"
        "v_cvt_pk_bf16_f32 v103, v110, v111\n"
        "v_mfma_f32_16x16x32_bf16 v[162:165], a[68:71], a[232:235], v[162:165]\n"
        "s_waitcnt lgkmcnt(15)\n"
        "buffer_load_dwordx4 v142, s[52:55], s64 offen offset:1024 lds\n"
        "ds_read_b128 a[232:235], v151 offset:1536\n"
        "v_fma_f32 v113, v113, s24, v239\n"
        "v_mfma_f32_16x16x32_bf16 v[72:75], a[236:239], a[144:147], 0\n"
        "s_waitcnt lgkmcnt(15)\n"
        "ds_read_b64_tr_b16 v[204:205], v233 offset:50720\n"
        "v_exp_f32 v112, v112\n"
        "v_mfma_f32_16x16x32_bf16 v[72:75], a[240:243], a[148:151], v[72:75]\n"
        "s_waitcnt lgkmcnt(15)\n"
        "v_exp_f32 v113, v113\n"
        "v_cvt_pk_bf16_f32 v200, v162, v163\n"
        "v_mfma_f32_16x16x32_bf16 v[72:75], a[244:247], a[152:155], v[72:75]\n"
        "s_waitcnt lgkmcnt(8)\n"
        "ds_read_b64_tr_b16 v[88:89], v232 offset:36928\n"
        "v_cvt_pk_bf16_f32 v201, v164, v165\n"
        "v_cvt_pk_bf16_f32 v206, v162, v163\n"
        "v_mfma_f32_16x16x32_bf16 v[72:75], a[248:251], a[156:159], v[72:75]\n"
        "s_waitcnt lgkmcnt(8)\n"
        "v_cvt_pk_bf16_f32 v207, v164, v165\n"
        "v_cvt_pk_bf16_f32 v112, v112, v113\n"
        "v_fma_f32 v114, v114, s24, v239\n"
        "v_mfma_f32_16x16x32_bf16 v[72:75], a[216:219], a[160:163], v[72:75]\n"
        "s_waitcnt lgkmcnt(6)\n"
        "buffer_load_dwordx4 v142, s[52:55], s64 offen offset:2048 lds\n"
        "s_add_u32 s41, s30, s40\n"
        "s_add_u32 s35, s47, 786432\n"
        "s_sub_u32 s46, s35, 100663296\n"
        "s_cmp_lt_u32 s46, 786432\n"
        "v_mfma_f32_16x16x32_bf16 v[72:75], a[220:223], a[164:167], v[72:75]\n"
        "s_cselect_b32 s35, s46, s35\n"
        "ds_read_b64_tr_b16 v[90:91], v232 offset:43072\n"
        "v_exp_f32 v114, v114\n"
        "v_mfma_f32_16x16x32_bf16 v[76:79], a[236:239], a[168:171], 0\n"
        "ds_read_b64_tr_b16 v[92:93], v232 offset:55360\n"
        "v_fma_f32 v115, v115, s24, v239\n"
        "v_fma_f32 v116, v116, s24, v239\n"
        "v_mfma_f32_16x16x32_bf16 v[76:79], a[240:243], a[172:175], v[76:79]\n"
        "v_exp_f32 v115, v115\n"
        "v_cvt_pk_bf16_f32 v128, v72, v73\n"
        "v_mfma_f32_16x16x32_bf16 v[76:79], a[244:247], a[176:179], v[76:79]\n"
        "ds_read_b64_tr_b16 v[94:95], v232 offset:61504\n"
        "v_exp_f32 v116, v116\n"
        "v_mfma_f32_16x16x32_bf16 v[76:79], a[248:251], a[180:183], v[76:79]\n"
        "s_mov_b32 m0, s48\n"
        "v_cvt_pk_bf16_f32 v129, v74, v75\n"
        "v_cvt_pk_bf16_f32 v113, v114, v115\n"
        "v_fma_f32 v117, v117, s24, v239\n"
        "v_mfma_f32_16x16x32_bf16 v[76:79], a[216:219], a[184:187], v[76:79]\n"
        "buffer_load_dwordx4 v249, s[12:15], s36 offen offset:0 lds\n"
        "ds_read_b64_tr_b16 v[104:105], v233 offset:49216\n"
        "v_mfma_f32_16x16x32_bf16 v[76:79], a[220:223], a[188:191], v[76:79]\n"
        "v_exp_f32 v117, v117\n"
        "v_fma_f32 v118, v118, s24, v239\n"
        "v_mfma_f32_16x16x32_bf16 v[162:165], a[236:239], a[192:195], 0\n"
        "ds_read_b128 a[236:239], v150 offset:1664\n"
        "ds_read_b64_tr_b16 v[106:107], v233 offset:50752\n"
        "v_cvt_pk_bf16_f32 v114, v116, v117\n"
        "v_mfma_f32_16x16x32_bf16 v[162:165], a[240:243], a[196:199], v[162:165]\n"
        "ds_read_b128 a[240:243], v151 offset:1664\n"
        "ds_read_b128 a[224:227], v151 offset:12288\n"
        "v_xor_b32 v253, 32, v249\n"
        "v_mfma_f32_16x16x32_bf16 v[162:165], a[244:247], a[200:203], v[162:165]\n"
        "ds_read_b128 a[244:247], v150 offset:1792\n"
        "v_exp_f32 v118, v118\n"
        "v_mfma_f32_16x16x32_bf16 v[162:165], a[248:251], a[204:207], v[162:165]\n"
        "ds_read_b128 a[248:251], v151 offset:1792\n"
        "buffer_load_dwordx4 v253, s[12:15], s36 offen offset:1024 lds\n"
        "s_mov_b32 s30, s29\n"
        "v_mfma_f32_16x16x32_bf16 v[162:165], a[216:219], a[208:211], v[162:165]\n"
        "s_waitcnt lgkmcnt(4)\n"
        "s_barrier\n"
        "ds_write_b128 v234, v[212:215] offset:36864\n"
        "v_mfma_f32_16x16x32_bf16 v[162:165], a[220:223], a[212:215], v[162:165]\n"
        "s_add_u32 s29, s29, s45\n"
        "ds_read_b128 a[220:223], v150 offset:12288\n"
        "v_cvt_pk_bf16_f32 v136, v76, v77\n"
        "v_mfma_f32_16x16x32_bf16 v[72:75], a[228:231], a[144:147], 0\n"
        "v_cvt_pk_bf16_f32 v137, v78, v79\n"
        "v_fma_f32 v119, v119, s24, v239\n"
        "v_mfma_f32_16x16x32_bf16 v[72:75], a[232:235], a[148:151], v[72:75]\n"
        "s_add_u32 m0, s41, 0\n"
        "v_exp_f32 v119, v119\n"
        "v_mfma_f32_16x16x32_bf16 v[72:75], a[236:239], a[152:155], v[72:75]\n"
        "s_waitcnt lgkmcnt(5)\n"
        "buffer_load_dwordx4 v242, s[8:11], s47 offen lds\n"
        "v_mfma_f32_16x16x32_bf16 v[72:75], a[240:243], a[156:159], v[72:75]\n"
        "s_waitcnt lgkmcnt(3)\n"
        "ds_write_b128 v234, v[216:219] offset:43008\n"
        "v_mfma_f32_16x16x32_bf16 v[72:75], a[244:247], a[160:163], v[72:75]\n"
        "s_waitcnt lgkmcnt(3)\n"
        "v_cvt_pk_bf16_f32 v144, v162, v163\n"
        "v_cvt_pk_bf16_f32 v145, v164, v165\n"
        "v_mfma_f32_16x16x32_bf16 v[72:75], a[248:251], a[164:167], v[72:75]\n"
        "v_cvt_pk_bf16_f32 v115, v118, v119\n"
        "v_fma_f32 v120, v120, s24, v239\n"
        "v_mfma_f32_16x16x32_bf16 v[76:79], a[228:231], a[168:171], 0\n"
        "ds_write_b128 v234, v[220:223] offset:49152\n"
        "v_mfma_f32_16x16x32_bf16 v[76:79], a[232:235], a[172:175], v[76:79]\n"
        "v_exp_f32 v120, v120\n"
        "v_mfma_f32_16x16x32_bf16 v[76:79], a[236:239], a[176:179], v[76:79]\n"
        "v_fma_f32 v121, v121, s24, v239\n"
        "v_cvt_pk_bf16_f32 v130, v72, v73\n"
        "v_mfma_f32_16x16x32_bf16 v[76:79], a[240:243], a[180:183], v[76:79]\n"
        "ds_write_b128 v234, v[224:227] offset:37120\n"
        "v_mfma_f32_16x16x32_bf16 v[76:79], a[244:247], a[184:187], v[76:79]\n"
        "v_cvt_pk_bf16_f32 v131, v74, v75\n"
        "v_fma_f32 v122, v122, s24, v239\n"
        "v_mfma_f32_16x16x32_bf16 v[76:79], a[248:251], a[188:191], v[76:79]\n"
        "v_exp_f32 v121, v121\n"
        "v_mfma_f32_16x16x32_bf16 v[162:165], a[228:231], a[192:195], 0\n"
        "ds_read_b128 a[228:231], v150 offset:12416\n"
        "v_cvt_pk_bf16_f32 v116, v120, v121\n"
        "v_mfma_f32_16x16x32_bf16 v[162:165], a[232:235], a[196:199], v[162:165]\n"
        "ds_read_b128 a[232:235], v151 offset:12416\n"
        "ds_read_b128 a[216:219], v151 offset:13824\n"
        "v_mfma_f32_16x16x32_bf16 v[162:165], a[236:239], a[200:203], v[162:165]\n"
        "ds_write_b128 v234, v[182:185] offset:43264\n"
        "v_mfma_f32_16x16x32_bf16 v[162:165], a[240:243], a[204:207], v[162:165]\n"
        "ds_read_b128 a[236:239], v150 offset:12544\n"
        "ds_read_b128 a[240:243], v151 offset:12544\n"
        "v_mfma_f32_16x16x32_bf16 v[162:165], a[244:247], a[208:211], v[162:165]\n"
        "v_cvt_pk_bf16_f32 v138, v76, v77\n"
        "v_cvt_pk_bf16_f32 v139, v78, v79\n"
        "v_mfma_f32_16x16x32_bf16 v[162:165], a[248:251], a[212:215], v[162:165]\n"
        "s_waitcnt lgkmcnt(9)\n"
        "ds_write_b128 v234, v[202:205] offset:49408\n"
        "v_mfma_f32_16x16x32_bf16 v[72:75], a[220:223], a[144:147], 0\n"
        "ds_read_b128 a[248:251], v150 offset:13824\n"
        "v_fma_f32 v123, v123, s24, v239\n"
        "v_mfma_f32_16x16x32_bf16 v[72:75], a[224:227], a[148:151], v[72:75]\n"
        "s_waitcnt lgkmcnt(7)\n"
        "v_exp_f32 v122, v122\n"
        "v_mfma_f32_16x16x32_bf16 v[72:75], a[228:231], a[152:155], v[72:75]\n"
        "s_waitcnt lgkmcnt(6)\n"
        "ds_write_b128 v234, v[88:91] offset:37376\n"
        "v_mfma_f32_16x16x32_bf16 v[72:75], a[232:235], a[156:159], v[72:75]\n"
        "s_waitcnt lgkmcnt(4)\n"
        "v_exp_f32 v123, v123\n"
        "v_mfma_f32_16x16x32_bf16 v[72:75], a[236:239], a[160:163], v[72:75]\n"
        "s_waitcnt lgkmcnt(3)\n"
        "v_cvt_pk_bf16_f32 v146, v162, v163\n"
        "v_cvt_pk_bf16_f32 v147, v164, v165\n"
        "v_mfma_f32_16x16x32_bf16 v[72:75], a[240:243], a[164:167], v[72:75]\n"
        "ds_write_b128 v234, v[92:95] offset:43520\n"
        "v_mfma_f32_16x16x32_bf16 v[76:79], a[220:223], a[168:171], 0\n"
        "v_cvt_pk_bf16_f32 v117, v122, v123\n"
        "v_fma_f32 v124, v124, s24, v239\n"
        "v_mfma_f32_16x16x32_bf16 v[76:79], a[224:227], a[172:175], v[76:79]\n"
        "v_fma_f32 v125, v125, s24, v239\n"
        "v_fma_f32 v126, v126, s24, v239\n"
        "v_mfma_f32_16x16x32_bf16 v[76:79], a[228:231], a[176:179], v[76:79]\n"
        "ds_write_b128 v234, v[104:107] offset:49664\n"
        "s_waitcnt vmcnt(3)\n"
        "s_barrier\n"
        "v_mfma_f32_16x16x32_bf16 v[76:79], a[232:235], a[180:183], v[76:79]\n"
        "ds_read_b64_tr_b16 v[108:109], v232 offset:0\n"
        "v_fma_f32 v127, v127, s24, v239\n"
        "v_mfma_f32_16x16x32_bf16 v[76:79], a[236:239], a[184:187], v[76:79]\n"
        "ds_read_b64_tr_b16 v[110:111], v232 offset:6144\n"
        "v_cvt_pk_bf16_f32 v132, v72, v73\n"
        "v_mfma_f32_16x16x32_bf16 v[76:79], a[240:243], a[188:191], v[76:79]\n"
        "v_exp_f32 v124, v124\n"
        "v_mfma_f32_16x16x32_bf16 v[162:165], a[220:223], a[192:195], 0\n"
        "ds_read_b128 a[220:223], v150 offset:13952\n"
        "ds_read_b64_tr_b16 v[120:121], v232 offset:18432\n"
        "v_mfma_f32_16x16x32_bf16 v[162:165], a[224:227], a[196:199], v[162:165]\n"
        "ds_read_b128 a[224:227], v151 offset:13952\n"
        "v_cvt_pk_bf16_f32 v133, v74, v75\n"
        "v_mfma_f32_16x16x32_bf16 v[162:165], a[228:231], a[200:203], v[162:165]\n"
        "ds_read_b128 a[228:231], v150 offset:14080\n"
        "ds_read_b64_tr_b16 v[122:123], v232 offset:24576\n"
        "v_mfma_f32_16x16x32_bf16 v[162:165], a[232:235], a[204:207], v[162:165]\n"
        "ds_read_b128 a[232:235], v151 offset:14080\n"
        "v_cvt_pk_bf16_f32 v140, v76, v77\n"
        "v_mfma_f32_16x16x32_bf16 v[162:165], a[236:239], a[208:211], v[162:165]\n"
        "ds_read_b128 a[236:239], v240 offset:13824\n"
        "ds_read_b128 a[244:247], v240 offset:13952\n"
        "v_mfma_f32_16x16x32_bf16 v[162:165], a[240:243], a[212:215], v[162:165]\n"
        "s_waitcnt lgkmcnt(13)\n"
        "ds_read_b128 a[240:243], v241 offset:13824\n"
        "v_cvt_pk_bf16_f32 v141, v78, v79\n"
        "v_mfma_f32_16x16x32_bf16 v[72:75], a[248:251], a[144:147], 0\n"
        "v_exp_f32 v125, v125\n"
        "v_mfma_f32_16x16x32_bf16 v[72:75], a[216:219], a[148:151], v[72:75]\n"
        "s_waitcnt lgkmcnt(8)\n"
        "v_cvt_pk_bf16_f32 v118, v124, v125\n"
        "v_mfma_f32_16x16x32_bf16 v[72:75], a[220:223], a[152:155], v[72:75]\n"
        "s_waitcnt lgkmcnt(6)\n"
        "ds_read_b64_tr_b16 v[124:125], v233 offset:12288\n"
        "ds_read_b64_tr_b16 v[222:223], v232 offset:32\n"
        "v_mfma_f32_16x16x32_bf16 v[72:75], a[224:227], a[156:159], v[72:75]\n"
        "s_waitcnt lgkmcnt(7)\n"
        "v_exp_f32 v126, v126\n"
        "v_mfma_f32_16x16x32_bf16 v[72:75], a[228:231], a[160:163], v[72:75]\n"
        "s_waitcnt lgkmcnt(5)\n"
        "ds_read_b64_tr_b16 v[224:225], v232 offset:6176\n"
        "v_cvt_pk_bf16_f32 v148, v162, v163\n"
        "v_mfma_f32_16x16x32_bf16 v[72:75], a[232:235], a[164:167], v[72:75]\n"
        "v_exp_f32 v127, v127\n"
        "v_mfma_f32_16x16x32_bf16 v[76:79], a[248:251], a[168:171], 0\n"
        "ds_read_b64_tr_b16 v[182:183], v232 offset:18464\n"
        "v_cvt_pk_bf16_f32 v119, v126, v127\n"
        "v_mfma_f32_16x16x32_bf16 v[76:79], a[216:219], a[172:175], v[76:79]\n"
        "ds_read_b64_tr_b16 v[126:127], v233 offset:13824\n"
        "v_cvt_pk_bf16_f32 v149, v164, v165\n"
        "v_mfma_f32_16x16x32_bf16 v[76:79], a[220:223], a[176:179], v[76:79]\n"
        "ds_read_b64_tr_b16 v[184:185], v232 offset:24608\n"
        "v_cvt_pk_bf16_f32 v134, v72, v73\n"
        "v_mfma_f32_16x16x32_bf16 v[76:79], a[224:227], a[180:183], v[76:79]\n"
        "v_cvt_pk_bf16_f32 v135, v74, v75\n"
        "v_mfma_f32_16x16x32_bf16 v[76:79], a[228:231], a[184:187], v[76:79]\n"
        "ds_read_b64_tr_b16 v[202:203], v233 offset:12320\n"
        "v_mfma_f32_16x16x32_bf16 v[76:79], a[232:235], a[188:191], v[76:79]\n"
        "v_mfma_f32_16x16x32_bf16 v[162:165], a[248:251], a[192:195], 0\n"
        "ds_read_b128 a[248:251], v241 offset:13952\n"
        "ds_read_b64_tr_b16 v[204:205], v233 offset:13856\n"
        "v_mfma_f32_16x16x32_bf16 v[162:165], a[216:219], a[196:199], v[162:165]\n"
        "ds_read_b128 a[216:219], v240 offset:14080\n"
        "v_mfma_f32_16x16x32_bf16 v[162:165], a[220:223], a[200:203], v[162:165]\n"
        "ds_read_b128 a[220:223], v241 offset:14080\n"
        "ds_read_b64_tr_b16 v[88:89], v232 offset:64\n"
        "v_mfma_f32_16x16x32_bf16 v[162:165], a[224:227], a[204:207], v[162:165]\n"
        "v_cvt_pk_bf16_f32 v142, v76, v77\n"
        "v_cvt_pk_bf16_f32 v143, v78, v79\n"
        "v_mfma_f32_16x16x32_bf16 v[162:165], a[228:231], a[208:211], v[162:165]\n"
        "ds_read_b64_tr_b16 v[90:91], v232 offset:6208\n"
        "v_mfma_f32_16x16x32_bf16 v[162:165], a[232:235], a[212:215], v[162:165]\n"
        "s_waitcnt lgkmcnt(15)\n"
        "v_mfma_f32_16x16x32_bf16 v[72:75], a[72:75], a[236:239], 0\n"
        "s_waitcnt lgkmcnt(13)\n"
        "ds_read_b64_tr_b16 v[92:93], v232 offset:18496\n"
        "v_mfma_f32_16x16x32_bf16 v[72:75], a[76:79], a[240:243], v[72:75]\n"
        "v_mfma_f32_16x16x32_bf16 v[72:75], a[80:83], a[244:247], v[72:75]\n"
        "s_waitcnt lgkmcnt(6)\n"
        "ds_read_b64_tr_b16 v[94:95], v232 offset:24640\n"
        "v_mfma_f32_16x16x32_bf16 v[72:75], a[84:87], a[248:251], v[72:75]\n"
        "s_waitcnt lgkmcnt(5)\n"
        "v_mfma_f32_16x16x32_bf16 v[72:75], a[88:91], a[216:219], v[72:75]\n"
        "s_waitcnt lgkmcnt(4)\n"
        "ds_read_b64_tr_b16 v[104:105], v233 offset:12352\n"
        "v_cvt_pk_bf16_f32 v150, v162, v163\n"
        "v_mfma_f32_16x16x32_bf16 v[72:75], a[92:95], a[220:223], v[72:75]\n"
        "v_cvt_pk_bf16_f32 v151, v164, v165\n"
        "v_mfma_f32_16x16x32_bf16 v[76:79], a[96:99], a[236:239], 0\n"
        "s_add_u32 m0, s41, 1024\n"
        "ds_read_b64_tr_b16 v[106:107], v233 offset:13888\n"
        "v_mfma_f32_16x16x32_bf16 v[76:79], a[100:103], a[240:243], v[76:79]\n"
        "v_mfma_f32_16x16x32_bf16 v[76:79], a[104:107], a[244:247], v[76:79]\n"
        "buffer_load_dwordx4 v243, s[8:11], s47 offen lds\n"
        "s_waitcnt lgkmcnt(0)\n"
        "s_barrier\n"
        "v_mfma_f32_16x16x32_bf16 v[76:79], a[108:111], a[248:251], v[76:79]\n"
        "ds_write_b128 v234, v[108:111] offset:0\n"
        "v_mfma_f32_16x16x32_bf16 v[76:79], a[112:115], a[216:219], v[76:79]\n"
        "v_cvt_pk_bf16_f32 v212, v72, v73\n"
        "v_cvt_pk_bf16_f32 v213, v74, v75\n"
        "v_mfma_f32_16x16x32_bf16 v[76:79], a[116:119], a[220:223], v[76:79]\n"
        "v_mfma_f32_16x16x32_bf16 v[162:165], a[120:123], a[236:239], 0\n"
        "ds_write_b128 v234, v[120:123] offset:6144\n"
        "v_mfma_f32_16x16x32_bf16 v[162:165], a[124:127], a[240:243], v[162:165]\n"
        "v_mfma_f32_16x16x32_bf16 v[162:165], a[128:131], a[244:247], v[162:165]\n"
        "v_mfma_f32_16x16x32_bf16 v[162:165], a[132:135], a[248:251], v[162:165]\n"
        "ds_write_b128 v234, v[124:127] offset:12288\n"
        "v_mfma_f32_16x16x32_bf16 v[162:165], a[136:139], a[216:219], v[162:165]\n"
        "v_mfma_f32_16x16x32_bf16 v[162:165], a[140:143], a[220:223], v[162:165]\n"
        "v_cvt_pk_bf16_f32 v214, v76, v77\n"
        "v_cvt_pk_bf16_f32 v215, v78, v79\n"
        "v_mfma_f32_16x16x32_bf16 v[72:75], a[0:3], a[236:239], 0\n"
        "ds_write_b128 v234, v[222:225] offset:256\n"
        "v_mfma_f32_16x16x32_bf16 v[72:75], a[4:7], a[240:243], v[72:75]\n"
        "v_mfma_f32_16x16x32_bf16 v[72:75], a[8:11], a[244:247], v[72:75]\n"
        "s_add_u32 m0, s41, 2048\n"
        "v_mfma_f32_16x16x32_bf16 v[72:75], a[12:15], a[248:251], v[72:75]\n"
        "ds_write_b128 v234, v[182:185] offset:6400\n"
        "v_mfma_f32_16x16x32_bf16 v[72:75], a[16:19], a[216:219], v[72:75]\n"
        "buffer_load_dwordx4 v244, s[8:11], s47 offen lds\n"
        "v_mfma_f32_16x16x32_bf16 v[72:75], a[20:23], a[220:223], v[72:75]\n"
        "v_cvt_pk_bf16_f32 v230, v162, v163\n"
        "v_cvt_pk_bf16_f32 v231, v164, v165\n"
        "v_mfma_f32_16x16x32_bf16 v[76:79], a[24:27], a[236:239], 0\n"
        "ds_write_b128 v234, v[202:205] offset:12544\n"
        "v_mfma_f32_16x16x32_bf16 v[76:79], a[28:31], a[240:243], v[76:79]\n"
        "v_mfma_f32_16x16x32_bf16 v[76:79], a[32:35], a[244:247], v[76:79]\n"
        "v_mfma_f32_16x16x32_bf16 v[76:79], a[36:39], a[248:251], v[76:79]\n"
        "ds_write_b128 v234, v[88:91] offset:512\n"
        "v_mfma_f32_16x16x32_bf16 v[76:79], a[40:43], a[216:219], v[76:79]\n"
        "v_cvt_pk_bf16_f32 v216, v72, v73\n"
        "v_cvt_pk_bf16_f32 v217, v74, v75\n"
        "v_mfma_f32_16x16x32_bf16 v[76:79], a[44:47], a[220:223], v[76:79]\n"
        "v_mfma_f32_16x16x32_bf16 v[162:165], a[48:51], a[236:239], 0\n"
        "ds_write_b128 v234, v[92:95] offset:6656\n"
        "v_mfma_f32_16x16x32_bf16 v[162:165], a[52:55], a[240:243], v[162:165]\n"
        "v_mfma_f32_16x16x32_bf16 v[162:165], a[56:59], a[244:247], v[162:165]\n"
        "v_mfma_f32_16x16x32_bf16 v[162:165], a[60:63], a[248:251], v[162:165]\n"
        "ds_write_b128 v234, v[104:107] offset:12800\n"
        "v_mfma_f32_16x16x32_bf16 v[162:165], a[64:67], a[216:219], v[162:165]\n"
        "s_add_u32 m0, s41, 12288\n"
        "v_cvt_pk_bf16_f32 v218, v76, v77\n"
        "v_cvt_pk_bf16_f32 v219, v78, v79\n"
        "v_mfma_f32_16x16x32_bf16 v[162:165], a[68:71], a[220:223], v[162:165]\n"
        "v_mfma_f32_16x16x32_bf16 v[88:91], a[252:255], v[64:67], 0\n"
        "buffer_load_dwordx4 v242, s[8:11], s35 offen lds\n"
        "v_mfma_f32_16x16x32_bf16 v[88:91], a[252:255], v[68:71], v[88:91]\n"
        "v_mfma_f32_16x16x32_bf16 v[92:95], v[132:135], v[68:71], 0\n"
        "v_mfma_f32_16x16x32_bf16 v[92:95], v[128:131], v[64:67], v[92:95]\n"
        "v_mfma_f32_16x16x32_bf16 v[182:185], v[136:139], v[64:67], 0\n"
        "v_mfma_f32_16x16x32_bf16 v[182:185], v[140:143], v[68:71], v[182:185]\n"
        "v_mfma_f32_16x16x32_bf16 v[202:205], v[148:151], v[68:71], 0\n"
        "s_cmp_eq_u32 s37, 100663296\n"
        "v_cvt_pk_bf16_f32 v220, v162, v163\n"
        "v_cvt_pk_bf16_f32 v221, v164, v165\n"
        "v_mfma_f32_16x16x32_bf16 v[202:205], v[144:147], v[64:67], v[202:205]\n"
        "s_cselect_b32 s46, 0, s37\n"
        "v_cvt_pk_bf16_f32 v226, v162, v163\n"
        "v_cvt_pk_bf16_f32 v227, v164, v165\n"
        "v_mfma_f32_16x16x32_bf16 v[72:75], a[252:255], v[80:83], 0\n"
        "s_add_u32 s38, s46, s43\n"
        "v_rcp_f32 v88, v88\n"
        "v_mfma_f32_16x16x32_bf16 v[72:75], a[252:255], v[84:87], v[72:75]\n"
        "s_add_u32 s39, s46, s44\n"
        "v_mul_f32 v92, v88, v92\n"
        "v_mul_f32 v93, v88, v93\n"
        "v_mfma_f32_16x16x32_bf16 v[76:79], v[132:135], v[84:87], 0\n"
        "s_add_u32 s37, s37, 6291456\n"
        "v_mul_f32 v94, v88, v94\n"
        "v_mul_f32 v95, v88, v95\n"
        "v_mfma_f32_16x16x32_bf16 v[76:79], v[128:131], v[80:83], v[76:79]\n"
        "v_rcp_f32 v72, v72\n"
        "v_mfma_f32_16x16x32_bf16 v[104:107], v[136:139], v[80:83], 0\n"
        "s_add_u32 m0, s41, 13312\n"
        "v_cvt_pk_bf16_f32 v92, v92, v93\n"
        "v_cvt_pk_bf16_f32 v93, v94, v95\n"
        "v_mfma_f32_16x16x32_bf16 v[104:107], v[140:143], v[84:87], v[104:107]\n"
        "v_mul_f32 v182, v88, v182\n"
        "v_mul_f32 v183, v88, v183\n"
        "v_mfma_f32_16x16x32_bf16 v[108:111], v[148:151], v[84:87], 0\n"
        "buffer_load_dwordx4 v243, s[8:11], s35 offen lds\n"
        "v_mfma_f32_16x16x32_bf16 v[108:111], v[144:147], v[80:83], v[108:111]\n"
        "v_mul_f32 v184, v88, v184\n"
        "v_mul_f32 v185, v88, v185\n"
        "v_mfma_f32_16x16x32_bf16 v[64:67], a[252:255], v[96:99], 0\n"
        "v_cvt_pk_bf16_f32 v94, v182, v183\n"
        "v_cvt_pk_bf16_f32 v95, v184, v185\n"
        "v_mfma_f32_16x16x32_bf16 v[64:67], a[252:255], v[100:103], v[64:67]\n"
        "ds_write_b128 v245, v[92:95] offset:0\n"
        "v_mfma_f32_16x16x32_bf16 v[68:71], v[132:135], v[100:103], 0\n"
        "v_mul_f32 v202, v88, v202\n"
        "v_mul_f32 v203, v88, v203\n"
        "v_mfma_f32_16x16x32_bf16 v[68:71], v[128:131], v[96:99], v[68:71]\n"
        "v_mul_f32 v204, v88, v204\n"
        "v_mul_f32 v205, v88, v205\n"
        "v_mfma_f32_16x16x32_bf16 v[120:123], v[136:139], v[96:99], 0\n"
        "v_cvt_pk_bf16_f32 v202, v202, v203\n"
        "v_cvt_pk_bf16_f32 v203, v204, v205\n"
        "v_mfma_f32_16x16x32_bf16 v[120:123], v[140:143], v[100:103], v[120:123]\n"
        "v_mul_f32 v76, v72, v76\n"
        "v_mul_f32 v77, v72, v77\n"
        "v_mfma_f32_16x16x32_bf16 v[124:127], v[148:151], v[100:103], 0\n"
        "ds_write_b64 v246, v[202:203] offset:0\n"
        "v_mfma_f32_16x16x32_bf16 v[124:127], v[144:147], v[96:99], v[124:127]\n"
        "v_rcp_f32 v64, v64\n"
        "v_mfma_f32_16x16x32_bf16 v[80:83], a[252:255], v[112:115], 0\n"
        "v_mul_f32 v78, v72, v78\n"
        "v_mul_f32 v79, v72, v79\n"
        "v_mfma_f32_16x16x32_bf16 v[80:83], a[252:255], v[116:119], v[80:83]\n"
        "v_cvt_pk_bf16_f32 v76, v76, v77\n"
        "v_cvt_pk_bf16_f32 v77, v78, v79\n"
        "v_mfma_f32_16x16x32_bf16 v[84:87], v[132:135], v[116:119], 0\n"
        "s_add_u32 m0, s41, 14336\n"
        "v_mul_f32 v104, v72, v104\n"
        "v_mul_f32 v105, v72, v105\n"
        "v_mfma_f32_16x16x32_bf16 v[84:87], v[128:131], v[112:115], v[84:87]\n"
        "v_mul_f32 v106, v72, v106\n"
        "v_mul_f32 v107, v72, v107\n"
        "v_mfma_f32_16x16x32_bf16 v[222:225], v[136:139], v[112:115], 0\n"
        "buffer_load_dwordx4 v244, s[8:11], s35 offen lds\n"
        "v_mfma_f32_16x16x32_bf16 v[222:225], v[140:143], v[116:119], v[222:225]\n"
        "ds_read_b128 a[224:227], v240 offset:0\n"
        "ds_read_b128 a[228:231], v241 offset:0\n"
        "v_mfma_f32_16x16x32_bf16 v[162:165], v[148:151], v[116:119], 0\n"
        "ds_read_b128 a[232:235], v240 offset:128\n"
        "v_cvt_pk_bf16_f32 v78, v104, v105\n"
        "v_mfma_f32_16x16x32_bf16 v[162:165], v[144:147], v[112:115], v[162:165]\n"
        "v_cvt_pk_bf16_f32 v79, v106, v107\n"
        "v_mul_f32 v108, v72, v108\n"
        "v_mfma_f32_16x16x32_bf16 v[96:99], a[236:239], a[144:147], 0\n"
        "ds_write_b128 v245, v[76:79] offset:256\n"
        "v_mfma_f32_16x16x32_bf16 v[96:99], a[240:243], a[148:151], v[96:99]\n"
        "v_mul_f32 v109, v72, v109\n"
        "v_mul_f32 v110, v72, v110\n"
        "v_mfma_f32_16x16x32_bf16 v[96:99], a[244:247], a[152:155], v[96:99]\n"
        "v_mul_f32 v111, v72, v111\n"
        "v_cvt_pk_bf16_f32 v108, v108, v109\n"
        "v_mfma_f32_16x16x32_bf16 v[96:99], a[248:251], a[156:159], v[96:99]\n"
        "v_cvt_pk_bf16_f32 v109, v110, v111\n"
        "v_mul_f32 v68, v64, v68\n"
        "v_mfma_f32_16x16x32_bf16 v[96:99], a[216:219], a[160:163], v[96:99]\n"
        "v_rcp_f32 v80, v80\n"
        "v_mfma_f32_16x16x32_bf16 v[96:99], a[220:223], a[164:167], v[96:99]\n"
        "ds_write_b64 v246, v[108:109] offset:256\n"
        "v_mfma_f32_16x16x32_bf16 v[100:103], a[236:239], a[168:171], 0\n"
        "v_mul_f32 v69, v64, v69\n"
        "v_mul_f32 v70, v64, v70\n"
        "v_mfma_f32_16x16x32_bf16 v[100:103], a[240:243], a[172:175], v[100:103]\n"
        "v_mul_f32 v71, v64, v71\n"
        "v_cvt_pk_bf16_f32 v68, v68, v69\n"
        "v_mfma_f32_16x16x32_bf16 v[100:103], a[244:247], a[176:179], v[100:103]\n"
        "v_cvt_pk_bf16_f32 v134, v96, v97\n"
        "v_cvt_pk_bf16_f32 v135, v98, v99\n"
        "v_mfma_f32_16x16x32_bf16 v[100:103], a[248:251], a[180:183], v[100:103]\n"
        "v_cvt_pk_bf16_f32 v69, v70, v71\n"
        "v_mul_f32 v120, v64, v120\n"
        "v_mfma_f32_16x16x32_bf16 v[100:103], a[216:219], a[184:187], v[100:103]\n"
        "v_mul_f32 v121, v64, v121\n"
        "v_mul_f32 v122, v64, v122\n"
        "v_mfma_f32_16x16x32_bf16 v[100:103], a[220:223], a[188:191], v[100:103]\n"
        "v_mul_f32 v123, v64, v123\n"
        "v_cvt_pk_bf16_f32 v70, v120, v121\n"
        "v_mfma_f32_16x16x32_bf16 v[112:115], a[236:239], a[192:195], 0\n"
        "ds_read_b128 a[236:239], v241 offset:128\n"
        "v_cvt_pk_bf16_f32 v71, v122, v123\n"
        "v_mfma_f32_16x16x32_bf16 v[112:115], a[240:243], a[196:199], v[112:115]\n"
        "ds_read_b128 a[240:243], v240 offset:256\n"
        "v_mul_f32 v124, v64, v124\n"
        "v_mfma_f32_16x16x32_bf16 v[112:115], a[244:247], a[200:203], v[112:115]\n"
        "ds_read_b128 a[244:247], v241 offset:256\n"
        "v_cvt_pk_bf16_f32 v142, v100, v101\n"
        "v_mfma_f32_16x16x32_bf16 v[112:115], a[248:251], a[204:207], v[112:115]\n"
        "ds_read_b128 a[248:251], v240 offset:1536\n"
        "v_cvt_pk_bf16_f32 v143, v102, v103\n"
        "v_mfma_f32_16x16x32_bf16 v[112:115], a[216:219], a[208:211], v[112:115]\n"
        "ds_read_b128 a[216:219], v241 offset:1536\n"
        "v_mul_f32 v125, v64, v125\n"
        "v_mfma_f32_16x16x32_bf16 v[112:115], a[220:223], a[212:215], v[112:115]\n"
        "s_waitcnt lgkmcnt(9)\n"
        "ds_read_b128 a[220:223], v240 offset:1664\n"
        "v_mul_f32 v126, v64, v126\n"
        "v_mfma_f32_16x16x32_bf16 v[96:99], a[224:227], a[144:147], 0\n"
        "s_waitcnt lgkmcnt(9)\n"
        "ds_write_b128 v245, v[68:71] offset:512\n"
        "v_mfma_f32_16x16x32_bf16 v[96:99], a[228:231], a[148:151], v[96:99]\n"
        "s_waitcnt lgkmcnt(9)\n"
        "v_mul_f32 v127, v64, v127\n"
        "v_cvt_pk_bf16_f32 v124, v124, v125\n"
        "v_mfma_f32_16x16x32_bf16 v[96:99], a[232:235], a[152:155], v[96:99]\n"
        "s_waitcnt lgkmcnt(6)\n"
        "v_cvt_pk_bf16_f32 v150, v112, v113\n"
        "v_cvt_pk_bf16_f32 v151, v114, v115\n"
        "v_mfma_f32_16x16x32_bf16 v[96:99], a[236:239], a[156:159], v[96:99]\n"
        "s_waitcnt lgkmcnt(5)\n"
        "v_cvt_pk_bf16_f32 v125, v126, v127\n"
        "v_mul_f32 v84, v80, v84\n"
        "v_mfma_f32_16x16x32_bf16 v[96:99], a[240:243], a[160:163], v[96:99]\n"
        "s_waitcnt lgkmcnt(4)\n"
        "v_mul_f32 v85, v80, v85\n"
        "v_mul_f32 v86, v80, v86\n"
        "v_mfma_f32_16x16x32_bf16 v[96:99], a[244:247], a[164:167], v[96:99]\n"
        "ds_write_b64 v246, v[124:125] offset:512\n"
        "v_mfma_f32_16x16x32_bf16 v[100:103], a[224:227], a[168:171], 0\n"
        "v_mul_f32 v87, v80, v87\n"
        "v_cvt_pk_bf16_f32 v84, v84, v85\n"
        "v_mfma_f32_16x16x32_bf16 v[100:103], a[228:231], a[172:175], v[100:103]\n"
        "v_cvt_pk_bf16_f32 v85, v86, v87\n"
        "v_mul_f32 v222, v80, v222\n"
        "v_mfma_f32_16x16x32_bf16 v[100:103], a[232:235], a[176:179], v[100:103]\n"
        "v_cvt_pk_bf16_f32 v128, v96, v97\n"
        "v_cvt_pk_bf16_f32 v129, v98, v99\n"
        "v_mfma_f32_16x16x32_bf16 v[100:103], a[236:239], a[180:183], v[100:103]\n"
        "v_mul_f32 v223, v80, v223\n"
        "v_mul_f32 v224, v80, v224\n"
        "v_mfma_f32_16x16x32_bf16 v[100:103], a[240:243], a[184:187], v[100:103]\n"
        "v_mul_f32 v225, v80, v225\n"
        "v_cvt_pk_bf16_f32 v86, v222, v223\n"
        "v_mfma_f32_16x16x32_bf16 v[100:103], a[244:247], a[188:191], v[100:103]\n"
        "v_cvt_pk_bf16_f32 v87, v224, v225\n"
        "v_mul_f32 v162, v80, v162\n"
        "v_mfma_f32_16x16x32_bf16 v[112:115], a[224:227], a[192:195], 0\n"
        "ds_read_b128 a[224:227], v241 offset:1664\n"
        "v_mul_f32 v163, v80, v163\n"
        "v_mfma_f32_16x16x32_bf16 v[112:115], a[228:231], a[196:199], v[112:115]\n"
        "ds_read_b128 a[228:231], v240 offset:1792\n"
        "v_mul_f32 v164, v80, v164\n"
        "v_mfma_f32_16x16x32_bf16 v[112:115], a[232:235], a[200:203], v[112:115]\n"
        "ds_read_b128 a[232:235], v241 offset:1792\n"
        "v_cvt_pk_bf16_f32 v136, v100, v101\n"
        "v_mfma_f32_16x16x32_bf16 v[112:115], a[236:239], a[204:207], v[112:115]\n"
        "ds_read_b128 a[236:239], v240 offset:12288\n"
        "v_cvt_pk_bf16_f32 v137, v102, v103\n"
        "v_mfma_f32_16x16x32_bf16 v[112:115], a[240:243], a[208:211], v[112:115]\n"
        "ds_read_b128 a[240:243], v241 offset:12288\n"
        "v_mul_f32 v165, v80, v165\n"
        "v_mfma_f32_16x16x32_bf16 v[112:115], a[244:247], a[212:215], v[112:115]\n"
        "s_waitcnt lgkmcnt(9)\n"
        "ds_read_b128 a[244:247], v240 offset:12416\n"
        "v_cvt_pk_bf16_f32 v162, v162, v163\n"
        "v_mfma_f32_16x16x32_bf16 v[96:99], a[248:251], a[144:147], 0\n"
        "s_waitcnt lgkmcnt(9)\n"
        "ds_write_b128 v245, v[84:87] offset:768\n"
        "v_mfma_f32_16x16x32_bf16 v[96:99], a[216:219], a[148:151], v[96:99]\n"
        "s_waitcnt lgkmcnt(9)\n"
        "v_cvt_pk_bf16_f32 v163, v164, v165\n"
        "v_mfma_f32_16x16x32_bf16 v[96:99], a[220:223], a[152:155], v[96:99]\n"
        "s_waitcnt lgkmcnt(6)\n"
        "v_mfma_f32_16x16x32_bf16 v[96:99], a[224:227], a[156:159], v[96:99]\n"
        "s_waitcnt lgkmcnt(5)\n"
        "v_cvt_pk_bf16_f32 v144, v112, v113\n"
        "v_cvt_pk_bf16_f32 v145, v114, v115\n"
        "v_mfma_f32_16x16x32_bf16 v[96:99], a[228:231], a[160:163], v[96:99]\n"
        "s_waitcnt lgkmcnt(4)\n"
        "v_mfma_f32_16x16x32_bf16 v[96:99], a[232:235], a[164:167], v[96:99]\n"
        "ds_write_b64 v246, v[162:163] offset:768\n"
        "v_mfma_f32_16x16x32_bf16 v[100:103], a[248:251], a[168:171], 0\n"
        "v_mfma_f32_16x16x32_bf16 v[100:103], a[216:219], a[172:175], v[100:103]\n"
        "v_mfma_f32_16x16x32_bf16 v[100:103], a[220:223], a[176:179], v[100:103]\n"
        "v_mfma_f32_16x16x32_bf16 v[100:103], a[224:227], a[180:183], v[100:103]\n"
        "v_mfma_f32_16x16x32_bf16 v[100:103], a[228:231], a[184:187], v[100:103]\n"
        "v_mfma_f32_16x16x32_bf16 v[100:103], a[232:235], a[188:191], v[100:103]\n"
        "v_mfma_f32_16x16x32_bf16 v[112:115], a[248:251], a[192:195], 0\n"
        "ds_read_b128 a[248:251], v241 offset:12416\n"
        "ds_read_b128 v[162:165], v251 offset:0\n"
        "v_mfma_f32_16x16x32_bf16 v[112:115], a[216:219], a[196:199], v[112:115]\n"
        "ds_read_b128 a[216:219], v240 offset:12544\n"
        "v_cvt_pk_bf16_f32 v130, v96, v97\n"
        "v_mfma_f32_16x16x32_bf16 v[112:115], a[220:223], a[200:203], v[112:115]\n"
        "ds_read_b128 a[220:223], v241 offset:12544\n"
        "v_cvt_pk_bf16_f32 v131, v98, v99\n"
        "v_mfma_f32_16x16x32_bf16 v[112:115], a[224:227], a[204:207], v[112:115]\n"
        "v_cvt_pk_bf16_f32 v138, v100, v101\n"
        "v_cvt_pk_bf16_f32 v139, v102, v103\n"
        "v_mfma_f32_16x16x32_bf16 v[112:115], a[228:231], a[208:211], v[112:115]\n"
        "ds_read_b128 v[182:185], v251 offset:512\n"
        "v_mfma_f32_16x16x32_bf16 v[112:115], a[232:235], a[212:215], v[112:115]\n"
        "s_waitcnt lgkmcnt(9)\n"
        "v_mfma_f32_16x16x32_bf16 v[96:99], a[236:239], a[144:147], 0\n"
        "s_waitcnt lgkmcnt(8)\n"
        "v_mfma_f32_16x16x32_bf16 v[96:99], a[240:243], a[148:151], v[96:99]\n"
        "s_waitcnt lgkmcnt(7)\n"
        "v_mfma_f32_16x16x32_bf16 v[96:99], a[244:247], a[152:155], v[96:99]\n"
        "s_waitcnt lgkmcnt(4)\n"
        "ds_read_b128 v[202:205], v251 offset:4096\n"
        "v_mfma_f32_16x16x32_bf16 v[96:99], a[248:251], a[156:159], v[96:99]\n"
        "s_waitcnt lgkmcnt(3)\n"
        "v_mfma_f32_16x16x32_bf16 v[96:99], a[216:219], a[160:163], v[96:99]\n"
        "s_waitcnt lgkmcnt(2)\n"
        "v_mfma_f32_16x16x32_bf16 v[96:99], a[220:223], a[164:167], v[96:99]\n"
        "s_add_u32 s47, s47, 6291456\n"
        "v_cvt_pk_bf16_f32 v146, v112, v113\n"
        "v_cvt_pk_bf16_f32 v147, v114, v115\n"
        "v_mfma_f32_16x16x32_bf16 v[100:103], a[236:239], a[168:171], 0\n"
        "ds_read_b128 v[222:225], v251 offset:4608\n"
        "s_add_u32 s36, s36, 2097152\n"
        "v_mfma_f32_16x16x32_bf16 v[100:103], a[240:243], a[172:175], v[100:103]\n"
        "s_xor_b32 s48, s48, 8192\n"
        "v_cvt_pk_bf16_f32 v132, v96, v97\n"
        "v_cvt_pk_bf16_f32 v133, v98, v99\n"
        "v_mfma_f32_16x16x32_bf16 v[100:103], a[244:247], a[176:179], v[100:103]\n"
        "v_pk_min_u16 v162, v162, s26\n"
        "v_pk_min_u16 v163, v163, s26\n"
        "v_mfma_f32_16x16x32_bf16 v[100:103], a[248:251], a[180:183], v[100:103]\n"
        "v_pk_min_u16 v164, v164, s26\n"
        "v_pk_min_u16 v165, v165, s26\n"
        "v_mfma_f32_16x16x32_bf16 v[100:103], a[216:219], a[184:187], v[100:103]\n"
        "v_add_u32 v240, s31, v240\n"
        "v_add_u32 v241, s31, v241\n"
        "v_mfma_f32_16x16x32_bf16 v[100:103], a[220:223], a[188:191], v[100:103]\n"
        "v_mfma_f32_16x16x32_bf16 v[112:115], a[236:239], a[192:195], 0\n"
        "s_waitcnt lgkmcnt(2)\n"
        "v_pk_min_u16 v182, v182, s26\n"
        "v_pk_min_u16 v183, v183, s26\n"
        "v_mfma_f32_16x16x32_bf16 v[112:115], a[240:243], a[196:199], v[112:115]\n"
        "v_pk_min_u16 v184, v184, s26\n"
        "v_pk_min_u16 v185, v185, s26\n"
        "v_mfma_f32_16x16x32_bf16 v[112:115], a[244:247], a[200:203], v[112:115]\n"
        "v_mfma_f32_16x16x32_bf16 v[112:115], a[248:251], a[204:207], v[112:115]\n"
        "v_cvt_pk_bf16_f32 v140, v100, v101\n"
        "v_cvt_pk_bf16_f32 v141, v102, v103\n"
        "v_mfma_f32_16x16x32_bf16 v[112:115], a[216:219], a[208:211], v[112:115]\n"
        "s_waitcnt lgkmcnt(1)\n"
        "v_pk_min_u16 v202, v202, s26\n"
        "v_pk_min_u16 v203, v203, s26\n"
        "v_mfma_f32_16x16x32_bf16 v[112:115], a[220:223], a[212:215], v[112:115]\n"
        "v_pk_min_u16 v204, v204, s26\n"
        "v_pk_min_u16 v205, v205, s26\n"
        "v_mfma_f32_16x16x32_bf16 v[64:67], v[152:155], v[156:159], v[0:3]\n"
        "v_mfma_f32_16x16x32_bf16 v[64:67], v[168:171], v[160:163], v[64:67]\n"
        "v_mfma_f32_16x16x32_bf16 v[68:71], v[172:175], v[156:159], v[4:7]\n"
        "s_waitcnt lgkmcnt(0)\n"
        "v_pk_min_u16 v222, v222, s26\n"
        "v_pk_min_u16 v223, v223, s26\n"
        "v_mfma_f32_16x16x32_bf16 v[68:71], v[188:191], v[164:167], v[68:71]\n"
        "ds_read_b128 v[162:165], v252 offset:0\n"
        "v_cvt_pk_bf16_f32 v148, v112, v113\n"
        "v_mfma_f32_16x16x32_bf16 v[80:83], v[152:155], v[176:179], v[16:19]\n"
        "v_cvt_pk_bf16_f32 v149, v114, v115\n"
        "v_pk_min_u16 v224, v224, s26\n"
        "v_mfma_f32_16x16x32_bf16 v[80:83], v[168:171], v[180:183], v[80:83]\n"
        "v_pk_min_u16 v225, v225, s26\n"
        "v_max3_f32 v232, v64, v65, v66\n"
        "v_mfma_f32_16x16x32_bf16 v[84:87], v[172:175], v[176:179], v[20:23]\n"
        "v_max3_f32 v232, v232, v67, v68\n"
        "v_max3_f32 v232, v232, v69, v70\n"
        "v_mfma_f32_16x16x32_bf16 v[84:87], v[188:191], v[184:187], v[84:87]\n"
        "ds_read_b128 v[182:185], v252 offset:512\n"
        "v_mfma_f32_16x16x32_bf16 v[96:99], v[152:155], v[196:199], v[32:35]\n"
        "v_max3_f32 v233, v80, v81, v82\n"
        "v_mfma_f32_16x16x32_bf16 v[96:99], v[168:171], v[200:203], v[96:99]\n"
        "v_mfma_f32_16x16x32_bf16 v[100:103], v[172:175], v[196:199], v[36:39]\n"
        "v_mfma_f32_16x16x32_bf16 v[100:103], v[188:191], v[204:207], v[100:103]\n"
        "ds_read_b128 v[202:205], v252 offset:4096\n"
        "v_mfma_f32_16x16x32_bf16 v[112:115], v[152:155], v[216:219], v[48:51]\n"
        "s_waitcnt lgkmcnt(2)\n"
        "v_pk_min_u16 v162, v162, s26\n"
        "v_pk_min_u16 v163, v163, s26\n"
        "v_mfma_f32_16x16x32_bf16 v[112:115], v[168:171], v[220:223], v[112:115]\n"
        "v_pk_min_u16 v164, v164, s26\n"
        "v_pk_min_u16 v165, v165, s26\n"
        "v_mfma_f32_16x16x32_bf16 v[116:119], v[172:175], v[216:219], v[52:55]\n"
        "v_max3_f32 v233, v233, v83, v84\n"
        "v_max3_f32 v233, v233, v85, v86\n"
        "v_mfma_f32_16x16x32_bf16 v[116:119], v[188:191], v[224:227], v[116:119]\n"
        "ds_read_b128 v[222:225], v252 offset:4608\n"
        "v_max3_f32 v234, v96, v97, v98\n"
        "v_mfma_f32_16x16x32_bf16 v[72:75], v[192:195], v[156:159], v[8:11]\n"
        "s_waitcnt lgkmcnt(2)\n"
        "v_pk_min_u16 v182, v182, s26\n"
        "v_pk_min_u16 v183, v183, s26\n"
        "v_mfma_f32_16x16x32_bf16 v[72:75], v[208:211], v[160:163], v[72:75]\n"
        "v_pk_min_u16 v184, v184, s26\n"
        "v_pk_min_u16 v185, v185, s26\n"
        "v_mfma_f32_16x16x32_bf16 v[76:79], v[212:215], v[156:159], v[12:15]\n"
        "v_max3_f32 v234, v234, v99, v100\n"
        "v_max3_f32 v234, v234, v101, v102\n"
        "v_mfma_f32_16x16x32_bf16 v[76:79], v[228:231], v[164:167], v[76:79]\n"
        "v_max3_f32 v235, v112, v113, v114\n"
        "v_max3_f32 v235, v235, v115, v116\n"
        "v_mfma_f32_16x16x32_bf16 v[88:91], v[192:195], v[176:179], v[24:27]\n"
        "s_barrier\n"
        "ds_read_b128 v[152:155], v247 offset:0\n"
        "ds_read_b128 v[160:163], v247 offset:4096\n"
        "v_mfma_f32_16x16x32_bf16 v[88:91], v[208:211], v[180:183], v[88:91]\n"
        "ds_read_b128 v[156:159], v247 offset:256\n"
        "ds_read_b128 v[164:167], v247 offset:4352\n"
        "v_mfma_f32_16x16x32_bf16 v[92:95], v[212:215], v[176:179], v[28:31]\n"
        "ds_read_b128 a[216:219], v248 offset:0\n"
        "ds_read_b128 a[220:223], v248 offset:6144\n"
        "v_mfma_f32_16x16x32_bf16 v[92:95], v[228:231], v[184:187], v[92:95]\n"
        "s_waitcnt lgkmcnt(7)\n"
        "v_pk_min_u16 v202, v202, s26\n"
        "v_pk_min_u16 v203, v203, s26\n"
        "v_mfma_f32_16x16x32_bf16 v[104:107], v[192:195], v[196:199], v[40:43]\n"
        "ds_read_b128 a[224:227], v254 offset:16\n"
        "ds_read_b128 a[228:231], v254 offset:6160\n"
        "v_mfma_f32_16x16x32_bf16 v[104:107], v[208:211], v[200:203], v[104:107]\n"
        "v_pk_min_u16 v204, v204, s26\n"
        "v_pk_min_u16 v205, v205, s26\n"
        "v_mfma_f32_16x16x32_bf16 v[108:111], v[212:215], v[196:199], v[44:47]\n"
        "s_waitcnt lgkmcnt(8)\n"
        "v_pk_min_u16 v222, v222, s26\n"
        "v_pk_min_u16 v223, v223, s26\n"
        "v_mfma_f32_16x16x32_bf16 v[108:111], v[228:231], v[204:207], v[108:111]\n"
        "v_pk_min_u16 v224, v224, s26\n"
        "v_pk_min_u16 v225, v225, s26\n"
        "v_mfma_f32_16x16x32_bf16 v[120:123], v[192:195], v[216:219], v[56:59]\n"
        "ds_read_b128 a[232:235], v248 offset:32\n"
        "v_max3_f32 v232, v232, v71, v72\n"
        "v_mfma_f32_16x16x32_bf16 v[120:123], v[208:211], v[220:223], v[120:123]\n"
        "v_max3_f32 v232, v232, v73, v74\n"
        "v_max3_f32 v232, v232, v75, v76\n"
        "v_mfma_f32_16x16x32_bf16 v[124:127], v[212:215], v[216:219], v[60:63]\n"
        "v_max3_f32 v232, v232, v77, v78\n"
        "v_max_f32 v232, v232, v79\n"
        "v_mfma_f32_16x16x32_bf16 v[124:127], v[228:231], v[224:227], v[124:127]\n"
        "s_waitcnt lgkmcnt(4)\n"
        "v_max3_f32 v233, v233, v87, v88\n"
        "v_max3_f32 v233, v233, v89, v90\n"
        "v_mfma_f32_16x16x32_bf16 v[172:175], v[152:155], a[216:219], 0\n"
        "s_waitcnt lgkmcnt(3)\n"
        "v_max3_f32 v233, v233, v91, v92\n"
        "v_max3_f32 v233, v233, v93, v94\n"
        "v_mfma_f32_16x16x32_bf16 v[172:175], v[160:163], a[220:223], v[172:175]\n"
        "v_max_f32 v233, v233, v95\n"
        "v_max3_f32 v234, v234, v103, v104\n"
        "v_mfma_f32_16x16x32_bf16 v[180:183], v[164:167], a[220:223], 0\n"
        "ds_read_b128 a[220:223], v248 offset:6176\n"
        "ds_read_b128 a[236:239], v247 offset:8192\n"
        "v_mfma_f32_16x16x32_bf16 v[180:183], v[156:159], a[216:219], v[180:183]\n"
        "s_waitcnt lgkmcnt(4)\n"
        "ds_read_b128 a[216:219], v254 offset:48\n"
        "ds_read_b128 a[244:247], v247 offset:12288\n"
        "v_mfma_f32_16x16x32_bf16 v[184:187], v[156:159], a[224:227], 0\n"
        "s_waitcnt lgkmcnt(5)\n"
        "ds_read_b128 a[240:243], v247 offset:8448\n"
        "ds_read_b128 a[248:251], v247 offset:12544\n"
        "v_mfma_f32_16x16x32_bf16 v[184:187], v[164:167], a[228:231], v[184:187]\n"
        "v_permlane32_swap_b32 v232, v233\n"
        "v_mfma_f32_16x16x32_bf16 v[176:179], v[160:163], a[228:231], 0\n"
        "ds_read_b128 a[228:231], v254 offset:6192\n"
        "v_max_f32 v232, v232, v233\n"
        "v_mfma_f32_16x16x32_bf16 v[176:179], v[152:155], a[224:227], v[176:179]\n"
        "s_waitcnt lgkmcnt(7)\n"
        "ds_read_b128 a[224:227], v248 offset:64\n"
        "v_mov_b32 v233, v232\n"
        "v_mfma_f32_16x16x32_bf16 v[212:215], v[152:155], a[232:235], 0\n"
        "s_waitcnt lgkmcnt(7)\n"
        "v_max3_f32 v234, v234, v105, v106\n"
        "v_max3_f32 v234, v234, v107, v108\n"
        "v_mfma_f32_16x16x32_bf16 v[212:215], v[160:163], a[220:223], v[212:215]\n"
        "v_permlane16_swap_b32 v232, v233\n"
        "v_mfma_f32_16x16x32_bf16 v[192:195], v[164:167], a[220:223], 0\n"
        "ds_read_b128 a[220:223], v248 offset:6208\n"
        "v_max_f32 v232, v232, v233\n"
        "v_mfma_f32_16x16x32_bf16 v[192:195], v[156:159], a[232:235], v[192:195]\n"
        "s_waitcnt lgkmcnt(6)\n"
        "ds_read_b128 a[232:235], v254 offset:80\n"
        "v_mul_f32 v236, s25, v232\n"
        "v_mfma_f32_16x16x32_bf16 v[196:199], v[156:159], a[216:219], 0\n"
        "s_waitcnt lgkmcnt(3)\n"
        "v_mul_f32 v237, s25, v232\n"
        "v_max3_f32 v234, v234, v109, v110\n"
        "v_mfma_f32_16x16x32_bf16 v[196:199], v[164:167], a[228:231], v[196:199]\n"
        "v_permlane32_swap_b32 v236, v237\n"
        "v_mfma_f32_16x16x32_bf16 v[216:219], v[160:163], a[228:231], 0\n"
        "ds_read_b128 a[228:231], v254 offset:6224\n"
        "v_max_f32 v234, v234, v111\n"
        "v_mfma_f32_16x16x32_bf16 v[216:219], v[152:155], a[216:219], v[216:219]\n"
        "s_waitcnt lgkmcnt(3)\n"
        "ds_read_b128 a[216:219], v248 offset:36864\n"
        "v_max3_f32 v235, v235, v117, v118\n"
        "v_mfma_f32_16x16x32_bf16 v[220:223], v[152:155], a[224:227], 0\n"
        "s_waitcnt lgkmcnt(3)\n"
        "v_max3_f32 v235, v235, v119, v120\n"
        "v_max3_f32 v235, v235, v121, v122\n"
        "v_mfma_f32_16x16x32_bf16 v[220:223], v[160:163], a[220:223], v[220:223]\n"
        "v_max3_f32 v235, v235, v123, v124\n"
        "v_max3_f32 v235, v235, v125, v126\n"
        "v_mfma_f32_16x16x32_bf16 v[200:203], v[164:167], a[220:223], 0\n"
        "ds_read_b128 a[220:223], v248 offset:43008\n"
        "v_max_f32 v235, v235, v127\n"
        "v_mfma_f32_16x16x32_bf16 v[200:203], v[156:159], a[224:227], v[200:203]\n"
        "s_waitcnt lgkmcnt(3)\n"
        "ds_read_b128 a[224:227], v254 offset:36880\n"
        "v_fma_f32 v64, v64, s24, v236\n"
        "v_mfma_f32_16x16x32_bf16 v[204:207], v[156:159], a[232:235], 0\n"
        "s_waitcnt lgkmcnt(3)\n"
        "v_permlane32_swap_b32 v234, v235\n"
        "v_mfma_f32_16x16x32_bf16 v[204:207], v[164:167], a[228:231], v[204:207]\n"
        "v_max_f32 v234, v234, v235\n"
        "v_mov_b32 v235, v234\n"
        "v_mfma_f32_16x16x32_bf16 v[224:227], v[160:163], a[228:231], 0\n"
        "ds_read_b128 a[228:231], v254 offset:43024\n"
        "v_fma_f32 v65, v65, s24, v236\n"
        "v_mfma_f32_16x16x32_bf16 v[224:227], v[152:155], a[232:235], v[224:227]\n"
        "s_waitcnt lgkmcnt(3)\n"
        "ds_read_b128 a[232:235], v248 offset:36896\n"
        "v_fma_f32 v66, v66, s24, v236\n"
        "v_mfma_f32_16x16x32_bf16 v[172:175], a[236:239], a[216:219], v[172:175]\n"
        "s_waitcnt lgkmcnt(3)\n"
        "v_permlane16_swap_b32 v234, v235\n"
        "v_mfma_f32_16x16x32_bf16 v[172:175], a[244:247], a[220:223], v[172:175]\n"
        "v_max_f32 v234, v234, v235\n"
        "v_mul_f32 v238, s25, v234\n"
        "v_mfma_f32_16x16x32_bf16 v[180:183], a[248:251], a[220:223], v[180:183]\n"
        "ds_read_b128 a[220:223], v248 offset:43040\n"
        "ds_read_b128 v[152:155], v247 offset:16384\n"
        "v_mfma_f32_16x16x32_bf16 v[180:183], a[240:243], a[216:219], v[180:183]\n"
        "s_waitcnt lgkmcnt(4)\n"
        "ds_read_b128 a[216:219], v254 offset:36912\n"
        "ds_read_b128 v[160:163], v247 offset:20480\n"
        "v_mfma_f32_16x16x32_bf16 v[184:187], a[240:243], a[224:227], v[184:187]\n"
        "s_waitcnt lgkmcnt(5)\n"
        "ds_read_b128 v[156:159], v247 offset:16640\n"
        "ds_read_b128 v[164:167], v247 offset:20736\n"
        "v_mfma_f32_16x16x32_bf16 v[184:187], a[248:251], a[228:231], v[184:187]\n"
        "v_mul_f32 v239, s25, v234\n"
        "v_fma_f32 v67, v67, s24, v236\n"
        "v_mfma_f32_16x16x32_bf16 v[176:179], a[244:247], a[228:231], v[176:179]\n"
        "ds_read_b128 a[228:231], v254 offset:43056\n"
        "v_fma_f32 v68, v68, s24, v236\n"
        "v_mfma_f32_16x16x32_bf16 v[176:179], a[236:239], a[224:227], v[176:179]\n"
        "s_waitcnt lgkmcnt(7)\n"
        "ds_read_b128 a[224:227], v248 offset:36928\n"
        "v_fma_f32 v69, v69, s24, v236\n"
        "v_mfma_f32_16x16x32_bf16 v[212:215], a[236:239], a[232:235], v[212:215]\n"
        "s_waitcnt lgkmcnt(7)\n"
        "v_permlane32_swap_b32 v238, v239\n"
        "v_mfma_f32_16x16x32_bf16 v[212:215], a[244:247], a[220:223], v[212:215]\n"
        "v_exp_f32 v64, v64\n"
        "v_mfma_f32_16x16x32_bf16 v[192:195], a[248:251], a[220:223], v[192:195]\n"
        "ds_read_b128 a[220:223], v248 offset:43072\n"
        "v_fma_f32 v70, v70, s24, v236\n"
        "v_mfma_f32_16x16x32_bf16 v[192:195], a[240:243], a[232:235], v[192:195]\n"
        "s_waitcnt lgkmcnt(6)\n"
        "ds_read_b128 a[232:235], v254 offset:36944\n"
        "v_fma_f32 v71, v71, s24, v236\n"
        "v_mfma_f32_16x16x32_bf16 v[196:199], a[240:243], a[216:219], v[196:199]\n"
        "s_waitcnt lgkmcnt(3)\n"
        "v_exp_f32 v65, v65\n"
        "v_mfma_f32_16x16x32_bf16 v[196:199], a[248:251], a[228:231], v[196:199]\n"
        "v_exp_f32 v66, v66\n"
        "v_mfma_f32_16x16x32_bf16 v[216:219], a[244:247], a[228:231], v[216:219]\n"
        "ds_read_b128 a[228:231], v254 offset:43088\n"
        "v_cvt_pk_bf16_f32 v64, v64, v65\n"
        "v_mfma_f32_16x16x32_bf16 v[216:219], a[236:239], a[216:219], v[216:219]\n"
        "s_waitcnt lgkmcnt(3)\n"
        "ds_read_b128 a[216:219], v248 offset:12288\n"
        "v_fma_f32 v72, v72, s24, v236\n"
        "v_mfma_f32_16x16x32_bf16 v[220:223], a[236:239], a[224:227], v[220:223]\n"
        "s_waitcnt lgkmcnt(3)\n"
        "v_exp_f32 v67, v67\n"
        "v_mfma_f32_16x16x32_bf16 v[220:223], a[244:247], a[220:223], v[220:223]\n"
        "v_exp_f32 v68, v68\n"
        "v_mfma_f32_16x16x32_bf16 v[200:203], a[248:251], a[220:223], v[200:203]\n"
        "ds_read_b128 a[220:223], v248 offset:49152\n"
        "v_cvt_pk_bf16_f32 v65, v66, v67\n"
        "v_mfma_f32_16x16x32_bf16 v[200:203], a[240:243], a[224:227], v[200:203]\n"
        "s_waitcnt lgkmcnt(3)\n"
        "ds_read_b128 a[224:227], v254 offset:12304\n"
        "v_fma_f32 v73, v73, s24, v236\n"
        "v_mfma_f32_16x16x32_bf16 v[204:207], a[240:243], a[232:235], v[204:207]\n"
        "s_waitcnt lgkmcnt(3)\n"
        "v_exp_f32 v69, v69\n"
        "v_mfma_f32_16x16x32_bf16 v[204:207], a[248:251], a[228:231], v[204:207]\n"
        "v_exp_f32 v70, v70\n"
        "v_mfma_f32_16x16x32_bf16 v[224:227], a[244:247], a[228:231], v[224:227]\n"
        "ds_read_b128 a[228:231], v254 offset:49168\n"
        "v_cvt_pk_bf16_f32 v66, v68, v69\n"
        "v_mfma_f32_16x16x32_bf16 v[224:227], a[236:239], a[232:235], v[224:227]\n"
        "s_waitcnt lgkmcnt(3)\n"
        "ds_read_b128 a[232:235], v248 offset:12320\n"
        "v_fma_f32 v74, v74, s24, v236\n"
        "v_mfma_f32_16x16x32_bf16 v[172:175], v[152:155], a[216:219], v[172:175]\n"
        "s_waitcnt lgkmcnt(3)\n"
        "v_exp_f32 v71, v71\n"
        "v_mfma_f32_16x16x32_bf16 v[172:175], v[160:163], a[220:223], v[172:175]\n"
        "v_exp_f32 v72, v72\n"
        "v_mfma_f32_16x16x32_bf16 v[180:183], v[164:167], a[220:223], v[180:183]\n"
        "ds_read_b128 a[220:223], v248 offset:49184\n"
        "v_cvt_pk_bf16_f32 v67, v70, v71\n"
        "v_mfma_f32_16x16x32_bf16 v[180:183], v[156:159], a[216:219], v[180:183]\n"
        "s_waitcnt lgkmcnt(3)\n"
        "ds_read_b128 a[216:219], v254 offset:12336\n"
        "v_fma_f32 v75, v75, s24, v236\n"
        "v_mfma_f32_16x16x32_bf16 v[184:187], v[156:159], a[224:227], v[184:187]\n"
        "s_waitcnt lgkmcnt(3)\n"
        "v_exp_f32 v73, v73\n"
        "v_mfma_f32_16x16x32_bf16 v[184:187], v[164:167], a[228:231], v[184:187]\n"
        "v_cvt_pk_bf16_f32 v68, v72, v73\n"
        "v_xor_b32 v251, 0x2000, v251\n"
        "v_mfma_f32_16x16x32_bf16 v[176:179], v[160:163], a[228:231], v[176:179]\n"
        "ds_read_b128 a[228:231], v254 offset:49200\n"
        "v_xor_b32 v252, 0x2000, v252\n"
        "v_mfma_f32_16x16x32_bf16 v[176:179], v[152:155], a[224:227], v[176:179]\n"
        "s_waitcnt lgkmcnt(3)\n"
        "ds_read_b128 a[224:227], v248 offset:12352\n"
        "v_add_u32 v245, s45, v245\n"
        "v_mfma_f32_16x16x32_bf16 v[212:215], v[152:155], a[232:235], v[212:215]\n"
        "s_waitcnt lgkmcnt(3)\n"
        "s_waitcnt vmcnt(0)\n"
        "s_barrier\n"
        "v_exp_f32 v74, v74\n"
        "v_mfma_f32_16x16x32_bf16 v[212:215], v[160:163], a[220:223], v[212:215]\n"
        "v_exp_f32 v75, v75\n"
        "v_mfma_f32_16x16x32_bf16 v[192:195], v[164:167], a[220:223], v[192:195]\n"
        "ds_read_b128 a[220:223], v248 offset:49216\n"
        "ds_read_b128 a[236:239], v240 offset:0\n"
        "v_mfma_f32_16x16x32_bf16 v[192:195], v[156:159], a[232:235], v[192:195]\n"
        "s_waitcnt lgkmcnt(4)\n"
        "ds_read_b128 a[232:235], v254 offset:12368\n"
        "ds_read_b128 a[240:243], v241 offset:0\n"
        "v_mfma_f32_16x16x32_bf16 v[196:199], v[156:159], a[216:219], v[196:199]\n"
        "s_waitcnt lgkmcnt(5)\n"
        "ds_read_b128 a[244:247], v240 offset:128\n"
        "v_cvt_pk_bf16_f32 v69, v74, v75\n"
        "v_mfma_f32_16x16x32_bf16 v[196:199], v[164:167], a[228:231], v[196:199]\n"
        "ds_read_b128 a[248:251], v241 offset:128\n"
        "v_add_u32 v246, s45, v246\n"
        "v_mfma_f32_16x16x32_bf16 v[216:219], v[160:163], a[228:231], v[216:219]\n"
        "ds_read_b128 a[228:231], v254 offset:49232\n"
        "v_add_u32 v247, s45, v247\n"
        "v_mfma_f32_16x16x32_bf16 v[216:219], v[152:155], a[216:219], v[216:219]\n"
        "s_waitcnt lgkmcnt(7)\n"
        "ds_read_b128 a[216:219], v240 offset:256\n"
        "v_mfma_f32_16x16x32_bf16 v[220:223], v[152:155], a[224:227], v[220:223]\n"
        "s_waitcnt lgkmcnt(7)\n"
        "v_mfma_f32_16x16x32_bf16 v[220:223], v[160:163], a[220:223], v[220:223]\n"
        "v_mfma_f32_16x16x32_bf16 v[200:203], v[164:167], a[220:223], v[200:203]\n"
        "ds_read_b128 a[220:223], v241 offset:256\n"
        "v_mfma_f32_16x16x32_bf16 v[200:203], v[156:159], a[224:227], v[200:203]\n"
        "s_waitcnt lgkmcnt(6)\n"
        "v_mfma_f32_16x16x32_bf16 v[204:207], v[156:159], a[232:235], v[204:207]\n"
        "s_waitcnt lgkmcnt(2)\n"
        "v_mfma_f32_16x16x32_bf16 v[204:207], v[164:167], a[228:231], v[204:207]\n"
        "v_mfma_f32_16x16x32_bf16 v[224:227], v[160:163], a[228:231], v[224:227]\n"
        "v_mfma_f32_16x16x32_bf16 v[224:227], v[152:155], a[232:235], v[224:227]\n"
        "s_waitcnt lgkmcnt(0)\n"
        ".p2align 6\n"
        "10:\n"
        "v_mfma_f32_16x16x32_bf16 v[162:165], a[72:75], a[236:239], 0\n"
        "v_fma_f32 v76, v76, s24, v236\n"
        "v_fma_f32 v77, v77, s24, v236\n"
        "v_mfma_f32_16x16x32_bf16 v[162:165], a[76:79], a[240:243], v[162:165]\n"
        "s_waitcnt lgkmcnt(3)\n"
        "s_add_u32 s41, s30, s40\n"
        "v_cvt_pk_bf16_f32 v232, v172, v176\n"
        "v_cvt_pk_bf16_f32 v233, v212, v216\n"
        "v_mfma_f32_16x16x32_bf16 v[162:165], a[80:83], a[244:247], v[162:165]\n"
        "s_waitcnt lgkmcnt(2)\n"
        "v_cvt_pk_bf16_f32 v234, v220, v224\n"
        "v_fma_f32 v78, v78, s24, v236\n"
        "v_mfma_f32_16x16x32_bf16 v[162:165], a[84:87], a[248:251], v[162:165]\n"
        "s_waitcnt lgkmcnt(1)\n"
        "buffer_store_dwordx3 v[232:234], v250, s[16:19], s38 offen offset:0 sc1\n"
        "v_mfma_f32_16x16x32_bf16 v[162:165], a[88:91], a[216:219], v[162:165]\n"
        "s_waitcnt lgkmcnt(0)\n"
        "v_exp_f32 v76, v76\n"
        "v_mfma_f32_16x16x32_bf16 v[162:165], a[92:95], a[220:223], v[162:165]\n"
        "v_exp_f32 v77, v77\n"
        "v_mfma_f32_16x16x32_bf16 v[72:75], a[116:119], a[220:223], 0\n"
        "v_fma_f32 v79, v79, s24, v236\n"
        "v_cvt_pk_bf16_f32 v70, v76, v77\n"
        "v_mfma_f32_16x16x32_bf16 v[72:75], a[112:115], a[216:219], v[72:75]\n"
        "v_exp_f32 v78, v78\n"
        "v_mfma_f32_16x16x32_bf16 v[72:75], a[108:111], a[248:251], v[72:75]\n"
        "v_exp_f32 v79, v79\n"
        "v_mfma_f32_16x16x32_bf16 v[72:75], a[104:107], a[244:247], v[72:75]\n"
        "v_cvt_pk_bf16_f32 v71, v78, v79\n"
        "v_cvt_pk_bf16_f32 v232, v173, v177\n"
        "v_mfma_f32_16x16x32_bf16 v[72:75], a[100:103], a[240:243], v[72:75]\n"
        "v_cvt_pk_bf16_f32 v233, v213, v217\n"
        "v_cvt_pk_bf16_f32 v234, v221, v225\n"
        "v_mfma_f32_16x16x32_bf16 v[72:75], a[96:99], a[236:239], v[72:75]\n"
        "buffer_store_dwordx3 v[232:234], v250, s[16:19], s38 offen offset:384 sc1\n"
        "v_mfma_f32_16x16x32_bf16 v[76:79], a[120:123], a[236:239], 0\n"
        "s_add_u32 s35, s47, 786432\n"
        "v_cvt_pk_bf16_f32 v152, v162, v163\n"
        "v_cvt_pk_bf16_f32 v153, v164, v165\n"
        "v_mfma_f32_16x16x32_bf16 v[76:79], a[124:127], a[240:243], v[76:79]\n"
        "s_sub_u32 s46, s35, 100663296\n"
        "v_fma_f32 v80, v80, s24, v237\n"
        "v_cvt_pk_bf16_f32 v154, v72, v73\n"
        "v_mfma_f32_16x16x32_bf16 v[76:79], a[128:131], a[244:247], v[76:79]\n"
        "s_cmp_lt_u32 s46, 786432\n"
        "v_exp_f32 v80, v80\n"
        "v_mfma_f32_16x16x32_bf16 v[76:79], a[132:135], a[248:251], v[76:79]\n"
        "s_cselect_b32 s35, s46, s35\n"
        "v_cvt_pk_bf16_f32 v155, v74, v75\n"
        "v_cvt_pk_bf16_f32 v232, v174, v178\n"
        "v_mfma_f32_16x16x32_bf16 v[76:79], a[136:139], a[216:219], v[76:79]\n"
        "v_cvt_pk_bf16_f32 v233, v214, v218\n"
        "v_cvt_pk_bf16_f32 v234, v222, v226\n"
        "v_mfma_f32_16x16x32_bf16 v[76:79], a[140:143], a[220:223], v[76:79]\n"
        "v_fma_f32 v81, v81, s24, v237\n"
        "v_fma_f32 v82, v82, s24, v237\n"
        "v_mfma_f32_16x16x32_bf16 v[162:165], a[20:23], a[220:223], 0\n"
        "v_exp_f32 v81, v81\n"
        "v_mfma_f32_16x16x32_bf16 v[162:165], a[16:19], a[216:219], v[162:165]\n"
        "v_exp_f32 v82, v82\n"
        "v_mfma_f32_16x16x32_bf16 v[162:165], a[12:15], a[248:251], v[162:165]\n"
        "buffer_store_dwordx3 v[232:234], v250, s[16:19], s38 offen offset:768 sc1\n"
        "v_mfma_f32_16x16x32_bf16 v[162:165], a[8:11], a[244:247], v[162:165]\n"
        "v_cvt_pk_bf16_f32 v168, v76, v77\n"
        "v_cvt_pk_bf16_f32 v169, v78, v79\n"
        "v_mfma_f32_16x16x32_bf16 v[162:165], a[4:7], a[240:243], v[162:165]\n"
        "v_cvt_pk_bf16_f32 v80, v80, v81\n"
        "v_fma_f32 v83, v83, s24, v237\n"
        "v_mfma_f32_16x16x32_bf16 v[162:165], a[0:3], a[236:239], v[162:165]\n"
        "v_exp_f32 v83, v83\n"
        "v_mfma_f32_16x16x32_bf16 v[72:75], a[24:27], a[236:239], 0\n"
        "v_cvt_pk_bf16_f32 v232, v175, v179\n"
        "v_cvt_pk_bf16_f32 v233, v215, v219\n"
        "v_mfma_f32_16x16x32_bf16 v[72:75], a[28:31], a[240:243], v[72:75]\n"
        "v_cvt_pk_bf16_f32 v234, v223, v227\n"
        "v_cvt_pk_bf16_f32 v81, v82, v83\n"
        "v_mfma_f32_16x16x32_bf16 v[72:75], a[32:35], a[244:247], v[72:75]\n"
        "v_cvt_pk_bf16_f32 v156, v162, v163\n"
        "v_cvt_pk_bf16_f32 v157, v164, v165\n"
        "v_mfma_f32_16x16x32_bf16 v[72:75], a[36:39], a[248:251], v[72:75]\n"
        "v_fma_f32 v84, v84, s24, v237\n"
        "v_fma_f32 v85, v85, s24, v237\n"
        "v_mfma_f32_16x16x32_bf16 v[72:75], a[40:43], a[216:219], v[72:75]\n"
        "v_exp_f32 v84, v84\n"
        "v_mfma_f32_16x16x32_bf16 v[72:75], a[44:47], a[220:223], v[72:75]\n"
        "buffer_store_dwordx3 v[232:234], v250, s[16:19], s38 offen offset:1152 sc1\n"
        "v_mfma_f32_16x16x32_bf16 v[76:79], a[68:71], a[220:223], 0\n"
        "v_exp_f32 v85, v85\n"
        "v_mfma_f32_16x16x32_bf16 v[76:79], a[64:67], a[216:219], v[76:79]\n"
        "v_cvt_pk_bf16_f32 v82, v84, v85\n"
        "v_fma_f32 v86, v86, s24, v237\n"
        "v_mfma_f32_16x16x32_bf16 v[76:79], a[60:63], a[248:251], v[76:79]\n"
        "ds_read_b128 a[224:227], v240 offset:1536\n"
        "v_cvt_pk_bf16_f32 v158, v72, v73\n"
        "v_mfma_f32_16x16x32_bf16 v[76:79], a[56:59], a[244:247], v[76:79]\n"
        "ds_read_b128 a[228:231], v241 offset:1536\n"
        "v_cvt_pk_bf16_f32 v159, v74, v75\n"
        "v_mfma_f32_16x16x32_bf16 v[76:79], a[52:55], a[240:243], v[76:79]\n"
        "ds_read_b128 a[232:235], v240 offset:1664\n"
        "v_cvt_pk_bf16_f32 v232, v180, v184\n"
        "v_mfma_f32_16x16x32_bf16 v[76:79], a[48:51], a[236:239], v[76:79]\n"
        "v_cvt_pk_bf16_f32 v233, v192, v196\n"
        "v_cvt_pk_bf16_f32 v234, v200, v204\n"
        "v_mfma_f32_16x16x32_bf16 v[162:165], a[236:239], a[144:147], 0\n"
        "v_exp_f32 v86, v86\n"
        "v_mfma_f32_16x16x32_bf16 v[162:165], a[240:243], a[148:151], v[162:165]\n"
        "v_fma_f32 v87, v87, s24, v237\n"
        "v_fma_f32 v88, v88, s24, v237\n"
        "v_mfma_f32_16x16x32_bf16 v[162:165], a[244:247], a[152:155], v[162:165]\n"
        "buffer_store_dwordx3 v[232:234], v250, s[16:19], s39 offen offset:0 sc1\n"
        "v_mfma_f32_16x16x32_bf16 v[162:165], a[248:251], a[156:159], v[162:165]\n"
        "v_exp_f32 v87, v87\n"
        "v_mfma_f32_16x16x32_bf16 v[162:165], a[216:219], a[160:163], v[162:165]\n"
        "v_cvt_pk_bf16_f32 v160, v76, v77\n"
        "v_cvt_pk_bf16_f32 v161, v78, v79\n"
        "v_mfma_f32_16x16x32_bf16 v[162:165], a[220:223], a[164:167], v[162:165]\n"
        "v_cvt_pk_bf16_f32 v166, v76, v77\n"
        "v_cvt_pk_bf16_f32 v167, v78, v79\n"
        "v_mfma_f32_16x16x32_bf16 v[72:75], a[220:223], a[188:191], 0\n"
        "v_exp_f32 v88, v88\n"
        "v_mfma_f32_16x16x32_bf16 v[72:75], a[216:219], a[184:187], v[72:75]\n"
        "v_cvt_pk_bf16_f32 v232, v181, v185\n"
        "v_cvt_pk_bf16_f32 v233, v193, v197\n"
        "v_mfma_f32_16x16x32_bf16 v[72:75], a[248:251], a[180:183], v[72:75]\n"
        "v_cvt_pk_bf16_f32 v234, v201, v205\n"
        "v_cvt_pk_bf16_f32 v212, v162, v163\n"
        "v_mfma_f32_16x16x32_bf16 v[72:75], a[244:247], a[176:179], v[72:75]\n"
        "v_cvt_pk_bf16_f32 v213, v164, v165\n"
        "v_cvt_pk_bf16_f32 v83, v86, v87\n"
        "v_mfma_f32_16x16x32_bf16 v[72:75], a[240:243], a[172:175], v[72:75]\n"
        "v_fma_f32 v89, v89, s24, v237\n"
        "v_fma_f32 v90, v90, s24, v237\n"
        "v_mfma_f32_16x16x32_bf16 v[72:75], a[236:239], a[168:171], v[72:75]\n"
        "buffer_store_dwordx3 v[232:234], v250, s[16:19], s39 offen offset:384 sc1\n"
        "v_mfma_f32_16x16x32_bf16 v[76:79], a[236:239], a[192:195], 0\n"
        "ds_read_b128 a[236:239], v241 offset:1664\n"
        "v_fma_f32 v91, v91, s24, v237\n"
        "v_mfma_f32_16x16x32_bf16 v[76:79], a[240:243], a[196:199], v[76:79]\n"
        "ds_read_b128 a[240:243], v240 offset:1792\n"
        "v_fma_f32 v92, v92, s24, v237\n"
        "v_mfma_f32_16x16x32_bf16 v[76:79], a[244:247], a[200:203], v[76:79]\n"
        "ds_read_b128 a[244:247], v241 offset:1792\n"
        "v_cvt_pk_bf16_f32 v214, v72, v73\n"
        "v_mfma_f32_16x16x32_bf16 v[76:79], a[248:251], a[204:207], v[76:79]\n"
        "v_exp_f32 v89, v89\n"
        "v_mfma_f32_16x16x32_bf16 v[76:79], a[216:219], a[208:211], v[76:79]\n"
        "v_cvt_pk_bf16_f32 v215, v74, v75\n"
        "v_cvt_pk_bf16_f32 v232, v182, v186\n"
        "v_mfma_f32_16x16x32_bf16 v[76:79], a[220:223], a[212:215], v[76:79]\n"
        "s_waitcnt lgkmcnt(5)\n"
        "v_cvt_pk_bf16_f32 v233, v194, v198\n"
        "v_cvt_pk_bf16_f32 v234, v202, v206\n"
        "v_mfma_f32_16x16x32_bf16 v[162:165], a[72:75], a[224:227], 0\n"
        "s_waitcnt lgkmcnt(4)\n"
        "v_exp_f32 v90, v90\n"
        "v_mfma_f32_16x16x32_bf16 v[162:165], a[76:79], a[228:231], v[162:165]\n"
        "s_waitcnt lgkmcnt(3)\n"
        "v_exp_f32 v91, v91\n"
        "v_mfma_f32_16x16x32_bf16 v[162:165], a[80:83], a[232:235], v[162:165]\n"
        "s_waitcnt lgkmcnt(2)\n"
        "buffer_store_dwordx3 v[232:234], v250, s[16:19], s39 offen offset:768 sc1\n"
        "v_mfma_f32_16x16x32_bf16 v[162:165], a[84:87], a[236:239], v[162:165]\n"
        "s_waitcnt lgkmcnt(1)\n"
        "v_exp_f32 v92, v92\n"
        "v_mfma_f32_16x16x32_bf16 v[162:165], a[88:91], a[240:243], v[162:165]\n"
        "s_waitcnt lgkmcnt(0)\n"
        "v_cvt_pk_bf16_f32 v216, v76, v77\n"
        "v_cvt_pk_bf16_f32 v217, v78, v79\n"
        "v_mfma_f32_16x16x32_bf16 v[162:165], a[92:95], a[244:247], v[162:165]\n"
        "v_cvt_pk_bf16_f32 v232, v183, v187\n"
        "v_cvt_pk_bf16_f32 v233, v195, v199\n"
        "v_mfma_f32_16x16x32_bf16 v[72:75], a[116:119], a[244:247], 0\n"
        "v_cvt_pk_bf16_f32 v234, v203, v207\n"
        "v_cvt_pk_bf16_f32 v84, v88, v89\n"
        "v_mfma_f32_16x16x32_bf16 v[72:75], a[112:115], a[240:243], v[72:75]\n"
        "v_cvt_pk_bf16_f32 v85, v90, v91\n"
        "v_fma_f32 v93, v93, s24, v237\n"
        "v_mfma_f32_16x16x32_bf16 v[72:75], a[108:111], a[236:239], v[72:75]\n"
        "v_exp_f32 v93, v93\n"
        "v_mfma_f32_16x16x32_bf16 v[72:75], a[104:107], a[232:235], v[72:75]\n"
        "v_cvt_pk_bf16_f32 v172, v162, v163\n"
        "v_cvt_pk_bf16_f32 v173, v164, v165\n"
        "v_mfma_f32_16x16x32_bf16 v[72:75], a[100:103], a[228:231], v[72:75]\n"
        "v_cvt_pk_bf16_f32 v86, v92, v93\n"
        "v_fma_f32 v94, v94, s24, v237\n"
        "v_mfma_f32_16x16x32_bf16 v[72:75], a[96:99], a[224:227], v[72:75]\n"
        "buffer_store_dwordx3 v[232:234], v250, s[16:19], s39 offen offset:1152 sc1\n"
        "v_mfma_f32_16x16x32_bf16 v[76:79], a[120:123], a[224:227], 0\n"
        "v_exp_f32 v94, v94\n"
        "v_mfma_f32_16x16x32_bf16 v[76:79], a[124:127], a[228:231], v[76:79]\n"
        "v_fma_f32 v95, v95, s24, v237\n"
        "v_fma_f32 v96, v96, s24, v238\n"
        "v_mfma_f32_16x16x32_bf16 v[76:79], a[128:131], a[232:235], v[76:79]\n"
        "v_exp_f32 v95, v95\n"
        "v_mfma_f32_16x16x32_bf16 v[76:79], a[132:135], a[236:239], v[76:79]\n"
        "v_exp_f32 v96, v96\n"
        "v_mfma_f32_16x16x32_bf16 v[76:79], a[136:139], a[240:243], v[76:79]\n"
        "v_cvt_pk_bf16_f32 v174, v72, v73\n"
        "v_cvt_pk_bf16_f32 v175, v74, v75\n"
        "v_mfma_f32_16x16x32_bf16 v[76:79], a[140:143], a[244:247], v[76:79]\n"
        "v_cvt_pk_bf16_f32 v87, v94, v95\n"
        "v_fma_f32 v97, v97, s24, v238\n"
        "v_mfma_f32_16x16x32_bf16 v[162:165], a[20:23], a[244:247], 0\n"
        "s_mov_b32 m0, s48\n"
        "v_exp_f32 v97, v97\n"
        "v_mfma_f32_16x16x32_bf16 v[162:165], a[16:19], a[240:243], v[162:165]\n"
        "v_cvt_pk_bf16_f32 v96, v96, v97\n"
        "v_fma_f32 v98, v98, s24, v238\n"
        "v_mfma_f32_16x16x32_bf16 v[162:165], a[12:15], a[236:239], v[162:165]\n"
        "buffer_load_dwordx4 v249, s[12:15], s36 offen offset:0 lds\n"
        "v_mfma_f32_16x16x32_bf16 v[162:165], a[8:11], a[232:235], v[162:165]\n"
        "v_exp_f32 v98, v98\n"
        "v_mfma_f32_16x16x32_bf16 v[162:165], a[4:7], a[228:231], v[162:165]\n"
        "v_cvt_pk_bf16_f32 v190, v76, v77\n"
        "v_cvt_pk_bf16_f32 v191, v78, v79\n"
        "v_mfma_f32_16x16x32_bf16 v[162:165], a[0:3], a[224:227], v[162:165]\n"
        "v_fma_f32 v99, v99, s24, v238\n"
        "v_fma_f32 v100, v100, s24, v238\n"
        "v_mfma_f32_16x16x32_bf16 v[72:75], a[24:27], a[224:227], 0\n"
        "v_exp_f32 v99, v99\n"
        "v_mfma_f32_16x16x32_bf16 v[72:75], a[28:31], a[228:231], v[72:75]\n"
        "v_exp_f32 v100, v100\n"
        "v_mfma_f32_16x16x32_bf16 v[72:75], a[32:35], a[232:235], v[72:75]\n"
        "v_cvt_pk_bf16_f32 v97, v98, v99\n"
        "v_cvt_pk_bf16_f32 v176, v162, v163\n"
        "v_mfma_f32_16x16x32_bf16 v[72:75], a[36:39], a[236:239], v[72:75]\n"
        "v_cvt_pk_bf16_f32 v177, v164, v165\n"
        "v_fma_f32 v101, v101, s24, v238\n"
        "v_mfma_f32_16x16x32_bf16 v[72:75], a[40:43], a[240:243], v[72:75]\n"
        "v_exp_f32 v101, v101\n"
        "v_mfma_f32_16x16x32_bf16 v[72:75], a[44:47], a[244:247], v[72:75]\n"
        "v_cvt_pk_bf16_f32 v98, v100, v101\n"
        "v_fma_f32 v102, v102, s24, v238\n"
        "v_mfma_f32_16x16x32_bf16 v[76:79], a[68:71], a[244:247], 0\n"
        "v_xor_b32 v253, 32, v249\n"
        "v_fma_f32 v103, v103, s24, v238\n"
        "v_mfma_f32_16x16x32_bf16 v[76:79], a[64:67], a[240:243], v[76:79]\n"
        "v_exp_f32 v102, v102\n"
        "v_mfma_f32_16x16x32_bf16 v[76:79], a[60:63], a[236:239], v[76:79]\n"
        "ds_read_b128 a[248:251], v240 offset:12288\n"
        "v_cvt_pk_bf16_f32 v178, v72, v73\n"
        "v_mfma_f32_16x16x32_bf16 v[76:79], a[56:59], a[232:235], v[76:79]\n"
        "ds_read_b128 a[216:219], v241 offset:12288\n"
        "v_cvt_pk_bf16_f32 v179, v74, v75\n"
        "v_mfma_f32_16x16x32_bf16 v[76:79], a[52:55], a[228:231], v[76:79]\n"
        "buffer_load_dwordx4 v253, s[12:15], s36 offen offset:1024 lds\n"
        "v_mfma_f32_16x16x32_bf16 v[76:79], a[48:51], a[224:227], v[76:79]\n"
        "ds_read_b128 a[220:223], v240 offset:12416\n"
        "v_fma_f32 v104, v104, s24, v238\n"
        "v_mfma_f32_16x16x32_bf16 v[162:165], a[224:227], a[144:147], 0\n"
        "v_exp_f32 v103, v103\n"
        "v_mfma_f32_16x16x32_bf16 v[162:165], a[228:231], a[148:151], v[162:165]\n"
        "v_exp_f32 v104, v104\n"
        "v_mfma_f32_16x16x32_bf16 v[162:165], a[232:235], a[152:155], v[162:165]\n"
        "v_cvt_pk_bf16_f32 v99, v102, v103\n"
        "v_cvt_pk_bf16_f32 v180, v76, v77\n"
        "v_mfma_f32_16x16x32_bf16 v[162:165], a[236:239], a[156:159], v[162:165]\n"
        "v_cvt_pk_bf16_f32 v181, v78, v79\n"
        "v_cvt_pk_bf16_f32 v186, v76, v77\n"
        "v_mfma_f32_16x16x32_bf16 v[162:165], a[240:243], a[160:163], v[162:165]\n"
        "v_cvt_pk_bf16_f32 v187, v78, v79\n"
        "v_fma_f32 v105, v105, s24, v238\n"
        "v_mfma_f32_16x16x32_bf16 v[162:165], a[244:247], a[164:167], v[162:165]\n"
        "v_exp_f32 v105, v105\n"
        "v_mfma_f32_16x16x32_bf16 v[72:75], a[244:247], a[188:191], 0\n"
        "v_cvt_pk_bf16_f32 v100, v104, v105\n"
        "v_fma_f32 v106, v106, s24, v238\n"
        "v_mfma_f32_16x16x32_bf16 v[72:75], a[240:243], a[184:187], v[72:75]\n"
        "v_exp_f32 v106, v106\n"
        "v_mfma_f32_16x16x32_bf16 v[72:75], a[236:239], a[180:183], v[72:75]\n"
        "v_fma_f32 v107, v107, s24, v238\n"
        "v_cvt_pk_bf16_f32 v218, v162, v163\n"
        "v_mfma_f32_16x16x32_bf16 v[72:75], a[232:235], a[176:179], v[72:75]\n"
        "v_exp_f32 v107, v107\n"
        "v_mfma_f32_16x16x32_bf16 v[72:75], a[228:231], a[172:175], v[72:75]\n"
        "s_add_u32 m0, s41, 0\n"
        "v_cvt_pk_bf16_f32 v219, v164, v165\n"
        "v_cvt_pk_bf16_f32 v101, v106, v107\n"
        "v_mfma_f32_16x16x32_bf16 v[72:75], a[224:227], a[168:171], v[72:75]\n"
        "v_fma_f32 v108, v108, s24, v238\n"
        "v_fma_f32 v109, v109, s24, v238\n"
        "v_mfma_f32_16x16x32_bf16 v[76:79], a[224:227], a[192:195], 0\n"
        "ds_read_b128 a[224:227], v241 offset:12416\n"
        "v_fma_f32 v110, v110, s24, v238\n"
        "v_mfma_f32_16x16x32_bf16 v[76:79], a[228:231], a[196:199], v[76:79]\n"
        "ds_read_b128 a[228:231], v240 offset:12544\n"
        "v_fma_f32 v111, v111, s24, v238\n"
        "v_mfma_f32_16x16x32_bf16 v[76:79], a[232:235], a[200:203], v[76:79]\n"
        "buffer_load_dwordx4 v242, s[8:11], s47 offen lds\n"
        "s_sub_u32 s31, s30, s28\n"
        "v_mfma_f32_16x16x32_bf16 v[76:79], a[236:239], a[204:207], v[76:79]\n"
        "ds_read_b128 a[232:235], v241 offset:12544\n"
        "s_sub_u32 s45, s28, s29\n"
        "v_cvt_pk_bf16_f32 v220, v72, v73\n"
        "v_mfma_f32_16x16x32_bf16 v[76:79], a[240:243], a[208:211], v[76:79]\n"
        "s_add_u32 s28, s28, s31\n"
        "v_cvt_pk_bf16_f32 v221, v74, v75\n"
        "v_fma_f32 v112, v112, s24, v239\n"
        "v_mfma_f32_16x16x32_bf16 v[76:79], a[244:247], a[212:215], v[76:79]\n"
        "s_waitcnt lgkmcnt(5)\n"
        "v_exp_f32 v108, v108\n"
        "v_mfma_f32_16x16x32_bf16 v[162:165], a[72:75], a[248:251], 0\n"
        "s_waitcnt lgkmcnt(4)\n"
        "v_exp_f32 v109, v109\n"
        "v_mfma_f32_16x16x32_bf16 v[162:165], a[76:79], a[216:219], v[162:165]\n"
        "s_waitcnt lgkmcnt(3)\n"
        "v_exp_f32 v110, v110\n"
        "v_mfma_f32_16x16x32_bf16 v[162:165], a[80:83], a[220:223], v[162:165]\n"
        "s_waitcnt lgkmcnt(2)\n"
        "v_exp_f32 v111, v111\n"
        "v_mfma_f32_16x16x32_bf16 v[162:165], a[84:87], a[224:227], v[162:165]\n"
        "s_waitcnt lgkmcnt(1)\n"
        "v_exp_f32 v112, v112\n"
        "v_mfma_f32_16x16x32_bf16 v[162:165], a[88:91], a[228:231], v[162:165]\n"
        "s_waitcnt lgkmcnt(0)\n"
        "v_cvt_pk_bf16_f32 v222, v76, v77\n"
        "v_cvt_pk_bf16_f32 v223, v78, v79\n"
        "v_mfma_f32_16x16x32_bf16 v[162:165], a[92:95], a[232:235], v[162:165]\n"
        "s_mov_b32 s30, s29\n"
        "v_cvt_pk_bf16_f32 v102, v108, v109\n"
        "v_cvt_pk_bf16_f32 v103, v110, v111\n"
        "v_mfma_f32_16x16x32_bf16 v[72:75], a[116:119], a[232:235], 0\n"
        "s_add_u32 m0, s41, 1024\n"
        "v_fma_f32 v113, v113, s24, v239\n"
        "v_fma_f32 v114, v114, s24, v239\n"
        "v_mfma_f32_16x16x32_bf16 v[72:75], a[112:115], a[228:231], v[72:75]\n"
        "s_add_u32 s29, s29, s45\n"
        "v_exp_f32 v113, v113\n"
        "v_mfma_f32_16x16x32_bf16 v[72:75], a[108:111], a[224:227], v[72:75]\n"
        "buffer_load_dwordx4 v243, s[8:11], s47 offen lds\n"
        "v_mfma_f32_16x16x32_bf16 v[72:75], a[104:107], a[220:223], v[72:75]\n"
        "v_exp_f32 v114, v114\n"
        "v_mfma_f32_16x16x32_bf16 v[72:75], a[100:103], a[216:219], v[72:75]\n"
        "v_cvt_pk_bf16_f32 v192, v162, v163\n"
        "v_cvt_pk_bf16_f32 v193, v164, v165\n"
        "v_mfma_f32_16x16x32_bf16 v[72:75], a[96:99], a[248:251], v[72:75]\n"
        "v_cvt_pk_bf16_f32 v112, v112, v113\n"
        "v_fma_f32 v115, v115, s24, v239\n"
        "v_mfma_f32_16x16x32_bf16 v[76:79], a[120:123], a[248:251], 0\n"
        "v_exp_f32 v115, v115\n"
        "v_mfma_f32_16x16x32_bf16 v[76:79], a[124:127], a[216:219], v[76:79]\n"
        "v_cvt_pk_bf16_f32 v113, v114, v115\n"
        "v_fma_f32 v116, v116, s24, v239\n"
        "v_mfma_f32_16x16x32_bf16 v[76:79], a[128:131], a[220:223], v[76:79]\n"
        "v_exp_f32 v116, v116\n"
        "v_mfma_f32_16x16x32_bf16 v[76:79], a[132:135], a[224:227], v[76:79]\n"
        "v_cvt_pk_bf16_f32 v194, v72, v73\n"
        "v_cvt_pk_bf16_f32 v195, v74, v75\n"
        "v_mfma_f32_16x16x32_bf16 v[76:79], a[136:139], a[228:231], v[76:79]\n"
        "v_fma_f32 v117, v117, s24, v239\n"
        "v_fma_f32 v118, v118, s24, v239\n"
        "v_mfma_f32_16x16x32_bf16 v[76:79], a[140:143], a[232:235], v[76:79]\n"
        "v_exp_f32 v117, v117\n"
        "v_mfma_f32_16x16x32_bf16 v[162:165], a[20:23], a[232:235], 0\n"
        "v_exp_f32 v118, v118\n"
        "v_mfma_f32_16x16x32_bf16 v[162:165], a[16:19], a[228:231], v[162:165]\n"
        "v_cvt_pk_bf16_f32 v114, v116, v117\n"
        "v_fma_f32 v119, v119, s24, v239\n"
        "v_mfma_f32_16x16x32_bf16 v[162:165], a[12:15], a[224:227], v[162:165]\n"
        "s_add_u32 m0, s41, 2048\n"
        "v_exp_f32 v119, v119\n"
        "v_mfma_f32_16x16x32_bf16 v[162:165], a[8:11], a[220:223], v[162:165]\n"
        "v_cvt_pk_bf16_f32 v208, v76, v77\n"
        "v_cvt_pk_bf16_f32 v209, v78, v79\n"
        "v_mfma_f32_16x16x32_bf16 v[162:165], a[4:7], a[216:219], v[162:165]\n"
        "buffer_load_dwordx4 v244, s[8:11], s47 offen lds\n"
        "v_mfma_f32_16x16x32_bf16 v[162:165], a[0:3], a[248:251], v[162:165]\n"
        "v_cvt_pk_bf16_f32 v115, v118, v119\n"
        "v_fma_f32 v120, v120, s24, v239\n"
        "v_mfma_f32_16x16x32_bf16 v[72:75], a[24:27], a[248:251], 0\n"
        "v_exp_f32 v120, v120\n"
        "v_mfma_f32_16x16x32_bf16 v[72:75], a[28:31], a[216:219], v[72:75]\n"
        "v_fma_f32 v121, v121, s24, v239\n"
        "v_fma_f32 v122, v122, s24, v239\n"
        "v_mfma_f32_16x16x32_bf16 v[72:75], a[32:35], a[220:223], v[72:75]\n"
        "v_exp_f32 v121, v121\n"
        "v_mfma_f32_16x16x32_bf16 v[72:75], a[36:39], a[224:227], v[72:75]\n"
        "v_exp_f32 v122, v122\n"
        "v_mfma_f32_16x16x32_bf16 v[72:75], a[40:43], a[228:231], v[72:75]\n"
        "v_cvt_pk_bf16_f32 v196, v162, v163\n"
        "v_cvt_pk_bf16_f32 v197, v164, v165\n"
        "v_mfma_f32_16x16x32_bf16 v[72:75], a[44:47], a[232:235], v[72:75]\n"
        "v_cvt_pk_bf16_f32 v116, v120, v121\n"
        "v_fma_f32 v123, v123, s24, v239\n"
        "v_mfma_f32_16x16x32_bf16 v[76:79], a[68:71], a[232:235], 0\n"
        "v_exp_f32 v123, v123\n"
        "v_mfma_f32_16x16x32_bf16 v[76:79], a[64:67], a[228:231], v[76:79]\n"
        "v_cvt_pk_bf16_f32 v117, v122, v123\n"
        "v_fma_f32 v124, v124, s24, v239\n"
        "v_mfma_f32_16x16x32_bf16 v[76:79], a[60:63], a[224:227], v[76:79]\n"
        "v_exp_f32 v124, v124\n"
        "v_mfma_f32_16x16x32_bf16 v[76:79], a[56:59], a[220:223], v[76:79]\n"
        "v_cvt_pk_bf16_f32 v198, v72, v73\n"
        "v_cvt_pk_bf16_f32 v199, v74, v75\n"
        "v_mfma_f32_16x16x32_bf16 v[76:79], a[52:55], a[216:219], v[76:79]\n"
        "s_add_u32 m0, s41, 12288\n"
        "v_fma_f32 v125, v125, s24, v239\n"
        "v_fma_f32 v126, v126, s24, v239\n"
        "v_mfma_f32_16x16x32_bf16 v[76:79], a[48:51], a[248:251], v[76:79]\n"
        "v_exp_f32 v125, v125\n"
        "v_mfma_f32_16x16x32_bf16 v[182:185], a[252:255], v[64:67], 0\n"
        "buffer_load_dwordx4 v242, s[8:11], s35 offen lds\n"
        "v_mfma_f32_16x16x32_bf16 v[182:185], a[252:255], v[68:71], v[182:185]\n"
        "v_exp_f32 v126, v126\n"
        "v_mfma_f32_16x16x32_bf16 v[202:205], v[132:135], v[68:71], 0\n"
        "v_cvt_pk_bf16_f32 v118, v124, v125\n"
        "v_fma_f32 v127, v127, s24, v239\n"
        "v_mfma_f32_16x16x32_bf16 v[202:205], v[128:131], v[64:67], v[202:205]\n"
        "v_exp_f32 v127, v127\n"
        "v_mfma_f32_16x16x32_bf16 v[88:91], v[136:139], v[64:67], 0\n"
        "v_cvt_pk_bf16_f32 v200, v76, v77\n"
        "v_cvt_pk_bf16_f32 v201, v78, v79\n"
        "v_mfma_f32_16x16x32_bf16 v[88:91], v[140:143], v[68:71], v[88:91]\n"
        "v_cvt_pk_bf16_f32 v206, v76, v77\n"
        "v_cvt_pk_bf16_f32 v207, v78, v79\n"
        "v_mfma_f32_16x16x32_bf16 v[92:95], v[148:151], v[68:71], 0\n"
        "s_cmp_eq_u32 s37, 100663296\n"
        "v_rcp_f32 v182, v182\n"
        "v_mfma_f32_16x16x32_bf16 v[92:95], v[144:147], v[64:67], v[92:95]\n"
        "s_cselect_b32 s46, 0, s37\n"
        "v_cvt_pk_bf16_f32 v119, v126, v127\n"
        "v_mul_f32 v202, v182, v202\n"
        "v_mfma_f32_16x16x32_bf16 v[64:67], a[252:255], v[80:83], 0\n"
        "s_add_u32 s38, s46, s43\n"
        "v_mul_f32 v203, v182, v203\n"
        "v_mul_f32 v204, v182, v204\n"
        "v_mfma_f32_16x16x32_bf16 v[64:67], a[252:255], v[84:87], v[64:67]\n"
        "s_add_u32 s39, s46, s44\n"
        "v_mul_f32 v205, v182, v205\n"
        "v_cvt_pk_bf16_f32 v202, v202, v203\n"
        "v_mfma_f32_16x16x32_bf16 v[68:71], v[132:135], v[84:87], 0\n"
        "s_add_u32 s37, s37, 6291456\n"
        "v_cvt_pk_bf16_f32 v203, v204, v205\n"
        "v_mul_f32 v88, v182, v88\n"
        "v_mfma_f32_16x16x32_bf16 v[68:71], v[128:131], v[80:83], v[68:71]\n"
        "v_rcp_f32 v64, v64\n"
        "v_mfma_f32_16x16x32_bf16 v[72:75], v[136:139], v[80:83], 0\n"
        "s_add_u32 m0, s41, 13312\n"
        "v_mul_f32 v89, v182, v89\n"
        "v_mul_f32 v90, v182, v90\n"
        "v_mfma_f32_16x16x32_bf16 v[72:75], v[140:143], v[84:87], v[72:75]\n"
        "v_mul_f32 v91, v182, v91\n"
        "v_cvt_pk_bf16_f32 v204, v88, v89\n"
        "v_mfma_f32_16x16x32_bf16 v[162:165], v[148:151], v[84:87], 0\n"
        "buffer_load_dwordx4 v243, s[8:11], s35 offen lds\n"
        "v_mfma_f32_16x16x32_bf16 v[162:165], v[144:147], v[80:83], v[162:165]\n"
        "v_cvt_pk_bf16_f32 v205, v90, v91\n"
        "v_mul_f32 v92, v182, v92\n"
        "v_mfma_f32_16x16x32_bf16 v[80:83], a[252:255], v[96:99], 0\n"
        "ds_write_b128 v245, v[202:205] offset:0\n"
        "v_mfma_f32_16x16x32_bf16 v[80:83], a[252:255], v[100:103], v[80:83]\n"
        "v_mul_f32 v93, v182, v93\n"
        "v_mul_f32 v94, v182, v94\n"
        "v_mfma_f32_16x16x32_bf16 v[84:87], v[132:135], v[100:103], 0\n"
        "v_mul_f32 v95, v182, v95\n"
        "v_cvt_pk_bf16_f32 v92, v92, v93\n"
        "v_mfma_f32_16x16x32_bf16 v[84:87], v[128:131], v[96:99], v[84:87]\n"
        "v_cvt_pk_bf16_f32 v93, v94, v95\n"
        "v_mul_f32 v68, v64, v68\n"
        "v_mfma_f32_16x16x32_bf16 v[76:79], v[136:139], v[96:99], 0\n"
        "v_rcp_f32 v80, v80\n"
        "v_mfma_f32_16x16x32_bf16 v[76:79], v[140:143], v[100:103], v[76:79]\n"
        "ds_write_b64 v246, v[92:93] offset:0\n"
        "v_mfma_f32_16x16x32_bf16 v[104:107], v[148:151], v[100:103], 0\n"
        "v_mul_f32 v69, v64, v69\n"
        "v_mul_f32 v70, v64, v70\n"
        "v_mfma_f32_16x16x32_bf16 v[104:107], v[144:147], v[96:99], v[104:107]\n"
        "v_mul_f32 v71, v64, v71\n"
        "v_cvt_pk_bf16_f32 v68, v68, v69\n"
        "v_mfma_f32_16x16x32_bf16 v[96:99], a[252:255], v[112:115], 0\n"
        "v_cvt_pk_bf16_f32 v69, v70, v71\n"
        "v_mul_f32 v72, v64, v72\n"
        "v_mfma_f32_16x16x32_bf16 v[96:99], a[252:255], v[116:119], v[96:99]\n"
        "v_mul_f32 v73, v64, v73\n"
        "v_mul_f32 v74, v64, v74\n"
        "v_mfma_f32_16x16x32_bf16 v[108:111], v[132:135], v[116:119], 0\n"
        "s_add_u32 m0, s41, 14336\n"
        "v_mul_f32 v75, v64, v75\n"
        "v_cvt_pk_bf16_f32 v70, v72, v73\n"
        "v_mfma_f32_16x16x32_bf16 v[108:111], v[128:131], v[112:115], v[108:111]\n"
        "v_mov_b32 v128, v212\n"
        "v_mov_b32 v129, v213\n"
        "v_mfma_f32_16x16x32_bf16 v[120:123], v[136:139], v[112:115], 0\n"
        "buffer_load_dwordx4 v244, s[8:11], s35 offen lds\n"
        "v_mfma_f32_16x16x32_bf16 v[120:123], v[140:143], v[116:119], v[120:123]\n"
        "ds_read_b128 a[236:239], v240 offset:13824\n"
        "v_mov_b32 v136, v214\n"
        "v_mfma_f32_16x16x32_bf16 v[124:127], v[148:151], v[116:119], 0\n"
        "ds_read_b128 a[240:243], v241 offset:13824\n"
        "v_mov_b32 v137, v215\n"
        "v_mfma_f32_16x16x32_bf16 v[124:127], v[144:147], v[112:115], v[124:127]\n"
        "ds_read_b128 a[244:247], v240 offset:13952\n"
        "v_mov_b32 v144, v216\n"
        "v_mfma_f32_16x16x32_bf16 v[100:103], a[248:251], a[144:147], 0\n"
        "v_mov_b32 v145, v217\n"
        "v_cvt_pk_bf16_f32 v71, v74, v75\n"
        "v_mfma_f32_16x16x32_bf16 v[100:103], a[216:219], a[148:151], v[100:103]\n"
        "ds_write_b128 v245, v[68:71] offset:256\n"
        "v_mfma_f32_16x16x32_bf16 v[100:103], a[220:223], a[152:155], v[100:103]\n"
        "v_mov_b32 v130, v218\n"
        "v_mov_b32 v131, v219\n"
        "v_mfma_f32_16x16x32_bf16 v[100:103], a[224:227], a[156:159], v[100:103]\n"
        "v_mov_b32 v138, v220\n"
        "v_mov_b32 v139, v221\n"
        "v_mfma_f32_16x16x32_bf16 v[100:103], a[228:231], a[160:163], v[100:103]\n"
        "v_mul_f32 v162, v64, v162\n"
        "v_mul_f32 v163, v64, v163\n"
        "v_mfma_f32_16x16x32_bf16 v[100:103], a[232:235], a[164:167], v[100:103]\n"
        "v_mul_f32 v164, v64, v164\n"
        "v_mul_f32 v165, v64, v165\n"
        "v_mfma_f32_16x16x32_bf16 v[112:115], a[232:235], a[188:191], 0\n"
        "v_mov_b32 v146, v222\n"
        "v_mov_b32 v147, v223\n"
        "v_mfma_f32_16x16x32_bf16 v[112:115], a[228:231], a[184:187], v[112:115]\n"
        "v_cvt_pk_bf16_f32 v162, v162, v163\n"
        "v_cvt_pk_bf16_f32 v163, v164, v165\n"
        "v_mfma_f32_16x16x32_bf16 v[112:115], a[224:227], a[180:183], v[112:115]\n"
        "ds_write_b64 v246, v[162:163] offset:256\n"
        "v_mfma_f32_16x16x32_bf16 v[112:115], a[220:223], a[176:179], v[112:115]\n"
        "v_cvt_pk_bf16_f32 v132, v100, v101\n"
        "v_cvt_pk_bf16_f32 v133, v102, v103\n"
        "v_mfma_f32_16x16x32_bf16 v[112:115], a[216:219], a[172:175], v[112:115]\n"
        "v_mul_f32 v84, v80, v84\n"
        "v_mul_f32 v85, v80, v85\n"
        "v_mfma_f32_16x16x32_bf16 v[112:115], a[248:251], a[168:171], v[112:115]\n"
        "v_rcp_f32 v96, v96\n"
        "v_mfma_f32_16x16x32_bf16 v[116:119], a[248:251], a[192:195], 0\n"
        "ds_read_b128 a[248:251], v241 offset:13952\n"
        "v_mul_f32 v86, v80, v86\n"
        "v_mfma_f32_16x16x32_bf16 v[116:119], a[216:219], a[196:199], v[116:119]\n"
        "ds_read_b128 a[216:219], v240 offset:14080\n"
        "v_mul_f32 v87, v80, v87\n"
        "v_mfma_f32_16x16x32_bf16 v[116:119], a[220:223], a[200:203], v[116:119]\n"
        "ds_read_b128 a[220:223], v241 offset:14080\n"
        "v_cvt_pk_bf16_f32 v140, v112, v113\n"
        "v_mfma_f32_16x16x32_bf16 v[116:119], a[224:227], a[204:207], v[116:119]\n"
        "v_cvt_pk_bf16_f32 v141, v114, v115\n"
        "v_cvt_pk_bf16_f32 v84, v84, v85\n"
        "v_mfma_f32_16x16x32_bf16 v[116:119], a[228:231], a[208:211], v[116:119]\n"
        "v_cvt_pk_bf16_f32 v85, v86, v87\n"
        "v_mul_f32 v76, v80, v76\n"
        "v_mfma_f32_16x16x32_bf16 v[116:119], a[232:235], a[212:215], v[116:119]\n"
        "s_waitcnt lgkmcnt(7)\n"
        "v_mul_f32 v77, v80, v77\n"
        "v_mul_f32 v78, v80, v78\n"
        "v_mfma_f32_16x16x32_bf16 v[100:103], a[236:239], a[144:147], 0\n"
        "s_waitcnt lgkmcnt(6)\n"
        "v_mul_f32 v79, v80, v79\n"
        "v_cvt_pk_bf16_f32 v86, v76, v77\n"
        "v_mfma_f32_16x16x32_bf16 v[100:103], a[240:243], a[148:151], v[100:103]\n"
        "s_waitcnt lgkmcnt(5)\n"
        "v_cvt_pk_bf16_f32 v87, v78, v79\n"
        "v_mul_f32 v104, v80, v104\n"
        "v_mfma_f32_16x16x32_bf16 v[100:103], a[244:247], a[152:155], v[100:103]\n"
        "s_waitcnt lgkmcnt(2)\n"
        "ds_write_b128 v245, v[84:87] offset:512\n"
        "v_mfma_f32_16x16x32_bf16 v[100:103], a[248:251], a[156:159], v[100:103]\n"
        "s_waitcnt lgkmcnt(2)\n"
        "v_cvt_pk_bf16_f32 v148, v116, v117\n"
        "v_cvt_pk_bf16_f32 v149, v118, v119\n"
        "v_mfma_f32_16x16x32_bf16 v[100:103], a[216:219], a[160:163], v[100:103]\n"
        "s_waitcnt lgkmcnt(1)\n"
        "v_mul_f32 v105, v80, v105\n"
        "v_mul_f32 v106, v80, v106\n"
        "v_mfma_f32_16x16x32_bf16 v[100:103], a[220:223], a[164:167], v[100:103]\n"
        "v_mul_f32 v107, v80, v107\n"
        "v_cvt_pk_bf16_f32 v104, v104, v105\n"
        "v_mfma_f32_16x16x32_bf16 v[112:115], a[220:223], a[188:191], 0\n"
        "v_cvt_pk_bf16_f32 v105, v106, v107\n"
        "v_mul_f32 v108, v96, v108\n"
        "v_mfma_f32_16x16x32_bf16 v[112:115], a[216:219], a[184:187], v[112:115]\n"
        "ds_write_b64 v246, v[104:105] offset:512\n"
        "v_mfma_f32_16x16x32_bf16 v[112:115], a[248:251], a[180:183], v[112:115]\n"
        "v_cvt_pk_bf16_f32 v134, v100, v101\n"
        "v_cvt_pk_bf16_f32 v135, v102, v103\n"
        "v_mfma_f32_16x16x32_bf16 v[112:115], a[244:247], a[176:179], v[112:115]\n"
        "v_mul_f32 v109, v96, v109\n"
        "v_mul_f32 v110, v96, v110\n"
        "v_mfma_f32_16x16x32_bf16 v[112:115], a[240:243], a[172:175], v[112:115]\n"
        "v_mul_f32 v111, v96, v111\n"
        "v_cvt_pk_bf16_f32 v108, v108, v109\n"
        "v_mfma_f32_16x16x32_bf16 v[112:115], a[236:239], a[168:171], v[112:115]\n"
        "v_cvt_pk_bf16_f32 v109, v110, v111\n"
        "v_mul_f32 v120, v96, v120\n"
        "v_mfma_f32_16x16x32_bf16 v[116:119], a[236:239], a[192:195], 0\n"
        "v_mul_f32 v121, v96, v121\n"
        "v_mul_f32 v122, v96, v122\n"
        "v_mfma_f32_16x16x32_bf16 v[116:119], a[240:243], a[196:199], v[116:119]\n"
        "v_mul_f32 v123, v96, v123\n"
        "v_cvt_pk_bf16_f32 v110, v120, v121\n"
        "v_mfma_f32_16x16x32_bf16 v[116:119], a[244:247], a[200:203], v[116:119]\n"
        "v_cvt_pk_bf16_f32 v142, v112, v113\n"
        "v_cvt_pk_bf16_f32 v143, v114, v115\n"
        "v_mfma_f32_16x16x32_bf16 v[116:119], a[248:251], a[204:207], v[116:119]\n"
        "v_cvt_pk_bf16_f32 v111, v122, v123\n"
        "v_mul_f32 v124, v96, v124\n"
        "v_mfma_f32_16x16x32_bf16 v[116:119], a[216:219], a[208:211], v[116:119]\n"
        "ds_write_b128 v245, v[108:111] offset:768\n"
        "v_mfma_f32_16x16x32_bf16 v[116:119], a[220:223], a[212:215], v[116:119]\n"
        "v_mul_f32 v125, v96, v125\n"
        "v_mul_f32 v126, v96, v126\n"
        "v_mfma_f32_16x16x32_bf16 v[100:103], a[20:23], a[220:223], 0\n"
        "v_mul_f32 v127, v96, v127\n"
        "v_cvt_pk_bf16_f32 v124, v124, v125\n"
        "v_mfma_f32_16x16x32_bf16 v[100:103], a[16:19], a[216:219], v[100:103]\n"
        "v_cvt_pk_bf16_f32 v125, v126, v127\n"
        "v_mfma_f32_16x16x32_bf16 v[100:103], a[12:15], a[248:251], v[100:103]\n"
        "v_cvt_pk_bf16_f32 v150, v116, v117\n"
        "v_cvt_pk_bf16_f32 v151, v118, v119\n"
        "v_mfma_f32_16x16x32_bf16 v[100:103], a[8:11], a[244:247], v[100:103]\n"
        "ds_write_b64 v246, v[124:125] offset:768\n"
        "v_mfma_f32_16x16x32_bf16 v[100:103], a[4:7], a[240:243], v[100:103]\n"
        "v_mfma_f32_16x16x32_bf16 v[100:103], a[0:3], a[236:239], v[100:103]\n"
        "v_mfma_f32_16x16x32_bf16 v[112:115], a[24:27], a[236:239], 0\n"
        "v_mfma_f32_16x16x32_bf16 v[112:115], a[28:31], a[240:243], v[112:115]\n"
        "v_mfma_f32_16x16x32_bf16 v[112:115], a[32:35], a[244:247], v[112:115]\n"
        "v_mfma_f32_16x16x32_bf16 v[112:115], a[36:39], a[248:251], v[112:115]\n"
        "v_mfma_f32_16x16x32_bf16 v[112:115], a[40:43], a[216:219], v[112:115]\n"
        "v_mfma_f32_16x16x32_bf16 v[112:115], a[44:47], a[220:223], v[112:115]\n"
        "v_mfma_f32_16x16x32_bf16 v[116:119], a[68:71], a[220:223], 0\n"
        "ds_read_b128 v[162:165], v251 offset:0\n"
        "v_cvt_pk_bf16_f32 v216, v100, v101\n"
        "v_mfma_f32_16x16x32_bf16 v[116:119], a[64:67], a[216:219], v[116:119]\n"
        "v_cvt_pk_bf16_f32 v217, v102, v103\n"
        "v_mfma_f32_16x16x32_bf16 v[116:119], a[60:63], a[248:251], v[116:119]\n"
        "v_mfma_f32_16x16x32_bf16 v[116:119], a[56:59], a[244:247], v[116:119]\n"
        "v_mfma_f32_16x16x32_bf16 v[116:119], a[52:55], a[240:243], v[116:119]\n"
        "ds_read_b128 v[182:185], v251 offset:512\n"
        "v_cvt_pk_bf16_f32 v218, v112, v113\n"
        "v_mfma_f32_16x16x32_bf16 v[116:119], a[48:51], a[236:239], v[116:119]\n"
        "v_cvt_pk_bf16_f32 v219, v114, v115\n"
        "v_mfma_f32_16x16x32_bf16 v[100:103], a[72:75], a[236:239], 0\n"
        "s_add_u32 s47, s47, 6291456\n"
        "v_mfma_f32_16x16x32_bf16 v[100:103], a[76:79], a[240:243], v[100:103]\n"
        "s_add_u32 s36, s36, 2097152\n"
        "v_mfma_f32_16x16x32_bf16 v[100:103], a[80:83], a[244:247], v[100:103]\n"
        "ds_read_b128 v[202:205], v251 offset:4096\n"
        "s_xor_b32 s48, s48, 8192\n"
        "v_cvt_pk_bf16_f32 v220, v116, v117\n"
        "v_mfma_f32_16x16x32_bf16 v[100:103], a[84:87], a[248:251], v[100:103]\n"
        "v_cvt_pk_bf16_f32 v221, v118, v119\n"
        "v_cvt_pk_bf16_f32 v226, v116, v117\n"
        "v_mfma_f32_16x16x32_bf16 v[100:103], a[88:91], a[216:219], v[100:103]\n"
        "v_cvt_pk_bf16_f32 v227, v118, v119\n"
        "v_mfma_f32_16x16x32_bf16 v[100:103], a[92:95], a[220:223], v[100:103]\n"
        "v_mfma_f32_16x16x32_bf16 v[112:115], a[116:119], a[220:223], 0\n"
        "ds_read_b128 v[222:225], v251 offset:4608\n"
        "v_mfma_f32_16x16x32_bf16 v[112:115], a[112:115], a[216:219], v[112:115]\n"
        "v_mfma_f32_16x16x32_bf16 v[112:115], a[108:111], a[248:251], v[112:115]\n"
        "s_waitcnt lgkmcnt(3)\n"
        "v_pk_min_u16 v162, v162, s26\n"
        "v_pk_min_u16 v163, v163, s26\n"
        "v_mfma_f32_16x16x32_bf16 v[112:115], a[104:107], a[244:247], v[112:115]\n"
        "v_pk_min_u16 v164, v164, s26\n"
        "v_cvt_pk_bf16_f32 v212, v100, v101\n"
        "v_mfma_f32_16x16x32_bf16 v[112:115], a[100:103], a[240:243], v[112:115]\n"
        "v_cvt_pk_bf16_f32 v213, v102, v103\n"
        "v_pk_min_u16 v165, v165, s26\n"
        "v_mfma_f32_16x16x32_bf16 v[112:115], a[96:99], a[236:239], v[112:115]\n"
        "v_add_u32 v240, s31, v240\n"
        "v_add_u32 v241, s31, v241\n"
        "v_mfma_f32_16x16x32_bf16 v[116:119], a[120:123], a[236:239], 0\n"
        "s_waitcnt lgkmcnt(2)\n"
        "v_pk_min_u16 v182, v182, s26\n"
        "v_pk_min_u16 v183, v183, s26\n"
        "v_mfma_f32_16x16x32_bf16 v[116:119], a[124:127], a[240:243], v[116:119]\n"
        "v_pk_min_u16 v184, v184, s26\n"
        "v_pk_min_u16 v185, v185, s26\n"
        "v_mfma_f32_16x16x32_bf16 v[116:119], a[128:131], a[244:247], v[116:119]\n"
        "v_cvt_pk_bf16_f32 v214, v112, v113\n"
        "v_cvt_pk_bf16_f32 v215, v114, v115\n"
        "v_mfma_f32_16x16x32_bf16 v[116:119], a[132:135], a[248:251], v[116:119]\n"
        "v_mfma_f32_16x16x32_bf16 v[116:119], a[136:139], a[216:219], v[116:119]\n"
        "s_waitcnt lgkmcnt(1)\n"
        "v_pk_min_u16 v202, v202, s26\n"
        "v_pk_min_u16 v203, v203, s26\n"
        "v_mfma_f32_16x16x32_bf16 v[116:119], a[140:143], a[220:223], v[116:119]\n"
        "v_pk_min_u16 v204, v204, s26\n"
        "v_pk_min_u16 v205, v205, s26\n"
        "v_mfma_f32_16x16x32_bf16 v[64:67], v[152:155], v[156:159], v[0:3]\n"
        "v_mfma_f32_16x16x32_bf16 v[64:67], v[168:171], v[160:163], v[64:67]\n"
        "v_mfma_f32_16x16x32_bf16 v[68:71], v[172:175], v[156:159], v[4:7]\n"
        "s_waitcnt lgkmcnt(0)\n"
        "v_pk_min_u16 v222, v222, s26\n"
        "v_pk_min_u16 v223, v223, s26\n"
        "v_mfma_f32_16x16x32_bf16 v[68:71], v[188:191], v[164:167], v[68:71]\n"
        "ds_read_b128 v[162:165], v252 offset:0\n"
        "v_cvt_pk_bf16_f32 v230, v116, v117\n"
        "v_mfma_f32_16x16x32_bf16 v[80:83], v[152:155], v[176:179], v[16:19]\n"
        "v_cvt_pk_bf16_f32 v231, v118, v119\n"
        "v_pk_min_u16 v224, v224, s26\n"
        "v_mfma_f32_16x16x32_bf16 v[80:83], v[168:171], v[180:183], v[80:83]\n"
        "v_pk_min_u16 v225, v225, s26\n"
        "v_max3_f32 v232, v64, v65, v66\n"
        "v_mfma_f32_16x16x32_bf16 v[84:87], v[172:175], v[176:179], v[20:23]\n"
        "v_max3_f32 v232, v232, v67, v68\n"
        "v_max3_f32 v232, v232, v69, v70\n"
        "v_mfma_f32_16x16x32_bf16 v[84:87], v[188:191], v[184:187], v[84:87]\n"
        "ds_read_b128 v[182:185], v252 offset:512\n"
        "v_mfma_f32_16x16x32_bf16 v[96:99], v[152:155], v[196:199], v[32:35]\n"
        "v_max3_f32 v233, v80, v81, v82\n"
        "v_mfma_f32_16x16x32_bf16 v[96:99], v[168:171], v[200:203], v[96:99]\n"
        "v_mfma_f32_16x16x32_bf16 v[100:103], v[172:175], v[196:199], v[36:39]\n"
        "v_mfma_f32_16x16x32_bf16 v[100:103], v[188:191], v[204:207], v[100:103]\n"
        "ds_read_b128 v[202:205], v252 offset:4096\n"
        "v_mfma_f32_16x16x32_bf16 v[112:115], v[152:155], v[216:219], v[48:51]\n"
        "s_waitcnt lgkmcnt(2)\n"
        "v_pk_min_u16 v162, v162, s26\n"
        "v_pk_min_u16 v163, v163, s26\n"
        "v_mfma_f32_16x16x32_bf16 v[112:115], v[168:171], v[220:223], v[112:115]\n"
        "v_pk_min_u16 v164, v164, s26\n"
        "v_pk_min_u16 v165, v165, s26\n"
        "v_mfma_f32_16x16x32_bf16 v[116:119], v[172:175], v[216:219], v[52:55]\n"
        "v_max3_f32 v233, v233, v83, v84\n"
        "v_max3_f32 v233, v233, v85, v86\n"
        "v_mfma_f32_16x16x32_bf16 v[116:119], v[188:191], v[224:227], v[116:119]\n"
        "ds_read_b128 v[222:225], v252 offset:4608\n"
        "v_max3_f32 v234, v96, v97, v98\n"
        "v_mfma_f32_16x16x32_bf16 v[72:75], v[192:195], v[156:159], v[8:11]\n"
        "s_waitcnt lgkmcnt(2)\n"
        "v_pk_min_u16 v182, v182, s26\n"
        "v_pk_min_u16 v183, v183, s26\n"
        "v_mfma_f32_16x16x32_bf16 v[72:75], v[208:211], v[160:163], v[72:75]\n"
        "v_pk_min_u16 v184, v184, s26\n"
        "v_pk_min_u16 v185, v185, s26\n"
        "v_mfma_f32_16x16x32_bf16 v[76:79], v[212:215], v[156:159], v[12:15]\n"
        "v_max3_f32 v234, v234, v99, v100\n"
        "v_max3_f32 v234, v234, v101, v102\n"
        "v_mfma_f32_16x16x32_bf16 v[76:79], v[228:231], v[164:167], v[76:79]\n"
        "v_max3_f32 v235, v112, v113, v114\n"
        "v_max3_f32 v235, v235, v115, v116\n"
        "v_mfma_f32_16x16x32_bf16 v[88:91], v[192:195], v[176:179], v[24:27]\n"
        "s_barrier\n"
        "ds_read_b128 v[152:155], v247 offset:0\n"
        "s_waitcnt lgkmcnt(2)\n"
        "v_pk_min_u16 v202, v202, s26\n"
        "v_mfma_f32_16x16x32_bf16 v[88:91], v[208:211], v[180:183], v[88:91]\n"
        "ds_read_b128 v[160:163], v247 offset:4096\n"
        "v_pk_min_u16 v203, v203, s26\n"
        "v_mfma_f32_16x16x32_bf16 v[92:95], v[212:215], v[176:179], v[28:31]\n"
        "ds_read_b128 v[156:159], v247 offset:256\n"
        "v_pk_min_u16 v204, v204, s26\n"
        "v_mfma_f32_16x16x32_bf16 v[92:95], v[228:231], v[184:187], v[92:95]\n"
        "ds_read_b128 v[164:167], v247 offset:4352\n"
        "v_pk_min_u16 v205, v205, s26\n"
        "v_mfma_f32_16x16x32_bf16 v[104:107], v[192:195], v[196:199], v[40:43]\n"
        "ds_read_b128 a[216:219], v248 offset:0\n"
        "s_waitcnt lgkmcnt(5)\n"
        "v_pk_min_u16 v222, v222, s26\n"
        "v_mfma_f32_16x16x32_bf16 v[104:107], v[208:211], v[200:203], v[104:107]\n"
        "ds_read_b128 a[220:223], v248 offset:6144\n"
        "v_pk_min_u16 v223, v223, s26\n"
        "v_mfma_f32_16x16x32_bf16 v[108:111], v[212:215], v[196:199], v[44:47]\n"
        "ds_read_b128 a[224:227], v254 offset:16\n"
        "v_pk_min_u16 v224, v224, s26\n"
        "v_mfma_f32_16x16x32_bf16 v[108:111], v[228:231], v[204:207], v[108:111]\n"
        "ds_read_b128 a[228:231], v254 offset:6160\n"
        "v_pk_min_u16 v225, v225, s26\n"
        "v_mfma_f32_16x16x32_bf16 v[120:123], v[192:195], v[216:219], v[56:59]\n"
        "ds_read_b128 a[232:235], v248 offset:32\n"
        "v_max3_f32 v232, v232, v71, v72\n"
        "v_mfma_f32_16x16x32_bf16 v[120:123], v[208:211], v[220:223], v[120:123]\n"
        "v_max3_f32 v232, v232, v73, v74\n"
        "v_max3_f32 v232, v232, v75, v76\n"
        "v_mfma_f32_16x16x32_bf16 v[124:127], v[212:215], v[216:219], v[60:63]\n"
        "v_max3_f32 v232, v232, v77, v78\n"
        "v_max_f32 v232, v232, v79\n"
        "v_mfma_f32_16x16x32_bf16 v[124:127], v[228:231], v[224:227], v[124:127]\n"
        "s_waitcnt lgkmcnt(4)\n"
        "v_max3_f32 v233, v233, v87, v88\n"
        "v_max3_f32 v233, v233, v89, v90\n"
        "v_mfma_f32_16x16x32_bf16 v[172:175], v[152:155], a[216:219], 0\n"
        "s_waitcnt lgkmcnt(3)\n"
        "v_max3_f32 v233, v233, v91, v92\n"
        "v_max3_f32 v233, v233, v93, v94\n"
        "v_mfma_f32_16x16x32_bf16 v[172:175], v[160:163], a[220:223], v[172:175]\n"
        "v_max_f32 v233, v233, v95\n"
        "v_max3_f32 v234, v234, v103, v104\n"
        "v_mfma_f32_16x16x32_bf16 v[180:183], v[164:167], a[220:223], 0\n"
        "ds_read_b128 a[220:223], v248 offset:6176\n"
        "v_max3_f32 v234, v234, v105, v106\n"
        "v_mfma_f32_16x16x32_bf16 v[180:183], v[156:159], a[216:219], v[180:183]\n"
        "s_waitcnt lgkmcnt(3)\n"
        "ds_read_b128 a[216:219], v254 offset:48\n"
        "v_max3_f32 v234, v234, v107, v108\n"
        "v_mfma_f32_16x16x32_bf16 v[184:187], v[156:159], a[224:227], 0\n"
        "s_waitcnt lgkmcnt(3)\n"
        "ds_read_b128 a[236:239], v247 offset:8192\n"
        "v_max3_f32 v234, v234, v109, v110\n"
        "v_mfma_f32_16x16x32_bf16 v[184:187], v[164:167], a[228:231], v[184:187]\n"
        "ds_read_b128 a[244:247], v247 offset:12288\n"
        "v_max_f32 v234, v234, v111\n"
        "v_mfma_f32_16x16x32_bf16 v[176:179], v[160:163], a[228:231], 0\n"
        "ds_read_b128 a[228:231], v254 offset:6192\n"
        "v_max3_f32 v235, v235, v117, v118\n"
        "v_mfma_f32_16x16x32_bf16 v[176:179], v[152:155], a[224:227], v[176:179]\n"
        "s_waitcnt lgkmcnt(5)\n"
        "ds_read_b128 a[224:227], v248 offset:64\n"
        "v_max3_f32 v235, v235, v119, v120\n"
        "v_mfma_f32_16x16x32_bf16 v[212:215], v[152:155], a[232:235], 0\n"
        "s_waitcnt lgkmcnt(5)\n"
        "ds_read_b128 a[240:243], v247 offset:8448\n"
        "v_max3_f32 v235, v235, v121, v122\n"
        "v_mfma_f32_16x16x32_bf16 v[212:215], v[160:163], a[220:223], v[212:215]\n"
        "ds_read_b128 a[248:251], v247 offset:12544\n"
        "v_max3_f32 v235, v235, v123, v124\n"
        "v_mfma_f32_16x16x32_bf16 v[192:195], v[164:167], a[220:223], 0\n"
        "ds_read_b128 a[220:223], v248 offset:6208\n"
        "v_max3_f32 v235, v235, v125, v126\n"
        "v_mfma_f32_16x16x32_bf16 v[192:195], v[156:159], a[232:235], v[192:195]\n"
        "s_waitcnt lgkmcnt(7)\n"
        "ds_read_b128 a[232:235], v254 offset:80\n"
        "v_max_f32 v235, v235, v127\n"
        "v_mfma_f32_16x16x32_bf16 v[196:199], v[156:159], a[216:219], 0\n"
        "s_waitcnt lgkmcnt(5)\n"
        "v_permlane32_swap_b32 v232, v233\n"
        "v_mfma_f32_16x16x32_bf16 v[196:199], v[164:167], a[228:231], v[196:199]\n"
        "v_max_f32 v232, v232, v233\n"
        "v_mov_b32 v233, v232\n"
        "v_mfma_f32_16x16x32_bf16 v[216:219], v[160:163], a[228:231], 0\n"
        "ds_read_b128 a[228:231], v254 offset:6224\n"
        "v_xor_b32 v251, 0x2000, v251\n"
        "v_mfma_f32_16x16x32_bf16 v[216:219], v[152:155], a[216:219], v[216:219]\n"
        "s_waitcnt lgkmcnt(5)\n"
        "ds_read_b128 a[216:219], v248 offset:36864\n"
        "v_xor_b32 v252, 0x2000, v252\n"
        "v_mfma_f32_16x16x32_bf16 v[220:223], v[152:155], a[224:227], 0\n"
        "s_waitcnt lgkmcnt(3)\n"
        "v_permlane16_swap_b32 v232, v233\n"
        "v_mfma_f32_16x16x32_bf16 v[220:223], v[160:163], a[220:223], v[220:223]\n"
        "v_max_f32 v232, v232, v233\n"
        "v_mul_f32 v236, s25, v232\n"
        "v_mfma_f32_16x16x32_bf16 v[200:203], v[164:167], a[220:223], 0\n"
        "ds_read_b128 a[220:223], v248 offset:43008\n"
        "v_mul_f32 v237, s25, v232\n"
        "v_mfma_f32_16x16x32_bf16 v[200:203], v[156:159], a[224:227], v[200:203]\n"
        "s_waitcnt lgkmcnt(3)\n"
        "ds_read_b128 a[224:227], v254 offset:36880\n"
        "v_add_u32 v245, s45, v245\n"
        "v_mfma_f32_16x16x32_bf16 v[204:207], v[156:159], a[232:235], 0\n"
        "s_waitcnt lgkmcnt(3)\n"
        "v_permlane32_swap_b32 v236, v237\n"
        "v_mfma_f32_16x16x32_bf16 v[204:207], v[164:167], a[228:231], v[204:207]\n"
        "v_permlane32_swap_b32 v234, v235\n"
        "v_mfma_f32_16x16x32_bf16 v[224:227], v[160:163], a[228:231], 0\n"
        "ds_read_b128 a[228:231], v254 offset:43024\n"
        "v_max_f32 v234, v234, v235\n"
        "v_mfma_f32_16x16x32_bf16 v[224:227], v[152:155], a[232:235], v[224:227]\n"
        "s_waitcnt lgkmcnt(3)\n"
        "ds_read_b128 a[232:235], v248 offset:36896\n"
        "v_mov_b32 v235, v234\n"
        "v_mfma_f32_16x16x32_bf16 v[172:175], a[236:239], a[216:219], v[172:175]\n"
        "s_waitcnt lgkmcnt(3)\n"
        "v_fma_f32 v64, v64, s24, v236\n"
        "v_fma_f32 v65, v65, s24, v236\n"
        "v_mfma_f32_16x16x32_bf16 v[172:175], a[244:247], a[220:223], v[172:175]\n"
        "v_permlane16_swap_b32 v234, v235\n"
        "v_mfma_f32_16x16x32_bf16 v[180:183], a[248:251], a[220:223], v[180:183]\n"
        "ds_read_b128 a[220:223], v248 offset:43040\n"
        "v_max_f32 v234, v234, v235\n"
        "v_mfma_f32_16x16x32_bf16 v[180:183], a[240:243], a[216:219], v[180:183]\n"
        "s_waitcnt lgkmcnt(3)\n"
        "ds_read_b128 a[216:219], v254 offset:36912\n"
        "v_mul_f32 v238, s25, v234\n"
        "v_mfma_f32_16x16x32_bf16 v[184:187], a[240:243], a[224:227], v[184:187]\n"
        "s_waitcnt lgkmcnt(3)\n"
        "ds_read_b128 v[152:155], v247 offset:16384\n"
        "v_mul_f32 v239, s25, v234\n"
        "v_mfma_f32_16x16x32_bf16 v[184:187], a[248:251], a[228:231], v[184:187]\n"
        "ds_read_b128 v[160:163], v247 offset:20480\n"
        "v_fma_f32 v66, v66, s24, v236\n"
        "v_mfma_f32_16x16x32_bf16 v[176:179], a[244:247], a[228:231], v[176:179]\n"
        "ds_read_b128 a[228:231], v254 offset:43056\n"
        "v_fma_f32 v67, v67, s24, v236\n"
        "v_mfma_f32_16x16x32_bf16 v[176:179], a[236:239], a[224:227], v[176:179]\n"
        "s_waitcnt lgkmcnt(5)\n"
        "ds_read_b128 a[224:227], v248 offset:36928\n"
        "v_fma_f32 v68, v68, s24, v236\n"
        "v_mfma_f32_16x16x32_bf16 v[212:215], a[236:239], a[232:235], v[212:215]\n"
        "s_waitcnt lgkmcnt(5)\n"
        "ds_read_b128 v[156:159], v247 offset:16640\n"
        "v_fma_f32 v69, v69, s24, v236\n"
        "v_mfma_f32_16x16x32_bf16 v[212:215], a[244:247], a[220:223], v[212:215]\n"
        "ds_read_b128 v[164:167], v247 offset:20736\n"
        "v_fma_f32 v70, v70, s24, v236\n"
        "v_mfma_f32_16x16x32_bf16 v[192:195], a[248:251], a[220:223], v[192:195]\n"
        "ds_read_b128 a[220:223], v248 offset:43072\n"
        "v_fma_f32 v71, v71, s24, v236\n"
        "v_mfma_f32_16x16x32_bf16 v[192:195], a[240:243], a[232:235], v[192:195]\n"
        "s_waitcnt lgkmcnt(7)\n"
        "ds_read_b128 a[232:235], v254 offset:36944\n"
        "v_fma_f32 v72, v72, s24, v236\n"
        "v_mfma_f32_16x16x32_bf16 v[196:199], a[240:243], a[216:219], v[196:199]\n"
        "s_waitcnt lgkmcnt(5)\n"
        "v_permlane32_swap_b32 v238, v239\n"
        "v_mfma_f32_16x16x32_bf16 v[196:199], a[248:251], a[228:231], v[196:199]\n"
        "v_exp_f32 v64, v64\n"
        "v_mfma_f32_16x16x32_bf16 v[216:219], a[244:247], a[228:231], v[216:219]\n"
        "ds_read_b128 a[228:231], v254 offset:43088\n"
        "v_fma_f32 v73, v73, s24, v236\n"
        "v_mfma_f32_16x16x32_bf16 v[216:219], a[236:239], a[216:219], v[216:219]\n"
        "s_waitcnt lgkmcnt(5)\n"
        "ds_read_b128 a[216:219], v248 offset:12288\n"
        "v_fma_f32 v74, v74, s24, v236\n"
        "v_mfma_f32_16x16x32_bf16 v[220:223], a[236:239], a[224:227], v[220:223]\n"
        "s_waitcnt lgkmcnt(3)\n"
        "v_exp_f32 v65, v65\n"
        "v_mfma_f32_16x16x32_bf16 v[220:223], a[244:247], a[220:223], v[220:223]\n"
        "v_exp_f32 v66, v66\n"
        "v_mfma_f32_16x16x32_bf16 v[200:203], a[248:251], a[220:223], v[200:203]\n"
        "ds_read_b128 a[220:223], v248 offset:49152\n"
        "v_cvt_pk_bf16_f32 v64, v64, v65\n"
        "v_mfma_f32_16x16x32_bf16 v[200:203], a[240:243], a[224:227], v[200:203]\n"
        "s_waitcnt lgkmcnt(3)\n"
        "ds_read_b128 a[224:227], v254 offset:12304\n"
        "v_fma_f32 v75, v75, s24, v236\n"
        "v_mfma_f32_16x16x32_bf16 v[204:207], a[240:243], a[232:235], v[204:207]\n"
        "s_waitcnt lgkmcnt(3)\n"
        "v_exp_f32 v67, v67\n"
        "v_mfma_f32_16x16x32_bf16 v[204:207], a[248:251], a[228:231], v[204:207]\n"
        "v_exp_f32 v68, v68\n"
        "v_mfma_f32_16x16x32_bf16 v[224:227], a[244:247], a[228:231], v[224:227]\n"
        "ds_read_b128 a[228:231], v254 offset:49168\n"
        "v_cvt_pk_bf16_f32 v65, v66, v67\n"
        "v_mfma_f32_16x16x32_bf16 v[224:227], a[236:239], a[232:235], v[224:227]\n"
        "s_waitcnt lgkmcnt(3)\n"
        "ds_read_b128 a[232:235], v248 offset:12320\n"
        "v_add_u32 v246, s45, v246\n"
        "v_mfma_f32_16x16x32_bf16 v[172:175], v[152:155], a[216:219], v[172:175]\n"
        "s_waitcnt lgkmcnt(3)\n"
        "v_exp_f32 v69, v69\n"
        "v_mfma_f32_16x16x32_bf16 v[172:175], v[160:163], a[220:223], v[172:175]\n"
        "v_cvt_pk_bf16_f32 v66, v68, v69\n"
        "v_add_u32 v247, s45, v247\n"
        "v_mfma_f32_16x16x32_bf16 v[180:183], v[164:167], a[220:223], v[180:183]\n"
        "ds_read_b128 a[220:223], v248 offset:49184\n"
        "v_mfma_f32_16x16x32_bf16 v[180:183], v[156:159], a[216:219], v[180:183]\n"
        "s_waitcnt lgkmcnt(3)\n"
        "ds_read_b128 a[216:219], v254 offset:12336\n"
        "v_mfma_f32_16x16x32_bf16 v[184:187], v[156:159], a[224:227], v[184:187]\n"
        "s_waitcnt lgkmcnt(3)\n"
        "v_exp_f32 v70, v70\n"
        "v_mfma_f32_16x16x32_bf16 v[184:187], v[164:167], a[228:231], v[184:187]\n"
        "v_exp_f32 v71, v71\n"
        "v_mfma_f32_16x16x32_bf16 v[176:179], v[160:163], a[228:231], v[176:179]\n"
        "ds_read_b128 a[228:231], v254 offset:49200\n"
        "v_cvt_pk_bf16_f32 v67, v70, v71\n"
        "v_mfma_f32_16x16x32_bf16 v[176:179], v[152:155], a[224:227], v[176:179]\n"
        "s_waitcnt lgkmcnt(3)\n"
        "ds_read_b128 a[224:227], v248 offset:12352\n"
        "v_mfma_f32_16x16x32_bf16 v[212:215], v[152:155], a[232:235], v[212:215]\n"
        "s_waitcnt lgkmcnt(3)\n"
        "s_waitcnt vmcnt(0)\n"
        "s_barrier\n"
        "v_exp_f32 v72, v72\n"
        "v_mfma_f32_16x16x32_bf16 v[212:215], v[160:163], a[220:223], v[212:215]\n"
        "v_exp_f32 v73, v73\n"
        "v_mfma_f32_16x16x32_bf16 v[192:195], v[164:167], a[220:223], v[192:195]\n"
        "ds_read_b128 a[220:223], v248 offset:49216\n"
        "v_cvt_pk_bf16_f32 v68, v72, v73\n"
        "v_mfma_f32_16x16x32_bf16 v[192:195], v[156:159], a[232:235], v[192:195]\n"
        "s_waitcnt lgkmcnt(3)\n"
        "ds_read_b128 a[232:235], v254 offset:12368\n"
        "v_mfma_f32_16x16x32_bf16 v[196:199], v[156:159], a[216:219], v[196:199]\n"
        "s_waitcnt lgkmcnt(3)\n"
        "ds_read_b128 a[236:239], v240 offset:0\n"
        "v_mfma_f32_16x16x32_bf16 v[196:199], v[164:167], a[228:231], v[196:199]\n"
        "ds_read_b128 a[240:243], v241 offset:0\n"
        "v_mfma_f32_16x16x32_bf16 v[216:219], v[160:163], a[228:231], v[216:219]\n"
        "ds_read_b128 a[228:231], v254 offset:49232\n"
        "v_mfma_f32_16x16x32_bf16 v[216:219], v[152:155], a[216:219], v[216:219]\n"
        "s_waitcnt lgkmcnt(5)\n"
        "ds_read_b128 a[244:247], v240 offset:128\n"
        "v_mfma_f32_16x16x32_bf16 v[220:223], v[152:155], a[224:227], v[220:223]\n"
        "s_waitcnt lgkmcnt(5)\n"
        "ds_read_b128 a[248:251], v241 offset:128\n"
        "v_mfma_f32_16x16x32_bf16 v[220:223], v[160:163], a[220:223], v[220:223]\n"
        "ds_read_b128 a[216:219], v240 offset:256\n"
        "v_mfma_f32_16x16x32_bf16 v[200:203], v[164:167], a[220:223], v[200:203]\n"
        "ds_read_b128 a[220:223], v241 offset:256\n"
        "v_mfma_f32_16x16x32_bf16 v[200:203], v[156:159], a[224:227], v[200:203]\n"
        "s_waitcnt lgkmcnt(7)\n"
        "v_exp_f32 v74, v74\n"
        "v_mfma_f32_16x16x32_bf16 v[204:207], v[156:159], a[232:235], v[204:207]\n"
        "s_waitcnt lgkmcnt(4)\n"
        "v_exp_f32 v75, v75\n"
        "v_mfma_f32_16x16x32_bf16 v[204:207], v[164:167], a[228:231], v[204:207]\n"
        "s_sub_u32 s27, s27, 1\n"
        "v_cvt_pk_bf16_f32 v69, v74, v75\n"
        "v_mfma_f32_16x16x32_bf16 v[224:227], v[160:163], a[228:231], v[224:227]\n"
        "s_cmp_lg_u32 s27, 0\n"
        "v_mfma_f32_16x16x32_bf16 v[224:227], v[152:155], a[232:235], v[224:227]\n"
        "s_cbranch_scc1 10b\n"
        "20:\n"
        "s_waitcnt lgkmcnt(0)\n"
        "s_nop 7\n"
        "v_mfma_f32_16x16x32_bf16 v[48:51], a[252:255], v[64:67], 0\n"
        "s_and_b32 s46, s42, 1\n"
        "s_lshl_b32 s31, s46, 8\n"
        "s_mul_i32 s45, s46, 786432\n"
        "s_sub_u32 s37, s37, s45\n"
        "s_cmp_eq_u32 s46, 0\n"
        "s_cselect_b32 s43, s43, s44\n"
        "s_add_u32 s28, s37, 786432\n"
        "s_cmp_eq_u32 s28, 100663296\n"
        "s_cselect_b32 s28, 0, s28\n"
        "v_fma_f32 v76, v76, s24, v236\n"
        "v_exp_f32 v76, v76\n"
        "v_fma_f32 v77, v77, s24, v236\n"
        "v_exp_f32 v77, v77\n"
        "v_fma_f32 v78, v78, s24, v236\n"
        "v_cvt_pk_bf16_f32 v70, v76, v77\n"
        "v_exp_f32 v78, v78\n"
        "v_fma_f32 v79, v79, s24, v236\n"
        "v_exp_f32 v79, v79\n"
        "v_subrev_u32 v247, s31, v247\n"
        "v_cvt_pk_bf16_f32 v71, v78, v79\n"
        "s_nop 1\n"
        "v_mfma_f32_16x16x32_bf16 v[48:51], a[252:255], v[68:71], v[48:51]\n"
        "v_cvt_pk_bf16_f32 v232, v172, v176\n"
        "v_cvt_pk_bf16_f32 v233, v212, v216\n"
        "v_cvt_pk_bf16_f32 v234, v220, v224\n"
        "v_fma_f32 v80, v80, s24, v237\n"
        "v_exp_f32 v80, v80\n"
        "v_mfma_f32_16x16x32_bf16 v[52:55], v[132:135], v[68:71], 0\n"
        "buffer_store_dwordx3 v[232:234], v250, s[16:19], s38 offen offset:0 sc1\n"
        "ds_read_b128 a[216:219], v248 offset:0\n"
        "v_mfma_f32_16x16x32_bf16 v[52:55], v[128:131], v[64:67], v[52:55]\n"
        "ds_read_b128 a[220:223], v248 offset:6144\n"
        "v_cvt_pk_bf16_f32 v232, v173, v177\n"
        "v_cvt_pk_bf16_f32 v233, v213, v217\n"
        "v_cvt_pk_bf16_f32 v234, v221, v225\n"
        "v_fma_f32 v81, v81, s24, v237\n"
        "v_fma_f32 v82, v82, s24, v237\n"
        "v_mfma_f32_16x16x32_bf16 v[56:59], v[136:139], v[64:67], 0\n"
        "buffer_store_dwordx3 v[232:234], v250, s[16:19], s38 offen offset:384 sc1\n"
        "ds_read_b128 a[224:227], v248 offset:36864\n"
        "v_mfma_f32_16x16x32_bf16 v[56:59], v[140:143], v[68:71], v[56:59]\n"
        "ds_read_b128 a[228:231], v248 offset:43008\n"
        "v_cvt_pk_bf16_f32 v232, v174, v178\n"
        "v_cvt_pk_bf16_f32 v233, v214, v218\n"
        "v_cvt_pk_bf16_f32 v234, v222, v226\n"
        "v_exp_f32 v81, v81\n"
        "v_mfma_f32_16x16x32_bf16 v[60:63], v[148:151], v[68:71], 0\n"
        "buffer_store_dwordx3 v[232:234], v250, s[16:19], s38 offen offset:768 sc1\n"
        "ds_read_b128 a[232:235], v248 offset:12288\n"
        "v_mfma_f32_16x16x32_bf16 v[60:63], v[144:147], v[64:67], v[60:63]\n"
        "ds_read_b128 a[236:239], v248 offset:49152\n"
        "v_cvt_pk_bf16_f32 v232, v175, v179\n"
        "v_cvt_pk_bf16_f32 v233, v215, v219\n"
        "v_cvt_pk_bf16_f32 v234, v223, v227\n"
        "v_cvt_pk_bf16_f32 v80, v80, v81\n"
        "v_fma_f32 v83, v83, s24, v237\n"
        "v_exp_f32 v82, v82\n"
        "v_exp_f32 v83, v83\n"
        "v_fma_f32 v84, v84, s24, v237\n"
        "v_cvt_pk_bf16_f32 v81, v82, v83\n"
        "v_exp_f32 v84, v84\n"
        "v_fma_f32 v85, v85, s24, v237\n"
        "v_exp_f32 v85, v85\n"
        "v_fma_f32 v86, v86, s24, v237\n"
        "v_cvt_pk_bf16_f32 v82, v84, v85\n"
        "v_exp_f32 v86, v86\n"
        "v_fma_f32 v87, v87, s24, v237\n"
        "v_exp_f32 v87, v87\n"
        "v_fma_f32 v88, v88, s24, v237\n"
        "v_cvt_pk_bf16_f32 v83, v86, v87\n"
        "s_nop 1\n"
        "v_mfma_f32_16x16x32_bf16 v[168:171], a[252:255], v[80:83], 0\n"
        "buffer_store_dwordx3 v[232:234], v250, s[16:19], s38 offen offset:1152 sc1\n"
        "ds_read_b128 a[240:243], v254 offset:16\n"
        "v_exp_f32 v88, v88\n"
        "v_fma_f32 v89, v89, s24, v237\n"
        "v_exp_f32 v89, v89\n"
        "v_fma_f32 v90, v90, s24, v237\n"
        "v_cvt_pk_bf16_f32 v84, v88, v89\n"
        "v_exp_f32 v90, v90\n"
        "v_fma_f32 v91, v91, s24, v237\n"
        "v_exp_f32 v91, v91\n"
        "v_fma_f32 v92, v92, s24, v237\n"
        "v_cvt_pk_bf16_f32 v85, v90, v91\n"
        "v_exp_f32 v92, v92\n"
        "v_fma_f32 v93, v93, s24, v237\n"
        "v_exp_f32 v93, v93\n"
        "v_fma_f32 v94, v94, s24, v237\n"
        "v_cvt_pk_bf16_f32 v86, v92, v93\n"
        "v_exp_f32 v94, v94\n"
        "v_fma_f32 v95, v95, s24, v237\n"
        "v_exp_f32 v95, v95\n"
        "v_cvt_pk_bf16_f32 v232, v180, v184\n"
        "v_cvt_pk_bf16_f32 v87, v94, v95\n"
        "s_nop 1\n"
        "v_mfma_f32_16x16x32_bf16 v[168:171], a[252:255], v[84:87], v[168:171]\n"
        "ds_read_b128 a[244:247], v254 offset:6160\n"
        "v_cvt_pk_bf16_f32 v233, v192, v196\n"
        "v_cvt_pk_bf16_f32 v234, v200, v204\n"
        "v_rcp_f32 v48, v48\n"
        "v_fma_f32 v96, v96, s24, v238\n"
        "v_mfma_f32_16x16x32_bf16 v[188:191], v[132:135], v[84:87], 0\n"
        "buffer_store_dwordx3 v[232:234], v250, s[16:19], s39 offen offset:0 sc1\n"
        "ds_read_b128 a[248:251], v254 offset:36880\n"
        "v_mfma_f32_16x16x32_bf16 v[188:191], v[128:131], v[80:83], v[188:191]\n"
        "v_cvt_pk_bf16_f32 v232, v181, v185\n"
        "v_cvt_pk_bf16_f32 v233, v193, v197\n"
        "v_cvt_pk_bf16_f32 v234, v201, v205\n"
        "v_mul_f32 v52, v48, v52\n"
        "v_mul_f32 v53, v48, v53\n"
        "v_mul_f32 v54, v48, v54\n"
        "v_mfma_f32_16x16x32_bf16 v[208:211], v[136:139], v[80:83], 0\n"
        "buffer_store_dwordx3 v[232:234], v250, s[16:19], s39 offen offset:384 sc1\n"
        "v_mul_f32 v55, v48, v55\n"
        "v_mfma_f32_16x16x32_bf16 v[208:211], v[140:143], v[84:87], v[208:211]\n"
        "v_cvt_pk_bf16_f32 v232, v182, v186\n"
        "v_cvt_pk_bf16_f32 v233, v194, v198\n"
        "v_cvt_pk_bf16_f32 v234, v202, v206\n"
        "v_cvt_pk_bf16_f32 v52, v52, v53\n"
        "v_cvt_pk_bf16_f32 v53, v54, v55\n"
        "v_mul_f32 v56, v48, v56\n"
        "v_mfma_f32_16x16x32_bf16 v[228:231], v[148:151], v[84:87], 0\n"
        "buffer_store_dwordx3 v[232:234], v250, s[16:19], s39 offen offset:768 sc1\n"
        "v_mul_f32 v57, v48, v57\n"
        "v_mfma_f32_16x16x32_bf16 v[228:231], v[144:147], v[80:83], v[228:231]\n"
        "v_cvt_pk_bf16_f32 v232, v183, v187\n"
        "v_cvt_pk_bf16_f32 v233, v195, v199\n"
        "v_cvt_pk_bf16_f32 v234, v203, v207\n"
        "v_mul_f32 v58, v48, v58\n"
        "v_mul_f32 v59, v48, v59\n"
        "v_cvt_pk_bf16_f32 v54, v56, v57\n"
        "v_cvt_pk_bf16_f32 v55, v58, v59\n"
        "v_mul_f32 v60, v48, v60\n"
        "v_mul_f32 v61, v48, v61\n"
        "v_mul_f32 v62, v48, v62\n"
        "v_mul_f32 v63, v48, v63\n"
        "v_cvt_pk_bf16_f32 v60, v60, v61\n"
        "v_cvt_pk_bf16_f32 v61, v62, v63\n"
        "ds_write_b128 v245, v[52:55] offset:0\n"
        "ds_write_b64 v246, v[60:61] offset:0\n"
        "v_rcp_f32 v168, v168\n"
        "v_exp_f32 v96, v96\n"
        "v_mul_f32 v188, v168, v188\n"
        "v_mul_f32 v189, v168, v189\n"
        "v_mul_f32 v190, v168, v190\n"
        "v_mul_f32 v191, v168, v191\n"
        "v_cvt_pk_bf16_f32 v188, v188, v189\n"
        "v_cvt_pk_bf16_f32 v189, v190, v191\n"
        "v_mul_f32 v208, v168, v208\n"
        "v_mul_f32 v209, v168, v209\n"
        "v_mul_f32 v210, v168, v210\n"
        "v_mul_f32 v211, v168, v211\n"
        "v_cvt_pk_bf16_f32 v190, v208, v209\n"
        "v_cvt_pk_bf16_f32 v191, v210, v211\n"
        "v_mul_f32 v228, v168, v228\n"
        "v_mul_f32 v229, v168, v229\n"
        "v_mul_f32 v230, v168, v230\n"
        "v_mul_f32 v231, v168, v231\n"
        "v_cvt_pk_bf16_f32 v228, v228, v229\n"
        "v_cvt_pk_bf16_f32 v229, v230, v231\n"
        "ds_write_b128 v245, v[188:191] offset:256\n"
        "ds_write_b64 v246, v[228:229] offset:256\n"
        "s_waitcnt lgkmcnt(0)\n"
        "s_barrier\n"
        "ds_read_b128 v[152:155], v247 offset:0\n"
        "s_waitcnt lgkmcnt(0)\n"
        "v_mfma_f32_16x16x32_bf16 v[0:3], v[152:155], a[216:219], 0\n"
        "buffer_store_dwordx3 v[232:234], v250, s[16:19], s39 offen offset:1152 sc1\n"
        "ds_read_b128 v[156:159], v247 offset:4096\n"
        "s_waitcnt lgkmcnt(0)\n"
        "v_mfma_f32_16x16x32_bf16 v[0:3], v[156:159], a[220:223], v[0:3]\n"
        "ds_read_b128 v[172:175], v247 offset:8192\n"
        "ds_read_b128 v[176:179], v247 offset:12288\n"
        "ds_read_b128 v[212:215], v247 offset:16384\n"
        "ds_read_b128 v[216:219], v247 offset:20480\n"
        "s_waitcnt lgkmcnt(3)\n"
        "v_mfma_f32_16x16x32_bf16 v[0:3], v[172:175], a[224:227], v[0:3]\n"
        "s_waitcnt lgkmcnt(2)\n"
        "ds_read_b128 a[216:219], v254 offset:43024\n"
        "ds_read_b128 a[220:223], v254 offset:12304\n"
        "v_fma_f32 v97, v97, s24, v238\n"
        "v_fma_f32 v98, v98, s24, v238\n"
        "v_mfma_f32_16x16x32_bf16 v[0:3], v[176:179], a[228:231], v[0:3]\n"
        "s_waitcnt lgkmcnt(3)\n"
        "ds_read_b128 a[224:227], v254 offset:49168\n"
        "ds_read_b128 a[228:231], v248 offset:32\n"
        "v_exp_f32 v97, v97\n"
        "v_mfma_f32_16x16x32_bf16 v[0:3], v[212:215], a[232:235], v[0:3]\n"
        "s_waitcnt lgkmcnt(4)\n"
        "ds_read_b128 a[232:235], v248 offset:6176\n"
        "v_exp_f32 v98, v98\n"
        "v_cvt_pk_bf16_f32 v96, v96, v97\n"
        "v_mfma_f32_16x16x32_bf16 v[0:3], v[216:219], a[236:239], v[0:3]\n"
        "ds_read_b128 a[236:239], v248 offset:36896\n"
        "v_fma_f32 v99, v99, s24, v238\n"
        "v_exp_f32 v99, v99\n"
        "v_mfma_f32_16x16x32_bf16 v[4:7], v[152:155], a[240:243], 0\n"
        "ds_read_b128 a[240:243], v248 offset:43040\n"
        "v_cvt_pk_bf16_f32 v97, v98, v99\n"
        "v_fma_f32 v100, v100, s24, v238\n"
        "v_fma_f32 v101, v101, s24, v238\n"
        "v_mfma_f32_16x16x32_bf16 v[4:7], v[156:159], a[244:247], v[4:7]\n"
        "ds_read_b128 a[244:247], v248 offset:12320\n"
        "v_exp_f32 v100, v100\n"
        "v_fma_f32 v102, v102, s24, v238\n"
        "v_mfma_f32_16x16x32_bf16 v[4:7], v[172:175], a[248:251], v[4:7]\n"
        "s_waitcnt lgkmcnt(7)\n"
        "ds_read_b128 a[248:251], v248 offset:49184\n"
        "v_exp_f32 v101, v101\n"
        "v_fma_f32 v103, v103, s24, v238\n"
        "v_mfma_f32_16x16x32_bf16 v[4:7], v[176:179], a[216:219], v[4:7]\n"
        "s_waitcnt lgkmcnt(7)\n"
        "v_cvt_pk_bf16_f32 v98, v100, v101\n"
        "v_exp_f32 v102, v102\n"
        "v_fma_f32 v104, v104, s24, v238\n"
        "v_mfma_f32_16x16x32_bf16 v[4:7], v[212:215], a[220:223], v[4:7]\n"
        "s_waitcnt lgkmcnt(6)\n"
        "v_exp_f32 v103, v103\n"
        "v_exp_f32 v104, v104\n"
        "v_mfma_f32_16x16x32_bf16 v[4:7], v[216:219], a[224:227], v[4:7]\n"
        "s_waitcnt lgkmcnt(5)\n"
        "v_cvt_pk_bf16_f32 v99, v102, v103\n"
        "v_fma_f32 v105, v105, s24, v238\n"
        "v_exp_f32 v105, v105\n"
        "v_mfma_f32_16x16x32_bf16 v[8:11], v[152:155], a[228:231], 0\n"
        "s_waitcnt lgkmcnt(4)\n"
        "ds_read_b128 a[216:219], v254 offset:48\n"
        "v_cvt_pk_bf16_f32 v100, v104, v105\n"
        "v_fma_f32 v106, v106, s24, v238\n"
        "v_fma_f32 v107, v107, s24, v238\n"
        "v_mfma_f32_16x16x32_bf16 v[8:11], v[156:159], a[232:235], v[8:11]\n"
        "s_waitcnt lgkmcnt(4)\n"
        "ds_read_b128 a[220:223], v254 offset:6192\n"
        "v_exp_f32 v106, v106\n"
        "v_fma_f32 v108, v108, s24, v238\n"
        "v_mfma_f32_16x16x32_bf16 v[8:11], v[172:175], a[236:239], v[8:11]\n"
        "s_waitcnt lgkmcnt(4)\n"
        "ds_read_b128 a[224:227], v254 offset:36912\n"
        "v_exp_f32 v107, v107\n"
        "v_fma_f32 v109, v109, s24, v238\n"
        "v_mfma_f32_16x16x32_bf16 v[8:11], v[176:179], a[240:243], v[8:11]\n"
        "s_waitcnt lgkmcnt(4)\n"
        "ds_read_b128 a[228:231], v254 offset:43056\n"
        "v_cvt_pk_bf16_f32 v101, v106, v107\n"
        "v_exp_f32 v108, v108\n"
        "v_mfma_f32_16x16x32_bf16 v[8:11], v[212:215], a[244:247], v[8:11]\n"
        "s_waitcnt lgkmcnt(4)\n"
        "ds_read_b128 a[232:235], v254 offset:12336\n"
        "v_exp_f32 v109, v109\n"
        "v_fma_f32 v110, v110, s24, v238\n"
        "v_mfma_f32_16x16x32_bf16 v[8:11], v[216:219], a[248:251], v[8:11]\n"
        "ds_read_b128 a[236:239], v254 offset:49200\n"
        "s_add_u32 s38, s37, s43\n"
        "v_cvt_pk_bf16_f32 v102, v108, v109\n"
        "v_exp_f32 v110, v110\n"
        "v_mfma_f32_16x16x32_bf16 v[64:67], a[252:255], v[96:99], 0\n"
        "ds_read_b128 a[240:243], v248 offset:64\n"
        "s_add_u32 s39, s28, s43\n"
        "v_fma_f32 v111, v111, s24, v238\n"
        "v_exp_f32 v111, v111\n"
        "v_fma_f32 v112, v112, s24, v239\n"
        "v_cvt_pk_bf16_f32 v103, v110, v111\n"
        "s_nop 1\n"
        "v_mfma_f32_16x16x32_bf16 v[64:67], a[252:255], v[100:103], v[64:67]\n"
        "ds_read_b128 a[244:247], v248 offset:6208\n"
        "v_exp_f32 v112, v112\n"
        "v_fma_f32 v113, v113, s24, v239\n"
        "v_mfma_f32_16x16x32_bf16 v[68:71], v[132:135], v[100:103], 0\n"
        "ds_read_b128 a[248:251], v248 offset:36928\n"
        "v_exp_f32 v113, v113\n"
        "v_fma_f32 v114, v114, s24, v239\n"
        "v_mfma_f32_16x16x32_bf16 v[68:71], v[128:131], v[96:99], v[68:71]\n"
        "v_rcp_f32 v64, v64\n"
        "v_exp_f32 v114, v114\n"
        "v_mfma_f32_16x16x32_bf16 v[72:75], v[136:139], v[96:99], 0\n"
        "v_cvt_pk_bf16_f32 v112, v112, v113\n"
        "v_fma_f32 v115, v115, s24, v239\n"
        "v_exp_f32 v115, v115\n"
        "v_mfma_f32_16x16x32_bf16 v[72:75], v[140:143], v[100:103], v[72:75]\n"
        "v_cvt_pk_bf16_f32 v113, v114, v115\n"
        "v_mul_f32 v68, v64, v68\n"
        "v_mul_f32 v69, v64, v69\n"
        "v_mul_f32 v70, v64, v70\n"
        "v_mfma_f32_16x16x32_bf16 v[76:79], v[148:151], v[100:103], 0\n"
        "v_mul_f32 v71, v64, v71\n"
        "v_cvt_pk_bf16_f32 v68, v68, v69\n"
        "v_cvt_pk_bf16_f32 v69, v70, v71\n"
        "v_mul_f32 v72, v64, v72\n"
        "v_mfma_f32_16x16x32_bf16 v[76:79], v[144:147], v[96:99], v[76:79]\n"
        "s_waitcnt lgkmcnt(8)\n"
        "v_mul_f32 v73, v64, v73\n"
        "v_mul_f32 v74, v64, v74\n"
        "v_mul_f32 v75, v64, v75\n"
        "v_cvt_pk_bf16_f32 v70, v72, v73\n"
        "v_mfma_f32_16x16x32_bf16 v[12:15], v[152:155], a[216:219], 0\n"
        "s_waitcnt lgkmcnt(7)\n"
        "ds_read_b128 a[216:219], v248 offset:43072\n"
        "v_cvt_pk_bf16_f32 v71, v74, v75\n"
        "v_fma_f32 v116, v116, s24, v239\n"
        "v_mul_f32 v76, v64, v76\n"
        "v_mfma_f32_16x16x32_bf16 v[12:15], v[156:159], a[220:223], v[12:15]\n"
        "s_waitcnt lgkmcnt(7)\n"
        "ds_read_b128 a[220:223], v248 offset:12352\n"
        "v_mul_f32 v77, v64, v77\n"
        "v_mul_f32 v78, v64, v78\n"
        "v_mul_f32 v79, v64, v79\n"
        "v_mfma_f32_16x16x32_bf16 v[12:15], v[172:175], a[224:227], v[12:15]\n"
        "s_waitcnt lgkmcnt(7)\n"
        "ds_write_b128 v245, v[68:71] offset:512\n"
        "v_mfma_f32_16x16x32_bf16 v[12:15], v[176:179], a[228:231], v[12:15]\n"
        "s_waitcnt lgkmcnt(7)\n"
        "ds_read_b128 a[224:227], v248 offset:49216\n"
        "ds_read_b128 a[228:231], v254 offset:80\n"
        "v_cvt_pk_bf16_f32 v76, v76, v77\n"
        "v_cvt_pk_bf16_f32 v77, v78, v79\n"
        "v_mfma_f32_16x16x32_bf16 v[12:15], v[212:215], a[232:235], v[12:15]\n"
        "s_waitcnt lgkmcnt(8)\n"
        "ds_read_b128 a[232:235], v254 offset:6224\n"
        "v_exp_f32 v116, v116\n"
        "v_fma_f32 v117, v117, s24, v239\n"
        "v_mfma_f32_16x16x32_bf16 v[12:15], v[216:219], a[236:239], v[12:15]\n"
        "s_waitcnt lgkmcnt(8)\n"
        "ds_write_b64 v246, v[76:77] offset:512\n"
        "ds_read_b128 a[236:239], v254 offset:36944\n"
        "v_mfma_f32_16x16x32_bf16 v[16:19], v[152:155], a[240:243], 0\n"
        "s_waitcnt lgkmcnt(9)\n"
        "ds_read_b128 a[240:243], v254 offset:43088\n"
        "v_exp_f32 v117, v117\n"
        "v_fma_f32 v118, v118, s24, v239\n"
        "v_mfma_f32_16x16x32_bf16 v[16:19], v[156:159], a[244:247], v[16:19]\n"
        "s_waitcnt lgkmcnt(9)\n"
        "ds_read_b128 a[244:247], v254 offset:12368\n"
        "v_exp_f32 v118, v118\n"
        "v_cvt_pk_bf16_f32 v114, v116, v117\n"
        "v_mfma_f32_16x16x32_bf16 v[16:19], v[172:175], a[248:251], v[16:19]\n"
        "s_waitcnt lgkmcnt(9)\n"
        "ds_read_b128 a[248:251], v254 offset:49232\n"
        "v_fma_f32 v119, v119, s24, v239\n"
        "v_exp_f32 v119, v119\n"
        "v_mfma_f32_16x16x32_bf16 v[16:19], v[176:179], a[216:219], v[16:19]\n"
        "s_waitcnt lgkmcnt(9)\n"
        "v_cvt_pk_bf16_f32 v115, v118, v119\n"
        "v_fma_f32 v120, v120, s24, v239\n"
        "v_exp_f32 v120, v120\n"
        "v_mfma_f32_16x16x32_bf16 v[16:19], v[212:215], a[220:223], v[16:19]\n"
        "s_waitcnt lgkmcnt(7)\n"
        "v_fma_f32 v121, v121, s24, v239\n"
        "v_exp_f32 v121, v121\n"
        "v_fma_f32 v122, v122, s24, v239\n"
        "v_mfma_f32_16x16x32_bf16 v[16:19], v[216:219], a[224:227], v[16:19]\n"
        "s_waitcnt lgkmcnt(6)\n"
        "v_cvt_pk_bf16_f32 v116, v120, v121\n"
        "v_exp_f32 v122, v122\n"
        "v_fma_f32 v123, v123, s24, v239\n"
        "v_mfma_f32_16x16x32_bf16 v[20:23], v[152:155], a[228:231], 0\n"
        "s_waitcnt lgkmcnt(5)\n"
        "ds_read_b128 a[216:219], v248 offset:0\n"
        "v_exp_f32 v123, v123\n"
        "v_fma_f32 v124, v124, s24, v239\n"
        "v_mfma_f32_16x16x32_bf16 v[20:23], v[156:159], a[232:235], v[20:23]\n"
        "s_waitcnt lgkmcnt(4)\n"
        "ds_read_b128 a[220:223], v248 offset:6144\n"
        "v_cvt_pk_bf16_f32 v117, v122, v123\n"
        "v_exp_f32 v124, v124\n"
        "v_mfma_f32_16x16x32_bf16 v[20:23], v[172:175], a[236:239], v[20:23]\n"
        "s_waitcnt lgkmcnt(4)\n"
        "ds_read_b128 a[224:227], v248 offset:36864\n"
        "v_fma_f32 v125, v125, s24, v239\n"
        "v_exp_f32 v125, v125\n"
        "v_mfma_f32_16x16x32_bf16 v[20:23], v[176:179], a[240:243], v[20:23]\n"
        "s_waitcnt lgkmcnt(4)\n"
        "ds_read_b128 a[228:231], v248 offset:43008\n"
        "v_cvt_pk_bf16_f32 v118, v124, v125\n"
        "v_fma_f32 v126, v126, s24, v239\n"
        "v_fma_f32 v127, v127, s24, v239\n"
        "v_mfma_f32_16x16x32_bf16 v[20:23], v[212:215], a[244:247], v[20:23]\n"
        "s_waitcnt lgkmcnt(4)\n"
        "ds_read_b128 a[232:235], v248 offset:12288\n"
        "v_exp_f32 v126, v126\n"
        "v_cvt_pk_bf16_f32 v192, v0, v4\n"
        "v_mfma_f32_16x16x32_bf16 v[20:23], v[216:219], a[248:251], v[20:23]\n"
        "ds_read_b128 a[236:239], v248 offset:49152\n"
        "v_exp_f32 v127, v127\n"
        "v_cvt_pk_bf16_f32 v193, v8, v12\n"
        "v_mfma_f32_16x16x32_bf16 v[80:83], a[252:255], v[112:115], 0\n"
        "ds_read_b128 a[240:243], v254 offset:16\n"
        "v_cvt_pk_bf16_f32 v119, v126, v127\n"
        "v_cvt_pk_bf16_f32 v196, v1, v5\n"
        "v_cvt_pk_bf16_f32 v197, v9, v13\n"
        "v_mfma_f32_16x16x32_bf16 v[80:83], a[252:255], v[116:119], v[80:83]\n"
        "ds_read_b128 a[244:247], v254 offset:6160\n"
        "v_cvt_pk_bf16_f32 v194, v16, v20\n"
        "v_cvt_pk_bf16_f32 v198, v17, v21\n"
        "v_cvt_pk_bf16_f32 v200, v2, v6\n"
        "v_mfma_f32_16x16x32_bf16 v[84:87], v[132:135], v[116:119], 0\n"
        "buffer_store_dwordx3 v[192:194], v250, s[16:19], s38 offen offset:0 sc1\n"
        "v_mfma_f32_16x16x32_bf16 v[84:87], v[128:131], v[112:115], v[84:87]\n"
        "ds_read_b128 a[248:251], v254 offset:36880\n"
        "v_rcp_f32 v80, v80\n"
        "v_cvt_pk_bf16_f32 v201, v10, v14\n"
        "v_mfma_f32_16x16x32_bf16 v[88:91], v[136:139], v[112:115], 0\n"
        "v_cvt_pk_bf16_f32 v202, v18, v22\n"
        "v_cvt_pk_bf16_f32 v204, v3, v7\n"
        "v_cvt_pk_bf16_f32 v205, v11, v15\n"
        "v_cvt_pk_bf16_f32 v206, v19, v23\n"
        "v_mfma_f32_16x16x32_bf16 v[88:91], v[140:143], v[116:119], v[88:91]\n"
        "buffer_store_dwordx3 v[196:198], v250, s[16:19], s38 offen offset:384 sc1\n"
        "v_mfma_f32_16x16x32_bf16 v[92:95], v[148:151], v[116:119], 0\n"
        "v_mul_f32 v84, v80, v84\n"
        "v_mul_f32 v85, v80, v85\n"
        "v_mul_f32 v86, v80, v86\n"
        "v_mul_f32 v87, v80, v87\n"
        "v_mfma_f32_16x16x32_bf16 v[92:95], v[144:147], v[112:115], v[92:95]\n"
        "v_cvt_pk_bf16_f32 v84, v84, v85\n"
        "v_cvt_pk_bf16_f32 v85, v86, v87\n"
        "v_mul_f32 v88, v80, v88\n"
        "v_mul_f32 v89, v80, v89\n"
        "v_mul_f32 v90, v80, v90\n"
        "v_mul_f32 v91, v80, v91\n"
        "v_cvt_pk_bf16_f32 v86, v88, v89\n"
        "v_cvt_pk_bf16_f32 v87, v90, v91\n"
        "v_mul_f32 v92, v80, v92\n"
        "v_mul_f32 v93, v80, v93\n"
        "v_mul_f32 v94, v80, v94\n"
        "v_mul_f32 v95, v80, v95\n"
        "v_cvt_pk_bf16_f32 v92, v92, v93\n"
        "v_cvt_pk_bf16_f32 v93, v94, v95\n"
        "ds_write_b128 v245, v[84:87] offset:768\n"
        "ds_write_b64 v246, v[92:93] offset:768\n"
        "s_waitcnt lgkmcnt(0)\n"
        "s_barrier\n"
        "ds_read_b128 v[128:131], v247 offset:512\n"
        "s_waitcnt lgkmcnt(0)\n"
        "v_mfma_f32_16x16x32_bf16 v[24:27], v[128:131], a[216:219], 0\n"
        "ds_read_b128 v[132:135], v247 offset:4608\n"
        "ds_read_b128 v[136:139], v247 offset:8704\n"
        "s_waitcnt lgkmcnt(1)\n"
        "v_mfma_f32_16x16x32_bf16 v[24:27], v[132:135], a[220:223], v[24:27]\n"
        "s_waitcnt lgkmcnt(0)\n"
        "ds_read_b128 v[140:143], v247 offset:12800\n"
        "ds_read_b128 v[144:147], v247 offset:16896\n"
        "v_mfma_f32_16x16x32_bf16 v[24:27], v[136:139], a[224:227], v[24:27]\n"
        "s_waitcnt lgkmcnt(1)\n"
        "ds_read_b128 v[148:151], v247 offset:20992\n"
        "ds_read_b128 a[216:219], v254 offset:43024\n"
        "v_mfma_f32_16x16x32_bf16 v[24:27], v[140:143], a[228:231], v[24:27]\n"
        "s_waitcnt lgkmcnt(2)\n"
        "buffer_store_dwordx3 v[200:202], v250, s[16:19], s38 offen offset:768 sc1\n"
        "v_mfma_f32_16x16x32_bf16 v[24:27], v[144:147], a[232:235], v[24:27]\n"
        "s_waitcnt lgkmcnt(1)\n"
        "ds_read_b128 a[220:223], v254 offset:12304\n"
        "ds_read_b128 a[224:227], v254 offset:49168\n"
        "v_mfma_f32_16x16x32_bf16 v[24:27], v[148:151], a[236:239], v[24:27]\n"
        "ds_read_b128 a[228:231], v248 offset:32\n"
        "ds_read_b128 a[232:235], v248 offset:6176\n"
        "v_mfma_f32_16x16x32_bf16 v[28:31], v[128:131], a[240:243], 0\n"
        "buffer_store_dwordx3 v[204:206], v250, s[16:19], s38 offen offset:1152 sc1\n"
        "v_mfma_f32_16x16x32_bf16 v[28:31], v[132:135], a[244:247], v[28:31]\n"
        "ds_read_b128 a[236:239], v248 offset:36896\n"
        "ds_read_b128 a[240:243], v248 offset:43040\n"
        "v_mfma_f32_16x16x32_bf16 v[28:31], v[136:139], a[248:251], v[28:31]\n"
        "s_waitcnt lgkmcnt(6)\n"
        "ds_read_b128 a[244:247], v248 offset:12320\n"
        "ds_read_b128 a[248:251], v248 offset:49184\n"
        "v_mfma_f32_16x16x32_bf16 v[28:31], v[140:143], a[216:219], v[28:31]\n"
        "s_waitcnt lgkmcnt(7)\n"
        "ds_read_b128 a[216:219], v254 offset:48\n"
        "v_mfma_f32_16x16x32_bf16 v[28:31], v[144:147], a[220:223], v[28:31]\n"
        "s_waitcnt lgkmcnt(7)\n"
        "ds_read_b128 a[220:223], v254 offset:6192\n"
        "v_mfma_f32_16x16x32_bf16 v[28:31], v[148:151], a[224:227], v[28:31]\n"
        "s_waitcnt lgkmcnt(7)\n"
        "ds_read_b128 a[224:227], v254 offset:36912\n"
        "v_mfma_f32_16x16x32_bf16 v[32:35], v[128:131], a[228:231], 0\n"
        "s_waitcnt lgkmcnt(7)\n"
        "ds_read_b128 a[228:231], v254 offset:43056\n"
        "v_mfma_f32_16x16x32_bf16 v[32:35], v[132:135], a[232:235], v[32:35]\n"
        "s_waitcnt lgkmcnt(7)\n"
        "ds_read_b128 a[232:235], v254 offset:12336\n"
        "v_mfma_f32_16x16x32_bf16 v[32:35], v[136:139], a[236:239], v[32:35]\n"
        "s_waitcnt lgkmcnt(7)\n"
        "ds_read_b128 a[236:239], v254 offset:49200\n"
        "v_mfma_f32_16x16x32_bf16 v[32:35], v[140:143], a[240:243], v[32:35]\n"
        "s_waitcnt lgkmcnt(7)\n"
        "ds_read_b128 a[240:243], v248 offset:64\n"
        "v_cvt_pk_bf16_f32 v180, v24, v28\n"
        "v_mfma_f32_16x16x32_bf16 v[32:35], v[144:147], a[244:247], v[32:35]\n"
        "s_waitcnt lgkmcnt(7)\n"
        "ds_read_b128 a[244:247], v248 offset:6208\n"
        "v_cvt_pk_bf16_f32 v184, v25, v29\n"
        "v_mfma_f32_16x16x32_bf16 v[32:35], v[148:151], a[248:251], v[32:35]\n"
        "s_waitcnt lgkmcnt(7)\n"
        "ds_read_b128 a[248:251], v248 offset:36928\n"
        "v_cvt_pk_bf16_f32 v220, v26, v30\n"
        "v_mfma_f32_16x16x32_bf16 v[36:39], v[128:131], a[216:219], 0\n"
        "s_waitcnt lgkmcnt(7)\n"
        "ds_read_b128 a[216:219], v248 offset:43072\n"
        "v_cvt_pk_bf16_f32 v224, v27, v31\n"
        "v_mfma_f32_16x16x32_bf16 v[36:39], v[132:135], a[220:223], v[36:39]\n"
        "s_waitcnt lgkmcnt(7)\n"
        "ds_read_b128 a[220:223], v248 offset:12352\n"
        "v_mfma_f32_16x16x32_bf16 v[36:39], v[136:139], a[224:227], v[36:39]\n"
        "s_waitcnt lgkmcnt(7)\n"
        "ds_read_b128 a[224:227], v248 offset:49216\n"
        "v_mfma_f32_16x16x32_bf16 v[36:39], v[140:143], a[228:231], v[36:39]\n"
        "s_waitcnt lgkmcnt(7)\n"
        "ds_read_b128 a[228:231], v254 offset:80\n"
        "v_mfma_f32_16x16x32_bf16 v[36:39], v[144:147], a[232:235], v[36:39]\n"
        "s_waitcnt lgkmcnt(7)\n"
        "ds_read_b128 a[232:235], v254 offset:6224\n"
        "v_mfma_f32_16x16x32_bf16 v[36:39], v[148:151], a[236:239], v[36:39]\n"
        "s_waitcnt lgkmcnt(7)\n"
        "ds_read_b128 a[236:239], v254 offset:36944\n"
        "v_mfma_f32_16x16x32_bf16 v[40:43], v[128:131], a[240:243], 0\n"
        "s_waitcnt lgkmcnt(7)\n"
        "ds_read_b128 a[240:243], v254 offset:43088\n"
        "v_mfma_f32_16x16x32_bf16 v[40:43], v[132:135], a[244:247], v[40:43]\n"
        "s_waitcnt lgkmcnt(7)\n"
        "ds_read_b128 a[244:247], v254 offset:12368\n"
        "v_mfma_f32_16x16x32_bf16 v[40:43], v[136:139], a[248:251], v[40:43]\n"
        "s_waitcnt lgkmcnt(7)\n"
        "ds_read_b128 a[248:251], v254 offset:49232\n"
        "v_mfma_f32_16x16x32_bf16 v[40:43], v[140:143], a[216:219], v[40:43]\n"
        "s_waitcnt lgkmcnt(7)\n"
        "v_cvt_pk_bf16_f32 v181, v32, v36\n"
        "v_cvt_pk_bf16_f32 v185, v33, v37\n"
        "v_mfma_f32_16x16x32_bf16 v[40:43], v[144:147], a[220:223], v[40:43]\n"
        "s_waitcnt lgkmcnt(6)\n"
        "v_cvt_pk_bf16_f32 v221, v34, v38\n"
        "v_cvt_pk_bf16_f32 v225, v35, v39\n"
        "v_mfma_f32_16x16x32_bf16 v[40:43], v[148:151], a[224:227], v[40:43]\n"
        "s_waitcnt lgkmcnt(5)\n"
        "v_mfma_f32_16x16x32_bf16 v[44:47], v[128:131], a[228:231], 0\n"
        "s_waitcnt lgkmcnt(4)\n"
        "v_mfma_f32_16x16x32_bf16 v[44:47], v[132:135], a[232:235], v[44:47]\n"
        "s_waitcnt lgkmcnt(3)\n"
        "v_mfma_f32_16x16x32_bf16 v[44:47], v[136:139], a[236:239], v[44:47]\n"
        "s_waitcnt lgkmcnt(2)\n"
        "v_mfma_f32_16x16x32_bf16 v[44:47], v[140:143], a[240:243], v[44:47]\n"
        "s_waitcnt lgkmcnt(1)\n"
        "v_mfma_f32_16x16x32_bf16 v[44:47], v[144:147], a[244:247], v[44:47]\n"
        "s_waitcnt lgkmcnt(0)\n"
        "v_mfma_f32_16x16x32_bf16 v[44:47], v[148:151], a[248:251], v[44:47]\n"
        "s_nop 7\n"
        "v_cvt_pk_bf16_f32 v182, v40, v44\n"
        "buffer_store_dwordx3 v[180:182], v250, s[16:19], s39 offen offset:0 sc1\n"
        "v_cvt_pk_bf16_f32 v186, v41, v45\n"
        "buffer_store_dwordx3 v[184:186], v250, s[16:19], s39 offen offset:384 sc1\n"
        "v_cvt_pk_bf16_f32 v222, v42, v46\n"
        "buffer_store_dwordx3 v[220:222], v250, s[16:19], s39 offen offset:768 sc1\n"
        "v_cvt_pk_bf16_f32 v226, v43, v47\n"
        "buffer_store_dwordx3 v[224:226], v250, s[16:19], s39 offen offset:1152 sc1\n"
        : "+{v240}"(xr[0]), "+{v241}"(xr[1]), "+{v251}"(mr[0]), "+{v252}"(mr[1]), "+{v242}"(dm[0]), "+{v243}"(dm[1]), "+{v244}"(dm[2]), "+{v245}"(ow), "+{v246}"(ow2), "+{v247}"(ord), "+{v248}"(wor), "+{v249}"(ms), "+{v250}"(st), "+{v142}"(l16), "+{v170}"(id0), "+{v171}"(id1), "+{v139}"(rs), "+{v144}"(rr), "+{v140}"(oa), "+{v141}"(ob), "+{v147}"(od), "+{v145}"(rbs), "+{v146}"(rbw), "+{v254}"(wor2), "+{v255}"(ml), "+{v148}"(pf0), "+{v149}"(pf1), "+{v128}"(wsrc[0]), "+{v129}"(wsrc[1]), "+{v130}"(wsrc[2]), "+{v131}"(wrd[0]), "+{v132}"(wrd[1]), "+{v133}"(wrd[2]), "+{s47}"(ya), "+{s36}"(mk), "+{s37}"(raw), "+{s43}"(x0), "+{s44}"(x1)
        : "{s[8:11]}"(srdx), "{s[12:15]}"(srdm), "{s[16:19]}"(srdo), "{s[20:23]}"(srdw), "{s[52:55]}"(srdwo), "{s[56:59]}"(srdrp), "{s[60:63]}"(srdrb), "{s42}"(wv), "s"(ldsb)
        : "v0", "v1", "v2", "v3", "v4", "v5", "v6", "v7", "v8", "v9", "v10", "v11", "v12", "v13", "v14", "v15", "v16", "v17", "v18", "v19", "v20", "v21", "v22", "v23", "v24", "v25", "v26", "v27", "v28", "v29", "v30", "v31", "v32", "v33", "v34", "v35", "v36", "v37", "v38", "v39", "v40", "v41", "v42", "v43", "v44", "v45", "v46", "v47", "v48", "v49", "v50", "v51", "v52", "v53", "v54", "v55", "v56", "v57", "v58", "v59", "v60", "v61", "v62", "v63", "v64", "v65", "v66", "v67", "v68", "v69", "v70", "v71", "v72", "v73", "v74", "v75", "v76", "v77", "v78", "v79", "v80", "v81", "v82", "v83", "v84", "v85", "v86", "v87", "v88", "v89", "v90", "v91", "v92", "v93", "v94", "v95", "v96", "v97", "v98", "v99", "v100", "v101", "v102", "v103", "v104", "v105", "v106", "v107", "v108", "v109", "v110", "v111", "v112", "v113", "v114", "v115", "v116", "v117", "v118", "v119", "v120", "v121", "v122", "v123", "v124", "v125", "v126", "v127", "v134", "v135", "v136", "v137", "v138", "v143", "v150", "v151", "v152", "v153", "v154", "v155", "v156", "v157", "v158", "v159", "v160", "v161", "v162", "v163", "v164", "v165", "v166", "v167", "v168", "v169", "v172", "v173", "v174", "v175", "v176", "v177", "v178", "v179", "v180", "v181", "v182", "v183", "v184", "v185", "v186", "v187", "v188", "v189", "v190", "v191", "v192", "v193", "v194", "v195", "v196", "v197", "v198", "v199", "v200", "v201", "v202", "v203", "v204", "v205", "v206", "v207", "v208", "v209", "v210", "v211", "v212", "v213", "v214", "v215", "v216", "v217", "v218", "v219", "v220", "v221", "v222", "v223", "v224", "v225", "v226", "v227", "v228", "v229", "v230", "v231", "v232", "v233", "v234", "v235", "v236", "v237", "v238", "v239", "v253", "a0", "a1", "a2", "a3", "a4", "a5", "a6", "a7", "a8", "a9", "a10", "a11", "a12", "a13", "a14", "a15", "a16", "a17", "a18", "a19", "a20", "a21", "a22", "a23", "a24", "a25", "a26", "a27", "a28", "a29", "a30", "a31", "a32", "a33", "a34", "a35", "a36", "a37", "a38", "a39", "a40", "a41", "a42", "a43", "a44", "a45", "a46", "a47", "a48", "a49", "a50", "a51", "a52", "a53", "a54", "a55", "a56", "a57", "a58", "a59", "a60", "a61", "a62", "a63", "a64", "a65", "a66", "a67", "a68", "a69", "a70", "a71", "a72", "a73", "a74", "a75", "a76", "a77", "a78", "a79", "a80", "a81", "a82", "a83", "a84", "a85", "a86", "a87", "a88", "a89", "a90", "a91", "a92", "a93", "a94", "a95", "a96", "a97", "a98", "a99", "a100", "a101", "a102", "a103", "a104", "a105", "a106", "a107", "a108", "a109", "a110", "a111", "a112", "a113", "a114", "a115", "a116", "a117", "a118", "a119", "a120", "a121", "a122", "a123", "a124", "a125", "a126", "a127", "a128", "a129", "a130", "a131", "a132", "a133", "a134", "a135", "a136", "a137", "a138", "a139", "a140", "a141", "a142", "a143", "a144", "a145", "a146", "a147", "a148", "a149", "a150", "a151", "a152", "a153", "a154", "a155", "a156", "a157", "a158", "a159", "a160", "a161", "a162", "a163", "a164", "a165", "a166", "a167", "a168", "a169", "a170", "a171", "a172", "a173", "a174", "a175", "a176", "a177", "a178", "a179", "a180", "a181", "a182", "a183", "a184", "a185", "a186", "a187", "a188", "a189", "a190", "a191", "a192", "a193", "a194", "a195", "a196", "a197", "a198", "a199", "a200", "a201", "a202", "a203", "a204", "a205", "a206", "a207", "a208", "a209", "a210", "a211", "a212", "a213", "a214", "a215", "a216", "a217", "a218", "a219", "a220", "a221", "a222", "a223", "a224", "a225", "a226", "a227", "a228", "a229", "a230", "a231", "a232", "a233", "a234", "a235", "a236", "a237", "a238", "a239", "a240", "a241", "a242", "a243", "a244", "a245", "a246", "a247", "a248", "a249", "a250", "a251", "a252", "a253", "a254", "a255", "s24", "s25", "s26", "s27", "s28", "s29", "s30", "s31", "s34", "s35", "s38", "s39", "s40", "s41", "s45", "s46", "s48", "s49", "s50", "s51", "s64", "s65", "s66", "s67", "vcc", "scc", "memory");
}


__global__ __launch_bounds__(256, 1) __attribute__((aligned(4096))) void win_attn_kernel(const char* __restrict__ x, const char* __restrict__ wqkv,
                                                          const char* __restrict__ wo, const char* __restrict__ rpb,
                                                          const char* __restrict__ rpi, const char* __restrict__ mask,
                                                          char* __restrict__ out) {
    __shared__ __attribute__((aligned(16))) char smem[163840];
    unsigned tid = threadIdx.x, w;
    const unsigned ldsb = (unsigned)(size_t)smem;
    asm volatile(
        "s_load_dwordx2 s[8:9], s[0:1], 0\n"
        "s_load_dwordx2 s[20:21], s[0:1], 8\n"
        "s_load_dwordx2 s[52:53], s[0:1], 16\n"
        "s_load_dwordx2 s[60:61], s[0:1], 24\n"
        "s_load_dwordx2 s[56:57], s[0:1], 32\n"
        "s_load_dwordx2 s[12:13], s[0:1], 40\n"
        "s_load_dwordx2 s[16:17], s[0:1], 48\n"
        "v_readfirstlane_b32 s42, v0\n"
        "v_and_b32 v1, 63, v0\n"
        "v_lshlrev_b32 v145, 3, v0\n"
        "s_lshr_b32 s42, s42, 6\n"
        "s_bfe_u32 s41, s2, 0x50003\n"
        "s_lshl2_add_u32 s41, s41, s42\n"
        "s_lshl_b32 s49, s41, 8\n"
        "v_and_b32 v10, 1, v0\n"
        "v_lshl_add_u32 v10, v10, 7, s49\n"
        "v_min_u32 v10, 32000, v10\n"
        "s_getpc_b64 s[38:39]\n"
        "global_load_dword v66, v10, s[38:39]\n"
        "v_bfe_u32 v2, v0, 4, 2\n"
        "s_and_b32 s51, s42, 1\n"
        "s_lshl_b32 s66, s51, 3\n"
        "v_xor_b32 v11, v1, v2\n"
        "v_xor_b32 v11, s66, v11\n"
        "v_lshlrev_b32 v139, 4, v11\n"
        "v_xor_b32 v144, 64, v139\n"
        "s_lshl_b32 s34, s42, 12\n"
        "s_mul_i32 s40, s42, 3072\n"
        "s_and_b32 s31, s2, 63\n"
        "s_lshl_b32 s31, s31, 3\n"
        "s_add_u32 s31, s31, 4\n"
        "s_lshr_b32 s45, s2, 6\n"
        "s_lshl_b32 s45, s45, 3\n"
        "s_add_u32 s47, s45, s42\n"
        "s_add_u32 s47, s47, 68\n"
        "s_mul_i32 s47, s47, 196608\n"
        "s_lshl_b32 s46, s42, 1\n"
        "s_lshl_b32 s49, s41, 11\n"
        "v_lshl_add_u32 v148, v1, 7, s49\n"
        "v_lshl_or_b32 v148, v2, 28, v148\n"
        "s_mov_b32 s62, 1800\n"
        "s_mov_b32 s63, 0x20000\n"
        "s_mov_b32 s58, 16384\n"
        "s_mov_b32 s59, 0x20000\n"
        "s_mov_b32 s22, 221184\n"
        "s_mov_b32 s23, 0x20000\n"
        "s_mov_b32 s10, 100663296\n"
        "s_mov_b32 s11, 0x20000\n"
        "v_mul_u32_u24 v15, 171, v1\n"
        "v_lshrrev_b32 v15, 12, v15\n"
        "v_mul_u32_u24 v13, 24, v15\n"
        "v_sub_u32 v18, v1, v13\n"
        "v_bfe_u32 v21, v15, 1, 1\n"
        "v_add_u32 v13, s31, v15\n"
        "v_and_b32 v13, 0x1ff, v13\n"
        "v_mul_u32_u24 v13, 384, v13\n"
        "v_bitop3_b32 v14, v21, v18, s46 bitop3:0x36\n"
        "v_lshl_add_u32 v242, v14, 4, v13\n"
        "v_add_u32 v12, 64, v1\n"
        "v_mul_u32_u24 v16, 171, v12\n"
        "v_lshrrev_b32 v16, 12, v16\n"
        "v_mul_u32_u24 v13, 24, v16\n"
        "v_sub_u32 v19, v12, v13\n"
        "v_bfe_u32 v22, v16, 1, 1\n"
        "v_add_u32 v13, s31, v16\n"
        "v_and_b32 v13, 0x1ff, v13\n"
        "v_mul_u32_u24 v13, 384, v13\n"
        "v_bitop3_b32 v14, v22, v19, s46 bitop3:0x36\n"
        "v_lshl_add_u32 v243, v14, 4, v13\n"
        "v_add_u32 v12, 128, v1\n"
        "v_mul_u32_u24 v17, 171, v12\n"
        "v_lshrrev_b32 v17, 12, v17\n"
        "v_mul_u32_u24 v13, 24, v17\n"
        "v_sub_u32 v20, v12, v13\n"
        "v_bfe_u32 v23, v17, 1, 1\n"
        "v_add_u32 v13, s31, v17\n"
        "v_and_b32 v13, 0x1ff, v13\n"
        "v_mul_u32_u24 v13, 384, v13\n"
        "v_bitop3_b32 v14, v23, v20, s46 bitop3:0x36\n"
        "v_lshl_add_u32 v244, v14, 4, v13\n"
        "s_mov_b32 s14, 33554432\n"
        "s_mov_b32 s15, 0x20000\n"
        "v_bfe_u32 v4, v0, 4, 1\n"
        "s_lshl_b32 s66, s51, 2\n"
        "v_bitop3_b32 v11, v4, v1, s66 bitop3:0x36\n"
        "v_lshlrev_b32 v249, 4, v11\n"
        "s_waitcnt lgkmcnt(0)\n"
        "buffer_load_dword v64, v148, s[20:23], 0 offen\n"
        "buffer_load_dwordx2 v[28:29], v145, s[60:63], 0 offen\n"
        "s_add_u32 m0, s34, 122880\n"
        "s_mov_b32 s64, s34\n"
        "buffer_load_dwordx4 v139, s[56:59], s64 offen lds\n"
        "v_xor_b32 v149, 32, v249\n"
        "s_lshl_b32 s48, s42, 11\n"
        "s_lshl_b32 s36, s2, 13\n"
        "s_add_u32 s36, s36, s48\n"
        "s_add_u32 s36, s36, 4194304\n"
        "s_add_u32 s48, s48, 147456\n"
        "s_mov_b32 s54, 73728\n"
        "s_mov_b32 s55, 0x20000\n"
        "s_mov_b32 s18, 100663296\n"
        "s_mov_b32 s19, 0x20000\n"
        "s_mul_i32 s65, s42, 9216\n"
        "v_lshl_or_b32 v13, v21, 1, s66\n"
        "buffer_load_dwordx4 v139, s[56:59], s64 offen offset:1024 lds\n"
        "v_xor_b32 v13, v13, v18\n"
        "v_mul_u32_u24 v14, 1152, v15\n"
        "v_lshl_add_u32 v128, v13, 4, v14\n"
        "v_lshl_or_b32 v13, v22, 1, s66\n"
        "v_xor_b32 v13, v13, v19\n"
        "v_mul_u32_u24 v14, 1152, v16\n"
        "v_lshl_add_u32 v129, v13, 4, v14\n"
        "v_subrev_u32 v129, 1024, v129\n"
        "v_lshl_or_b32 v13, v23, 1, s66\n"
        "v_xor_b32 v13, v13, v20\n"
        "v_mul_u32_u24 v14, 1152, v17\n"
        "v_lshl_add_u32 v130, v13, 4, v14\n"
        "buffer_load_dwordx4 v144, s[56:59], s64 offen offset:2048 lds\n"
        "v_subrev_u32 v130, 2048, v130\n"
        "v_bfe_u32 v6, v0, 2, 2\n"
        "v_and_b32 v7, 3, v0\n"
        "v_lshlrev_b32 v150, 4, v1\n"
        "v_lshl_add_u32 v8, v6, 3, v7\n"
        "v_and_b32 v9, 0xf0, v150\n"
        "v_lshlrev_b32 v146, 2, v0\n"
        "v_add_u32 v146, 139264, v146\n"
        "v_lshlrev_b32 v10, 5, v2\n"
        "v_xor_b32 v10, v10, v9\n"
        "v_lshl_add_u32 v10, v8, 8, v10\n"
        "v_add_u32 v140, 122880, v10\n"
        "buffer_load_dwordx4 v144, s[56:59], s64 offen offset:3072 lds\n"
        "v_xor_b32 v141, 16, v140\n"
        "v_xor_b32 v142, 128, v140\n"
        "v_xor_b32 v143, 144, v140\n"
        "s_lshl_b32 s67, s42, 10\n"
        "s_add_u32 s67, s67, 139264\n"
        "v_bfe_u32 v10, v0, 1, 3\n"
        "v_xor_b32 v10, v10, v2\n"
        "v_lshlrev_b32 v10, 4, v10\n"
        "v_mul_u32_u24 v11, 384, v8\n"
        "v_add_u32 v11, 73728, v11\n"
        "v_add_u32 v240, v10, v11\n"
        "v_xor_b32 v241, 64, v240\n"
        "s_add_u32 m0, s40, 73728\n"
        "s_sub_u32 s64, s47, 12582912\n"
        "buffer_load_dwordx4 v242, s[8:11], s64 offen nt lds\n"
        "v_lshl_add_u32 v10, v8, 7, v10\n"
        "v_add_u32 v251, 147456, v10\n"
        "v_xor_b32 v252, 64, v251\n"
        "v_lshl_add_u32 v10, v2, 3, v6\n"
        "v_mul_u32_u24 v10, 384, v10\n"
        "v_and_b32 v11, 1, v0\n"
        "v_lshl_add_u32 v10, v11, 3, v10\n"
        "v_and_b32 v12, 2, v6\n"
        "v_lshl_or_b32 v12, v4, 2, v12\n"
        "s_mul_i32 s35, s42, 6\n"
        "v_bfe_u32 v13, v0, 1, 1\n"
        "v_add_u32 v13, s35, v13\n"
        "s_add_u32 m0, s40, 74752\n"
        "s_sub_u32 s64, s47, 12582912\n"
        "buffer_load_dwordx4 v243, s[8:11], s64 offen nt lds\n"
        "v_xor_b32 v14, v13, v12\n"
        "v_lshl_add_u32 v131, v14, 4, v10\n"
        "v_add_u32 v136, 98304, v131\n"
        "v_add_u32 v13, 2, v13\n"
        "v_xor_b32 v14, v13, v12\n"
        "v_lshl_add_u32 v132, v14, 4, v10\n"
        "v_add_u32 v137, 98304, v132\n"
        "v_add_u32 v13, 2, v13\n"
        "v_xor_b32 v14, v13, v12\n"
        "v_lshl_add_u32 v133, v14, 4, v10\n"
        "v_add_u32 v138, 98304, v133\n"
        "v_lshlrev_b32 v10, 4, v11\n"
        "s_add_u32 m0, s40, 75776\n"
        "s_sub_u32 s64, s47, 12582912\n"
        "buffer_load_dwordx4 v244, s[8:11], s64 offen nt lds\n"
        "v_mov_b32 v12, 0x3fb9\n"
        "v_cmp_eq_u32 vcc, v6, v2\n"
        "v_lshlrev_b32 v10, v10, v12\n"
        "v_bfe_i32 v13, v0, 0, 1\n"
        "v_cndmask_b32 v10, 0, v10, vcc\n"
        "v_cmp_gt_u32 vcc, 2, v7\n"
        "v_and_b32 v13, 0xaaaaaaaa, v13\n"
        "v_or_b32 v255, 0x44444444, v13\n"
        "v_cndmask_b32 v170, 0, v10, vcc\n"
        "v_xor_b32 v171, v170, v10\n"
        "v_mov_b64 v[188:189], v[170:171]\n"
        "v_mov_b64 v[210:211], v[170:171]\n"
        "s_add_u32 m0, s40, 86016\n"
        "s_sub_u32 s64, s47, 11796480\n"
        "buffer_load_dwordx4 v242, s[8:11], s64 offen nt lds\n"
        "v_mov_b64 v[228:229], v[170:171]\n"
        "v_bfe_u32 v5, v0, 5, 1\n"
        "v_and_b32 v3, 15, v0\n"
        "v_lshl_add_u32 v10, v2, 10, v9\n"
        "s_add_u32 s49, s34, 73728\n"
        "v_add_u32 v245, s49, v10\n"
        "s_lshl_b32 s49, s51, 9\n"
        "s_add_u32 s49, s49, 73728\n"
        "v_add_u32 v247, s49, v10\n"
        "v_lshl_add_u32 v10, v5, 10, v9\n"
        "v_lshl_add_u32 v10, v4, 3, v10\n"
        "s_lshl_b32 s49, s42, 11\n"
        "s_add_u32 m0, s40, 87040\n"
        "s_sub_u32 s64, s47, 11796480\n"
        "buffer_load_dwordx4 v243, s[8:11], s64 offen nt lds\n"
        "s_add_u32 s49, s49, 90112\n"
        "v_add_u32 v246, s49, v10\n"
        "s_lshr_b32 s35, s42, 1\n"
        "s_mul_i32 s49, s35, 18432\n"
        "v_mul_u32_u24 v12, 1536, v2\n"
        "v_add_u32 v12, s49, v12\n"
        "v_mad_u32_u24 v13, v9, 6, v12\n"
        "v_lshl_add_u32 v248, v4, 4, v13\n"
        "v_mad_i32_i24 v254, v4, -16, v13\n"
        "v_mul_u32_u24 v10, 196608, v2\n"
        "v_lshl_add_u32 v10, v3, 2, v10\n"
        "s_lshl_b32 s49, s35, 6\n"
        "s_add_u32 m0, s40, 88064\n"
        "s_sub_u32 s64, s47, 11796480\n"
        "buffer_load_dwordx4 v244, s[8:11], s64 offen nt lds\n"
        "s_add_u32 s49, s49, 128\n"
        "v_add_u32 v250, s49, v10\n"
        "s_mul_i32 s26, s35, 192\n"
        "s_sub_u32 s26, s26, 128\n"
        "s_mul_i32 s43, s31, 384\n"
        "s_add_u32 s44, s31, 4\n"
        "s_and_b32 s44, s44, 511\n"
        "s_mul_i32 s44, s44, 384\n"
        "s_add_u32 s37, s45, s66\n"
        "s_add_u32 s37, s37, 4\n"
        "s_mul_i32 s37, s37, 196608\n"
        "s_mul_i32 s51, s51, 36864\n"
        "s_mov_b32 m0, s48\n"
        "s_sub_u32 s64, s36, 4194304\n"
        "buffer_load_dwordx4 v249, s[12:15], s64 offen nt lds\n"
        "s_mul_i32 s66, s35, 18432\n"
        "s_add_u32 s66, s66, s51\n"
        "v_add_u32 v12, s51, v12\n"
        "v_bfrev_b32 v14, v0\n"
        "v_bfe_u32 v10, v14, 28, 2\n"
        "v_mad_u32_u24 v10, v10, 6, v7\n"
        "v_bfe_u32 v13, v0, 1, 3\n"
        "v_mad_u32_u24 v13, v13, 6, v11\n"
        "v_add_u32 v13, 4, v13\n"
        "v_xor_b32 v10, v10, v4\n"
        "v_xor_b32 v13, v13, v4\n"
        "v_lshl_add_u32 v236, v10, 4, v12\n"
        "buffer_load_dwordx4 v149, s[12:15], s64 offen offset:1024 nt lds\n"
        "v_lshl_add_u32 v237, v13, 4, v12\n"
        "v_lshrrev_b32 v10, 2, v1\n"
        "v_mul_u32_u24 v10, 192, v10\n"
        "v_add_u32 v10, s66, v10\n"
        "v_lshlrev_b32 v13, 5, v4\n"
        "v_add_u32 v12, v10, v13\n"
        "v_sub_u32 v13, v10, v13\n"
        "v_lshrrev_b32 v14, 30, v14\n"
        "v_lshl_add_u32 v234, v14, 3, v12\n"
        "v_lshl_add_u32 v235, v14, 3, v13\n"
        "v_lshl_add_u32 v238, v7, 3, v12\n"
        "v_lshl_add_u32 v239, v7, 3, v13\n"
        "s_lshl_b32 s46, s35, 8\n"
        "s_mul_i32 s35, s35, -192\n"
        "s_movk_i32 s49, 1536\n"
        "v_mul_u32_u24 v10, 171, v1\n"
        "v_lshrrev_b32 v10, 11, v10\n"
        "v_mad_i32_i24 v13, v10, -12, v1\n"
        "v_lshrrev_b32 v13, 3, v13\n"
        "v_mul_u32_u24 v14, 192, v10\n"
        "v_add_u32 v14, s46, v14\n"
        "v_mad_i32_i24 v14, v13, s35, v14\n"
        "v_and_b32 v13, 4, v10\n"
        "v_lshlrev_b32 v13, 3, v13\n"
        "v_xad_u32 v124, v150, v13, v14\n"
        "v_lshrrev_b32 v13, 2, v10\n"
        "v_and_or_b32 v13, v10, 8, v13\n"
        "v_mad_u32_u24 v127, v13, s49, v124\n"
        "v_add_u32 v12, 64, v1\n"
        "v_mul_u32_u24 v10, 171, v12\n"
        "v_lshrrev_b32 v10, 11, v10\n"
        "v_mad_i32_i24 v13, v10, -12, v12\n"
        "v_lshrrev_b32 v13, 3, v13\n"
        "v_mul_u32_u24 v14, 192, v10\n"
        "v_add_u32 v14, s46, v14\n"
        "v_mad_i32_i24 v14, v13, s35, v14\n"
        "v_and_b32 v13, 4, v10\n"
        "v_lshlrev_b32 v13, 3, v13\n"
        "v_xad_u32 v125, v150, v13, v14\n"
        "v_lshrrev_b32 v13, 2, v10\n"
        "v_and_or_b32 v13, v10, 8, v13\n"
        "v_mad_u32_u24 v232, v13, s49, v125\n"
        "v_add_u32 v12, 128, v1\n"
        "v_mul_u32_u24 v10, 171, v12\n"
        "v_lshrrev_b32 v10, 11, v10\n"
        "v_mad_i32_i24 v13, v10, -12, v12\n"
        "v_lshrrev_b32 v13, 3, v13\n"
        "v_mul_u32_u24 v14, 192, v10\n"
        "v_add_u32 v14, s46, v14\n"
        "v_mad_i32_i24 v14, v13, s35, v14\n"
        "v_and_b32 v13, 4, v10\n"
        "v_lshlrev_b32 v13, 3, v13\n"
        "v_xad_u32 v126, v150, v13, v14\n"
        "v_lshrrev_b32 v13, 2, v10\n"
        "v_and_or_b32 v13, v10, 8, v13\n"
        "v_mad_u32_u24 v233, v13, s49, v126\n"
        "s_mov_b32 s24, 0x3fb8aa3b\n"
        "s_mov_b32 s25, 0x4f800000\n"
        "s_mov_b32 s50, 0x3e553b95\n"
        "s_mov_b32 s27, 13\n"
        "s_mov_b32 s28, 98304\n"
        "s_mov_b32 s30, 73728\n"
        "s_mov_b32 s29, 122880\n"
        "v_mov_b32 v253, 0x3f803f80\n"
        "s_nop 0\n"
        "v_accvgpr_write_b32 a252, v253\n"
        "v_accvgpr_write_b32 a253, v253\n"
        "v_accvgpr_write_b32 a254, v253\n"
        "v_accvgpr_write_b32 a255, v253\n"
        "s_waitcnt vmcnt(12)\n"
        "v_lshlrev_b32 v32, 16, v28\n"
        "v_and_b32 v33, 0xffff0000, v28\n"
        "v_lshlrev_b32 v34, 16, v29\n"
        "v_and_b32 v35, 0xffff0000, v29\n"
        "v_mul_f32 v32, s24, v32\n"
        "v_mul_f32 v33, s24, v33\n"
        "v_mul_f32 v34, s24, v34\n"
        "v_mul_f32 v35, s24, v35\n"
        "ds_write_b32 v146, v32 offset:0\n"
        "ds_write_b32 v146, v33 offset:1024\n"
        "ds_write_b32 v146, v34 offset:2048\n"
        "ds_write_b32 v146, v35 offset:3072\n"
        "s_waitcnt vmcnt(8) lgkmcnt(0)\n"
        "s_barrier\n"
        "ds_read_b128 v[0:3], v140 offset:0\n"
        "ds_read_b128 v[4:7], v141 offset:0\n"
        "ds_read_b128 v[8:11], v142 offset:0\n"
        "ds_read_b128 v[12:15], v143 offset:0\n"
        "ds_read_b128 v[16:19], v140 offset:1024\n"
        "ds_read_b128 v[20:23], v141 offset:1024\n"
        "ds_read_b128 v[24:27], v142 offset:1024\n"
        "ds_read_b128 v[28:31], v143 offset:1024\n"
        "ds_read_b128 v[32:35], v140 offset:8192\n"
        "ds_read_b128 v[36:39], v141 offset:8192\n"
        "ds_read_b128 v[40:43], v142 offset:8192\n"
        "ds_read_b128 v[44:47], v143 offset:8192\n"
        "ds_read_b128 v[48:51], v140 offset:9216\n"
        "ds_read_b128 v[52:55], v141 offset:9216\n"
        "ds_read_b128 v[56:59], v142 offset:9216\n"
        "ds_read_b128 v[60:63], v143 offset:9216\n"
        "s_add_u32 m0, s40, 0\n"
        "s_add_u32 s64, s65, 0\n"
        "buffer_load_dwordx4 v128, s[20:23], s64 offen lds\n"
        "s_waitcnt lgkmcnt(15)\n"
        "v_lshl_add_u32 v0, v0, 2, s67\n"
        "v_lshl_add_u32 v1, v1, 2, s67\n"
        "v_lshl_add_u32 v2, v2, 2, s67\n"
        "v_lshl_add_u32 v3, v3, 2, s67\n"
        "s_waitcnt lgkmcnt(14)\n"
        "v_lshl_add_u32 v4, v4, 2, s67\n"
        "v_lshl_add_u32 v5, v5, 2, s67\n"
        "v_lshl_add_u32 v6, v6, 2, s67\n"
        "v_lshl_add_u32 v7, v7, 2, s67\n"
        "ds_read_b32 v0, v0\n"
        "ds_read_b32 v1, v1\n"
        "ds_read_b32 v2, v2\n"
        "ds_read_b32 v3, v3\n"
        "ds_read_b32 v4, v4\n"
        "ds_read_b32 v5, v5\n"
        "ds_read_b32 v6, v6\n"
        "ds_read_b32 v7, v7\n"
        "buffer_load_dwordx4 v129, s[20:23], s64 offen offset:1024 lds\n"
        "s_waitcnt lgkmcnt(15)\n"
        "v_lshl_add_u32 v8, v8, 2, s67\n"
        "v_lshl_add_u32 v9, v9, 2, s67\n"
        "v_lshl_add_u32 v10, v10, 2, s67\n"
        "v_lshl_add_u32 v11, v11, 2, s67\n"
        "v_lshl_add_u32 v12, v12, 2, s67\n"
        "v_lshl_add_u32 v13, v13, 2, s67\n"
        "v_lshl_add_u32 v14, v14, 2, s67\n"
        "v_lshl_add_u32 v15, v15, 2, s67\n"
        "ds_read_b32 v8, v8\n"
        "ds_read_b32 v9, v9\n"
        "ds_read_b32 v10, v10\n"
        "ds_read_b32 v11, v11\n"
        "ds_read_b32 v12, v12\n"
        "ds_read_b32 v13, v13\n"
        "ds_read_b32 v14, v14\n"
        "ds_read_b32 v15, v15\n"
        "buffer_load_dwordx4 v130, s[20:23], s64 offen offset:2048 lds\n"
        "v_lshl_add_u32 v16, v16, 2, s67\n"
        "v_lshl_add_u32 v17, v17, 2, s67\n"
        "v_lshl_add_u32 v18, v18, 2, s67\n"
        "v_lshl_add_u32 v19, v19, 2, s67\n"
        "v_lshl_add_u32 v20, v20, 2, s67\n"
        "v_lshl_add_u32 v21, v21, 2, s67\n"
        "v_lshl_add_u32 v22, v22, 2, s67\n"
        "v_lshl_add_u32 v23, v23, 2, s67\n"
        "ds_read_b32 v16, v16\n"
        "ds_read_b32 v17, v17\n"
        "ds_read_b32 v18, v18\n"
        "ds_read_b32 v19, v19\n"
        "ds_read_b32 v20, v20\n"
        "ds_read_b32 v21, v21\n"
        "ds_read_b32 v22, v22\n"
        "ds_read_b32 v23, v23\n"
        "s_add_u32 m0, s40, 12288\n"
        "s_add_u32 s64, s65, 36864\n"
        "buffer_load_dwordx4 v128, s[20:23], s64 offen lds\n"
        "v_lshl_add_u32 v24, v24, 2, s67\n"
        "v_lshl_add_u32 v25, v25, 2, s67\n"
        "v_lshl_add_u32 v26, v26, 2, s67\n"
        "v_lshl_add_u32 v27, v27, 2, s67\n"
        "v_lshl_add_u32 v28, v28, 2, s67\n"
        "v_lshl_add_u32 v29, v29, 2, s67\n"
        "v_lshl_add_u32 v30, v30, 2, s67\n"
        "v_lshl_add_u32 v31, v31, 2, s67\n"
        "ds_read_b32 v24, v24\n"
        "ds_read_b32 v25, v25\n"
        "ds_read_b32 v26, v26\n"
        "ds_read_b32 v27, v27\n"
        "ds_read_b32 v28, v28\n"
        "ds_read_b32 v29, v29\n"
        "ds_read_b32 v30, v30\n"
        "ds_read_b32 v31, v31\n"
        "buffer_load_dwordx4 v129, s[20:23], s64 offen offset:1024 lds\n"
        "buffer_load_dwordx4 v130, s[20:23], s64 offen offset:2048 lds\n"
        "s_mov_b64 exec, 0\n"
        "s_cmp_eq_u32 s42, 0\n"
        "s_cbranch_scc1 41f\n"
        "s_cmp_eq_u32 s42, 1\n"
        "s_cbranch_scc1 42f\n"
        "s_cmp_eq_u32 s42, 2\n"
        "s_cbranch_scc1 43f\n"
        "s_branch 40f\n"
        ".fill 356, 4, 0xbf800000\n"
        "40:\n"
        "s_mov_b64 exec, -1\n"
        "s_add_u32 m0, s40, 24576\n"
        "s_add_u32 s64, s65, 73728\n"
        "buffer_load_dwordx4 v128, s[20:23], s64 offen lds\n"
        "v_lshl_add_u32 v32, v32, 2, s67\n"
        "v_lshl_add_u32 v33, v33, 2, s67\n"
        "v_lshl_add_u32 v34, v34, 2, s67\n"
        "v_lshl_add_u32 v35, v35, 2, s67\n"
        "s_waitcnt lgkmcnt(15)\n"
        "v_lshl_add_u32 v36, v36, 2, s67\n"
        "v_lshl_add_u32 v37, v37, 2, s67\n"
        "v_lshl_add_u32 v38, v38, 2, s67\n"
        "v_lshl_add_u32 v39, v39, 2, s67\n"
        "ds_read_b32 v32, v32\n"
        "ds_read_b32 v33, v33\n"
        "ds_read_b32 v34, v34\n"
        "ds_read_b32 v35, v35\n"
        "ds_read_b32 v36, v36\n"
        "ds_read_b32 v37, v37\n"
        "ds_read_b32 v38, v38\n"
        "ds_read_b32 v39, v39\n"
        "buffer_load_dwordx4 v129, s[20:23], s64 offen offset:1024 lds\n"
        "v_lshl_add_u32 v40, v40, 2, s67\n"
        "v_lshl_add_u32 v41, v41, 2, s67\n"
        "v_lshl_add_u32 v42, v42, 2, s67\n"
        "v_lshl_add_u32 v43, v43, 2, s67\n"
        "v_lshl_add_u32 v44, v44, 2, s67\n"
        "v_lshl_add_u32 v45, v45, 2, s67\n"
        "v_lshl_add_u32 v46, v46, 2, s67\n"
        "v_lshl_add_u32 v47, v47, 2, s67\n"
        "ds_read_b32 v40, v40\n"
        "ds_read_b32 v41, v41\n"
        "ds_read_b32 v42, v42\n"
        "ds_read_b32 v43, v43\n"
        "ds_read_b32 v44, v44\n"
        "ds_read_b32 v45, v45\n"
        "ds_read_b32 v46, v46\n"
        "ds_read_b32 v47, v47\n"
        "buffer_load_dwordx4 v130, s[20:23], s64 offen offset:2048 lds\n"
        "v_lshl_add_u32 v48, v48, 2, s67\n"
        "v_lshl_add_u32 v49, v49, 2, s67\n"
        "v_lshl_add_u32 v50, v50, 2, s67\n"
        "v_lshl_add_u32 v51, v51, 2, s67\n"
        "v_lshl_add_u32 v52, v52, 2, s67\n"
        "v_lshl_add_u32 v53, v53, 2, s67\n"
        "v_lshl_add_u32 v54, v54, 2, s67\n"
        "v_lshl_add_u32 v55, v55, 2, s67\n"
        "ds_read_b32 v48, v48\n"
        "ds_read_b32 v49, v49\n"
        "ds_read_b32 v50, v50\n"
        "ds_read_b32 v51, v51\n"
        "ds_read_b32 v52, v52\n"
        "ds_read_b32 v53, v53\n"
        "ds_read_b32 v54, v54\n"
        "ds_read_b32 v55, v55\n"
        "s_add_u32 m0, s40, 36864\n"
        "s_add_u32 s64, s65, 110592\n"
        "buffer_load_dwordx4 v128, s[20:23], s64 offen lds\n"
        "v_lshl_add_u32 v56, v56, 2, s67\n"
        "v_lshl_add_u32 v57, v57, 2, s67\n"
        "v_lshl_add_u32 v58, v58, 2, s67\n"
        "v_lshl_add_u32 v59, v59, 2, s67\n"
        "v_lshl_add_u32 v60, v60, 2, s67\n"
        "v_lshl_add_u32 v61, v61, 2, s67\n"
        "v_lshl_add_u32 v62, v62, 2, s67\n"
        "v_lshl_add_u32 v63, v63, 2, s67\n"
        "ds_read_b32 v56, v56\n"
        "ds_read_b32 v57, v57\n"
        "ds_read_b32 v58, v58\n"
        "ds_read_b32 v59, v59\n"
        "ds_read_b32 v60, v60\n"
        "ds_read_b32 v61, v61\n"
        "ds_read_b32 v62, v62\n"
        "ds_read_b32 v63, v63\n"
        "buffer_load_dwordx4 v129, s[20:23], s64 offen offset:1024 lds\n"
        "buffer_load_dwordx4 v130, s[20:23], s64 offen offset:2048 lds\n"
        "s_add_u32 m0, s40, 49152\n"
        "s_add_u32 s64, s65, 147456\n"
        "buffer_load_dwordx4 v128, s[20:23], s64 offen lds\n"
        "buffer_load_dwordx4 v129, s[20:23], s64 offen offset:1024 lds\n"
        "buffer_load_dwordx4 v130, s[20:23], s64 offen offset:2048 lds\n"
        "s_add_u32 m0, s40, 61440\n"
        "s_add_u32 s64, s65, 184320\n"
        "buffer_load_dwordx4 v128, s[20:23], s64 offen lds\n"
        "buffer_load_dwordx4 v129, s[20:23], s64 offen offset:1024 lds\n"
        "buffer_load_dwordx4 v130, s[20:23], s64 offen offset:2048 lds\n"
        "s_waitcnt vmcnt(12)\n"
        "s_barrier\n"
        "ds_read_b64_tr_b16 a[0:1], v131 offset:0\n"
        "ds_read_b128 a[144:147], v240 offset:0\n"
        "ds_read_b64_tr_b16 a[2:3], v131 offset:1536\n"
        "ds_read_b64_tr_b16 a[4:5], v131 offset:12288\n"
        "ds_read_b128 a[148:151], v241 offset:0\n"
        "ds_read_b64_tr_b16 a[6:7], v131 offset:13824\n"
        "ds_read_b64_tr_b16 a[24:25], v132 offset:0\n"
        "ds_read_b64_tr_b16 a[26:27], v132 offset:1536\n"
        "ds_read_b64_tr_b16 a[28:29], v132 offset:12288\n"
        "ds_read_b64_tr_b16 a[30:31], v132 offset:13824\n"
        "ds_read_b64_tr_b16 a[48:49], v133 offset:0\n"
        "ds_read_b64_tr_b16 a[50:51], v133 offset:1536\n"
        "ds_read_b64_tr_b16 a[52:53], v133 offset:12288\n"
        "ds_read_b64_tr_b16 a[54:55], v133 offset:13824\n"
        "ds_read_b128 a[160:163], v240 offset:1536\n"
        "ds_read_b128 a[164:167], v241 offset:1536\n"
        "s_waitcnt lgkmcnt(13)\n"
        "v_mfma_f32_16x16x32_bf16 v[96:99], a[0:3], a[144:147], 0\n"
        "s_waitcnt lgkmcnt(10)\n"
        "s_add_u32 m0, s40, 98304\n"
        "s_add_u32 s64, s65, 384\n"
        "ds_read_b128 a[176:179], v240 offset:12288\n"
        "v_mfma_f32_16x16x32_bf16 v[96:99], a[4:7], a[148:151], v[96:99]\n"
        "s_waitcnt lgkmcnt(7)\n"
        "buffer_load_dwordx4 v128, s[20:23], s64 offen lds\n"
        "v_mfma_f32_16x16x32_bf16 v[100:103], a[28:31], a[148:151], 0\n"
        "ds_read_b128 a[180:183], v241 offset:12288\n"
        "v_mfma_f32_16x16x32_bf16 v[100:103], a[24:27], a[144:147], v[100:103]\n"
        "s_waitcnt lgkmcnt(6)\n"
        "ds_read_b128 a[192:195], v240 offset:13824\n"
        "v_mfma_f32_16x16x32_bf16 v[104:107], a[48:51], a[144:147], 0\n"
        "s_waitcnt lgkmcnt(5)\n"
        "ds_read_b128 a[196:199], v241 offset:13824\n"
        "v_mfma_f32_16x16x32_bf16 v[104:107], a[52:55], a[148:151], v[104:107]\n"
        "s_waitcnt lgkmcnt(4)\n"
        "buffer_load_dwordx4 v129, s[20:23], s64 offen offset:1024 lds\n"
        "v_mfma_f32_16x16x32_bf16 v[108:111], a[4:7], a[164:167], 0\n"
        "ds_read_b128 a[152:155], v240 offset:128\n"
        "v_mfma_f32_16x16x32_bf16 v[108:111], a[0:3], a[160:163], v[108:111]\n"
        "ds_read_b128 a[156:159], v241 offset:128\n"
        "v_mfma_f32_16x16x32_bf16 v[112:115], a[24:27], a[160:163], 0\n"
        "ds_read_b128 a[168:171], v240 offset:1664\n"
        "v_mfma_f32_16x16x32_bf16 v[112:115], a[28:31], a[164:167], v[112:115]\n"
        "buffer_load_dwordx4 v130, s[20:23], s64 offen offset:2048 lds\n"
        "v_mfma_f32_16x16x32_bf16 v[116:119], a[52:55], a[164:167], 0\n"
        "s_add_u32 m0, s40, 110592\n"
        "ds_read_b128 a[172:175], v241 offset:1664\n"
        "v_mfma_f32_16x16x32_bf16 v[116:119], a[48:51], a[160:163], v[116:119]\n"
        "s_waitcnt lgkmcnt(7)\n"
        "s_add_u32 s64, s65, 37248\n"
        "v_mfma_f32_16x16x32_bf16 v[120:123], a[0:3], a[176:179], 0\n"
        "s_waitcnt vmcnt(9) lgkmcnt(6)\n"
        "s_barrier\n"
        "ds_read_b128 a[184:187], v240 offset:12416\n"
        "v_mfma_f32_16x16x32_bf16 v[120:123], a[4:7], a[180:183], v[120:123]\n"
        "buffer_load_dwordx4 v128, s[20:23], s64 offen lds\n"
        "v_mfma_f32_16x16x32_bf16 v[92:95], a[28:31], a[180:183], 0\n"
        "ds_read_b64_tr_b16 a[8:9], v131 offset:24576\n"
        "ds_read_b64_tr_b16 a[10:11], v131 offset:26112\n"
        "v_mfma_f32_16x16x32_bf16 v[92:95], a[24:27], a[176:179], v[92:95]\n"
        "ds_read_b64_tr_b16 a[12:13], v131 offset:36864\n"
        "v_mfma_f32_16x16x32_bf16 v[152:155], a[48:51], a[176:179], 0\n"
        "ds_read_b64_tr_b16 a[14:15], v131 offset:38400\n"
        "v_mfma_f32_16x16x32_bf16 v[152:155], a[52:55], a[180:183], v[152:155]\n"
        "s_waitcnt lgkmcnt(9)\n"
        "buffer_load_dwordx4 v129, s[20:23], s64 offen offset:1024 lds\n"
        "v_mfma_f32_16x16x32_bf16 v[172:175], a[4:7], a[196:199], 0\n"
        "ds_read_b64_tr_b16 a[32:33], v132 offset:24576\n"
        "ds_read_b64_tr_b16 a[34:35], v132 offset:26112\n"
        "v_mfma_f32_16x16x32_bf16 v[172:175], a[0:3], a[192:195], v[172:175]\n"
        "ds_read_b64_tr_b16 a[36:37], v132 offset:36864\n"
        "v_mfma_f32_16x16x32_bf16 v[192:195], a[24:27], a[192:195], 0\n"
        "ds_read_b64_tr_b16 a[38:39], v132 offset:38400\n"
        "v_mfma_f32_16x16x32_bf16 v[192:195], a[28:31], a[196:199], v[192:195]\n"
        "buffer_load_dwordx4 v130, s[20:23], s64 offen offset:2048 lds\n"
        "v_mfma_f32_16x16x32_bf16 v[212:215], a[52:55], a[196:199], 0\n"
        "s_add_u32 m0, s40, 0\n"
        "ds_read_b64_tr_b16 a[56:57], v133 offset:24576\n"
        "ds_read_b64_tr_b16 a[58:59], v133 offset:26112\n"
        "v_mfma_f32_16x16x32_bf16 v[212:215], a[48:51], a[192:195], v[212:215]\n"
        "s_waitcnt lgkmcnt(8)\n"
        "s_add_u32 s64, s65, 74112\n"
        "ds_read_b64_tr_b16 a[60:61], v133 offset:36864\n"
        "v_mfma_f32_16x16x32_bf16 v[96:99], a[8:11], a[152:155], v[96:99]\n"
        "s_waitcnt lgkmcnt(7)\n"
        "ds_read_b64_tr_b16 a[62:63], v133 offset:38400\n"
        "v_mfma_f32_16x16x32_bf16 v[96:99], a[12:15], a[156:159], v[96:99]\n"
        "s_waitcnt lgkmcnt(4)\n"
        "buffer_load_dwordx4 v128, s[20:23], s64 offen lds\n"
        "v_mfma_f32_16x16x32_bf16 v[100:103], a[36:39], a[156:159], v[100:103]\n"
        "ds_read_b128 a[188:191], v241 offset:12416\n"
        "v_mfma_f32_16x16x32_bf16 v[100:103], a[32:35], a[152:155], v[100:103]\n"
        "s_waitcnt lgkmcnt(3)\n"
        "ds_read_b128 a[200:203], v240 offset:13952\n"
        "v_mfma_f32_16x16x32_bf16 v[104:107], a[56:59], a[152:155], v[104:107]\n"
        "s_waitcnt lgkmcnt(2)\n"
        "ds_read_b128 a[204:207], v241 offset:13952\n"
        "v_mfma_f32_16x16x32_bf16 v[104:107], a[60:63], a[156:159], v[104:107]\n"
        "buffer_load_dwordx4 v129, s[20:23], s64 offen offset:1024 lds\n"
        "v_mfma_f32_16x16x32_bf16 v[108:111], a[12:15], a[172:175], v[108:111]\n"
        "ds_read_b128 a[216:219], v240 offset:256\n"
        "v_mfma_f32_16x16x32_bf16 v[108:111], a[8:11], a[168:171], v[108:111]\n"
        "ds_read_b128 a[220:223], v241 offset:256\n"
        "v_mfma_f32_16x16x32_bf16 v[112:115], a[32:35], a[168:171], v[112:115]\n"
        "ds_read_b128 a[224:227], v240 offset:1792\n"
        "v_mfma_f32_16x16x32_bf16 v[112:115], a[36:39], a[172:175], v[112:115]\n"
        "s_waitcnt vmcnt(8)\n"
        "buffer_load_dwordx4 v130, s[20:23], s64 offen offset:2048 lds\n"
        "v_mfma_f32_16x16x32_bf16 v[116:119], a[60:63], a[172:175], v[116:119]\n"
        "s_barrier\n"
        "ds_read_b128 a[228:231], v241 offset:1792\n"
        "v_mfma_f32_16x16x32_bf16 v[116:119], a[56:59], a[168:171], v[116:119]\n"
        "s_add_u32 m0, s40, 12288\n"
        "ds_read_b64_tr_b16 a[16:17], v131 offset:49152\n"
        "v_mfma_f32_16x16x32_bf16 v[120:123], a[8:11], a[184:187], v[120:123]\n"
        "s_waitcnt lgkmcnt(7)\n"
        "s_add_u32 s64, s65, 110976\n"
        "ds_read_b64_tr_b16 a[18:19], v131 offset:50688\n"
        "v_mfma_f32_16x16x32_bf16 v[120:123], a[12:15], a[188:191], v[120:123]\n"
        "buffer_load_dwordx4 v128, s[20:23], s64 offen lds\n"
        "v_mfma_f32_16x16x32_bf16 v[92:95], a[36:39], a[188:191], v[92:95]\n"
        "ds_read_b64_tr_b16 a[20:21], v131 offset:61440\n"
        "ds_read_b64_tr_b16 a[22:23], v131 offset:62976\n"
        "v_mfma_f32_16x16x32_bf16 v[92:95], a[32:35], a[184:187], v[92:95]\n"
        "ds_read_b64_tr_b16 a[40:41], v132 offset:49152\n"
        "v_mfma_f32_16x16x32_bf16 v[152:155], a[56:59], a[184:187], v[152:155]\n"
        "ds_read_b64_tr_b16 a[42:43], v132 offset:50688\n"
        "v_mfma_f32_16x16x32_bf16 v[152:155], a[60:63], a[188:191], v[152:155]\n"
        "s_waitcnt lgkmcnt(10)\n"
        "buffer_load_dwordx4 v129, s[20:23], s64 offen offset:1024 lds\n"
        "v_mfma_f32_16x16x32_bf16 v[172:175], a[12:15], a[204:207], v[172:175]\n"
        "ds_read_b64_tr_b16 a[44:45], v132 offset:61440\n"
        "ds_read_b64_tr_b16 a[46:47], v132 offset:62976\n"
        "v_mfma_f32_16x16x32_bf16 v[172:175], a[8:11], a[200:203], v[172:175]\n"
        "ds_read_b64_tr_b16 a[64:65], v133 offset:49152\n"
        "v_mfma_f32_16x16x32_bf16 v[192:195], a[32:35], a[200:203], v[192:195]\n"
        "ds_read_b64_tr_b16 a[66:67], v133 offset:50688\n"
        "v_mfma_f32_16x16x32_bf16 v[192:195], a[36:39], a[204:207], v[192:195]\n"
        "buffer_load_dwordx4 v130, s[20:23], s64 offen offset:2048 lds\n"
        "v_mfma_f32_16x16x32_bf16 v[212:215], a[60:63], a[204:207], v[212:215]\n"
        "s_add_u32 m0, s40, 24576\n"
        "ds_read_b64_tr_b16 a[68:69], v133 offset:61440\n"
        "ds_read_b64_tr_b16 a[70:71], v133 offset:62976\n"
        "v_mfma_f32_16x16x32_bf16 v[212:215], a[56:59], a[200:203], v[212:215]\n"
        "s_waitcnt lgkmcnt(10)\n"
        "s_add_u32 s64, s65, 147840\n"
        "ds_read_b128 a[232:235], v240 offset:12544\n"
        "v_mfma_f32_16x16x32_bf16 v[96:99], a[16:19], a[216:219], v[96:99]\n"
        "s_waitcnt lgkmcnt(9)\n"
        "ds_read_b128 a[236:239], v241 offset:12544\n"
        "v_mfma_f32_16x16x32_bf16 v[96:99], a[20:23], a[220:223], v[96:99]\n"
        "s_waitcnt lgkmcnt(6)\n"
        "buffer_load_dwordx4 v128, s[20:23], s64 offen lds\n"
        "v_mfma_f32_16x16x32_bf16 v[100:103], a[44:47], a[220:223], v[100:103]\n"
        "ds_read_b128 a[240:243], v240 offset:14080\n"
        "v_mfma_f32_16x16x32_bf16 v[100:103], a[40:43], a[216:219], v[100:103]\n"
        "s_waitcnt lgkmcnt(5)\n"
        "ds_read_b128 a[244:247], v241 offset:14080\n"
        "v_mfma_f32_16x16x32_bf16 v[104:107], a[64:67], a[216:219], v[104:107]\n"
        "s_waitcnt lgkmcnt(4)\n"
        "v_mfma_f32_16x16x32_bf16 v[104:107], a[68:71], a[220:223], v[104:107]\n"
        "buffer_load_dwordx4 v129, s[20:23], s64 offen offset:1024 lds\n"
        "v_mfma_f32_16x16x32_bf16 v[108:111], a[20:23], a[228:231], v[108:111]\n"
        "v_mul_f32 v96, s50, v96\n"
        "v_mul_f32 v97, s50, v97\n"
        "v_mfma_f32_16x16x32_bf16 v[108:111], a[16:19], a[224:227], v[108:111]\n"
        "v_mul_f32 v98, s50, v98\n"
        "v_mul_f32 v99, s50, v99\n"
        "v_mfma_f32_16x16x32_bf16 v[112:115], a[40:43], a[224:227], v[112:115]\n"
        "s_waitcnt vmcnt(8)\n"
        "v_cvt_pk_bf16_f32 v156, v96, v97\n"
        "v_cvt_pk_bf16_f32 v157, v98, v99\n"
        "v_mfma_f32_16x16x32_bf16 v[112:115], a[44:47], a[228:231], v[112:115]\n"
        "s_barrier\n"
        "buffer_load_dwordx4 v130, s[20:23], s64 offen offset:2048 lds\n"
        "v_mfma_f32_16x16x32_bf16 v[116:119], a[68:71], a[228:231], v[116:119]\n"
        "s_add_u32 m0, s40, 36864\n"
        "ds_read_b64_tr_b16 a[72:73], v136 offset:0\n"
        "v_mul_f32 v100, s50, v100\n"
        "v_mfma_f32_16x16x32_bf16 v[116:119], a[64:67], a[224:227], v[116:119]\n"
        "s_waitcnt lgkmcnt(4)\n"
        "s_add_u32 s64, s65, 184704\n"
        "ds_read_b64_tr_b16 a[74:75], v136 offset:1536\n"
        "v_mul_f32 v101, s50, v101\n"
        "v_mfma_f32_16x16x32_bf16 v[120:123], a[16:19], a[232:235], v[120:123]\n"
        "s_waitcnt lgkmcnt(4)\n"
        "ds_read_b64_tr_b16 a[76:77], v136 offset:12288\n"
        "v_mul_f32 v102, s50, v102\n"
        "v_mfma_f32_16x16x32_bf16 v[120:123], a[20:23], a[236:239], v[120:123]\n"
        "buffer_load_dwordx4 v128, s[20:23], s64 offen lds\n"
        "v_mfma_f32_16x16x32_bf16 v[92:95], a[44:47], a[236:239], v[92:95]\n"
        "ds_read_b64_tr_b16 a[78:79], v136 offset:13824\n"
        "ds_read_b64_tr_b16 a[96:97], v137 offset:0\n"
        "v_mfma_f32_16x16x32_bf16 v[92:95], a[40:43], a[232:235], v[92:95]\n"
        "ds_read_b64_tr_b16 a[98:99], v137 offset:1536\n"
        "v_mul_f32 v103, s50, v103\n"
        "v_mfma_f32_16x16x32_bf16 v[152:155], a[64:67], a[232:235], v[152:155]\n"
        "ds_read_b64_tr_b16 a[100:101], v137 offset:12288\n"
        "v_cvt_pk_bf16_f32 v158, v100, v101\n"
        "v_mfma_f32_16x16x32_bf16 v[152:155], a[68:71], a[236:239], v[152:155]\n"
        "s_waitcnt lgkmcnt(7)\n"
        "buffer_load_dwordx4 v129, s[20:23], s64 offen offset:1024 lds\n"
        "v_mfma_f32_16x16x32_bf16 v[172:175], a[20:23], a[244:247], v[172:175]\n"
        "ds_read_b64_tr_b16 a[102:103], v137 offset:13824\n"
        "ds_read_b64_tr_b16 a[120:121], v138 offset:0\n"
        "v_mfma_f32_16x16x32_bf16 v[172:175], a[16:19], a[240:243], v[172:175]\n"
        "ds_read_b64_tr_b16 a[122:123], v138 offset:1536\n"
        "v_cvt_pk_bf16_f32 v159, v102, v103\n"
        "v_mfma_f32_16x16x32_bf16 v[192:195], a[40:43], a[240:243], v[192:195]\n"
        "ds_read_b64_tr_b16 a[124:125], v138 offset:12288\n"
        "v_mul_f32 v104, s50, v104\n"
        "v_mfma_f32_16x16x32_bf16 v[192:195], a[44:47], a[244:247], v[192:195]\n"
        "buffer_load_dwordx4 v130, s[20:23], s64 offen offset:2048 lds\n"
        "v_mfma_f32_16x16x32_bf16 v[212:215], a[68:71], a[244:247], v[212:215]\n"
        "s_add_u32 m0, s40, 49152\n"
        "ds_read_b64_tr_b16 a[126:127], v138 offset:13824\n"
        "v_mul_f32 v105, s50, v105\n"
        "v_mfma_f32_16x16x32_bf16 v[212:215], a[64:67], a[240:243], v[212:215]\n"
        "s_waitcnt lgkmcnt(10)\n"
        "s_add_u32 s64, s65, 768\n"
        "v_mul_f32 v106, s50, v106\n"
        "v_mul_f32 v107, s50, v107\n"
        "v_mfma_f32_16x16x32_bf16 v[64:67], a[72:75], a[144:147], 0\n"
        "s_waitcnt lgkmcnt(8)\n"
        "v_cvt_pk_bf16_f32 v160, v104, v105\n"
        "v_cvt_pk_bf16_f32 v161, v106, v107\n"
        "v_mfma_f32_16x16x32_bf16 v[64:67], a[76:79], a[148:151], v[64:67]\n"
        "s_waitcnt lgkmcnt(4)\n"
        "buffer_load_dwordx4 v128, s[20:23], s64 offen lds\n"
        "v_mfma_f32_16x16x32_bf16 v[68:71], a[100:103], a[148:151], 0\n"
        "v_mul_f32 v108, s50, v108\n"
        "v_mul_f32 v109, s50, v109\n"
        "v_mfma_f32_16x16x32_bf16 v[68:71], a[96:99], a[144:147], v[68:71]\n"
        "s_waitcnt lgkmcnt(2)\n"
        "v_mov_b64 v[166:167], v[160:161]\n"
        "v_mul_f32 v110, s50, v110\n"
        "v_mfma_f32_16x16x32_bf16 v[72:75], a[120:123], a[144:147], 0\n"
        "s_waitcnt lgkmcnt(0)\n"
        "v_mul_f32 v111, s50, v111\n"
        "v_cvt_pk_bf16_f32 v176, v108, v109\n"
        "v_mfma_f32_16x16x32_bf16 v[72:75], a[124:127], a[148:151], v[72:75]\n"
        "buffer_load_dwordx4 v129, s[20:23], s64 offen offset:1024 lds\n"
        "v_mfma_f32_16x16x32_bf16 v[76:79], a[76:79], a[164:167], 0\n"
        "v_cvt_pk_bf16_f32 v177, v110, v111\n"
        "v_mul_f32 v112, s50, v112\n"
        "v_mfma_f32_16x16x32_bf16 v[76:79], a[72:75], a[160:163], v[76:79]\n"
        "s_waitcnt vmcnt(8)\n"
        "v_mul_f32 v113, s50, v113\n"
        "v_mul_f32 v114, s50, v114\n"
        "v_mfma_f32_16x16x32_bf16 v[80:83], a[96:99], a[160:163], 0\n"
        "s_barrier\n"
        "v_mul_f32 v115, s50, v115\n"
        "v_cvt_pk_bf16_f32 v178, v112, v113\n"
        "v_mfma_f32_16x16x32_bf16 v[80:83], a[100:103], a[164:167], v[80:83]\n"
        "buffer_load_dwordx4 v130, s[20:23], s64 offen offset:2048 lds\n"
        "v_mfma_f32_16x16x32_bf16 v[84:87], a[124:127], a[164:167], 0\n"
        "s_add_u32 m0, s40, 61440\n"
        "ds_read_b64_tr_b16 a[80:81], v131 offset:0\n"
        "ds_read_b64_tr_b16 a[82:83], v131 offset:1536\n"
        "v_mfma_f32_16x16x32_bf16 v[84:87], a[120:123], a[160:163], v[84:87]\n"
        "s_add_u32 s64, s65, 37632\n"
        "ds_read_b64_tr_b16 a[84:85], v131 offset:12288\n"
        "v_cvt_pk_bf16_f32 v179, v114, v115\n"
        "v_mfma_f32_16x16x32_bf16 v[88:91], a[72:75], a[176:179], 0\n"
        "ds_read_b64_tr_b16 a[86:87], v131 offset:13824\n"
        "v_mul_f32 v116, s50, v116\n"
        "v_mfma_f32_16x16x32_bf16 v[88:91], a[76:79], a[180:183], v[88:91]\n"
        "buffer_load_dwordx4 v128, s[20:23], s64 offen lds\n"
        "v_mfma_f32_16x16x32_bf16 v[108:111], a[100:103], a[180:183], 0\n"
        "ds_read_b64_tr_b16 a[104:105], v132 offset:0\n"
        "ds_read_b64_tr_b16 a[106:107], v132 offset:1536\n"
        "v_mfma_f32_16x16x32_bf16 v[108:111], a[96:99], a[176:179], v[108:111]\n"
        "ds_read_b64_tr_b16 a[108:109], v132 offset:12288\n"
        "v_mul_f32 v117, s50, v117\n"
        "v_mfma_f32_16x16x32_bf16 v[112:115], a[120:123], a[176:179], 0\n"
        "ds_read_b64_tr_b16 a[110:111], v132 offset:13824\n"
        "v_mul_f32 v118, s50, v118\n"
        "v_mfma_f32_16x16x32_bf16 v[112:115], a[124:127], a[180:183], v[112:115]\n"
        "buffer_load_dwordx4 v129, s[20:23], s64 offen offset:1024 lds\n"
        "v_mfma_f32_16x16x32_bf16 v[96:99], a[76:79], a[196:199], 0\n"
        "ds_read_b64_tr_b16 a[128:129], v133 offset:0\n"
        "ds_read_b64_tr_b16 a[130:131], v133 offset:1536\n"
        "v_mfma_f32_16x16x32_bf16 v[96:99], a[72:75], a[192:195], v[96:99]\n"
        "ds_read_b64_tr_b16 a[132:133], v133 offset:12288\n"
        "v_mul_f32 v119, s50, v119\n"
        "v_mfma_f32_16x16x32_bf16 v[100:103], a[96:99], a[192:195], 0\n"
        "ds_read_b64_tr_b16 a[134:135], v133 offset:13824\n"
        "v_cvt_pk_bf16_f32 v180, v116, v117\n"
        "v_mfma_f32_16x16x32_bf16 v[100:103], a[100:103], a[196:199], v[100:103]\n"
        "buffer_load_dwordx4 v130, s[20:23], s64 offen offset:2048 lds\n"
        "v_mfma_f32_16x16x32_bf16 v[104:107], a[124:127], a[196:199], 0\n"
        "v_cvt_pk_bf16_f32 v181, v118, v119\n"
        "v_mul_f32 v120, s50, v120\n"
        "v_mfma_f32_16x16x32_bf16 v[104:107], a[120:123], a[192:195], v[104:107]\n"
        "s_waitcnt lgkmcnt(10)\n"
        "v_mul_f32 v121, s50, v121\n"
        "v_mov_b64 v[186:187], v[180:181]\n"
        "v_mfma_f32_16x16x32_bf16 v[64:67], a[80:83], a[152:155], v[64:67]\n"
        "s_waitcnt lgkmcnt(8)\n"
        "v_mul_f32 v122, s50, v122\n"
        "v_mul_f32 v123, s50, v123\n"
        "v_mfma_f32_16x16x32_bf16 v[64:67], a[84:87], a[156:159], v[64:67]\n"
        "s_waitcnt lgkmcnt(4)\n"
        "v_cvt_pk_bf16_f32 v196, v120, v121\n"
        "v_cvt_pk_bf16_f32 v197, v122, v123\n"
        "v_mfma_f32_16x16x32_bf16 v[68:71], a[108:111], a[156:159], v[68:71]\n"
        "v_mul_f32 v92, s50, v92\n"
        "v_mul_f32 v93, s50, v93\n"
        "v_mfma_f32_16x16x32_bf16 v[68:71], a[104:107], a[152:155], v[68:71]\n"
        "s_waitcnt lgkmcnt(2)\n"
        "v_mul_f32 v94, s50, v94\n"
        "v_mul_f32 v95, s50, v95\n"
        "v_mfma_f32_16x16x32_bf16 v[72:75], a[128:131], a[152:155], v[72:75]\n"
        "s_waitcnt lgkmcnt(0)\n"
        "v_cvt_pk_bf16_f32 v198, v92, v93\n"
        "v_cvt_pk_bf16_f32 v199, v94, v95\n"
        "v_mfma_f32_16x16x32_bf16 v[72:75], a[132:135], a[156:159], v[72:75]\n"
        "v_mul_f32 v152, s50, v152\n"
        "v_mul_f32 v153, s50, v153\n"
        "v_mfma_f32_16x16x32_bf16 v[76:79], a[84:87], a[172:175], v[76:79]\n"
        "v_mul_f32 v154, s50, v154\n"
        "v_mul_f32 v155, s50, v155\n"
        "v_mfma_f32_16x16x32_bf16 v[76:79], a[80:83], a[168:171], v[76:79]\n"
        "s_waitcnt vmcnt(6)\n"
        "v_cvt_pk_bf16_f32 v200, v152, v153\n"
        "v_cvt_pk_bf16_f32 v201, v154, v155\n"
        "v_mfma_f32_16x16x32_bf16 v[80:83], a[104:107], a[168:171], v[80:83]\n"
        "s_barrier\n"
        "s_add_u32 m0, s40, 0\n"
        "v_mul_f32 v172, s50, v172\n"
        "v_mfma_f32_16x16x32_bf16 v[80:83], a[108:111], a[172:175], v[80:83]\n"
        "s_add_u32 s64, s65, 74496\n"
        "ds_read_b64_tr_b16 a[88:89], v131 offset:24576\n"
        "v_mul_f32 v173, s50, v173\n"
        "v_mfma_f32_16x16x32_bf16 v[84:87], a[132:135], a[172:175], v[84:87]\n"
        "ds_read_b64_tr_b16 a[90:91], v131 offset:26112\n"
        "v_mov_b64 v[206:207], v[200:201]\n"
        "v_mfma_f32_16x16x32_bf16 v[84:87], a[128:131], a[168:171], v[84:87]\n"
        "buffer_load_dwordx4 v128, s[20:23], s64 offen lds\n"
        "v_mfma_f32_16x16x32_bf16 v[88:91], a[80:83], a[184:187], v[88:91]\n"
        "ds_read_b64_tr_b16 a[92:93], v131 offset:36864\n"
        "ds_read_b64_tr_b16 a[94:95], v131 offset:38400\n"
        "v_mfma_f32_16x16x32_bf16 v[88:91], a[84:87], a[188:191], v[88:91]\n"
        "ds_read_b64_tr_b16 a[112:113], v132 offset:24576\n"
        "v_mul_f32 v174, s50, v174\n"
        "v_mfma_f32_16x16x32_bf16 v[108:111], a[108:111], a[188:191], v[108:111]\n"
        "ds_read_b64_tr_b16 a[114:115], v132 offset:26112\n"
        "v_mul_f32 v175, s50, v175\n"
        "v_mfma_f32_16x16x32_bf16 v[108:111], a[104:107], a[184:187], v[108:111]\n"
        "buffer_load_dwordx4 v129, s[20:23], s64 offen offset:1024 lds\n"
        "v_mfma_f32_16x16x32_bf16 v[112:115], a[128:131], a[184:187], v[112:115]\n"
        "ds_read_b64_tr_b16 a[116:117], v132 offset:36864\n"
        "ds_read_b64_tr_b16 a[118:119], v132 offset:38400\n"
        "v_mfma_f32_16x16x32_bf16 v[112:115], a[132:135], a[188:191], v[112:115]\n"
        "ds_read_b64_tr_b16 a[136:137], v133 offset:24576\n"
        "v_cvt_pk_bf16_f32 v216, v172, v173\n"
        "v_mfma_f32_16x16x32_bf16 v[96:99], a[84:87], a[204:207], v[96:99]\n"
        "ds_read_b64_tr_b16 a[138:139], v133 offset:26112\n"
        "v_cvt_pk_bf16_f32 v217, v174, v175\n"
        "v_mfma_f32_16x16x32_bf16 v[96:99], a[80:83], a[200:203], v[96:99]\n"
        "buffer_load_dwordx4 v130, s[20:23], s64 offen offset:2048 lds\n"
        "v_mfma_f32_16x16x32_bf16 v[100:103], a[104:107], a[200:203], v[100:103]\n"
        "s_add_u32 m0, s40, 12288\n"
        "ds_read_b64_tr_b16 a[140:141], v133 offset:36864\n"
        "ds_read_b64_tr_b16 a[142:143], v133 offset:38400\n"
        "v_mfma_f32_16x16x32_bf16 v[100:103], a[108:111], a[204:207], v[100:103]\n"
        "s_add_u32 s64, s65, 111360\n"
        "v_mul_f32 v192, s50, v192\n"
        "v_mul_f32 v193, s50, v193\n"
        "v_mfma_f32_16x16x32_bf16 v[104:107], a[132:135], a[204:207], v[104:107]\n"
        "buffer_load_dwordx4 v128, s[20:23], s64 offen lds\n"
        "v_mfma_f32_16x16x32_bf16 v[104:107], a[128:131], a[200:203], v[104:107]\n"
        "s_waitcnt lgkmcnt(10)\n"
        "v_mul_f32 v194, s50, v194\n"
        "v_mul_f32 v195, s50, v195\n"
        "v_mfma_f32_16x16x32_bf16 v[64:67], a[88:91], a[216:219], v[64:67]\n"
        "s_waitcnt lgkmcnt(8)\n"
        "v_cvt_pk_bf16_f32 v218, v192, v193\n"
        "v_cvt_pk_bf16_f32 v219, v194, v195\n"
        "v_mfma_f32_16x16x32_bf16 v[64:67], a[92:95], a[220:223], v[64:67]\n"
        "s_waitcnt lgkmcnt(4)\n"
        "v_mul_f32 v212, s50, v212\n"
        "v_mul_f32 v213, s50, v213\n"
        "v_mfma_f32_16x16x32_bf16 v[68:71], a[116:119], a[220:223], v[68:71]\n"
        "buffer_load_dwordx4 v129, s[20:23], s64 offen offset:1024 lds\n"
        "v_mfma_f32_16x16x32_bf16 v[68:71], a[112:115], a[216:219], v[68:71]\n"
        "s_waitcnt lgkmcnt(2)\n"
        "v_mul_f32 v214, s50, v214\n"
        "v_mul_f32 v215, s50, v215\n"
        "v_mfma_f32_16x16x32_bf16 v[72:75], a[136:139], a[216:219], v[72:75]\n"
        "s_waitcnt lgkmcnt(0)\n"
        "v_cvt_pk_bf16_f32 v220, v212, v213\n"
        "v_cvt_pk_bf16_f32 v221, v214, v215\n"
        "v_mfma_f32_16x16x32_bf16 v[72:75], a[140:143], a[220:223], v[72:75]\n"
        "v_mov_b64 v[226:227], v[220:221]\n"
        "v_cvt_pk_bf16_f32 v152, v64, v65\n"
        "v_mfma_f32_16x16x32_bf16 v[76:79], a[92:95], a[228:231], v[76:79]\n"
        "buffer_load_dwordx4 v130, s[20:23], s64 offen offset:2048 lds\n"
        "v_mfma_f32_16x16x32_bf16 v[76:79], a[88:91], a[224:227], v[76:79]\n"
        "v_cvt_pk_bf16_f32 v153, v66, v67\n"
        "v_cvt_pk_bf16_f32 v154, v68, v69\n"
        "v_mfma_f32_16x16x32_bf16 v[80:83], a[112:115], a[224:227], v[80:83]\n"
        "v_cvt_pk_bf16_f32 v155, v70, v71\n"
        "v_cvt_pk_bf16_f32 v168, v72, v73\n"
        "v_mfma_f32_16x16x32_bf16 v[80:83], a[116:119], a[228:231], v[80:83]\n"
        "ds_read_b128 v[162:165], v251 offset:0\n"
        "v_cvt_pk_bf16_f32 v169, v74, v75\n"
        "v_mfma_f32_16x16x32_bf16 v[84:87], a[140:143], a[228:231], v[84:87]\n"
        "v_cvt_pk_bf16_f32 v172, v76, v77\n"
        "v_cvt_pk_bf16_f32 v173, v78, v79\n"
        "v_mfma_f32_16x16x32_bf16 v[84:87], a[136:139], a[224:227], v[84:87]\n"
        "s_waitcnt vmcnt(6)\n"
        "ds_read_b128 v[182:185], v251 offset:512\n"
        "v_mfma_f32_16x16x32_bf16 v[88:91], a[88:91], a[232:235], v[88:91]\n"
        "s_barrier\n"
        "s_add_u32 m0, s40, 24576\n"
        "v_cvt_pk_bf16_f32 v174, v80, v81\n"
        "v_mfma_f32_16x16x32_bf16 v[88:91], a[92:95], a[236:239], v[88:91]\n"
        "s_add_u32 s64, s65, 148224\n"
        "ds_read_b64_tr_b16 a[144:145], v131 offset:49152\n"
        "v_cvt_pk_bf16_f32 v175, v82, v83\n"
        "v_mfma_f32_16x16x32_bf16 v[108:111], a[116:119], a[236:239], v[108:111]\n"
        "ds_read_b64_tr_b16 a[146:147], v131 offset:50688\n"
        "v_cvt_pk_bf16_f32 v190, v84, v85\n"
        "v_mfma_f32_16x16x32_bf16 v[108:111], a[112:115], a[232:235], v[108:111]\n"
        "buffer_load_dwordx4 v128, s[20:23], s64 offen lds\n"
        "v_mfma_f32_16x16x32_bf16 v[112:115], a[136:139], a[232:235], v[112:115]\n"
        "ds_read_b64_tr_b16 a[148:149], v131 offset:61440\n"
        "ds_read_b64_tr_b16 a[150:151], v131 offset:62976\n"
        "v_mfma_f32_16x16x32_bf16 v[112:115], a[140:143], a[236:239], v[112:115]\n"
        "ds_read_b64_tr_b16 a[168:169], v132 offset:49152\n"
        "v_cvt_pk_bf16_f32 v191, v86, v87\n"
        "v_mfma_f32_16x16x32_bf16 v[96:99], a[92:95], a[244:247], v[96:99]\n"
        "ds_read_b64_tr_b16 a[170:171], v132 offset:50688\n"
        "v_cvt_pk_bf16_f32 v192, v88, v89\n"
        "v_mfma_f32_16x16x32_bf16 v[96:99], a[88:91], a[240:243], v[96:99]\n"
        "buffer_load_dwordx4 v129, s[20:23], s64 offen offset:1024 lds\n"
        "v_mfma_f32_16x16x32_bf16 v[100:103], a[112:115], a[240:243], v[100:103]\n"
        "ds_read_b64_tr_b16 a[172:173], v132 offset:61440\n"
        "ds_read_b64_tr_b16 a[174:175], v132 offset:62976\n"
        "v_mfma_f32_16x16x32_bf16 v[100:103], a[116:119], a[244:247], v[100:103]\n"
        "ds_read_b64_tr_b16 a[192:193], v133 offset:49152\n"
        "v_cvt_pk_bf16_f32 v193, v90, v91\n"
        "v_mfma_f32_16x16x32_bf16 v[104:107], a[140:143], a[244:247], v[104:107]\n"
        "ds_read_b64_tr_b16 a[194:195], v133 offset:50688\n"
        "ds_read_b128 v[202:205], v251 offset:4096\n"
        "v_mfma_f32_16x16x32_bf16 v[104:107], a[136:139], a[240:243], v[104:107]\n"
        "buffer_load_dwordx4 v130, s[20:23], s64 offen offset:2048 lds\n"
        "v_mfma_f32_16x16x32_bf16 v[64:67], v[152:155], v[156:159], v[0:3]\n"
        "41:\n"
        "s_cbranch_execz 44f\n"
        "s_waitcnt lgkmcnt(12)\n"
        "s_add_u32 m0, s40, 36864\n"
        "ds_read_b64_tr_b16 a[196:197], v133 offset:61440\n"
        "ds_read_b64_tr_b16 a[198:199], v133 offset:62976\n"
        "v_mfma_f32_16x16x32_bf16 v[64:67], v[168:171], v[160:163], v[64:67]\n"
        "s_add_u32 s64, s65, 185088\n"
        "v_cvt_pk_bf16_f32 v194, v108, v109\n"
        "v_cvt_pk_bf16_f32 v195, v110, v111\n"
        "v_mfma_f32_16x16x32_bf16 v[68:71], v[172:175], v[156:159], v[4:7]\n"
        "buffer_load_dwordx4 v128, s[20:23], s64 offen lds\n"
        "v_mfma_f32_16x16x32_bf16 v[68:71], v[188:191], v[164:167], v[68:71]\n"
        "ds_read_b128 v[222:225], v251 offset:4608\n"
        "v_cvt_pk_bf16_f32 v208, v112, v113\n"
        "v_mfma_f32_16x16x32_bf16 v[72:75], v[152:155], v[176:179], v[16:19]\n"
        "s_waitcnt lgkmcnt(14)\n"
        "v_cvt_pk_bf16_f32 v209, v114, v115\n"
        "v_cvt_pk_bf16_f32 v212, v96, v97\n"
        "v_mfma_f32_16x16x32_bf16 v[72:75], v[168:171], v[180:183], v[72:75]\n"
        "v_cvt_pk_bf16_f32 v213, v98, v99\n"
        "v_cvt_pk_bf16_f32 v214, v100, v101\n"
        "v_mfma_f32_16x16x32_bf16 v[76:79], v[172:175], v[176:179], v[20:23]\n"
        "buffer_load_dwordx4 v129, s[20:23], s64 offen offset:1024 lds\n"
        "v_mfma_f32_16x16x32_bf16 v[76:79], v[188:191], v[184:187], v[76:79]\n"
        "ds_read_b128 v[162:165], v252 offset:0\n"
        "v_cvt_pk_bf16_f32 v215, v102, v103\n"
        "v_mfma_f32_16x16x32_bf16 v[80:83], v[152:155], v[196:199], v[32:35]\n"
        "s_waitcnt lgkmcnt(4)\n"
        "ds_read_b128 v[182:185], v252 offset:512\n"
        "v_cvt_pk_bf16_f32 v230, v104, v105\n"
        "v_mfma_f32_16x16x32_bf16 v[80:83], v[168:171], v[200:203], v[80:83]\n"
        "ds_read_b128 a[248:251], v240 offset:0\n"
        "v_cvt_pk_bf16_f32 v231, v106, v107\n"
        "v_mfma_f32_16x16x32_bf16 v[84:87], v[172:175], v[196:199], v[36:39]\n"
        "buffer_load_dwordx4 v130, s[20:23], s64 offen offset:2048 lds\n"
        "v_mfma_f32_16x16x32_bf16 v[84:87], v[188:191], v[204:207], v[84:87]\n"
        "s_add_u32 m0, s40, 98304\n"
        "ds_read_b128 v[202:205], v252 offset:4096\n"
        "v_mfma_f32_16x16x32_bf16 v[88:91], v[152:155], v[216:219], v[48:51]\n"
        "s_waitcnt vmcnt(6) lgkmcnt(4)\n"
        "s_barrier\n"
        "v_exp_f32 v64, v64\n"
        "v_mfma_f32_16x16x32_bf16 v[88:91], v[168:171], v[220:223], v[88:91]\n"
        "ds_read_b64_tr_b16 a[152:153], v131 offset:0\n"
        "ds_read_b128 a[216:219], v241 offset:0\n"
        "v_mfma_f32_16x16x32_bf16 v[92:95], v[172:175], v[216:219], v[52:55]\n"
        "ds_read_b64_tr_b16 a[154:155], v131 offset:1536\n"
        "ds_read_b128 a[220:223], v240 offset:128\n"
        "v_mfma_f32_16x16x32_bf16 v[92:95], v[188:191], v[224:227], v[92:95]\n"
        "buffer_load_dword v148, s[52:55], 0 offen lds\n"
        "v_mfma_f32_16x16x32_bf16 v[108:111], v[212:215], v[156:159], v[12:15]\n"
        "s_waitcnt lgkmcnt(7)\n"
        "s_add_u32 m0, s40, 98304\n"
        "s_sub_u32 s64, s47, 6291456\n"
        "ds_read_b64_tr_b16 a[156:157], v131 offset:12288\n"
        "v_mfma_f32_16x16x32_bf16 v[108:111], v[228:231], v[164:167], v[108:111]\n"
        "ds_read_b128 a[224:227], v241 offset:128\n"
        "ds_read_b64_tr_b16 a[158:159], v131 offset:13824\n"
        "v_mfma_f32_16x16x32_bf16 v[112:115], v[212:215], v[176:179], v[28:31]\n"
        "s_waitcnt lgkmcnt(9)\n"
        "buffer_load_dwordx4 v242, s[8:11], s64 offen lds\n"
        "v_mfma_f32_16x16x32_bf16 v[112:115], v[228:231], v[184:187], v[112:115]\n"
        "s_add_u32 m0, s40, 99328\n"
        "s_sub_u32 s64, s47, 6291456\n"
        "ds_read_b64_tr_b16 a[176:177], v132 offset:0\n"
        "v_mfma_f32_16x16x32_bf16 v[116:119], v[212:215], v[196:199], v[44:47]\n"
        "s_waitcnt lgkmcnt(8)\n"
        "ds_read_b64_tr_b16 a[178:179], v132 offset:1536\n"
        "ds_read_b64_tr_b16 a[180:181], v132 offset:12288\n"
        "v_mfma_f32_16x16x32_bf16 v[116:119], v[228:231], v[204:207], v[116:119]\n"
        "ds_read_b64_tr_b16 a[182:183], v132 offset:13824\n"
        "ds_read_b64_tr_b16 a[200:201], v133 offset:0\n"
        "v_mfma_f32_16x16x32_bf16 v[120:123], a[248:251], a[144:147], 0\n"
        "s_waitcnt lgkmcnt(10)\n"
        "buffer_load_dwordx4 v243, s[8:11], s64 offen lds\n"
        "v_mfma_f32_16x16x32_bf16 v[120:123], a[216:219], a[148:151], v[120:123]\n"
        "s_waitcnt lgkmcnt(8)\n"
        "s_add_u32 m0, s40, 100352\n"
        "s_sub_u32 s64, s47, 6291456\n"
        "ds_read_b64_tr_b16 a[202:203], v133 offset:1536\n"
        "v_mfma_f32_16x16x32_bf16 v[120:123], a[220:223], a[152:155], v[120:123]\n"
        "s_waitcnt lgkmcnt(6)\n"
        "ds_read_b64_tr_b16 a[204:205], v133 offset:12288\n"
        "ds_read_b64_tr_b16 a[206:207], v133 offset:13824\n"
        "v_mfma_f32_16x16x32_bf16 v[120:123], a[224:227], a[156:159], v[120:123]\n"
        "s_waitcnt lgkmcnt(4)\n"
        "buffer_load_dwordx4 v244, s[8:11], s64 offen lds\n"
        "v_mfma_f32_16x16x32_bf16 v[96:99], a[224:227], a[180:183], 0\n"
        "s_add_u32 m0, s40, 110592\n"
        "ds_read_b128 a[228:231], v240 offset:1536\n"
        "v_mfma_f32_16x16x32_bf16 v[96:99], a[220:223], a[176:179], v[96:99]\n"
        "s_sub_u32 s64, s47, 5505024\n"
        "ds_read_b128 a[232:235], v241 offset:1536\n"
        "v_mfma_f32_16x16x32_bf16 v[96:99], a[216:219], a[172:175], v[96:99]\n"
        "buffer_load_dwordx4 v242, s[8:11], s64 offen lds\n"
        "v_mfma_f32_16x16x32_bf16 v[96:99], a[248:251], a[168:171], v[96:99]\n"
        "s_add_u32 m0, s40, 111616\n"
        "ds_read_b128 a[236:239], v240 offset:1664\n"
        "ds_read_b128 a[240:243], v241 offset:1664\n"
        "v_mfma_f32_16x16x32_bf16 v[100:103], a[248:251], a[192:195], 0\n"
        "s_sub_u32 s64, s47, 5505024\n"
        "v_exp_f32 v65, v65\n"
        "v_mfma_f32_16x16x32_bf16 v[100:103], a[216:219], a[196:199], v[100:103]\n"
        "s_waitcnt lgkmcnt(6)\n"
        "v_exp_f32 v66, v66\n"
        "v_mfma_f32_16x16x32_bf16 v[100:103], a[220:223], a[200:203], v[100:103]\n"
        "s_waitcnt lgkmcnt(4)\n"
        "buffer_load_dwordx4 v243, s[8:11], s64 offen lds\n"
        "v_mfma_f32_16x16x32_bf16 v[100:103], a[224:227], a[204:207], v[100:103]\n"
        "s_add_u32 m0, s40, 112640\n"
        "ds_read_b128 a[244:247], v240 offset:12288\n"
        "v_cvt_pk_bf16_f32 v64, v64, v65\n"
        "v_exp_f32 v68, v68\n"
        "v_exp_f32 v67, v67\n"
        "v_exp_f32 v69, v69\n"
        "v_cvt_pk_bf16_f32 v65, v66, v67\n"
        "v_exp_f32 v70, v70\n"
        "v_cvt_pk_bf16_f32 v66, v68, v69\n"
        "v_exp_f32 v71, v71\n"
        "v_exp_f32 v72, v72\n"
        "v_cvt_pk_bf16_f32 v67, v70, v71\n"
        "v_mfma_f32_16x16x32_bf16 v[68:71], v[192:195], v[156:159], v[8:11]\n"
        "s_sub_u32 s64, s47, 5505024\n"
        "v_exp_f32 v73, v73\n"
        "v_mfma_f32_16x16x32_bf16 v[68:71], v[208:211], v[160:163], v[68:71]\n"
        "s_waitcnt lgkmcnt(1)\n"
        "v_exp_f32 v74, v74\n"
        "v_mfma_f32_16x16x32_bf16 v[104:107], a[240:243], a[156:159], 0\n"
        "buffer_load_dwordx4 v244, s[8:11], s64 offen lds\n"
        "v_mfma_f32_16x16x32_bf16 v[104:107], a[236:239], a[152:155], v[104:107]\n"
        "s_xor_b32 m0, s48, 8192\n"
        "ds_read_b128 a[248:251], v241 offset:12288\n"
        "v_cvt_pk_bf16_f32 v72, v72, v73\n"
        "v_mfma_f32_16x16x32_bf16 v[104:107], a[232:235], a[148:151], v[104:107]\n"
        "s_sub_u32 s64, s36, 2097152\n"
        "v_exp_f32 v75, v75\n"
        "v_mfma_f32_16x16x32_bf16 v[104:107], a[228:231], a[144:147], v[104:107]\n"
        "s_waitcnt vmcnt(7)\n"
        "ds_read_b128 a[216:219], v240 offset:12416\n"
        "v_cvt_pk_bf16_f32 v73, v74, v75\n"
        "v_mfma_f32_16x16x32_bf16 v[156:159], a[228:231], a[168:171], 0\n"
        "s_barrier\n"
        "buffer_load_dwordx4 v249, s[12:15], s64 offen lds\n"
        "v_mfma_f32_16x16x32_bf16 v[156:159], a[232:235], a[172:175], v[156:159]\n"
        "ds_read_b64_tr_b16 a[160:161], v131 offset:24576\n"
        "v_mfma_f32_16x16x32_bf16 v[156:159], a[236:239], a[176:179], v[156:159]\n"
        "ds_read_b64_tr_b16 a[162:163], v131 offset:26112\n"
        "v_mfma_f32_16x16x32_bf16 v[156:159], a[240:243], a[180:183], v[156:159]\n"
        "ds_read_b64_tr_b16 a[164:165], v131 offset:36864\n"
        "ds_read_b128 a[220:223], v241 offset:12416\n"
        "v_mfma_f32_16x16x32_bf16 v[160:163], a[240:243], a[204:207], 0\n"
        "ds_read_b64_tr_b16 a[166:167], v131 offset:38400\n"
        "v_mfma_f32_16x16x32_bf16 v[160:163], a[236:239], a[200:203], v[160:163]\n"
        "buffer_load_dwordx4 v149, s[12:15], s64 offen offset:1024 lds\n"
        "v_mfma_f32_16x16x32_bf16 v[160:163], a[232:235], a[196:199], v[160:163]\n"
        "ds_read_b64_tr_b16 a[184:185], v132 offset:24576\n"
        "ds_read_b64_tr_b16 a[186:187], v132 offset:26112\n"
        "v_mfma_f32_16x16x32_bf16 v[160:163], a[228:231], a[192:195], v[160:163]\n"
        "ds_read_b64_tr_b16 a[188:189], v132 offset:36864\n"
        "v_exp_f32 v76, v76\n"
        "v_exp_f32 v77, v77\n"
        "v_exp_f32 v78, v78\n"
        "v_cvt_pk_bf16_f32 v74, v76, v77\n"
        "v_exp_f32 v79, v79\n"
        "v_exp_f32 v80, v80\n"
        "v_cvt_pk_bf16_f32 v75, v78, v79\n"
        "v_mfma_f32_16x16x32_bf16 v[76:79], v[192:195], v[176:179], v[24:27]\n"
        "ds_read_b64_tr_b16 a[190:191], v132 offset:38400\n"
        "v_mfma_f32_16x16x32_bf16 v[76:79], v[208:211], v[180:183], v[76:79]\n"
        "s_waitcnt lgkmcnt(11)\n"
        "ds_read_b64_tr_b16 a[208:209], v133 offset:24576\n"
        "v_mfma_f32_16x16x32_bf16 v[164:167], a[244:247], a[144:147], 0\n"
        "s_waitcnt lgkmcnt(11)\n"
        "ds_read_b64_tr_b16 a[210:211], v133 offset:26112\n"
        "v_mfma_f32_16x16x32_bf16 v[164:167], a[248:251], a[148:151], v[164:167]\n"
        "s_waitcnt lgkmcnt(11)\n"
        "ds_read_b64_tr_b16 a[212:213], v133 offset:36864\n"
        "v_mfma_f32_16x16x32_bf16 v[164:167], a[216:219], a[152:155], v[164:167]\n"
        "s_waitcnt lgkmcnt(8)\n"
        "ds_read_b64_tr_b16 a[214:215], v133 offset:38400\n"
        "v_mfma_f32_16x16x32_bf16 v[164:167], a[220:223], a[156:159], v[164:167]\n"
        "v_exp_f32 v81, v81\n"
        "v_mfma_f32_16x16x32_bf16 v[176:179], a[220:223], a[180:183], 0\n"
        "ds_read_b128 a[224:227], v240 offset:13824\n"
        "v_cvt_pk_bf16_f32 v80, v80, v81\n"
        "v_mfma_f32_16x16x32_bf16 v[176:179], a[216:219], a[176:179], v[176:179]\n"
        "ds_read_b128 a[228:231], v241 offset:13824\n"
        "v_mfma_f32_16x16x32_bf16 v[176:179], a[248:251], a[172:175], v[176:179]\n"
        "ds_read_b128 a[232:235], v240 offset:13952\n"
        "v_mfma_f32_16x16x32_bf16 v[176:179], a[244:247], a[168:171], v[176:179]\n"
        "ds_read_b128 a[236:239], v241 offset:13952\n"
        "v_mfma_f32_16x16x32_bf16 v[180:183], a[244:247], a[192:195], 0\n"
        "v_exp_f32 v82, v82\n"
        "v_mfma_f32_16x16x32_bf16 v[180:183], a[248:251], a[196:199], v[180:183]\n"
        "v_exp_f32 v83, v83\n"
        "v_mfma_f32_16x16x32_bf16 v[180:183], a[216:219], a[200:203], v[180:183]\n"
        "ds_read_b128 v[222:225], v252 offset:4608\n"
        "v_cvt_pk_bf16_f32 v81, v82, v83\n"
        "v_mfma_f32_16x16x32_bf16 v[180:183], a[220:223], a[204:207], v[180:183]\n"
        "v_exp_f32 v84, v84\n"
        "v_exp_f32 v85, v85\n"
        "v_exp_f32 v86, v86\n"
        "v_cvt_pk_bf16_f32 v82, v84, v85\n"
        "v_exp_f32 v87, v87\n"
        "v_exp_f32 v88, v88\n"
        "v_cvt_pk_bf16_f32 v83, v86, v87\n"
        "v_mfma_f32_16x16x32_bf16 v[84:87], v[192:195], v[196:199], v[40:43]\n"
        "v_exp_f32 v89, v89\n"
        "v_mfma_f32_16x16x32_bf16 v[84:87], v[208:211], v[200:203], v[84:87]\n"
        "s_waitcnt lgkmcnt(1)\n"
        "v_exp_f32 v90, v90\n"
        "v_mfma_f32_16x16x32_bf16 v[184:187], a[236:239], a[156:159], 0\n"
        "ds_read_b128 a[240:243], v240 offset:256\n"
        "v_cvt_pk_bf16_f32 v88, v88, v89\n"
        "v_mfma_f32_16x16x32_bf16 v[184:187], a[232:235], a[152:155], v[184:187]\n"
        "v_exp_f32 v91, v91\n"
        "v_mfma_f32_16x16x32_bf16 v[184:187], a[228:231], a[148:151], v[184:187]\n"
        "ds_read_b128 a[244:247], v241 offset:256\n"
        "v_cvt_pk_bf16_f32 v89, v90, v91\n"
        "v_mfma_f32_16x16x32_bf16 v[184:187], a[224:227], a[144:147], v[184:187]\n"
        "v_exp_f32 v92, v92\n"
        "v_mfma_f32_16x16x32_bf16 v[196:199], a[224:227], a[168:171], 0\n"
        "v_exp_f32 v93, v93\n"
        "v_mfma_f32_16x16x32_bf16 v[196:199], a[228:231], a[172:175], v[196:199]\n"
        "v_exp_f32 v94, v94\n"
        "v_mfma_f32_16x16x32_bf16 v[196:199], a[232:235], a[176:179], v[196:199]\n"
        "ds_read_b128 a[248:251], v240 offset:1792\n"
        "v_cvt_pk_bf16_f32 v90, v92, v93\n"
        "v_mfma_f32_16x16x32_bf16 v[196:199], a[236:239], a[180:183], v[196:199]\n"
        "v_exp_f32 v95, v95\n"
        "v_mfma_f32_16x16x32_bf16 v[200:203], a[236:239], a[204:207], 0\n"
        "ds_read_b128 a[216:219], v241 offset:1792\n"
        "v_cvt_pk_bf16_f32 v91, v94, v95\n"
        "v_mfma_f32_16x16x32_bf16 v[200:203], a[232:235], a[200:203], v[200:203]\n"
        "v_exp_f32 v108, v108\n"
        "v_mfma_f32_16x16x32_bf16 v[200:203], a[228:231], a[196:199], v[200:203]\n"
        "v_exp_f32 v109, v109\n"
        "v_mfma_f32_16x16x32_bf16 v[200:203], a[224:227], a[192:195], v[200:203]\n"
        "v_exp_f32 v110, v110\n"
        "v_exp_f32 v111, v111\n"
        "v_exp_f32 v70, v70\n"
        "v_exp_f32 v69, v69\n"
        "v_exp_f32 v68, v68\n"
        "v_exp_f32 v71, v71\n"
        "v_cvt_pk_bf16_f32 v68, v68, v69\n"
        "v_cvt_pk_bf16_f32 v69, v70, v71\n"
        "v_cvt_pk_bf16_f32 v70, v108, v109\n"
        "v_cvt_pk_bf16_f32 v71, v110, v111\n"
        "v_mfma_f32_16x16x32_bf16 v[108:111], v[192:195], v[216:219], v[56:59]\n"
        "s_waitcnt lgkmcnt(4)\n"
        "v_exp_f32 v112, v112\n"
        "v_mfma_f32_16x16x32_bf16 v[108:111], v[208:211], v[220:223], v[108:111]\n"
        "v_exp_f32 v113, v113\n"
        "v_exp_f32 v114, v114\n"
        "v_exp_f32 v115, v115\n"
        "v_exp_f32 v78, v78\n"
        "v_exp_f32 v77, v77\n"
        "v_exp_f32 v76, v76\n"
        "v_exp_f32 v79, v79\n"
        "v_cvt_pk_bf16_f32 v76, v76, v77\n"
        "v_cvt_pk_bf16_f32 v77, v78, v79\n"
        "v_cvt_pk_bf16_f32 v78, v112, v113\n"
        "v_cvt_pk_bf16_f32 v79, v114, v115\n"
        "v_mfma_f32_16x16x32_bf16 v[112:115], v[212:215], v[216:219], v[60:63]\n"
        "v_exp_f32 v116, v116\n"
        "v_mfma_f32_16x16x32_bf16 v[112:115], v[228:231], v[224:227], v[112:115]\n"
        "s_waitcnt lgkmcnt(3)\n"
        "v_exp_f32 v117, v117\n"
        "v_mfma_f32_16x16x32_bf16 v[120:123], a[240:243], a[160:163], v[120:123]\n"
        "s_waitcnt lgkmcnt(2)\n"
        "v_exp_f32 v118, v118\n"
        "v_mfma_f32_16x16x32_bf16 v[120:123], a[244:247], a[164:167], v[120:123]\n"
        "v_exp_f32 v119, v119\n"
        "v_mfma_f32_16x16x32_bf16 v[96:99], a[244:247], a[188:191], v[96:99]\n"
        "ds_read_b128 a[220:223], v240 offset:12544\n"
        "v_mfma_f32_16x16x32_bf16 v[96:99], a[240:243], a[184:187], v[96:99]\n"
        "ds_read_b128 a[224:227], v241 offset:12544\n"
        "v_mfma_f32_16x16x32_bf16 v[100:103], a[240:243], a[208:211], v[100:103]\n"
        "v_exp_f32 v84, v84\n"
        "v_mfma_f32_16x16x32_bf16 v[100:103], a[244:247], a[212:215], v[100:103]\n"
        "s_waitcnt lgkmcnt(2)\n"
        "v_exp_f32 v85, v85\n"
        "v_mfma_f32_16x16x32_bf16 v[104:107], a[216:219], a[164:167], v[104:107]\n"
        "v_exp_f32 v86, v86\n"
        "v_mfma_f32_16x16x32_bf16 v[104:107], a[248:251], a[160:163], v[104:107]\n"
        "v_cvt_pk_bf16_f32 v84, v84, v85\n"
        "v_cvt_pk_bf16_f32 v128, v120, v121\n"
        "v_mfma_f32_16x16x32_bf16 v[156:159], a[248:251], a[184:187], v[156:159]\n"
        "ds_read_b128 a[228:231], v240 offset:14080\n"
        "v_cvt_pk_bf16_f32 v129, v122, v123\n"
        "v_mfma_f32_16x16x32_bf16 v[156:159], a[216:219], a[188:191], v[156:159]\n"
        "ds_read_b128 a[232:235], v241 offset:14080\n"
        "v_cvt_pk_bf16_f32 v136, v96, v97\n"
        "v_mfma_f32_16x16x32_bf16 v[160:163], a[216:219], a[212:215], v[160:163]\n"
        "v_exp_f32 v87, v87\n"
        "v_mfma_f32_16x16x32_bf16 v[160:163], a[248:251], a[208:211], v[160:163]\n"
        "s_waitcnt lgkmcnt(3)\n"
        "v_cvt_pk_bf16_f32 v85, v86, v87\n"
        "v_cvt_pk_bf16_f32 v86, v116, v117\n"
        "v_mfma_f32_16x16x32_bf16 v[164:167], a[220:223], a[160:163], v[164:167]\n"
        "s_waitcnt lgkmcnt(2)\n"
        "v_cvt_pk_bf16_f32 v87, v118, v119\n"
        "v_cvt_pk_bf16_f32 v137, v98, v99\n"
        "v_mfma_f32_16x16x32_bf16 v[164:167], a[224:227], a[164:167], v[164:167]\n"
        "v_exp_f32 v108, v108\n"
        "v_mfma_f32_16x16x32_bf16 v[176:179], a[224:227], a[188:191], v[176:179]\n"
        "v_exp_f32 v109, v109\n"
        "v_mfma_f32_16x16x32_bf16 v[176:179], a[220:223], a[184:187], v[176:179]\n"
        "s_waitcnt lgkmcnt(0)\n"
        "v_exp_f32 v110, v110\n"
        "v_mfma_f32_16x16x32_bf16 v[180:183], a[220:223], a[208:211], v[180:183]\n"
        "s_barrier\n"
        "v_cvt_pk_bf16_f32 v92, v108, v109\n"
        "v_cvt_pk_bf16_f32 v144, v100, v101\n"
        "v_mfma_f32_16x16x32_bf16 v[180:183], a[224:227], a[212:215], v[180:183]\n"
        "v_exp_f32 v111, v111\n"
        "v_mfma_f32_16x16x32_bf16 v[184:187], a[232:235], a[164:167], v[184:187]\n"
        "v_cvt_pk_bf16_f32 v93, v110, v111\n"
        "v_cvt_pk_bf16_f32 v145, v102, v103\n"
        "v_mfma_f32_16x16x32_bf16 v[184:187], a[228:231], a[160:163], v[184:187]\n"
        "v_exp_f32 v112, v112\n"
        "v_mfma_f32_16x16x32_bf16 v[196:199], a[228:231], a[184:187], v[196:199]\n"
        "v_exp_f32 v113, v113\n"
        "v_mfma_f32_16x16x32_bf16 v[196:199], a[232:235], a[188:191], v[196:199]\n"
        "v_exp_f32 v114, v114\n"
        "v_mfma_f32_16x16x32_bf16 v[200:203], a[232:235], a[212:215], v[200:203]\n"
        "v_cvt_pk_bf16_f32 v94, v112, v113\n"
        "v_cvt_pk_bf16_f32 v130, v104, v105\n"
        "v_mfma_f32_16x16x32_bf16 v[200:203], a[228:231], a[208:211], v[200:203]\n"
        "v_exp_f32 v115, v115\n"
        "v_mov_b64 v[120:121], 0\n"
        "s_nop 1\n"
        "v_smfmac_f32_16x16x64_bf16 v[120:123], a[252:255], v[64:71], v255\n"
        "v_cvt_pk_bf16_f32 v95, v114, v115\n"
        "v_cvt_pk_bf16_f32 v131, v106, v107\n"
        "s_nop 1\n"
        "v_mfma_f32_16x16x32_bf16 v[116:119], v[128:131], v[64:67], 0\n"
        "v_cvt_pk_bf16_f32 v138, v156, v157\n"
        "v_cvt_pk_bf16_f32 v139, v158, v159\n"
        "v_cvt_pk_bf16_f32 v132, v164, v165\n"
        "v_cvt_pk_bf16_f32 v133, v166, v167\n"
        "v_cvt_pk_bf16_f32 v134, v184, v185\n"
        "v_cvt_pk_bf16_f32 v135, v186, v187\n"
        "s_nop 1\n"
        "v_mfma_f32_16x16x32_bf16 v[116:119], v[132:135], v[68:71], v[116:119]\n"
        "v_cvt_pk_bf16_f32 v146, v160, v161\n"
        "v_cvt_pk_bf16_f32 v147, v162, v163\n"
        "v_cvt_pk_bf16_f32 v140, v176, v177\n"
        "v_cvt_pk_bf16_f32 v141, v178, v179\n"
        "v_cvt_pk_bf16_f32 v142, v196, v197\n"
        "v_cvt_pk_bf16_f32 v143, v198, v199\n"
        "s_nop 1\n"
        "v_mfma_f32_16x16x32_bf16 v[152:155], v[140:143], v[68:71], 0\n"
        "v_mov_b64 v[172:173], 0\n"
        "v_cvt_pk_bf16_f32 v148, v180, v181\n"
        "v_mfma_f32_16x16x32_bf16 v[152:155], v[136:139], v[64:67], v[152:155]\n"
        "v_cvt_pk_bf16_f32 v149, v182, v183\n"
        "v_cvt_pk_bf16_f32 v150, v200, v201\n"
        "v_mfma_f32_16x16x32_bf16 v[160:163], v[144:147], v[64:67], 0\n"
        "v_cvt_pk_bf16_f32 v151, v202, v203\n"
        "v_mov_b64 v[192:193], 0\n"
        "s_nop 0\n"
        "v_mfma_f32_16x16x32_bf16 v[160:163], v[148:151], v[68:71], v[160:163]\n"
        "v_add_f32 v120, v120, v121\n"
        "v_add_u32 v240, 24576, v240\n"
        "v_smfmac_f32_16x16x64_bf16 v[172:175], a[252:255], v[72:79], v255\n"
        "v_add_u32 v241, 24576, v241\n"
        "v_mfma_f32_16x16x32_bf16 v[176:179], v[128:131], v[72:75], 0\n"
        "v_rcp_f32 v121, v120\n"
        "v_mfma_f32_16x16x32_bf16 v[176:179], v[132:135], v[76:79], v[176:179]\n"
        "v_mov_b64 v[212:213], 0\n"
        "v_mul_f32 v116, v121, v116\n"
        "v_mfma_f32_16x16x32_bf16 v[180:183], v[140:143], v[76:79], 0\n"
        "v_mul_f32 v117, v121, v117\n"
        "v_mul_f32 v118, v121, v118\n"
        "v_mfma_f32_16x16x32_bf16 v[180:183], v[136:139], v[72:75], v[180:183]\n"
        "v_mul_f32 v119, v121, v119\n"
        "v_cvt_pk_bf16_f32 v116, v116, v117\n"
        "v_mfma_f32_16x16x32_bf16 v[184:187], v[144:147], v[72:75], 0\n"
        "v_cvt_pk_bf16_f32 v117, v118, v119\n"
        "v_mul_f32 v152, v121, v152\n"
        "v_mfma_f32_16x16x32_bf16 v[184:187], v[148:151], v[76:79], v[184:187]\n"
        "s_waitcnt vmcnt(5)\n"
        "v_mul_f32 v153, v121, v153\n"
        "v_maximum3_f32 v253, v253, v120, v121\n"
        "v_smfmac_f32_16x16x64_bf16 v[192:195], a[252:255], v[80:87], v255\n"
        "s_barrier\n"
        "ds_read_b128 a[236:239], v240 offset:0\n"
        "v_mul_f32 v154, v121, v154\n"
        "v_mfma_f32_16x16x32_bf16 v[196:199], v[128:131], v[80:83], 0\n"
        "s_add_u32 m0, s66, 0\n"
        "ds_read_b128 a[240:243], v241 offset:0\n"
        "v_mul_f32 v155, v121, v155\n"
        "v_mfma_f32_16x16x32_bf16 v[196:199], v[132:135], v[84:87], v[196:199]\n"
        "s_add_u32 s64, s51, 0\n"
        "v_cvt_pk_bf16_f32 v118, v152, v153\n"
        "v_cvt_pk_bf16_f32 v119, v154, v155\n"
        "v_mfma_f32_16x16x32_bf16 v[200:203], v[140:143], v[84:87], 0\n"
        "ds_read_b128 a[244:247], v240 offset:128\n"
        "v_mul_f32 v160, v121, v160\n"
        "v_mfma_f32_16x16x32_bf16 v[200:203], v[136:139], v[80:83], v[200:203]\n"
        "buffer_load_dwordx4 v124, s[52:55], s64 offen lds\n"
        "v_mfma_f32_16x16x32_bf16 v[204:207], v[144:147], v[80:83], 0\n"
        "ds_read_b128 a[248:251], v241 offset:128\n"
        "v_mul_f32 v161, v121, v161\n"
        "v_mfma_f32_16x16x32_bf16 v[204:207], v[148:151], v[84:87], v[204:207]\n"
        "ds_read_b128 a[216:219], v240 offset:256\n"
        "v_mul_f32 v162, v121, v162\n"
        "v_smfmac_f32_16x16x64_bf16 v[212:215], a[252:255], v[88:95], v255\n"
        "ds_read_b128 a[220:223], v241 offset:256\n"
        "v_mul_f32 v163, v121, v163\n"
        "v_mfma_f32_16x16x32_bf16 v[216:219], v[128:131], v[88:91], 0\n"
        "ds_write_b128 v245, v[116:119] offset:0\n"
        "v_mfma_f32_16x16x32_bf16 v[216:219], v[132:135], v[92:95], v[216:219]\n"
        "v_cvt_pk_bf16_f32 v160, v160, v161\n"
        "v_cvt_pk_bf16_f32 v161, v162, v163\n"
        "v_mfma_f32_16x16x32_bf16 v[220:223], v[140:143], v[92:95], 0\n"
        "v_add_f32 v172, v172, v173\n"
        "v_add_f32 v192, v192, v193\n"
        "v_mfma_f32_16x16x32_bf16 v[220:223], v[136:139], v[88:91], v[220:223]\n"
        "v_rcp_f32 v173, v172\n"
        "v_mfma_f32_16x16x32_bf16 v[224:227], v[144:147], v[88:91], 0\n"
        "ds_write_b64 v246, v[160:161] offset:0\n"
        "v_mfma_f32_16x16x32_bf16 v[224:227], v[148:151], v[92:95], v[224:227]\n"
        "s_waitcnt lgkmcnt(7)\n"
        "v_mul_f32 v176, v173, v176\n"
        "v_mul_f32 v177, v173, v177\n"
        "v_mfma_f32_16x16x32_bf16 v[96:99], a[72:75], a[236:239], 0\n"
        "s_waitcnt lgkmcnt(6)\n"
        "v_mul_f32 v178, v173, v178\n"
        "v_mul_f32 v179, v173, v179\n"
        "v_mfma_f32_16x16x32_bf16 v[96:99], a[76:79], a[240:243], v[96:99]\n"
        "s_waitcnt lgkmcnt(5)\n"
        "buffer_load_dwordx4 v125, s[52:55], s64 offen offset:1024 lds\n"
        "v_mfma_f32_16x16x32_bf16 v[96:99], a[80:83], a[244:247], v[96:99]\n"
        "s_waitcnt lgkmcnt(4)\n"
        "v_cvt_pk_bf16_f32 v176, v176, v177\n"
        "v_cvt_pk_bf16_f32 v177, v178, v179\n"
        "v_mfma_f32_16x16x32_bf16 v[96:99], a[84:87], a[248:251], v[96:99]\n"
        "s_waitcnt lgkmcnt(3)\n"
        "v_rcp_f32 v193, v192\n"
        "v_mfma_f32_16x16x32_bf16 v[96:99], a[88:91], a[216:219], v[96:99]\n"
        "s_waitcnt lgkmcnt(2)\n"
        "v_mul_f32 v180, v173, v180\n"
        "v_mul_f32 v181, v173, v181\n"
        "v_mfma_f32_16x16x32_bf16 v[96:99], a[92:95], a[220:223], v[96:99]\n"
        "s_add_u32 s41, s29, s40\n"
        "v_maximum3_f32 v253, v253, v172, v173\n"
        "v_mul_f32 v182, v173, v182\n"
        "v_mfma_f32_16x16x32_bf16 v[100:103], a[116:119], a[220:223], 0\n"
        "s_add_u32 s35, s47, 786432\n"
        "v_mul_f32 v183, v173, v183\n"
        "v_cvt_pk_bf16_f32 v178, v180, v181\n"
        "v_mfma_f32_16x16x32_bf16 v[100:103], a[112:115], a[216:219], v[100:103]\n"
        "s_sub_u32 s46, s35, 100663296\n"
        "v_cvt_pk_bf16_f32 v179, v182, v183\n"
        "v_cvt_pk_bf16_f32 v152, v96, v97\n"
        "v_mfma_f32_16x16x32_bf16 v[100:103], a[108:111], a[248:251], v[100:103]\n"
        "s_cmp_lt_u32 s46, 786432\n"
        "buffer_load_dwordx4 v126, s[52:55], s64 offen offset:2048 lds\n"
        "v_mfma_f32_16x16x32_bf16 v[100:103], a[104:107], a[244:247], v[100:103]\n"
        "s_cselect_b32 s35, s46, s35\n"
        "v_cvt_pk_bf16_f32 v153, v98, v99\n"
        "v_mul_f32 v184, v173, v184\n"
        "v_mfma_f32_16x16x32_bf16 v[100:103], a[100:103], a[240:243], v[100:103]\n"
        "s_sub_u32 s31, s28, s30\n"
        "ds_write_b128 v245, v[176:179] offset:256\n"
        "v_mfma_f32_16x16x32_bf16 v[100:103], a[96:99], a[236:239], v[100:103]\n"
        "s_sub_u32 s45, s29, s28\n"
        "v_mul_f32 v185, v173, v185\n"
        "v_mul_f32 v186, v173, v186\n"
        "v_mfma_f32_16x16x32_bf16 v[104:107], a[120:123], a[236:239], 0\n"
        "s_mov_b32 s29, s30\n"
        "v_mul_f32 v187, v173, v187\n"
        "v_cvt_pk_bf16_f32 v184, v184, v185\n"
        "v_mfma_f32_16x16x32_bf16 v[104:107], a[124:127], a[240:243], v[104:107]\n"
        "s_mov_b32 s30, s28\n"
        "v_cvt_pk_bf16_f32 v185, v186, v187\n"
        "v_cvt_pk_bf16_f32 v154, v100, v101\n"
        "v_mfma_f32_16x16x32_bf16 v[104:107], a[128:131], a[244:247], v[104:107]\n"
        "s_add_u32 m0, s66, 3072\n"
        "v_cvt_pk_bf16_f32 v155, v102, v103\n"
        "v_mul_f32 v196, v193, v196\n"
        "v_mfma_f32_16x16x32_bf16 v[104:107], a[132:135], a[248:251], v[104:107]\n"
        "s_add_u32 s64, s51, 6144\n"
        "ds_write_b64 v246, v[184:185] offset:256\n"
        "v_mfma_f32_16x16x32_bf16 v[104:107], a[136:139], a[216:219], v[104:107]\n"
        "s_add_u32 s28, s28, s45\n"
        "v_mul_f32 v197, v193, v197\n"
        "v_mul_f32 v198, v193, v198\n"
        "v_mfma_f32_16x16x32_bf16 v[104:107], a[140:143], a[220:223], v[104:107]\n"
        "v_mul_f32 v199, v193, v199\n"
        "v_cvt_pk_bf16_f32 v196, v196, v197\n"
        "v_mfma_f32_16x16x32_bf16 v[96:99], a[20:23], a[220:223], 0\n"
        "v_cvt_pk_bf16_f32 v197, v198, v199\n"
        "v_mul_f32 v200, v193, v200\n"
        "v_mfma_f32_16x16x32_bf16 v[96:99], a[16:19], a[216:219], v[96:99]\n"
        "buffer_load_dwordx4 v124, s[52:55], s64 offen lds\n"
        "v_mfma_f32_16x16x32_bf16 v[96:99], a[12:15], a[248:251], v[96:99]\n"
        "v_cvt_pk_bf16_f32 v168, v104, v105\n"
        "v_cvt_pk_bf16_f32 v169, v106, v107\n"
        "v_mfma_f32_16x16x32_bf16 v[96:99], a[8:11], a[244:247], v[96:99]\n"
        "v_mul_f32 v201, v193, v201\n"
        "v_maximum3_f32 v253, v253, v192, v193\n"
        "v_mfma_f32_16x16x32_bf16 v[96:99], a[4:7], a[240:243], v[96:99]\n"
        "v_mul_f32 v202, v193, v202\n"
        "v_mul_f32 v203, v193, v203\n"
        "v_mfma_f32_16x16x32_bf16 v[96:99], a[0:3], a[236:239], v[96:99]\n"
        "v_cvt_pk_bf16_f32 v198, v200, v201\n"
        "v_cvt_pk_bf16_f32 v199, v202, v203\n"
        "v_mfma_f32_16x16x32_bf16 v[100:103], a[24:27], a[236:239], 0\n"
        "ds_write_b128 v245, v[196:199] offset:512\n"
        "v_mfma_f32_16x16x32_bf16 v[100:103], a[28:31], a[240:243], v[100:103]\n"
        "v_mul_f32 v204, v193, v204\n"
        "v_mul_f32 v205, v193, v205\n"
        "v_mfma_f32_16x16x32_bf16 v[100:103], a[32:35], a[244:247], v[100:103]\n"
        "v_mul_f32 v96, s50, v96\n"
        "v_mul_f32 v97, s50, v97\n"
        "v_mfma_f32_16x16x32_bf16 v[100:103], a[36:39], a[248:251], v[100:103]\n"
        "v_mul_f32 v98, s50, v98\n"
        "v_mul_f32 v99, s50, v99\n"
        "v_mfma_f32_16x16x32_bf16 v[100:103], a[40:43], a[216:219], v[100:103]\n"
        "buffer_load_dwordx4 v125, s[52:55], s64 offen offset:1024 lds\n"
        "v_mfma_f32_16x16x32_bf16 v[100:103], a[44:47], a[220:223], v[100:103]\n"
        "v_cvt_pk_bf16_f32 v156, v96, v97\n"
        "v_cvt_pk_bf16_f32 v157, v98, v99\n"
        "v_mfma_f32_16x16x32_bf16 v[104:107], a[68:71], a[220:223], 0\n"
        "v_mul_f32 v206, v193, v206\n"
        "v_mul_f32 v207, v193, v207\n"
        "v_mfma_f32_16x16x32_bf16 v[104:107], a[64:67], a[216:219], v[104:107]\n"
        "v_cvt_pk_bf16_f32 v204, v204, v205\n"
        "v_cvt_pk_bf16_f32 v205, v206, v207\n"
        "v_mfma_f32_16x16x32_bf16 v[104:107], a[60:63], a[248:251], v[104:107]\n"
        "v_mul_f32 v100, s50, v100\n"
        "v_mul_f32 v101, s50, v101\n"
        "v_mfma_f32_16x16x32_bf16 v[104:107], a[56:59], a[244:247], v[104:107]\n"
        "v_mul_f32 v102, s50, v102\n"
        "v_mul_f32 v103, s50, v103\n"
        "v_mfma_f32_16x16x32_bf16 v[104:107], a[52:55], a[240:243], v[104:107]\n"
        "v_cvt_pk_bf16_f32 v158, v100, v101\n"
        "v_cvt_pk_bf16_f32 v159, v102, v103\n"
        "v_mfma_f32_16x16x32_bf16 v[104:107], a[48:51], a[236:239], v[104:107]\n"
        "buffer_load_dwordx4 v126, s[52:55], s64 offen offset:2048 lds\n"
        "v_mfma_f32_16x16x32_bf16 v[96:99], a[236:239], a[144:147], 0\n"
        "ds_write_b64 v246, v[204:205] offset:512\n"
        "v_mfma_f32_16x16x32_bf16 v[96:99], a[240:243], a[148:151], v[96:99]\n"
        "v_add_f32 v212, v212, v213\n"
        "v_xor_b32 v251, 0x2000, v251\n"
        "v_mfma_f32_16x16x32_bf16 v[96:99], a[244:247], a[152:155], v[96:99]\n"
        "v_rcp_f32 v213, v212\n"
        "v_mfma_f32_16x16x32_bf16 v[96:99], a[248:251], a[156:159], v[96:99]\n"
        "v_mul_f32 v104, s50, v104\n"
        "v_mul_f32 v105, s50, v105\n"
        "v_mfma_f32_16x16x32_bf16 v[96:99], a[216:219], a[160:163], v[96:99]\n"
        "v_mul_f32 v106, s50, v106\n"
        "v_mul_f32 v107, s50, v107\n"
        "v_mfma_f32_16x16x32_bf16 v[96:99], a[220:223], a[164:167], v[96:99]\n"
        "s_add_u32 m0, s66, 6144\n"
        "v_cvt_pk_bf16_f32 v160, v104, v105\n"
        "v_cvt_pk_bf16_f32 v161, v106, v107\n"
        "v_mfma_f32_16x16x32_bf16 v[100:103], a[220:223], a[188:191], 0\n"
        "s_add_u32 s64, s51, 18432\n"
        "v_mov_b64 v[166:167], v[160:161]\n"
        "v_mul_f32 v216, v213, v216\n"
        "v_mfma_f32_16x16x32_bf16 v[100:103], a[216:219], a[184:187], v[100:103]\n"
        "v_mul_f32 v217, v213, v217\n"
        "v_mul_f32 v218, v213, v218\n"
        "v_mfma_f32_16x16x32_bf16 v[100:103], a[248:251], a[180:183], v[100:103]\n"
        "buffer_load_dwordx4 v124, s[52:55], s64 offen lds\n"
        "v_mfma_f32_16x16x32_bf16 v[100:103], a[244:247], a[176:179], v[100:103]\n"
        "ds_read_b128 a[224:227], v240 offset:1536\n"
        "ds_read_b128 a[228:231], v241 offset:1536\n"
        "v_mfma_f32_16x16x32_bf16 v[100:103], a[240:243], a[172:175], v[100:103]\n"
        "ds_read_b128 a[232:235], v240 offset:1664\n"
        "v_mul_f32 v219, v213, v219\n"
        "v_mfma_f32_16x16x32_bf16 v[100:103], a[236:239], a[168:171], v[100:103]\n"
        "v_cvt_pk_bf16_f32 v216, v216, v217\n"
        "v_cvt_pk_bf16_f32 v217, v218, v219\n"
        "v_mfma_f32_16x16x32_bf16 v[104:107], a[236:239], a[192:195], 0\n"
        "ds_read_b128 a[236:239], v241 offset:1664\n"
        "v_cvt_pk_bf16_f32 v128, v96, v97\n"
        "v_mfma_f32_16x16x32_bf16 v[104:107], a[240:243], a[196:199], v[104:107]\n"
        "ds_read_b128 a[240:243], v240 offset:1792\n"
        "v_cvt_pk_bf16_f32 v129, v98, v99\n"
        "v_mfma_f32_16x16x32_bf16 v[104:107], a[244:247], a[200:203], v[104:107]\n"
        "ds_read_b128 a[244:247], v241 offset:1792\n"
        "v_mul_f32 v220, v213, v220\n"
        "v_mfma_f32_16x16x32_bf16 v[104:107], a[248:251], a[204:207], v[104:107]\n"
        "v_mul_f32 v221, v213, v221\n"
        "v_maximum3_f32 v253, v253, v212, v213\n"
        "v_mfma_f32_16x16x32_bf16 v[104:107], a[216:219], a[208:211], v[104:107]\n"
        "v_mul_f32 v222, v213, v222\n"
        "v_mul_f32 v223, v213, v223\n"
        "v_mfma_f32_16x16x32_bf16 v[104:107], a[220:223], a[212:215], v[104:107]\n"
        "s_waitcnt lgkmcnt(5)\n"
        "buffer_load_dwordx4 v125, s[52:55], s64 offen offset:1024 lds\n"
        "v_mfma_f32_16x16x32_bf16 v[96:99], a[72:75], a[224:227], 0\n"
        "s_waitcnt lgkmcnt(4)\n"
        "v_cvt_pk_bf16_f32 v218, v220, v221\n"
        "v_cvt_pk_bf16_f32 v219, v222, v223\n"
        "v_mfma_f32_16x16x32_bf16 v[96:99], a[76:79], a[228:231], v[96:99]\n"
        "s_waitcnt lgkmcnt(3)\n"
        "v_xor_b32 v252, 0x2000, v252\n"
        "v_mul_f32 v224, v213, v224\n"
        "v_mfma_f32_16x16x32_bf16 v[96:99], a[80:83], a[232:235], v[96:99]\n"
        "s_waitcnt lgkmcnt(2)\n"
        "ds_write_b128 v245, v[216:219] offset:768\n"
        "v_mfma_f32_16x16x32_bf16 v[96:99], a[84:87], a[236:239], v[96:99]\n"
        "42:\n"
        "s_cbranch_execz 45f\n"
        "s_waitcnt lgkmcnt(2)\n"
        "v_mul_f32 v225, v213, v225\n"
        "v_cvt_pk_bf16_f32 v136, v100, v101\n"
        "v_mfma_f32_16x16x32_bf16 v[96:99], a[88:91], a[240:243], v[96:99]\n"
        "s_waitcnt lgkmcnt(1)\n"
        "v_cvt_pk_bf16_f32 v137, v102, v103\n"
        "v_mul_f32 v226, v213, v226\n"
        "v_mfma_f32_16x16x32_bf16 v[96:99], a[92:95], a[244:247], v[96:99]\n"
        "s_waitcnt vmcnt(8)\n"
        "v_mul_f32 v227, v213, v227\n"
        "v_cvt_pk_bf16_f32 v224, v224, v225\n"
        "v_mfma_f32_16x16x32_bf16 v[100:103], a[116:119], a[244:247], 0\n"
        "s_barrier\n"
        "v_cvt_pk_bf16_f32 v225, v226, v227\n"
        "v_cvt_pk_bf16_f32 v144, v104, v105\n"
        "v_mfma_f32_16x16x32_bf16 v[100:103], a[112:115], a[240:243], v[100:103]\n"
        "ds_write_b64 v246, v[224:225] offset:768\n"
        "v_mfma_f32_16x16x32_bf16 v[100:103], a[108:111], a[236:239], v[100:103]\n"
        "v_cvt_pk_bf16_f32 v145, v106, v107\n"
        "v_cvt_pk_bf16_f32 v172, v96, v97\n"
        "v_mfma_f32_16x16x32_bf16 v[100:103], a[104:107], a[232:235], v[100:103]\n"
        "v_cvt_pk_bf16_f32 v173, v98, v99\n"
        "v_add_u32 v245, s31, v245\n"
        "v_mfma_f32_16x16x32_bf16 v[100:103], a[100:103], a[228:231], v[100:103]\n"
        "buffer_load_dwordx4 v126, s[52:55], s64 offen offset:2048 lds\n"
        "v_mfma_f32_16x16x32_bf16 v[100:103], a[96:99], a[224:227], v[100:103]\n"
        "s_waitcnt vmcnt(3)\n"
        "ds_read_b64_tr_b16 v[88:89], v234 offset:0\n"
        "ds_read_b64_tr_b16 v[90:91], v234 offset:3072\n"
        "v_mfma_f32_16x16x32_bf16 v[104:107], a[120:123], a[224:227], 0\n"
        "ds_read_b64_tr_b16 v[92:93], v235 offset:32\n"
        "v_add_u32 v246, s31, v246\n"
        "v_mfma_f32_16x16x32_bf16 v[104:107], a[124:127], a[228:231], v[104:107]\n"
        "ds_read_b64_tr_b16 v[94:95], v235 offset:3104\n"
        "v_mfma_f32_16x16x32_bf16 v[104:107], a[128:131], a[232:235], v[104:107]\n"
        "s_add_u32 m0, s66, 9216\n"
        "ds_read_b64_tr_b16 v[120:121], v234 offset:64\n"
        "ds_read_b128 v[162:165], v251 offset:0\n"
        "v_mfma_f32_16x16x32_bf16 v[104:107], a[132:135], a[236:239], v[104:107]\n"
        "s_add_u32 s64, s51, 24576\n"
        "ds_read_b64_tr_b16 v[122:123], v234 offset:3136\n"
        "v_cvt_pk_bf16_f32 v174, v100, v101\n"
        "v_mfma_f32_16x16x32_bf16 v[104:107], a[136:139], a[240:243], v[104:107]\n"
        "ds_read_b64_tr_b16 v[216:217], v235 offset:96\n"
        "v_cvt_pk_bf16_f32 v175, v102, v103\n"
        "v_mfma_f32_16x16x32_bf16 v[104:107], a[140:143], a[244:247], v[104:107]\n"
        "ds_read_b64_tr_b16 v[218:219], v235 offset:3168\n"
        "v_mfma_f32_16x16x32_bf16 v[96:99], a[20:23], a[244:247], 0\n"
        "buffer_load_dwordx4 v124, s[52:55], s64 offen lds\n"
        "v_mfma_f32_16x16x32_bf16 v[96:99], a[16:19], a[240:243], v[96:99]\n"
        "ds_read_b64_tr_b16 v[220:221], v238 offset:128\n"
        "ds_read_b64_tr_b16 v[222:223], v238 offset:3200\n"
        "v_mfma_f32_16x16x32_bf16 v[96:99], a[12:15], a[236:239], v[96:99]\n"
        "ds_read_b64_tr_b16 v[224:225], v239 offset:160\n"
        "v_cvt_pk_bf16_f32 v190, v104, v105\n"
        "v_mfma_f32_16x16x32_bf16 v[96:99], a[8:11], a[232:235], v[96:99]\n"
        "ds_read_b64_tr_b16 v[226:227], v239 offset:3232\n"
        "v_cvt_pk_bf16_f32 v191, v106, v107\n"
        "v_mfma_f32_16x16x32_bf16 v[96:99], a[4:7], a[228:231], v[96:99]\n"
        "s_waitcnt lgkmcnt(11)\n"
        "ds_write_b128 v236, v[88:91] offset:0\n"
        "v_mfma_f32_16x16x32_bf16 v[96:99], a[0:3], a[224:227], v[96:99]\n"
        "v_mfma_f32_16x16x32_bf16 v[64:67], v[152:155], v[156:159], v[0:3]\n"
        "s_waitcnt lgkmcnt(8)\n"
        "v_mfma_f32_16x16x32_bf16 v[64:67], v[168:171], v[160:163], v[64:67]\n"
        "v_mfma_f32_16x16x32_bf16 v[68:71], v[172:175], v[156:159], v[4:7]\n"
        "buffer_load_dwordx4 v125, s[52:55], s64 offen offset:1024 lds\n"
        "v_mfma_f32_16x16x32_bf16 v[68:71], v[188:191], v[164:167], v[68:71]\n"
        "ds_write_b128 v236, v[92:95] offset:384\n"
        "v_mfma_f32_16x16x32_bf16 v[100:103], a[24:27], a[224:227], 0\n"
        "v_mfma_f32_16x16x32_bf16 v[100:103], a[28:31], a[228:231], v[100:103]\n"
        "v_mul_f32 v96, s50, v96\n"
        "v_mul_f32 v97, s50, v97\n"
        "v_mfma_f32_16x16x32_bf16 v[100:103], a[32:35], a[232:235], v[100:103]\n"
        "v_mul_f32 v98, s50, v98\n"
        "v_mul_f32 v99, s50, v99\n"
        "v_mfma_f32_16x16x32_bf16 v[100:103], a[36:39], a[236:239], v[100:103]\n"
        "v_cvt_pk_bf16_f32 v176, v96, v97\n"
        "v_cvt_pk_bf16_f32 v177, v98, v99\n"
        "v_mfma_f32_16x16x32_bf16 v[100:103], a[40:43], a[240:243], v[100:103]\n"
        "s_waitcnt lgkmcnt(8)\n"
        "ds_write_b128 v236, v[120:123] offset:768\n"
        "v_mfma_f32_16x16x32_bf16 v[100:103], a[44:47], a[244:247], v[100:103]\n"
        "v_exp_f32 v64, v64\n"
        "v_mfma_f32_16x16x32_bf16 v[104:107], a[68:71], a[244:247], 0\n"
        "v_exp_f32 v65, v65\n"
        "v_mfma_f32_16x16x32_bf16 v[104:107], a[64:67], a[240:243], v[104:107]\n"
        "v_exp_f32 v66, v66\n"
        "v_mfma_f32_16x16x32_bf16 v[104:107], a[60:63], a[236:239], v[104:107]\n"
        "buffer_load_dwordx4 v126, s[52:55], s64 offen offset:2048 lds\n"
        "v_mfma_f32_16x16x32_bf16 v[104:107], a[56:59], a[232:235], v[104:107]\n"
        "ds_read_b128 v[182:185], v251 offset:512\n"
        "v_mul_f32 v100, s50, v100\n"
        "v_mfma_f32_16x16x32_bf16 v[104:107], a[52:55], a[228:231], v[104:107]\n"
        "v_mul_f32 v101, s50, v101\n"
        "v_mul_f32 v102, s50, v102\n"
        "v_mfma_f32_16x16x32_bf16 v[104:107], a[48:51], a[224:227], v[104:107]\n"
        "v_mul_f32 v103, s50, v103\n"
        "v_cvt_pk_bf16_f32 v178, v100, v101\n"
        "v_mfma_f32_16x16x32_bf16 v[96:99], a[224:227], a[144:147], 0\n"
        "ds_read_b128 a[248:251], v240 offset:12288\n"
        "v_cvt_pk_bf16_f32 v179, v102, v103\n"
        "v_mfma_f32_16x16x32_bf16 v[96:99], a[228:231], a[148:151], v[96:99]\n"
        "s_add_u32 m0, s66, 12288\n"
        "s_waitcnt lgkmcnt(9)\n"
        "ds_write_b128 v236, v[216:219] offset:1152\n"
        "v_mfma_f32_16x16x32_bf16 v[96:99], a[232:235], a[152:155], v[96:99]\n"
        "s_add_u32 s64, s51, 12288\n"
        "v_mul_f32 v104, s50, v104\n"
        "v_mul_f32 v105, s50, v105\n"
        "v_mfma_f32_16x16x32_bf16 v[96:99], a[236:239], a[156:159], v[96:99]\n"
        "v_mul_f32 v106, s50, v106\n"
        "v_mul_f32 v107, s50, v107\n"
        "v_mfma_f32_16x16x32_bf16 v[96:99], a[240:243], a[160:163], v[96:99]\n"
        "v_cvt_pk_bf16_f32 v180, v104, v105\n"
        "v_cvt_pk_bf16_f32 v181, v106, v107\n"
        "v_mfma_f32_16x16x32_bf16 v[96:99], a[244:247], a[164:167], v[96:99]\n"
        "buffer_load_dwordx4 v127, s[52:55], s64 offen lds\n"
        "v_mfma_f32_16x16x32_bf16 v[72:75], v[152:155], v[176:179], v[16:19]\n"
        "s_waitcnt lgkmcnt(2)\n"
        "ds_write_b128 v237, v[220:223] offset:0\n"
        "v_mfma_f32_16x16x32_bf16 v[72:75], v[168:171], v[180:183], v[72:75]\n"
        "v_mov_b64 v[186:187], v[180:181]\n"
        "v_cvt_pk_bf16_f32 v64, v64, v65\n"
        "v_mfma_f32_16x16x32_bf16 v[76:79], v[172:175], v[176:179], v[20:23]\n"
        "v_exp_f32 v67, v67\n"
        "v_mfma_f32_16x16x32_bf16 v[76:79], v[188:191], v[184:187], v[76:79]\n"
        "v_cvt_pk_bf16_f32 v130, v96, v97\n"
        "v_cvt_pk_bf16_f32 v131, v98, v99\n"
        "v_mfma_f32_16x16x32_bf16 v[100:103], a[244:247], a[188:191], 0\n"
        "ds_write_b128 v237, v[224:227] offset:768\n"
        "v_mfma_f32_16x16x32_bf16 v[100:103], a[240:243], a[184:187], v[100:103]\n"
        "v_exp_f32 v68, v68\n"
        "v_mfma_f32_16x16x32_bf16 v[100:103], a[236:239], a[180:183], v[100:103]\n"
        "v_exp_f32 v69, v69\n"
        "v_mfma_f32_16x16x32_bf16 v[100:103], a[232:235], a[176:179], v[100:103]\n"
        "v_exp_f32 v70, v70\n"
        "v_mfma_f32_16x16x32_bf16 v[100:103], a[228:231], a[172:175], v[100:103]\n"
        "buffer_load_dwordx4 v232, s[52:55], s64 offen offset:1024 lds\n"
        "v_mfma_f32_16x16x32_bf16 v[100:103], a[224:227], a[168:171], v[100:103]\n"
        "ds_read_b128 a[216:219], v241 offset:12288\n"
        "ds_read_b128 a[220:223], v240 offset:12416\n"
        "v_mfma_f32_16x16x32_bf16 v[104:107], a[224:227], a[192:195], 0\n"
        "ds_read_b128 a[224:227], v241 offset:12416\n"
        "v_cvt_pk_bf16_f32 v65, v66, v67\n"
        "v_mfma_f32_16x16x32_bf16 v[104:107], a[228:231], a[196:199], v[104:107]\n"
        "ds_read_b128 a[228:231], v240 offset:12544\n"
        "v_cvt_pk_bf16_f32 v66, v68, v69\n"
        "v_mfma_f32_16x16x32_bf16 v[104:107], a[232:235], a[200:203], v[104:107]\n"
        "ds_read_b128 a[232:235], v241 offset:12544\n"
        "v_cvt_pk_bf16_f32 v138, v100, v101\n"
        "v_mfma_f32_16x16x32_bf16 v[104:107], a[236:239], a[204:207], v[104:107]\n"
        "v_cvt_pk_bf16_f32 v139, v102, v103\n"
        "v_mfma_f32_16x16x32_bf16 v[104:107], a[240:243], a[208:211], v[104:107]\n"
        "v_exp_f32 v71, v71\n"
        "v_mfma_f32_16x16x32_bf16 v[104:107], a[244:247], a[212:215], v[104:107]\n"
        "s_waitcnt lgkmcnt(8)\n"
        "v_exp_f32 v72, v72\n"
        "v_mfma_f32_16x16x32_bf16 v[96:99], a[72:75], a[248:251], 0\n"
        "s_waitcnt lgkmcnt(4)\n"
        "buffer_load_dwordx4 v233, s[52:55], s64 offen offset:2048 lds\n"
        "v_mfma_f32_16x16x32_bf16 v[96:99], a[76:79], a[216:219], v[96:99]\n"
        "s_waitcnt lgkmcnt(3)\n"
        "v_exp_f32 v73, v73\n"
        "v_mfma_f32_16x16x32_bf16 v[96:99], a[80:83], a[220:223], v[96:99]\n"
        "s_waitcnt lgkmcnt(2)\n"
        "v_exp_f32 v74, v74\n"
        "v_mfma_f32_16x16x32_bf16 v[96:99], a[84:87], a[224:227], v[96:99]\n"
        "s_waitcnt lgkmcnt(1)\n"
        "v_cvt_pk_bf16_f32 v146, v104, v105\n"
        "v_cvt_pk_bf16_f32 v147, v106, v107\n"
        "v_mfma_f32_16x16x32_bf16 v[96:99], a[88:91], a[228:231], v[96:99]\n"
        "s_waitcnt vmcnt(3) lgkmcnt(0)\n"
        "ds_read_b64_tr_b16 v[88:89], v234 offset:6144\n"
        "v_cvt_pk_bf16_f32 v67, v70, v71\n"
        "v_mfma_f32_16x16x32_bf16 v[96:99], a[92:95], a[232:235], v[96:99]\n"
        "ds_read_b64_tr_b16 v[90:91], v234 offset:9216\n"
        "v_cvt_pk_bf16_f32 v72, v72, v73\n"
        "v_mfma_f32_16x16x32_bf16 v[100:103], a[116:119], a[232:235], 0\n"
        "s_add_u32 m0, s66, 15360\n"
        "ds_read_b64_tr_b16 v[92:93], v235 offset:6176\n"
        "v_mfma_f32_16x16x32_bf16 v[100:103], a[112:115], a[228:231], v[100:103]\n"
        "s_add_u32 s64, s51, 13824\n"
        "ds_read_b64_tr_b16 v[94:95], v235 offset:9248\n"
        "v_cvt_pk_bf16_f32 v192, v96, v97\n"
        "v_mfma_f32_16x16x32_bf16 v[100:103], a[108:111], a[224:227], v[100:103]\n"
        "ds_read_b64_tr_b16 v[120:121], v234 offset:6208\n"
        "v_cvt_pk_bf16_f32 v193, v98, v99\n"
        "v_mfma_f32_16x16x32_bf16 v[100:103], a[104:107], a[220:223], v[100:103]\n"
        "ds_read_b64_tr_b16 v[122:123], v234 offset:9280\n"
        "v_mfma_f32_16x16x32_bf16 v[100:103], a[100:103], a[216:219], v[100:103]\n"
        "buffer_load_dwordx4 v127, s[52:55], s64 offen lds\n"
        "v_mfma_f32_16x16x32_bf16 v[100:103], a[96:99], a[248:251], v[100:103]\n"
        "ds_read_b64_tr_b16 v[216:217], v235 offset:6240\n"
        "ds_read_b64_tr_b16 v[218:219], v235 offset:9312\n"
        "v_mfma_f32_16x16x32_bf16 v[104:107], a[120:123], a[248:251], 0\n"
        "ds_read_b64_tr_b16 v[220:221], v238 offset:6272\n"
        "v_mfma_f32_16x16x32_bf16 v[104:107], a[124:127], a[216:219], v[104:107]\n"
        "ds_read_b64_tr_b16 v[222:223], v238 offset:9344\n"
        "v_mfma_f32_16x16x32_bf16 v[104:107], a[128:131], a[220:223], v[104:107]\n"
        "ds_read_b64_tr_b16 v[224:225], v239 offset:6304\n"
        "v_cvt_pk_bf16_f32 v194, v100, v101\n"
        "v_mfma_f32_16x16x32_bf16 v[104:107], a[132:135], a[224:227], v[104:107]\n"
        "ds_read_b64_tr_b16 v[226:227], v239 offset:9376\n"
        "v_cvt_pk_bf16_f32 v195, v102, v103\n"
        "v_mfma_f32_16x16x32_bf16 v[104:107], a[136:139], a[228:231], v[104:107]\n"
        "s_waitcnt lgkmcnt(10)\n"
        "ds_write_b128 v236, v[88:91] offset:6144\n"
        "v_mfma_f32_16x16x32_bf16 v[104:107], a[140:143], a[232:235], v[104:107]\n"
        "v_exp_f32 v75, v75\n"
        "v_mfma_f32_16x16x32_bf16 v[96:99], a[20:23], a[232:235], 0\n"
        "v_exp_f32 v76, v76\n"
        "v_mfma_f32_16x16x32_bf16 v[96:99], a[16:19], a[228:231], v[96:99]\n"
        "v_exp_f32 v77, v77\n"
        "v_mfma_f32_16x16x32_bf16 v[96:99], a[12:15], a[224:227], v[96:99]\n"
        "buffer_load_dwordx4 v232, s[52:55], s64 offen offset:1024 lds\n"
        "v_mfma_f32_16x16x32_bf16 v[96:99], a[8:11], a[220:223], v[96:99]\n"
        "s_waitcnt lgkmcnt(9)\n"
        "ds_write_b128 v236, v[92:95] offset:6528\n"
        "v_mfma_f32_16x16x32_bf16 v[96:99], a[4:7], a[216:219], v[96:99]\n"
        "v_cvt_pk_bf16_f32 v208, v104, v105\n"
        "v_cvt_pk_bf16_f32 v209, v106, v107\n"
        "v_mfma_f32_16x16x32_bf16 v[96:99], a[0:3], a[248:251], v[96:99]\n"
        "v_exp_f32 v78, v78\n"
        "v_mfma_f32_16x16x32_bf16 v[100:103], a[24:27], a[248:251], 0\n"
        "v_exp_f32 v79, v79\n"
        "v_mfma_f32_16x16x32_bf16 v[100:103], a[28:31], a[216:219], v[100:103]\n"
        "ds_read_b128 v[162:165], v252 offset:0\n"
        "v_cvt_pk_bf16_f32 v73, v74, v75\n"
        "v_mfma_f32_16x16x32_bf16 v[100:103], a[32:35], a[220:223], v[100:103]\n"
        "ds_read_b128 v[182:185], v252 offset:512\n"
        "v_mul_f32 v96, s50, v96\n"
        "v_mfma_f32_16x16x32_bf16 v[100:103], a[36:39], a[224:227], v[100:103]\n"
        "buffer_load_dwordx4 v233, s[52:55], s64 offen offset:2048 lds\n"
        "v_mfma_f32_16x16x32_bf16 v[100:103], a[40:43], a[228:231], v[100:103]\n"
        "s_waitcnt lgkmcnt(10)\n"
        "ds_write_b128 v236, v[120:123] offset:6912\n"
        "v_mfma_f32_16x16x32_bf16 v[100:103], a[44:47], a[232:235], v[100:103]\n"
        "v_mul_f32 v97, s50, v97\n"
        "v_mul_f32 v98, s50, v98\n"
        "v_mfma_f32_16x16x32_bf16 v[104:107], a[68:71], a[232:235], 0\n"
        "v_mul_f32 v99, s50, v99\n"
        "v_cvt_pk_bf16_f32 v196, v96, v97\n"
        "v_mfma_f32_16x16x32_bf16 v[104:107], a[64:67], a[228:231], v[104:107]\n"
        "v_cvt_pk_bf16_f32 v197, v98, v99\n"
        "v_cvt_pk_bf16_f32 v74, v76, v77\n"
        "v_mfma_f32_16x16x32_bf16 v[104:107], a[60:63], a[224:227], v[104:107]\n"
        "s_waitcnt lgkmcnt(9)\n"
        "ds_write_b128 v236, v[216:219] offset:7296\n"
        "v_mfma_f32_16x16x32_bf16 v[104:107], a[56:59], a[220:223], v[104:107]\n"
        "v_mul_f32 v100, s50, v100\n"
        "v_mul_f32 v101, s50, v101\n"
        "v_mfma_f32_16x16x32_bf16 v[104:107], a[52:55], a[216:219], v[104:107]\n"
        "s_add_u32 m0, s41, 0\n"
        "v_mul_f32 v102, s50, v102\n"
        "v_mul_f32 v103, s50, v103\n"
        "v_mfma_f32_16x16x32_bf16 v[104:107], a[48:51], a[248:251], v[104:107]\n"
        "v_cvt_pk_bf16_f32 v198, v100, v101\n"
        "v_cvt_pk_bf16_f32 v199, v102, v103\n"
        "v_mfma_f32_16x16x32_bf16 v[68:71], v[192:195], v[156:159], v[8:11]\n"
        "s_waitcnt lgkmcnt(3)\n"
        "buffer_load_dwordx4 v242, s[8:11], s47 offen lds\n"
        "v_mfma_f32_16x16x32_bf16 v[68:71], v[208:211], v[160:163], v[68:71]\n"
        "ds_write_b128 v237, v[220:223] offset:6144\n"
        "v_mfma_f32_16x16x32_bf16 v[96:99], a[248:251], a[144:147], 0\n"
        "v_cvt_pk_bf16_f32 v75, v78, v79\n"
        "v_mul_f32 v104, s50, v104\n"
        "v_mfma_f32_16x16x32_bf16 v[96:99], a[216:219], a[148:151], v[96:99]\n"
        "s_add_u32 m0, s41, 1024\n"
        "v_mul_f32 v105, s50, v105\n"
        "v_mul_f32 v106, s50, v106\n"
        "v_mfma_f32_16x16x32_bf16 v[96:99], a[220:223], a[152:155], v[96:99]\n"
        "v_mul_f32 v107, s50, v107\n"
        "v_cvt_pk_bf16_f32 v200, v104, v105\n"
        "v_mfma_f32_16x16x32_bf16 v[96:99], a[224:227], a[156:159], v[96:99]\n"
        "buffer_load_dwordx4 v243, s[8:11], s47 offen lds\n"
        "v_mfma_f32_16x16x32_bf16 v[96:99], a[228:231], a[160:163], v[96:99]\n"
        "ds_read_b128 v[202:205], v251 offset:4096\n"
        "v_cvt_pk_bf16_f32 v201, v106, v107\n"
        "v_mfma_f32_16x16x32_bf16 v[96:99], a[232:235], a[164:167], v[96:99]\n"
        "ds_write_b128 v237, v[224:227] offset:6912\n"
        "v_mfma_f32_16x16x32_bf16 v[76:79], v[192:195], v[176:179], v[24:27]\n"
        "s_waitcnt lgkmcnt(5)\n"
        "s_add_u32 m0, s41, 2048\n"
        "v_mov_b64 v[206:207], v[200:201]\n"
        "v_mfma_f32_16x16x32_bf16 v[76:79], v[208:211], v[180:183], v[76:79]\n"
        "v_exp_f32 v68, v68\n"
        "v_mfma_f32_16x16x32_bf16 v[100:103], a[232:235], a[188:191], 0\n"
        "v_exp_f32 v69, v69\n"
        "v_mfma_f32_16x16x32_bf16 v[100:103], a[228:231], a[184:187], v[100:103]\n"
        "buffer_load_dwordx4 v244, s[8:11], s47 offen lds\n"
        "v_mfma_f32_16x16x32_bf16 v[100:103], a[224:227], a[180:183], v[100:103]\n"
        "v_cvt_pk_bf16_f32 v132, v96, v97\n"
        "v_cvt_pk_bf16_f32 v133, v98, v99\n"
        "v_mfma_f32_16x16x32_bf16 v[100:103], a[220:223], a[176:179], v[100:103]\n"
        "s_add_u32 m0, s41, 12288\n"
        "ds_read_b128 a[236:239], v240 offset:13824\n"
        "v_cvt_pk_bf16_f32 v68, v68, v69\n"
        "v_mfma_f32_16x16x32_bf16 v[100:103], a[216:219], a[172:175], v[100:103]\n"
        "s_waitcnt vmcnt(3)\n"
        "ds_read_b64_tr_b16 v[220:221], v234 offset:12288\n"
        "v_mfma_f32_16x16x32_bf16 v[100:103], a[248:251], a[168:171], v[100:103]\n"
        "ds_read_b64_tr_b16 v[222:223], v234 offset:15360\n"
        "v_mfma_f32_16x16x32_bf16 v[80:83], v[152:155], v[196:199], v[32:35]\n"
        "s_waitcnt lgkmcnt(4)\n"
        "buffer_load_dwordx4 v242, s[8:11], s35 offen lds\n"
        "v_mfma_f32_16x16x32_bf16 v[80:83], v[168:171], v[200:203], v[80:83]\n"
        "ds_read_b64_tr_b16 v[224:225], v235 offset:12320\n"
        "ds_read_b64_tr_b16 v[226:227], v235 offset:15392\n"
        "v_mfma_f32_16x16x32_bf16 v[104:107], a[248:251], a[192:195], 0\n"
        "s_add_u32 m0, s41, 13312\n"
        "ds_read_b128 a[240:243], v241 offset:13824\n"
        "ds_read_b64_tr_b16 v[88:89], v234 offset:12352\n"
        "v_mfma_f32_16x16x32_bf16 v[104:107], a[216:219], a[196:199], v[104:107]\n"
        "ds_read_b128 a[244:247], v240 offset:13952\n"
        "ds_read_b64_tr_b16 v[90:91], v234 offset:15424\n"
        "v_mfma_f32_16x16x32_bf16 v[104:107], a[220:223], a[200:203], v[104:107]\n"
        "ds_read_b128 a[248:251], v241 offset:13952\n"
        "ds_read_b64_tr_b16 v[92:93], v235 offset:12384\n"
        "v_mfma_f32_16x16x32_bf16 v[104:107], a[224:227], a[204:207], v[104:107]\n"
        "ds_read_b128 a[216:219], v240 offset:14080\n"
        "ds_read_b64_tr_b16 v[94:95], v235 offset:15456\n"
        "v_mfma_f32_16x16x32_bf16 v[104:107], a[228:231], a[208:211], v[104:107]\n"
        "buffer_load_dwordx4 v243, s[8:11], s35 offen lds\n"
        "v_mfma_f32_16x16x32_bf16 v[104:107], a[232:235], a[212:215], v[104:107]\n"
        "s_add_u32 m0, s41, 14336\n"
        "ds_read_b128 a[220:223], v241 offset:14080\n"
        "ds_read_b64_tr_b16 v[120:121], v238 offset:12416\n"
        "v_mfma_f32_16x16x32_bf16 v[84:87], v[172:175], v[196:199], v[36:39]\n"
        "ds_read_b64_tr_b16 v[122:123], v238 offset:15488\n"
        "ds_read_b64_tr_b16 v[124:125], v239 offset:12448\n"
        "v_mfma_f32_16x16x32_bf16 v[84:87], v[188:191], v[204:207], v[84:87]\n"
        "s_waitcnt lgkmcnt(15)\n"
        "ds_read_b64_tr_b16 v[126:127], v239 offset:15520\n"
        "v_cvt_pk_bf16_f32 v140, v100, v101\n"
        "v_mfma_f32_16x16x32_bf16 v[96:99], a[72:75], a[236:239], 0\n"
        "s_waitcnt lgkmcnt(12)\n"
        "buffer_load_dwordx4 v244, s[8:11], s35 offen lds\n"
        "v_mfma_f32_16x16x32_bf16 v[96:99], a[76:79], a[240:243], v[96:99]\n"
        "s_waitcnt lgkmcnt(10)\n"
        "v_cvt_pk_bf16_f32 v141, v102, v103\n"
        "v_cvt_pk_bf16_f32 v148, v104, v105\n"
        "v_mfma_f32_16x16x32_bf16 v[96:99], a[80:83], a[244:247], v[96:99]\n"
        "s_waitcnt lgkmcnt(8)\n"
        "s_mov_b32 m0, s48\n"
        "v_cvt_pk_bf16_f32 v149, v106, v107\n"
        "v_mfma_f32_16x16x32_bf16 v[96:99], a[84:87], a[248:251], v[96:99]\n"
        "s_waitcnt lgkmcnt(6)\n"
        "v_exp_f32 v70, v70\n"
        "v_mfma_f32_16x16x32_bf16 v[96:99], a[88:91], a[216:219], v[96:99]\n"
        "s_waitcnt lgkmcnt(4)\n"
        "buffer_load_dwordx4 v249, s[12:15], s36 offen offset:0 lds\n"
        "v_mfma_f32_16x16x32_bf16 v[96:99], a[92:95], a[220:223], v[96:99]\n"
        "ds_read_b128 v[202:205], v252 offset:4096\n"
        "v_xor_b32 v116, 32, v249\n"
        "v_mfma_f32_16x16x32_bf16 v[100:103], a[116:119], a[220:223], 0\n"
        "ds_write_b128 v236, v[220:223] offset:12288\n"
        "v_mfma_f32_16x16x32_bf16 v[100:103], a[112:115], a[216:219], v[100:103]\n"
        "v_exp_f32 v71, v71\n"
        "v_mfma_f32_16x16x32_bf16 v[100:103], a[108:111], a[248:251], v[100:103]\n"
        "v_exp_f32 v76, v76\n"
        "v_mfma_f32_16x16x32_bf16 v[100:103], a[104:107], a[244:247], v[100:103]\n"
        "v_cvt_pk_bf16_f32 v212, v96, v97\n"
        "v_cvt_pk_bf16_f32 v213, v98, v99\n"
        "v_mfma_f32_16x16x32_bf16 v[100:103], a[100:103], a[240:243], v[100:103]\n"
        "buffer_load_dwordx4 v116, s[12:15], s36 offen offset:1024 lds\n"
        "v_mfma_f32_16x16x32_bf16 v[100:103], a[96:99], a[236:239], v[100:103]\n"
        "ds_write_b128 v236, v[224:227] offset:12672\n"
        "v_mfma_f32_16x16x32_bf16 v[104:107], a[120:123], a[236:239], 0\n"
        "v_exp_f32 v77, v77\n"
        "v_mfma_f32_16x16x32_bf16 v[104:107], a[124:127], a[240:243], v[104:107]\n"
        "v_exp_f32 v78, v78\n"
        "v_mfma_f32_16x16x32_bf16 v[104:107], a[128:131], a[244:247], v[104:107]\n"
        "v_exp_f32 v79, v79\n"
        "v_mfma_f32_16x16x32_bf16 v[104:107], a[132:135], a[248:251], v[104:107]\n"
        "v_cvt_pk_bf16_f32 v214, v100, v101\n"
        "v_cvt_pk_bf16_f32 v215, v102, v103\n"
        "v_mfma_f32_16x16x32_bf16 v[104:107], a[136:139], a[216:219], v[104:107]\n"
        "ds_write_b128 v236, v[88:91] offset:13056\n"
        "v_mfma_f32_16x16x32_bf16 v[104:107], a[140:143], a[220:223], v[104:107]\n"
        "v_cvt_pk_bf16_f32 v69, v70, v71\n"
        "v_cvt_pk_bf16_f32 v76, v76, v77\n"
        "v_mfma_f32_16x16x32_bf16 v[96:99], a[20:23], a[220:223], 0\n"
        "v_exp_f32 v80, v80\n"
        "v_mfma_f32_16x16x32_bf16 v[96:99], a[16:19], a[216:219], v[96:99]\n"
        "v_exp_f32 v81, v81\n"
        "v_mfma_f32_16x16x32_bf16 v[96:99], a[12:15], a[248:251], v[96:99]\n"
        "v_exp_f32 v82, v82\n"
        "v_mfma_f32_16x16x32_bf16 v[96:99], a[8:11], a[244:247], v[96:99]\n"
        "v_cvt_pk_bf16_f32 v230, v104, v105\n"
        "v_cvt_pk_bf16_f32 v231, v106, v107\n"
        "v_mfma_f32_16x16x32_bf16 v[96:99], a[4:7], a[240:243], v[96:99]\n"
        "s_cmp_eq_u32 s37, 100663296\n"
        "ds_write_b128 v236, v[92:95] offset:13440\n"
        "v_mfma_f32_16x16x32_bf16 v[96:99], a[0:3], a[236:239], v[96:99]\n"
        "s_cselect_b32 s46, 0, s37\n"
        "v_cvt_pk_bf16_f32 v77, v78, v79\n"
        "v_cvt_pk_bf16_f32 v80, v80, v81\n"
        "v_mfma_f32_16x16x32_bf16 v[108:111], v[212:215], v[156:159], v[12:15]\n"
        "s_add_u32 s38, s46, s43\n"
        "v_exp_f32 v83, v83\n"
        "v_mfma_f32_16x16x32_bf16 v[108:111], v[228:231], v[164:167], v[108:111]\n"
        "s_add_u32 s39, s46, s44\n"
        "v_mul_f32 v96, s50, v96\n"
        "v_mul_f32 v97, s50, v97\n"
        "v_mfma_f32_16x16x32_bf16 v[112:115], v[212:215], v[176:179], v[28:31]\n"
        "s_add_u32 s37, s37, 6291456\n"
        "v_mul_f32 v98, s50, v98\n"
        "v_mul_f32 v99, s50, v99\n"
        "v_mfma_f32_16x16x32_bf16 v[112:115], v[228:231], v[184:187], v[112:115]\n"
        "s_waitcnt lgkmcnt(7)\n"
        "ds_write_b128 v237, v[120:123] offset:12288\n"
        "v_mfma_f32_16x16x32_bf16 v[116:119], v[212:215], v[196:199], v[44:47]\n"
        "s_waitcnt lgkmcnt(5)\n"
        "v_cvt_pk_bf16_f32 v216, v96, v97\n"
        "v_cvt_pk_bf16_f32 v217, v98, v99\n"
        "v_mfma_f32_16x16x32_bf16 v[116:119], v[228:231], v[204:207], v[116:119]\n"
        "v_cvt_pk_bf16_f32 v81, v82, v83\n"
        "v_add_u32 v240, s45, v240\n"
        "v_mfma_f32_16x16x32_bf16 v[100:103], a[24:27], a[236:239], 0\n"
        "v_exp_f32 v84, v84\n"
        "v_mfma_f32_16x16x32_bf16 v[100:103], a[28:31], a[240:243], v[100:103]\n"
        "v_exp_f32 v85, v85\n"
        "v_mfma_f32_16x16x32_bf16 v[100:103], a[32:35], a[244:247], v[100:103]\n"
        "v_exp_f32 v86, v86\n"
        "v_mfma_f32_16x16x32_bf16 v[100:103], a[36:39], a[248:251], v[100:103]\n"
        "ds_read_b128 v[222:225], v251 offset:4608\n"
        "v_cvt_pk_bf16_f32 v82, v84, v85\n"
        "v_mfma_f32_16x16x32_bf16 v[100:103], a[40:43], a[216:219], v[100:103]\n"
        "ds_write_b128 v237, v[124:127] offset:13056\n"
        "v_mfma_f32_16x16x32_bf16 v[100:103], a[44:47], a[220:223], v[100:103]\n"
        "v_exp_f32 v87, v87\n"
        "v_add_u32 v241, s45, v241\n"
        "v_cvt_pk_bf16_f32 v83, v86, v87\n"
        "v_mfma_f32_16x16x32_bf16 v[84:87], v[192:195], v[196:199], v[40:43]\n"
        "v_exp_f32 v108, v108\n"
        "v_mfma_f32_16x16x32_bf16 v[84:87], v[208:211], v[200:203], v[84:87]\n"
        "v_exp_f32 v109, v109\n"
        "v_mfma_f32_16x16x32_bf16 v[104:107], a[68:71], a[220:223], 0\n"
        "v_mul_f32 v100, s50, v100\n"
        "v_mul_f32 v101, s50, v101\n"
        "v_mfma_f32_16x16x32_bf16 v[104:107], a[64:67], a[216:219], v[104:107]\n"
        "v_mul_f32 v102, s50, v102\n"
        "v_mul_f32 v103, s50, v103\n"
        "v_mfma_f32_16x16x32_bf16 v[104:107], a[60:63], a[248:251], v[104:107]\n"
        "v_cvt_pk_bf16_f32 v218, v100, v101\n"
        "v_cvt_pk_bf16_f32 v219, v102, v103\n"
        "v_mfma_f32_16x16x32_bf16 v[104:107], a[56:59], a[244:247], v[104:107]\n"
        "s_waitcnt lgkmcnt(0)\n"
        "v_exp_f32 v110, v110\n"
        "v_mfma_f32_16x16x32_bf16 v[104:107], a[52:55], a[240:243], v[104:107]\n"
        "s_barrier\n"
        "v_exp_f32 v111, v111\n"
        "v_mfma_f32_16x16x32_bf16 v[104:107], a[48:51], a[236:239], v[104:107]\n"
        "v_exp_f32 v112, v112\n"
        "v_mfma_f32_16x16x32_bf16 v[96:99], a[236:239], a[144:147], 0\n"
        "v_exp_f32 v113, v113\n"
        "v_mfma_f32_16x16x32_bf16 v[96:99], a[240:243], a[148:151], v[96:99]\n"
        "v_exp_f32 v114, v114\n"
        "v_mfma_f32_16x16x32_bf16 v[96:99], a[244:247], a[152:155], v[96:99]\n"
        "v_exp_f32 v115, v115\n"
        "v_mfma_f32_16x16x32_bf16 v[96:99], a[248:251], a[156:159], v[96:99]\n"
        "v_mul_f32 v104, s50, v104\n"
        "v_mul_f32 v105, s50, v105\n"
        "v_mfma_f32_16x16x32_bf16 v[96:99], a[216:219], a[160:163], v[96:99]\n"
        "v_mul_f32 v106, s50, v106\n"
        "v_mul_f32 v107, s50, v107\n"
        "v_mfma_f32_16x16x32_bf16 v[96:99], a[220:223], a[164:167], v[96:99]\n"
        "v_cvt_pk_bf16_f32 v220, v104, v105\n"
        "v_cvt_pk_bf16_f32 v221, v106, v107\n"
        "v_mfma_f32_16x16x32_bf16 v[88:91], v[152:155], v[216:219], v[48:51]\n"
        "ds_read_b128 v[156:159], v247 offset:0\n"
        "v_mov_b64 v[226:227], v[220:221]\n"
        "v_mfma_f32_16x16x32_bf16 v[88:91], v[168:171], v[220:223], v[88:91]\n"
        "ds_read_b128 v[164:167], v247 offset:4096\n"
        "v_cvt_pk_bf16_f32 v70, v108, v109\n"
        "v_mfma_f32_16x16x32_bf16 v[92:95], v[172:175], v[216:219], v[52:55]\n"
        "s_add_u32 s47, s47, 6291456\n"
        "ds_read_b128 v[160:163], v247 offset:256\n"
        "v_cvt_pk_bf16_f32 v134, v96, v97\n"
        "v_mfma_f32_16x16x32_bf16 v[92:95], v[188:191], v[224:227], v[92:95]\n"
        "s_add_u32 s36, s36, 2097152\n"
        "ds_read_b128 v[222:225], v252 offset:4608\n"
        "v_cvt_pk_bf16_f32 v135, v98, v99\n"
        "v_mfma_f32_16x16x32_bf16 v[100:103], a[220:223], a[188:191], 0\n"
        "s_xor_b32 s48, s48, 8192\n"
        "ds_read_b128 v[152:155], v247 offset:4352\n"
        "v_cvt_pk_bf16_f32 v71, v110, v111\n"
        "v_mfma_f32_16x16x32_bf16 v[100:103], a[216:219], a[184:187], v[100:103]\n"
        "v_exp_f32 v116, v116\n"
        "v_mfma_f32_16x16x32_bf16 v[100:103], a[248:251], a[180:183], v[100:103]\n"
        "v_exp_f32 v117, v117\n"
        "v_mfma_f32_16x16x32_bf16 v[100:103], a[244:247], a[176:179], v[100:103]\n"
        "v_exp_f32 v118, v118\n"
        "v_mfma_f32_16x16x32_bf16 v[100:103], a[240:243], a[172:175], v[100:103]\n"
        "v_exp_f32 v119, v119\n"
        "v_mfma_f32_16x16x32_bf16 v[100:103], a[236:239], a[168:171], v[100:103]\n"
        "v_exp_f32 v84, v84\n"
        "v_mfma_f32_16x16x32_bf16 v[104:107], a[236:239], a[192:195], 0\n"
        "ds_read_b128 a[236:239], v248 offset:0\n"
        "v_cvt_pk_bf16_f32 v78, v112, v113\n"
        "v_mfma_f32_16x16x32_bf16 v[104:107], a[240:243], a[196:199], v[104:107]\n"
        "ds_read_b128 a[240:243], v248 offset:6144\n"
        "v_cvt_pk_bf16_f32 v79, v114, v115\n"
        "v_mfma_f32_16x16x32_bf16 v[104:107], a[244:247], a[200:203], v[104:107]\n"
        "ds_read_b128 a[244:247], v254 offset:16\n"
        "v_cvt_pk_bf16_f32 v142, v100, v101\n"
        "v_mfma_f32_16x16x32_bf16 v[104:107], a[248:251], a[204:207], v[104:107]\n"
        "ds_read_b128 a[248:251], v254 offset:6160\n"
        "v_cvt_pk_bf16_f32 v143, v102, v103\n"
        "v_mfma_f32_16x16x32_bf16 v[104:107], a[216:219], a[208:211], v[104:107]\n"
        "v_exp_f32 v85, v85\n"
        "v_mfma_f32_16x16x32_bf16 v[104:107], a[220:223], a[212:215], v[104:107]\n"
        "v_exp_f32 v86, v86\n"
        "v_mfma_f32_16x16x32_bf16 v[108:111], v[192:195], v[216:219], v[56:59]\n"
        "s_waitcnt lgkmcnt(5)\n"
        "ds_read_b128 a[216:219], v248 offset:32\n"
        "v_cvt_pk_bf16_f32 v84, v84, v85\n"
        "v_mfma_f32_16x16x32_bf16 v[108:111], v[208:211], v[220:223], v[108:111]\n"
        "v_exp_f32 v87, v87\n"
        "v_mfma_f32_16x16x32_bf16 v[112:115], v[212:215], v[216:219], v[60:63]\n"
        "ds_read_b128 a[220:223], v248 offset:6176\n"
        "v_cvt_pk_bf16_f32 v150, v104, v105\n"
        "v_mfma_f32_16x16x32_bf16 v[112:115], v[228:231], v[224:227], v[112:115]\n"
        "s_waitcnt lgkmcnt(5)\n"
        "ds_read_b128 a[224:227], v254 offset:48\n"
        "v_cvt_pk_bf16_f32 v151, v106, v107\n"
        "v_mfma_f32_16x16x32_bf16 v[172:175], v[156:159], a[236:239], 0\n"
        "s_waitcnt lgkmcnt(5)\n"
        "ds_read_b128 a[228:231], v254 offset:6192\n"
        "v_cvt_pk_bf16_f32 v85, v86, v87\n"
        "v_mfma_f32_16x16x32_bf16 v[172:175], v[164:167], a[240:243], v[172:175]\n"
        "v_cvt_pk_bf16_f32 v86, v116, v117\n"
        "v_mov_b64 v[116:117], 0\n"
        "v_mfma_f32_16x16x32_bf16 v[200:203], v[152:155], a[240:243], 0\n"
        "ds_read_b128 a[232:235], v248 offset:64\n"
        "v_cvt_pk_bf16_f32 v87, v118, v119\n"
        "v_mfma_f32_16x16x32_bf16 v[200:203], v[160:163], a[236:239], v[200:203]\n"
        "v_exp_f32 v88, v88\n"
        "v_smfmac_f32_16x16x64_bf16 v[116:119], a[252:255], v[64:71], v255\n"
        "v_exp_f32 v89, v89\n"
        "v_mfma_f32_16x16x32_bf16 v[120:123], v[128:131], v[64:67], 0\n"
        "v_exp_f32 v90, v90\n"
        "v_mfma_f32_16x16x32_bf16 v[120:123], v[132:135], v[68:71], v[120:123]\n"
        "ds_read_b128 a[236:239], v248 offset:6208\n"
        "v_cvt_pk_bf16_f32 v88, v88, v89\n"
        "v_mfma_f32_16x16x32_bf16 v[124:127], v[140:143], v[68:71], 0\n"
        "v_exp_f32 v91, v91\n"
        "v_mfma_f32_16x16x32_bf16 v[124:127], v[136:139], v[64:67], v[124:127]\n"
        "v_cvt_pk_bf16_f32 v89, v90, v91\n"
        "v_add_f32 v116, v116, v117\n"
        "v_mfma_f32_16x16x32_bf16 v[96:99], v[144:147], v[64:67], 0\n"
        "v_exp_f32 v92, v92\n"
        "v_mfma_f32_16x16x32_bf16 v[96:99], v[148:151], v[68:71], v[96:99]\n"
        "s_waitcnt lgkmcnt(7)\n"
        "v_exp_f32 v93, v93\n"
        "v_mfma_f32_16x16x32_bf16 v[204:207], v[160:163], a[244:247], 0\n"
        "s_waitcnt lgkmcnt(6)\n"
        "v_exp_f32 v94, v94\n"
        "v_mfma_f32_16x16x32_bf16 v[204:207], v[152:155], a[248:251], v[204:207]\n"
        "ds_read_b128 a[240:243], v254 offset:80\n"
        "v_cvt_pk_bf16_f32 v90, v92, v93\n"
        "v_mfma_f32_16x16x32_bf16 v[176:179], v[164:167], a[248:251], 0\n"
        "v_exp_f32 v95, v95\n"
        "v_mfma_f32_16x16x32_bf16 v[176:179], v[156:159], a[244:247], v[176:179]\n"
        "s_waitcnt lgkmcnt(6)\n"
        "ds_read_b128 a[244:247], v254 offset:6224\n"
        "v_cvt_pk_bf16_f32 v91, v94, v95\n"
        "v_mfma_f32_16x16x32_bf16 v[180:183], v[156:159], a[216:219], 0\n"
        "s_waitcnt lgkmcnt(6)\n"
        "ds_read_b128 a[248:251], v248 offset:36864\n"
        "v_mov_b64 v[100:101], 0\n"
        "v_mfma_f32_16x16x32_bf16 v[180:183], v[164:167], a[220:223], v[180:183]\n"
        "v_exp_f32 v108, v108\n"
        "v_mfma_f32_16x16x32_bf16 v[212:215], v[152:155], a[220:223], 0\n"
        "v_exp_f32 v109, v109\n"
        "v_mfma_f32_16x16x32_bf16 v[212:215], v[160:163], a[216:219], v[212:215]\n"
        "s_waitcnt lgkmcnt(6)\n"
        "v_exp_f32 v110, v110\n"
        "v_mfma_f32_16x16x32_bf16 v[216:219], v[160:163], a[224:227], 0\n"
        "s_waitcnt lgkmcnt(5)\n"
        "v_rcp_f32 v117, v116\n"
        "v_mfma_f32_16x16x32_bf16 v[216:219], v[152:155], a[228:231], v[216:219]\n"
        "v_cvt_pk_bf16_f32 v92, v108, v109\n"
        "v_mul_f32 v120, v117, v120\n"
        "v_mfma_f32_16x16x32_bf16 v[184:187], v[164:167], a[228:231], 0\n"
        "v_exp_f32 v111, v111\n"
        "v_mfma_f32_16x16x32_bf16 v[184:187], v[156:159], a[224:227], v[184:187]\n"
        "s_waitcnt lgkmcnt(4)\n"
        "v_mul_f32 v121, v117, v121\n"
        "v_mul_f32 v122, v117, v122\n"
        "v_mfma_f32_16x16x32_bf16 v[192:195], v[156:159], a[232:235], 0\n"
        "s_waitcnt lgkmcnt(3)\n"
        "v_mul_f32 v123, v117, v123\n"
        "v_cvt_pk_bf16_f32 v93, v110, v111\n"
        "v_mfma_f32_16x16x32_bf16 v[192:195], v[164:167], a[236:239], v[192:195]\n"
        "43:\n"
        "s_cbranch_execz 46f\n"
        "v_exp_f32 v112, v112\n"
        "v_mfma_f32_16x16x32_bf16 v[220:223], v[152:155], a[236:239], 0\n"
        "v_cvt_pk_bf16_f32 v120, v120, v121\n"
        "v_cvt_pk_bf16_f32 v121, v122, v123\n"
        "v_mfma_f32_16x16x32_bf16 v[220:223], v[160:163], a[232:235], v[220:223]\n"
        "ds_read_b128 v[232:235], v247 offset:8192\n"
        "v_mul_f32 v124, v117, v124\n"
        "v_smfmac_f32_16x16x64_bf16 v[100:103], a[252:255], v[72:79], v255\n"
        "ds_read_b128 v[64:67], v247 offset:12288\n"
        "v_mul_f32 v125, v117, v125\n"
        "v_mfma_f32_16x16x32_bf16 v[104:107], v[128:131], v[72:75], 0\n"
        "ds_read_b128 v[236:239], v247 offset:8448\n"
        "v_maximum3_f32 v253, v253, v116, v117\n"
        "v_mfma_f32_16x16x32_bf16 v[104:107], v[132:135], v[76:79], v[104:107]\n"
        "ds_read_b128 v[68:71], v247 offset:12544\n"
        "ds_read_b128 a[216:219], v248 offset:43008\n"
        "v_mfma_f32_16x16x32_bf16 v[108:111], v[140:143], v[76:79], 0\n"
        "v_exp_f32 v113, v113\n"
        "v_mfma_f32_16x16x32_bf16 v[108:111], v[136:139], v[72:75], v[108:111]\n"
        "ds_write_b32 v245, v120 offset:0\n"
        "v_exp_f32 v114, v114\n"
        "v_cvt_pk_bf16_f32 v94, v112, v113\n"
        "v_exp_f32 v115, v115\n"
        "v_mul_f32 v126, v117, v126\n"
        "v_cvt_pk_bf16_f32 v95, v114, v115\n"
        "v_mfma_f32_16x16x32_bf16 v[112:115], v[144:147], v[72:75], 0\n"
        "ds_read_b128 a[220:223], v254 offset:36880\n"
        "v_mul_f32 v127, v117, v127\n"
        "v_mfma_f32_16x16x32_bf16 v[112:115], v[148:151], v[76:79], v[112:115]\n"
        "s_waitcnt lgkmcnt(9)\n"
        "ds_read_b128 a[224:227], v254 offset:43024\n"
        "v_cvt_pk_bf16_f32 v124, v124, v125\n"
        "v_mfma_f32_16x16x32_bf16 v[224:227], v[160:163], a[240:243], 0\n"
        "s_waitcnt lgkmcnt(9)\n"
        "ds_write_b32 v245, v121 offset:4\n"
        "v_mfma_f32_16x16x32_bf16 v[224:227], v[152:155], a[244:247], v[224:227]\n"
        "v_cvt_pk_bf16_f32 v125, v126, v127\n"
        "v_mul_f32 v96, v117, v96\n"
        "v_mfma_f32_16x16x32_bf16 v[196:199], v[164:167], a[244:247], 0\n"
        "ds_read_b128 a[228:231], v248 offset:36896\n"
        "v_mul_f32 v97, v117, v97\n"
        "v_mfma_f32_16x16x32_bf16 v[196:199], v[156:159], a[240:243], v[196:199]\n"
        "s_waitcnt lgkmcnt(9)\n"
        "ds_read_b128 a[232:235], v248 offset:43040\n"
        "v_mul_f32 v98, v117, v98\n"
        "v_mfma_f32_16x16x32_bf16 v[172:175], v[232:235], a[248:251], v[172:175]\n"
        "s_waitcnt lgkmcnt(6)\n"
        "ds_write_b32 v245, v124 offset:8\n"
        "v_mfma_f32_16x16x32_bf16 v[172:175], v[64:67], a[216:219], v[172:175]\n"
        "ds_write_b32 v245, v125 offset:12\n"
        "v_mfma_f32_16x16x32_bf16 v[200:203], v[68:71], a[216:219], v[200:203]\n"
        "ds_read_b128 a[236:239], v254 offset:36912\n"
        "v_mul_f32 v99, v117, v99\n"
        "v_mfma_f32_16x16x32_bf16 v[200:203], v[236:239], a[248:251], v[200:203]\n"
        "s_waitcnt lgkmcnt(7)\n"
        "ds_read_b128 a[240:243], v254 offset:43056\n"
        "v_cvt_pk_bf16_f32 v96, v96, v97\n"
        "v_mfma_f32_16x16x32_bf16 v[204:207], v[236:239], a[220:223], v[204:207]\n"
        "s_waitcnt lgkmcnt(7)\n"
        "v_cvt_pk_bf16_f32 v97, v98, v99\n"
        "v_add_f32 v100, v100, v101\n"
        "v_mfma_f32_16x16x32_bf16 v[204:207], v[68:71], a[224:227], v[204:207]\n"
        "ds_write_b32 v246, v96 offset:0\n"
        "v_mfma_f32_16x16x32_bf16 v[176:179], v[64:67], a[224:227], v[176:179]\n"
        "ds_write_b32 v246, v97 offset:4\n"
        "v_mfma_f32_16x16x32_bf16 v[176:179], v[232:235], a[220:223], v[176:179]\n"
        "s_waitcnt lgkmcnt(7)\n"
        "v_rcp_f32 v101, v100\n"
        "v_mfma_f32_16x16x32_bf16 v[180:183], v[232:235], a[228:231], v[180:183]\n"
        "s_waitcnt lgkmcnt(6)\n"
        "v_mul_f32 v104, v101, v104\n"
        "v_mul_f32 v105, v101, v105\n"
        "v_mfma_f32_16x16x32_bf16 v[180:183], v[64:67], a[232:235], v[180:183]\n"
        "v_mov_b64 v[116:117], 0\n"
        "v_mul_f32 v106, v101, v106\n"
        "v_mfma_f32_16x16x32_bf16 v[212:215], v[68:71], a[232:235], v[212:215]\n"
        "v_mul_f32 v107, v101, v107\n"
        "v_cvt_pk_bf16_f32 v104, v104, v105\n"
        "v_mfma_f32_16x16x32_bf16 v[212:215], v[236:239], a[228:231], v[212:215]\n"
        "s_waitcnt lgkmcnt(3)\n"
        "v_cvt_pk_bf16_f32 v105, v106, v107\n"
        "v_mul_f32 v108, v101, v108\n"
        "v_mfma_f32_16x16x32_bf16 v[216:219], v[236:239], a[236:239], v[216:219]\n"
        "s_waitcnt lgkmcnt(2)\n"
        "ds_write_b32 v245, v104 offset:256\n"
        "v_mfma_f32_16x16x32_bf16 v[216:219], v[68:71], a[240:243], v[216:219]\n"
        "ds_read_b128 a[244:247], v248 offset:36928\n"
        "v_mul_f32 v109, v101, v109\n"
        "v_mfma_f32_16x16x32_bf16 v[184:187], v[64:67], a[240:243], v[184:187]\n"
        "ds_read_b128 a[248:251], v248 offset:43072\n"
        "v_maximum3_f32 v253, v253, v100, v101\n"
        "v_mfma_f32_16x16x32_bf16 v[184:187], v[232:235], a[236:239], v[184:187]\n"
        "ds_write_b32 v245, v105 offset:260\n"
        "v_smfmac_f32_16x16x64_bf16 v[116:119], a[252:255], v[80:87], v255\n"
        "v_mul_f32 v110, v101, v110\n"
        "v_mul_f32 v111, v101, v111\n"
        "v_mfma_f32_16x16x32_bf16 v[120:123], v[128:131], v[80:83], 0\n"
        "ds_read_b128 a[216:219], v254 offset:36944\n"
        "v_cvt_pk_bf16_f32 v108, v108, v109\n"
        "v_mfma_f32_16x16x32_bf16 v[120:123], v[132:135], v[84:87], v[120:123]\n"
        "ds_read_b128 a[220:223], v254 offset:43088\n"
        "v_cvt_pk_bf16_f32 v109, v110, v111\n"
        "v_mfma_f32_16x16x32_bf16 v[124:127], v[140:143], v[84:87], 0\n"
        "ds_read_b128 v[156:159], v247 offset:16384\n"
        "v_mul_f32 v112, v101, v112\n"
        "v_mfma_f32_16x16x32_bf16 v[124:127], v[136:139], v[80:83], v[124:127]\n"
        "ds_read_b128 v[164:167], v247 offset:20480\n"
        "v_mul_f32 v113, v101, v113\n"
        "v_mfma_f32_16x16x32_bf16 v[96:99], v[144:147], v[80:83], 0\n"
        "ds_read_b128 v[160:163], v247 offset:16640\n"
        "ds_read_b128 a[224:227], v248 offset:12288\n"
        "v_mfma_f32_16x16x32_bf16 v[96:99], v[148:151], v[84:87], v[96:99]\n"
        "s_waitcnt lgkmcnt(8)\n"
        "ds_read_b128 v[152:155], v247 offset:20736\n"
        "ds_read_b128 a[228:231], v248 offset:49152\n"
        "v_mfma_f32_16x16x32_bf16 v[192:195], v[232:235], a[244:247], v[192:195]\n"
        "s_waitcnt lgkmcnt(9)\n"
        "ds_write_b32 v245, v108 offset:264\n"
        "v_mfma_f32_16x16x32_bf16 v[192:195], v[64:67], a[248:251], v[192:195]\n"
        "ds_write_b32 v245, v109 offset:268\n"
        "v_mfma_f32_16x16x32_bf16 v[220:223], v[68:71], a[248:251], v[220:223]\n"
        "ds_read_b128 a[232:235], v254 offset:12304\n"
        "v_mul_f32 v114, v101, v114\n"
        "v_mfma_f32_16x16x32_bf16 v[220:223], v[236:239], a[244:247], v[220:223]\n"
        "s_waitcnt lgkmcnt(10)\n"
        "ds_read_b128 a[236:239], v254 offset:49168\n"
        "v_mul_f32 v115, v101, v115\n"
        "v_mfma_f32_16x16x32_bf16 v[224:227], v[236:239], a[216:219], v[224:227]\n"
        "s_waitcnt lgkmcnt(10)\n"
        "ds_read_b128 a[240:243], v248 offset:12320\n"
        "v_cvt_pk_bf16_f32 v112, v112, v113\n"
        "v_mfma_f32_16x16x32_bf16 v[224:227], v[68:71], a[220:223], v[224:227]\n"
        "v_cvt_pk_bf16_f32 v113, v114, v115\n"
        "v_add_f32 v116, v116, v117\n"
        "v_mfma_f32_16x16x32_bf16 v[196:199], v[64:67], a[220:223], v[196:199]\n"
        "ds_write_b32 v246, v112 offset:256\n"
        "v_mfma_f32_16x16x32_bf16 v[196:199], v[232:235], a[216:219], v[196:199]\n"
        "s_waitcnt lgkmcnt(8)\n"
        "ds_read_b128 a[244:247], v248 offset:49184\n"
        "v_add_u32 v247, s31, v247\n"
        "v_mfma_f32_16x16x32_bf16 v[172:175], v[156:159], a[224:227], v[172:175]\n"
        "s_waitcnt lgkmcnt(7)\n"
        "ds_write_b32 v246, v113 offset:260\n"
        "v_mfma_f32_16x16x32_bf16 v[172:175], v[164:167], a[228:231], v[172:175]\n"
        "v_rcp_f32 v117, v116\n"
        "v_mfma_f32_16x16x32_bf16 v[200:203], v[152:155], a[228:231], v[200:203]\n"
        "ds_read_b128 a[248:251], v254 offset:12336\n"
        "v_mul_f32 v120, v117, v120\n"
        "v_mfma_f32_16x16x32_bf16 v[200:203], v[160:163], a[224:227], v[200:203]\n"
        "s_waitcnt lgkmcnt(6)\n"
        "ds_read_b128 a[216:219], v254 offset:49200\n"
        "v_mul_f32 v121, v117, v121\n"
        "v_mfma_f32_16x16x32_bf16 v[204:207], v[160:163], a[232:235], v[204:207]\n"
        "s_waitcnt lgkmcnt(6)\n"
        "v_mul_f32 v122, v117, v122\n"
        "v_mul_f32 v123, v117, v123\n"
        "v_mfma_f32_16x16x32_bf16 v[204:207], v[152:155], a[236:239], v[204:207]\n"
        "v_cvt_pk_bf16_f32 v120, v120, v121\n"
        "v_cvt_pk_bf16_f32 v121, v122, v123\n"
        "v_mfma_f32_16x16x32_bf16 v[176:179], v[164:167], a[236:239], v[176:179]\n"
        "ds_read_b128 a[220:223], v248 offset:12352\n"
        "v_mul_f32 v124, v117, v124\n"
        "v_mfma_f32_16x16x32_bf16 v[176:179], v[156:159], a[232:235], v[176:179]\n"
        "s_waitcnt lgkmcnt(6)\n"
        "ds_read_b128 a[224:227], v248 offset:49216\n"
        "v_mul_f32 v125, v117, v125\n"
        "v_mfma_f32_16x16x32_bf16 v[180:183], v[156:159], a[240:243], v[180:183]\n"
        "s_waitcnt lgkmcnt(5)\n"
        "ds_write_b32 v245, v120 offset:512\n"
        "v_mfma_f32_16x16x32_bf16 v[180:183], v[164:167], a[244:247], v[180:183]\n"
        "ds_write_b32 v245, v121 offset:516\n"
        "v_mfma_f32_16x16x32_bf16 v[212:215], v[152:155], a[244:247], v[212:215]\n"
        "ds_read_b128 a[228:231], v254 offset:12368\n"
        "v_maximum3_f32 v253, v253, v116, v117\n"
        "v_mfma_f32_16x16x32_bf16 v[212:215], v[160:163], a[240:243], v[212:215]\n"
        "s_waitcnt lgkmcnt(6)\n"
        "ds_read_b128 a[232:235], v254 offset:49232\n"
        "v_mul_f32 v126, v117, v126\n"
        "v_mfma_f32_16x16x32_bf16 v[216:219], v[160:163], a[248:251], v[216:219]\n"
        "s_waitcnt lgkmcnt(6)\n"
        "v_mul_f32 v127, v117, v127\n"
        "v_cvt_pk_bf16_f32 v124, v124, v125\n"
        "v_mfma_f32_16x16x32_bf16 v[216:219], v[152:155], a[216:219], v[216:219]\n"
        "v_cvt_pk_bf16_f32 v125, v126, v127\n"
        "v_mul_f32 v96, v117, v96\n"
        "v_mfma_f32_16x16x32_bf16 v[184:187], v[164:167], a[216:219], v[184:187]\n"
        "s_waitcnt vmcnt(0) lgkmcnt(5)\n"
        "ds_write_b32 v245, v124 offset:520\n"
        "v_mfma_f32_16x16x32_bf16 v[184:187], v[156:159], a[248:251], v[184:187]\n"
        "s_barrier\n"
        "ds_write_b32 v245, v125 offset:524\n"
        "v_mfma_f32_16x16x32_bf16 v[192:195], v[156:159], a[220:223], v[192:195]\n"
        "s_waitcnt lgkmcnt(6)\n"
        "v_mul_f32 v97, v117, v97\n"
        "v_mul_f32 v98, v117, v98\n"
        "v_mfma_f32_16x16x32_bf16 v[192:195], v[164:167], a[224:227], v[192:195]\n"
        "v_mul_f32 v99, v117, v99\n"
        "v_cvt_pk_bf16_f32 v96, v96, v97\n"
        "v_mfma_f32_16x16x32_bf16 v[220:223], v[152:155], a[224:227], v[220:223]\n"
        "v_cvt_pk_bf16_f32 v97, v98, v99\n"
        "v_mov_b64 v[80:81], 0\n"
        "v_mfma_f32_16x16x32_bf16 v[220:223], v[160:163], a[220:223], v[220:223]\n"
        "s_waitcnt lgkmcnt(3)\n"
        "ds_write_b32 v246, v96 offset:512\n"
        "v_mfma_f32_16x16x32_bf16 v[224:227], v[160:163], a[228:231], v[224:227]\n"
        "s_waitcnt lgkmcnt(3)\n"
        "ds_write_b32 v246, v97 offset:516\n"
        "v_mfma_f32_16x16x32_bf16 v[224:227], v[152:155], a[232:235], v[224:227]\n"
        "ds_read_b128 a[236:239], v240 offset:0\n"
        "v_mfma_f32_16x16x32_bf16 v[196:199], v[164:167], a[232:235], v[196:199]\n"
        "ds_read_b128 a[240:243], v241 offset:0\n"
        "v_mfma_f32_16x16x32_bf16 v[196:199], v[156:159], a[228:231], v[196:199]\n"
        "ds_read_b128 a[244:247], v240 offset:128\n"
        "v_smfmac_f32_16x16x64_bf16 v[80:83], a[252:255], v[88:95], v255\n"
        "ds_read_b128 a[248:251], v241 offset:128\n"
        "v_mfma_f32_16x16x32_bf16 v[84:87], v[128:131], v[88:91], 0\n"
        "ds_read_b128 a[216:219], v240 offset:256\n"
        "v_mfma_f32_16x16x32_bf16 v[84:87], v[132:135], v[92:95], v[84:87]\n"
        "ds_read_b128 a[220:223], v241 offset:256\n"
        "v_mfma_f32_16x16x32_bf16 v[232:235], v[140:143], v[92:95], 0\n"
        "v_mfma_f32_16x16x32_bf16 v[232:235], v[136:139], v[88:91], v[232:235]\n"
        "v_mfma_f32_16x16x32_bf16 v[236:239], v[144:147], v[88:91], 0\n"
        "v_mfma_f32_16x16x32_bf16 v[236:239], v[148:151], v[92:95], v[236:239]\n"
        "s_waitcnt lgkmcnt(0)\n"
        "s_branch 11f\n"
        ".p2align 6\n"
        "10:\n"
        "v_mfma_f32_16x16x32_bf16 v[232:235], v[140:143], v[92:95], 0\n"
        "v_mfma_f32_16x16x32_bf16 v[232:235], v[136:139], v[88:91], v[232:235]\n"
        "v_mfma_f32_16x16x32_bf16 v[236:239], v[144:147], v[88:91], 0\n"
        "v_mfma_f32_16x16x32_bf16 v[236:239], v[148:151], v[92:95], v[236:239]\n"
        "11:\n"
        "s_waitcnt lgkmcnt(5)\n"
        "v_mfma_f32_16x16x32_bf16 v[96:99], a[72:75], a[236:239], 0\n"
        "s_waitcnt lgkmcnt(4)\n"
        "v_and_b32 v117, 60, v250\n"
        "v_add3_u32 v117, v250, v117, s26\n"
        "v_mfma_f32_16x16x32_bf16 v[96:99], a[76:79], a[240:243], v[96:99]\n"
        "s_waitcnt lgkmcnt(3)\n"
        "v_cvt_pk_bf16_f32 v108, v172, v176\n"
        "v_cvt_pk_bf16_f32 v109, v180, v184\n"
        "v_mfma_f32_16x16x32_bf16 v[96:99], a[80:83], a[244:247], v[96:99]\n"
        "s_waitcnt lgkmcnt(2)\n"
        "v_cvt_pk_bf16_f32 v110, v192, v196\n"
        "v_add_f32 v80, v80, v81\n"
        "v_mfma_f32_16x16x32_bf16 v[96:99], a[84:87], a[248:251], v[96:99]\n"
        "s_waitcnt lgkmcnt(1)\n"
        "buffer_store_dwordx2 v[108:109], v117, s[16:19], s38 offen offset:0 sc1\n"
        "v_mfma_f32_16x16x32_bf16 v[96:99], a[88:91], a[216:219], v[96:99]\n"
        "s_waitcnt lgkmcnt(0)\n"
        "v_rcp_f32 v81, v80\n"
        "v_mfma_f32_16x16x32_bf16 v[96:99], a[92:95], a[220:223], v[96:99]\n"
        "s_add_u32 s41, s29, s40\n"
        "v_cvt_pk_bf16_f32 v112, v173, v177\n"
        "v_cvt_pk_bf16_f32 v113, v181, v185\n"
        "v_mfma_f32_16x16x32_bf16 v[100:103], a[116:119], a[220:223], 0\n"
        "s_add_u32 s35, s47, 786432\n"
        "v_mul_f32 v84, v81, v84\n"
        "v_mul_f32 v85, v81, v85\n"
        "v_mfma_f32_16x16x32_bf16 v[100:103], a[112:115], a[216:219], v[100:103]\n"
        "s_sub_u32 s46, s35, 100663296\n"
        "v_cvt_pk_bf16_f32 v152, v96, v97\n"
        "v_cvt_pk_bf16_f32 v153, v98, v99\n"
        "v_mfma_f32_16x16x32_bf16 v[100:103], a[108:111], a[248:251], v[100:103]\n"
        "s_cmp_lt_u32 s46, 786432\n"
        "buffer_store_dword v110, v250, s[16:19], s38 offen offset:0 sc1\n"
        "v_mfma_f32_16x16x32_bf16 v[100:103], a[104:107], a[244:247], v[100:103]\n"
        "s_cselect_b32 s35, s46, s35\n"
        "v_cvt_pk_bf16_f32 v114, v193, v197\n"
        "v_mul_f32 v86, v81, v86\n"
        "v_mfma_f32_16x16x32_bf16 v[100:103], a[100:103], a[240:243], v[100:103]\n"
        "s_sub_u32 s31, s28, s30\n"
        "v_mul_f32 v87, v81, v87\n"
        "v_cvt_pk_bf16_f32 v84, v84, v85\n"
        "v_mfma_f32_16x16x32_bf16 v[100:103], a[96:99], a[236:239], v[100:103]\n"
        "s_sub_u32 s45, s29, s28\n"
        "ds_write_b32 v245, v84 offset:768\n"
        "v_mfma_f32_16x16x32_bf16 v[104:107], a[120:123], a[236:239], 0\n"
        "s_mov_b32 s29, s30\n"
        "v_cvt_pk_bf16_f32 v85, v86, v87\n"
        "v_mul_f32 v232, v81, v232\n"
        "v_mfma_f32_16x16x32_bf16 v[104:107], a[124:127], a[240:243], v[104:107]\n"
        "s_mov_b32 s30, s28\n"
        "buffer_store_dwordx2 v[112:113], v117, s[16:19], s38 offen offset:384 sc1\n"
        "v_mfma_f32_16x16x32_bf16 v[104:107], a[128:131], a[244:247], v[104:107]\n"
        "s_add_u32 s28, s28, s45\n"
        "v_cvt_pk_bf16_f32 v154, v100, v101\n"
        "v_cvt_pk_bf16_f32 v155, v102, v103\n"
        "v_mfma_f32_16x16x32_bf16 v[104:107], a[132:135], a[248:251], v[104:107]\n"
        "ds_write_b32 v245, v85 offset:772\n"
        "v_mfma_f32_16x16x32_bf16 v[104:107], a[136:139], a[216:219], v[104:107]\n"
        "v_cvt_pk_bf16_f32 v108, v174, v178\n"
        "v_cvt_pk_bf16_f32 v109, v182, v186\n"
        "v_mfma_f32_16x16x32_bf16 v[104:107], a[140:143], a[220:223], v[104:107]\n"
        "v_mul_f32 v233, v81, v233\n"
        "v_maximum3_f32 v253, v253, v80, v81\n"
        "v_mfma_f32_16x16x32_bf16 v[96:99], a[20:23], a[220:223], 0\n"
        "buffer_store_dword v114, v250, s[16:19], s38 offen offset:384 sc1\n"
        "v_mfma_f32_16x16x32_bf16 v[96:99], a[16:19], a[216:219], v[96:99]\n"
        "v_cvt_pk_bf16_f32 v110, v194, v198\n"
        "v_mul_f32 v234, v81, v234\n"
        "v_mfma_f32_16x16x32_bf16 v[96:99], a[12:15], a[248:251], v[96:99]\n"
        "v_cvt_pk_bf16_f32 v168, v104, v105\n"
        "v_cvt_pk_bf16_f32 v169, v106, v107\n"
        "v_mfma_f32_16x16x32_bf16 v[96:99], a[8:11], a[244:247], v[96:99]\n"
        "v_mul_f32 v235, v81, v235\n"
        "v_cvt_pk_bf16_f32 v232, v232, v233\n"
        "v_mfma_f32_16x16x32_bf16 v[96:99], a[4:7], a[240:243], v[96:99]\n"
        "ds_write_b32 v245, v232 offset:776\n"
        "v_mfma_f32_16x16x32_bf16 v[96:99], a[0:3], a[236:239], v[96:99]\n"
        "buffer_store_dwordx2 v[108:109], v117, s[16:19], s38 offen offset:768 sc1\n"
        "v_mfma_f32_16x16x32_bf16 v[100:103], a[24:27], a[236:239], 0\n"
        "v_cvt_pk_bf16_f32 v112, v175, v179\n"
        "v_cvt_pk_bf16_f32 v113, v183, v187\n"
        "v_mfma_f32_16x16x32_bf16 v[100:103], a[28:31], a[240:243], v[100:103]\n"
        "v_cvt_pk_bf16_f32 v233, v234, v235\n"
        "v_xor_b32 v251, 0x2000, v251\n"
        "v_mfma_f32_16x16x32_bf16 v[100:103], a[32:35], a[244:247], v[100:103]\n"
        "v_mul_f32 v96, s50, v96\n"
        "v_mul_f32 v97, s50, v97\n"
        "v_mfma_f32_16x16x32_bf16 v[100:103], a[36:39], a[248:251], v[100:103]\n"
        "v_mul_f32 v98, s50, v98\n"
        "v_mul_f32 v99, s50, v99\n"
        "v_mfma_f32_16x16x32_bf16 v[100:103], a[40:43], a[216:219], v[100:103]\n"
        "v_cvt_pk_bf16_f32 v156, v96, v97\n"
        "v_cvt_pk_bf16_f32 v157, v98, v99\n"
        "v_mfma_f32_16x16x32_bf16 v[100:103], a[44:47], a[220:223], v[100:103]\n"
        "buffer_store_dword v110, v250, s[16:19], s38 offen offset:768 sc1\n"
        "v_mfma_f32_16x16x32_bf16 v[104:107], a[68:71], a[220:223], 0\n"
        "ds_write_b32 v245, v233 offset:780\n"
        "v_mfma_f32_16x16x32_bf16 v[104:107], a[64:67], a[216:219], v[104:107]\n"
        "v_cvt_pk_bf16_f32 v114, v195, v199\n"
        "v_xor_b32 v252, 0x2000, v252\n"
        "v_mfma_f32_16x16x32_bf16 v[104:107], a[60:63], a[248:251], v[104:107]\n"
        "ds_read_b128 a[224:227], v240 offset:1536\n"
        "v_mul_f32 v100, s50, v100\n"
        "v_mfma_f32_16x16x32_bf16 v[104:107], a[56:59], a[244:247], v[104:107]\n"
        "v_mul_f32 v101, s50, v101\n"
        "v_mul_f32 v102, s50, v102\n"
        "v_mfma_f32_16x16x32_bf16 v[104:107], a[52:55], a[240:243], v[104:107]\n"
        "v_mul_f32 v103, s50, v103\n"
        "v_cvt_pk_bf16_f32 v158, v100, v101\n"
        "v_mfma_f32_16x16x32_bf16 v[104:107], a[48:51], a[236:239], v[104:107]\n"
        "buffer_store_dwordx2 v[112:113], v117, s[16:19], s38 offen offset:1152 sc1\n"
        "v_mfma_f32_16x16x32_bf16 v[96:99], a[236:239], a[144:147], 0\n"
        "ds_read_b128 a[228:231], v241 offset:1536\n"
        "v_cvt_pk_bf16_f32 v159, v102, v103\n"
        "v_mfma_f32_16x16x32_bf16 v[96:99], a[240:243], a[148:151], v[96:99]\n"
        "ds_read_b128 a[232:235], v240 offset:1664\n"
        "v_cvt_pk_bf16_f32 v108, v200, v204\n"
        "v_mfma_f32_16x16x32_bf16 v[96:99], a[244:247], a[152:155], v[96:99]\n"
        "v_mul_f32 v104, s50, v104\n"
        "v_mul_f32 v105, s50, v105\n"
        "v_mfma_f32_16x16x32_bf16 v[96:99], a[248:251], a[156:159], v[96:99]\n"
        "buffer_store_dword v114, v250, s[16:19], s38 offen offset:1152 sc1\n"
        "v_mfma_f32_16x16x32_bf16 v[96:99], a[216:219], a[160:163], v[96:99]\n"
        "v_mul_f32 v106, s50, v106\n"
        "v_mul_f32 v107, s50, v107\n"
        "v_mfma_f32_16x16x32_bf16 v[96:99], a[220:223], a[164:167], v[96:99]\n"
        "v_cvt_pk_bf16_f32 v160, v104, v105\n"
        "v_cvt_pk_bf16_f32 v161, v106, v107\n"
        "v_mfma_f32_16x16x32_bf16 v[100:103], a[220:223], a[188:191], 0\n"
        "v_mov_b64 v[166:167], v[160:161]\n"
        "v_cvt_pk_bf16_f32 v109, v212, v216\n"
        "v_mfma_f32_16x16x32_bf16 v[100:103], a[216:219], a[184:187], v[100:103]\n"
        "buffer_store_dwordx2 v[108:109], v117, s[16:19], s39 offen offset:0 sc1\n"
        "v_mfma_f32_16x16x32_bf16 v[100:103], a[248:251], a[180:183], v[100:103]\n"
        "v_cvt_pk_bf16_f32 v128, v96, v97\n"
        "v_cvt_pk_bf16_f32 v129, v98, v99\n"
        "v_mfma_f32_16x16x32_bf16 v[100:103], a[244:247], a[176:179], v[100:103]\n"
        "v_cvt_pk_bf16_f32 v110, v220, v224\n"
        "v_cvt_pk_bf16_f32 v112, v201, v205\n"
        "v_mfma_f32_16x16x32_bf16 v[100:103], a[240:243], a[172:175], v[100:103]\n"
        "v_cvt_pk_bf16_f32 v113, v213, v217\n"
        "v_mul_f32 v236, v81, v236\n"
        "v_mfma_f32_16x16x32_bf16 v[100:103], a[236:239], a[168:171], v[100:103]\n"
        "v_mul_f32 v237, v81, v237\n"
        "v_mul_f32 v238, v81, v238\n"
        "v_mfma_f32_16x16x32_bf16 v[104:107], a[236:239], a[192:195], 0\n"
        "ds_read_b128 a[236:239], v241 offset:1664\n"
        "v_mul_f32 v239, v81, v239\n"
        "v_mfma_f32_16x16x32_bf16 v[104:107], a[240:243], a[196:199], v[104:107]\n"
        "ds_read_b128 a[240:243], v240 offset:1792\n"
        "v_cvt_pk_bf16_f32 v236, v236, v237\n"
        "v_mfma_f32_16x16x32_bf16 v[104:107], a[244:247], a[200:203], v[104:107]\n"
        "ds_read_b128 a[244:247], v241 offset:1792\n"
        "v_cvt_pk_bf16_f32 v136, v100, v101\n"
        "v_mfma_f32_16x16x32_bf16 v[104:107], a[248:251], a[204:207], v[104:107]\n"
        "buffer_store_dword v110, v250, s[16:19], s39 offen offset:0 sc1\n"
        "v_mfma_f32_16x16x32_bf16 v[104:107], a[216:219], a[208:211], v[104:107]\n"
        "v_cvt_pk_bf16_f32 v137, v102, v103\n"
        "v_cvt_pk_bf16_f32 v237, v238, v239\n"
        "v_mfma_f32_16x16x32_bf16 v[104:107], a[220:223], a[212:215], v[104:107]\n"
        "s_waitcnt lgkmcnt(5)\n"
        "buffer_store_dwordx2 v[112:113], v117, s[16:19], s39 offen offset:384 sc1\n"
        "v_mfma_f32_16x16x32_bf16 v[96:99], a[72:75], a[224:227], 0\n"
        "s_waitcnt lgkmcnt(4)\n"
        "ds_write_b32 v246, v236 offset:768\n"
        "v_mfma_f32_16x16x32_bf16 v[96:99], a[76:79], a[228:231], v[96:99]\n"
        "s_waitcnt lgkmcnt(4)\n"
        "ds_write_b32 v246, v237 offset:772\n"
        "v_mfma_f32_16x16x32_bf16 v[96:99], a[80:83], a[232:235], v[96:99]\n"
        "s_waitcnt lgkmcnt(4)\n"
        "ds_read_b128 v[162:165], v251 offset:0\n"
        "v_cvt_pk_bf16_f32 v114, v221, v225\n"
        "v_mfma_f32_16x16x32_bf16 v[96:99], a[84:87], a[236:239], v[96:99]\n"
        "s_waitcnt lgkmcnt(4)\n"
        "v_cvt_pk_bf16_f32 v144, v104, v105\n"
        "v_cvt_pk_bf16_f32 v145, v106, v107\n"
        "v_mfma_f32_16x16x32_bf16 v[96:99], a[88:91], a[240:243], v[96:99]\n"
        "s_waitcnt lgkmcnt(3)\n"
        "buffer_store_dword v114, v250, s[16:19], s39 offen offset:384 sc1\n"
        "v_mfma_f32_16x16x32_bf16 v[96:99], a[92:95], a[244:247], v[96:99]\n"
        "v_cvt_pk_bf16_f32 v108, v202, v206\n"
        "v_cvt_pk_bf16_f32 v109, v214, v218\n"
        "v_mfma_f32_16x16x32_bf16 v[100:103], a[116:119], a[244:247], 0\n"
        "v_cvt_pk_bf16_f32 v110, v222, v226\n"
        "v_add_u32 v245, s31, v245\n"
        "v_mfma_f32_16x16x32_bf16 v[100:103], a[112:115], a[240:243], v[100:103]\n"
        "v_add_u32 v246, s31, v246\n"
        "v_mfma_f32_16x16x32_bf16 v[100:103], a[108:111], a[236:239], v[100:103]\n"
        "v_cvt_pk_bf16_f32 v172, v96, v97\n"
        "v_cvt_pk_bf16_f32 v173, v98, v99\n"
        "v_mfma_f32_16x16x32_bf16 v[100:103], a[104:107], a[232:235], v[100:103]\n"
        "buffer_store_dwordx2 v[108:109], v117, s[16:19], s39 offen offset:768 sc1\n"
        "v_mfma_f32_16x16x32_bf16 v[100:103], a[100:103], a[228:231], v[100:103]\n"
        "v_cvt_pk_bf16_f32 v112, v203, v207\n"
        "v_cvt_pk_bf16_f32 v113, v215, v219\n"
        "v_mfma_f32_16x16x32_bf16 v[100:103], a[96:99], a[224:227], v[100:103]\n"
        "s_nop 2\n"
        "v_mfma_f32_16x16x32_bf16 v[104:107], a[120:123], a[224:227], 0\n"
        "s_nop 2\n"
        "v_mfma_f32_16x16x32_bf16 v[104:107], a[124:127], a[228:231], v[104:107]\n"
        "v_cvt_pk_bf16_f32 v174, v100, v101\n"
        "v_cvt_pk_bf16_f32 v175, v102, v103\n"
        "v_mfma_f32_16x16x32_bf16 v[104:107], a[128:131], a[232:235], v[104:107]\n"
        "buffer_store_dword v110, v250, s[16:19], s39 offen offset:768 sc1\n"
        "v_mfma_f32_16x16x32_bf16 v[104:107], a[132:135], a[236:239], v[104:107]\n"
        "v_cvt_pk_bf16_f32 v114, v223, v227\n"
        "v_mfma_f32_16x16x32_bf16 v[104:107], a[136:139], a[240:243], v[104:107]\n"
        "v_mfma_f32_16x16x32_bf16 v[104:107], a[140:143], a[244:247], v[104:107]\n"
        "s_nop 2\n"
        "v_mfma_f32_16x16x32_bf16 v[96:99], a[20:23], a[244:247], 0\n"
        "s_nop 2\n"
        "v_mfma_f32_16x16x32_bf16 v[96:99], a[16:19], a[240:243], v[96:99]\n"
        "buffer_store_dwordx2 v[112:113], v117, s[16:19], s39 offen offset:1152 sc1\n"
        "v_mfma_f32_16x16x32_bf16 v[96:99], a[12:15], a[236:239], v[96:99]\n"
        "v_cvt_pk_bf16_f32 v190, v104, v105\n"
        "v_cvt_pk_bf16_f32 v191, v106, v107\n"
        "v_mfma_f32_16x16x32_bf16 v[96:99], a[8:11], a[232:235], v[96:99]\n"
        "v_mfma_f32_16x16x32_bf16 v[96:99], a[4:7], a[228:231], v[96:99]\n"
        "ds_read_b128 v[182:185], v251 offset:512\n"
        "v_mfma_f32_16x16x32_bf16 v[96:99], a[0:3], a[224:227], v[96:99]\n"
        "s_nop 2\n"
        "v_mfma_f32_16x16x32_bf16 v[64:67], v[152:155], v[156:159], v[0:3]\n"
        "s_waitcnt lgkmcnt(1)\n"
        "buffer_store_dword v114, v250, s[16:19], s39 offen offset:1152 sc1\n"
        "v_mfma_f32_16x16x32_bf16 v[64:67], v[168:171], v[160:163], v[64:67]\n"
        "s_nop 0\n"
        "v_mfma_f32_16x16x32_bf16 v[68:71], v[172:175], v[156:159], v[4:7]\n"
        "v_mul_f32 v96, s50, v96\n"
        "v_mul_f32 v97, s50, v97\n"
        "v_mfma_f32_16x16x32_bf16 v[68:71], v[188:191], v[164:167], v[68:71]\n"
        "v_mul_f32 v98, s50, v98\n"
        "v_mul_f32 v99, s50, v99\n"
        "v_mfma_f32_16x16x32_bf16 v[100:103], a[24:27], a[224:227], 0\n"
        "v_cvt_pk_bf16_f32 v176, v96, v97\n"
        "v_cvt_pk_bf16_f32 v177, v98, v99\n"
        "v_mfma_f32_16x16x32_bf16 v[100:103], a[28:31], a[228:231], v[100:103]\n"
        "v_exp_f32 v64, v64\n"
        "v_mfma_f32_16x16x32_bf16 v[100:103], a[32:35], a[232:235], v[100:103]\n"
        "s_add_u32 m0, s41, 0\n"
        "v_exp_f32 v65, v65\n"
        "v_mfma_f32_16x16x32_bf16 v[100:103], a[36:39], a[236:239], v[100:103]\n"
        "v_exp_f32 v66, v66\n"
        "v_mfma_f32_16x16x32_bf16 v[100:103], a[40:43], a[240:243], v[100:103]\n"
        "buffer_load_dwordx4 v242, s[8:11], s47 offen lds\n"
        "v_mfma_f32_16x16x32_bf16 v[100:103], a[44:47], a[244:247], v[100:103]\n"
        "v_exp_f32 v67, v67\n"
        "v_mfma_f32_16x16x32_bf16 v[104:107], a[68:71], a[244:247], 0\n"
        "v_exp_f32 v68, v68\n"
        "v_mfma_f32_16x16x32_bf16 v[104:107], a[64:67], a[240:243], v[104:107]\n"
        "s_add_u32 m0, s41, 1024\n"
        "v_exp_f32 v69, v69\n"
        "v_mfma_f32_16x16x32_bf16 v[104:107], a[60:63], a[236:239], v[104:107]\n"
        "v_exp_f32 v70, v70\n"
        "v_mfma_f32_16x16x32_bf16 v[104:107], a[56:59], a[232:235], v[104:107]\n"
        "buffer_load_dwordx4 v243, s[8:11], s47 offen lds\n"
        "v_mfma_f32_16x16x32_bf16 v[104:107], a[52:55], a[228:231], v[104:107]\n"
        "v_mul_f32 v100, s50, v100\n"
        "v_mul_f32 v101, s50, v101\n"
        "v_mfma_f32_16x16x32_bf16 v[104:107], a[48:51], a[224:227], v[104:107]\n"
        "v_mul_f32 v102, s50, v102\n"
        "v_mul_f32 v103, s50, v103\n"
        "v_mfma_f32_16x16x32_bf16 v[96:99], a[224:227], a[144:147], 0\n"
        "s_add_u32 m0, s41, 2048\n"
        "v_cvt_pk_bf16_f32 v178, v100, v101\n"
        "v_cvt_pk_bf16_f32 v179, v102, v103\n"
        "v_mfma_f32_16x16x32_bf16 v[96:99], a[228:231], a[148:151], v[96:99]\n"
        "ds_read_b128 a[248:251], v240 offset:12288\n"
        "v_mul_f32 v104, s50, v104\n"
        "v_mfma_f32_16x16x32_bf16 v[96:99], a[232:235], a[152:155], v[96:99]\n"
        "buffer_load_dwordx4 v244, s[8:11], s47 offen lds\n"
        "v_mfma_f32_16x16x32_bf16 v[96:99], a[236:239], a[156:159], v[96:99]\n"
        "v_mul_f32 v105, s50, v105\n"
        "v_mul_f32 v106, s50, v106\n"
        "v_mfma_f32_16x16x32_bf16 v[96:99], a[240:243], a[160:163], v[96:99]\n"
        "v_mul_f32 v107, s50, v107\n"
        "v_cvt_pk_bf16_f32 v180, v104, v105\n"
        "v_mfma_f32_16x16x32_bf16 v[96:99], a[244:247], a[164:167], v[96:99]\n"
        "s_add_u32 m0, s41, 12288\n"
        "ds_read_b128 a[216:219], v241 offset:12288\n"
        "v_cvt_pk_bf16_f32 v181, v106, v107\n"
        "v_mfma_f32_16x16x32_bf16 v[72:75], v[152:155], v[176:179], v[16:19]\n"
        "s_waitcnt lgkmcnt(2)\n"
        "ds_read_b128 a[220:223], v240 offset:12416\n"
        "v_mov_b64 v[186:187], v[180:181]\n"
        "v_mfma_f32_16x16x32_bf16 v[72:75], v[168:171], v[180:183], v[72:75]\n"
        "buffer_load_dwordx4 v242, s[8:11], s35 offen lds\n"
        "v_mfma_f32_16x16x32_bf16 v[76:79], v[172:175], v[176:179], v[20:23]\n"
        "v_cvt_pk_bf16_f32 v130, v96, v97\n"
        "v_cvt_pk_bf16_f32 v131, v98, v99\n"
        "v_mfma_f32_16x16x32_bf16 v[76:79], v[188:191], v[184:187], v[76:79]\n"
        "v_exp_f32 v71, v71\n"
        "v_mfma_f32_16x16x32_bf16 v[100:103], a[244:247], a[188:191], 0\n"
        "s_add_u32 m0, s41, 13312\n"
        "v_exp_f32 v72, v72\n"
        "v_mfma_f32_16x16x32_bf16 v[100:103], a[240:243], a[184:187], v[100:103]\n"
        "v_exp_f32 v73, v73\n"
        "v_mfma_f32_16x16x32_bf16 v[100:103], a[236:239], a[180:183], v[100:103]\n"
        "buffer_load_dwordx4 v243, s[8:11], s35 offen lds\n"
        "v_mfma_f32_16x16x32_bf16 v[100:103], a[232:235], a[176:179], v[100:103]\n"
        "v_exp_f32 v74, v74\n"
        "v_mfma_f32_16x16x32_bf16 v[100:103], a[228:231], a[172:175], v[100:103]\n"
        "v_exp_f32 v75, v75\n"
        "v_mfma_f32_16x16x32_bf16 v[100:103], a[224:227], a[168:171], v[100:103]\n"
        "s_add_u32 m0, s41, 14336\n"
        "v_exp_f32 v76, v76\n"
        "v_mfma_f32_16x16x32_bf16 v[104:107], a[224:227], a[192:195], 0\n"
        "ds_read_b128 a[224:227], v241 offset:12416\n"
        "v_cvt_pk_bf16_f32 v64, v64, v65\n"
        "v_mfma_f32_16x16x32_bf16 v[104:107], a[228:231], a[196:199], v[104:107]\n"
        "ds_read_b128 a[228:231], v240 offset:12544\n"
        "v_cvt_pk_bf16_f32 v65, v66, v67\n"
        "v_mfma_f32_16x16x32_bf16 v[104:107], a[232:235], a[200:203], v[104:107]\n"
        "ds_read_b128 a[232:235], v241 offset:12544\n"
        "v_cvt_pk_bf16_f32 v138, v100, v101\n"
        "v_mfma_f32_16x16x32_bf16 v[104:107], a[236:239], a[204:207], v[104:107]\n"
        "buffer_load_dwordx4 v244, s[8:11], s35 offen lds\n"
        "v_mfma_f32_16x16x32_bf16 v[104:107], a[240:243], a[208:211], v[104:107]\n"
        "s_mov_b32 m0, s48\n"
        "v_cvt_pk_bf16_f32 v139, v102, v103\n"
        "v_cvt_pk_bf16_f32 v66, v68, v69\n"
        "v_mfma_f32_16x16x32_bf16 v[104:107], a[244:247], a[212:215], v[104:107]\n"
        "s_waitcnt lgkmcnt(5)\n"
        "v_exp_f32 v77, v77\n"
        "v_mfma_f32_16x16x32_bf16 v[96:99], a[72:75], a[248:251], 0\n"
        "s_waitcnt lgkmcnt(4)\n"
        "buffer_load_dwordx4 v249, s[12:15], s36 offen offset:0 lds\n"
        "v_mfma_f32_16x16x32_bf16 v[96:99], a[76:79], a[216:219], v[96:99]\n"
        "s_waitcnt lgkmcnt(3)\n"
        "ds_read_b128 v[162:165], v252 offset:0\n"
        "v_xor_b32 v116, 32, v249\n"
        "v_mfma_f32_16x16x32_bf16 v[96:99], a[80:83], a[220:223], v[96:99]\n"
        "s_waitcnt lgkmcnt(3)\n"
        "ds_read_b128 v[182:185], v252 offset:512\n"
        "v_cvt_pk_bf16_f32 v146, v104, v105\n"
        "v_mfma_f32_16x16x32_bf16 v[96:99], a[84:87], a[224:227], v[96:99]\n"
        "s_waitcnt lgkmcnt(3)\n"
        "v_cvt_pk_bf16_f32 v147, v106, v107\n"
        "v_cvt_pk_bf16_f32 v67, v70, v71\n"
        "v_mfma_f32_16x16x32_bf16 v[96:99], a[88:91], a[228:231], v[96:99]\n"
        "s_waitcnt lgkmcnt(2)\n"
        "v_exp_f32 v78, v78\n"
        "v_mfma_f32_16x16x32_bf16 v[96:99], a[92:95], a[232:235], v[96:99]\n"
        "buffer_load_dwordx4 v116, s[12:15], s36 offen offset:1024 lds\n"
        "v_mfma_f32_16x16x32_bf16 v[100:103], a[116:119], a[232:235], 0\n"
        "v_exp_f32 v79, v79\n"
        "v_mfma_f32_16x16x32_bf16 v[100:103], a[112:115], a[228:231], v[100:103]\n"
        "v_cvt_pk_bf16_f32 v72, v72, v73\n"
        "v_cvt_pk_bf16_f32 v73, v74, v75\n"
        "v_mfma_f32_16x16x32_bf16 v[100:103], a[108:111], a[224:227], v[100:103]\n"
        "v_cvt_pk_bf16_f32 v74, v76, v77\n"
        "v_cvt_pk_bf16_f32 v192, v96, v97\n"
        "v_mfma_f32_16x16x32_bf16 v[100:103], a[104:107], a[220:223], v[100:103]\n"
        "v_cvt_pk_bf16_f32 v193, v98, v99\n"
        "v_cvt_pk_bf16_f32 v75, v78, v79\n"
        "v_mfma_f32_16x16x32_bf16 v[100:103], a[100:103], a[216:219], v[100:103]\n"
        "v_mfma_f32_16x16x32_bf16 v[100:103], a[96:99], a[248:251], v[100:103]\n"
        "s_nop 2\n"
        "v_mfma_f32_16x16x32_bf16 v[104:107], a[120:123], a[248:251], 0\n"
        "s_nop 2\n"
        "v_mfma_f32_16x16x32_bf16 v[104:107], a[124:127], a[216:219], v[104:107]\n"
        "v_cvt_pk_bf16_f32 v194, v100, v101\n"
        "v_cvt_pk_bf16_f32 v195, v102, v103\n"
        "v_mfma_f32_16x16x32_bf16 v[104:107], a[128:131], a[220:223], v[104:107]\n"
        "v_mfma_f32_16x16x32_bf16 v[104:107], a[132:135], a[224:227], v[104:107]\n"
        "44:\n"
        "s_cbranch_execz 40b\n"
        "v_mfma_f32_16x16x32_bf16 v[104:107], a[136:139], a[228:231], v[104:107]\n"
        "v_mfma_f32_16x16x32_bf16 v[104:107], a[140:143], a[232:235], v[104:107]\n"
        "s_nop 2\n"
        "v_mfma_f32_16x16x32_bf16 v[96:99], a[20:23], a[232:235], 0\n"
        "s_nop 2\n"
        "v_mfma_f32_16x16x32_bf16 v[96:99], a[16:19], a[228:231], v[96:99]\n"
        "v_cvt_pk_bf16_f32 v208, v104, v105\n"
        "v_cvt_pk_bf16_f32 v209, v106, v107\n"
        "v_mfma_f32_16x16x32_bf16 v[96:99], a[12:15], a[224:227], v[96:99]\n"
        "v_mfma_f32_16x16x32_bf16 v[96:99], a[8:11], a[220:223], v[96:99]\n"
        "v_mfma_f32_16x16x32_bf16 v[96:99], a[4:7], a[216:219], v[96:99]\n"
        "ds_read_b128 v[202:205], v251 offset:4096\n"
        "v_mfma_f32_16x16x32_bf16 v[96:99], a[0:3], a[248:251], v[96:99]\n"
        "s_nop 2\n"
        "v_mfma_f32_16x16x32_bf16 v[68:71], v[192:195], v[156:159], v[8:11]\n"
        "s_waitcnt lgkmcnt(2)\n"
        "s_nop 1\n"
        "v_mfma_f32_16x16x32_bf16 v[68:71], v[208:211], v[160:163], v[68:71]\n"
        "v_mfma_f32_16x16x32_bf16 v[76:79], v[192:195], v[176:179], v[24:27]\n"
        "s_waitcnt lgkmcnt(1)\n"
        "v_mul_f32 v96, s50, v96\n"
        "v_mul_f32 v97, s50, v97\n"
        "v_mfma_f32_16x16x32_bf16 v[76:79], v[208:211], v[180:183], v[76:79]\n"
        "v_mul_f32 v98, s50, v98\n"
        "v_mul_f32 v99, s50, v99\n"
        "v_mfma_f32_16x16x32_bf16 v[100:103], a[24:27], a[248:251], 0\n"
        "v_cvt_pk_bf16_f32 v196, v96, v97\n"
        "v_cvt_pk_bf16_f32 v197, v98, v99\n"
        "v_mfma_f32_16x16x32_bf16 v[100:103], a[28:31], a[216:219], v[100:103]\n"
        "v_exp_f32 v68, v68\n"
        "v_mfma_f32_16x16x32_bf16 v[100:103], a[32:35], a[220:223], v[100:103]\n"
        "v_exp_f32 v69, v69\n"
        "v_mfma_f32_16x16x32_bf16 v[100:103], a[36:39], a[224:227], v[100:103]\n"
        "v_exp_f32 v70, v70\n"
        "v_mfma_f32_16x16x32_bf16 v[100:103], a[40:43], a[228:231], v[100:103]\n"
        "v_exp_f32 v71, v71\n"
        "v_mfma_f32_16x16x32_bf16 v[100:103], a[44:47], a[232:235], v[100:103]\n"
        "v_exp_f32 v76, v76\n"
        "v_mfma_f32_16x16x32_bf16 v[104:107], a[68:71], a[232:235], 0\n"
        "v_exp_f32 v77, v77\n"
        "v_mfma_f32_16x16x32_bf16 v[104:107], a[64:67], a[228:231], v[104:107]\n"
        "v_exp_f32 v78, v78\n"
        "v_mfma_f32_16x16x32_bf16 v[104:107], a[60:63], a[224:227], v[104:107]\n"
        "v_exp_f32 v79, v79\n"
        "v_mfma_f32_16x16x32_bf16 v[104:107], a[56:59], a[220:223], v[104:107]\n"
        "v_mul_f32 v100, s50, v100\n"
        "v_mul_f32 v101, s50, v101\n"
        "v_mfma_f32_16x16x32_bf16 v[104:107], a[52:55], a[216:219], v[104:107]\n"
        "v_mul_f32 v102, s50, v102\n"
        "v_mul_f32 v103, s50, v103\n"
        "v_mfma_f32_16x16x32_bf16 v[104:107], a[48:51], a[248:251], v[104:107]\n"
        "v_cvt_pk_bf16_f32 v198, v100, v101\n"
        "v_cvt_pk_bf16_f32 v199, v102, v103\n"
        "v_mfma_f32_16x16x32_bf16 v[96:99], a[248:251], a[144:147], 0\n"
        "ds_read_b128 a[236:239], v240 offset:13824\n"
        "v_cvt_pk_bf16_f32 v68, v68, v69\n"
        "v_mfma_f32_16x16x32_bf16 v[96:99], a[216:219], a[148:151], v[96:99]\n"
        "ds_read_b128 a[240:243], v241 offset:13824\n"
        "v_cvt_pk_bf16_f32 v69, v70, v71\n"
        "v_mfma_f32_16x16x32_bf16 v[96:99], a[220:223], a[152:155], v[96:99]\n"
        "v_mul_f32 v104, s50, v104\n"
        "v_mul_f32 v105, s50, v105\n"
        "v_mfma_f32_16x16x32_bf16 v[96:99], a[224:227], a[156:159], v[96:99]\n"
        "v_mul_f32 v106, s50, v106\n"
        "v_mul_f32 v107, s50, v107\n"
        "v_mfma_f32_16x16x32_bf16 v[96:99], a[228:231], a[160:163], v[96:99]\n"
        "v_cvt_pk_bf16_f32 v200, v104, v105\n"
        "v_cvt_pk_bf16_f32 v201, v106, v107\n"
        "v_mfma_f32_16x16x32_bf16 v[96:99], a[232:235], a[164:167], v[96:99]\n"
        "ds_read_b128 a[244:247], v240 offset:13952\n"
        "v_mov_b64 v[206:207], v[200:201]\n"
        "v_mfma_f32_16x16x32_bf16 v[80:83], v[152:155], v[196:199], v[32:35]\n"
        "s_waitcnt lgkmcnt(3)\n"
        "v_cvt_pk_bf16_f32 v76, v76, v77\n"
        "v_cvt_pk_bf16_f32 v77, v78, v79\n"
        "v_mfma_f32_16x16x32_bf16 v[80:83], v[168:171], v[200:203], v[80:83]\n"
        "s_nop 0\n"
        "v_mfma_f32_16x16x32_bf16 v[84:87], v[172:175], v[196:199], v[36:39]\n"
        "v_cvt_pk_bf16_f32 v132, v96, v97\n"
        "v_cvt_pk_bf16_f32 v133, v98, v99\n"
        "v_mfma_f32_16x16x32_bf16 v[84:87], v[188:191], v[204:207], v[84:87]\n"
        "ds_read_b128 v[202:205], v252 offset:4096\n"
        "v_mfma_f32_16x16x32_bf16 v[100:103], a[232:235], a[188:191], 0\n"
        "v_mfma_f32_16x16x32_bf16 v[100:103], a[228:231], a[184:187], v[100:103]\n"
        "v_exp_f32 v80, v80\n"
        "v_mfma_f32_16x16x32_bf16 v[100:103], a[224:227], a[180:183], v[100:103]\n"
        "v_exp_f32 v81, v81\n"
        "v_mfma_f32_16x16x32_bf16 v[100:103], a[220:223], a[176:179], v[100:103]\n"
        "v_exp_f32 v82, v82\n"
        "v_mfma_f32_16x16x32_bf16 v[100:103], a[216:219], a[172:175], v[100:103]\n"
        "v_exp_f32 v83, v83\n"
        "v_mfma_f32_16x16x32_bf16 v[100:103], a[248:251], a[168:171], v[100:103]\n"
        "v_exp_f32 v84, v84\n"
        "v_mfma_f32_16x16x32_bf16 v[104:107], a[248:251], a[192:195], 0\n"
        "ds_read_b128 a[248:251], v241 offset:13952\n"
        "v_cvt_pk_bf16_f32 v80, v80, v81\n"
        "v_mfma_f32_16x16x32_bf16 v[104:107], a[216:219], a[196:199], v[104:107]\n"
        "ds_read_b128 a[216:219], v240 offset:14080\n"
        "v_cvt_pk_bf16_f32 v81, v82, v83\n"
        "v_mfma_f32_16x16x32_bf16 v[104:107], a[220:223], a[200:203], v[104:107]\n"
        "ds_read_b128 a[220:223], v241 offset:14080\n"
        "v_cvt_pk_bf16_f32 v140, v100, v101\n"
        "v_mfma_f32_16x16x32_bf16 v[104:107], a[224:227], a[204:207], v[104:107]\n"
        "v_cvt_pk_bf16_f32 v141, v102, v103\n"
        "v_mfma_f32_16x16x32_bf16 v[104:107], a[228:231], a[208:211], v[104:107]\n"
        "v_exp_f32 v85, v85\n"
        "v_mfma_f32_16x16x32_bf16 v[104:107], a[232:235], a[212:215], v[104:107]\n"
        "s_waitcnt lgkmcnt(6)\n"
        "v_exp_f32 v86, v86\n"
        "v_mfma_f32_16x16x32_bf16 v[96:99], a[72:75], a[236:239], 0\n"
        "s_waitcnt lgkmcnt(5)\n"
        "v_cvt_pk_bf16_f32 v82, v84, v85\n"
        "v_mfma_f32_16x16x32_bf16 v[96:99], a[76:79], a[240:243], v[96:99]\n"
        "s_waitcnt lgkmcnt(4)\n"
        "v_exp_f32 v87, v87\n"
        "v_mfma_f32_16x16x32_bf16 v[96:99], a[80:83], a[244:247], v[96:99]\n"
        "s_waitcnt lgkmcnt(2)\n"
        "v_cvt_pk_bf16_f32 v83, v86, v87\n"
        "v_mfma_f32_16x16x32_bf16 v[96:99], a[84:87], a[248:251], v[96:99]\n"
        "s_waitcnt lgkmcnt(1)\n"
        "v_cvt_pk_bf16_f32 v148, v104, v105\n"
        "v_cvt_pk_bf16_f32 v149, v106, v107\n"
        "v_mfma_f32_16x16x32_bf16 v[96:99], a[88:91], a[216:219], v[96:99]\n"
        "s_waitcnt lgkmcnt(0)\n"
        "v_mfma_f32_16x16x32_bf16 v[96:99], a[92:95], a[220:223], v[96:99]\n"
        "s_nop 2\n"
        "v_mfma_f32_16x16x32_bf16 v[84:87], v[192:195], v[196:199], v[40:43]\n"
        "s_nop 1\n"
        "v_mfma_f32_16x16x32_bf16 v[84:87], v[208:211], v[200:203], v[84:87]\n"
        "v_mfma_f32_16x16x32_bf16 v[100:103], a[116:119], a[220:223], 0\n"
        "v_cvt_pk_bf16_f32 v212, v96, v97\n"
        "v_cvt_pk_bf16_f32 v213, v98, v99\n"
        "v_mfma_f32_16x16x32_bf16 v[100:103], a[112:115], a[216:219], v[100:103]\n"
        "v_add_u32 v240, s45, v240\n"
        "v_add_u32 v241, s45, v241\n"
        "v_mfma_f32_16x16x32_bf16 v[100:103], a[108:111], a[248:251], v[100:103]\n"
        "v_mfma_f32_16x16x32_bf16 v[100:103], a[104:107], a[244:247], v[100:103]\n"
        "v_exp_f32 v84, v84\n"
        "v_mfma_f32_16x16x32_bf16 v[100:103], a[100:103], a[240:243], v[100:103]\n"
        "v_exp_f32 v85, v85\n"
        "v_mfma_f32_16x16x32_bf16 v[100:103], a[96:99], a[236:239], v[100:103]\n"
        "v_exp_f32 v86, v86\n"
        "v_mfma_f32_16x16x32_bf16 v[104:107], a[120:123], a[236:239], 0\n"
        "v_exp_f32 v87, v87\n"
        "v_mfma_f32_16x16x32_bf16 v[104:107], a[124:127], a[240:243], v[104:107]\n"
        "v_cvt_pk_bf16_f32 v84, v84, v85\n"
        "v_cvt_pk_bf16_f32 v85, v86, v87\n"
        "v_mfma_f32_16x16x32_bf16 v[104:107], a[128:131], a[244:247], v[104:107]\n"
        "v_mfma_f32_16x16x32_bf16 v[104:107], a[132:135], a[248:251], v[104:107]\n"
        "v_cvt_pk_bf16_f32 v214, v100, v101\n"
        "v_cvt_pk_bf16_f32 v215, v102, v103\n"
        "v_mfma_f32_16x16x32_bf16 v[104:107], a[136:139], a[216:219], v[104:107]\n"
        "v_mfma_f32_16x16x32_bf16 v[104:107], a[140:143], a[220:223], v[104:107]\n"
        "s_nop 2\n"
        "v_mfma_f32_16x16x32_bf16 v[96:99], a[20:23], a[220:223], 0\n"
        "s_nop 2\n"
        "v_mfma_f32_16x16x32_bf16 v[96:99], a[16:19], a[216:219], v[96:99]\n"
        "v_cvt_pk_bf16_f32 v230, v104, v105\n"
        "v_cvt_pk_bf16_f32 v231, v106, v107\n"
        "v_mfma_f32_16x16x32_bf16 v[96:99], a[12:15], a[248:251], v[96:99]\n"
        "s_barrier\n"
        "v_mfma_f32_16x16x32_bf16 v[96:99], a[8:11], a[244:247], v[96:99]\n"
        "s_cmp_eq_u32 s37, 100663296\n"
        "v_mfma_f32_16x16x32_bf16 v[96:99], a[4:7], a[240:243], v[96:99]\n"
        "s_cselect_b32 s46, 0, s37\n"
        "v_mfma_f32_16x16x32_bf16 v[96:99], a[0:3], a[236:239], v[96:99]\n"
        "s_add_u32 s38, s46, s43\n"
        "s_nop 1\n"
        "v_mfma_f32_16x16x32_bf16 v[108:111], v[212:215], v[156:159], v[12:15]\n"
        "s_add_u32 s39, s46, s44\n"
        "ds_read_b128 v[222:225], v251 offset:4608\n"
        "v_mfma_f32_16x16x32_bf16 v[108:111], v[228:231], v[164:167], v[108:111]\n"
        "s_add_u32 s37, s37, 6291456\n"
        "v_mul_f32 v96, s50, v96\n"
        "v_mul_f32 v97, s50, v97\n"
        "v_mfma_f32_16x16x32_bf16 v[112:115], v[212:215], v[176:179], v[28:31]\n"
        "v_mul_f32 v98, s50, v98\n"
        "v_mul_f32 v99, s50, v99\n"
        "v_mfma_f32_16x16x32_bf16 v[112:115], v[228:231], v[184:187], v[112:115]\n"
        "v_cvt_pk_bf16_f32 v216, v96, v97\n"
        "v_cvt_pk_bf16_f32 v217, v98, v99\n"
        "v_mfma_f32_16x16x32_bf16 v[116:119], v[212:215], v[196:199], v[44:47]\n"
        "v_exp_f32 v108, v108\n"
        "v_mfma_f32_16x16x32_bf16 v[116:119], v[228:231], v[204:207], v[116:119]\n"
        "v_exp_f32 v109, v109\n"
        "v_mfma_f32_16x16x32_bf16 v[100:103], a[24:27], a[236:239], 0\n"
        "v_exp_f32 v110, v110\n"
        "v_mfma_f32_16x16x32_bf16 v[100:103], a[28:31], a[240:243], v[100:103]\n"
        "v_exp_f32 v111, v111\n"
        "v_mfma_f32_16x16x32_bf16 v[100:103], a[32:35], a[244:247], v[100:103]\n"
        "v_exp_f32 v112, v112\n"
        "v_mfma_f32_16x16x32_bf16 v[100:103], a[36:39], a[248:251], v[100:103]\n"
        "v_exp_f32 v113, v113\n"
        "v_mfma_f32_16x16x32_bf16 v[100:103], a[40:43], a[216:219], v[100:103]\n"
        "v_exp_f32 v114, v114\n"
        "v_mfma_f32_16x16x32_bf16 v[100:103], a[44:47], a[220:223], v[100:103]\n"
        "v_exp_f32 v115, v115\n"
        "v_mfma_f32_16x16x32_bf16 v[104:107], a[68:71], a[220:223], 0\n"
        "v_exp_f32 v116, v116\n"
        "v_mfma_f32_16x16x32_bf16 v[104:107], a[64:67], a[216:219], v[104:107]\n"
        "v_exp_f32 v117, v117\n"
        "v_mfma_f32_16x16x32_bf16 v[104:107], a[60:63], a[248:251], v[104:107]\n"
        "v_exp_f32 v118, v118\n"
        "v_mfma_f32_16x16x32_bf16 v[104:107], a[56:59], a[244:247], v[104:107]\n"
        "v_mul_f32 v100, s50, v100\n"
        "v_mul_f32 v101, s50, v101\n"
        "v_mfma_f32_16x16x32_bf16 v[104:107], a[52:55], a[240:243], v[104:107]\n"
        "v_mul_f32 v102, s50, v102\n"
        "v_mul_f32 v103, s50, v103\n"
        "v_mfma_f32_16x16x32_bf16 v[104:107], a[48:51], a[236:239], v[104:107]\n"
        "v_cvt_pk_bf16_f32 v218, v100, v101\n"
        "v_cvt_pk_bf16_f32 v219, v102, v103\n"
        "v_mfma_f32_16x16x32_bf16 v[96:99], a[236:239], a[144:147], 0\n"
        "v_exp_f32 v119, v119\n"
        "v_mfma_f32_16x16x32_bf16 v[96:99], a[240:243], a[148:151], v[96:99]\n"
        "v_cvt_pk_bf16_f32 v70, v108, v109\n"
        "v_cvt_pk_bf16_f32 v71, v110, v111\n"
        "v_mfma_f32_16x16x32_bf16 v[96:99], a[244:247], a[152:155], v[96:99]\n"
        "v_mul_f32 v104, s50, v104\n"
        "v_mul_f32 v105, s50, v105\n"
        "v_mfma_f32_16x16x32_bf16 v[96:99], a[248:251], a[156:159], v[96:99]\n"
        "v_mul_f32 v106, s50, v106\n"
        "v_mul_f32 v107, s50, v107\n"
        "v_mfma_f32_16x16x32_bf16 v[96:99], a[216:219], a[160:163], v[96:99]\n"
        "v_cvt_pk_bf16_f32 v220, v104, v105\n"
        "v_cvt_pk_bf16_f32 v221, v106, v107\n"
        "v_mfma_f32_16x16x32_bf16 v[96:99], a[220:223], a[164:167], v[96:99]\n"
        "ds_read_b128 v[156:159], v247 offset:0\n"
        "v_mov_b64 v[226:227], v[220:221]\n"
        "v_mfma_f32_16x16x32_bf16 v[88:91], v[152:155], v[216:219], v[48:51]\n"
        "s_waitcnt lgkmcnt(1)\n"
        "ds_read_b128 v[164:167], v247 offset:4096\n"
        "v_cvt_pk_bf16_f32 v78, v112, v113\n"
        "v_mfma_f32_16x16x32_bf16 v[88:91], v[168:171], v[220:223], v[88:91]\n"
        "ds_read_b128 v[160:163], v247 offset:256\n"
        "v_cvt_pk_bf16_f32 v79, v114, v115\n"
        "v_mfma_f32_16x16x32_bf16 v[92:95], v[172:175], v[216:219], v[52:55]\n"
        "s_add_u32 s47, s47, 6291456\n"
        "ds_read_b128 v[152:155], v247 offset:4352\n"
        "v_cvt_pk_bf16_f32 v134, v96, v97\n"
        "v_mfma_f32_16x16x32_bf16 v[92:95], v[188:191], v[224:227], v[92:95]\n"
        "s_add_u32 s36, s36, 2097152\n"
        "ds_read_b128 v[222:225], v252 offset:4608\n"
        "v_cvt_pk_bf16_f32 v135, v98, v99\n"
        "v_mfma_f32_16x16x32_bf16 v[100:103], a[220:223], a[188:191], 0\n"
        "s_xor_b32 s48, s48, 8192\n"
        "v_exp_f32 v88, v88\n"
        "v_mfma_f32_16x16x32_bf16 v[100:103], a[216:219], a[184:187], v[100:103]\n"
        "v_exp_f32 v89, v89\n"
        "v_mfma_f32_16x16x32_bf16 v[100:103], a[248:251], a[180:183], v[100:103]\n"
        "v_exp_f32 v90, v90\n"
        "v_mfma_f32_16x16x32_bf16 v[100:103], a[244:247], a[176:179], v[100:103]\n"
        "v_exp_f32 v91, v91\n"
        "v_mfma_f32_16x16x32_bf16 v[100:103], a[240:243], a[172:175], v[100:103]\n"
        "v_exp_f32 v92, v92\n"
        "v_mfma_f32_16x16x32_bf16 v[100:103], a[236:239], a[168:171], v[100:103]\n"
        "v_exp_f32 v93, v93\n"
        "v_mfma_f32_16x16x32_bf16 v[104:107], a[236:239], a[192:195], 0\n"
        "ds_read_b128 a[236:239], v248 offset:0\n"
        "v_cvt_pk_bf16_f32 v86, v116, v117\n"
        "v_mfma_f32_16x16x32_bf16 v[104:107], a[240:243], a[196:199], v[104:107]\n"
        "ds_read_b128 a[240:243], v248 offset:6144\n"
        "v_cvt_pk_bf16_f32 v87, v118, v119\n"
        "v_mfma_f32_16x16x32_bf16 v[104:107], a[244:247], a[200:203], v[104:107]\n"
        "ds_read_b128 a[244:247], v254 offset:16\n"
        "v_mov_b64 v[116:117], 0\n"
        "v_mfma_f32_16x16x32_bf16 v[104:107], a[248:251], a[204:207], v[104:107]\n"
        "v_cvt_pk_bf16_f32 v142, v100, v101\n"
        "v_cvt_pk_bf16_f32 v143, v102, v103\n"
        "v_mfma_f32_16x16x32_bf16 v[104:107], a[216:219], a[208:211], v[104:107]\n"
        "ds_read_b128 a[248:251], v254 offset:6160\n"
        "v_cvt_pk_bf16_f32 v88, v88, v89\n"
        "v_mfma_f32_16x16x32_bf16 v[104:107], a[220:223], a[212:215], v[104:107]\n"
        "ds_read_b128 a[216:219], v248 offset:32\n"
        "v_cvt_pk_bf16_f32 v89, v90, v91\n"
        "v_mfma_f32_16x16x32_bf16 v[108:111], v[192:195], v[216:219], v[56:59]\n"
        "s_waitcnt lgkmcnt(5)\n"
        "ds_read_b128 a[220:223], v248 offset:6176\n"
        "v_cvt_pk_bf16_f32 v90, v92, v93\n"
        "v_mfma_f32_16x16x32_bf16 v[108:111], v[208:211], v[220:223], v[108:111]\n"
        "v_exp_f32 v94, v94\n"
        "v_mfma_f32_16x16x32_bf16 v[112:115], v[212:215], v[216:219], v[60:63]\n"
        "ds_read_b128 a[224:227], v254 offset:48\n"
        "v_cvt_pk_bf16_f32 v150, v104, v105\n"
        "v_mfma_f32_16x16x32_bf16 v[112:115], v[228:231], v[224:227], v[112:115]\n"
        "s_waitcnt lgkmcnt(6)\n"
        "ds_read_b128 a[228:231], v254 offset:6192\n"
        "v_cvt_pk_bf16_f32 v151, v106, v107\n"
        "v_mfma_f32_16x16x32_bf16 v[172:175], v[156:159], a[236:239], 0\n"
        "s_waitcnt lgkmcnt(6)\n"
        "v_exp_f32 v95, v95\n"
        "v_mfma_f32_16x16x32_bf16 v[172:175], v[164:167], a[240:243], v[172:175]\n"
        "ds_read_b128 a[232:235], v248 offset:64\n"
        "v_cvt_pk_bf16_f32 v91, v94, v95\n"
        "v_mfma_f32_16x16x32_bf16 v[200:203], v[152:155], a[240:243], 0\n"
        "v_exp_f32 v108, v108\n"
        "v_mfma_f32_16x16x32_bf16 v[200:203], v[160:163], a[236:239], v[200:203]\n"
        "v_exp_f32 v109, v109\n"
        "v_smfmac_f32_16x16x64_bf16 v[116:119], a[252:255], v[64:71], v255\n"
        "ds_read_b128 a[236:239], v248 offset:6208\n"
        "v_cvt_pk_bf16_f32 v92, v108, v109\n"
        "v_mfma_f32_16x16x32_bf16 v[120:123], v[128:131], v[64:67], 0\n"
        "v_exp_f32 v110, v110\n"
        "v_mfma_f32_16x16x32_bf16 v[120:123], v[132:135], v[68:71], v[120:123]\n"
        "v_exp_f32 v111, v111\n"
        "v_mfma_f32_16x16x32_bf16 v[124:127], v[140:143], v[68:71], 0\n"
        "v_exp_f32 v112, v112\n"
        "v_mfma_f32_16x16x32_bf16 v[124:127], v[136:139], v[64:67], v[124:127]\n"
        "v_exp_f32 v113, v113\n"
        "v_mfma_f32_16x16x32_bf16 v[96:99], v[144:147], v[64:67], 0\n"
        "v_exp_f32 v114, v114\n"
        "v_mfma_f32_16x16x32_bf16 v[96:99], v[148:151], v[68:71], v[96:99]\n"
        "s_waitcnt lgkmcnt(7)\n"
        "ds_read_b128 a[240:243], v254 offset:80\n"
        "v_add_f32 v116, v116, v117\n"
        "v_mfma_f32_16x16x32_bf16 v[204:207], v[160:163], a[244:247], 0\n"
        "s_waitcnt lgkmcnt(7)\n"
        "v_rcp_f32 v117, v116\n"
        "v_mfma_f32_16x16x32_bf16 v[204:207], v[152:155], a[248:251], v[204:207]\n"
        "v_exp_f32 v115, v115\n"
        "v_mfma_f32_16x16x32_bf16 v[176:179], v[164:167], a[248:251], 0\n"
        "v_mul_f32 v120, v117, v120\n"
        "v_mul_f32 v121, v117, v121\n"
        "v_mfma_f32_16x16x32_bf16 v[176:179], v[156:159], a[244:247], v[176:179]\n"
        "s_waitcnt lgkmcnt(6)\n"
        "ds_read_b128 a[244:247], v254 offset:6224\n"
        "v_mul_f32 v122, v117, v122\n"
        "v_mfma_f32_16x16x32_bf16 v[180:183], v[156:159], a[216:219], 0\n"
        "s_waitcnt lgkmcnt(6)\n"
        "ds_read_b128 a[248:251], v248 offset:36864\n"
        "v_mov_b64 v[100:101], 0\n"
        "v_mfma_f32_16x16x32_bf16 v[180:183], v[164:167], a[220:223], v[180:183]\n"
        "ds_read_b128 v[232:235], v247 offset:8192\n"
        "v_mul_f32 v123, v117, v123\n"
        "v_mfma_f32_16x16x32_bf16 v[212:215], v[152:155], a[220:223], 0\n"
        "ds_read_b128 v[64:67], v247 offset:12288\n"
        "v_cvt_pk_bf16_f32 v93, v110, v111\n"
        "v_mfma_f32_16x16x32_bf16 v[212:215], v[160:163], a[216:219], v[212:215]\n"
        "s_waitcnt lgkmcnt(8)\n"
        "ds_read_b128 v[236:239], v247 offset:8448\n"
        "v_cvt_pk_bf16_f32 v120, v120, v121\n"
        "v_mfma_f32_16x16x32_bf16 v[216:219], v[160:163], a[224:227], 0\n"
        "s_waitcnt lgkmcnt(8)\n"
        "ds_read_b128 v[68:71], v247 offset:12544\n"
        "v_cvt_pk_bf16_f32 v121, v122, v123\n"
        "v_mfma_f32_16x16x32_bf16 v[216:219], v[152:155], a[228:231], v[216:219]\n"
        "ds_write_b32 v245, v120 offset:0\n"
        "v_mfma_f32_16x16x32_bf16 v[184:187], v[164:167], a[228:231], 0\n"
        "ds_write_b32 v245, v121 offset:4\n"
        "v_mfma_f32_16x16x32_bf16 v[184:187], v[156:159], a[224:227], v[184:187]\n"
        "s_waitcnt lgkmcnt(10)\n"
        "ds_read_b128 a[216:219], v248 offset:43008\n"
        "v_cvt_pk_bf16_f32 v94, v112, v113\n"
        "v_mfma_f32_16x16x32_bf16 v[192:195], v[156:159], a[232:235], 0\n"
        "s_waitcnt lgkmcnt(10)\n"
        "v_mul_f32 v124, v117, v124\n"
        "v_mul_f32 v125, v117, v125\n"
        "v_mfma_f32_16x16x32_bf16 v[192:195], v[164:167], a[236:239], v[192:195]\n"
        "v_maximum3_f32 v253, v253, v116, v117\n"
        "v_mul_f32 v126, v117, v126\n"
        "v_mfma_f32_16x16x32_bf16 v[220:223], v[152:155], a[236:239], 0\n"
        "v_mul_f32 v127, v117, v127\n"
        "v_cvt_pk_bf16_f32 v95, v114, v115\n"
        "v_mfma_f32_16x16x32_bf16 v[220:223], v[160:163], a[232:235], v[220:223]\n"
        "ds_read_b128 a[220:223], v254 offset:36880\n"
        "v_cvt_pk_bf16_f32 v124, v124, v125\n"
        "v_smfmac_f32_16x16x64_bf16 v[100:103], a[252:255], v[72:79], v255\n"
        "ds_read_b128 a[224:227], v254 offset:43024\n"
        "v_cvt_pk_bf16_f32 v125, v126, v127\n"
        "v_mfma_f32_16x16x32_bf16 v[104:107], v[128:131], v[72:75], 0\n"
        "ds_write_b32 v245, v124 offset:8\n"
        "v_mfma_f32_16x16x32_bf16 v[104:107], v[132:135], v[76:79], v[104:107]\n"
        "ds_write_b32 v245, v125 offset:12\n"
        "v_mfma_f32_16x16x32_bf16 v[108:111], v[140:143], v[76:79], 0\n"
        "v_mul_f32 v96, v117, v96\n"
        "v_mul_f32 v97, v117, v97\n"
        "v_mfma_f32_16x16x32_bf16 v[108:111], v[136:139], v[72:75], v[108:111]\n"
        "v_mul_f32 v98, v117, v98\n"
        "v_mul_f32 v99, v117, v99\n"
        "v_mfma_f32_16x16x32_bf16 v[112:115], v[144:147], v[72:75], 0\n"
        "ds_read_b128 a[228:231], v248 offset:36896\n"
        "v_cvt_pk_bf16_f32 v96, v96, v97\n"
        "v_mfma_f32_16x16x32_bf16 v[112:115], v[148:151], v[76:79], v[112:115]\n"
        "s_waitcnt lgkmcnt(14)\n"
        "ds_read_b128 a[232:235], v248 offset:43040\n"
        "v_cvt_pk_bf16_f32 v97, v98, v99\n"
        "v_mfma_f32_16x16x32_bf16 v[224:227], v[160:163], a[240:243], 0\n"
        "s_waitcnt lgkmcnt(14)\n"
        "ds_write_b32 v246, v96 offset:0\n"
        "v_mfma_f32_16x16x32_bf16 v[224:227], v[152:155], a[244:247], v[224:227]\n"
        "ds_write_b32 v246, v97 offset:4\n"
        "v_mfma_f32_16x16x32_bf16 v[196:199], v[164:167], a[244:247], 0\n"
        "ds_read_b128 a[236:239], v254 offset:36912\n"
        "v_add_f32 v100, v100, v101\n"
        "v_mfma_f32_16x16x32_bf16 v[196:199], v[156:159], a[240:243], v[196:199]\n"
        "s_waitcnt lgkmcnt(15)\n"
        "v_rcp_f32 v101, v100\n"
        "v_mfma_f32_16x16x32_bf16 v[172:175], v[232:235], a[248:251], v[172:175]\n"
        "s_waitcnt lgkmcnt(9)\n"
        "ds_read_b128 a[240:243], v254 offset:43056\n"
        "v_mul_f32 v104, v101, v104\n"
        "v_mfma_f32_16x16x32_bf16 v[172:175], v[64:67], a[216:219], v[172:175]\n"
        "ds_read_b128 a[244:247], v248 offset:36928\n"
        "v_mul_f32 v105, v101, v105\n"
        "v_mfma_f32_16x16x32_bf16 v[200:203], v[68:71], a[216:219], v[200:203]\n"
        "v_mul_f32 v106, v101, v106\n"
        "v_mul_f32 v107, v101, v107\n"
        "v_mfma_f32_16x16x32_bf16 v[200:203], v[236:239], a[248:251], v[200:203]\n"
        "s_waitcnt lgkmcnt(10)\n"
        "ds_read_b128 a[248:251], v248 offset:43072\n"
        "v_cvt_pk_bf16_f32 v104, v104, v105\n"
        "v_mfma_f32_16x16x32_bf16 v[204:207], v[236:239], a[220:223], v[204:207]\n"
        "s_waitcnt lgkmcnt(10)\n"
        "ds_read_b128 a[216:219], v254 offset:36944\n"
        "v_mov_b64 v[116:117], 0\n"
        "v_mfma_f32_16x16x32_bf16 v[204:207], v[68:71], a[224:227], v[204:207]\n"
        "v_cvt_pk_bf16_f32 v105, v106, v107\n"
        "v_mul_f32 v108, v101, v108\n"
        "v_mfma_f32_16x16x32_bf16 v[176:179], v[64:67], a[224:227], v[176:179]\n"
        "ds_write_b32 v245, v104 offset:256\n"
        "v_mfma_f32_16x16x32_bf16 v[176:179], v[232:235], a[220:223], v[176:179]\n"
        "s_waitcnt lgkmcnt(9)\n"
        "ds_write_b32 v245, v105 offset:260\n"
        "v_mfma_f32_16x16x32_bf16 v[180:183], v[232:235], a[228:231], v[180:183]\n"
        "s_waitcnt lgkmcnt(9)\n"
        "ds_read_b128 a[220:223], v254 offset:43088\n"
        "v_mul_f32 v109, v101, v109\n"
        "v_mfma_f32_16x16x32_bf16 v[180:183], v[64:67], a[232:235], v[180:183]\n"
        "v_maximum3_f32 v253, v253, v100, v101\n"
        "v_mul_f32 v110, v101, v110\n"
        "v_mfma_f32_16x16x32_bf16 v[212:215], v[68:71], a[232:235], v[212:215]\n"
        "v_mul_f32 v111, v101, v111\n"
        "v_cvt_pk_bf16_f32 v108, v108, v109\n"
        "v_mfma_f32_16x16x32_bf16 v[212:215], v[236:239], a[228:231], v[212:215]\n"
        "s_waitcnt lgkmcnt(7)\n"
        "ds_read_b128 v[156:159], v247 offset:16384\n"
        "v_cvt_pk_bf16_f32 v109, v110, v111\n"
        "v_mfma_f32_16x16x32_bf16 v[216:219], v[236:239], a[236:239], v[216:219]\n"
        "s_waitcnt lgkmcnt(7)\n"
        "ds_write_b32 v245, v108 offset:264\n"
        "v_mfma_f32_16x16x32_bf16 v[216:219], v[68:71], a[240:243], v[216:219]\n"
        "ds_write_b32 v245, v109 offset:268\n"
        "v_mfma_f32_16x16x32_bf16 v[184:187], v[64:67], a[240:243], v[184:187]\n"
        "v_mul_f32 v112, v101, v112\n"
        "v_mul_f32 v113, v101, v113\n"
        "v_mfma_f32_16x16x32_bf16 v[184:187], v[232:235], a[236:239], v[184:187]\n"
        "v_mul_f32 v114, v101, v114\n"
        "v_mul_f32 v115, v101, v115\n"
        "v_smfmac_f32_16x16x64_bf16 v[116:119], a[252:255], v[80:87], v255\n"
        "ds_read_b128 v[164:167], v247 offset:20480\n"
        "v_cvt_pk_bf16_f32 v112, v112, v113\n"
        "v_mfma_f32_16x16x32_bf16 v[120:123], v[128:131], v[80:83], 0\n"
        "ds_read_b128 v[160:163], v247 offset:16640\n"
        "v_cvt_pk_bf16_f32 v113, v114, v115\n"
        "v_mfma_f32_16x16x32_bf16 v[120:123], v[132:135], v[84:87], v[120:123]\n"
        "ds_write_b32 v246, v112 offset:256\n"
        "v_mfma_f32_16x16x32_bf16 v[124:127], v[140:143], v[84:87], 0\n"
        "ds_write_b32 v246, v113 offset:260\n"
        "v_mfma_f32_16x16x32_bf16 v[124:127], v[136:139], v[80:83], v[124:127]\n"
        "ds_read_b128 a[224:227], v248 offset:12288\n"
        "v_add_f32 v116, v116, v117\n"
        "v_mfma_f32_16x16x32_bf16 v[96:99], v[144:147], v[80:83], 0\n"
        "ds_read_b128 v[152:155], v247 offset:20736\n"
        "v_mfma_f32_16x16x32_bf16 v[96:99], v[148:151], v[84:87], v[96:99]\n"
        "s_waitcnt lgkmcnt(14)\n"
        "ds_read_b128 a[228:231], v248 offset:49152\n"
        "v_mfma_f32_16x16x32_bf16 v[192:195], v[232:235], a[244:247], v[192:195]\n"
        "s_waitcnt lgkmcnt(14)\n"
        "v_rcp_f32 v117, v116\n"
        "v_mfma_f32_16x16x32_bf16 v[192:195], v[64:67], a[248:251], v[192:195]\n"
        "ds_read_b128 a[232:235], v254 offset:12304\n"
        "v_mul_f32 v120, v117, v120\n"
        "v_mfma_f32_16x16x32_bf16 v[220:223], v[68:71], a[248:251], v[220:223]\n"
        "ds_read_b128 a[236:239], v254 offset:49168\n"
        "v_mul_f32 v121, v117, v121\n"
        "v_mfma_f32_16x16x32_bf16 v[220:223], v[236:239], a[244:247], v[220:223]\n"
        "s_waitcnt lgkmcnt(15)\n"
        "ds_read_b128 a[240:243], v248 offset:12320\n"
        "v_mul_f32 v122, v117, v122\n"
        "v_mfma_f32_16x16x32_bf16 v[224:227], v[236:239], a[216:219], v[224:227]\n"
        "s_waitcnt lgkmcnt(13)\n"
        "ds_read_b128 a[244:247], v248 offset:49184\n"
        "v_mul_f32 v123, v117, v123\n"
        "v_mfma_f32_16x16x32_bf16 v[224:227], v[68:71], a[220:223], v[224:227]\n"
        "ds_read_b128 a[248:251], v254 offset:12336\n"
        "v_cvt_pk_bf16_f32 v120, v120, v121\n"
        "v_mfma_f32_16x16x32_bf16 v[196:199], v[64:67], a[220:223], v[196:199]\n"
        "ds_read_b128 a[220:223], v248 offset:12352\n"
        "v_cvt_pk_bf16_f32 v121, v122, v123\n"
        "v_mfma_f32_16x16x32_bf16 v[196:199], v[232:235], a[216:219], v[196:199]\n"
        "s_waitcnt lgkmcnt(8)\n"
        "ds_write_b32 v245, v120 offset:512\n"
        "v_mfma_f32_16x16x32_bf16 v[172:175], v[156:159], a[224:227], v[172:175]\n"
        "s_waitcnt lgkmcnt(7)\n"
        "ds_read_b128 a[216:219], v254 offset:49200\n"
        "v_mul_f32 v124, v117, v124\n"
        "v_mfma_f32_16x16x32_bf16 v[172:175], v[164:167], a[228:231], v[172:175]\n"
        "ds_write_b32 v245, v121 offset:516\n"
        "v_mfma_f32_16x16x32_bf16 v[200:203], v[152:155], a[228:231], v[200:203]\n"
        "ds_read_b128 a[228:231], v254 offset:12368\n"
        "v_mul_f32 v125, v117, v125\n"
        "v_mfma_f32_16x16x32_bf16 v[200:203], v[160:163], a[224:227], v[200:203]\n"
        "s_waitcnt lgkmcnt(9)\n"
        "ds_read_b128 a[224:227], v248 offset:49216\n"
        "v_maximum3_f32 v253, v253, v116, v117\n"
        "v_mfma_f32_16x16x32_bf16 v[204:207], v[160:163], a[232:235], v[204:207]\n"
        "s_waitcnt lgkmcnt(9)\n"
        "v_mul_f32 v126, v117, v126\n"
        "v_mul_f32 v127, v117, v127\n"
        "v_mfma_f32_16x16x32_bf16 v[204:207], v[152:155], a[236:239], v[204:207]\n"
        "v_cvt_pk_bf16_f32 v124, v124, v125\n"
        "v_cvt_pk_bf16_f32 v125, v126, v127\n"
        "v_mfma_f32_16x16x32_bf16 v[176:179], v[164:167], a[236:239], v[176:179]\n"
        "ds_write_b32 v245, v124 offset:520\n"
        "v_mfma_f32_16x16x32_bf16 v[176:179], v[156:159], a[232:235], v[176:179]\n"
        "s_waitcnt lgkmcnt(9)\n"
        "ds_write_b32 v245, v125 offset:524\n"
        "v_mfma_f32_16x16x32_bf16 v[180:183], v[156:159], a[240:243], v[180:183]\n"
        "s_waitcnt lgkmcnt(9)\n"
        "ds_read_b128 a[232:235], v254 offset:49232\n"
        "v_mul_f32 v96, v117, v96\n"
        "v_mfma_f32_16x16x32_bf16 v[180:183], v[164:167], a[244:247], v[180:183]\n"
        "v_mul_f32 v97, v117, v97\n"
        "v_mul_f32 v98, v117, v98\n"
        "v_mfma_f32_16x16x32_bf16 v[212:215], v[152:155], a[244:247], v[212:215]\n"
        "v_mul_f32 v99, v117, v99\n"
        "v_cvt_pk_bf16_f32 v96, v96, v97\n"
        "v_mfma_f32_16x16x32_bf16 v[212:215], v[160:163], a[240:243], v[212:215]\n"
        "s_waitcnt lgkmcnt(9)\n"
        "v_cvt_pk_bf16_f32 v97, v98, v99\n"
        "v_add_u32 v247, s31, v247\n"
        "v_mfma_f32_16x16x32_bf16 v[216:219], v[160:163], a[248:251], v[216:219]\n"
        "s_waitcnt lgkmcnt(6)\n"
        "v_mov_b64 v[80:81], 0\n"
        "v_mfma_f32_16x16x32_bf16 v[216:219], v[152:155], a[216:219], v[216:219]\n"
        "ds_write_b32 v246, v96 offset:512\n"
        "v_mfma_f32_16x16x32_bf16 v[184:187], v[164:167], a[216:219], v[184:187]\n"
        "s_waitcnt vmcnt(0)\n"
        "ds_write_b32 v246, v97 offset:516\n"
        "v_mfma_f32_16x16x32_bf16 v[184:187], v[156:159], a[248:251], v[184:187]\n"
        "s_barrier\n"
        "v_mfma_f32_16x16x32_bf16 v[192:195], v[156:159], a[220:223], v[192:195]\n"
        "s_waitcnt lgkmcnt(5)\n"
        "v_mfma_f32_16x16x32_bf16 v[192:195], v[164:167], a[224:227], v[192:195]\n"
        "ds_read_b128 a[236:239], v240 offset:0\n"
        "v_mfma_f32_16x16x32_bf16 v[220:223], v[152:155], a[224:227], v[220:223]\n"
        "ds_read_b128 a[240:243], v241 offset:0\n"
        "v_mfma_f32_16x16x32_bf16 v[220:223], v[160:163], a[220:223], v[220:223]\n"
        "ds_read_b128 a[244:247], v240 offset:128\n"
        "v_mfma_f32_16x16x32_bf16 v[224:227], v[160:163], a[228:231], v[224:227]\n"
        "s_waitcnt lgkmcnt(5)\n"
        "ds_read_b128 a[248:251], v241 offset:128\n"
        "v_mfma_f32_16x16x32_bf16 v[224:227], v[152:155], a[232:235], v[224:227]\n"
        "ds_read_b128 a[216:219], v240 offset:256\n"
        "v_mfma_f32_16x16x32_bf16 v[196:199], v[164:167], a[232:235], v[196:199]\n"
        "ds_read_b128 a[220:223], v241 offset:256\n"
        "v_mfma_f32_16x16x32_bf16 v[196:199], v[156:159], a[228:231], v[196:199]\n"
        "v_smfmac_f32_16x16x64_bf16 v[80:83], a[252:255], v[88:95], v255\n"
        "s_sub_u32 s27, s27, 1\n"
        "v_mfma_f32_16x16x32_bf16 v[84:87], v[128:131], v[88:91], 0\n"
        "s_cmp_lg_u32 s27, 0\n"
        "v_mfma_f32_16x16x32_bf16 v[84:87], v[132:135], v[92:95], v[84:87]\n"
        "s_cbranch_scc1 10b\n"
        "v_mfma_f32_16x16x32_bf16 v[232:235], v[140:143], v[92:95], 0\n"
        "v_mfma_f32_16x16x32_bf16 v[232:235], v[136:139], v[88:91], v[232:235]\n"
        "v_mfma_f32_16x16x32_bf16 v[236:239], v[144:147], v[88:91], 0\n"
        "v_mfma_f32_16x16x32_bf16 v[236:239], v[148:151], v[92:95], v[236:239]\n"
        "20:\n"
        "s_waitcnt lgkmcnt(0)\n"
        "v_mfma_f32_16x16x32_bf16 v[96:99], a[72:75], a[236:239], 0\n"
        "v_and_b32 v242, 60, v250\n"
        "v_add3_u32 v242, v250, v242, s26\n"
        "v_mfma_f32_16x16x32_bf16 v[96:99], a[76:79], a[240:243], v[96:99]\n"
        "v_cvt_pk_bf16_f32 v108, v172, v176\n"
        "v_cvt_pk_bf16_f32 v109, v180, v184\n"
        "v_mfma_f32_16x16x32_bf16 v[96:99], a[80:83], a[244:247], v[96:99]\n"
        "v_cvt_pk_bf16_f32 v110, v192, v196\n"
        "v_add_f32 v80, v80, v81\n"
        "v_mfma_f32_16x16x32_bf16 v[96:99], a[84:87], a[248:251], v[96:99]\n"
        "buffer_store_dwordx2 v[108:109], v242, s[16:19], s38 offen offset:0 sc1\n"
        "v_mfma_f32_16x16x32_bf16 v[96:99], a[88:91], a[216:219], v[96:99]\n"
        "v_rcp_f32 v81, v80\n"
        "v_mfma_f32_16x16x32_bf16 v[96:99], a[92:95], a[220:223], v[96:99]\n"
        "v_cvt_pk_bf16_f32 v112, v173, v177\n"
        "v_cvt_pk_bf16_f32 v113, v181, v185\n"
        "v_mfma_f32_16x16x32_bf16 v[100:103], a[116:119], a[220:223], 0\n"
        "v_mul_f32 v84, v81, v84\n"
        "v_mul_f32 v85, v81, v85\n"
        "v_mfma_f32_16x16x32_bf16 v[100:103], a[112:115], a[216:219], v[100:103]\n"
        "v_mul_f32 v86, v81, v86\n"
        "v_mul_f32 v87, v81, v87\n"
        "v_mfma_f32_16x16x32_bf16 v[100:103], a[108:111], a[248:251], v[100:103]\n"
        "s_sub_u32 s31, s29, s30\n"
        "buffer_store_dword v110, v250, s[16:19], s38 offen offset:0 sc1\n"
        "v_mfma_f32_16x16x32_bf16 v[100:103], a[104:107], a[244:247], v[100:103]\n"
        "v_cvt_pk_bf16_f32 v152, v96, v97\n"
        "v_cvt_pk_bf16_f32 v153, v98, v99\n"
        "v_mfma_f32_16x16x32_bf16 v[100:103], a[100:103], a[240:243], v[100:103]\n"
        "v_cvt_pk_bf16_f32 v114, v193, v197\n"
        "v_cvt_pk_bf16_f32 v84, v84, v85\n"
        "v_mfma_f32_16x16x32_bf16 v[100:103], a[96:99], a[236:239], v[100:103]\n"
        "ds_write_b32 v245, v84 offset:768\n"
        "v_mfma_f32_16x16x32_bf16 v[104:107], a[120:123], a[236:239], 0\n"
        "v_cvt_pk_bf16_f32 v85, v86, v87\n"
        "v_mul_f32 v232, v81, v232\n"
        "v_mfma_f32_16x16x32_bf16 v[104:107], a[124:127], a[240:243], v[104:107]\n"
        "buffer_store_dwordx2 v[112:113], v242, s[16:19], s38 offen offset:384 sc1\n"
        "v_mfma_f32_16x16x32_bf16 v[104:107], a[128:131], a[244:247], v[104:107]\n"
        "ds_write_b32 v245, v85 offset:772\n"
        "v_mfma_f32_16x16x32_bf16 v[104:107], a[132:135], a[248:251], v[104:107]\n"
        "v_cvt_pk_bf16_f32 v154, v100, v101\n"
        "v_cvt_pk_bf16_f32 v155, v102, v103\n"
        "v_mfma_f32_16x16x32_bf16 v[104:107], a[136:139], a[216:219], v[104:107]\n"
        "45:\n"
        "s_cbranch_execz 40b\n"
        "v_cvt_pk_bf16_f32 v108, v174, v178\n"
        "v_cvt_pk_bf16_f32 v109, v182, v186\n"
        "v_mfma_f32_16x16x32_bf16 v[104:107], a[140:143], a[220:223], v[104:107]\n"
        "v_mul_f32 v233, v81, v233\n"
        "v_maximum3_f32 v253, v253, v80, v81\n"
        "v_mfma_f32_16x16x32_bf16 v[96:99], a[20:23], a[220:223], 0\n"
        "buffer_store_dword v114, v250, s[16:19], s38 offen offset:384 sc1\n"
        "v_mfma_f32_16x16x32_bf16 v[96:99], a[16:19], a[216:219], v[96:99]\n"
        "v_cvt_pk_bf16_f32 v110, v194, v198\n"
        "v_mul_f32 v234, v81, v234\n"
        "v_mfma_f32_16x16x32_bf16 v[96:99], a[12:15], a[248:251], v[96:99]\n"
        "v_cvt_pk_bf16_f32 v168, v104, v105\n"
        "v_cvt_pk_bf16_f32 v169, v106, v107\n"
        "v_mfma_f32_16x16x32_bf16 v[96:99], a[8:11], a[244:247], v[96:99]\n"
        "v_mul_f32 v235, v81, v235\n"
        "v_cvt_pk_bf16_f32 v232, v232, v233\n"
        "v_mfma_f32_16x16x32_bf16 v[96:99], a[4:7], a[240:243], v[96:99]\n"
        "ds_write_b32 v245, v232 offset:776\n"
        "v_mfma_f32_16x16x32_bf16 v[96:99], a[0:3], a[236:239], v[96:99]\n"
        "buffer_store_dwordx2 v[108:109], v242, s[16:19], s38 offen offset:768 sc1\n"
        "v_mfma_f32_16x16x32_bf16 v[100:103], a[24:27], a[236:239], 0\n"
        "v_cvt_pk_bf16_f32 v112, v175, v179\n"
        "v_cvt_pk_bf16_f32 v113, v183, v187\n"
        "v_mfma_f32_16x16x32_bf16 v[100:103], a[28:31], a[240:243], v[100:103]\n"
        "v_cvt_pk_bf16_f32 v233, v234, v235\n"
        "v_xor_b32 v251, 0x2000, v251\n"
        "v_mfma_f32_16x16x32_bf16 v[100:103], a[32:35], a[244:247], v[100:103]\n"
        "v_mul_f32 v96, s50, v96\n"
        "v_mul_f32 v97, s50, v97\n"
        "v_mfma_f32_16x16x32_bf16 v[100:103], a[36:39], a[248:251], v[100:103]\n"
        "v_mul_f32 v98, s50, v98\n"
        "v_mul_f32 v99, s50, v99\n"
        "v_mfma_f32_16x16x32_bf16 v[100:103], a[40:43], a[216:219], v[100:103]\n"
        "buffer_store_dword v110, v250, s[16:19], s38 offen offset:768 sc1\n"
        "v_mfma_f32_16x16x32_bf16 v[100:103], a[44:47], a[220:223], v[100:103]\n"
        "v_cvt_pk_bf16_f32 v156, v96, v97\n"
        "v_cvt_pk_bf16_f32 v157, v98, v99\n"
        "v_mfma_f32_16x16x32_bf16 v[104:107], a[68:71], a[220:223], 0\n"
        "ds_write_b32 v245, v233 offset:780\n"
        "v_mfma_f32_16x16x32_bf16 v[104:107], a[64:67], a[216:219], v[104:107]\n"
        "v_cvt_pk_bf16_f32 v114, v195, v199\n"
        "v_xor_b32 v252, 0x2000, v252\n"
        "v_mfma_f32_16x16x32_bf16 v[104:107], a[60:63], a[248:251], v[104:107]\n"
        "v_mul_f32 v100, s50, v100\n"
        "v_mul_f32 v101, s50, v101\n"
        "v_mfma_f32_16x16x32_bf16 v[104:107], a[56:59], a[244:247], v[104:107]\n"
        "buffer_store_dwordx2 v[112:113], v242, s[16:19], s38 offen offset:1152 sc1\n"
        "v_mfma_f32_16x16x32_bf16 v[104:107], a[52:55], a[240:243], v[104:107]\n"
        "v_mul_f32 v102, s50, v102\n"
        "v_mul_f32 v103, s50, v103\n"
        "v_mfma_f32_16x16x32_bf16 v[104:107], a[48:51], a[236:239], v[104:107]\n"
        "v_cvt_pk_bf16_f32 v158, v100, v101\n"
        "v_cvt_pk_bf16_f32 v159, v102, v103\n"
        "v_mfma_f32_16x16x32_bf16 v[96:99], a[236:239], a[144:147], 0\n"
        "ds_read_b128 a[224:227], v240 offset:1536\n"
        "v_cvt_pk_bf16_f32 v108, v200, v204\n"
        "v_mfma_f32_16x16x32_bf16 v[96:99], a[240:243], a[148:151], v[96:99]\n"
        "ds_read_b128 a[228:231], v241 offset:1536\n"
        "v_cvt_pk_bf16_f32 v109, v212, v216\n"
        "v_mfma_f32_16x16x32_bf16 v[96:99], a[244:247], a[152:155], v[96:99]\n"
        "buffer_store_dword v114, v250, s[16:19], s38 offen offset:1152 sc1\n"
        "v_mfma_f32_16x16x32_bf16 v[96:99], a[248:251], a[156:159], v[96:99]\n"
        "v_mul_f32 v104, s50, v104\n"
        "v_mul_f32 v105, s50, v105\n"
        "v_mfma_f32_16x16x32_bf16 v[96:99], a[216:219], a[160:163], v[96:99]\n"
        "v_mul_f32 v106, s50, v106\n"
        "v_mul_f32 v107, s50, v107\n"
        "v_mfma_f32_16x16x32_bf16 v[96:99], a[220:223], a[164:167], v[96:99]\n"
        "v_cvt_pk_bf16_f32 v160, v104, v105\n"
        "v_cvt_pk_bf16_f32 v161, v106, v107\n"
        "v_mfma_f32_16x16x32_bf16 v[100:103], a[220:223], a[188:191], 0\n"
        "ds_read_b128 a[232:235], v240 offset:1664\n"
        "v_mov_b64 v[166:167], v[160:161]\n"
        "v_mfma_f32_16x16x32_bf16 v[100:103], a[216:219], a[184:187], v[100:103]\n"
        "buffer_store_dwordx2 v[108:109], v242, s[16:19], s39 offen offset:0 sc1\n"
        "v_mfma_f32_16x16x32_bf16 v[100:103], a[248:251], a[180:183], v[100:103]\n"
        "v_cvt_pk_bf16_f32 v128, v96, v97\n"
        "v_cvt_pk_bf16_f32 v129, v98, v99\n"
        "v_mfma_f32_16x16x32_bf16 v[100:103], a[244:247], a[176:179], v[100:103]\n"
        "v_cvt_pk_bf16_f32 v110, v220, v224\n"
        "v_cvt_pk_bf16_f32 v112, v201, v205\n"
        "v_mfma_f32_16x16x32_bf16 v[100:103], a[240:243], a[172:175], v[100:103]\n"
        "v_cvt_pk_bf16_f32 v113, v213, v217\n"
        "v_mul_f32 v236, v81, v236\n"
        "v_mfma_f32_16x16x32_bf16 v[100:103], a[236:239], a[168:171], v[100:103]\n"
        "v_mul_f32 v237, v81, v237\n"
        "v_mul_f32 v238, v81, v238\n"
        "v_mfma_f32_16x16x32_bf16 v[104:107], a[236:239], a[192:195], 0\n"
        "buffer_store_dword v110, v250, s[16:19], s39 offen offset:0 sc1\n"
        "v_mfma_f32_16x16x32_bf16 v[104:107], a[240:243], a[196:199], v[104:107]\n"
        "ds_read_b128 a[236:239], v241 offset:1664\n"
        "ds_read_b128 a[240:243], v240 offset:1792\n"
        "v_mfma_f32_16x16x32_bf16 v[104:107], a[244:247], a[200:203], v[104:107]\n"
        "ds_read_b128 a[244:247], v241 offset:1792\n"
        "v_cvt_pk_bf16_f32 v136, v100, v101\n"
        "v_mfma_f32_16x16x32_bf16 v[104:107], a[248:251], a[204:207], v[104:107]\n"
        "v_cvt_pk_bf16_f32 v137, v102, v103\n"
        "v_mul_f32 v239, v81, v239\n"
        "v_mfma_f32_16x16x32_bf16 v[104:107], a[216:219], a[208:211], v[104:107]\n"
        "v_cvt_pk_bf16_f32 v236, v236, v237\n"
        "v_cvt_pk_bf16_f32 v237, v238, v239\n"
        "v_mfma_f32_16x16x32_bf16 v[104:107], a[220:223], a[212:215], v[104:107]\n"
        "s_waitcnt lgkmcnt(5)\n"
        "buffer_store_dwordx2 v[112:113], v242, s[16:19], s39 offen offset:384 sc1\n"
        "v_mfma_f32_16x16x32_bf16 v[96:99], a[72:75], a[224:227], 0\n"
        "s_waitcnt lgkmcnt(4)\n"
        "ds_write_b32 v246, v236 offset:768\n"
        "v_mfma_f32_16x16x32_bf16 v[96:99], a[76:79], a[228:231], v[96:99]\n"
        "s_waitcnt lgkmcnt(4)\n"
        "ds_write_b32 v246, v237 offset:772\n"
        "v_mfma_f32_16x16x32_bf16 v[96:99], a[80:83], a[232:235], v[96:99]\n"
        "s_waitcnt lgkmcnt(4)\n"
        "ds_read_b128 v[162:165], v251 offset:0\n"
        "v_cvt_pk_bf16_f32 v114, v221, v225\n"
        "v_mfma_f32_16x16x32_bf16 v[96:99], a[84:87], a[236:239], v[96:99]\n"
        "s_waitcnt lgkmcnt(4)\n"
        "v_cvt_pk_bf16_f32 v144, v104, v105\n"
        "v_cvt_pk_bf16_f32 v145, v106, v107\n"
        "v_mfma_f32_16x16x32_bf16 v[96:99], a[88:91], a[240:243], v[96:99]\n"
        "s_waitcnt lgkmcnt(3)\n"
        "buffer_store_dword v114, v250, s[16:19], s39 offen offset:384 sc1\n"
        "v_mfma_f32_16x16x32_bf16 v[96:99], a[92:95], a[244:247], v[96:99]\n"
        "v_cvt_pk_bf16_f32 v108, v202, v206\n"
        "v_cvt_pk_bf16_f32 v109, v214, v218\n"
        "v_mfma_f32_16x16x32_bf16 v[100:103], a[116:119], a[244:247], 0\n"
        "v_cvt_pk_bf16_f32 v110, v222, v226\n"
        "v_add_u32 v245, s31, v245\n"
        "v_mfma_f32_16x16x32_bf16 v[100:103], a[112:115], a[240:243], v[100:103]\n"
        "v_add_u32 v246, s31, v246\n"
        "v_mfma_f32_16x16x32_bf16 v[100:103], a[108:111], a[236:239], v[100:103]\n"
        "v_cvt_pk_bf16_f32 v172, v96, v97\n"
        "v_cvt_pk_bf16_f32 v173, v98, v99\n"
        "v_mfma_f32_16x16x32_bf16 v[100:103], a[104:107], a[232:235], v[100:103]\n"
        "buffer_store_dwordx2 v[108:109], v242, s[16:19], s39 offen offset:768 sc1\n"
        "v_mfma_f32_16x16x32_bf16 v[100:103], a[100:103], a[228:231], v[100:103]\n"
        "v_cvt_pk_bf16_f32 v112, v203, v207\n"
        "v_cvt_pk_bf16_f32 v113, v215, v219\n"
        "v_mfma_f32_16x16x32_bf16 v[100:103], a[96:99], a[224:227], v[100:103]\n"
        "s_nop 2\n"
        "v_mfma_f32_16x16x32_bf16 v[104:107], a[120:123], a[224:227], 0\n"
        "s_nop 2\n"
        "v_mfma_f32_16x16x32_bf16 v[104:107], a[124:127], a[228:231], v[104:107]\n"
        "v_cvt_pk_bf16_f32 v174, v100, v101\n"
        "v_cvt_pk_bf16_f32 v175, v102, v103\n"
        "v_mfma_f32_16x16x32_bf16 v[104:107], a[128:131], a[232:235], v[104:107]\n"
        "buffer_store_dword v110, v250, s[16:19], s39 offen offset:768 sc1\n"
        "v_mfma_f32_16x16x32_bf16 v[104:107], a[132:135], a[236:239], v[104:107]\n"
        "v_cvt_pk_bf16_f32 v114, v223, v227\n"
        "v_mfma_f32_16x16x32_bf16 v[104:107], a[136:139], a[240:243], v[104:107]\n"
        "v_mfma_f32_16x16x32_bf16 v[104:107], a[140:143], a[244:247], v[104:107]\n"
        "s_nop 2\n"
        "v_mfma_f32_16x16x32_bf16 v[96:99], a[20:23], a[244:247], 0\n"
        "ds_read_b128 a[248:251], v240 offset:12288\n"
        "s_nop 0\n"
        "v_mfma_f32_16x16x32_bf16 v[96:99], a[16:19], a[240:243], v[96:99]\n"
        "buffer_store_dwordx2 v[112:113], v242, s[16:19], s39 offen offset:1152 sc1\n"
        "v_mfma_f32_16x16x32_bf16 v[96:99], a[12:15], a[236:239], v[96:99]\n"
        "ds_read_b128 a[216:219], v241 offset:12288\n"
        "v_cvt_pk_bf16_f32 v190, v104, v105\n"
        "v_mfma_f32_16x16x32_bf16 v[96:99], a[8:11], a[232:235], v[96:99]\n"
        "ds_read_b128 a[220:223], v240 offset:12416\n"
        "v_cvt_pk_bf16_f32 v191, v106, v107\n"
        "v_mfma_f32_16x16x32_bf16 v[96:99], a[4:7], a[228:231], v[96:99]\n"
        "ds_read_b128 v[182:185], v251 offset:512\n"
        "v_mfma_f32_16x16x32_bf16 v[96:99], a[0:3], a[224:227], v[96:99]\n"
        "s_nop 2\n"
        "v_mfma_f32_16x16x32_bf16 v[0:3], v[152:155], v[156:159], v[0:3]\n"
        "s_waitcnt lgkmcnt(4)\n"
        "buffer_store_dword v114, v250, s[16:19], s39 offen offset:1152 sc1\n"
        "v_mfma_f32_16x16x32_bf16 v[0:3], v[168:171], v[160:163], v[0:3]\n"
        "s_nop 0\n"
        "v_mfma_f32_16x16x32_bf16 v[4:7], v[172:175], v[156:159], v[4:7]\n"
        "v_mul_f32 v96, s50, v96\n"
        "v_mul_f32 v97, s50, v97\n"
        "v_mfma_f32_16x16x32_bf16 v[4:7], v[188:191], v[164:167], v[4:7]\n"
        "v_mul_f32 v98, s50, v98\n"
        "v_mul_f32 v99, s50, v99\n"
        "v_mfma_f32_16x16x32_bf16 v[100:103], a[24:27], a[224:227], 0\n"
        "v_cvt_pk_bf16_f32 v176, v96, v97\n"
        "v_cvt_pk_bf16_f32 v177, v98, v99\n"
        "v_mfma_f32_16x16x32_bf16 v[100:103], a[28:31], a[228:231], v[100:103]\n"
        "v_exp_f32 v0, v0\n"
        "v_mfma_f32_16x16x32_bf16 v[100:103], a[32:35], a[232:235], v[100:103]\n"
        "v_exp_f32 v1, v1\n"
        "v_mfma_f32_16x16x32_bf16 v[100:103], a[36:39], a[236:239], v[100:103]\n"
        "v_exp_f32 v2, v2\n"
        "v_mfma_f32_16x16x32_bf16 v[100:103], a[40:43], a[240:243], v[100:103]\n"
        "v_exp_f32 v3, v3\n"
        "v_mfma_f32_16x16x32_bf16 v[100:103], a[44:47], a[244:247], v[100:103]\n"
        "v_exp_f32 v4, v4\n"
        "v_mfma_f32_16x16x32_bf16 v[104:107], a[48:51], a[224:227], 0\n"
        "ds_read_b128 a[224:227], v241 offset:12416\n"
        "v_cvt_pk_bf16_f32 v64, v0, v1\n"
        "v_mfma_f32_16x16x32_bf16 v[104:107], a[52:55], a[228:231], v[104:107]\n"
        "ds_read_b128 a[228:231], v240 offset:12544\n"
        "v_cvt_pk_bf16_f32 v65, v2, v3\n"
        "v_mfma_f32_16x16x32_bf16 v[104:107], a[56:59], a[232:235], v[104:107]\n"
        "s_barrier\n"
        "ds_read_b128 a[232:235], v241 offset:12544\n"
        "v_mul_f32 v100, s50, v100\n"
        "v_mfma_f32_16x16x32_bf16 v[104:107], a[60:63], a[236:239], v[104:107]\n"
        "v_mul_f32 v101, s50, v101\n"
        "v_mul_f32 v102, s50, v102\n"
        "v_mfma_f32_16x16x32_bf16 v[104:107], a[64:67], a[240:243], v[104:107]\n"
        "v_mul_f32 v103, s50, v103\n"
        "v_cvt_pk_bf16_f32 v178, v100, v101\n"
        "v_mfma_f32_16x16x32_bf16 v[104:107], a[68:71], a[244:247], v[104:107]\n"
        "s_waitcnt lgkmcnt(6)\n"
        "v_cvt_pk_bf16_f32 v179, v102, v103\n"
        "v_mfma_f32_16x16x32_bf16 v[96:99], a[72:75], a[248:251], 0\n"
        "s_waitcnt lgkmcnt(5)\n"
        "v_exp_f32 v5, v5\n"
        "v_mfma_f32_16x16x32_bf16 v[96:99], a[76:79], a[216:219], v[96:99]\n"
        "s_waitcnt lgkmcnt(4)\n"
        "v_exp_f32 v6, v6\n"
        "v_mfma_f32_16x16x32_bf16 v[96:99], a[80:83], a[220:223], v[96:99]\n"
        "s_waitcnt lgkmcnt(2)\n"
        "v_exp_f32 v7, v7\n"
        "v_mfma_f32_16x16x32_bf16 v[96:99], a[84:87], a[224:227], v[96:99]\n"
        "s_waitcnt lgkmcnt(1)\n"
        "v_mul_f32 v104, s50, v104\n"
        "v_mul_f32 v105, s50, v105\n"
        "v_mfma_f32_16x16x32_bf16 v[96:99], a[88:91], a[228:231], v[96:99]\n"
        "s_waitcnt lgkmcnt(0)\n"
        "v_mul_f32 v106, s50, v106\n"
        "v_mul_f32 v107, s50, v107\n"
        "v_mfma_f32_16x16x32_bf16 v[96:99], a[92:95], a[232:235], v[96:99]\n"
        "v_cvt_pk_bf16_f32 v180, v104, v105\n"
        "v_cvt_pk_bf16_f32 v181, v106, v107\n"
        "v_mfma_f32_16x16x32_bf16 v[16:19], v[152:155], v[176:179], v[16:19]\n"
        "ds_read_b128 v[162:165], v252 offset:0\n"
        "v_mov_b64 v[186:187], v[180:181]\n"
        "v_mfma_f32_16x16x32_bf16 v[16:19], v[168:171], v[180:183], v[16:19]\n"
        "s_and_b32 s41, s42, 1\n"
        "v_cvt_pk_bf16_f32 v66, v4, v5\n"
        "v_cvt_pk_bf16_f32 v192, v96, v97\n"
        "v_mfma_f32_16x16x32_bf16 v[20:23], v[172:175], v[176:179], v[20:23]\n"
        "s_lshl_b32 s45, s41, 8\n"
        "v_cvt_pk_bf16_f32 v193, v98, v99\n"
        "v_cvt_pk_bf16_f32 v67, v6, v7\n"
        "v_mfma_f32_16x16x32_bf16 v[20:23], v[188:191], v[184:187], v[20:23]\n"
        "s_sub_u32 s45, s31, s45\n"
        "v_exp_f32 v16, v16\n"
        "v_mfma_f32_16x16x32_bf16 v[100:103], a[116:119], a[232:235], 0\n"
        "s_mul_i32 s48, s41, 786432\n"
        "ds_read_b128 v[182:185], v252 offset:512\n"
        "v_add_u32 v243, s45, v247\n"
        "v_mfma_f32_16x16x32_bf16 v[100:103], a[112:115], a[228:231], v[100:103]\n"
        "s_sub_u32 s48, s37, s48\n"
        "v_exp_f32 v17, v17\n"
        "v_mfma_f32_16x16x32_bf16 v[100:103], a[108:111], a[224:227], v[100:103]\n"
        "s_cmp_eq_u32 s41, 0\n"
        "v_exp_f32 v18, v18\n"
        "v_mfma_f32_16x16x32_bf16 v[100:103], a[104:107], a[220:223], v[100:103]\n"
        "s_cselect_b32 s41, s43, s44\n"
        "v_exp_f32 v19, v19\n"
        "v_mfma_f32_16x16x32_bf16 v[100:103], a[100:103], a[216:219], v[100:103]\n"
        "s_add_u32 s35, s48, 6291456\n"
        "v_exp_f32 v20, v20\n"
        "v_mfma_f32_16x16x32_bf16 v[100:103], a[96:99], a[248:251], v[100:103]\n"
        "s_add_u32 s35, s35, s41\n"
        "v_exp_f32 v21, v21\n"
        "v_mfma_f32_16x16x32_bf16 v[104:107], a[120:123], a[248:251], 0\n"
        "s_add_u32 s36, s48, 7077888\n"
        "v_exp_f32 v22, v22\n"
        "v_mfma_f32_16x16x32_bf16 v[104:107], a[124:127], a[216:219], v[104:107]\n"
        "s_cmp_eq_u32 s36, 100663296\n"
        "v_exp_f32 v23, v23\n"
        "v_mfma_f32_16x16x32_bf16 v[104:107], a[128:131], a[220:223], v[104:107]\n"
        "s_cselect_b32 s36, 0, s36\n"
        "v_cvt_pk_bf16_f32 v194, v100, v101\n"
        "v_cvt_pk_bf16_f32 v195, v102, v103\n"
        "v_mfma_f32_16x16x32_bf16 v[104:107], a[132:135], a[224:227], v[104:107]\n"
        "s_add_u32 s36, s36, s41\n"
        "v_cvt_pk_bf16_f32 v72, v16, v17\n"
        "v_cvt_pk_bf16_f32 v73, v18, v19\n"
        "v_mfma_f32_16x16x32_bf16 v[104:107], a[136:139], a[228:231], v[104:107]\n"
        "s_lshl_b32 s29, s42, 8\n"
        "v_cvt_pk_bf16_f32 v74, v20, v21\n"
        "v_cvt_pk_bf16_f32 v75, v22, v23\n"
        "v_mfma_f32_16x16x32_bf16 v[104:107], a[140:143], a[232:235], v[104:107]\n"
        "s_add_u32 s29, s29, 147456\n"
        "v_mbcnt_lo_u32_b32 v244, -1, 0\n"
        "v_mbcnt_hi_u32_b32 v244, -1, v244\n"
        "v_mfma_f32_16x16x32_bf16 v[96:99], a[20:23], a[232:235], 0\n"
        "s_mov_b32 s47, 147456\n"
        "ds_read_b128 a[236:239], v240 offset:13824\n"
        "v_lshl_add_u32 v249, v244, 4, s47\n"
        "v_mfma_f32_16x16x32_bf16 v[96:99], a[16:19], a[228:231], v[96:99]\n"
        "ds_read_b128 a[240:243], v241 offset:13824\n"
        "v_cvt_pk_bf16_f32 v208, v104, v105\n"
        "v_mfma_f32_16x16x32_bf16 v[96:99], a[12:15], a[224:227], v[96:99]\n"
        "ds_read_b128 a[244:247], v240 offset:13952\n"
        "v_cvt_pk_bf16_f32 v209, v106, v107\n"
        "v_mfma_f32_16x16x32_bf16 v[96:99], a[8:11], a[220:223], v[96:99]\n"
        "v_lshl_add_u32 v244, v244, 2, s29\n"
        "v_mfma_f32_16x16x32_bf16 v[96:99], a[4:7], a[216:219], v[96:99]\n"
        "ds_read_b128 v[202:205], v251 offset:4096\n"
        "v_mfma_f32_16x16x32_bf16 v[96:99], a[0:3], a[248:251], v[96:99]\n"
        "s_nop 2\n"
        "v_mfma_f32_16x16x32_bf16 v[8:11], v[192:195], v[156:159], v[8:11]\n"
        "s_waitcnt lgkmcnt(5)\n"
        "s_nop 1\n"
        "v_mfma_f32_16x16x32_bf16 v[8:11], v[208:211], v[160:163], v[8:11]\n"
        "v_mfma_f32_16x16x32_bf16 v[24:27], v[192:195], v[176:179], v[24:27]\n"
        "s_waitcnt lgkmcnt(4)\n"
        "v_mul_f32 v96, s50, v96\n"
        "v_mul_f32 v97, s50, v97\n"
        "v_mfma_f32_16x16x32_bf16 v[24:27], v[208:211], v[180:183], v[24:27]\n"
        "v_mul_f32 v98, s50, v98\n"
        "v_mul_f32 v99, s50, v99\n"
        "v_mfma_f32_16x16x32_bf16 v[100:103], a[24:27], a[248:251], 0\n"
        "v_cvt_pk_bf16_f32 v196, v96, v97\n"
        "v_cvt_pk_bf16_f32 v197, v98, v99\n"
        "v_mfma_f32_16x16x32_bf16 v[100:103], a[28:31], a[216:219], v[100:103]\n"
        "v_exp_f32 v8, v8\n"
        "v_mfma_f32_16x16x32_bf16 v[100:103], a[32:35], a[220:223], v[100:103]\n"
        "v_exp_f32 v9, v9\n"
        "v_mfma_f32_16x16x32_bf16 v[100:103], a[36:39], a[224:227], v[100:103]\n"
        "v_exp_f32 v10, v10\n"
        "v_mfma_f32_16x16x32_bf16 v[100:103], a[40:43], a[228:231], v[100:103]\n"
        "v_exp_f32 v11, v11\n"
        "v_mfma_f32_16x16x32_bf16 v[100:103], a[44:47], a[232:235], v[100:103]\n"
        "v_exp_f32 v24, v24\n"
        "v_mfma_f32_16x16x32_bf16 v[104:107], a[48:51], a[248:251], 0\n"
        "ds_read_b128 a[248:251], v241 offset:13952\n"
        "v_cvt_pk_bf16_f32 v68, v8, v9\n"
        "v_mfma_f32_16x16x32_bf16 v[104:107], a[52:55], a[216:219], v[104:107]\n"
        "ds_read_b128 a[216:219], v240 offset:14080\n"
        "v_cvt_pk_bf16_f32 v69, v10, v11\n"
        "v_mfma_f32_16x16x32_bf16 v[104:107], a[56:59], a[220:223], v[104:107]\n"
        "ds_read_b128 a[220:223], v241 offset:14080\n"
        "v_mul_f32 v100, s50, v100\n"
        "v_mfma_f32_16x16x32_bf16 v[104:107], a[60:63], a[224:227], v[104:107]\n"
        "v_mul_f32 v101, s50, v101\n"
        "v_mul_f32 v102, s50, v102\n"
        "v_mfma_f32_16x16x32_bf16 v[104:107], a[64:67], a[228:231], v[104:107]\n"
        "v_mul_f32 v103, s50, v103\n"
        "v_cvt_pk_bf16_f32 v198, v100, v101\n"
        "v_mfma_f32_16x16x32_bf16 v[104:107], a[68:71], a[232:235], v[104:107]\n"
        "s_waitcnt lgkmcnt(6)\n"
        "v_cvt_pk_bf16_f32 v199, v102, v103\n"
        "v_mfma_f32_16x16x32_bf16 v[96:99], a[72:75], a[236:239], 0\n"
        "s_waitcnt lgkmcnt(5)\n"
        "v_exp_f32 v25, v25\n"
        "v_mfma_f32_16x16x32_bf16 v[96:99], a[76:79], a[240:243], v[96:99]\n"
        "s_waitcnt lgkmcnt(4)\n"
        "v_exp_f32 v26, v26\n"
        "v_mfma_f32_16x16x32_bf16 v[96:99], a[80:83], a[244:247], v[96:99]\n"
        "s_waitcnt lgkmcnt(2)\n"
        "v_exp_f32 v27, v27\n"
        "v_mfma_f32_16x16x32_bf16 v[96:99], a[84:87], a[248:251], v[96:99]\n"
        "s_waitcnt lgkmcnt(1)\n"
        "v_mul_f32 v104, s50, v104\n"
        "v_mul_f32 v105, s50, v105\n"
        "v_mfma_f32_16x16x32_bf16 v[96:99], a[88:91], a[216:219], v[96:99]\n"
        "s_waitcnt lgkmcnt(0)\n"
        "v_mul_f32 v106, s50, v106\n"
        "v_mul_f32 v107, s50, v107\n"
        "v_mfma_f32_16x16x32_bf16 v[96:99], a[92:95], a[220:223], v[96:99]\n"
        "v_cvt_pk_bf16_f32 v200, v104, v105\n"
        "v_cvt_pk_bf16_f32 v201, v106, v107\n"
        "v_mfma_f32_16x16x32_bf16 v[32:35], v[152:155], v[196:199], v[32:35]\n"
        "v_mov_b64 v[206:207], v[200:201]\n"
        "v_cvt_pk_bf16_f32 v76, v24, v25\n"
        "v_mfma_f32_16x16x32_bf16 v[32:35], v[168:171], v[200:203], v[32:35]\n"
        "v_cvt_pk_bf16_f32 v77, v26, v27\n"
        "v_mfma_f32_16x16x32_bf16 v[36:39], v[172:175], v[196:199], v[36:39]\n"
        "v_cvt_pk_bf16_f32 v212, v96, v97\n"
        "v_cvt_pk_bf16_f32 v213, v98, v99\n"
        "v_mfma_f32_16x16x32_bf16 v[36:39], v[188:191], v[204:207], v[36:39]\n"
        "ds_read_b128 v[202:205], v252 offset:4096\n"
        "v_mfma_f32_16x16x32_bf16 v[100:103], a[116:119], a[220:223], 0\n"
        "v_mfma_f32_16x16x32_bf16 v[100:103], a[112:115], a[216:219], v[100:103]\n"
        "v_exp_f32 v32, v32\n"
        "v_mfma_f32_16x16x32_bf16 v[100:103], a[108:111], a[248:251], v[100:103]\n"
        "s_cmp_eq_u32 s37, 100663296\n"
        "v_exp_f32 v33, v33\n"
        "v_mfma_f32_16x16x32_bf16 v[100:103], a[104:107], a[244:247], v[100:103]\n"
        "s_cselect_b32 s46, 0, s37\n"
        "v_exp_f32 v34, v34\n"
        "v_mfma_f32_16x16x32_bf16 v[100:103], a[100:103], a[240:243], v[100:103]\n"
        "s_add_u32 s38, s46, s43\n"
        "ds_read_b128 a[72:75], v248 offset:0\n"
        "v_cvt_pk_bf16_f32 v80, v32, v33\n"
        "v_mfma_f32_16x16x32_bf16 v[100:103], a[96:99], a[236:239], v[100:103]\n"
        "s_add_u32 s39, s46, s44\n"
        "v_exp_f32 v35, v35\n"
        "v_mfma_f32_16x16x32_bf16 v[40:43], v[192:195], v[196:199], v[40:43]\n"
        "s_waitcnt lgkmcnt(1)\n"
        "ds_read_b128 a[76:79], v248 offset:6144\n"
        "v_cvt_pk_bf16_f32 v81, v34, v35\n"
        "v_mfma_f32_16x16x32_bf16 v[40:43], v[208:211], v[200:203], v[40:43]\n"
        "v_exp_f32 v36, v36\n"
        "v_mfma_f32_16x16x32_bf16 v[104:107], a[120:123], a[236:239], 0\n"
        "ds_read_b128 a[80:83], v248 offset:36864\n"
        "v_cvt_pk_bf16_f32 v214, v100, v101\n"
        "v_mfma_f32_16x16x32_bf16 v[104:107], a[124:127], a[240:243], v[104:107]\n"
        "ds_read_b128 a[84:87], v248 offset:43008\n"
        "v_cvt_pk_bf16_f32 v215, v102, v103\n"
        "v_mfma_f32_16x16x32_bf16 v[104:107], a[128:131], a[244:247], v[104:107]\n"
        "v_exp_f32 v37, v37\n"
        "v_mfma_f32_16x16x32_bf16 v[104:107], a[132:135], a[248:251], v[104:107]\n"
        "v_exp_f32 v38, v38\n"
        "v_mfma_f32_16x16x32_bf16 v[104:107], a[136:139], a[216:219], v[104:107]\n"
        "ds_read_b128 a[88:91], v248 offset:12288\n"
        "v_cvt_pk_bf16_f32 v82, v36, v37\n"
        "v_mfma_f32_16x16x32_bf16 v[104:107], a[140:143], a[220:223], v[104:107]\n"
        "v_exp_f32 v39, v39\n"
        "v_mfma_f32_16x16x32_bf16 v[96:99], a[20:23], a[220:223], 0\n"
        "ds_read_b128 a[92:95], v248 offset:49152\n"
        "v_cvt_pk_bf16_f32 v83, v38, v39\n"
        "v_mfma_f32_16x16x32_bf16 v[96:99], a[16:19], a[216:219], v[96:99]\n"
        "v_exp_f32 v40, v40\n"
        "v_mfma_f32_16x16x32_bf16 v[96:99], a[12:15], a[248:251], v[96:99]\n"
        "v_exp_f32 v41, v41\n"
        "v_mfma_f32_16x16x32_bf16 v[96:99], a[8:11], a[244:247], v[96:99]\n"
        "v_cvt_pk_bf16_f32 v230, v104, v105\n"
        "v_cvt_pk_bf16_f32 v231, v106, v107\n"
        "v_mfma_f32_16x16x32_bf16 v[96:99], a[4:7], a[240:243], v[96:99]\n"
        "v_exp_f32 v42, v42\n"
        "v_mfma_f32_16x16x32_bf16 v[96:99], a[0:3], a[236:239], v[96:99]\n"
        "ds_read_b128 a[96:99], v254 offset:49168\n"
        "v_cvt_pk_bf16_f32 v84, v40, v41\n"
        "v_mfma_f32_16x16x32_bf16 v[12:15], v[212:215], v[156:159], v[12:15]\n"
        "v_exp_f32 v43, v43\n"
        "v_mfma_f32_16x16x32_bf16 v[12:15], v[228:231], v[164:167], v[12:15]\n"
        "ds_read_b128 v[222:225], v251 offset:4608\n"
        "v_cvt_pk_bf16_f32 v85, v42, v43\n"
        "v_mfma_f32_16x16x32_bf16 v[28:31], v[212:215], v[176:179], v[28:31]\n"
        "v_mul_f32 v96, s50, v96\n"
        "v_mul_f32 v97, s50, v97\n"
        "v_mfma_f32_16x16x32_bf16 v[28:31], v[228:231], v[184:187], v[28:31]\n"
        "v_mul_f32 v98, s50, v98\n"
        "v_mul_f32 v99, s50, v99\n"
        "v_mfma_f32_16x16x32_bf16 v[44:47], v[212:215], v[196:199], v[44:47]\n"
        "v_cvt_pk_bf16_f32 v216, v96, v97\n"
        "v_cvt_pk_bf16_f32 v217, v98, v99\n"
        "v_mfma_f32_16x16x32_bf16 v[44:47], v[228:231], v[204:207], v[44:47]\n"
        "v_exp_f32 v12, v12\n"
        "v_mfma_f32_16x16x32_bf16 v[100:103], a[24:27], a[236:239], 0\n"
        "v_exp_f32 v13, v13\n"
        "v_mfma_f32_16x16x32_bf16 v[100:103], a[28:31], a[240:243], v[100:103]\n"
        "v_exp_f32 v14, v14\n"
        "v_mfma_f32_16x16x32_bf16 v[100:103], a[32:35], a[244:247], v[100:103]\n"
        "ds_read_b128 a[100:103], v254 offset:12304\n"
        "v_cvt_pk_bf16_f32 v70, v12, v13\n"
        "v_mfma_f32_16x16x32_bf16 v[100:103], a[36:39], a[248:251], v[100:103]\n"
        "v_exp_f32 v15, v15\n"
        "v_mfma_f32_16x16x32_bf16 v[100:103], a[40:43], a[216:219], v[100:103]\n"
        "ds_read_b128 a[104:107], v254 offset:43024\n"
        "v_cvt_pk_bf16_f32 v71, v14, v15\n"
        "v_mfma_f32_16x16x32_bf16 v[100:103], a[44:47], a[220:223], v[100:103]\n"
        "v_exp_f32 v28, v28\n"
        "v_mfma_f32_16x16x32_bf16 v[104:107], a[68:71], a[220:223], 0\n"
        "v_exp_f32 v29, v29\n"
        "v_mfma_f32_16x16x32_bf16 v[104:107], a[64:67], a[216:219], v[104:107]\n"
        "v_exp_f32 v30, v30\n"
        "v_mfma_f32_16x16x32_bf16 v[104:107], a[60:63], a[248:251], v[104:107]\n"
        "ds_read_b128 a[108:111], v254 offset:36880\n"
        "v_cvt_pk_bf16_f32 v78, v28, v29\n"
        "v_mfma_f32_16x16x32_bf16 v[104:107], a[56:59], a[244:247], v[104:107]\n"
        "v_mul_f32 v100, s50, v100\n"
        "v_mul_f32 v101, s50, v101\n"
        "v_mfma_f32_16x16x32_bf16 v[104:107], a[52:55], a[240:243], v[104:107]\n"
        "v_mul_f32 v102, s50, v102\n"
        "v_mul_f32 v103, s50, v103\n"
        "v_mfma_f32_16x16x32_bf16 v[104:107], a[48:51], a[236:239], v[104:107]\n"
        "v_cvt_pk_bf16_f32 v218, v100, v101\n"
        "v_cvt_pk_bf16_f32 v219, v102, v103\n"
        "v_mfma_f32_16x16x32_bf16 v[96:99], a[236:239], a[144:147], 0\n"
        "v_exp_f32 v31, v31\n"
        "v_mfma_f32_16x16x32_bf16 v[96:99], a[240:243], a[148:151], v[96:99]\n"
        "ds_read_b128 a[112:115], v254 offset:6160\n"
        "v_cvt_pk_bf16_f32 v79, v30, v31\n"
        "v_mfma_f32_16x16x32_bf16 v[96:99], a[244:247], a[152:155], v[96:99]\n"
        "v_mul_f32 v104, s50, v104\n"
        "v_mul_f32 v105, s50, v105\n"
        "v_mfma_f32_16x16x32_bf16 v[96:99], a[248:251], a[156:159], v[96:99]\n"
        "v_mul_f32 v106, s50, v106\n"
        "v_mul_f32 v107, s50, v107\n"
        "v_mfma_f32_16x16x32_bf16 v[96:99], a[216:219], a[160:163], v[96:99]\n"
        "v_cvt_pk_bf16_f32 v220, v104, v105\n"
        "v_cvt_pk_bf16_f32 v221, v106, v107\n"
        "v_mfma_f32_16x16x32_bf16 v[96:99], a[220:223], a[164:167], v[96:99]\n"
        "ds_read_b128 a[224:227], v240 offset:12288\n"
        "v_mov_b64 v[226:227], v[220:221]\n"
        "v_mfma_f32_16x16x32_bf16 v[48:51], v[152:155], v[216:219], v[48:51]\n"
        "s_waitcnt lgkmcnt(5)\n"
        "v_exp_f32 v44, v44\n"
        "v_mfma_f32_16x16x32_bf16 v[48:51], v[168:171], v[220:223], v[48:51]\n"
        "v_exp_f32 v45, v45\n"
        "v_mfma_f32_16x16x32_bf16 v[52:55], v[172:175], v[216:219], v[52:55]\n"
        "v_exp_f32 v46, v46\n"
        "v_mfma_f32_16x16x32_bf16 v[52:55], v[188:191], v[224:227], v[52:55]\n"
        "ds_read_b128 v[222:225], v252 offset:4608\n"
        "v_cvt_pk_bf16_f32 v134, v96, v97\n"
        "v_mfma_f32_16x16x32_bf16 v[100:103], a[220:223], a[188:191], 0\n"
        "ds_read_b128 a[228:231], v241 offset:12288\n"
        "v_cvt_pk_bf16_f32 v135, v98, v99\n"
        "v_mfma_f32_16x16x32_bf16 v[100:103], a[216:219], a[184:187], v[100:103]\n"
        "ds_read_b128 a[232:235], v240 offset:12416\n"
        "v_cvt_pk_bf16_f32 v86, v44, v45\n"
        "v_mfma_f32_16x16x32_bf16 v[100:103], a[248:251], a[180:183], v[100:103]\n"
        "v_exp_f32 v47, v47\n"
        "v_mfma_f32_16x16x32_bf16 v[100:103], a[244:247], a[176:179], v[100:103]\n"
        "ds_read_b128 a[116:119], v254 offset:16\n"
        "v_cvt_pk_bf16_f32 v87, v46, v47\n"
        "v_mfma_f32_16x16x32_bf16 v[100:103], a[240:243], a[172:175], v[100:103]\n"
        "v_exp_f32 v48, v48\n"
        "v_mfma_f32_16x16x32_bf16 v[100:103], a[236:239], a[168:171], v[100:103]\n"
        "v_exp_f32 v49, v49\n"
        "v_mfma_f32_16x16x32_bf16 v[56:59], v[192:195], v[216:219], v[56:59]\n"
        "s_waitcnt lgkmcnt(3)\n"
        "v_exp_f32 v50, v50\n"
        "v_mfma_f32_16x16x32_bf16 v[56:59], v[208:211], v[220:223], v[56:59]\n"
        "ds_read_b128 a[120:123], v248 offset:32\n"
        "v_cvt_pk_bf16_f32 v88, v48, v49\n"
        "v_mfma_f32_16x16x32_bf16 v[60:63], v[212:215], v[216:219], v[60:63]\n"
        "v_exp_f32 v51, v51\n"
        "v_mfma_f32_16x16x32_bf16 v[60:63], v[228:231], v[224:227], v[60:63]\n"
        "v_cvt_pk_bf16_f32 v142, v100, v101\n"
        "v_cvt_pk_bf16_f32 v143, v102, v103\n"
        "v_mfma_f32_16x16x32_bf16 v[104:107], a[236:239], a[192:195], 0\n"
        "ds_read_b128 a[236:239], v241 offset:12416\n"
        "v_cvt_pk_bf16_f32 v89, v50, v51\n"
        "v_mfma_f32_16x16x32_bf16 v[104:107], a[240:243], a[196:199], v[104:107]\n"
        "ds_read_b128 a[240:243], v240 offset:12544\n"
        "v_mfma_f32_16x16x32_bf16 v[104:107], a[244:247], a[200:203], v[104:107]\n"
        "ds_read_b128 a[244:247], v241 offset:12544\n"
        "v_mfma_f32_16x16x32_bf16 v[104:107], a[248:251], a[204:207], v[104:107]\n"
        "v_exp_f32 v52, v52\n"
        "v_mfma_f32_16x16x32_bf16 v[104:107], a[216:219], a[208:211], v[104:107]\n"
        "v_exp_f32 v53, v53\n"
        "v_mfma_f32_16x16x32_bf16 v[104:107], a[220:223], a[212:215], v[104:107]\n"
        "v_exp_f32 v54, v54\n"
        "v_mfma_f32_16x16x32_bf16 v[96:99], a[224:227], a[144:147], 0\n"
        "s_waitcnt lgkmcnt(6)\n"
        "ds_read_b128 a[248:251], v240 offset:1536\n"
        "v_cvt_pk_bf16_f32 v90, v52, v53\n"
        "v_mfma_f32_16x16x32_bf16 v[96:99], a[228:231], a[148:151], v[96:99]\n"
        "s_waitcnt lgkmcnt(6)\n"
        "v_exp_f32 v55, v55\n"
        "v_mfma_f32_16x16x32_bf16 v[96:99], a[232:235], a[152:155], v[96:99]\n"
        "s_waitcnt lgkmcnt(3)\n"
        "ds_read_b128 a[216:219], v241 offset:1536\n"
        "v_cvt_pk_bf16_f32 v150, v104, v105\n"
        "v_mfma_f32_16x16x32_bf16 v[96:99], a[236:239], a[156:159], v[96:99]\n"
        "s_waitcnt lgkmcnt(3)\n"
        "ds_read_b128 a[220:223], v240 offset:1664\n"
        "v_cvt_pk_bf16_f32 v151, v106, v107\n"
        "v_mfma_f32_16x16x32_bf16 v[96:99], a[240:243], a[160:163], v[96:99]\n"
        "s_waitcnt lgkmcnt(3)\n"
        "ds_read_b128 a[124:127], v248 offset:6176\n"
        "v_cvt_pk_bf16_f32 v91, v54, v55\n"
        "v_mfma_f32_16x16x32_bf16 v[96:99], a[244:247], a[164:167], v[96:99]\n"
        "v_exp_f32 v56, v56\n"
        "v_mfma_f32_16x16x32_bf16 v[100:103], a[244:247], a[188:191], 0\n"
        "v_exp_f32 v57, v57\n"
        "v_mfma_f32_16x16x32_bf16 v[100:103], a[240:243], a[184:187], v[100:103]\n"
        "v_exp_f32 v58, v58\n"
        "v_mfma_f32_16x16x32_bf16 v[100:103], a[236:239], a[180:183], v[100:103]\n"
        "ds_read_b128 v[32:35], v247 offset:0\n"
        "v_cvt_pk_bf16_f32 v92, v56, v57\n"
        "v_mfma_f32_16x16x32_bf16 v[100:103], a[232:235], a[176:179], v[100:103]\n"
        "v_cvt_pk_bf16_f32 v132, v96, v97\n"
        "v_cvt_pk_bf16_f32 v133, v98, v99\n"
        "v_mfma_f32_16x16x32_bf16 v[100:103], a[228:231], a[172:175], v[100:103]\n"
        "v_exp_f32 v59, v59\n"
        "v_mfma_f32_16x16x32_bf16 v[100:103], a[224:227], a[168:171], v[100:103]\n"
        "ds_read_b128 v[156:159], v247 offset:4096\n"
        "v_cvt_pk_bf16_f32 v93, v58, v59\n"
        "v_mfma_f32_16x16x32_bf16 v[104:107], a[224:227], a[192:195], 0\n"
        "ds_read_b128 a[224:227], v241 offset:1664\n"
        "v_mfma_f32_16x16x32_bf16 v[104:107], a[228:231], a[196:199], v[104:107]\n"
        "ds_read_b128 a[228:231], v240 offset:1792\n"
        "v_mfma_f32_16x16x32_bf16 v[104:107], a[232:235], a[200:203], v[104:107]\n"
        "ds_read_b128 a[232:235], v241 offset:1792\n"
        "v_cvt_pk_bf16_f32 v140, v100, v101\n"
        "v_mfma_f32_16x16x32_bf16 v[104:107], a[236:239], a[204:207], v[104:107]\n"
        "ds_read_b128 v[160:163], v247 offset:8192\n"
        "v_cvt_pk_bf16_f32 v141, v102, v103\n"
        "v_mfma_f32_16x16x32_bf16 v[104:107], a[240:243], a[208:211], v[104:107]\n"
        "v_exp_f32 v60, v60\n"
        "v_mfma_f32_16x16x32_bf16 v[104:107], a[244:247], a[212:215], v[104:107]\n"
        "s_waitcnt lgkmcnt(9)\n"
        "v_exp_f32 v61, v61\n"
        "v_mfma_f32_16x16x32_bf16 v[96:99], a[248:251], a[144:147], 0\n"
        "s_waitcnt lgkmcnt(8)\n"
        "v_exp_f32 v62, v62\n"
        "v_mfma_f32_16x16x32_bf16 v[96:99], a[216:219], a[148:151], v[96:99]\n"
        "s_waitcnt lgkmcnt(7)\n"
        "ds_read_b128 v[164:167], v247 offset:12288\n"
        "v_cvt_pk_bf16_f32 v94, v60, v61\n"
        "v_mfma_f32_16x16x32_bf16 v[96:99], a[220:223], a[152:155], v[96:99]\n"
        "s_waitcnt lgkmcnt(4)\n"
        "v_exp_f32 v63, v63\n"
        "v_mfma_f32_16x16x32_bf16 v[96:99], a[224:227], a[156:159], v[96:99]\n"
        "s_waitcnt lgkmcnt(3)\n"
        "v_cvt_pk_bf16_f32 v148, v104, v105\n"
        "v_cvt_pk_bf16_f32 v149, v106, v107\n"
        "v_mfma_f32_16x16x32_bf16 v[96:99], a[228:231], a[160:163], v[96:99]\n"
        "s_waitcnt lgkmcnt(2)\n"
        "ds_read_b128 v[152:155], v247 offset:16384\n"
        "v_cvt_pk_bf16_f32 v95, v62, v63\n"
        "v_mfma_f32_16x16x32_bf16 v[96:99], a[232:235], a[164:167], v[96:99]\n"
        "46:\n"
        "s_cbranch_execz 40b\n"
        "ds_read_b128 v[168:171], v247 offset:20480\n"
        "s_nop 0\n"
        "v_mfma_f32_16x16x32_bf16 v[100:103], a[232:235], a[188:191], 0\n"
        "ds_read_b128 a[128:131], v248 offset:36896\n"
        "s_nop 0\n"
        "v_mfma_f32_16x16x32_bf16 v[100:103], a[228:231], a[184:187], v[100:103]\n"
        "ds_read_b128 a[132:135], v248 offset:43040\n"
        "v_mfma_f32_16x16x32_bf16 v[100:103], a[224:227], a[180:183], v[100:103]\n"
        "ds_read_b128 a[136:139], v248 offset:12320\n"
        "v_cvt_pk_bf16_f32 v130, v96, v97\n"
        "v_mfma_f32_16x16x32_bf16 v[100:103], a[220:223], a[176:179], v[100:103]\n"
        "ds_read_b128 a[140:143], v248 offset:49184\n"
        "v_cvt_pk_bf16_f32 v131, v98, v99\n"
        "v_mfma_f32_16x16x32_bf16 v[100:103], a[216:219], a[172:175], v[100:103]\n"
        "ds_read_b128 a[0:3], v254 offset:49200\n"
        "v_mfma_f32_16x16x32_bf16 v[100:103], a[248:251], a[168:171], v[100:103]\n"
        "ds_read_b128 a[4:7], v254 offset:12336\n"
        "v_mfma_f32_16x16x32_bf16 v[104:107], a[248:251], a[192:195], 0\n"
        "ds_read_b128 a[8:11], v254 offset:43056\n"
        "v_mov_b64 v[116:117], 0\n"
        "v_mfma_f32_16x16x32_bf16 v[104:107], a[216:219], a[196:199], v[104:107]\n"
        "ds_read_b128 a[12:15], v254 offset:36912\n"
        "s_nop 0\n"
        "v_mfma_f32_16x16x32_bf16 v[104:107], a[220:223], a[200:203], v[104:107]\n"
        "ds_read_b128 a[16:19], v254 offset:6192\n"
        "v_cvt_pk_bf16_f32 v138, v100, v101\n"
        "v_mfma_f32_16x16x32_bf16 v[104:107], a[224:227], a[204:207], v[104:107]\n"
        "ds_read_b128 a[20:23], v254 offset:48\n"
        "v_cvt_pk_bf16_f32 v139, v102, v103\n"
        "v_mfma_f32_16x16x32_bf16 v[104:107], a[228:231], a[208:211], v[104:107]\n"
        "ds_read_b128 a[24:27], v248 offset:64\n"
        "v_mfma_f32_16x16x32_bf16 v[104:107], a[232:235], a[212:215], v[104:107]\n"
        "ds_read_b128 a[28:31], v248 offset:6208\n"
        "v_mfma_f32_16x16x32_bf16 v[172:175], v[32:35], a[72:75], 0\n"
        "ds_read_b128 a[32:35], v248 offset:36928\n"
        "v_mfma_f32_16x16x32_bf16 v[172:175], v[156:159], a[76:79], v[172:175]\n"
        "s_waitcnt lgkmcnt(15)\n"
        "ds_read_b128 a[36:39], v248 offset:43072\n"
        "v_mfma_f32_16x16x32_bf16 v[172:175], v[160:163], a[80:83], v[172:175]\n"
        "s_waitcnt lgkmcnt(15)\n"
        "ds_read_b128 a[40:43], v248 offset:12352\n"
        "v_mfma_f32_16x16x32_bf16 v[172:175], v[164:167], a[84:87], v[172:175]\n"
        "s_waitcnt lgkmcnt(15)\n"
        "v_cvt_pk_bf16_f32 v146, v104, v105\n"
        "v_cvt_pk_bf16_f32 v147, v106, v107\n"
        "v_mfma_f32_16x16x32_bf16 v[172:175], v[152:155], a[88:91], v[172:175]\n"
        "s_waitcnt lgkmcnt(15)\n"
        "ds_read_b128 a[44:47], v248 offset:49216\n"
        "v_mfma_f32_16x16x32_bf16 v[172:175], v[168:171], a[92:95], v[172:175]\n"
        "ds_read_b128 a[48:51], v254 offset:49232\n"
        "v_smfmac_f32_16x16x64_bf16 v[116:119], a[252:255], v[64:71], v255\n"
        "ds_read_b128 a[52:55], v254 offset:12368\n"
        "v_mfma_f32_16x16x32_bf16 v[120:123], v[128:131], v[64:67], 0\n"
        "ds_read_b128 a[56:59], v254 offset:43088\n"
        "v_mfma_f32_16x16x32_bf16 v[120:123], v[132:135], v[68:71], v[120:123]\n"
        "ds_read_b128 a[60:63], v254 offset:36944\n"
        "v_mfma_f32_16x16x32_bf16 v[124:127], v[140:143], v[68:71], 0\n"
        "ds_read_b128 a[64:67], v254 offset:6224\n"
        "v_mfma_f32_16x16x32_bf16 v[124:127], v[136:139], v[64:67], v[124:127]\n"
        "ds_read_b128 a[68:71], v254 offset:80\n"
        "v_add_f32 v116, v116, v117\n"
        "v_mfma_f32_16x16x32_bf16 v[232:235], v[144:147], v[64:67], 0\n"
        "v_rcp_f32 v117, v116\n"
        "v_mfma_f32_16x16x32_bf16 v[232:235], v[148:151], v[68:71], v[232:235]\n"
        "v_mul_f32 v120, v117, v120\n"
        "v_mul_f32 v121, v117, v121\n"
        "v_mfma_f32_16x16x32_bf16 v[176:179], v[168:171], a[96:99], 0\n"
        "v_mul_f32 v122, v117, v122\n"
        "v_mul_f32 v123, v117, v123\n"
        "v_mfma_f32_16x16x32_bf16 v[176:179], v[152:155], a[100:103], v[176:179]\n"
        "v_cvt_pk_bf16_f32 v120, v120, v121\n"
        "v_cvt_pk_bf16_f32 v121, v122, v123\n"
        "v_mfma_f32_16x16x32_bf16 v[176:179], v[164:167], a[104:107], v[176:179]\n"
        "ds_write_b32 v245, v120 offset:0\n"
        "v_mfma_f32_16x16x32_bf16 v[176:179], v[160:163], a[108:111], v[176:179]\n"
        "ds_write_b32 v245, v121 offset:4\n"
        "v_mfma_f32_16x16x32_bf16 v[176:179], v[156:159], a[112:115], v[176:179]\n"
        "v_mul_f32 v124, v117, v124\n"
        "v_mul_f32 v125, v117, v125\n"
        "v_mfma_f32_16x16x32_bf16 v[176:179], v[32:35], a[116:119], v[176:179]\n"
        "v_maximum3_f32 v253, v253, v116, v117\n"
        "v_mul_f32 v126, v117, v126\n"
        "v_mfma_f32_16x16x32_bf16 v[180:183], v[32:35], a[120:123], 0\n"
        "v_mov_b64 v[100:101], 0\n"
        "v_mul_f32 v127, v117, v127\n"
        "v_mfma_f32_16x16x32_bf16 v[180:183], v[156:159], a[124:127], v[180:183]\n"
        "s_waitcnt lgkmcnt(15)\n"
        "ds_read_b128 v[0:3], v247 offset:256\n"
        "v_cvt_pk_bf16_f32 v124, v124, v125\n"
        "v_mfma_f32_16x16x32_bf16 v[180:183], v[160:163], a[128:131], v[180:183]\n"
        "s_waitcnt lgkmcnt(15)\n"
        "ds_read_b128 v[4:7], v247 offset:4352\n"
        "v_cvt_pk_bf16_f32 v125, v126, v127\n"
        "v_mfma_f32_16x16x32_bf16 v[180:183], v[164:167], a[132:135], v[180:183]\n"
        "s_waitcnt lgkmcnt(15)\n"
        "ds_write_b32 v245, v124 offset:8\n"
        "v_mfma_f32_16x16x32_bf16 v[180:183], v[152:155], a[136:139], v[180:183]\n"
        "s_waitcnt lgkmcnt(15)\n"
        "ds_write_b32 v245, v125 offset:12\n"
        "v_mfma_f32_16x16x32_bf16 v[180:183], v[168:171], a[140:143], v[180:183]\n"
        "s_waitcnt lgkmcnt(15)\n"
        "ds_read_b128 v[8:11], v247 offset:8448\n"
        "v_mul_f32 v232, v117, v232\n"
        "v_mfma_f32_16x16x32_bf16 v[184:187], v[168:171], a[0:3], 0\n"
        "s_waitcnt lgkmcnt(15)\n"
        "ds_read_b128 v[16:19], v247 offset:12544\n"
        "v_mul_f32 v233, v117, v233\n"
        "v_mfma_f32_16x16x32_bf16 v[184:187], v[152:155], a[4:7], v[184:187]\n"
        "s_waitcnt lgkmcnt(15)\n"
        "ds_read_b128 v[20:23], v247 offset:16640\n"
        "v_mul_f32 v234, v117, v234\n"
        "v_mfma_f32_16x16x32_bf16 v[184:187], v[164:167], a[8:11], v[184:187]\n"
        "s_waitcnt lgkmcnt(15)\n"
        "ds_read_b128 v[12:15], v247 offset:20736\n"
        "v_mul_f32 v235, v117, v235\n"
        "v_mfma_f32_16x16x32_bf16 v[184:187], v[160:163], a[12:15], v[184:187]\n"
        "s_waitcnt lgkmcnt(15)\n"
        "v_cvt_pk_bf16_f32 v232, v232, v233\n"
        "v_cvt_pk_bf16_f32 v233, v234, v235\n"
        "v_mfma_f32_16x16x32_bf16 v[184:187], v[156:159], a[16:19], v[184:187]\n"
        "s_waitcnt lgkmcnt(15)\n"
        "ds_write_b32 v246, v232 offset:0\n"
        "v_mfma_f32_16x16x32_bf16 v[184:187], v[32:35], a[20:23], v[184:187]\n"
        "v_cvt_pk_bf16_f32 v188, v172, v176\n"
        "v_cvt_pk_bf16_f32 v208, v173, v177\n"
        "v_smfmac_f32_16x16x64_bf16 v[100:103], a[252:255], v[72:79], v255\n"
        "ds_write_b32 v246, v233 offset:4\n"
        "v_mfma_f32_16x16x32_bf16 v[104:107], v[128:131], v[72:75], 0\n"
        "v_cvt_pk_bf16_f32 v228, v174, v178\n"
        "s_nop 0\n"
        "v_mfma_f32_16x16x32_bf16 v[104:107], v[132:135], v[76:79], v[104:107]\n"
        "v_cvt_pk_bf16_f32 v189, v180, v184\n"
        "v_cvt_pk_bf16_f32 v209, v181, v185\n"
        "v_mfma_f32_16x16x32_bf16 v[108:111], v[140:143], v[76:79], 0\n"
        "buffer_store_dwordx2 v[188:189], v242, s[16:19], s38 offen offset:0 sc1\n"
        "v_mfma_f32_16x16x32_bf16 v[108:111], v[136:139], v[72:75], v[108:111]\n"
        "v_cvt_pk_bf16_f32 v229, v182, v186\n"
        "v_cvt_pk_bf16_f32 v236, v175, v179\n"
        "v_mfma_f32_16x16x32_bf16 v[112:115], v[144:147], v[72:75], 0\n"
        "v_cvt_pk_bf16_f32 v237, v183, v187\n"
        "v_add_f32 v100, v100, v101\n"
        "v_mfma_f32_16x16x32_bf16 v[112:115], v[148:151], v[76:79], v[112:115]\n"
        "s_waitcnt lgkmcnt(15)\n"
        "buffer_store_dwordx2 v[208:209], v242, s[16:19], s38 offen offset:384 sc1\n"
        "v_mfma_f32_16x16x32_bf16 v[192:195], v[32:35], a[24:27], 0\n"
        "s_waitcnt lgkmcnt(15)\n"
        "v_rcp_f32 v101, v100\n"
        "v_mfma_f32_16x16x32_bf16 v[192:195], v[156:159], a[28:31], v[192:195]\n"
        "s_waitcnt lgkmcnt(15)\n"
        "v_mul_f32 v104, v101, v104\n"
        "v_mul_f32 v105, v101, v105\n"
        "v_mfma_f32_16x16x32_bf16 v[192:195], v[160:163], a[32:35], v[192:195]\n"
        "s_waitcnt lgkmcnt(15)\n"
        "buffer_store_dwordx2 v[228:229], v242, s[16:19], s38 offen offset:768 sc1\n"
        "v_mfma_f32_16x16x32_bf16 v[192:195], v[164:167], a[36:39], v[192:195]\n"
        "s_waitcnt lgkmcnt(15)\n"
        "v_mul_f32 v106, v101, v106\n"
        "v_mul_f32 v107, v101, v107\n"
        "v_mfma_f32_16x16x32_bf16 v[192:195], v[152:155], a[40:43], v[192:195]\n"
        "s_waitcnt lgkmcnt(15)\n"
        "v_cvt_pk_bf16_f32 v104, v104, v105\n"
        "v_cvt_pk_bf16_f32 v105, v106, v107\n"
        "v_mfma_f32_16x16x32_bf16 v[192:195], v[168:171], a[44:47], v[192:195]\n"
        "s_waitcnt lgkmcnt(15)\n"
        "buffer_store_dwordx2 v[236:237], v242, s[16:19], s38 offen offset:1152 sc1\n"
        "v_mfma_f32_16x16x32_bf16 v[196:199], v[168:171], a[48:51], 0\n"
        "s_waitcnt lgkmcnt(15)\n"
        "v_mov_b64 v[48:49], 0\n"
        "v_mul_f32 v108, v101, v108\n"
        "v_mfma_f32_16x16x32_bf16 v[196:199], v[152:155], a[52:55], v[196:199]\n"
        "s_waitcnt lgkmcnt(15)\n"
        "ds_write_b32 v245, v104 offset:256\n"
        "v_mfma_f32_16x16x32_bf16 v[196:199], v[164:167], a[56:59], v[196:199]\n"
        "s_waitcnt lgkmcnt(15)\n"
        "ds_write_b32 v245, v105 offset:260\n"
        "v_mfma_f32_16x16x32_bf16 v[196:199], v[160:163], a[60:63], v[196:199]\n"
        "s_waitcnt lgkmcnt(15)\n"
        "v_mul_f32 v109, v101, v109\n"
        "v_maximum3_f32 v253, v253, v100, v101\n"
        "v_mfma_f32_16x16x32_bf16 v[196:199], v[156:159], a[64:67], v[196:199]\n"
        "s_waitcnt lgkmcnt(14)\n"
        "v_mul_f32 v110, v101, v110\n"
        "v_mul_f32 v111, v101, v111\n"
        "v_mfma_f32_16x16x32_bf16 v[196:199], v[32:35], a[68:71], v[196:199]\n"
        "s_waitcnt lgkmcnt(11)\n"
        "v_cvt_pk_bf16_f32 v108, v108, v109\n"
        "v_cvt_pk_bf16_f32 v109, v110, v111\n"
        "v_mfma_f32_16x16x32_bf16 v[200:203], v[0:3], a[72:75], 0\n"
        "s_waitcnt lgkmcnt(10)\n"
        "ds_write_b32 v245, v108 offset:264\n"
        "v_mfma_f32_16x16x32_bf16 v[200:203], v[4:7], a[76:79], v[200:203]\n"
        "s_waitcnt lgkmcnt(8)\n"
        "ds_write_b32 v245, v109 offset:268\n"
        "v_mfma_f32_16x16x32_bf16 v[200:203], v[8:11], a[80:83], v[200:203]\n"
        "s_waitcnt lgkmcnt(8)\n"
        "v_mul_f32 v112, v101, v112\n"
        "v_cvt_pk_bf16_f32 v190, v192, v196\n"
        "v_mfma_f32_16x16x32_bf16 v[200:203], v[16:19], a[84:87], v[200:203]\n"
        "s_waitcnt lgkmcnt(7)\n"
        "buffer_store_dword v190, v250, s[16:19], s38 offen offset:0 sc1\n"
        "v_mfma_f32_16x16x32_bf16 v[200:203], v[20:23], a[88:91], v[200:203]\n"
        "s_waitcnt lgkmcnt(6)\n"
        "v_mul_f32 v113, v101, v113\n"
        "v_cvt_pk_bf16_f32 v210, v193, v197\n"
        "v_mfma_f32_16x16x32_bf16 v[200:203], v[12:15], a[92:95], v[200:203]\n"
        "v_mul_f32 v114, v101, v114\n"
        "v_mul_f32 v115, v101, v115\n"
        "v_smfmac_f32_16x16x64_bf16 v[48:51], a[252:255], v[80:87], v255\n"
        "buffer_store_dword v210, v250, s[16:19], s38 offen offset:384 sc1\n"
        "v_mfma_f32_16x16x32_bf16 v[52:55], v[128:131], v[80:83], 0\n"
        "v_cvt_pk_bf16_f32 v112, v112, v113\n"
        "v_cvt_pk_bf16_f32 v113, v114, v115\n"
        "v_mfma_f32_16x16x32_bf16 v[52:55], v[132:135], v[84:87], v[52:55]\n"
        "ds_write_b32 v246, v112 offset:256\n"
        "v_mfma_f32_16x16x32_bf16 v[56:59], v[140:143], v[84:87], 0\n"
        "v_cvt_pk_bf16_f32 v230, v194, v198\n"
        "v_cvt_pk_bf16_f32 v238, v195, v199\n"
        "v_mfma_f32_16x16x32_bf16 v[56:59], v[136:139], v[80:83], v[56:59]\n"
        "buffer_store_dword v230, v250, s[16:19], s38 offen offset:768 sc1\n"
        "v_mfma_f32_16x16x32_bf16 v[60:63], v[144:147], v[80:83], 0\n"
        "ds_write_b32 v246, v113 offset:260\n"
        "v_mfma_f32_16x16x32_bf16 v[60:63], v[148:151], v[84:87], v[60:63]\n"
        "buffer_store_dword v238, v250, s[16:19], s38 offen offset:1152 sc1\n"
        "v_mfma_f32_16x16x32_bf16 v[204:207], v[12:15], a[96:99], 0\n"
        "v_add_f32 v48, v48, v49\n"
        "v_mfma_f32_16x16x32_bf16 v[204:207], v[20:23], a[100:103], v[204:207]\n"
        "v_rcp_f32 v49, v48\n"
        "v_mfma_f32_16x16x32_bf16 v[204:207], v[16:19], a[104:107], v[204:207]\n"
        "v_mul_f32 v52, v49, v52\n"
        "v_mul_f32 v53, v49, v53\n"
        "v_mfma_f32_16x16x32_bf16 v[204:207], v[8:11], a[108:111], v[204:207]\n"
        "v_mul_f32 v54, v49, v54\n"
        "v_mul_f32 v55, v49, v55\n"
        "v_mfma_f32_16x16x32_bf16 v[204:207], v[4:7], a[112:115], v[204:207]\n"
        "v_cvt_pk_bf16_f32 v52, v52, v53\n"
        "v_cvt_pk_bf16_f32 v53, v54, v55\n"
        "v_mfma_f32_16x16x32_bf16 v[204:207], v[0:3], a[116:119], v[204:207]\n"
        "ds_write_b32 v245, v52 offset:512\n"
        "v_mfma_f32_16x16x32_bf16 v[212:215], v[0:3], a[120:123], 0\n"
        "v_mov_b64 v[64:65], 0\n"
        "v_mul_f32 v56, v49, v56\n"
        "v_mfma_f32_16x16x32_bf16 v[212:215], v[4:7], a[124:127], v[212:215]\n"
        "ds_write_b32 v245, v53 offset:516\n"
        "v_mfma_f32_16x16x32_bf16 v[212:215], v[8:11], a[128:131], v[212:215]\n"
        "v_mul_f32 v57, v49, v57\n"
        "v_maximum3_f32 v253, v253, v48, v49\n"
        "v_mfma_f32_16x16x32_bf16 v[212:215], v[16:19], a[132:135], v[212:215]\n"
        "v_mul_f32 v58, v49, v58\n"
        "v_mul_f32 v59, v49, v59\n"
        "v_mfma_f32_16x16x32_bf16 v[212:215], v[20:23], a[136:139], v[212:215]\n"
        "v_cvt_pk_bf16_f32 v56, v56, v57\n"
        "v_cvt_pk_bf16_f32 v57, v58, v59\n"
        "v_mfma_f32_16x16x32_bf16 v[212:215], v[12:15], a[140:143], v[212:215]\n"
        "ds_write_b32 v245, v56 offset:520\n"
        "v_mfma_f32_16x16x32_bf16 v[216:219], v[12:15], a[0:3], 0\n"
        "ds_write_b32 v245, v57 offset:524\n"
        "v_mfma_f32_16x16x32_bf16 v[216:219], v[20:23], a[4:7], v[216:219]\n"
        "v_mul_f32 v60, v49, v60\n"
        "v_mul_f32 v61, v49, v61\n"
        "v_mfma_f32_16x16x32_bf16 v[216:219], v[16:19], a[8:11], v[216:219]\n"
        "v_mul_f32 v62, v49, v62\n"
        "v_mul_f32 v63, v49, v63\n"
        "v_mfma_f32_16x16x32_bf16 v[216:219], v[8:11], a[12:15], v[216:219]\n"
        "v_cvt_pk_bf16_f32 v60, v60, v61\n"
        "v_cvt_pk_bf16_f32 v61, v62, v63\n"
        "v_mfma_f32_16x16x32_bf16 v[216:219], v[4:7], a[16:19], v[216:219]\n"
        "ds_write_b32 v246, v60 offset:512\n"
        "v_mfma_f32_16x16x32_bf16 v[216:219], v[0:3], a[20:23], v[216:219]\n"
        "v_cvt_pk_bf16_f32 v188, v200, v204\n"
        "v_cvt_pk_bf16_f32 v208, v201, v205\n"
        "v_smfmac_f32_16x16x64_bf16 v[64:67], a[252:255], v[88:95], v255\n"
        "ds_write_b32 v246, v61 offset:516\n"
        "v_mfma_f32_16x16x32_bf16 v[68:71], v[128:131], v[88:91], 0\n"
        "v_cvt_pk_bf16_f32 v228, v202, v206\n"
        "s_nop 0\n"
        "v_mfma_f32_16x16x32_bf16 v[68:71], v[132:135], v[92:95], v[68:71]\n"
        "s_waitcnt lgkmcnt(6)\n"
        "v_cvt_pk_bf16_f32 v189, v212, v216\n"
        "v_cvt_pk_bf16_f32 v209, v213, v217\n"
        "v_mfma_f32_16x16x32_bf16 v[72:75], v[140:143], v[92:95], 0\n"
        "s_barrier\n"
        "buffer_store_dwordx2 v[188:189], v242, s[16:19], s39 offen offset:0 sc1\n"
        "v_mfma_f32_16x16x32_bf16 v[72:75], v[136:139], v[88:91], v[72:75]\n"
        "ds_read_b128 a[168:171], v243 offset:0\n"
        "v_cvt_pk_bf16_f32 v229, v214, v218\n"
        "v_mfma_f32_16x16x32_bf16 v[76:79], v[144:147], v[88:91], 0\n"
        "ds_read_b128 a[172:175], v243 offset:4096\n"
        "v_cvt_pk_bf16_f32 v236, v203, v207\n"
        "v_mfma_f32_16x16x32_bf16 v[76:79], v[148:151], v[92:95], v[76:79]\n"
        "buffer_store_dwordx2 v[208:209], v242, s[16:19], s39 offen offset:384 sc1\n"
        "v_mfma_f32_16x16x32_bf16 v[220:223], v[0:3], a[24:27], 0\n"
        "ds_read_b128 a[176:179], v243 offset:8192\n"
        "v_cvt_pk_bf16_f32 v237, v215, v219\n"
        "v_mfma_f32_16x16x32_bf16 v[220:223], v[4:7], a[28:31], v[220:223]\n"
        "ds_read_b128 a[180:183], v243 offset:12288\n"
        "v_add_f32 v64, v64, v65\n"
        "v_mfma_f32_16x16x32_bf16 v[220:223], v[8:11], a[32:35], v[220:223]\n"
        "buffer_store_dwordx2 v[228:229], v242, s[16:19], s39 offen offset:768 sc1\n"
        "v_mfma_f32_16x16x32_bf16 v[220:223], v[16:19], a[36:39], v[220:223]\n"
        "ds_read_b128 a[184:187], v243 offset:16384\n"
        "v_mfma_f32_16x16x32_bf16 v[220:223], v[20:23], a[40:43], v[220:223]\n"
        "v_rcp_f32 v65, v64\n"
        "v_mfma_f32_16x16x32_bf16 v[220:223], v[12:15], a[44:47], v[220:223]\n"
        "buffer_store_dwordx2 v[236:237], v242, s[16:19], s39 offen offset:1152 sc1\n"
        "v_mfma_f32_16x16x32_bf16 v[224:227], v[12:15], a[48:51], 0\n"
        "ds_read_b128 a[188:191], v243 offset:20480\n"
        "v_mul_f32 v68, v65, v68\n"
        "v_mfma_f32_16x16x32_bf16 v[224:227], v[20:23], a[52:55], v[224:227]\n"
        "v_mul_f32 v69, v65, v69\n"
        "v_mul_f32 v70, v65, v70\n"
        "v_mfma_f32_16x16x32_bf16 v[224:227], v[16:19], a[56:59], v[224:227]\n"
        "v_mul_f32 v71, v65, v71\n"
        "v_cvt_pk_bf16_f32 v68, v68, v69\n"
        "v_mfma_f32_16x16x32_bf16 v[224:227], v[8:11], a[60:63], v[224:227]\n"
        "v_cvt_pk_bf16_f32 v69, v70, v71\n"
        "v_mul_f32 v72, v65, v72\n"
        "v_mfma_f32_16x16x32_bf16 v[224:227], v[4:7], a[64:67], v[224:227]\n"
        "ds_write_b32 v245, v68 offset:768\n"
        "v_mfma_f32_16x16x32_bf16 v[224:227], v[0:3], a[68:71], v[224:227]\n"
        "s_waitcnt lgkmcnt(6)\n"
        "ds_write_b32 v245, v69 offset:772\n"
        "v_mfma_f32_16x16x32_bf16 v[24:27], a[168:171], a[72:75], 0\n"
        "s_waitcnt lgkmcnt(6)\n"
        "v_mul_f32 v73, v65, v73\n"
        "v_maximum3_f32 v253, v253, v64, v65\n"
        "v_mfma_f32_16x16x32_bf16 v[24:27], a[172:175], a[76:79], v[24:27]\n"
        "s_waitcnt lgkmcnt(5)\n"
        "v_mul_f32 v74, v65, v74\n"
        "v_mul_f32 v75, v65, v75\n"
        "v_mfma_f32_16x16x32_bf16 v[24:27], a[176:179], a[80:83], v[24:27]\n"
        "s_waitcnt lgkmcnt(4)\n"
        "v_cvt_pk_bf16_f32 v72, v72, v73\n"
        "v_cvt_pk_bf16_f32 v73, v74, v75\n"
        "v_mfma_f32_16x16x32_bf16 v[24:27], a[180:183], a[84:87], v[24:27]\n"
        "s_waitcnt lgkmcnt(3)\n"
        "v_cvt_pk_bf16_f32 v190, v220, v224\n"
        "v_mul_f32 v76, v65, v76\n"
        "v_mfma_f32_16x16x32_bf16 v[24:27], a[184:187], a[88:91], v[24:27]\n"
        "s_waitcnt lgkmcnt(2)\n"
        "buffer_store_dword v190, v250, s[16:19], s39 offen offset:0 sc1\n"
        "v_mfma_f32_16x16x32_bf16 v[24:27], a[188:191], a[92:95], v[24:27]\n"
        "ds_write_b32 v245, v72 offset:776\n"
        "v_mfma_f32_16x16x32_bf16 v[28:31], a[188:191], a[96:99], 0\n"
        "ds_write_b32 v245, v73 offset:780\n"
        "v_mfma_f32_16x16x32_bf16 v[28:31], a[184:187], a[100:103], v[28:31]\n"
        "v_mul_f32 v77, v65, v77\n"
        "v_cvt_pk_bf16_f32 v210, v221, v225\n"
        "v_mfma_f32_16x16x32_bf16 v[28:31], a[180:183], a[104:107], v[28:31]\n"
        "buffer_store_dword v210, v250, s[16:19], s39 offen offset:384 sc1\n"
        "v_mfma_f32_16x16x32_bf16 v[28:31], a[176:179], a[108:111], v[28:31]\n"
        "v_mul_f32 v78, v65, v78\n"
        "v_mul_f32 v79, v65, v79\n"
        "v_mfma_f32_16x16x32_bf16 v[28:31], a[172:175], a[112:115], v[28:31]\n"
        "v_cvt_pk_bf16_f32 v76, v76, v77\n"
        "v_cvt_pk_bf16_f32 v77, v78, v79\n"
        "v_mfma_f32_16x16x32_bf16 v[28:31], a[168:171], a[116:119], v[28:31]\n"
        "ds_write_b32 v246, v76 offset:768\n"
        "v_mfma_f32_16x16x32_bf16 v[32:35], a[168:171], a[120:123], 0\n"
        "v_cvt_pk_bf16_f32 v230, v222, v226\n"
        "v_cvt_pk_bf16_f32 v238, v223, v227\n"
        "v_mfma_f32_16x16x32_bf16 v[32:35], a[172:175], a[124:127], v[32:35]\n"
        "buffer_store_dword v230, v250, s[16:19], s39 offen offset:768 sc1\n"
        "v_mfma_f32_16x16x32_bf16 v[32:35], a[176:179], a[128:131], v[32:35]\n"
        "buffer_store_dword v238, v250, s[16:19], s39 offen offset:1152 sc1\n"
        "v_mfma_f32_16x16x32_bf16 v[32:35], a[180:183], a[132:135], v[32:35]\n"
        "ds_write_b32 v246, v77 offset:772\n"
        "v_mfma_f32_16x16x32_bf16 v[32:35], a[184:187], a[136:139], v[32:35]\n"
        "ds_write_b32 v244, v253\n"
        "v_mfma_f32_16x16x32_bf16 v[32:35], a[188:191], a[140:143], v[32:35]\n"
        "v_cvt_pk_bf16_f32 v172, v24, v28\n"
        "v_mfma_f32_16x16x32_bf16 v[36:39], a[188:191], a[0:3], 0\n"
        "v_mfma_f32_16x16x32_bf16 v[36:39], a[184:187], a[4:7], v[36:39]\n"
        "v_mfma_f32_16x16x32_bf16 v[36:39], a[180:183], a[8:11], v[36:39]\n"
        "v_cvt_pk_bf16_f32 v176, v25, v29\n"
        "v_mfma_f32_16x16x32_bf16 v[36:39], a[176:179], a[12:15], v[36:39]\n"
        "v_mfma_f32_16x16x32_bf16 v[36:39], a[172:175], a[16:19], v[36:39]\n"
        "s_waitcnt lgkmcnt(0)\n"
        "v_mfma_f32_16x16x32_bf16 v[36:39], a[168:171], a[20:23], v[36:39]\n"
        "s_barrier\n"
        "ds_read_b128 a[192:195], v243 offset:512\n"
        "v_cvt_pk_bf16_f32 v180, v26, v30\n"
        "v_mfma_f32_16x16x32_bf16 v[40:43], a[168:171], a[24:27], 0\n"
        "ds_read_b128 a[196:199], v243 offset:4608\n"
        "s_nop 0\n"
        "v_mfma_f32_16x16x32_bf16 v[40:43], a[172:175], a[28:31], v[40:43]\n"
        "ds_read_b128 a[200:203], v243 offset:8704\n"
        "v_cvt_pk_bf16_f32 v173, v32, v36\n"
        "v_mfma_f32_16x16x32_bf16 v[40:43], a[176:179], a[32:35], v[40:43]\n"
        "ds_read_b128 a[204:207], v243 offset:12800\n"
        "v_cvt_pk_bf16_f32 v177, v33, v37\n"
        "v_mfma_f32_16x16x32_bf16 v[40:43], a[180:183], a[36:39], v[40:43]\n"
        "buffer_store_dwordx2 v[172:173], v242, s[16:19], s35 offen offset:0 sc1\n"
        "v_mfma_f32_16x16x32_bf16 v[40:43], a[184:187], a[40:43], v[40:43]\n"
        "ds_read_b128 a[208:211], v243 offset:16896\n"
        "v_cvt_pk_bf16_f32 v181, v34, v38\n"
        "v_mfma_f32_16x16x32_bf16 v[40:43], a[188:191], a[44:47], v[40:43]\n"
        "ds_read_b128 a[212:215], v243 offset:20992\n"
        "v_cvt_pk_bf16_f32 v184, v27, v31\n"
        "v_mfma_f32_16x16x32_bf16 v[44:47], a[188:191], a[48:51], 0\n"
        "buffer_store_dwordx2 v[176:177], v242, s[16:19], s35 offen offset:384 sc1\n"
        "v_mfma_f32_16x16x32_bf16 v[44:47], a[184:187], a[52:55], v[44:47]\n"
        "ds_read_b128 v[100:103], v249\n"
        "v_cvt_pk_bf16_f32 v185, v35, v39\n"
        "v_mfma_f32_16x16x32_bf16 v[44:47], a[180:183], a[56:59], v[44:47]\n"
        "v_mfma_f32_16x16x32_bf16 v[44:47], a[176:179], a[60:63], v[44:47]\n"
        "buffer_store_dwordx2 v[180:181], v242, s[16:19], s35 offen offset:768 sc1\n"
        "v_mfma_f32_16x16x32_bf16 v[44:47], a[172:175], a[64:67], v[44:47]\n"
        "v_mfma_f32_16x16x32_bf16 v[44:47], a[168:171], a[68:71], v[44:47]\n"
        "s_waitcnt lgkmcnt(6)\n"
        "s_nop 1\n"
        "v_mfma_f32_16x16x32_bf16 v[0:3], a[192:195], a[72:75], 0\n"
        "s_waitcnt lgkmcnt(5)\n"
        "buffer_store_dwordx2 v[184:185], v242, s[16:19], s35 offen offset:1152 sc1\n"
        "v_mfma_f32_16x16x32_bf16 v[0:3], a[196:199], a[76:79], v[0:3]\n"
        "s_waitcnt lgkmcnt(4)\n"
        "s_nop 1\n"
        "v_mfma_f32_16x16x32_bf16 v[0:3], a[200:203], a[80:83], v[0:3]\n"
        "s_waitcnt lgkmcnt(3)\n"
        "v_cvt_pk_bf16_f32 v174, v40, v44\n"
        "v_cvt_pk_bf16_f32 v178, v41, v45\n"
        "v_mfma_f32_16x16x32_bf16 v[0:3], a[204:207], a[84:87], v[0:3]\n"
        "s_waitcnt lgkmcnt(2)\n"
        "buffer_store_dword v174, v250, s[16:19], s35 offen offset:0 sc1\n"
        "v_mfma_f32_16x16x32_bf16 v[0:3], a[208:211], a[88:91], v[0:3]\n"
        "s_waitcnt lgkmcnt(1)\n"
        "v_cvt_pk_bf16_f32 v182, v42, v46\n"
        "v_cvt_pk_bf16_f32 v186, v43, v47\n"
        "v_mfma_f32_16x16x32_bf16 v[0:3], a[212:215], a[92:95], v[0:3]\n"
        "v_mfma_f32_16x16x32_bf16 v[4:7], a[212:215], a[96:99], 0\n"
        "buffer_store_dword v178, v250, s[16:19], s35 offen offset:384 sc1\n"
        "v_mfma_f32_16x16x32_bf16 v[4:7], a[208:211], a[100:103], v[4:7]\n"
        "s_waitcnt lgkmcnt(0)\n"
        "v_maximum3_f32 v100, v100, v101, v102\n"
        "v_maximum3_f32 v100, v100, v103, v103\n"
        "v_mfma_f32_16x16x32_bf16 v[4:7], a[204:207], a[104:107], v[4:7]\n"
        "v_cmp_nge_f32 vcc, s25, v100\n"
        "v_mfma_f32_16x16x32_bf16 v[4:7], a[200:203], a[108:111], v[4:7]\n"
        "buffer_store_dword v182, v250, s[16:19], s35 offen offset:768 sc1\n"
        "v_mfma_f32_16x16x32_bf16 v[4:7], a[196:199], a[112:115], v[4:7]\n"
        "v_mfma_f32_16x16x32_bf16 v[4:7], a[192:195], a[116:119], v[4:7]\n"
        "v_mfma_f32_16x16x32_bf16 v[8:11], a[192:195], a[120:123], 0\n"
        "buffer_store_dword v186, v250, s[16:19], s35 offen offset:1152 sc1\n"
        "v_mfma_f32_16x16x32_bf16 v[8:11], a[196:199], a[124:127], v[8:11]\n"
        "v_mfma_f32_16x16x32_bf16 v[8:11], a[200:203], a[128:131], v[8:11]\n"
        "v_mfma_f32_16x16x32_bf16 v[8:11], a[204:207], a[132:135], v[8:11]\n"
        "v_mfma_f32_16x16x32_bf16 v[8:11], a[208:211], a[136:139], v[8:11]\n"
        "v_mfma_f32_16x16x32_bf16 v[8:11], a[212:215], a[140:143], v[8:11]\n"
        "v_mfma_f32_16x16x32_bf16 v[12:15], a[212:215], a[0:3], 0\n"
        "v_cvt_pk_bf16_f32 v192, v0, v4\n"
        "v_mfma_f32_16x16x32_bf16 v[12:15], a[208:211], a[4:7], v[12:15]\n"
        "v_cvt_pk_bf16_f32 v196, v1, v5\n"
        "v_mfma_f32_16x16x32_bf16 v[12:15], a[204:207], a[8:11], v[12:15]\n"
        "v_mfma_f32_16x16x32_bf16 v[12:15], a[200:203], a[12:15], v[12:15]\n"
        "v_cvt_pk_bf16_f32 v200, v2, v6\n"
        "v_mfma_f32_16x16x32_bf16 v[12:15], a[196:199], a[16:19], v[12:15]\n"
        "v_mfma_f32_16x16x32_bf16 v[12:15], a[192:195], a[20:23], v[12:15]\n"
        "v_cvt_pk_bf16_f32 v204, v3, v7\n"
        "s_nop 0\n"
        "v_mfma_f32_16x16x32_bf16 v[16:19], a[192:195], a[24:27], 0\n"
        "s_nop 2\n"
        "v_mfma_f32_16x16x32_bf16 v[16:19], a[196:199], a[28:31], v[16:19]\n"
        "v_mfma_f32_16x16x32_bf16 v[16:19], a[200:203], a[32:35], v[16:19]\n"
        "v_cvt_pk_bf16_f32 v193, v8, v12\n"
        "v_cvt_pk_bf16_f32 v197, v9, v13\n"
        "v_mfma_f32_16x16x32_bf16 v[16:19], a[204:207], a[36:39], v[16:19]\n"
        "buffer_store_dwordx2 v[192:193], v242, s[16:19], s36 offen offset:0 sc1\n"
        "v_mfma_f32_16x16x32_bf16 v[16:19], a[208:211], a[40:43], v[16:19]\n"
        "v_cvt_pk_bf16_f32 v201, v10, v14\n"
        "v_cvt_pk_bf16_f32 v205, v11, v15\n"
        "v_mfma_f32_16x16x32_bf16 v[16:19], a[212:215], a[44:47], v[16:19]\n"
        "buffer_store_dwordx2 v[196:197], v242, s[16:19], s36 offen offset:384 sc1\n"
        "v_mfma_f32_16x16x32_bf16 v[20:23], a[212:215], a[48:51], 0\n"
        "v_mfma_f32_16x16x32_bf16 v[20:23], a[208:211], a[52:55], v[20:23]\n"
        "buffer_store_dwordx2 v[200:201], v242, s[16:19], s36 offen offset:768 sc1\n"
        "v_mfma_f32_16x16x32_bf16 v[20:23], a[204:207], a[56:59], v[20:23]\n"
        "v_mfma_f32_16x16x32_bf16 v[20:23], a[200:203], a[60:63], v[20:23]\n"
        "buffer_store_dwordx2 v[204:205], v242, s[16:19], s36 offen offset:1152 sc1\n"
        "v_mfma_f32_16x16x32_bf16 v[20:23], a[196:199], a[64:67], v[20:23]\n"
        "v_mfma_f32_16x16x32_bf16 v[20:23], a[192:195], a[68:71], v[20:23]\n"
        "s_nop 2\n"
        "s_nop 4\n"
        "v_cvt_pk_bf16_f32 v194, v16, v20\n"
        "buffer_store_dword v194, v250, s[16:19], s36 offen offset:0 sc1\n"
        "v_cvt_pk_bf16_f32 v198, v17, v21\n"
        "buffer_store_dword v198, v250, s[16:19], s36 offen offset:384 sc1\n"
        "v_cvt_pk_bf16_f32 v202, v18, v22\n"
        "buffer_store_dword v202, v250, s[16:19], s36 offen offset:768 sc1\n"
        "v_cvt_pk_bf16_f32 v206, v19, v23\n"
        "buffer_store_dword v206, v250, s[16:19], s36 offen offset:1152 sc1\n"
        "s_cmp_lg_u64 vcc, 0\n"
        "s_cbranch_scc1 30f\n"
        "s_endpgm\n"
        "30:\n"
        : "+{v0}"(tid), "=&{s42}"(w)
        : "{s[0:1]}"(__builtin_amdgcn_kernarg_segment_ptr()), "{s2}"(blockIdx.x), "s"(ldsb)
        : "v1", "v2", "v3", "v4", "v5", "v6", "v7", "v8", "v9", "v10", "v11", "v12", "v13", "v14", "v15", "v16", "v17", "v18", "v19", "v20", "v21", "v22", "v23", "v24", "v25", "v26", "v27", "v28", "v29", "v30", "v31", "v32", "v33", "v34", "v35", "v36", "v37", "v38", "v39", "v40", "v41", "v42", "v43", "v44", "v45", "v46", "v47", "v48", "v49", "v50", "v51", "v52", "v53", "v54", "v55", "v56", "v57", "v58", "v59", "v60", "v61", "v62", "v63", "v64", "v65", "v66", "v67", "v68", "v69", "v70", "v71", "v72", "v73", "v74", "v75", "v76", "v77", "v78", "v79", "v80", "v81", "v82", "v83", "v84", "v85", "v86", "v87", "v88", "v89", "v90", "v91", "v92", "v93", "v94", "v95", "v96", "v97", "v98", "v99", "v100", "v101", "v102", "v103", "v104", "v105", "v106", "v107", "v108", "v109", "v110", "v111", "v112", "v113", "v114", "v115", "v116", "v117", "v118", "v119", "v120", "v121", "v122", "v123", "v124", "v125", "v126", "v127", "v128", "v129", "v130", "v131", "v132", "v133", "v134", "v135", "v136", "v137", "v138", "v139", "v140", "v141", "v142", "v143", "v144", "v145", "v146", "v147", "v148", "v149", "v150", "v151", "v152", "v153", "v154", "v155", "v156", "v157", "v158", "v159", "v160", "v161", "v162", "v163", "v164", "v165", "v166", "v167", "v168", "v169", "v170", "v171", "v172", "v173", "v174", "v175", "v176", "v177", "v178", "v179", "v180", "v181", "v182", "v183", "v184", "v185", "v186", "v187", "v188", "v189", "v190", "v191", "v192", "v193", "v194", "v195", "v196", "v197", "v198", "v199", "v200", "v201", "v202", "v203", "v204", "v205", "v206", "v207", "v208", "v209", "v210", "v211", "v212", "v213", "v214", "v215", "v216", "v217", "v218", "v219", "v220", "v221", "v222", "v223", "v224", "v225", "v226", "v227", "v228", "v229", "v230", "v231", "v232", "v233", "v234", "v235", "v236", "v237", "v238", "v239", "v240", "v241", "v242", "v243", "v244", "v245", "v246", "v247", "v248", "v249", "v250", "v251", "v252", "v253", "v254", "v255", "a0", "a1", "a2", "a3", "a4", "a5", "a6", "a7", "a8", "a9", "a10", "a11", "a12", "a13", "a14", "a15", "a16", "a17", "a18", "a19", "a20", "a21", "a22", "a23", "a24", "a25", "a26", "a27", "a28", "a29", "a30", "a31", "a32", "a33", "a34", "a35", "a36", "a37", "a38", "a39", "a40", "a41", "a42", "a43", "a44", "a45", "a46", "a47", "a48", "a49", "a50", "a51", "a52", "a53", "a54", "a55", "a56", "a57", "a58", "a59", "a60", "a61", "a62", "a63", "a64", "a65", "a66", "a67", "a68", "a69", "a70", "a71", "a72", "a73", "a74", "a75", "a76", "a77", "a78", "a79", "a80", "a81", "a82", "a83", "a84", "a85", "a86", "a87", "a88", "a89", "a90", "a91", "a92", "a93", "a94", "a95", "a96", "a97", "a98", "a99", "a100", "a101", "a102", "a103", "a104", "a105", "a106", "a107", "a108", "a109", "a110", "a111", "a112", "a113", "a114", "a115", "a116", "a117", "a118", "a119", "a120", "a121", "a122", "a123", "a124", "a125", "a126", "a127", "a128", "a129", "a130", "a131", "a132", "a133", "a134", "a135", "a136", "a137", "a138", "a139", "a140", "a141", "a142", "a143", "a144", "a145", "a146", "a147", "a148", "a149", "a150", "a151", "a152", "a153", "a154", "a155", "a156", "a157", "a158", "a159", "a160", "a161", "a162", "a163", "a164", "a165", "a166", "a167", "a168", "a169", "a170", "a171", "a172", "a173", "a174", "a175", "a176", "a177", "a178", "a179", "a180", "a181", "a182", "a183", "a184", "a185", "a186", "a187", "a188", "a189", "a190", "a191", "a192", "a193", "a194", "a195", "a196", "a197", "a198", "a199", "a200", "a201", "a202", "a203", "a204", "a205", "a206", "a207", "a208", "a209", "a210", "a211", "a212", "a213", "a214", "a215", "a216", "a217", "a218", "a219", "a220", "a221", "a222", "a223", "a224", "a225", "a226", "a227", "a228", "a229", "a230", "a231", "a232", "a233", "a234", "a235", "a236", "a237", "a238", "a239", "a240", "a241", "a242", "a243", "a244", "a245", "a246", "a247", "a248", "a249", "a250", "a251", "a252", "a253", "a254", "a255", "s8", "s9", "s10", "s11", "s12", "s13", "s14", "s15", "s16", "s17", "s18", "s19", "s20", "s21", "s22", "s23", "s24", "s25", "s26", "s27", "s28", "s29", "s30", "s31", "s34", "s35", "s36", "s37", "s38", "s39", "s40", "s41", "s43", "s44", "s45", "s46", "s47", "s48", "s49", "s50", "s51", "s52", "s53", "s54", "s55", "s56", "s57", "s58", "s59", "s60", "s61", "s62", "s63", "s64", "s65", "s66", "s67", "vcc", "scc", "memory");
    {
        unsigned l2;
        asm volatile("s_waitcnt vmcnt(0) lgkmcnt(0)\n s_barrier\n v_mbcnt_lo_u32_b32 %0, -1, 0\n v_mbcnt_hi_u32_b32 %0, -1, %0\n s_nop 1"
                     : "=v"(l2) :: "memory");
        guarded_pass(x, wqkv, wo, rpb, rpi, mask, out, ldsb, l2, w);
    }
}


extern "C" void kernel_launch(void* const* d_in, const int* in_sizes, int n_in, void* d_out, int out_size, void* d_ws,
                              size_t ws_size, hipStream_t stream) {
    win_attn_kernel<<<256, 256, 0, stream>>>((const char*)d_in[0], (const char*)d_in[1], (const char*)d_in[2],
                                             (const char*)d_in[3], (const char*)d_in[4], (const char*)d_in[5],
                                             (char*)d_out);

}
```

```cpp
#include <hip/hip_runtime.h>
#include <hip/hip_bf16.h>

typedef unsigned int u32x4 __attribute__((ext_vector_type(4)));

__device__ __forceinline__ void guarded_pass(const char* x, const char* wqkv, const char* wo, const char* rpb, const char* rpi,
                                             const char* mask, char* out, unsigned ldsb, const unsigned lane, const unsigned w, const unsigned bid) {
    const unsigned wx = bid & 63u, wy0 = bid >> 6;
    unsigned dm[3], wsrc[3];
#pragma unroll
    for (int p = 0; p < 3; ++p) {
        unsigned gch = 64u * p + lane, t = gch / 24u, c1 = gch % 24u;
        dm[p] = ((8u * wx + 4u + t) & 511u) * 384u + ((c1 ^ (((t >> 1) & 1u) | (w << 1))) << 4);
        wsrc[p] = t * 1152u + ((c1 ^ (2u * (((t >> 1) & 1u) | ((w & 1u) << 1)))) << 4) - 1024u * p;
    }
    u32x4 srdx = {(unsigned)(size_t)x, (unsigned)((size_t)x >> 32) & 0xffffu, 100663296u, 0x20000u};
    u32x4 srdw = {(unsigned)(size_t)wqkv, (unsigned)((size_t)wqkv >> 32) & 0xffffu, 221184u, 0x20000u};
    unsigned ya = (8u * wy0 + 36u + w) * 196608u;
    unsigned wv = w;
    unsigned w3 = w * 3072u, w9 = w * 9216u;
    __builtin_amdgcn_sched_barrier(0);
    asm volatile(
        "s_nop 4\n"
        "s_add_u32 m0, s40, 0\n"
        "s_add_u32 s64, s65, 0\n"
        "buffer_load_dwordx4 v128, s[20:23], s64 offen lds\n"
        "buffer_load_dwordx4 v129, s[20:23], s64 offen offset:1024 lds\n"
        "buffer_load_dwordx4 v130, s[20:23], s64 offen offset:2048 lds\n"
        "s_add_u32 m0, s40, 12288\n"
        "s_add_u32 s64, s65, 36864\n"
        "buffer_load_dwordx4 v128, s[20:23], s64 offen lds\n"
        "buffer_load_dwordx4 v129, s[20:23], s64 offen offset:1024 lds\n"
        "buffer_load_dwordx4 v130, s[20:23], s64 offen offset:2048 lds\n"
        "s_add_u32 m0, s40, 24576\n"
        "s_add_u32 s64, s65, 73728\n"
        "buffer_load_dwordx4 v128, s[20:23], s64 offen lds\n"
        "buffer_load_dwordx4 v129, s[20:23], s64 offen offset:1024 lds\n"
        "buffer_load_dwordx4 v130, s[20:23], s64 offen offset:2048 lds\n"
        :: "{v242}"(dm[0]), "{v243}"(dm[1]), "{v244}"(dm[2]), "{v128}"(wsrc[0]), "{v129}"(wsrc[1]), "{v130}"(wsrc[2]), "{s[8:11]}"(srdx), "{s[20:23]}"(srdw), "{s47}"(ya), "{s40}"(w3), "{s65}"(w9), "s"(ldsb)
        : "s64", "scc", "memory");
    __builtin_amdgcn_sched_barrier(0);

    const unsigned rho = lane & 15u, g = lane >> 4;
    const unsigned cg = w >> 1, T = w & 1u;
    const unsigned ql = 8u * (rho >> 2) + (rho & 3u);
    unsigned xr[2], mr[2];
#pragma unroll
    for (int j = 0; j < 2; ++j) {
        xr[j] = 73728u + ql * 384u + (((4u * j + g) ^ (rho >> 1)) << 4);
        mr[j] = 147456u + 8192u + ql * 128u + (((4u * j + g) ^ (rho >> 1)) << 4);
    }
    unsigned ow = 122880u + (4u * w + g) * 1024u + rho * 16u;
    unsigned ow2 = 122880u + (16u + 2u * w + (g >> 1)) * 1024u + rho * 16u + (g & 1u) * 8u;
    unsigned ord = 122880u + g * 1024u + (32u * T + rho) * 16u;
    __builtin_amdgcn_sched_barrier(0);
    asm volatile(
        "s_nop 4\n"
        "s_add_u32 m0, s40, 36864\n"
        "s_add_u32 s64, s65, 110592\n"
        "buffer_load_dwordx4 v128, s[20:23], s64 offen lds\n"
        "buffer_load_dwordx4 v129, s[20:23], s64 offen offset:1024 lds\n"
        "buffer_load_dwordx4 v130, s[20:23], s64 offen offset:2048 lds\n"
        :: "{v242}"(dm[0]), "{v243}"(dm[1]), "{v244}"(dm[2]), "{v128}"(wsrc[0]), "{v129}"(wsrc[1]), "{v130}"(wsrc[2]), "{s[8:11]}"(srdx), "{s[20:23]}"(srdw), "{s47}"(ya), "{s40}"(w3), "{s65}"(w9), "s"(ldsb)
        : "s64", "scc", "memory");
    __builtin_amdgcn_sched_barrier(0);
    unsigned wor = 0u + cg * 18432u + g * 1536u + rho * 96u + (g & 1u) * 16u;
    unsigned wor2 = 0u + cg * 18432u + g * 1536u + rho * 96u - (g & 1u) * 16u;
    unsigned ml = 147456u + w * 2048u + lane * 16u;
    unsigned ms = (lane >> 3) * 128u + (((lane & 7u) ^ (((lane >> 4) & 1u) | ((w & 1u) << 2))) << 4);
    unsigned st = g * 196608u + cg * 192u + rho * 12u;
    unsigned l16 = lane * 16u;
    unsigned id0 = (rho == 4u * g ? 0x3f80u : 0u) | (rho == 4u * g + 1u ? 0x3f800000u : 0u);
    unsigned id1 = (rho == 4u * g + 2u ? 0x3f80u : 0u) | (rho == 4u * g + 3u ? 0x3f800000u : 0u);
    unsigned x0 = (8u * wx + 4u) * 384u, x1 = ((8u * wx + 8u) & 511u) * 384u;
    u32x4 srdm = {(unsigned)(size_t)mask, (unsigned)((size_t)mask >> 32) & 0xffffu, 33554432u, 0x20000u};
    u32x4 srdo = {(unsigned)(size_t)out, (unsigned)((size_t)out >> 32) & 0xffffu, 100663296u, 0x20000u};
    u32x4 srdwo = {(unsigned)(size_t)wo, (unsigned)((size_t)wo >> 32) & 0xffffu, 73728u, 0x20000u};
    u32x4 srdrp = {(unsigned)(size_t)rpi, (unsigned)((size_t)rpi >> 32) & 0xffffu, 16384u, 0x20000u};
    u32x4 srdrb = {(unsigned)(size_t)rpb, (unsigned)((size_t)rpb >> 32) & 0xffffu, 1800u, 0x20000u};
    __builtin_amdgcn_sched_barrier(0);
    asm volatile(
        "s_nop 4\n"
        "s_add_u32 m0, s40, 49152\n"
        "s_add_u32 s64, s65, 147456\n"
        "buffer_load_dwordx4 v128, s[20:23], s64 offen lds\n"
        "buffer_load_dwordx4 v129, s[20:23], s64 offen offset:1024 lds\n"
        "buffer_load_dwordx4 v130, s[20:23], s64 offen offset:2048 lds\n"
        :: "{v242}"(dm[0]), "{v243}"(dm[1]), "{v244}"(dm[2]), "{v128}"(wsrc[0]), "{v129}"(wsrc[1]), "{v130}"(wsrc[2]), "{s[8:11]}"(srdx), "{s[20:23]}"(srdw), "{s47}"(ya), "{s40}"(w3), "{s65}"(w9), "s"(ldsb)
        : "s64", "scc", "memory");
    __builtin_amdgcn_sched_barrier(0);
    const unsigned q4 = (lane >> 2) & 3u, p4 = lane & 3u;
    unsigned wrd[3];
#pragma unroll
    for (int p = 0; p < 3; ++p) {
        unsigned c = 6u * w + 2u * p + (p4 >> 1);
        wrd[p] = (8u * g + q4) * 384u + ((c ^ (2u * ((q4 >> 1) | ((g & 1u) << 1)))) << 4) + (p4 & 1u) * 8u;
    }
    unsigned od = cg * 18432u + g * 1536u + (((48u * T + rho) ^ (g & 1u)) << 4);
    unsigned rbs = lane * 8u + 2u * w, rbw = 151552u + w * 1024u + lane * 4u;
    const unsigned pfi = ((bid >> 3) & 31u) * 4u + w;
    unsigned pf0 = pfi * 2048u + lane * 16u, pf1 = pfi * 1024u + lane * 16u;
    __builtin_amdgcn_sched_barrier(0);
    asm volatile(
        "s_nop 4\n"
        "s_add_u32 m0, s40, 61440\n"
        "s_add_u32 s64, s65, 184320\n"
        "buffer_load_dwordx4 v128, s[20:23], s64 offen lds\n"
        "buffer_load_dwordx4 v129, s[20:23], s64 offen offset:1024 lds\n"
        "buffer_load_dwordx4 v130, s[20:23], s64 offen offset:2048 lds\n"
        :: "{v242}"(dm[0]), "{v243}"(dm[1]), "{v244}"(dm[2]), "{v128}"(wsrc[0]), "{v129}"(wsrc[1]), "{v130}"(wsrc[2]), "{s[8:11]}"(srdx), "{s[20:23]}"(srdw), "{s47}"(ya), "{s40}"(w3), "{s65}"(w9), "s"(ldsb)
        : "s64", "scc", "memory");
    __builtin_amdgcn_sched_barrier(0);
    unsigned rs = g * 256u + ((rho ^ (g | ((w & 1u) << 3))) << 4);
    unsigned rr = 135168u + ql * 256u + (((2u * g) ^ rho) << 4);
    unsigned oa = (4u * g + q4) * 384u + 96u * w + p4 * 8u;
    unsigned ob = (48u * (g >> 1) + 8u * (g & 1u) + q4) * 384u + 96u * w + p4 * 8u;
    unsigned mk = (bid + 256u) * 8192u + w * 2048u;
    unsigned raw = (8u * wy0 + 4u + 4u * T) * 196608u;
    asm volatile(
        "s_nop 4\n"
        "s_mul_i32 s40, s42, 3072\n"
        "s_mul_i32 s65, s42, 9216\n"
        "s_lshl_b32 s48, s42, 11\n"
        "s_add_u32 s48, s48, 147456\n"
        "s_mov_b32 s24, 0x3fb8aa3b\n"
        "s_mov_b32 s25, 0xbfb8aa3b\n"
        "s_mov_b32 s26, 0xff7fff7f\n"
        "s_mov_b32 s49, 0x3e13cd3a\n"
        "s_mov_b32 s50, 0x3e13cd3a\n"
        "s_mov_b32 s51, 0x3e13cd3a\n"
        "s_mov_b32 s27, 14\n"
        "s_mov_b32 s28, 73728\n"
        "s_mov_b32 s29, 122880\n"
        "s_mov_b32 s30, 98304\n"
        "s_mov_b32 s38, 0x80000000\n"
        "s_mov_b32 s39, 0x80000000\n"
        "s_lshl_b32 s67, s42, 10\n"
        "s_add_u32 s67, s67, 151552\n"
        "v_mov_b32 v188, v170\n"
        "v_mov_b32 v189, v171\n"
        "v_mov_b32 v210, v170\n"
        "v_mov_b32 v211, v171\n"
        "v_mov_b32 v228, v170\n"
        "v_mov_b32 v229, v171\n"
        "v_accvgpr_write_b32 a252, v140\n"
        "v_add_u32 v136, 98304, v131\n"
        "v_accvgpr_write_b32 a253, v141\n"
        "v_add_u32 v137, 98304, v132\n"
        "v_accvgpr_write_b32 a254, v147\n"
        "v_add_u32 v138, 98304, v133\n"
        "v_mov_b32 v150, v240\n"
        "v_mov_b32 v151, v241\n"
        "v_xor_b32 v253, 64, v139\n"
        "v_xor_b32 v232, 0, v144\n"
        "v_xor_b32 v233, 16, v144\n"
        "v_xor_b32 v234, 128, v144\n"
        "v_xor_b32 v235, 144, v144\n"
        "s_lshl_b32 s46, s42, 12\n"
        "buffer_load_dwordx4 v[64:67], v148, s[20:23], 0 offen\n"
        "buffer_load_dwordx4 v[68:71], v148, s[20:23], 0 offen offset:1024\n"
        "buffer_load_dwordx4 v[72:75], v149, s[52:55], 0 offen\n"
        "s_waitcnt vmcnt(12)\n"
        "s_barrier\n"
        "s_add_u32 m0, s40, 73728\n"
        "ds_read_b64_tr_b16 v[76:77], v131 offset:0\n"
        "ds_read_b64_tr_b16 v[78:79], v131 offset:1536\n"
        "ds_read_b64_tr_b16 v[80:81], v131 offset:12288\n"
        "ds_read_b64_tr_b16 v[82:83], v131 offset:13824\n"
        "ds_read_b64_tr_b16 v[84:85], v131 offset:24576\n"
        "ds_read_b64_tr_b16 v[86:87], v131 offset:26112\n"
        "ds_read_b64_tr_b16 v[88:89], v132 offset:0\n"
        "ds_read_b64_tr_b16 v[90:91], v132 offset:1536\n"
        "ds_read_b64_tr_b16 v[92:93], v132 offset:12288\n"
        "ds_read_b64_tr_b16 v[94:95], v132 offset:13824\n"
        "ds_read_b64_tr_b16 v[104:105], v132 offset:24576\n"
        "ds_read_b64_tr_b16 v[106:107], v132 offset:26112\n"
        "ds_read_b64_tr_b16 v[108:109], v133 offset:0\n"
        "ds_read_b64_tr_b16 v[110:111], v133 offset:1536\n"
        "ds_read_b64_tr_b16 v[116:117], v133 offset:12288\n"
        "ds_read_b64_tr_b16 v[118:119], v133 offset:13824\n"
        "ds_read_b64_tr_b16 v[120:121], v133 offset:24576\n"
        "ds_read_b64_tr_b16 v[122:123], v133 offset:26112\n"
        "s_sub_u32 s64, s47, 6291456\n"
        "buffer_load_dwordx4 v242, s[8:11], s64 offen lds\n"
        "s_add_u32 m0, s40, 74752\n"
        "s_sub_u32 s64, s47, 6291456\n"
        "buffer_load_dwordx4 v243, s[8:11], s64 offen lds\n"
        "s_add_u32 m0, s40, 75776\n"
        "s_sub_u32 s64, s47, 6291456\n"
        "buffer_load_dwordx4 v244, s[8:11], s64 offen lds\n"
        "s_add_u32 m0, s40, 86016\n"
        "s_sub_u32 s64, s47, 5505024\n"
        "buffer_load_dwordx4 v242, s[8:11], s64 offen lds\n"
        "s_add_u32 m0, s40, 87040\n"
        "s_sub_u32 s64, s47, 5505024\n"
        "buffer_load_dwordx4 v243, s[8:11], s64 offen lds\n"
        "s_add_u32 m0, s40, 88064\n"
        "s_sub_u32 s64, s47, 5505024\n"
        "buffer_load_dwordx4 v244, s[8:11], s64 offen lds\n"
        "s_waitcnt lgkmcnt(15)\n"
        "v_lshlrev_b32 v0, 16, v76\n"
        "v_lshlrev_b32 v2, 16, v77\n"
        "v_lshlrev_b32 v4, 16, v78\n"
        "v_lshlrev_b32 v6, 16, v79\n"
        "v_and_b32 v1, 0xffff0000, v76\n"
        "v_and_b32 v3, 0xffff0000, v77\n"
        "v_and_b32 v5, 0xffff0000, v78\n"
        "v_and_b32 v7, 0xffff0000, v79\n"
        "v_pk_mul_f32 v[0:1], v[0:1], s[50:51]\n"
        "v_pk_mul_f32 v[2:3], v[2:3], s[50:51]\n"
        "v_pk_mul_f32 v[4:5], v[4:5], s[50:51]\n"
        "v_pk_mul_f32 v[6:7], v[6:7], s[50:51]\n"
        "v_cvt_pk_bf16_f32 v0, v0, v1\n"
        "v_cvt_pk_bf16_f32 v2, v2, v3\n"
        "v_cvt_pk_bf16_f32 v4, v4, v5\n"
        "v_cvt_pk_bf16_f32 v6, v6, v7\n"
        "v_accvgpr_write_b32 a0, v0\n"
        "v_accvgpr_write_b32 a1, v2\n"
        "v_accvgpr_write_b32 a2, v4\n"
        "v_accvgpr_write_b32 a3, v6\n"
        "v_lshlrev_b32 v8, 16, v80\n"
        "v_lshlrev_b32 v10, 16, v81\n"
        "s_waitcnt lgkmcnt(14)\n"
        "v_lshlrev_b32 v12, 16, v82\n"
        "v_lshlrev_b32 v14, 16, v83\n"
        "v_and_b32 v9, 0xffff0000, v80\n"
        "v_and_b32 v11, 0xffff0000, v81\n"
        "v_and_b32 v13, 0xffff0000, v82\n"
        "v_and_b32 v15, 0xffff0000, v83\n"
        "v_pk_mul_f32 v[8:9], v[8:9], s[50:51]\n"
        "v_pk_mul_f32 v[10:11], v[10:11], s[50:51]\n"
        "v_pk_mul_f32 v[12:13], v[12:13], s[50:51]\n"
        "v_pk_mul_f32 v[14:15], v[14:15], s[50:51]\n"
        "v_cvt_pk_bf16_f32 v8, v8, v9\n"
        "v_cvt_pk_bf16_f32 v10, v10, v11\n"
        "v_cvt_pk_bf16_f32 v12, v12, v13\n"
        "v_cvt_pk_bf16_f32 v14, v14, v15\n"
        "v_accvgpr_write_b32 a4, v8\n"
        "v_accvgpr_write_b32 a5, v10\n"
        "v_accvgpr_write_b32 a6, v12\n"
        "v_accvgpr_write_b32 a7, v14\n"
        "s_waitcnt lgkmcnt(13)\n"
        "v_lshlrev_b32 v0, 16, v84\n"
        "v_lshlrev_b32 v2, 16, v85\n"
        "s_waitcnt lgkmcnt(12)\n"
        "v_lshlrev_b32 v4, 16, v86\n"
        "v_lshlrev_b32 v6, 16, v87\n"
        "v_and_b32 v1, 0xffff0000, v84\n"
        "v_and_b32 v3, 0xffff0000, v85\n"
        "v_and_b32 v5, 0xffff0000, v86\n"
        "v_and_b32 v7, 0xffff0000, v87\n"
        "v_pk_mul_f32 v[0:1], v[0:1], s[50:51]\n"
        "v_pk_mul_f32 v[2:3], v[2:3], s[50:51]\n"
        "v_pk_mul_f32 v[4:5], v[4:5], s[50:51]\n"
        "v_pk_mul_f32 v[6:7], v[6:7], s[50:51]\n"
        "v_cvt_pk_bf16_f32 v0, v0, v1\n"
        "v_cvt_pk_bf16_f32 v2, v2, v3\n"
        "v_cvt_pk_bf16_f32 v4, v4, v5\n"
        "v_cvt_pk_bf16_f32 v6, v6, v7\n"
        "v_accvgpr_write_b32 a8, v0\n"
        "v_accvgpr_write_b32 a9, v2\n"
        "v_accvgpr_write_b32 a10, v4\n"
        "v_accvgpr_write_b32 a11, v6\n"
        "s_waitcnt lgkmcnt(11)\n"
        "v_lshlrev_b32 v8, 16, v88\n"
        "v_lshlrev_b32 v10, 16, v89\n"
        "s_waitcnt lgkmcnt(10)\n"
        "v_lshlrev_b32 v12, 16, v90\n"
        "v_lshlrev_b32 v14, 16, v91\n"
        "buffer_load_ushort v236, v145, s[60:63], 0 offen offset:0\n"
        "buffer_load_ushort v237, v145, s[60:63], 0 offen offset:512\n"
        "buffer_load_ushort v238, v145, s[60:63], 0 offen offset:1024\n"
        "buffer_load_ushort v239, v145, s[60:63], 0 offen offset:1536\n"
        "v_and_b32 v9, 0xffff0000, v88\n"
        "v_and_b32 v11, 0xffff0000, v89\n"
        "v_and_b32 v13, 0xffff0000, v90\n"
        "v_and_b32 v15, 0xffff0000, v91\n"
        "v_pk_mul_f32 v[8:9], v[8:9], s[50:51]\n"
        "v_pk_mul_f32 v[10:11], v[10:11], s[50:51]\n"
        "v_pk_mul_f32 v[12:13], v[12:13], s[50:51]\n"
        "v_pk_mul_f32 v[14:15], v[14:15], s[50:51]\n"
        "s_add_u32 m0, s46, 135168\n"
        "v_cvt_pk_bf16_f32 v8, v8, v9\n"
        "v_cvt_pk_bf16_f32 v10, v10, v11\n"
        "v_cvt_pk_bf16_f32 v12, v12, v13\n"
        "v_cvt_pk_bf16_f32 v14, v14, v15\n"
        "s_mov_b32 s64, s46\n"
        "v_accvgpr_write_b32 a24, v8\n"
        "v_accvgpr_write_b32 a25, v10\n"
        "v_accvgpr_write_b32 a26, v12\n"
        "v_accvgpr_write_b32 a27, v14\n"
        "buffer_load_dwordx4 v139, s[56:59], s64 offen lds\n"
        "s_waitcnt lgkmcnt(9)\n"
        "v_lshlrev_b32 v0, 16, v92\n"
        "v_lshlrev_b32 v2, 16, v93\n"
        "s_waitcnt lgkmcnt(8)\n"
        "v_lshlrev_b32 v4, 16, v94\n"
        "v_lshlrev_b32 v6, 16, v95\n"
        "s_waitcnt lgkmcnt(0)\n"
        "s_waitcnt vmcnt(14)\n"
        "s_barrier\n"
        "v_and_b32 v1, 0xffff0000, v92\n"
        "v_and_b32 v3, 0xffff0000, v93\n"
        "v_and_b32 v5, 0xffff0000, v94\n"
        "v_and_b32 v7, 0xffff0000, v95\n"
        "ds_read_b64_tr_b16 v[152:153], v131 offset:36864\n"
        "ds_read_b64_tr_b16 v[154:155], v131 offset:38400\n"
        "ds_read_b64_tr_b16 v[172:173], v131 offset:49152\n"
        "ds_read_b64_tr_b16 v[174:175], v131 offset:50688\n"
        "ds_read_b64_tr_b16 v[192:193], v131 offset:61440\n"
        "ds_read_b64_tr_b16 v[194:195], v131 offset:62976\n"
        "ds_read_b64_tr_b16 v[212:213], v132 offset:36864\n"
        "ds_read_b64_tr_b16 v[214:215], v132 offset:38400\n"
        "ds_read_b64_tr_b16 v[156:157], v132 offset:49152\n"
        "ds_read_b64_tr_b16 v[158:159], v132 offset:50688\n"
        "ds_read_b64_tr_b16 v[176:177], v132 offset:61440\n"
        "ds_read_b64_tr_b16 v[178:179], v132 offset:62976\n"
        "ds_read_b64_tr_b16 v[196:197], v133 offset:36864\n"
        "ds_read_b64_tr_b16 v[198:199], v133 offset:38400\n"
        "ds_read_b64_tr_b16 v[216:217], v133 offset:49152\n"
        "ds_read_b64_tr_b16 v[218:219], v133 offset:50688\n"
        "ds_read_b64_tr_b16 v[124:125], v133 offset:61440\n"
        "ds_read_b64_tr_b16 v[126:127], v133 offset:62976\n"
        "v_pk_mul_f32 v[0:1], v[0:1], s[50:51]\n"
        "v_pk_mul_f32 v[2:3], v[2:3], s[50:51]\n"
        "v_pk_mul_f32 v[4:5], v[4:5], s[50:51]\n"
        "v_pk_mul_f32 v[6:7], v[6:7], s[50:51]\n"
        "v_cvt_pk_bf16_f32 v0, v0, v1\n"
        "v_cvt_pk_bf16_f32 v2, v2, v3\n"
        "v_cvt_pk_bf16_f32 v4, v4, v5\n"
        "v_cvt_pk_bf16_f32 v6, v6, v7\n"
        "buffer_load_dwordx4 v139, s[56:59], s64 offen offset:1024 lds\n"
        "v_accvgpr_write_b32 a28, v0\n"
        "v_accvgpr_write_b32 a29, v2\n"
        "v_accvgpr_write_b32 a30, v4\n"
        "v_accvgpr_write_b32 a31, v6\n"
        "v_lshlrev_b32 v8, 16, v104\n"
        "v_lshlrev_b32 v10, 16, v105\n"
        "v_lshlrev_b32 v12, 16, v106\n"
        "v_lshlrev_b32 v14, 16, v107\n"
        "v_and_b32 v9, 0xffff0000, v104\n"
        "v_and_b32 v11, 0xffff0000, v105\n"
        "v_and_b32 v13, 0xffff0000, v106\n"
        "v_and_b32 v15, 0xffff0000, v107\n"
        "v_pk_mul_f32 v[8:9], v[8:9], s[50:51]\n"
        "v_pk_mul_f32 v[10:11], v[10:11], s[50:51]\n"
        "v_pk_mul_f32 v[12:13], v[12:13], s[50:51]\n"
        "v_pk_mul_f32 v[14:15], v[14:15], s[50:51]\n"
        "buffer_load_dwordx4 v253, s[56:59], s64 offen offset:2048 lds\n"
        "v_cvt_pk_bf16_f32 v8, v8, v9\n"
        "v_cvt_pk_bf16_f32 v10, v10, v11\n"
        "v_cvt_pk_bf16_f32 v12, v12, v13\n"
        "v_cvt_pk_bf16_f32 v14, v14, v15\n"
        "v_accvgpr_write_b32 a32, v8\n"
        "v_accvgpr_write_b32 a33, v10\n"
        "v_accvgpr_write_b32 a34, v12\n"
        "v_accvgpr_write_b32 a35, v14\n"
        "v_lshlrev_b32 v0, 16, v108\n"
        "v_lshlrev_b32 v2, 16, v109\n"
        "v_lshlrev_b32 v4, 16, v110\n"
        "v_lshlrev_b32 v6, 16, v111\n"
        "v_and_b32 v1, 0xffff0000, v108\n"
        "v_and_b32 v3, 0xffff0000, v109\n"
        "v_and_b32 v5, 0xffff0000, v110\n"
        "v_and_b32 v7, 0xffff0000, v111\n"
        "buffer_load_dwordx4 v253, s[56:59], s64 offen offset:3072 lds\n"
        "v_pk_mul_f32 v[0:1], v[0:1], s[50:51]\n"
        "v_pk_mul_f32 v[2:3], v[2:3], s[50:51]\n"
        "v_pk_mul_f32 v[4:5], v[4:5], s[50:51]\n"
        "v_pk_mul_f32 v[6:7], v[6:7], s[50:51]\n"
        "v_cvt_pk_bf16_f32 v0, v0, v1\n"
        "v_cvt_pk_bf16_f32 v2, v2, v3\n"
        "v_cvt_pk_bf16_f32 v4, v4, v5\n"
        "v_cvt_pk_bf16_f32 v6, v6, v7\n"
        "v_accvgpr_write_b32 a48, v0\n"
        "v_accvgpr_write_b32 a49, v2\n"
        "v_accvgpr_write_b32 a50, v4\n"
        "v_accvgpr_write_b32 a51, v6\n"
        "v_lshlrev_b32 v8, 16, v116\n"
        "v_lshlrev_b32 v10, 16, v117\n"
        "v_lshlrev_b32 v12, 16, v118\n"
        "v_lshlrev_b32 v14, 16, v119\n"
        "v_and_b32 v9, 0xffff0000, v116\n"
        "v_and_b32 v11, 0xffff0000, v117\n"
        "v_and_b32 v13, 0xffff0000, v118\n"
        "v_and_b32 v15, 0xffff0000, v119\n"
        "v_pk_mul_f32 v[8:9], v[8:9], s[50:51]\n"
        "v_pk_mul_f32 v[10:11], v[10:11], s[50:51]\n"
        "v_pk_mul_f32 v[12:13], v[12:13], s[50:51]\n"
        "v_pk_mul_f32 v[14:15], v[14:15], s[50:51]\n"
        "v_cvt_pk_bf16_f32 v8, v8, v9\n"
        "v_cvt_pk_bf16_f32 v10, v10, v11\n"
        "v_cvt_pk_bf16_f32 v12, v12, v13\n"
        "v_cvt_pk_bf16_f32 v14, v14, v15\n"
        "v_accvgpr_write_b32 a52, v8\n"
        "v_accvgpr_write_b32 a53, v10\n"
        "v_accvgpr_write_b32 a54, v12\n"
        "v_accvgpr_write_b32 a55, v14\n"
        "v_lshlrev_b32 v0, 16, v120\n"
        "v_lshlrev_b32 v2, 16, v121\n"
        "v_lshlrev_b32 v4, 16, v122\n"
        "v_lshlrev_b32 v6, 16, v123\n"
        "v_and_b32 v1, 0xffff0000, v120\n"
        "v_and_b32 v3, 0xffff0000, v121\n"
        "v_and_b32 v5, 0xffff0000, v122\n"
        "v_and_b32 v7, 0xffff0000, v123\n"
        "v_pk_mul_f32 v[0:1], v[0:1], s[50:51]\n"
        "v_pk_mul_f32 v[2:3], v[2:3], s[50:51]\n"
        "v_pk_mul_f32 v[4:5], v[4:5], s[50:51]\n"
        "v_pk_mul_f32 v[6:7], v[6:7], s[50:51]\n"
        "v_cvt_pk_bf16_f32 v0, v0, v1\n"
        "v_cvt_pk_bf16_f32 v2, v2, v3\n"
        "v_cvt_pk_bf16_f32 v4, v4, v5\n"
        "v_cvt_pk_bf16_f32 v6, v6, v7\n"
        "v_accvgpr_write_b32 a56, v0\n"
        "v_accvgpr_write_b32 a57, v2\n"
        "v_accvgpr_write_b32 a58, v4\n"
        "v_accvgpr_write_b32 a59, v6\n"
        "s_waitcnt lgkmcnt(15)\n"
        "v_lshlrev_b32 v8, 16, v152\n"
        "v_lshlrev_b32 v10, 16, v153\n"
        "v_lshlrev_b32 v12, 16, v154\n"
        "v_lshlrev_b32 v14, 16, v155\n"
        "v_and_b32 v9, 0xffff0000, v152\n"
        "v_and_b32 v11, 0xffff0000, v153\n"
        "v_and_b32 v13, 0xffff0000, v154\n"
        "v_and_b32 v15, 0xffff0000, v155\n"
        "v_pk_mul_f32 v[8:9], v[8:9], s[50:51]\n"
        "v_pk_mul_f32 v[10:11], v[10:11], s[50:51]\n"
        "v_pk_mul_f32 v[12:13], v[12:13], s[50:51]\n"
        "v_pk_mul_f32 v[14:15], v[14:15], s[50:51]\n"
        "v_cvt_pk_bf16_f32 v8, v8, v9\n"
        "v_cvt_pk_bf16_f32 v10, v10, v11\n"
        "v_cvt_pk_bf16_f32 v12, v12, v13\n"
        "v_cvt_pk_bf16_f32 v14, v14, v15\n"
        "v_accvgpr_write_b32 a12, v8\n"
        "v_accvgpr_write_b32 a13, v10\n"
        "v_accvgpr_write_b32 a14, v12\n"
        "v_accvgpr_write_b32 a15, v14\n"
        "v_lshlrev_b32 v0, 16, v172\n"
        "v_lshlrev_b32 v2, 16, v173\n"
        "s_waitcnt lgkmcnt(14)\n"
        "v_lshlrev_b32 v4, 16, v174\n"
        "v_lshlrev_b32 v6, 16, v175\n"
        "v_and_b32 v1, 0xffff0000, v172\n"
        "v_and_b32 v3, 0xffff0000, v173\n"
        "v_and_b32 v5, 0xffff0000, v174\n"
        "v_and_b32 v7, 0xffff0000, v175\n"
        "v_pk_mul_f32 v[0:1], v[0:1], s[50:51]\n"
        "v_pk_mul_f32 v[2:3], v[2:3], s[50:51]\n"
        "v_pk_mul_f32 v[4:5], v[4:5], s[50:51]\n"
        "v_pk_mul_f32 v[6:7], v[6:7], s[50:51]\n"
        "v_cvt_pk_bf16_f32 v0, v0, v1\n"
        "v_cvt_pk_bf16_f32 v2, v2, v3\n"
        "v_cvt_pk_bf16_f32 v4, v4, v5\n"
        "v_cvt_pk_bf16_f32 v6, v6, v7\n"
        "v_accvgpr_write_b32 a16, v0\n"
        "v_accvgpr_write_b32 a17, v2\n"
        "v_accvgpr_write_b32 a18, v4\n"
        "v_accvgpr_write_b32 a19, v6\n"
        "s_waitcnt lgkmcnt(13)\n"
        "v_lshlrev_b32 v8, 16, v192\n"
        "v_lshlrev_b32 v10, 16, v193\n"
        "s_waitcnt lgkmcnt(12)\n"
        "v_lshlrev_b32 v12, 16, v194\n"
        "v_lshlrev_b32 v14, 16, v195\n"
        "v_and_b32 v9, 0xffff0000, v192\n"
        "v_and_b32 v11, 0xffff0000, v193\n"
        "v_and_b32 v13, 0xffff0000, v194\n"
        "v_and_b32 v15, 0xffff0000, v195\n"
        "v_pk_mul_f32 v[8:9], v[8:9], s[50:51]\n"
        "v_pk_mul_f32 v[10:11], v[10:11], s[50:51]\n"
        "v_pk_mul_f32 v[12:13], v[12:13], s[50:51]\n"
        "v_pk_mul_f32 v[14:15], v[14:15], s[50:51]\n"
        "v_cvt_pk_bf16_f32 v8, v8, v9\n"
        "v_cvt_pk_bf16_f32 v10, v10, v11\n"
        "v_cvt_pk_bf16_f32 v12, v12, v13\n"
        "v_cvt_pk_bf16_f32 v14, v14, v15\n"
        "v_accvgpr_write_b32 a20, v8\n"
        "v_accvgpr_write_b32 a21, v10\n"
        "v_accvgpr_write_b32 a22, v12\n"
        "v_accvgpr_write_b32 a23, v14\n"
        "s_waitcnt lgkmcnt(11)\n"
        "v_lshlrev_b32 v0, 16, v212\n"
        "v_lshlrev_b32 v2, 16, v213\n"
        "s_waitcnt lgkmcnt(10)\n"
        "v_lshlrev_b32 v4, 16, v214\n"
        "v_lshlrev_b32 v6, 16, v215\n"
        "v_and_b32 v1, 0xffff0000, v212\n"
        "v_and_b32 v3, 0xffff0000, v213\n"
        "v_and_b32 v5, 0xffff0000, v214\n"
        "v_and_b32 v7, 0xffff0000, v215\n"
        "v_pk_mul_f32 v[0:1], v[0:1], s[50:51]\n"
        "v_pk_mul_f32 v[2:3], v[2:3], s[50:51]\n"
        "v_pk_mul_f32 v[4:5], v[4:5], s[50:51]\n"
        "v_pk_mul_f32 v[6:7], v[6:7], s[50:51]\n"
        "v_cvt_pk_bf16_f32 v0, v0, v1\n"
        "v_cvt_pk_bf16_f32 v2, v2, v3\n"
        "v_cvt_pk_bf16_f32 v4, v4, v5\n"
        "v_cvt_pk_bf16_f32 v6, v6, v7\n"
        "s_add_u32 m0, s40, 98304\n"
        "v_accvgpr_write_b32 a36, v0\n"
        "v_accvgpr_write_b32 a37, v2\n"
        "v_accvgpr_write_b32 a38, v4\n"
        "v_accvgpr_write_b32 a39, v6\n"
        "s_add_u32 s64, s65, 384\n"
        "s_waitcnt lgkmcnt(9)\n"
        "v_lshlrev_b32 v8, 16, v156\n"
        "v_lshlrev_b32 v10, 16, v157\n"
        "s_waitcnt lgkmcnt(8)\n"
        "v_lshlrev_b32 v12, 16, v158\n"
        "v_lshlrev_b32 v14, 16, v159\n"
        "buffer_load_dwordx4 v128, s[20:23], s64 offen lds\n"
        "v_and_b32 v9, 0xffff0000, v156\n"
        "v_and_b32 v11, 0xffff0000, v157\n"
        "v_and_b32 v13, 0xffff0000, v158\n"
        "v_and_b32 v15, 0xffff0000, v159\n"
        "v_pk_mul_f32 v[8:9], v[8:9], s[50:51]\n"
        "v_pk_mul_f32 v[10:11], v[10:11], s[50:51]\n"
        "v_pk_mul_f32 v[12:13], v[12:13], s[50:51]\n"
        "v_pk_mul_f32 v[14:15], v[14:15], s[50:51]\n"
        "v_cvt_pk_bf16_f32 v8, v8, v9\n"
        "v_cvt_pk_bf16_f32 v10, v10, v11\n"
        "v_cvt_pk_bf16_f32 v12, v12, v13\n"
        "v_cvt_pk_bf16_f32 v14, v14, v15\n"
        "v_accvgpr_write_b32 a40, v8\n"
        "v_accvgpr_write_b32 a41, v10\n"
        "v_accvgpr_write_b32 a42, v12\n"
        "v_accvgpr_write_b32 a43, v14\n"
        "buffer_load_dwordx4 v129, s[20:23], s64 offen offset:1024 lds\n"
        "s_waitcnt lgkmcnt(7)\n"
        "v_lshlrev_b32 v0, 16, v176\n"
        "v_lshlrev_b32 v2, 16, v177\n"
        "s_waitcnt lgkmcnt(6)\n"
        "v_lshlrev_b32 v4, 16, v178\n"
        "v_lshlrev_b32 v6, 16, v179\n"
        "v_and_b32 v1, 0xffff0000, v176\n"
        "v_and_b32 v3, 0xffff0000, v177\n"
        "v_and_b32 v5, 0xffff0000, v178\n"
        "v_and_b32 v7, 0xffff0000, v179\n"
        "v_pk_mul_f32 v[0:1], v[0:1], s[50:51]\n"
        "v_pk_mul_f32 v[2:3], v[2:3], s[50:51]\n"
        "v_pk_mul_f32 v[4:5], v[4:5], s[50:51]\n"
        "v_pk_mul_f32 v[6:7], v[6:7], s[50:51]\n"
        "v_cvt_pk_bf16_f32 v0, v0, v1\n"
        "v_cvt_pk_bf16_f32 v2, v2, v3\n"
        "v_cvt_pk_bf16_f32 v4, v4, v5\n"
        "v_cvt_pk_bf16_f32 v6, v6, v7\n"
        "buffer_load_dwordx4 v130, s[20:23], s64 offen offset:2048 lds\n"
        "v_accvgpr_write_b32 a44, v0\n"
        "v_accvgpr_write_b32 a45, v2\n"
        "v_accvgpr_write_b32 a46, v4\n"
        "v_accvgpr_write_b32 a47, v6\n"
        "s_waitcnt lgkmcnt(5)\n"
        "v_lshlrev_b32 v8, 16, v196\n"
        "v_lshlrev_b32 v10, 16, v197\n"
        "s_waitcnt lgkmcnt(4)\n"
        "v_lshlrev_b32 v12, 16, v198\n"
        "v_lshlrev_b32 v14, 16, v199\n"
        "s_add_u32 m0, s40, 110592\n"
        "v_and_b32 v9, 0xffff0000, v196\n"
        "v_and_b32 v11, 0xffff0000, v197\n"
        "v_and_b32 v13, 0xffff0000, v198\n"
        "v_and_b32 v15, 0xffff0000, v199\n"
        "s_add_u32 s64, s65, 37248\n"
        "v_pk_mul_f32 v[8:9], v[8:9], s[50:51]\n"
        "v_pk_mul_f32 v[10:11], v[10:11], s[50:51]\n"
        "v_pk_mul_f32 v[12:13], v[12:13], s[50:51]\n"
        "v_pk_mul_f32 v[14:15], v[14:15], s[50:51]\n"
        "buffer_load_dwordx4 v128, s[20:23], s64 offen lds\n"
        "v_cvt_pk_bf16_f32 v8, v8, v9\n"
        "v_cvt_pk_bf16_f32 v10, v10, v11\n"
        "v_cvt_pk_bf16_f32 v12, v12, v13\n"
        "v_cvt_pk_bf16_f32 v14, v14, v15\n"
        "v_accvgpr_write_b32 a60, v8\n"
        "v_accvgpr_write_b32 a61, v10\n"
        "v_accvgpr_write_b32 a62, v12\n"
        "v_accvgpr_write_b32 a63, v14\n"
        "s_waitcnt lgkmcnt(3)\n"
        "v_lshlrev_b32 v0, 16, v216\n"
        "v_lshlrev_b32 v2, 16, v217\n"
        "s_waitcnt lgkmcnt(2)\n"
        "v_lshlrev_b32 v4, 16, v218\n"
        "v_lshlrev_b32 v6, 16, v219\n"
        "v_and_b32 v1, 0xffff0000, v216\n"
        "v_and_b32 v3, 0xffff0000, v217\n"
        "v_and_b32 v5, 0xffff0000, v218\n"
        "v_and_b32 v7, 0xffff0000, v219\n"
        "buffer_load_dwordx4 v129, s[20:23], s64 offen offset:1024 lds\n"
        "v_pk_mul_f32 v[0:1], v[0:1], s[50:51]\n"
        "v_pk_mul_f32 v[2:3], v[2:3], s[50:51]\n"
        "v_pk_mul_f32 v[4:5], v[4:5], s[50:51]\n"
        "v_pk_mul_f32 v[6:7], v[6:7], s[50:51]\n"
        "v_cvt_pk_bf16_f32 v0, v0, v1\n"
        "v_cvt_pk_bf16_f32 v2, v2, v3\n"
        "v_cvt_pk_bf16_f32 v4, v4, v5\n"
        "v_cvt_pk_bf16_f32 v6, v6, v7\n"
        "v_accvgpr_write_b32 a64, v0\n"
        "v_accvgpr_write_b32 a65, v2\n"
        "v_accvgpr_write_b32 a66, v4\n"
        "v_accvgpr_write_b32 a67, v6\n"
        "s_waitcnt lgkmcnt(1)\n"
        "v_lshlrev_b32 v8, 16, v124\n"
        "v_lshlrev_b32 v10, 16, v125\n"
        "s_waitcnt lgkmcnt(0)\n"
        "v_lshlrev_b32 v12, 16, v126\n"
        "v_lshlrev_b32 v14, 16, v127\n"
        "buffer_load_dwordx4 v130, s[20:23], s64 offen offset:2048 lds\n"
        "v_and_b32 v9, 0xffff0000, v124\n"
        "v_and_b32 v11, 0xffff0000, v125\n"
        "v_and_b32 v13, 0xffff0000, v126\n"
        "v_and_b32 v15, 0xffff0000, v127\n"
        "v_pk_mul_f32 v[8:9], v[8:9], s[50:51]\n"
        "v_pk_mul_f32 v[10:11], v[10:11], s[50:51]\n"
        "v_pk_mul_f32 v[12:13], v[12:13], s[50:51]\n"
        "v_pk_mul_f32 v[14:15], v[14:15], s[50:51]\n"
        "s_add_u32 m0, s40, 122880\n"
        "v_cvt_pk_bf16_f32 v8, v8, v9\n"
        "v_cvt_pk_bf16_f32 v10, v10, v11\n"
        "v_cvt_pk_bf16_f32 v12, v12, v13\n"
        "v_cvt_pk_bf16_f32 v14, v14, v15\n"
        "s_add_u32 s64, s65, 74112\n"
        "v_accvgpr_write_b32 a68, v8\n"
        "v_accvgpr_write_b32 a69, v10\n"
        "v_accvgpr_write_b32 a70, v12\n"
        "v_accvgpr_write_b32 a71, v14\n"
        "buffer_load_dwordx4 v128, s[20:23], s64 offen lds\n"
        "buffer_load_dwordx4 v129, s[20:23], s64 offen offset:1024 lds\n"
        "buffer_load_dwordx4 v130, s[20:23], s64 offen offset:2048 lds\n"
        "s_waitcnt vmcnt(20)\n"
        "s_barrier\n"
        "ds_read_b128 a[224:227], v150 offset:0\n"
        "s_waitcnt lgkmcnt(0)\n"
        "v_mfma_f32_16x16x32_bf16 v[96:99], a[0:3], a[224:227], 0\n"
        "ds_read_b128 a[228:231], v151 offset:0\n"
        "ds_read_b128 a[232:235], v150 offset:128\n"
        "s_add_u32 m0, s40, 0\n"
        "s_add_u32 s64, s65, 110976\n"
        "s_waitcnt lgkmcnt(1)\n"
        "v_mfma_f32_16x16x32_bf16 v[96:99], a[4:7], a[228:231], v[96:99]\n"
        "s_waitcnt lgkmcnt(0)\n"
        "ds_read_b128 a[236:239], v151 offset:128\n"
        "ds_read_b128 a[240:243], v150 offset:256\n"
        "v_mfma_f32_16x16x32_bf16 v[96:99], a[8:11], a[232:235], v[96:99]\n"
        "s_waitcnt lgkmcnt(1)\n"
        "ds_read_b128 a[244:247], v151 offset:256\n"
        "buffer_load_dwordx4 v128, s[20:23], s64 offen lds\n"
        "v_mfma_f32_16x16x32_bf16 v[96:99], a[12:15], a[236:239], v[96:99]\n"
        "s_waitcnt lgkmcnt(1)\n"
        "ds_read_b128 a[248:251], v150 offset:1536\n"
        "ds_read_b128 a[216:219], v151 offset:1536\n"
        "v_mfma_f32_16x16x32_bf16 v[96:99], a[16:19], a[240:243], v[96:99]\n"
        "s_waitcnt lgkmcnt(2)\n"
        "ds_read_b128 a[220:223], v150 offset:1664\n"
        "v_mfma_f32_16x16x32_bf16 v[96:99], a[20:23], a[244:247], v[96:99]\n"
        "buffer_load_dwordx4 v129, s[20:23], s64 offen offset:1024 lds\n"
        "v_mfma_f32_16x16x32_bf16 v[100:103], a[24:27], a[224:227], 0\n"
        "v_mfma_f32_16x16x32_bf16 v[100:103], a[28:31], a[228:231], v[100:103]\n"
        "v_mfma_f32_16x16x32_bf16 v[100:103], a[32:35], a[232:235], v[100:103]\n"
        "v_mfma_f32_16x16x32_bf16 v[100:103], a[36:39], a[236:239], v[100:103]\n"
        "buffer_load_dwordx4 v130, s[20:23], s64 offen offset:2048 lds\n"
        "v_mfma_f32_16x16x32_bf16 v[100:103], a[40:43], a[240:243], v[100:103]\n"
        "s_waitcnt vmcnt(19)\n"
        "v_lshlrev_b32 v236, 16, v236\n"
        "v_cvt_pk_bf16_f32 v156, v96, v97\n"
        "v_mfma_f32_16x16x32_bf16 v[100:103], a[44:47], a[244:247], v[100:103]\n"
        "s_add_u32 m0, s40, 12288\n"
        "ds_write_b32 v146, v236 offset:0\n"
        "v_mfma_f32_16x16x32_bf16 v[112:115], a[48:51], a[224:227], 0\n"
        "ds_read_b128 a[224:227], v151 offset:1664\n"
        "s_add_u32 s64, s65, 147840\n"
        "v_cvt_pk_bf16_f32 v157, v98, v99\n"
        "s_waitcnt vmcnt(18)\n"
        "v_lshlrev_b32 v237, 16, v237\n"
        "v_mfma_f32_16x16x32_bf16 v[112:115], a[52:55], a[228:231], v[112:115]\n"
        "ds_read_b128 a[228:231], v150 offset:1792\n"
        "buffer_load_dwordx4 v128, s[20:23], s64 offen lds\n"
        "v_cvt_pk_bf16_f32 v158, v100, v101\n"
        "v_mfma_f32_16x16x32_bf16 v[112:115], a[56:59], a[232:235], v[112:115]\n"
        "ds_read_b128 a[232:235], v151 offset:1792\n"
        "ds_write_b32 v146, v237 offset:256\n"
        "v_mfma_f32_16x16x32_bf16 v[112:115], a[60:63], a[236:239], v[112:115]\n"
        "v_cvt_pk_bf16_f32 v159, v102, v103\n"
        "s_waitcnt vmcnt(18)\n"
        "v_lshlrev_b32 v238, 16, v238\n"
        "s_waitcnt vmcnt(17)\n"
        "v_lshlrev_b32 v239, 16, v239\n"
        "v_mfma_f32_16x16x32_bf16 v[112:115], a[64:67], a[240:243], v[112:115]\n"
        "v_mfma_f32_16x16x32_bf16 v[112:115], a[68:71], a[244:247], v[112:115]\n"
        "s_waitcnt lgkmcnt(7)\n"
        "ds_write_b32 v146, v238 offset:512\n"
        "buffer_load_dwordx4 v129, s[20:23], s64 offen offset:1024 lds\n"
        "v_mfma_f32_16x16x32_bf16 v[96:99], a[0:3], a[248:251], 0\n"
        "s_waitcnt lgkmcnt(7)\n"
        "ds_write_b32 v146, v239 offset:768\n"
        "s_add_u32 s35, s47, 786432\n"
        "s_sub_u32 s46, s35, 100663296\n"
        "s_cmp_lt_u32 s46, 786432\n"
        "s_waitcnt vmcnt(5)\n"
        "s_barrier\n"
        "v_mfma_f32_16x16x32_bf16 v[96:99], a[4:7], a[216:219], v[96:99]\n"
        "s_waitcnt lgkmcnt(7)\n"
        "s_cselect_b32 s35, s46, s35\n"
        "s_add_u32 s41, s30, s40\n"
        "ds_read_b64_tr_b16 a[72:73], v136 offset:0\n"
        "ds_read_b64_tr_b16 a[74:75], v136 offset:1536\n"
        "v_mfma_f32_16x16x32_bf16 v[96:99], a[8:11], a[220:223], v[96:99]\n"
        "s_waitcnt lgkmcnt(7)\n"
        "ds_read_b64_tr_b16 a[76:77], v136 offset:12288\n"
        "ds_read_b64_tr_b16 a[78:79], v136 offset:13824\n"
        "v_cvt_pk_bf16_f32 v160, v112, v113\n"
        "v_mfma_f32_16x16x32_bf16 v[96:99], a[12:15], a[224:227], v[96:99]\n"
        "s_waitcnt lgkmcnt(8)\n"
        "buffer_load_dwordx4 v130, s[20:23], s64 offen offset:2048 lds\n"
        "ds_read_b64_tr_b16 a[80:81], v136 offset:24576\n"
        "v_cvt_pk_bf16_f32 v161, v114, v115\n"
        "v_mfma_f32_16x16x32_bf16 v[96:99], a[16:19], a[228:231], v[96:99]\n"
        "s_waitcnt lgkmcnt(8)\n"
        "ds_read_b64_tr_b16 a[82:83], v136 offset:26112\n"
        "ds_read_b64_tr_b16 a[96:97], v137 offset:0\n"
        "v_cvt_pk_bf16_f32 v166, v112, v113\n"
        "v_mfma_f32_16x16x32_bf16 v[96:99], a[20:23], a[232:235], v[96:99]\n"
        "s_add_u32 m0, s40, 24576\n"
        "ds_read_b64_tr_b16 a[98:99], v137 offset:1536\n"
        "ds_read_b128 a[236:239], v150 offset:12288\n"
        "v_cvt_pk_bf16_f32 v167, v114, v115\n"
        "v_mfma_f32_16x16x32_bf16 v[100:103], a[24:27], a[248:251], 0\n"
        "s_add_u32 s64, s65, 184704\n"
        "ds_read_b64_tr_b16 a[100:101], v137 offset:12288\n"
        "ds_read_b128 a[240:243], v151 offset:12288\n"
        "v_cvt_pk_bf16_f32 v176, v96, v97\n"
        "v_mfma_f32_16x16x32_bf16 v[100:103], a[28:31], a[216:219], v[100:103]\n"
        "buffer_load_dwordx4 v128, s[20:23], s64 offen lds\n"
        "ds_read_b64_tr_b16 a[102:103], v137 offset:13824\n"
        "v_cvt_pk_bf16_f32 v177, v98, v99\n"
        "v_mfma_f32_16x16x32_bf16 v[100:103], a[32:35], a[220:223], v[100:103]\n"
        "ds_read_b128 v[0:3], v232 offset:0\n"
        "ds_read_b128 a[244:247], v150 offset:12416\n"
        "v_mfma_f32_16x16x32_bf16 v[100:103], a[36:39], a[224:227], v[100:103]\n"
        "ds_read_b64_tr_b16 a[104:105], v137 offset:24576\n"
        "ds_read_b64_tr_b16 a[106:107], v137 offset:26112\n"
        "v_mfma_f32_16x16x32_bf16 v[100:103], a[40:43], a[228:231], v[100:103]\n"
        "ds_read_b64_tr_b16 a[120:121], v138 offset:0\n"
        "ds_read_b64_tr_b16 a[122:123], v138 offset:1536\n"
        "s_waitcnt lgkmcnt(5)\n"
        "v_lshl_add_u32 v0, v0, 2, s67\n"
        "v_mfma_f32_16x16x32_bf16 v[100:103], a[44:47], a[232:235], v[100:103]\n"
        "buffer_load_dwordx4 v129, s[20:23], s64 offen offset:1024 lds\n"
        "ds_read_b64_tr_b16 a[124:125], v138 offset:12288\n"
        "v_lshl_add_u32 v1, v1, 2, s67\n"
        "v_mfma_f32_16x16x32_bf16 v[112:115], a[48:51], a[248:251], 0\n"
        "ds_read_b128 a[248:251], v151 offset:12416\n"
        "ds_read_b128 v[16:19], v232 offset:1024\n"
        "v_lshl_add_u32 v2, v2, 2, s67\n"
        "v_mfma_f32_16x16x32_bf16 v[112:115], a[52:55], a[216:219], v[112:115]\n"
        "ds_read_b128 a[216:219], v150 offset:12544\n"
        "ds_read_b64_tr_b16 a[126:127], v138 offset:13824\n"
        "v_cvt_pk_bf16_f32 v178, v100, v101\n"
        "v_mfma_f32_16x16x32_bf16 v[112:115], a[56:59], a[220:223], v[112:115]\n"
        "ds_read_b128 a[220:223], v151 offset:12544\n"
        "ds_read_b64_tr_b16 a[128:129], v138 offset:24576\n"
        "v_cvt_pk_bf16_f32 v179, v102, v103\n"
        "v_mfma_f32_16x16x32_bf16 v[112:115], a[60:63], a[224:227], v[112:115]\n"
        "buffer_load_dwordx4 v130, s[20:23], s64 offen offset:2048 lds\n"
        "ds_read_b64_tr_b16 a[130:131], v138 offset:26112\n"
        "v_lshl_add_u32 v3, v3, 2, s67\n"
        "v_mfma_f32_16x16x32_bf16 v[112:115], a[64:67], a[228:231], v[112:115]\n"
        "ds_read_b128 v[32:35], v232 offset:8192\n"
        "ds_read_b128 v[48:51], v232 offset:9216\n"
        "s_waitcnt lgkmcnt(7)\n"
        "v_lshl_add_u32 v16, v16, 2, s67\n"
        "v_mfma_f32_16x16x32_bf16 v[112:115], a[68:71], a[232:235], v[112:115]\n"
        "ds_read_b128 a[224:227], v150 offset:13824\n"
        "ds_read_b128 a[228:231], v151 offset:13824\n"
        "v_xor_b32 v253, 32, v249\n"
        "v_mfma_f32_16x16x32_bf16 v[96:99], a[0:3], a[236:239], 0\n"
        "ds_read_b128 a[232:235], v150 offset:13952\n"
        "ds_read_b32 v0, v0\n"
        "v_lshl_add_u32 v17, v17, 2, s67\n"
        "v_mfma_f32_16x16x32_bf16 v[96:99], a[4:7], a[240:243], v[96:99]\n"
        "s_xor_b32 m0, s48, 8192\n"
        "ds_read_b32 v1, v1\n"
        "ds_read_b32 v2, v2\n"
        "v_cvt_pk_bf16_f32 v180, v112, v113\n"
        "v_mfma_f32_16x16x32_bf16 v[96:99], a[8:11], a[244:247], v[96:99]\n"
        "s_sub_u32 s64, s36, 2097152\n"
        "ds_read_b128 v[4:7], v233 offset:0\n"
        "ds_read_b32 v3, v3\n"
        "v_cvt_pk_bf16_f32 v181, v114, v115\n"
        "v_mfma_f32_16x16x32_bf16 v[96:99], a[12:15], a[248:251], v[96:99]\n"
        "s_waitcnt lgkmcnt(14)\n"
        "buffer_load_dwordx4 v249, s[12:15], s64 offen lds\n"
        "ds_read_b32 v16, v16\n"
        "v_cvt_pk_bf16_f32 v186, v112, v113\n"
        "v_mfma_f32_16x16x32_bf16 v[96:99], a[16:19], a[216:219], v[96:99]\n"
        "s_waitcnt lgkmcnt(13)\n"
        "ds_read_b32 v17, v17\n"
        "v_cvt_pk_bf16_f32 v187, v114, v115\n"
        "v_lshl_add_u32 v18, v18, 2, s67\n"
        "v_mfma_f32_16x16x32_bf16 v[96:99], a[20:23], a[220:223], v[96:99]\n"
        "ds_read_b32 v18, v18\n"
        "v_lshl_add_u32 v19, v19, 2, s67\n"
        "s_waitcnt lgkmcnt(12)\n"
        "v_lshl_add_u32 v32, v32, 2, s67\n"
        "v_mfma_f32_16x16x32_bf16 v[100:103], a[24:27], a[236:239], 0\n"
        "s_barrier\n"
        "ds_read_b32 v19, v19\n"
        "ds_read_b32 v32, v32\n"
        "v_lshl_add_u32 v33, v33, 2, s67\n"
        "v_mfma_f32_16x16x32_bf16 v[100:103], a[28:31], a[240:243], v[100:103]\n"
        "buffer_load_dwordx4 v253, s[12:15], s64 offen offset:1024 lds\n"
        "ds_read_b128 v[20:23], v233 offset:1024\n"
        "v_cvt_pk_bf16_f32 v196, v96, v97\n"
        "v_mfma_f32_16x16x32_bf16 v[100:103], a[32:35], a[244:247], v[100:103]\n"
        "ds_read_b32 v33, v33\n"
        "v_cvt_pk_bf16_f32 v197, v98, v99\n"
        "v_lshl_add_u32 v34, v34, 2, s67\n"
        "v_mfma_f32_16x16x32_bf16 v[100:103], a[36:39], a[248:251], v[100:103]\n"
        "ds_read_b32 v34, v34\n"
        "v_lshl_add_u32 v35, v35, 2, s67\n"
        "s_waitcnt lgkmcnt(15)\n"
        "v_lshl_add_u32 v48, v48, 2, s67\n"
        "v_mfma_f32_16x16x32_bf16 v[100:103], a[40:43], a[216:219], v[100:103]\n"
        "s_add_u32 m0, s41, 0\n"
        "ds_read_b32 v35, v35\n"
        "ds_read_b32 v48, v48\n"
        "v_lshl_add_u32 v49, v49, 2, s67\n"
        "v_mfma_f32_16x16x32_bf16 v[100:103], a[44:47], a[220:223], v[100:103]\n"
        "buffer_load_dwordx4 v242, s[8:11], s47 offen lds\n"
        "ds_read_b32 v49, v49\n"
        "v_mfma_f32_16x16x32_bf16 v[112:115], a[48:51], a[236:239], 0\n"
        "ds_read_b128 a[236:239], v151 offset:13952\n"
        "ds_read_b128 v[36:39], v233 offset:8192\n"
        "v_lshl_add_u32 v50, v50, 2, s67\n"
        "v_mfma_f32_16x16x32_bf16 v[112:115], a[52:55], a[240:243], v[112:115]\n"
        "ds_read_b128 a[240:243], v150 offset:14080\n"
        "ds_read_b32 v50, v50\n"
        "v_cvt_pk_bf16_f32 v198, v100, v101\n"
        "v_mfma_f32_16x16x32_bf16 v[112:115], a[56:59], a[244:247], v[112:115]\n"
        "ds_read_b128 a[244:247], v151 offset:14080\n"
        "v_cvt_pk_bf16_f32 v199, v102, v103\n"
        "v_lshl_add_u32 v51, v51, 2, s67\n"
        "v_mfma_f32_16x16x32_bf16 v[112:115], a[60:63], a[248:251], v[112:115]\n"
        "s_add_u32 m0, s41, 1024\n"
        "ds_read_b128 a[248:251], v150 offset:0\n"
        "ds_read_b32 v51, v51\n"
        "s_waitcnt lgkmcnt(15)\n"
        "v_lshl_add_u32 v4, v4, 2, s67\n"
        "v_mfma_f32_16x16x32_bf16 v[112:115], a[64:67], a[216:219], v[112:115]\n"
        "buffer_load_dwordx4 v243, s[8:11], s47 offen lds\n"
        "ds_read_b128 a[216:219], v151 offset:0\n"
        "v_mfma_f32_16x16x32_bf16 v[112:115], a[68:71], a[220:223], v[112:115]\n"
        "ds_read_b128 v[52:55], v233 offset:9216\n"
        "ds_read_b128 a[220:223], v150 offset:128\n"
        "v_lshl_add_u32 v5, v5, 2, s67\n"
        "v_mfma_f32_16x16x32_bf16 v[96:99], a[0:3], a[224:227], 0\n"
        "ds_read_b32 v4, v4\n"
        "ds_read_b32 v5, v5\n"
        "v_lshl_add_u32 v6, v6, 2, s67\n"
        "v_mfma_f32_16x16x32_bf16 v[96:99], a[4:7], a[228:231], v[96:99]\n"
        "ds_read_b32 v6, v6\n"
        "v_cvt_pk_bf16_f32 v200, v112, v113\n"
        "v_cvt_pk_bf16_f32 v201, v114, v115\n"
        "v_mfma_f32_16x16x32_bf16 v[96:99], a[8:11], a[232:235], v[96:99]\n"
        "s_waitcnt lgkmcnt(12)\n"
        "s_add_u32 m0, s41, 2048\n"
        "v_cvt_pk_bf16_f32 v206, v112, v113\n"
        "v_cvt_pk_bf16_f32 v207, v114, v115\n"
        "v_lshl_add_u32 v7, v7, 2, s67\n"
        "v_mfma_f32_16x16x32_bf16 v[96:99], a[12:15], a[236:239], v[96:99]\n"
        "s_waitcnt lgkmcnt(10)\n"
        "buffer_load_dwordx4 v244, s[8:11], s47 offen lds\n"
        "ds_read_b32 v7, v7\n"
        "v_mfma_f32_16x16x32_bf16 v[96:99], a[16:19], a[240:243], v[96:99]\n"
        "s_waitcnt lgkmcnt(9)\n"
        "ds_read_b128 v[8:11], v234 offset:0\n"
        "v_lshl_add_u32 v20, v20, 2, s67\n"
        "v_lshl_add_u32 v21, v21, 2, s67\n"
        "v_mfma_f32_16x16x32_bf16 v[96:99], a[20:23], a[244:247], v[96:99]\n"
        "ds_read_b32 v20, v20\n"
        "ds_read_b32 v21, v21\n"
        "v_lshl_add_u32 v22, v22, 2, s67\n"
        "v_mfma_f32_16x16x32_bf16 v[100:103], a[24:27], a[224:227], 0\n"
        "s_waitcnt vmcnt(5)\n"
        "s_barrier\n"
        "ds_read_b32 v22, v22\n"
        "v_lshl_add_u32 v23, v23, 2, s67\n"
        "v_lshl_add_u32 v36, v36, 2, s67\n"
        "v_mfma_f32_16x16x32_bf16 v[100:103], a[28:31], a[228:231], v[100:103]\n"
        "s_add_u32 m0, s41, 12288\n"
        "ds_read_b64_tr_b16 a[84:85], v131 offset:0\n"
        "ds_read_b64_tr_b16 a[86:87], v131 offset:1536\n"
        "ds_read_b64_tr_b16 a[88:89], v131 offset:12288\n"
        "v_mfma_f32_16x16x32_bf16 v[100:103], a[32:35], a[232:235], v[100:103]\n"
        "ds_read_b64_tr_b16 a[90:91], v131 offset:13824\n"
        "ds_read_b64_tr_b16 a[92:93], v131 offset:24576\n"
        "ds_read_b64_tr_b16 a[94:95], v131 offset:26112\n"
        "s_add_u32 s47, s47, 6291456\n"
        "v_mfma_f32_16x16x32_bf16 v[100:103], a[36:39], a[236:239], v[100:103]\n"
        "ds_read_b64_tr_b16 a[108:109], v132 offset:0\n"
        "ds_read_b64_tr_b16 a[110:111], v132 offset:1536\n"
        "ds_read_b64_tr_b16 a[112:113], v132 offset:12288\n"
        "s_sub_u32 s31, s30, s28\n"
        "v_mfma_f32_16x16x32_bf16 v[100:103], a[40:43], a[240:243], v[100:103]\n"
        "ds_read_b64_tr_b16 a[114:115], v132 offset:13824\n"
        "ds_read_b64_tr_b16 a[116:117], v132 offset:24576\n"
        "ds_read_b64_tr_b16 a[118:119], v132 offset:26112\n"
        "s_sub_u32 s45, s28, s29\n"
        "v_mfma_f32_16x16x32_bf16 v[100:103], a[44:47], a[244:247], v[100:103]\n"
        "ds_read_b64_tr_b16 a[132:133], v133 offset:0\n"
        "ds_read_b64_tr_b16 a[134:135], v133 offset:1536\n"
        "ds_read_b64_tr_b16 a[136:137], v133 offset:12288\n"
        "s_add_u32 s28, s28, s31\n"
        "v_mfma_f32_16x16x32_bf16 v[112:115], a[48:51], a[224:227], 0\n"
        "ds_read_b64_tr_b16 a[138:139], v133 offset:13824\n"
        "ds_read_b64_tr_b16 a[140:141], v133 offset:24576\n"
        "ds_read_b64_tr_b16 a[142:143], v133 offset:26112\n"
        "s_mov_b32 s30, s29\n"
        "v_mfma_f32_16x16x32_bf16 v[112:115], a[52:55], a[228:231], v[112:115]\n"
        "buffer_load_dwordx4 v242, s[8:11], s35 offen lds\n"
        "ds_read_b128 a[224:227], v151 offset:128\n"
        "s_add_u32 m0, s41, 13312\n"
        "v_mfma_f32_16x16x32_bf16 v[112:115], a[56:59], a[232:235], v[112:115]\n"
        "ds_read_b128 a[228:231], v150 offset:256\n"
        "buffer_load_dwordx4 v243, s[8:11], s35 offen lds\n"
        "s_add_u32 s29, s29, s45\n"
        "v_mfma_f32_16x16x32_bf16 v[112:115], a[60:63], a[236:239], v[112:115]\n"
        "ds_read_b128 a[232:235], v151 offset:256\n"
        "ds_read_b128 v[24:27], v234 offset:1024\n"
        "v_cvt_pk_bf16_f32 v216, v96, v97\n"
        "v_mfma_f32_16x16x32_bf16 v[112:115], a[64:67], a[240:243], v[112:115]\n"
        "ds_read_b32 v23, v23\n"
        "ds_read_b128 v[40:43], v234 offset:8192\n"
        "v_cvt_pk_bf16_f32 v217, v98, v99\n"
        "v_mfma_f32_16x16x32_bf16 v[112:115], a[68:71], a[244:247], v[112:115]\n"
        "s_waitcnt lgkmcnt(15)\n"
        "s_add_u32 m0, s41, 14336\n"
        "ds_read_b32 v36, v36\n"
        "ds_read_b128 a[236:239], v150 offset:1536\n"
        "v_cvt_pk_bf16_f32 v218, v100, v101\n"
        "v_mfma_f32_16x16x32_bf16 v[96:99], a[72:75], a[248:251], 0\n"
        "s_waitcnt lgkmcnt(15)\n"
        "buffer_load_dwordx4 v244, s[8:11], s35 offen lds\n"
        "ds_read_b128 a[240:243], v151 offset:1536\n"
        "v_mfma_f32_16x16x32_bf16 v[96:99], a[76:79], a[216:219], v[96:99]\n"
        "s_waitcnt lgkmcnt(15)\n"
        "ds_read_b128 a[244:247], v150 offset:1664\n"
        "ds_read_b128 v[56:59], v234 offset:9216\n"
        "v_cvt_pk_bf16_f32 v219, v102, v103\n"
        "v_mfma_f32_16x16x32_bf16 v[96:99], a[80:83], a[220:223], v[96:99]\n"
        "s_waitcnt lgkmcnt(10)\n"
        "v_cvt_pk_bf16_f32 v220, v112, v113\n"
        "v_cvt_pk_bf16_f32 v221, v114, v115\n"
        "v_cvt_pk_bf16_f32 v226, v112, v113\n"
        "v_mfma_f32_16x16x32_bf16 v[96:99], a[84:87], a[224:227], v[96:99]\n"
        "s_waitcnt lgkmcnt(9)\n"
        "s_add_u32 m0, s40, 36864\n"
        "v_cvt_pk_bf16_f32 v227, v114, v115\n"
        "v_lshl_add_u32 v37, v37, 2, s67\n"
        "v_lshl_add_u32 v38, v38, 2, s67\n"
        "v_mfma_f32_16x16x32_bf16 v[96:99], a[88:91], a[228:231], v[96:99]\n"
        "s_waitcnt lgkmcnt(8)\n"
        "s_add_u32 s64, s65, 768\n"
        "ds_read_b32 v37, v37\n"
        "ds_read_b32 v38, v38\n"
        "v_lshl_add_u32 v39, v39, 2, s67\n"
        "v_mfma_f32_16x16x32_bf16 v[96:99], a[92:95], a[232:235], v[96:99]\n"
        "buffer_load_dwordx4 v128, s[20:23], s64 offen lds\n"
        "ds_read_b32 v39, v39\n"
        "v_lshl_add_u32 v52, v52, 2, s67\n"
        "v_mfma_f32_16x16x32_bf16 v[100:103], a[96:99], a[248:251], 0\n"
        "ds_read_b32 v52, v52\n"
        "ds_read_b128 v[12:15], v235 offset:0\n"
        "v_lshl_add_u32 v53, v53, 2, s67\n"
        "v_mfma_f32_16x16x32_bf16 v[100:103], a[100:103], a[216:219], v[100:103]\n"
        "ds_read_b32 v53, v53\n"
        "v_cvt_pk_bf16_f32 v152, v96, v97\n"
        "v_cvt_pk_bf16_f32 v153, v98, v99\n"
        "v_mfma_f32_16x16x32_bf16 v[100:103], a[104:107], a[220:223], v[100:103]\n"
        "v_lshl_add_u32 v54, v54, 2, s67\n"
        "v_lshl_add_u32 v55, v55, 2, s67\n"
        "v_lshl_add_u32 v8, v8, 2, s67\n"
        "v_mfma_f32_16x16x32_bf16 v[100:103], a[108:111], a[224:227], v[100:103]\n"
        "ds_read_b32 v54, v54\n"
        "ds_read_b32 v55, v55\n"
        "v_lshl_add_u32 v9, v9, 2, s67\n"
        "v_mfma_f32_16x16x32_bf16 v[100:103], a[112:115], a[228:231], v[100:103]\n"
        "buffer_load_dwordx4 v129, s[20:23], s64 offen offset:1024 lds\n"
        "ds_read_b32 v8, v8\n"
        "v_lshl_add_u32 v10, v10, 2, s67\n"
        "v_mfma_f32_16x16x32_bf16 v[100:103], a[116:119], a[232:235], v[100:103]\n"
        "ds_read_b32 v9, v9\n"
        "ds_read_b32 v10, v10\n"
        "v_lshl_add_u32 v11, v11, 2, s67\n"
        "v_mfma_f32_16x16x32_bf16 v[112:115], a[120:123], a[248:251], 0\n"
        "ds_read_b128 a[248:251], v151 offset:1664\n"
        "ds_read_b32 v11, v11\n"
        "s_waitcnt lgkmcnt(15)\n"
        "v_lshl_add_u32 v24, v24, 2, s67\n"
        "v_mfma_f32_16x16x32_bf16 v[112:115], a[124:127], a[216:219], v[112:115]\n"
        "ds_read_b128 a[216:219], v150 offset:1792\n"
        "ds_read_b128 v[28:31], v235 offset:1024\n"
        "v_cvt_pk_bf16_f32 v154, v100, v101\n"
        "v_mfma_f32_16x16x32_bf16 v[112:115], a[128:131], a[220:223], v[112:115]\n"
        "ds_read_b128 a[220:223], v151 offset:1792\n"
        "ds_read_b32 v24, v24\n"
        "v_cvt_pk_bf16_f32 v155, v102, v103\n"
        "v_mfma_f32_16x16x32_bf16 v[112:115], a[132:135], a[224:227], v[112:115]\n"
        "buffer_load_dwordx4 v130, s[20:23], s64 offen offset:2048 lds\n"
        "ds_read_b128 a[224:227], v150 offset:12288\n"
        "v_lshl_add_u32 v25, v25, 2, s67\n"
        "v_mfma_f32_16x16x32_bf16 v[112:115], a[136:139], a[228:231], v[112:115]\n"
        "ds_read_b32 v25, v25\n"
        "ds_read_b128 v[44:47], v235 offset:8192\n"
        "v_lshl_add_u32 v26, v26, 2, s67\n"
        "v_mfma_f32_16x16x32_bf16 v[112:115], a[140:143], a[232:235], v[112:115]\n"
        "ds_read_b32 v26, v26\n"
        "ds_read_b128 a[228:231], v151 offset:12288\n"
        "v_lshl_add_u32 v27, v27, 2, s67\n"
        "v_mfma_f32_16x16x32_bf16 v[96:99], a[72:75], a[236:239], 0\n"
        "s_add_u32 m0, s40, 49152\n"
        "ds_read_b32 v27, v27\n"
        "ds_read_b128 a[232:235], v150 offset:12416\n"
        "v_lshl_add_u32 v40, v40, 2, s67\n"
        "v_mfma_f32_16x16x32_bf16 v[96:99], a[76:79], a[240:243], v[96:99]\n"
        "s_waitcnt lgkmcnt(15)\n"
        "s_add_u32 s64, s65, 37632\n"
        "ds_read_b32 v40, v40\n"
        "v_cvt_pk_bf16_f32 v168, v112, v113\n"
        "v_cvt_pk_bf16_f32 v169, v114, v115\n"
        "v_mfma_f32_16x16x32_bf16 v[96:99], a[80:83], a[244:247], v[96:99]\n"
        "s_waitcnt lgkmcnt(13)\n"
        "buffer_load_dwordx4 v128, s[20:23], s64 offen lds\n"
        "v_lshl_add_u32 v41, v41, 2, s67\n"
        "v_lshl_add_u32 v42, v42, 2, s67\n"
        "v_mfma_f32_16x16x32_bf16 v[96:99], a[84:87], a[248:251], v[96:99]\n"
        "s_waitcnt lgkmcnt(11)\n"
        "ds_read_b32 v41, v41\n"
        "ds_read_b32 v42, v42\n"
        "v_lshl_add_u32 v43, v43, 2, s67\n"
        "v_mfma_f32_16x16x32_bf16 v[96:99], a[88:91], a[216:219], v[96:99]\n"
        "s_waitcnt lgkmcnt(11)\n"
        "ds_read_b32 v43, v43\n"
        "ds_read_b128 v[60:63], v235 offset:9216\n"
        "v_lshl_add_u32 v56, v56, 2, s67\n"
        "v_mfma_f32_16x16x32_bf16 v[96:99], a[92:95], a[220:223], v[96:99]\n"
        "ds_read_b32 v56, v56\n"
        "v_lshl_add_u32 v57, v57, 2, s67\n"
        "v_lshl_add_u32 v58, v58, 2, s67\n"
        "v_mfma_f32_16x16x32_bf16 v[100:103], a[96:99], a[236:239], 0\n"
        "ds_read_b32 v57, v57\n"
        "ds_read_b32 v58, v58\n"
        "v_lshl_add_u32 v59, v59, 2, s67\n"
        "v_mfma_f32_16x16x32_bf16 v[100:103], a[100:103], a[240:243], v[100:103]\n"
        "buffer_load_dwordx4 v129, s[20:23], s64 offen offset:1024 lds\n"
        "ds_read_b32 v59, v59\n"
        "v_cvt_pk_bf16_f32 v172, v96, v97\n"
        "v_mfma_f32_16x16x32_bf16 v[100:103], a[104:107], a[244:247], v[100:103]\n"
        "v_cvt_pk_bf16_f32 v173, v98, v99\n"
        "v_lshl_add_u32 v12, v12, 2, s67\n"
        "v_lshl_add_u32 v13, v13, 2, s67\n"
        "v_mfma_f32_16x16x32_bf16 v[100:103], a[108:111], a[248:251], v[100:103]\n"
        "ds_read_b32 v12, v12\n"
        "ds_read_b32 v13, v13\n"
        "v_lshl_add_u32 v14, v14, 2, s67\n"
        "v_mfma_f32_16x16x32_bf16 v[100:103], a[112:115], a[216:219], v[100:103]\n"
        "ds_read_b32 v14, v14\n"
        "v_lshl_add_u32 v15, v15, 2, s67\n"
        "v_lshl_add_u32 v28, v28, 2, s67\n"
        "v_mfma_f32_16x16x32_bf16 v[100:103], a[116:119], a[220:223], v[100:103]\n"
        "ds_read_b32 v15, v15\n"
        "ds_read_b32 v28, v28\n"
        "v_lshl_add_u32 v29, v29, 2, s67\n"
        "v_mfma_f32_16x16x32_bf16 v[112:115], a[120:123], a[236:239], 0\n"
        "ds_read_b128 a[236:239], v151 offset:12416\n"
        "buffer_load_dwordx4 v130, s[20:23], s64 offen offset:2048 lds\n"
        "v_lshl_add_u32 v30, v30, 2, s67\n"
        "v_mfma_f32_16x16x32_bf16 v[112:115], a[124:127], a[240:243], v[112:115]\n"
        "ds_read_b128 a[240:243], v150 offset:12544\n"
        "ds_read_b32 v29, v29\n"
        "v_cvt_pk_bf16_f32 v174, v100, v101\n"
        "v_mfma_f32_16x16x32_bf16 v[112:115], a[128:131], a[244:247], v[112:115]\n"
        "ds_read_b128 a[244:247], v151 offset:12544\n"
        "ds_read_b32 v30, v30\n"
        "v_cvt_pk_bf16_f32 v175, v102, v103\n"
        "v_mfma_f32_16x16x32_bf16 v[112:115], a[132:135], a[248:251], v[112:115]\n"
        "s_add_u32 m0, s40, 61440\n"
        "ds_read_b128 a[248:251], v150 offset:13824\n"
        "v_lshl_add_u32 v31, v31, 2, s67\n"
        "s_waitcnt lgkmcnt(15)\n"
        "v_lshl_add_u32 v44, v44, 2, s67\n"
        "v_mfma_f32_16x16x32_bf16 v[112:115], a[136:139], a[216:219], v[112:115]\n"
        "s_add_u32 s64, s65, 74496\n"
        "ds_read_b32 v31, v31\n"
        "ds_read_b128 a[216:219], v151 offset:13824\n"
        "v_lshl_add_u32 v45, v45, 2, s67\n"
        "v_mfma_f32_16x16x32_bf16 v[112:115], a[140:143], a[220:223], v[112:115]\n"
        "buffer_load_dwordx4 v128, s[20:23], s64 offen lds\n"
        "ds_read_b128 a[220:223], v150 offset:13952\n"
        "v_lshl_add_u32 v46, v46, 2, s67\n"
        "v_mfma_f32_16x16x32_bf16 v[96:99], a[72:75], a[224:227], 0\n"
        "ds_read_b32 v44, v44\n"
        "ds_read_b32 v45, v45\n"
        "v_lshl_add_u32 v47, v47, 2, s67\n"
        "v_mfma_f32_16x16x32_bf16 v[96:99], a[76:79], a[228:231], v[96:99]\n"
        "ds_read_b32 v46, v46\n"
        "ds_read_b32 v47, v47\n"
        "v_cvt_pk_bf16_f32 v190, v112, v113\n"
        "v_mfma_f32_16x16x32_bf16 v[96:99], a[80:83], a[232:235], v[96:99]\n"
        "s_waitcnt lgkmcnt(12)\n"
        "v_cvt_pk_bf16_f32 v191, v114, v115\n"
        "v_lshl_add_u32 v60, v60, 2, s67\n"
        "v_lshl_add_u32 v61, v61, 2, s67\n"
        "v_mfma_f32_16x16x32_bf16 v[96:99], a[84:87], a[236:239], v[96:99]\n"
        "s_waitcnt lgkmcnt(11)\n"
        "ds_read_b32 v60, v60\n"
        "ds_read_b32 v61, v61\n"
        "v_lshl_add_u32 v62, v62, 2, s67\n"
        "v_mfma_f32_16x16x32_bf16 v[96:99], a[88:91], a[240:243], v[96:99]\n"
        "s_waitcnt lgkmcnt(11)\n"
        "buffer_load_dwordx4 v129, s[20:23], s64 offen offset:1024 lds\n"
        "ds_read_b32 v62, v62\n"
        "v_lshl_add_u32 v63, v63, 2, s67\n"
        "v_mfma_f32_16x16x32_bf16 v[96:99], a[92:95], a[244:247], v[96:99]\n"
        "ds_read_b32 v63, v63\n"
        "v_add_u32 v240, s31, v240\n"
        "v_add_u32 v241, s31, v241\n"
        "v_mfma_f32_16x16x32_bf16 v[100:103], a[96:99], a[224:227], 0\n"
        "v_add_u32 v245, s45, v245\n"
        "v_add_u32 v246, s45, v246\n"
        "v_add_u32 v247, s45, v247\n"
        "v_mfma_f32_16x16x32_bf16 v[100:103], a[100:103], a[228:231], v[100:103]\n"
        "v_cvt_pk_bf16_f32 v192, v96, v97\n"
        "v_cvt_pk_bf16_f32 v193, v98, v99\n"
        "v_mfma_f32_16x16x32_bf16 v[100:103], a[104:107], a[232:235], v[100:103]\n"
        "v_mfma_f32_16x16x32_bf16 v[100:103], a[108:111], a[236:239], v[100:103]\n"
        "buffer_load_dwordx4 v130, s[20:23], s64 offen offset:2048 lds\n"
        "v_mfma_f32_16x16x32_bf16 v[100:103], a[112:115], a[240:243], v[100:103]\n"
        "v_mfma_f32_16x16x32_bf16 v[100:103], a[116:119], a[244:247], v[100:103]\n"
        "v_mfma_f32_16x16x32_bf16 v[112:115], a[120:123], a[224:227], 0\n"
        "ds_read_b128 a[224:227], v151 offset:13952\n"
        "v_mfma_f32_16x16x32_bf16 v[112:115], a[124:127], a[228:231], v[112:115]\n"
        "ds_read_b128 a[228:231], v150 offset:14080\n"
        "v_mfma_f32_16x16x32_bf16 v[112:115], a[128:131], a[232:235], v[112:115]\n"
        "ds_read_b128 a[232:235], v151 offset:14080\n"
        "v_mfma_f32_16x16x32_bf16 v[112:115], a[132:135], a[236:239], v[112:115]\n"
        "v_mfma_f32_16x16x32_bf16 v[112:115], a[136:139], a[240:243], v[112:115]\n"
        "v_cvt_pk_bf16_f32 v194, v100, v101\n"
        "v_cvt_pk_bf16_f32 v195, v102, v103\n"
        "v_mfma_f32_16x16x32_bf16 v[112:115], a[140:143], a[244:247], v[112:115]\n"
        "s_waitcnt lgkmcnt(14)\n"
        "s_waitcnt vmcnt(15)\n"
        "s_barrier\n"
        "ds_read_b128 v[162:165], v251 offset:0\n"
        "ds_read_b128 v[182:185], v251 offset:512\n"
        "v_mfma_f32_16x16x32_bf16 v[96:99], a[72:75], a[248:251], 0\n"
        "s_waitcnt lgkmcnt(14)\n"
        "s_add_u32 m0, s40, 0\n"
        "v_mfma_f32_16x16x32_bf16 v[96:99], a[76:79], a[216:219], v[96:99]\n"
        "s_waitcnt lgkmcnt(13)\n"
        "s_add_u32 s64, s65, 111360\n"
        "v_mfma_f32_16x16x32_bf16 v[96:99], a[80:83], a[220:223], v[96:99]\n"
        "s_waitcnt lgkmcnt(4)\n"
        "buffer_load_dwordx4 v128, s[20:23], s64 offen lds\n"
        "ds_read_b128 v[202:205], v251 offset:4096\n"
        "v_cvt_pk_bf16_f32 v208, v112, v113\n"
        "v_mfma_f32_16x16x32_bf16 v[96:99], a[84:87], a[224:227], v[96:99]\n"
        "s_waitcnt lgkmcnt(4)\n"
        "v_cvt_pk_bf16_f32 v209, v114, v115\n"
        "v_mfma_f32_16x16x32_bf16 v[96:99], a[88:91], a[228:231], v[96:99]\n"
        "s_waitcnt lgkmcnt(3)\n"
        "v_mfma_f32_16x16x32_bf16 v[96:99], a[92:95], a[232:235], v[96:99]\n"
        "v_mfma_f32_16x16x32_bf16 v[100:103], a[96:99], a[248:251], 0\n"
        "ds_read_b128 v[222:225], v251 offset:4608\n"
        "v_mfma_f32_16x16x32_bf16 v[100:103], a[100:103], a[216:219], v[100:103]\n"
        "buffer_load_dwordx4 v129, s[20:23], s64 offen offset:1024 lds\n"
        "v_mfma_f32_16x16x32_bf16 v[100:103], a[104:107], a[220:223], v[100:103]\n"
        "s_waitcnt lgkmcnt(3)\n"
        "v_pk_min_u16 v162, v162, s26\n"
        "v_pk_min_u16 v163, v163, s26\n"
        "v_pk_min_u16 v164, v164, s26\n"
        "v_mfma_f32_16x16x32_bf16 v[100:103], a[108:111], a[224:227], v[100:103]\n"
        "v_cvt_pk_bf16_f32 v212, v96, v97\n"
        "v_cvt_pk_bf16_f32 v213, v98, v99\n"
        "v_pk_min_u16 v165, v165, s26\n"
        "v_mfma_f32_16x16x32_bf16 v[100:103], a[112:115], a[228:231], v[100:103]\n"
        "v_mfma_f32_16x16x32_bf16 v[100:103], a[116:119], a[232:235], v[100:103]\n"
        "v_mfma_f32_16x16x32_bf16 v[112:115], a[120:123], a[248:251], 0\n"
        "buffer_load_dwordx4 v130, s[20:23], s64 offen offset:2048 lds\n"
        "s_waitcnt lgkmcnt(2)\n"
        "v_pk_min_u16 v182, v182, s26\n"
        "v_pk_min_u16 v183, v183, s26\n"
        "v_mfma_f32_16x16x32_bf16 v[112:115], a[124:127], a[216:219], v[112:115]\n"
        "v_pk_min_u16 v184, v184, s26\n"
        "v_pk_min_u16 v185, v185, s26\n"
        "v_mfma_f32_16x16x32_bf16 v[112:115], a[128:131], a[220:223], v[112:115]\n"
        "v_cvt_pk_bf16_f32 v214, v100, v101\n"
        "v_cvt_pk_bf16_f32 v215, v102, v103\n"
        "v_mfma_f32_16x16x32_bf16 v[112:115], a[132:135], a[224:227], v[112:115]\n"
        "s_add_u32 m0, s40, 12288\n"
        "v_mfma_f32_16x16x32_bf16 v[112:115], a[136:139], a[228:231], v[112:115]\n"
        "s_add_u32 s64, s65, 148224\n"
        "s_waitcnt lgkmcnt(1)\n"
        "v_pk_min_u16 v202, v202, s26\n"
        "v_pk_min_u16 v203, v203, s26\n"
        "v_pk_min_u16 v204, v204, s26\n"
        "v_mfma_f32_16x16x32_bf16 v[112:115], a[140:143], a[232:235], v[112:115]\n"
        "buffer_load_dwordx4 v128, s[20:23], s64 offen lds\n"
        "v_pk_min_u16 v205, v205, s26\n"
        "v_mfma_f32_16x16x32_bf16 v[64:67], v[152:155], v[156:159], v[0:3]\n"
        "v_mfma_f32_16x16x32_bf16 v[64:67], v[168:171], v[160:163], v[64:67]\n"
        "v_mfma_f32_16x16x32_bf16 v[68:71], v[172:175], v[156:159], v[4:7]\n"
        "s_waitcnt lgkmcnt(0)\n"
        "v_pk_min_u16 v222, v222, s26\n"
        "v_pk_min_u16 v223, v223, s26\n"
        "v_pk_min_u16 v224, v224, s26\n"
        "v_mfma_f32_16x16x32_bf16 v[68:71], v[188:191], v[164:167], v[68:71]\n"
        "ds_read_b128 v[162:165], v252 offset:0\n"
        "v_cvt_pk_bf16_f32 v230, v112, v113\n"
        "v_cvt_pk_bf16_f32 v231, v114, v115\n"
        "v_mfma_f32_16x16x32_bf16 v[80:83], v[152:155], v[176:179], v[16:19]\n"
        "buffer_load_dwordx4 v129, s[20:23], s64 offen offset:1024 lds\n"
        "v_pk_min_u16 v225, v225, s26\n"
        "v_max3_f32 v232, v64, v65, v66\n"
        "v_mfma_f32_16x16x32_bf16 v[80:83], v[168:171], v[180:183], v[80:83]\n"
        "v_max3_f32 v232, v232, v67, v68\n"
        "v_max3_f32 v232, v232, v69, v70\n"
        "v_mfma_f32_16x16x32_bf16 v[84:87], v[172:175], v[176:179], v[20:23]\n"
        "v_mfma_f32_16x16x32_bf16 v[84:87], v[188:191], v[184:187], v[84:87]\n"
        "ds_read_b128 v[182:185], v252 offset:512\n"
        "v_mfma_f32_16x16x32_bf16 v[96:99], v[152:155], v[196:199], v[32:35]\n"
        "v_mfma_f32_16x16x32_bf16 v[96:99], v[168:171], v[200:203], v[96:99]\n"
        "buffer_load_dwordx4 v130, s[20:23], s64 offen offset:2048 lds\n"
        "v_max3_f32 v233, v80, v81, v82\n"
        "v_mfma_f32_16x16x32_bf16 v[100:103], v[172:175], v[196:199], v[36:39]\n"
        "v_mfma_f32_16x16x32_bf16 v[100:103], v[188:191], v[204:207], v[100:103]\n"
        "ds_read_b128 v[202:205], v252 offset:4096\n"
        "s_waitcnt vmcnt(15)\n"
        "s_barrier\n"
        "v_max3_f32 v233, v233, v83, v84\n"
        "v_max3_f32 v233, v233, v85, v86\n"
        "v_mfma_f32_16x16x32_bf16 v[112:115], v[152:155], v[216:219], v[48:51]\n"
        "s_add_u32 m0, s40, 24576\n"
        "ds_read_b128 a[236:239], v240 offset:0\n"
        "s_waitcnt lgkmcnt(3)\n"
        "v_pk_min_u16 v162, v162, s26\n"
        "v_pk_min_u16 v163, v163, s26\n"
        "v_mfma_f32_16x16x32_bf16 v[112:115], v[168:171], v[220:223], v[112:115]\n"
        "s_add_u32 s64, s65, 185088\n"
        "ds_read_b128 a[240:243], v241 offset:0\n"
        "v_pk_min_u16 v164, v164, s26\n"
        "v_pk_min_u16 v165, v165, s26\n"
        "v_mfma_f32_16x16x32_bf16 v[116:119], v[172:175], v[216:219], v[52:55]\n"
        "buffer_load_dwordx4 v128, s[20:23], s64 offen lds\n"
        "ds_read_b128 a[244:247], v240 offset:128\n"
        "v_max3_f32 v234, v96, v97, v98\n"
        "v_mfma_f32_16x16x32_bf16 v[116:119], v[188:191], v[224:227], v[116:119]\n"
        "ds_read_b128 v[222:225], v252 offset:4608\n"
        "ds_read_b128 a[248:251], v241 offset:128\n"
        "v_max3_f32 v234, v234, v99, v100\n"
        "v_mfma_f32_16x16x32_bf16 v[72:75], v[192:195], v[156:159], v[8:11]\n"
        "ds_read_b128 a[216:219], v240 offset:256\n"
        "s_waitcnt lgkmcnt(7)\n"
        "v_pk_min_u16 v182, v182, s26\n"
        "v_pk_min_u16 v183, v183, s26\n"
        "v_mfma_f32_16x16x32_bf16 v[72:75], v[208:211], v[160:163], v[72:75]\n"
        "ds_read_b128 a[220:223], v241 offset:256\n"
        "v_pk_min_u16 v184, v184, s26\n"
        "v_pk_min_u16 v185, v185, s26\n"
        "v_mfma_f32_16x16x32_bf16 v[76:79], v[212:215], v[156:159], v[12:15]\n"
        "v_max3_f32 v234, v234, v101, v102\n"
        "v_max3_f32 v235, v112, v113, v114\n"
        "v_max3_f32 v235, v235, v115, v116\n"
        "v_mfma_f32_16x16x32_bf16 v[76:79], v[228:231], v[164:167], v[76:79]\n"
        "buffer_load_dwordx4 v129, s[20:23], s64 offen offset:1024 lds\n"
        "v_max3_f32 v232, v232, v71, v72\n"
        "v_max3_f32 v232, v232, v73, v74\n"
        "v_mfma_f32_16x16x32_bf16 v[88:91], v[192:195], v[176:179], v[24:27]\n"
        "s_waitcnt lgkmcnt(7)\n"
        "v_pk_min_u16 v202, v202, s26\n"
        "v_pk_min_u16 v203, v203, s26\n"
        "v_pk_min_u16 v204, v204, s26\n"
        "v_mfma_f32_16x16x32_bf16 v[88:91], v[208:211], v[180:183], v[88:91]\n"
        "v_pk_min_u16 v205, v205, s26\n"
        "v_max3_f32 v232, v232, v75, v76\n"
        "v_max3_f32 v232, v232, v77, v78\n"
        "v_mfma_f32_16x16x32_bf16 v[92:95], v[212:215], v[176:179], v[28:31]\n"
        "v_max_f32 v232, v232, v79\n"
        "v_max3_f32 v235, v235, v117, v118\n"
        "v_xor_b32 v251, 0x2000, v251\n"
        "v_mfma_f32_16x16x32_bf16 v[92:95], v[228:231], v[184:187], v[92:95]\n"
        "v_max3_f32 v233, v233, v87, v88\n"
        "v_max3_f32 v233, v233, v89, v90\n"
        "v_xor_b32 v252, 0x2000, v252\n"
        "v_mfma_f32_16x16x32_bf16 v[104:107], v[192:195], v[196:199], v[40:43]\n"
        "buffer_load_dwordx4 v130, s[20:23], s64 offen offset:2048 lds\n"
        "s_waitcnt lgkmcnt(3)\n"
        "v_pk_min_u16 v222, v222, s26\n"
        "v_pk_min_u16 v223, v223, s26\n"
        "v_mfma_f32_16x16x32_bf16 v[104:107], v[208:211], v[200:203], v[104:107]\n"
        "v_pk_min_u16 v224, v224, s26\n"
        "v_pk_min_u16 v225, v225, s26\n"
        "v_max3_f32 v233, v233, v91, v92\n"
        "v_mfma_f32_16x16x32_bf16 v[108:111], v[212:215], v[196:199], v[44:47]\n"
        "s_waitcnt vmcnt(9)\n"
        "s_barrier\n"
        "v_max3_f32 v233, v233, v93, v94\n"
        "v_max_f32 v233, v233, v95\n"
        "v_mfma_f32_16x16x32_bf16 v[108:111], v[228:231], v[204:207], v[108:111]\n"
        "ds_read_b64_tr_b16 a[144:145], v131 offset:36864\n"
        "ds_read_b64_tr_b16 a[146:147], v131 offset:38400\n"
        "v_max3_f32 v234, v234, v103, v104\n"
        "v_mfma_f32_16x16x32_bf16 v[120:123], v[192:195], v[216:219], v[56:59]\n"
        "s_mov_b32 m0, s48\n"
        "s_xor_b32 s48, s48, 8192\n"
        "ds_read_b64_tr_b16 a[148:149], v131 offset:49152\n"
        "ds_read_b64_tr_b16 a[150:151], v131 offset:50688\n"
        "v_mfma_f32_16x16x32_bf16 v[120:123], v[208:211], v[220:223], v[120:123]\n"
        "buffer_load_dwordx4 v249, s[12:15], s36 offen offset:0 lds\n"
        "ds_read_b64_tr_b16 a[152:153], v131 offset:61440\n"
        "v_mfma_f32_16x16x32_bf16 v[124:127], v[212:215], v[216:219], v[60:63]\n"
        "ds_read_b64_tr_b16 a[154:155], v131 offset:62976\n"
        "ds_read_b64_tr_b16 a[168:169], v132 offset:36864\n"
        "v_xor_b32 v253, 32, v249\n"
        "v_mfma_f32_16x16x32_bf16 v[124:127], v[228:231], v[224:227], v[124:127]\n"
        "ds_read_b64_tr_b16 a[170:171], v132 offset:38400\n"
        "ds_read_b64_tr_b16 a[172:173], v132 offset:49152\n"
        "v_max3_f32 v234, v234, v105, v106\n"
        "v_mfma_f32_16x16x32_bf16 v[212:215], a[72:75], a[236:239], 0\n"
        "ds_read_b64_tr_b16 a[174:175], v132 offset:50688\n"
        "ds_read_b64_tr_b16 a[176:177], v132 offset:61440\n"
        "v_max3_f32 v234, v234, v107, v108\n"
        "v_mfma_f32_16x16x32_bf16 v[212:215], a[76:79], a[240:243], v[212:215]\n"
        "ds_read_b64_tr_b16 a[178:179], v132 offset:62976\n"
        "ds_read_b64_tr_b16 a[192:193], v133 offset:36864\n"
        "v_max3_f32 v234, v234, v109, v110\n"
        "v_mfma_f32_16x16x32_bf16 v[212:215], a[80:83], a[244:247], v[212:215]\n"
        "s_waitcnt lgkmcnt(15)\n"
        "buffer_load_dwordx4 v253, s[12:15], s36 offen offset:1024 lds\n"
        "s_add_u32 s36, s36, 2097152\n"
        "ds_read_b64_tr_b16 a[194:195], v133 offset:38400\n"
        "v_mfma_f32_16x16x32_bf16 v[212:215], a[84:87], a[248:251], v[212:215]\n"
        "s_waitcnt lgkmcnt(15)\n"
        "ds_read_b64_tr_b16 a[196:197], v133 offset:49152\n"
        "ds_read_b64_tr_b16 a[198:199], v133 offset:50688\n"
        "v_max_f32 v234, v234, v111\n"
        "v_mfma_f32_16x16x32_bf16 v[212:215], a[88:91], a[216:219], v[212:215]\n"
        "s_waitcnt lgkmcnt(15)\n"
        "ds_read_b64_tr_b16 a[200:201], v133 offset:61440\n"
        "ds_read_b64_tr_b16 a[202:203], v133 offset:62976\n"
        "v_max3_f32 v235, v235, v119, v120\n"
        "v_mfma_f32_16x16x32_bf16 v[212:215], a[92:95], a[220:223], v[212:215]\n"
        "v_permlane32_swap_b32 v232, v233\n"
        "v_max_f32 v232, v232, v233\n"
        "v_mfma_f32_16x16x32_bf16 v[216:219], a[96:99], a[236:239], 0\n"
        "v_mov_b32 v233, v232\n"
        "v_max3_f32 v235, v235, v121, v122\n"
        "v_max3_f32 v235, v235, v123, v124\n"
        "v_mfma_f32_16x16x32_bf16 v[216:219], a[100:103], a[240:243], v[216:219]\n"
        "v_permlane16_swap_b32 v232, v233\n"
        "v_max_f32 v232, v232, v233\n"
        "v_mfma_f32_16x16x32_bf16 v[216:219], a[104:107], a[244:247], v[216:219]\n"
        "v_mul_f32 v236, s25, v232\n"
        "v_mul_f32 v237, s25, v232\n"
        "v_max3_f32 v235, v235, v125, v126\n"
        "v_mfma_f32_16x16x32_bf16 v[216:219], a[108:111], a[248:251], v[216:219]\n"
        "v_permlane32_swap_b32 v236, v237\n"
        "v_max_f32 v235, v235, v127\n"
        "v_mfma_f32_16x16x32_bf16 v[216:219], a[112:115], a[216:219], v[216:219]\n"
        "v_cvt_pk_bf16_f32 v152, v212, v213\n"
        "v_permlane32_swap_b32 v234, v235\n"
        "v_mfma_f32_16x16x32_bf16 v[216:219], a[116:119], a[220:223], v[216:219]\n"
        "v_max_f32 v234, v234, v235\n"
        "v_mov_b32 v235, v234\n"
        "v_cvt_pk_bf16_f32 v153, v214, v215\n"
        "v_mfma_f32_16x16x32_bf16 v[162:165], a[120:123], a[236:239], 0\n"
        "v_permlane16_swap_b32 v234, v235\n"
        "v_max_f32 v234, v234, v235\n"
        "v_mfma_f32_16x16x32_bf16 v[162:165], a[124:127], a[240:243], v[162:165]\n"
        "v_mul_f32 v238, s25, v234\n"
        "v_mul_f32 v239, s25, v234\n"
        "v_cvt_pk_bf16_f32 v154, v216, v217\n"
        "v_mfma_f32_16x16x32_bf16 v[162:165], a[128:131], a[244:247], v[162:165]\n"
        "ds_read_b128 a[224:227], v240 offset:1536\n"
        "v_permlane32_swap_b32 v238, v239\n"
        "v_mfma_f32_16x16x32_bf16 v[162:165], a[132:135], a[248:251], v[162:165]\n"
        "ds_read_b128 a[228:231], v241 offset:1536\n"
        "v_cvt_pk_bf16_f32 v155, v218, v219\n"
        "v_fma_f32 v64, v64, s24, v236\n"
        "v_mfma_f32_16x16x32_bf16 v[162:165], a[136:139], a[216:219], v[162:165]\n"
        "ds_read_b128 a[232:235], v240 offset:1664\n"
        "v_exp_f32 v64, v64\n"
        "v_mfma_f32_16x16x32_bf16 v[162:165], a[140:143], a[220:223], v[162:165]\n"
        "v_fma_f32 v65, v65, s24, v236\n"
        "v_exp_f32 v65, v65\n"
        "v_mfma_f32_16x16x32_bf16 v[212:215], a[0:3], a[236:239], 0\n"
        "v_cvt_pk_bf16_f32 v64, v64, v65\n"
        "v_fma_f32 v66, v66, s24, v236\n"
        "v_fma_f32 v67, v67, s24, v236\n"
        "v_mfma_f32_16x16x32_bf16 v[212:215], a[4:7], a[240:243], v[212:215]\n"
        "v_exp_f32 v66, v66\n"
        "v_cvt_pk_bf16_f32 v168, v162, v163\n"
        "v_mfma_f32_16x16x32_bf16 v[212:215], a[8:11], a[244:247], v[212:215]\n"
        "v_exp_f32 v67, v67\n"
        "v_cvt_pk_bf16_f32 v169, v164, v165\n"
        "v_mfma_f32_16x16x32_bf16 v[212:215], a[12:15], a[248:251], v[212:215]\n"
        "v_cvt_pk_bf16_f32 v65, v66, v67\n"
        "v_fma_f32 v68, v68, s24, v236\n"
        "v_fma_f32 v69, v69, s24, v236\n"
        "v_mfma_f32_16x16x32_bf16 v[212:215], a[16:19], a[216:219], v[212:215]\n"
        "v_exp_f32 v68, v68\n"
        "v_fma_f32 v70, v70, s24, v236\n"
        "v_mfma_f32_16x16x32_bf16 v[212:215], a[20:23], a[220:223], v[212:215]\n"
        "v_exp_f32 v69, v69\n"
        "v_fma_f32 v71, v71, s24, v236\n"
        "v_mfma_f32_16x16x32_bf16 v[216:219], a[24:27], a[236:239], 0\n"
        "s_waitcnt lgkmcnt(3)\n"
        "s_waitcnt vmcnt(2)\n"
        "s_barrier\n"
        "v_exp_f32 v70, v70\n"
        "v_cvt_pk_bf16_f32 v66, v68, v69\n"
        "v_mfma_f32_16x16x32_bf16 v[216:219], a[28:31], a[240:243], v[216:219]\n"
        "ds_read_b64_tr_b16 a[156:157], v131 offset:0\n"
        "s_add_u32 m0, s65, 36864\n"
        "ds_read_b64_tr_b16 a[158:159], v131 offset:1536\n"
        "v_cvt_pk_bf16_f32 v156, v212, v213\n"
        "v_mfma_f32_16x16x32_bf16 v[216:219], a[32:35], a[244:247], v[216:219]\n"
        "s_add_u32 s64, s65, 36864\n"
        "ds_read_b64_tr_b16 a[160:161], v131 offset:12288\n"
        "ds_read_b64_tr_b16 a[162:163], v131 offset:13824\n"
        "v_cvt_pk_bf16_f32 v157, v214, v215\n"
        "v_mfma_f32_16x16x32_bf16 v[216:219], a[36:39], a[248:251], v[216:219]\n"
        "buffer_load_dwordx4 v142, s[52:55], s64 offen lds\n"
        "ds_read_b64_tr_b16 a[164:165], v131 offset:24576\n"
        "v_fma_f32 v72, v72, s24, v236\n"
        "v_mfma_f32_16x16x32_bf16 v[216:219], a[40:43], a[216:219], v[216:219]\n"
        "ds_read_b64_tr_b16 a[166:167], v131 offset:26112\n"
        "ds_read_b64_tr_b16 a[180:181], v132 offset:0\n"
        "v_fma_f32 v73, v73, s24, v236\n"
        "v_mfma_f32_16x16x32_bf16 v[216:219], a[44:47], a[220:223], v[216:219]\n"
        "ds_read_b64_tr_b16 a[182:183], v132 offset:1536\n"
        "ds_read_b64_tr_b16 a[184:185], v132 offset:12288\n"
        "v_fma_f32 v74, v74, s24, v236\n"
        "v_mfma_f32_16x16x32_bf16 v[162:165], a[48:51], a[236:239], 0\n"
        "ds_read_b128 a[236:239], v241 offset:1664\n"
        "ds_read_b64_tr_b16 a[186:187], v132 offset:13824\n"
        "v_fma_f32 v75, v75, s24, v236\n"
        "v_mfma_f32_16x16x32_bf16 v[162:165], a[52:55], a[240:243], v[162:165]\n"
        "ds_read_b128 a[240:243], v240 offset:1792\n"
        "ds_read_b64_tr_b16 a[188:189], v132 offset:24576\n"
        "v_cvt_pk_bf16_f32 v158, v216, v217\n"
        "v_mfma_f32_16x16x32_bf16 v[162:165], a[56:59], a[244:247], v[162:165]\n"
        "ds_read_b128 a[244:247], v241 offset:1792\n"
        "buffer_load_dwordx4 v142, s[52:55], s64 offen offset:1024 lds\n"
        "v_cvt_pk_bf16_f32 v159, v218, v219\n"
        "v_mfma_f32_16x16x32_bf16 v[162:165], a[60:63], a[248:251], v[162:165]\n"
        "ds_read_b64_tr_b16 a[190:191], v132 offset:26112\n"
        "ds_read_b64_tr_b16 a[204:205], v133 offset:0\n"
        "v_fma_f32 v76, v76, s24, v236\n"
        "v_mfma_f32_16x16x32_bf16 v[162:165], a[64:67], a[216:219], v[162:165]\n"
        "ds_read_b64_tr_b16 a[206:207], v133 offset:1536\n"
        "ds_read_b64_tr_b16 a[208:209], v133 offset:12288\n"
        "v_fma_f32 v77, v77, s24, v236\n"
        "v_mfma_f32_16x16x32_bf16 v[162:165], a[68:71], a[220:223], v[162:165]\n"
        "s_waitcnt lgkmcnt(15)\n"
        "ds_read_b64_tr_b16 a[210:211], v133 offset:13824\n"
        "ds_read_b64_tr_b16 a[212:213], v133 offset:24576\n"
        "v_fma_f32 v78, v78, s24, v236\n"
        "v_mfma_f32_16x16x32_bf16 v[212:215], a[72:75], a[224:227], 0\n"
        "s_waitcnt lgkmcnt(15)\n"
        "ds_read_b64_tr_b16 a[214:215], v133 offset:26112\n"
        "v_exp_f32 v71, v71\n"
        "v_mfma_f32_16x16x32_bf16 v[212:215], a[76:79], a[228:231], v[212:215]\n"
        "s_waitcnt lgkmcnt(15)\n"
        "buffer_load_dwordx4 v142, s[52:55], s64 offen offset:2048 lds\n"
        "v_exp_f32 v72, v72\n"
        "v_mfma_f32_16x16x32_bf16 v[212:215], a[80:83], a[232:235], v[212:215]\n"
        "s_waitcnt lgkmcnt(11)\n"
        "v_exp_f32 v73, v73\n"
        "v_cvt_pk_bf16_f32 v160, v162, v163\n"
        "v_mfma_f32_16x16x32_bf16 v[212:215], a[84:87], a[236:239], v[212:215]\n"
        "s_waitcnt lgkmcnt(9)\n"
        "v_exp_f32 v74, v74\n"
        "v_cvt_pk_bf16_f32 v161, v164, v165\n"
        "v_mfma_f32_16x16x32_bf16 v[212:215], a[88:91], a[240:243], v[212:215]\n"
        "s_waitcnt lgkmcnt(7)\n"
        "s_add_u32 m0, s65, 39936\n"
        "v_cvt_pk_bf16_f32 v166, v162, v163\n"
        "v_cvt_pk_bf16_f32 v167, v164, v165\n"
        "v_cvt_pk_bf16_f32 v67, v70, v71\n"
        "v_mfma_f32_16x16x32_bf16 v[212:215], a[92:95], a[244:247], v[212:215]\n"
        "s_add_u32 s64, s65, 39936\n"
        "v_exp_f32 v75, v75\n"
        "v_cvt_pk_bf16_f32 v68, v72, v73\n"
        "v_mfma_f32_16x16x32_bf16 v[216:219], a[96:99], a[224:227], 0\n"
        "buffer_load_dwordx4 v142, s[52:55], s64 offen lds\n"
        "v_exp_f32 v76, v76\n"
        "v_mfma_f32_16x16x32_bf16 v[216:219], a[100:103], a[228:231], v[216:219]\n"
        "v_exp_f32 v77, v77\n"
        "v_cvt_pk_bf16_f32 v172, v212, v213\n"
        "v_mfma_f32_16x16x32_bf16 v[216:219], a[104:107], a[232:235], v[216:219]\n"
        "v_exp_f32 v78, v78\n"
        "v_cvt_pk_bf16_f32 v173, v214, v215\n"
        "v_mfma_f32_16x16x32_bf16 v[216:219], a[108:111], a[236:239], v[216:219]\n"
        "v_cvt_pk_bf16_f32 v69, v74, v75\n"
        "v_cvt_pk_bf16_f32 v70, v76, v77\n"
        "v_fma_f32 v79, v79, s24, v236\n"
        "v_mfma_f32_16x16x32_bf16 v[216:219], a[112:115], a[240:243], v[216:219]\n"
        "s_waitcnt lgkmcnt(0)\n"
        "s_barrier\n"
        "v_exp_f32 v79, v79\n"
        "v_fma_f32 v80, v80, s24, v237\n"
        "v_mfma_f32_16x16x32_bf16 v[216:219], a[116:119], a[244:247], v[216:219]\n"
        "buffer_load_dwordx4 v142, s[52:55], s64 offen offset:1024 lds\n"
        "v_exp_f32 v80, v80\n"
        "v_mfma_f32_16x16x32_bf16 v[162:165], a[120:123], a[224:227], 0\n"
        "v_cvt_pk_bf16_f32 v71, v78, v79\n"
        "v_fma_f32 v81, v81, s24, v237\n"
        "v_fma_f32 v82, v82, s24, v237\n"
        "v_mfma_f32_16x16x32_bf16 v[162:165], a[124:127], a[228:231], v[162:165]\n"
        "v_exp_f32 v81, v81\n"
        "v_cvt_pk_bf16_f32 v174, v216, v217\n"
        "v_mfma_f32_16x16x32_bf16 v[162:165], a[128:131], a[232:235], v[162:165]\n"
        "ds_read_b128 a[248:251], v240 offset:12288\n"
        "v_exp_f32 v82, v82\n"
        "v_mfma_f32_16x16x32_bf16 v[162:165], a[132:135], a[236:239], v[162:165]\n"
        "ds_read_b128 a[216:219], v241 offset:12288\n"
        "v_cvt_pk_bf16_f32 v175, v218, v219\n"
        "v_cvt_pk_bf16_f32 v80, v80, v81\n"
        "v_mfma_f32_16x16x32_bf16 v[162:165], a[136:139], a[240:243], v[162:165]\n"
        "buffer_load_dwordx4 v142, s[52:55], s64 offen offset:2048 lds\n"
        "ds_read_b128 a[220:223], v240 offset:12416\n"
        "v_fma_f32 v83, v83, s24, v237\n"
        "v_mfma_f32_16x16x32_bf16 v[162:165], a[140:143], a[244:247], v[162:165]\n"
        "v_exp_f32 v83, v83\n"
        "v_fma_f32 v84, v84, s24, v237\n"
        "v_mfma_f32_16x16x32_bf16 v[212:215], a[0:3], a[224:227], 0\n"
        "v_exp_f32 v84, v84\n"
        "v_cvt_pk_bf16_f32 v81, v82, v83\n"
        "v_mfma_f32_16x16x32_bf16 v[212:215], a[4:7], a[228:231], v[212:215]\n"
        "s_add_u32 m0, s65, 43008\n"
        "v_fma_f32 v85, v85, s24, v237\n"
        "v_exp_f32 v85, v85\n"
        "v_mfma_f32_16x16x32_bf16 v[212:215], a[8:11], a[232:235], v[212:215]\n"
        "s_add_u32 s64, s65, 43008\n"
        "v_cvt_pk_bf16_f32 v190, v162, v163\n"
        "v_cvt_pk_bf16_f32 v191, v164, v165\n"
        "v_cvt_pk_bf16_f32 v82, v84, v85\n"
        "v_mfma_f32_16x16x32_bf16 v[212:215], a[12:15], a[236:239], v[212:215]\n"
        "buffer_load_dwordx4 v142, s[52:55], s64 offen lds\n"
        "v_fma_f32 v86, v86, s24, v237\n"
        "v_fma_f32 v87, v87, s24, v237\n"
        "v_mfma_f32_16x16x32_bf16 v[212:215], a[16:19], a[240:243], v[212:215]\n"
        "v_exp_f32 v86, v86\n"
        "v_fma_f32 v88, v88, s24, v237\n"
        "v_mfma_f32_16x16x32_bf16 v[212:215], a[20:23], a[244:247], v[212:215]\n"
        "v_exp_f32 v87, v87\n"
        "v_fma_f32 v89, v89, s24, v237\n"
        "v_mfma_f32_16x16x32_bf16 v[216:219], a[24:27], a[224:227], 0\n"
        "v_exp_f32 v88, v88\n"
        "v_cvt_pk_bf16_f32 v83, v86, v87\n"
        "v_mfma_f32_16x16x32_bf16 v[216:219], a[28:31], a[228:231], v[216:219]\n"
        "v_exp_f32 v89, v89\n"
        "v_fma_f32 v90, v90, s24, v237\n"
        "v_mfma_f32_16x16x32_bf16 v[216:219], a[32:35], a[232:235], v[216:219]\n"
        "buffer_load_dwordx4 v142, s[52:55], s64 offen offset:1024 lds\n"
        "v_exp_f32 v90, v90\n"
        "v_mfma_f32_16x16x32_bf16 v[216:219], a[36:39], a[236:239], v[216:219]\n"
        "v_cvt_pk_bf16_f32 v176, v212, v213\n"
        "v_cvt_pk_bf16_f32 v177, v214, v215\n"
        "v_cvt_pk_bf16_f32 v84, v88, v89\n"
        "v_mfma_f32_16x16x32_bf16 v[216:219], a[40:43], a[240:243], v[216:219]\n"
        "v_fma_f32 v91, v91, s24, v237\n"
        "v_exp_f32 v91, v91\n"
        "v_mfma_f32_16x16x32_bf16 v[216:219], a[44:47], a[244:247], v[216:219]\n"
        "v_cvt_pk_bf16_f32 v85, v90, v91\n"
        "v_fma_f32 v92, v92, s24, v237\n"
        "v_fma_f32 v93, v93, s24, v237\n"
        "v_mfma_f32_16x16x32_bf16 v[162:165], a[48:51], a[224:227], 0\n"
        "ds_read_b128 a[224:227], v241 offset:12416\n"
        "v_exp_f32 v92, v92\n"
        "v_mfma_f32_16x16x32_bf16 v[162:165], a[52:55], a[228:231], v[162:165]\n"
        "ds_read_b128 a[228:231], v240 offset:12544\n"
        "buffer_load_dwordx4 v142, s[52:55], s64 offen offset:2048 lds\n"
        "v_cvt_pk_bf16_f32 v178, v216, v217\n"
        "v_mfma_f32_16x16x32_bf16 v[162:165], a[56:59], a[232:235], v[162:165]\n"
        "ds_read_b128 a[232:235], v241 offset:12544\n"
        "v_exp_f32 v93, v93\n"
        "v_mfma_f32_16x16x32_bf16 v[162:165], a[60:63], a[236:239], v[162:165]\n"
        "v_cvt_pk_bf16_f32 v179, v218, v219\n"
        "v_cvt_pk_bf16_f32 v86, v92, v93\n"
        "v_fma_f32 v94, v94, s24, v237\n"
        "v_mfma_f32_16x16x32_bf16 v[162:165], a[64:67], a[240:243], v[162:165]\n"
        "s_add_u32 m0, s65, 0\n"
        "v_exp_f32 v94, v94\n"
        "v_fma_f32 v95, v95, s24, v237\n"
        "v_mfma_f32_16x16x32_bf16 v[162:165], a[68:71], a[244:247], v[162:165]\n"
        "s_waitcnt lgkmcnt(5)\n"
        "s_add_u32 s64, s65, 0\n"
        "v_exp_f32 v95, v95\n"
        "v_fma_f32 v96, v96, s24, v238\n"
        "v_mfma_f32_16x16x32_bf16 v[72:75], a[72:75], a[248:251], 0\n"
        "s_waitcnt lgkmcnt(4)\n"
        "buffer_load_dwordx4 v142, s[52:55], s64 offen lds\n"
        "v_exp_f32 v96, v96\n"
        "v_mfma_f32_16x16x32_bf16 v[72:75], a[76:79], a[216:219], v[72:75]\n"
        "s_waitcnt lgkmcnt(3)\n"
        "v_cvt_pk_bf16_f32 v87, v94, v95\n"
        "v_cvt_pk_bf16_f32 v180, v162, v163\n"
        "v_cvt_pk_bf16_f32 v181, v164, v165\n"
        "v_mfma_f32_16x16x32_bf16 v[72:75], a[80:83], a[220:223], v[72:75]\n"
        "s_waitcnt lgkmcnt(2)\n"
        "v_cvt_pk_bf16_f32 v186, v162, v163\n"
        "v_cvt_pk_bf16_f32 v187, v164, v165\n"
        "v_fma_f32 v97, v97, s24, v238\n"
        "v_mfma_f32_16x16x32_bf16 v[72:75], a[84:87], a[224:227], v[72:75]\n"
        "s_waitcnt lgkmcnt(1)\n"
        "v_exp_f32 v97, v97\n"
        "v_fma_f32 v98, v98, s24, v238\n"
        "v_mfma_f32_16x16x32_bf16 v[72:75], a[88:91], a[228:231], v[72:75]\n"
        "s_waitcnt lgkmcnt(0)\n"
        "v_exp_f32 v98, v98\n"
        "v_accvgpr_read_b32 v232, a252\n"
        "v_mfma_f32_16x16x32_bf16 v[72:75], a[92:95], a[232:235], v[72:75]\n"
        "buffer_load_dwordx4 v142, s[52:55], s64 offen offset:1024 lds\n"
        "v_accvgpr_read_b32 v233, a253\n"
        "v_accvgpr_read_b32 v234, a254\n"
        "v_mfma_f32_16x16x32_bf16 v[76:79], a[96:99], a[248:251], 0\n"
        "v_mov_b32 v235, 0x3f803f80\n"
        "v_accvgpr_write_b32 a252, v235\n"
        "v_accvgpr_write_b32 a253, v235\n"
        "v_mfma_f32_16x16x32_bf16 v[76:79], a[100:103], a[216:219], v[76:79]\n"
        "v_cvt_pk_bf16_f32 v192, v72, v73\n"
        "v_cvt_pk_bf16_f32 v193, v74, v75\n"
        "v_accvgpr_write_b32 a254, v235\n"
        "v_mfma_f32_16x16x32_bf16 v[76:79], a[104:107], a[220:223], v[76:79]\n"
        "v_accvgpr_write_b32 a255, v235\n"
        "v_cvt_pk_bf16_f32 v96, v96, v97\n"
        "v_fma_f32 v99, v99, s24, v238\n"
        "v_mfma_f32_16x16x32_bf16 v[76:79], a[108:111], a[224:227], v[76:79]\n"
        "v_exp_f32 v99, v99\n"
        "v_fma_f32 v100, v100, s24, v238\n"
        "v_mfma_f32_16x16x32_bf16 v[76:79], a[112:115], a[228:231], v[76:79]\n"
        "buffer_load_dwordx4 v142, s[52:55], s64 offen offset:2048 lds\n"
        "v_exp_f32 v100, v100\n"
        "v_mfma_f32_16x16x32_bf16 v[76:79], a[116:119], a[232:235], v[76:79]\n"
        "v_cvt_pk_bf16_f32 v97, v98, v99\n"
        "v_fma_f32 v101, v101, s24, v238\n"
        "v_fma_f32 v102, v102, s24, v238\n"
        "v_mfma_f32_16x16x32_bf16 v[162:165], a[120:123], a[248:251], 0\n"
        "ds_read_b128 a[236:239], v150 offset:0\n"
        "v_exp_f32 v101, v101\n"
        "v_mfma_f32_16x16x32_bf16 v[162:165], a[124:127], a[216:219], v[162:165]\n"
        "s_add_u32 m0, s65, 3072\n"
        "ds_read_b128 a[240:243], v151 offset:0\n"
        "v_exp_f32 v102, v102\n"
        "v_mfma_f32_16x16x32_bf16 v[162:165], a[128:131], a[220:223], v[162:165]\n"
        "s_add_u32 s64, s65, 3072\n"
        "s_waitcnt vmcnt(3)\n"
        "s_barrier\n"
        "ds_read_b64_tr_b16 v[212:213], v232 offset:36864\n"
        "ds_read_b128 a[244:247], v150 offset:128\n"
        "v_mfma_f32_16x16x32_bf16 v[162:165], a[132:135], a[224:227], v[162:165]\n"
        "buffer_load_dwordx4 v142, s[52:55], s64 offen lds\n"
        "v_cvt_pk_bf16_f32 v194, v76, v77\n"
        "v_cvt_pk_bf16_f32 v195, v78, v79\n"
        "v_mfma_f32_16x16x32_bf16 v[162:165], a[136:139], a[228:231], v[162:165]\n"
        "ds_read_b64_tr_b16 v[214:215], v232 offset:43008\n"
        "v_cvt_pk_bf16_f32 v98, v100, v101\n"
        "v_fma_f32 v103, v103, s24, v238\n"
        "v_mfma_f32_16x16x32_bf16 v[162:165], a[140:143], a[232:235], v[162:165]\n"
        "v_exp_f32 v103, v103\n"
        "v_fma_f32 v104, v104, s24, v238\n"
        "v_mfma_f32_16x16x32_bf16 v[72:75], a[0:3], a[248:251], 0\n"
        "ds_read_b64_tr_b16 v[216:217], v232 offset:55296\n"
        "v_exp_f32 v104, v104\n"
        "v_mfma_f32_16x16x32_bf16 v[72:75], a[4:7], a[216:219], v[72:75]\n"
        "v_cvt_pk_bf16_f32 v99, v102, v103\n"
        "v_fma_f32 v105, v105, s24, v238\n"
        "v_cvt_pk_bf16_f32 v208, v162, v163\n"
        "v_mfma_f32_16x16x32_bf16 v[72:75], a[8:11], a[220:223], v[72:75]\n"
        "buffer_load_dwordx4 v142, s[52:55], s64 offen offset:1024 lds\n"
        "ds_read_b64_tr_b16 v[218:219], v232 offset:61440\n"
        "v_cvt_pk_bf16_f32 v209, v164, v165\n"
        "v_mfma_f32_16x16x32_bf16 v[72:75], a[12:15], a[224:227], v[72:75]\n"
        "v_exp_f32 v105, v105\n"
        "v_fma_f32 v106, v106, s24, v238\n"
        "v_mfma_f32_16x16x32_bf16 v[72:75], a[16:19], a[228:231], v[72:75]\n"
        "ds_read_b64_tr_b16 v[220:221], v233 offset:49152\n"
        "v_exp_f32 v106, v106\n"
        "v_mfma_f32_16x16x32_bf16 v[72:75], a[20:23], a[232:235], v[72:75]\n"
        "v_cvt_pk_bf16_f32 v100, v104, v105\n"
        "v_fma_f32 v107, v107, s24, v238\n"
        "v_fma_f32 v108, v108, s24, v238\n"
        "v_mfma_f32_16x16x32_bf16 v[76:79], a[24:27], a[248:251], 0\n"
        "ds_read_b64_tr_b16 v[222:223], v233 offset:50688\n"
        "v_exp_f32 v107, v107\n"
        "v_mfma_f32_16x16x32_bf16 v[76:79], a[28:31], a[216:219], v[76:79]\n"
        "buffer_load_dwordx4 v142, s[52:55], s64 offen offset:2048 lds\n"
        "v_exp_f32 v108, v108\n"
        "v_mfma_f32_16x16x32_bf16 v[76:79], a[32:35], a[220:223], v[76:79]\n"
        "ds_read_b64_tr_b16 v[224:225], v232 offset:36896\n"
        "v_cvt_pk_bf16_f32 v196, v72, v73\n"
        "v_cvt_pk_bf16_f32 v197, v74, v75\n"
        "v_mfma_f32_16x16x32_bf16 v[76:79], a[36:39], a[224:227], v[76:79]\n"
        "v_cvt_pk_bf16_f32 v101, v106, v107\n"
        "v_fma_f32 v109, v109, s24, v238\n"
        "v_fma_f32 v110, v110, s24, v238\n"
        "v_mfma_f32_16x16x32_bf16 v[76:79], a[40:43], a[228:231], v[76:79]\n"
        "s_add_u32 m0, s65, 6144\n"
        "ds_read_b64_tr_b16 v[226:227], v232 offset:43040\n"
        "v_exp_f32 v109, v109\n"
        "v_mfma_f32_16x16x32_bf16 v[76:79], a[44:47], a[232:235], v[76:79]\n"
        "s_add_u32 s64, s65, 6144\n"
        "v_exp_f32 v110, v110\n"
        "v_cvt_pk_bf16_f32 v102, v108, v109\n"
        "v_mfma_f32_16x16x32_bf16 v[162:165], a[48:51], a[248:251], 0\n"
        "ds_read_b128 a[248:251], v151 offset:128\n"
        "buffer_load_dwordx4 v142, s[52:55], s64 offen lds\n"
        "v_fma_f32 v111, v111, s24, v238\n"
        "v_mfma_f32_16x16x32_bf16 v[162:165], a[52:55], a[216:219], v[162:165]\n"
        "ds_read_b128 a[216:219], v150 offset:256\n"
        "ds_read_b64_tr_b16 v[182:183], v232 offset:55328\n"
        "v_cvt_pk_bf16_f32 v198, v76, v77\n"
        "v_mfma_f32_16x16x32_bf16 v[162:165], a[56:59], a[220:223], v[162:165]\n"
        "ds_read_b128 a[220:223], v151 offset:256\n"
        "ds_read_b64_tr_b16 v[184:185], v232 offset:61472\n"
        "s_sub_u32 s31, s30, s28\n"
        "v_cvt_pk_bf16_f32 v199, v78, v79\n"
        "v_mfma_f32_16x16x32_bf16 v[162:165], a[60:63], a[224:227], v[162:165]\n"
        "s_sub_u32 s45, s28, s29\n"
        "v_exp_f32 v111, v111\n"
        "v_fma_f32 v112, v112, s24, v239\n"
        "v_mfma_f32_16x16x32_bf16 v[162:165], a[64:67], a[228:231], v[162:165]\n"
        "ds_read_b64_tr_b16 v[202:203], v233 offset:49184\n"
        "ds_read_b128 a[228:231], v150 offset:1536\n"
        "s_add_u32 s28, s28, s31\n"
        "v_cvt_pk_bf16_f32 v103, v110, v111\n"
        "v_mfma_f32_16x16x32_bf16 v[162:165], a[68:71], a[232:235], v[162:165]\n"
        "s_waitcnt lgkmcnt(15)\n"
        "buffer_load_dwordx4 v142, s[52:55], s64 offen offset:1024 lds\n"
        "ds_read_b128 a[232:235], v151 offset:1536\n"
        "v_fma_f32 v113, v113, s24, v239\n"
        "v_mfma_f32_16x16x32_bf16 v[72:75], a[236:239], a[144:147], 0\n"
        "s_waitcnt lgkmcnt(15)\n"
        "ds_read_b64_tr_b16 v[204:205], v233 offset:50720\n"
        "v_exp_f32 v112, v112\n"
        "v_mfma_f32_16x16x32_bf16 v[72:75], a[240:243], a[148:151], v[72:75]\n"
        "s_waitcnt lgkmcnt(15)\n"
        "v_exp_f32 v113, v113\n"
        "v_cvt_pk_bf16_f32 v200, v162, v163\n"
        "v_mfma_f32_16x16x32_bf16 v[72:75], a[244:247], a[152:155], v[72:75]\n"
        "s_waitcnt lgkmcnt(8)\n"
        "ds_read_b64_tr_b16 v[88:89], v232 offset:36928\n"
        "v_cvt_pk_bf16_f32 v201, v164, v165\n"
        "v_cvt_pk_bf16_f32 v206, v162, v163\n"
        "v_mfma_f32_16x16x32_bf16 v[72:75], a[248:251], a[156:159], v[72:75]\n"
        "s_waitcnt lgkmcnt(8)\n"
        "v_cvt_pk_bf16_f32 v207, v164, v165\n"
        "v_cvt_pk_bf16_f32 v112, v112, v113\n"
        "v_fma_f32 v114, v114, s24, v239\n"
        "v_mfma_f32_16x16x32_bf16 v[72:75], a[216:219], a[160:163], v[72:75]\n"
        "s_waitcnt lgkmcnt(6)\n"
        "buffer_load_dwordx4 v142, s[52:55], s64 offen offset:2048 lds\n"
        "s_add_u32 s41, s30, s40\n"
        "s_add_u32 s35, s47, 786432\n"
        "s_sub_u32 s46, s35, 100663296\n"
        "s_cmp_lt_u32 s46, 786432\n"
        "v_mfma_f32_16x16x32_bf16 v[72:75], a[220:223], a[164:167], v[72:75]\n"
        "s_cselect_b32 s35, s46, s35\n"
        "ds_read_b64_tr_b16 v[90:91], v232 offset:43072\n"
        "v_exp_f32 v114, v114\n"
        "v_mfma_f32_16x16x32_bf16 v[76:79], a[236:239], a[168:171], 0\n"
        "ds_read_b64_tr_b16 v[92:93], v232 offset:55360\n"
        "v_fma_f32 v115, v115, s24, v239\n"
        "v_fma_f32 v116, v116, s24, v239\n"
        "v_mfma_f32_16x16x32_bf16 v[76:79], a[240:243], a[172:175], v[76:79]\n"
        "v_exp_f32 v115, v115\n"
        "v_cvt_pk_bf16_f32 v128, v72, v73\n"
        "v_mfma_f32_16x16x32_bf16 v[76:79], a[244:247], a[176:179], v[76:79]\n"
        "ds_read_b64_tr_b16 v[94:95], v232 offset:61504\n"
        "v_exp_f32 v116, v116\n"
        "v_mfma_f32_16x16x32_bf16 v[76:79], a[248:251], a[180:183], v[76:79]\n"
        "s_mov_b32 m0, s48\n"
        "v_cvt_pk_bf16_f32 v129, v74, v75\n"
        "v_cvt_pk_bf16_f32 v113, v114, v115\n"
        "v_fma_f32 v117, v117, s24, v239\n"
        "v_mfma_f32_16x16x32_bf16 v[76:79], a[216:219], a[184:187], v[76:79]\n"
        "buffer_load_dwordx4 v249, s[12:15], s36 offen offset:0 lds\n"
        "ds_read_b64_tr_b16 v[104:105], v233 offset:49216\n"
        "v_mfma_f32_16x16x32_bf16 v[76:79], a[220:223], a[188:191], v[76:79]\n"
        "v_exp_f32 v117, v117\n"
        "v_fma_f32 v118, v118, s24, v239\n"
        "v_mfma_f32_16x16x32_bf16 v[162:165], a[236:239], a[192:195], 0\n"
        "ds_read_b128 a[236:239], v150 offset:1664\n"
        "ds_read_b64_tr_b16 v[106:107], v233 offset:50752\n"
        "v_cvt_pk_bf16_f32 v114, v116, v117\n"
        "v_mfma_f32_16x16x32_bf16 v[162:165], a[240:243], a[196:199], v[162:165]\n"
        "ds_read_b128 a[240:243], v151 offset:1664\n"
        "ds_read_b128 a[224:227], v151 offset:12288\n"
        "v_xor_b32 v253, 32, v249\n"
        "v_mfma_f32_16x16x32_bf16 v[162:165], a[244:247], a[200:203], v[162:165]\n"
        "ds_read_b128 a[244:247], v150 offset:1792\n"
        "v_exp_f32 v118, v118\n"
        "v_mfma_f32_16x16x32_bf16 v[162:165], a[248:251], a[204:207], v[162:165]\n"
        "ds_read_b128 a[248:251], v151 offset:1792\n"
        "buffer_load_dwordx4 v253, s[12:15], s36 offen offset:1024 lds\n"
        "s_mov_b32 s30, s29\n"
        "v_mfma_f32_16x16x32_bf16 v[162:165], a[216:219], a[208:211], v[162:165]\n"
        "s_waitcnt lgkmcnt(4)\n"
        "s_barrier\n"
        "ds_write_b128 v234, v[212:215] offset:36864\n"
        "v_mfma_f32_16x16x32_bf16 v[162:165], a[220:223], a[212:215], v[162:165]\n"
        "s_add_u32 s29, s29, s45\n"
        "ds_read_b128 a[220:223], v150 offset:12288\n"
        "v_cvt_pk_bf16_f32 v136, v76, v77\n"
        "v_mfma_f32_16x16x32_bf16 v[72:75], a[228:231], a[144:147], 0\n"
        "v_cvt_pk_bf16_f32 v137, v78, v79\n"
        "v_fma_f32 v119, v119, s24, v239\n"
        "v_mfma_f32_16x16x32_bf16 v[72:75], a[232:235], a[148:151], v[72:75]\n"
        "s_add_u32 m0, s41, 0\n"
        "v_exp_f32 v119, v119\n"
        "v_mfma_f32_16x16x32_bf16 v[72:75], a[236:239], a[152:155], v[72:75]\n"
        "s_waitcnt lgkmcnt(5)\n"
        "buffer_load_dwordx4 v242, s[8:11], s47 offen lds\n"
        "v_mfma_f32_16x16x32_bf16 v[72:75], a[240:243], a[156:159], v[72:75]\n"
        "s_waitcnt lgkmcnt(3)\n"
        "ds_write_b128 v234, v[216:219] offset:43008\n"
        "v_mfma_f32_16x16x32_bf16 v[72:75], a[244:247], a[160:163], v[72:75]\n"
        "s_waitcnt lgkmcnt(3)\n"
        "v_cvt_pk_bf16_f32 v144, v162, v163\n"
        "v_cvt_pk_bf16_f32 v145, v164, v165\n"
        "v_mfma_f32_16x16x32_bf16 v[72:75], a[248:251], a[164:167], v[72:75]\n"
        "v_cvt_pk_bf16_f32 v115, v118, v119\n"
        "v_fma_f32 v120, v120, s24, v239\n"
        "v_mfma_f32_16x16x32_bf16 v[76:79], a[228:231], a[168:171], 0\n"
        "ds_write_b128 v234, v[220:223] offset:49152\n"
        "v_mfma_f32_16x16x32_bf16 v[76:79], a[232:235], a[172:175], v[76:79]\n"
        "v_exp_f32 v120, v120\n"
        "v_mfma_f32_16x16x32_bf16 v[76:79], a[236:239], a[176:179], v[76:79]\n"
        "v_fma_f32 v121, v121, s24, v239\n"
        "v_cvt_pk_bf16_f32 v130, v72, v73\n"
        "v_mfma_f32_16x16x32_bf16 v[76:79], a[240:243], a[180:183], v[76:79]\n"
        "ds_write_b128 v234, v[224:227] offset:37120\n"
        "v_mfma_f32_16x16x32_bf16 v[76:79], a[244:247], a[184:187], v[76:79]\n"
        "v_cvt_pk_bf16_f32 v131, v74, v75\n"
        "v_fma_f32 v122, v122, s24, v239\n"
        "v_mfma_f32_16x16x32_bf16 v[76:79], a[248:251], a[188:191], v[76:79]\n"
        "v_exp_f32 v121, v121\n"
        "v_mfma_f32_16x16x32_bf16 v[162:165], a[228:231], a[192:195], 0\n"
        "ds_read_b128 a[228:231], v150 offset:12416\n"
        "v_cvt_pk_bf16_f32 v116, v120, v121\n"
        "v_mfma_f32_16x16x32_bf16 v[162:165], a[232:235], a[196:199], v[162:165]\n"
        "ds_read_b128 a[232:235], v151 offset:12416\n"
        "ds_read_b128 a[216:219], v151 offset:13824\n"
        "v_mfma_f32_16x16x32_bf16 v[162:165], a[236:239], a[200:203], v[162:165]\n"
        "ds_write_b128 v234, v[182:185] offset:43264\n"
        "v_mfma_f32_16x16x32_bf16 v[162:165], a[240:243], a[204:207], v[162:165]\n"
        "ds_read_b128 a[236:239], v150 offset:12544\n"
        "ds_read_b128 a[240:243], v151 offset:12544\n"
        "v_mfma_f32_16x16x32_bf16 v[162:165], a[244:247], a[208:211], v[162:165]\n"
        "v_cvt_pk_bf16_f32 v138, v76, v77\n"
        "v_cvt_pk_bf16_f32 v139, v78, v79\n"
        "v_mfma_f32_16x16x32_bf16 v[162:165], a[248:251], a[212:215], v[162:165]\n"
        "s_waitcnt lgkmcnt(9)\n"
        "ds_write_b128 v234, v[202:205] offset:49408\n"
        "v_mfma_f32_16x16x32_bf16 v[72:75], a[220:223], a[144:147], 0\n"
        "ds_read_b128 a[248:251], v150 offset:13824\n"
        "v_fma_f32 v123, v123, s24, v239\n"
        "v_mfma_f32_16x16x32_bf16 v[72:75], a[224:227], a[148:151], v[72:75]\n"
        "s_waitcnt lgkmcnt(7)\n"
        "v_exp_f32 v122, v122\n"
        "v_mfma_f32_16x16x32_bf16 v[72:75], a[228:231], a[152:155], v[72:75]\n"
        "s_waitcnt lgkmcnt(6)\n"
        "ds_write_b128 v234, v[88:91] offset:37376\n"
        "v_mfma_f32_16x16x32_bf16 v[72:75], a[232:235], a[156:159], v[72:75]\n"
        "s_waitcnt lgkmcnt(4)\n"
        "v_exp_f32 v123, v123\n"
        "v_mfma_f32_16x16x32_bf16 v[72:75], a[236:239], a[160:163], v[72:75]\n"
        "s_waitcnt lgkmcnt(3)\n"
        "v_cvt_pk_bf16_f32 v146, v162, v163\n"
        "v_cvt_pk_bf16_f32 v147, v164, v165\n"
        "v_mfma_f32_16x16x32_bf16 v[72:75], a[240:243], a[164:167], v[72:75]\n"
        "ds_write_b128 v234, v[92:95] offset:43520\n"
        "v_mfma_f32_16x16x32_bf16 v[76:79], a[220:223], a[168:171], 0\n"
        "v_cvt_pk_bf16_f32 v117, v122, v123\n"
        "v_fma_f32 v124, v124, s24, v239\n"
        "v_mfma_f32_16x16x32_bf16 v[76:79], a[224:227], a[172:175], v[76:79]\n"
        "v_fma_f32 v125, v125, s24, v239\n"
        "v_fma_f32 v126, v126, s24, v239\n"
        "v_mfma_f32_16x16x32_bf16 v[76:79], a[228:231], a[176:179], v[76:79]\n"
        "ds_write_b128 v234, v[104:107] offset:49664\n"
        "s_waitcnt vmcnt(3)\n"
        "s_barrier\n"
        "v_mfma_f32_16x16x32_bf16 v[76:79], a[232:235], a[180:183], v[76:79]\n"
        "ds_read_b64_tr_b16 v[108:109], v232 offset:0\n"
        "v_fma_f32 v127, v127, s24, v239\n"
        "v_mfma_f32_16x16x32_bf16 v[76:79], a[236:239], a[184:187], v[76:79]\n"
        "ds_read_b64_tr_b16 v[110:111], v232 offset:6144\n"
        "v_cvt_pk_bf16_f32 v132, v72, v73\n"
        "v_mfma_f32_16x16x32_bf16 v[76:79], a[240:243], a[188:191], v[76:79]\n"
        "v_exp_f32 v124, v124\n"
        "v_mfma_f32_16x16x32_bf16 v[162:165], a[220:223], a[192:195], 0\n"
        "ds_read_b128 a[220:223], v150 offset:13952\n"
        "ds_read_b64_tr_b16 v[120:121], v232 offset:18432\n"
        "v_mfma_f32_16x16x32_bf16 v[162:165], a[224:227], a[196:199], v[162:165]\n"
        "ds_read_b128 a[224:227], v151 offset:13952\n"
        "v_cvt_pk_bf16_f32 v133, v74, v75\n"
        "v_mfma_f32_16x16x32_bf16 v[162:165], a[228:231], a[200:203], v[162:165]\n"
        "ds_read_b128 a[228:231], v150 offset:14080\n"
        "ds_read_b64_tr_b16 v[122:123], v232 offset:24576\n"
        "v_mfma_f32_16x16x32_bf16 v[162:165], a[232:235], a[204:207], v[162:165]\n"
        "ds_read_b128 a[232:235], v151 offset:14080\n"
        "v_cvt_pk_bf16_f32 v140, v76, v77\n"
        "v_mfma_f32_16x16x32_bf16 v[162:165], a[236:239], a[208:211], v[162:165]\n"
        "ds_read_b128 a[236:239], v240 offset:13824\n"
        "ds_read_b128 a[244:247], v240 offset:13952\n"
        "v_mfma_f32_16x16x32_bf16 v[162:165], a[240:243], a[212:215], v[162:165]\n"
        "s_waitcnt lgkmcnt(13)\n"
        "ds_read_b128 a[240:243], v241 offset:13824\n"
        "v_cvt_pk_bf16_f32 v141, v78, v79\n"
        "v_mfma_f32_16x16x32_bf16 v[72:75], a[248:251], a[144:147], 0\n"
        "v_exp_f32 v125, v125\n"
        "v_mfma_f32_16x16x32_bf16 v[72:75], a[216:219], a[148:151], v[72:75]\n"
        "s_waitcnt lgkmcnt(8)\n"
        "v_cvt_pk_bf16_f32 v118, v124, v125\n"
        "v_mfma_f32_16x16x32_bf16 v[72:75], a[220:223], a[152:155], v[72:75]\n"
        "s_waitcnt lgkmcnt(6)\n"
        "ds_read_b64_tr_b16 v[124:125], v233 offset:12288\n"
        "ds_read_b64_tr_b16 v[222:223], v232 offset:32\n"
        "v_mfma_f32_16x16x32_bf16 v[72:75], a[224:227], a[156:159], v[72:75]\n"
        "s_waitcnt lgkmcnt(7)\n"
        "v_exp_f32 v126, v126\n"
        "v_mfma_f32_16x16x32_bf16 v[72:75], a[228:231], a[160:163], v[72:75]\n"
        "s_waitcnt lgkmcnt(5)\n"
        "ds_read_b64_tr_b16 v[224:225], v232 offset:6176\n"
        "v_cvt_pk_bf16_f32 v148, v162, v163\n"
        "v_mfma_f32_16x16x32_bf16 v[72:75], a[232:235], a[164:167], v[72:75]\n"
        "v_exp_f32 v127, v127\n"
        "v_mfma_f32_16x16x32_bf16 v[76:79], a[248:251], a[168:171], 0\n"
        "ds_read_b64_tr_b16 v[182:183], v232 offset:18464\n"
        "v_cvt_pk_bf16_f32 v119, v126, v127\n"
        "v_mfma_f32_16x16x32_bf16 v[76:79], a[216:219], a[172:175], v[76:79]\n"
        "ds_read_b64_tr_b16 v[126:127], v233 offset:13824\n"
        "v_cvt_pk_bf16_f32 v149, v164, v165\n"
        "v_mfma_f32_16x16x32_bf16 v[76:79], a[220:223], a[176:179], v[76:79]\n"
        "ds_read_b64_tr_b16 v[184:185], v232 offset:24608\n"
        "v_cvt_pk_bf16_f32 v134, v72, v73\n"
        "v_mfma_f32_16x16x32_bf16 v[76:79], a[224:227], a[180:183], v[76:79]\n"
        "v_cvt_pk_bf16_f32 v135, v74, v75\n"
        "v_mfma_f32_16x16x32_bf16 v[76:79], a[228:231], a[184:187], v[76:79]\n"
        "ds_read_b64_tr_b16 v[202:203], v233 offset:12320\n"
        "v_mfma_f32_16x16x32_bf16 v[76:79], a[232:235], a[188:191], v[76:79]\n"
        "v_mfma_f32_16x16x32_bf16 v[162:165], a[248:251], a[192:195], 0\n"
        "ds_read_b128 a[248:251], v241 offset:13952\n"
        "ds_read_b64_tr_b16 v[204:205], v233 offset:13856\n"
        "v_mfma_f32_16x16x32_bf16 v[162:165], a[216:219], a[196:199], v[162:165]\n"
        "ds_read_b128 a[216:219], v240 offset:14080\n"
        "v_mfma_f32_16x16x32_bf16 v[162:165], a[220:223], a[200:203], v[162:165]\n"
        "ds_read_b128 a[220:223], v241 offset:14080\n"
        "ds_read_b64_tr_b16 v[88:89], v232 offset:64\n"
        "v_mfma_f32_16x16x32_bf16 v[162:165], a[224:227], a[204:207], v[162:165]\n"
        "v_cvt_pk_bf16_f32 v142, v76, v77\n"
        "v_cvt_pk_bf16_f32 v143, v78, v79\n"
        "v_mfma_f32_16x16x32_bf16 v[162:165], a[228:231], a[208:211], v[162:165]\n"
        "ds_read_b64_tr_b16 v[90:91], v232 offset:6208\n"
        "v_mfma_f32_16x16x32_bf16 v[162:165], a[232:235], a[212:215], v[162:165]\n"
        "s_waitcnt lgkmcnt(15)\n"
        "v_mfma_f32_16x16x32_bf16 v[72:75], a[72:75], a[236:239], 0\n"
        "s_waitcnt lgkmcnt(13)\n"
        "ds_read_b64_tr_b16 v[92:93], v232 offset:18496\n"
        "v_mfma_f32_16x16x32_bf16 v[72:75], a[76:79], a[240:243], v[72:75]\n"
        "v_mfma_f32_16x16x32_bf16 v[72:75], a[80:83], a[244:247], v[72:75]\n"
        "s_waitcnt lgkmcnt(6)\n"
        "ds_read_b64_tr_b16 v[94:95], v232 offset:24640\n"
        "v_mfma_f32_16x16x32_bf16 v[72:75], a[84:87], a[248:251], v[72:75]\n"
        "s_waitcnt lgkmcnt(5)\n"
        "v_mfma_f32_16x16x32_bf16 v[72:75], a[88:91], a[216:219], v[72:75]\n"
        "s_waitcnt lgkmcnt(4)\n"
        "ds_read_b64_tr_b16 v[104:105], v233 offset:12352\n"
        "v_cvt_pk_bf16_f32 v150, v162, v163\n"
        "v_mfma_f32_16x16x32_bf16 v[72:75], a[92:95], a[220:223], v[72:75]\n"
        "v_cvt_pk_bf16_f32 v151, v164, v165\n"
        "v_mfma_f32_16x16x32_bf16 v[76:79], a[96:99], a[236:239], 0\n"
        "s_add_u32 m0, s41, 1024\n"
        "ds_read_b64_tr_b16 v[106:107], v233 offset:13888\n"
        "v_mfma_f32_16x16x32_bf16 v[76:79], a[100:103], a[240:243], v[76:79]\n"
        "v_mfma_f32_16x16x32_bf16 v[76:79], a[104:107], a[244:247], v[76:79]\n"
        "buffer_load_dwordx4 v243, s[8:11], s47 offen lds\n"
        "s_waitcnt lgkmcnt(0)\n"
        "s_barrier\n"
        "v_mfma_f32_16x16x32_bf16 v[76:79], a[108:111], a[248:251], v[76:79]\n"
        "ds_write_b128 v234, v[108:111] offset:0\n"
        "v_mfma_f32_16x16x32_bf16 v[76:79], a[112:115], a[216:219], v[76:79]\n"
        "v_cvt_pk_bf16_f32 v212, v72, v73\n"
        "v_cvt_pk_bf16_f32 v213, v74, v75\n"
        "v_mfma_f32_16x16x32_bf16 v[76:79], a[116:119], a[220:223], v[76:79]\n"
        "v_mfma_f32_16x16x32_bf16 v[162:165], a[120:123], a[236:239], 0\n"
        "ds_write_b128 v234, v[120:123] offset:6144\n"
        "v_mfma_f32_16x16x32_bf16 v[162:165], a[124:127], a[240:243], v[162:165]\n"
        "v_mfma_f32_16x16x32_bf16 v[162:165], a[128:131], a[244:247], v[162:165]\n"
        "v_mfma_f32_16x16x32_bf16 v[162:165], a[132:135], a[248:251], v[162:165]\n"
        "ds_write_b128 v234, v[124:127] offset:12288\n"
        "v_mfma_f32_16x16x32_bf16 v[162:165], a[136:139], a[216:219], v[162:165]\n"
        "v_mfma_f32_16x16x32_bf16 v[162:165], a[140:143], a[220:223], v[162:165]\n"
        "v_cvt_pk_bf16_f32 v214, v76, v77\n"
        "v_cvt_pk_bf16_f32 v215, v78, v79\n"
        "v_mfma_f32_16x16x32_bf16 v[72:75], a[0:3], a[236:239], 0\n"
        "ds_write_b128 v234, v[222:225] offset:256\n"
        "v_mfma_f32_16x16x32_bf16 v[72:75], a[4:7], a[240:243], v[72:75]\n"
        "v_mfma_f32_16x16x32_bf16 v[72:75], a[8:11], a[244:247], v[72:75]\n"
        "s_add_u32 m0, s41, 2048\n"
        "v_mfma_f32_16x16x32_bf16 v[72:75], a[12:15], a[248:251], v[72:75]\n"
        "ds_write_b128 v234, v[182:185] offset:6400\n"
        "v_mfma_f32_16x16x32_bf16 v[72:75], a[16:19], a[216:219], v[72:75]\n"
        "buffer_load_dwordx4 v244, s[8:11], s47 offen lds\n"
        "v_mfma_f32_16x16x32_bf16 v[72:75], a[20:23], a[220:223], v[72:75]\n"
        "v_cvt_pk_bf16_f32 v230, v162, v163\n"
        "v_cvt_pk_bf16_f32 v231, v164, v165\n"
        "v_mfma_f32_16x16x32_bf16 v[76:79], a[24:27], a[236:239], 0\n"
        "ds_write_b128 v234, v[202:205] offset:12544\n"
        "v_mfma_f32_16x16x32_bf16 v[76:79], a[28:31], a[240:243], v[76:79]\n"
        "v_mfma_f32_16x16x32_bf16 v[76:79], a[32:35], a[244:247], v[76:79]\n"
        "v_mfma_f32_16x16x32_bf16 v[76:79], a[36:39], a[248:251], v[76:79]\n"
        "ds_write_b128 v234, v[88:91] offset:512\n"
        "v_mfma_f32_16x16x32_bf16 v[76:79], a[40:43], a[216:219], v[76:79]\n"
        "v_cvt_pk_bf16_f32 v216, v72, v73\n"
        "v_cvt_pk_bf16_f32 v217, v74, v75\n"
        "v_mfma_f32_16x16x32_bf16 v[76:79], a[44:47], a[220:223], v[76:79]\n"
        "v_mfma_f32_16x16x32_bf16 v[162:165], a[48:51], a[236:239], 0\n"
        "ds_write_b128 v234, v[92:95] offset:6656\n"
        "v_mfma_f32_16x16x32_bf16 v[162:165], a[52:55], a[240:243], v[162:165]\n"
        "v_mfma_f32_16x16x32_bf16 v[162:165], a[56:59], a[244:247], v[162:165]\n"
        "v_mfma_f32_16x16x32_bf16 v[162:165], a[60:63], a[248:251], v[162:165]\n"
        "ds_write_b128 v234, v[104:107] offset:12800\n"
        "v_mfma_f32_16x16x32_bf16 v[162:165], a[64:67], a[216:219], v[162:165]\n"
        "s_add_u32 m0, s41, 12288\n"
        "v_cvt_pk_bf16_f32 v218, v76, v77\n"
        "v_cvt_pk_bf16_f32 v219, v78, v79\n"
        "v_mfma_f32_16x16x32_bf16 v[162:165], a[68:71], a[220:223], v[162:165]\n"
        "v_mfma_f32_16x16x32_bf16 v[88:91], a[252:255], v[64:67], 0\n"
        "buffer_load_dwordx4 v242, s[8:11], s35 offen lds\n"
        "v_mfma_f32_16x16x32_bf16 v[88:91], a[252:255], v[68:71], v[88:91]\n"
        "v_mfma_f32_16x16x32_bf16 v[92:95], v[132:135], v[68:71], 0\n"
        "v_mfma_f32_16x16x32_bf16 v[92:95], v[128:131], v[64:67], v[92:95]\n"
        "v_mfma_f32_16x16x32_bf16 v[182:185], v[136:139], v[64:67], 0\n"
        "v_mfma_f32_16x16x32_bf16 v[182:185], v[140:143], v[68:71], v[182:185]\n"
        "v_mfma_f32_16x16x32_bf16 v[202:205], v[148:151], v[68:71], 0\n"
        "s_cmp_eq_u32 s37, 100663296\n"
        "v_cvt_pk_bf16_f32 v220, v162, v163\n"
        "v_cvt_pk_bf16_f32 v221, v164, v165\n"
        "v_mfma_f32_16x16x32_bf16 v[202:205], v[144:147], v[64:67], v[202:205]\n"
        "s_cselect_b32 s46, 0, s37\n"
        "v_cvt_pk_bf16_f32 v226, v162, v163\n"
        "v_cvt_pk_bf16_f32 v227, v164, v165\n"
        "v_mfma_f32_16x16x32_bf16 v[72:75], a[252:255], v[80:83], 0\n"
        "s_add_u32 s38, s46, s43\n"
        "v_rcp_f32 v88, v88\n"
        "v_mfma_f32_16x16x32_bf16 v[72:75], a[252:255], v[84:87], v[72:75]\n"
        "s_add_u32 s39, s46, s44\n"
        "v_mul_f32 v92, v88, v92\n"
        "v_mul_f32 v93, v88, v93\n"
        "v_mfma_f32_16x16x32_bf16 v[76:79], v[132:135], v[84:87], 0\n"
        "s_add_u32 s37, s37, 6291456\n"
        "v_mul_f32 v94, v88, v94\n"
        "v_mul_f32 v95, v88, v95\n"
        "v_mfma_f32_16x16x32_bf16 v[76:79], v[128:131], v[80:83], v[76:79]\n"
        "v_rcp_f32 v72, v72\n"
        "v_mfma_f32_16x16x32_bf16 v[104:107], v[136:139], v[80:83], 0\n"
        "s_add_u32 m0, s41, 13312\n"
        "v_cvt_pk_bf16_f32 v92, v92, v93\n"
        "v_cvt_pk_bf16_f32 v93, v94, v95\n"
        "v_mfma_f32_16x16x32_bf16 v[104:107], v[140:143], v[84:87], v[104:107]\n"
        "v_mul_f32 v182, v88, v182\n"
        "v_mul_f32 v183, v88, v183\n"
        "v_mfma_f32_16x16x32_bf16 v[108:111], v[148:151], v[84:87], 0\n"
        "buffer_load_dwordx4 v243, s[8:11], s35 offen lds\n"
        "v_mfma_f32_16x16x32_bf16 v[108:111], v[144:147], v[80:83], v[108:111]\n"
        "v_mul_f32 v184, v88, v184\n"
        "v_mul_f32 v185, v88, v185\n"
        "v_mfma_f32_16x16x32_bf16 v[64:67], a[252:255], v[96:99], 0\n"
        "v_cvt_pk_bf16_f32 v94, v182, v183\n"
        "v_cvt_pk_bf16_f32 v95, v184, v185\n"
        "v_mfma_f32_16x16x32_bf16 v[64:67], a[252:255], v[100:103], v[64:67]\n"
        "ds_write_b128 v245, v[92:95] offset:0\n"
        "v_mfma_f32_16x16x32_bf16 v[68:71], v[132:135], v[100:103], 0\n"
        "v_mul_f32 v202, v88, v202\n"
        "v_mul_f32 v203, v88, v203\n"
        "v_mfma_f32_16x16x32_bf16 v[68:71], v[128:131], v[96:99], v[68:71]\n"
        "v_mul_f32 v204, v88, v204\n"
        "v_mul_f32 v205, v88, v205\n"
        "v_mfma_f32_16x16x32_bf16 v[120:123], v[136:139], v[96:99], 0\n"
        "v_cvt_pk_bf16_f32 v202, v202, v203\n"
        "v_cvt_pk_bf16_f32 v203, v204, v205\n"
        "v_mfma_f32_16x16x32_bf16 v[120:123], v[140:143], v[100:103], v[120:123]\n"
        "v_mul_f32 v76, v72, v76\n"
        "v_mul_f32 v77, v72, v77\n"
        "v_mfma_f32_16x16x32_bf16 v[124:127], v[148:151], v[100:103], 0\n"
        "ds_write_b64 v246, v[202:203] offset:0\n"
        "v_mfma_f32_16x16x32_bf16 v[124:127], v[144:147], v[96:99], v[124:127]\n"
        "v_rcp_f32 v64, v64\n"
        "v_mfma_f32_16x16x32_bf16 v[80:83], a[252:255], v[112:115], 0\n"
        "v_mul_f32 v78, v72, v78\n"
        "v_mul_f32 v79, v72, v79\n"
        "v_mfma_f32_16x16x32_bf16 v[80:83], a[252:255], v[116:119], v[80:83]\n"
        "v_cvt_pk_bf16_f32 v76, v76, v77\n"
        "v_cvt_pk_bf16_f32 v77, v78, v79\n"
        "v_mfma_f32_16x16x32_bf16 v[84:87], v[132:135], v[116:119], 0\n"
        "s_add_u32 m0, s41, 14336\n"
        "v_mul_f32 v104, v72, v104\n"
        "v_mul_f32 v105, v72, v105\n"
        "v_mfma_f32_16x16x32_bf16 v[84:87], v[128:131], v[112:115], v[84:87]\n"
        "v_mul_f32 v106, v72, v106\n"
        "v_mul_f32 v107, v72, v107\n"
        "v_mfma_f32_16x16x32_bf16 v[222:225], v[136:139], v[112:115], 0\n"
        "buffer_load_dwordx4 v244, s[8:11], s35 offen lds\n"
        "v_mfma_f32_16x16x32_bf16 v[222:225], v[140:143], v[116:119], v[222:225]\n"
        "ds_read_b128 a[224:227], v240 offset:0\n"
        "ds_read_b128 a[228:231], v241 offset:0\n"
        "v_mfma_f32_16x16x32_bf16 v[162:165], v[148:151], v[116:119], 0\n"
        "ds_read_b128 a[232:235], v240 offset:128\n"
        "v_cvt_pk_bf16_f32 v78, v104, v105\n"
        "v_mfma_f32_16x16x32_bf16 v[162:165], v[144:147], v[112:115], v[162:165]\n"
        "v_cvt_pk_bf16_f32 v79, v106, v107\n"
        "v_mul_f32 v108, v72, v108\n"
        "v_mfma_f32_16x16x32_bf16 v[96:99], a[236:239], a[144:147], 0\n"
        "ds_write_b128 v245, v[76:79] offset:256\n"
        "v_mfma_f32_16x16x32_bf16 v[96:99], a[240:243], a[148:151], v[96:99]\n"
        "v_mul_f32 v109, v72, v109\n"
        "v_mul_f32 v110, v72, v110\n"
        "v_mfma_f32_16x16x32_bf16 v[96:99], a[244:247], a[152:155], v[96:99]\n"
        "v_mul_f32 v111, v72, v111\n"
        "v_cvt_pk_bf16_f32 v108, v108, v109\n"
        "v_mfma_f32_16x16x32_bf16 v[96:99], a[248:251], a[156:159], v[96:99]\n"
        "v_cvt_pk_bf16_f32 v109, v110, v111\n"
        "v_mul_f32 v68, v64, v68\n"
        "v_mfma_f32_16x16x32_bf16 v[96:99], a[216:219], a[160:163], v[96:99]\n"
        "v_rcp_f32 v80, v80\n"
        "v_mfma_f32_16x16x32_bf16 v[96:99], a[220:223], a[164:167], v[96:99]\n"
        "ds_write_b64 v246, v[108:109] offset:256\n"
        "v_mfma_f32_16x16x32_bf16 v[100:103], a[236:239], a[168:171], 0\n"
        "v_mul_f32 v69, v64, v69\n"
        "v_mul_f32 v70, v64, v70\n"
        "v_mfma_f32_16x16x32_bf16 v[100:103], a[240:243], a[172:175], v[100:103]\n"
        "v_mul_f32 v71, v64, v71\n"
        "v_cvt_pk_bf16_f32 v68, v68, v69\n"
        "v_mfma_f32_16x16x32_bf16 v[100:103], a[244:247], a[176:179], v[100:103]\n"
        "v_cvt_pk_bf16_f32 v134, v96, v97\n"
        "v_cvt_pk_bf16_f32 v135, v98, v99\n"
        "v_mfma_f32_16x16x32_bf16 v[100:103], a[248:251], a[180:183], v[100:103]\n"
        "v_cvt_pk_bf16_f32 v69, v70, v71\n"
        "v_mul_f32 v120, v64, v120\n"
        "v_mfma_f32_16x16x32_bf16 v[100:103], a[216:219], a[184:187], v[100:103]\n"
        "v_mul_f32 v121, v64, v121\n"
        "v_mul_f32 v122, v64, v122\n"
        "v_mfma_f32_16x16x32_bf16 v[100:103], a[220:223], a[188:191], v[100:103]\n"
        "v_mul_f32 v123, v64, v123\n"
        "v_cvt_pk_bf16_f32 v70, v120, v121\n"
        "v_mfma_f32_16x16x32_bf16 v[112:115], a[236:239], a[192:195], 0\n"
        "ds_read_b128 a[236:239], v241 offset:128\n"
        "v_cvt_pk_bf16_f32 v71, v122, v123\n"
        "v_mfma_f32_16x16x32_bf16 v[112:115], a[240:243], a[196:199], v[112:115]\n"
        "ds_read_b128 a[240:243], v240 offset:256\n"
        "v_mul_f32 v124, v64, v124\n"
        "v_mfma_f32_16x16x32_bf16 v[112:115], a[244:247], a[200:203], v[112:115]\n"
        "ds_read_b128 a[244:247], v241 offset:256\n"
        "v_cvt_pk_bf16_f32 v142, v100, v101\n"
        "v_mfma_f32_16x16x32_bf16 v[112:115], a[248:251], a[204:207], v[112:115]\n"
        "ds_read_b128 a[248:251], v240 offset:1536\n"
        "v_cvt_pk_bf16_f32 v143, v102, v103\n"
        "v_mfma_f32_16x16x32_bf16 v[112:115], a[216:219], a[208:211], v[112:115]\n"
        "ds_read_b128 a[216:219], v241 offset:1536\n"
        "v_mul_f32 v125, v64, v125\n"
        "v_mfma_f32_16x16x32_bf16 v[112:115], a[220:223], a[212:215], v[112:115]\n"
        "s_waitcnt lgkmcnt(9)\n"
        "ds_read_b128 a[220:223], v240 offset:1664\n"
        "v_mul_f32 v126, v64, v126\n"
        "v_mfma_f32_16x16x32_bf16 v[96:99], a[224:227], a[144:147], 0\n"
        "s_waitcnt lgkmcnt(9)\n"
        "ds_write_b128 v245, v[68:71] offset:512\n"
        "v_mfma_f32_16x16x32_bf16 v[96:99], a[228:231], a[148:151], v[96:99]\n"
        "s_waitcnt lgkmcnt(9)\n"
        "v_mul_f32 v127, v64, v127\n"
        "v_cvt_pk_bf16_f32 v124, v124, v125\n"
        "v_mfma_f32_16x16x32_bf16 v[96:99], a[232:235], a[152:155], v[96:99]\n"
        "s_waitcnt lgkmcnt(6)\n"
        "v_cvt_pk_bf16_f32 v150, v112, v113\n"
        "v_cvt_pk_bf16_f32 v151, v114, v115\n"
        "v_mfma_f32_16x16x32_bf16 v[96:99], a[236:239], a[156:159], v[96:99]\n"
        "s_waitcnt lgkmcnt(5)\n"
        "v_cvt_pk_bf16_f32 v125, v126, v127\n"
        "v_mul_f32 v84, v80, v84\n"
        "v_mfma_f32_16x16x32_bf16 v[96:99], a[240:243], a[160:163], v[96:99]\n"
        "s_waitcnt lgkmcnt(4)\n"
        "v_mul_f32 v85, v80, v85\n"
        "v_mul_f32 v86, v80, v86\n"
        "v_mfma_f32_16x16x32_bf16 v[96:99], a[244:247], a[164:167], v[96:99]\n"
        "ds_write_b64 v246, v[124:125] offset:512\n"
        "v_mfma_f32_16x16x32_bf16 v[100:103], a[224:227], a[168:171], 0\n"
        "v_mul_f32 v87, v80, v87\n"
        "v_cvt_pk_bf16_f32 v84, v84, v85\n"
        "v_mfma_f32_16x16x32_bf16 v[100:103], a[228:231], a[172:175], v[100:103]\n"
        "v_cvt_pk_bf16_f32 v85, v86, v87\n"
        "v_mul_f32 v222, v80, v222\n"
        "v_mfma_f32_16x16x32_bf16 v[100:103], a[232:235], a[176:179], v[100:103]\n"
        "v_cvt_pk_bf16_f32 v128, v96, v97\n"
        "v_cvt_pk_bf16_f32 v129, v98, v99\n"
        "v_mfma_f32_16x16x32_bf16 v[100:103], a[236:239], a[180:183], v[100:103]\n"
        "v_mul_f32 v223, v80, v223\n"
        "v_mul_f32 v224, v80, v224\n"
        "v_mfma_f32_16x16x32_bf16 v[100:103], a[240:243], a[184:187], v[100:103]\n"
        "v_mul_f32 v225, v80, v225\n"
        "v_cvt_pk_bf16_f32 v86, v222, v223\n"
        "v_mfma_f32_16x16x32_bf16 v[100:103], a[244:247], a[188:191], v[100:103]\n"
        "v_cvt_pk_bf16_f32 v87, v224, v225\n"
        "v_mul_f32 v162, v80, v162\n"
        "v_mfma_f32_16x16x32_bf16 v[112:115], a[224:227], a[192:195], 0\n"
        "ds_read_b128 a[224:227], v241 offset:1664\n"
        "v_mul_f32 v163, v80, v163\n"
        "v_mfma_f32_16x16x32_bf16 v[112:115], a[228:231], a[196:199], v[112:115]\n"
        "ds_read_b128 a[228:231], v240 offset:1792\n"
        "v_mul_f32 v164, v80, v164\n"
        "v_mfma_f32_16x16x32_bf16 v[112:115], a[232:235], a[200:203], v[112:115]\n"
        "ds_read_b128 a[232:235], v241 offset:1792\n"
        "v_cvt_pk_bf16_f32 v136, v100, v101\n"
        "v_mfma_f32_16x16x32_bf16 v[112:115], a[236:239], a[204:207], v[112:115]\n"
        "ds_read_b128 a[236:239], v240 offset:12288\n"
        "v_cvt_pk_bf16_f32 v137, v102, v103\n"
        "v_mfma_f32_16x16x32_bf16 v[112:115], a[240:243], a[208:211], v[112:115]\n"
        "ds_read_b128 a[240:243], v241 offset:12288\n"
        "v_mul_f32 v165, v80, v165\n"
        "v_mfma_f32_16x16x32_bf16 v[112:115], a[244:247], a[212:215], v[112:115]\n"
        "s_waitcnt lgkmcnt(9)\n"
        "ds_read_b128 a[244:247], v240 offset:12416\n"
        "v_cvt_pk_bf16_f32 v162, v162, v163\n"
        "v_mfma_f32_16x16x32_bf16 v[96:99], a[248:251], a[144:147], 0\n"
        "s_waitcnt lgkmcnt(9)\n"
        "ds_write_b128 v245, v[84:87] offset:768\n"
        "v_mfma_f32_16x16x32_bf16 v[96:99], a[216:219], a[148:151], v[96:99]\n"
        "s_waitcnt lgkmcnt(9)\n"
        "v_cvt_pk_bf16_f32 v163, v164, v165\n"
        "v_mfma_f32_16x16x32_bf16 v[96:99], a[220:223], a[152:155], v[96:99]\n"
        "s_waitcnt lgkmcnt(6)\n"
        "v_mfma_f32_16x16x32_bf16 v[96:99], a[224:227], a[156:159], v[96:99]\n"
        "s_waitcnt lgkmcnt(5)\n"
        "v_cvt_pk_bf16_f32 v144, v112, v113\n"
        "v_cvt_pk_bf16_f32 v145, v114, v115\n"
        "v_mfma_f32_16x16x32_bf16 v[96:99], a[228:231], a[160:163], v[96:99]\n"
        "s_waitcnt lgkmcnt(4)\n"
        "v_mfma_f32_16x16x32_bf16 v[96:99], a[232:235], a[164:167], v[96:99]\n"
        "ds_write_b64 v246, v[162:163] offset:768\n"
        "v_mfma_f32_16x16x32_bf16 v[100:103], a[248:251], a[168:171], 0\n"
        "v_mfma_f32_16x16x32_bf16 v[100:103], a[216:219], a[172:175], v[100:103]\n"
        "v_mfma_f32_16x16x32_bf16 v[100:103], a[220:223], a[176:179], v[100:103]\n"
        "v_mfma_f32_16x16x32_bf16 v[100:103], a[224:227], a[180:183], v[100:103]\n"
        "v_mfma_f32_16x16x32_bf16 v[100:103], a[228:231], a[184:187], v[100:103]\n"
        "v_mfma_f32_16x16x32_bf16 v[100:103], a[232:235], a[188:191], v[100:103]\n"
        "v_mfma_f32_16x16x32_bf16 v[112:115], a[248:251], a[192:195], 0\n"
        "ds_read_b128 a[248:251], v241 offset:12416\n"
        "ds_read_b128 v[162:165], v251 offset:0\n"
        "v_mfma_f32_16x16x32_bf16 v[112:115], a[216:219], a[196:199], v[112:115]\n"
        "ds_read_b128 a[216:219], v240 offset:12544\n"
        "v_cvt_pk_bf16_f32 v130, v96, v97\n"
        "v_mfma_f32_16x16x32_bf16 v[112:115], a[220:223], a[200:203], v[112:115]\n"
        "ds_read_b128 a[220:223], v241 offset:12544\n"
        "v_cvt_pk_bf16_f32 v131, v98, v99\n"
        "v_mfma_f32_16x16x32_bf16 v[112:115], a[224:227], a[204:207], v[112:115]\n"
        "v_cvt_pk_bf16_f32 v138, v100, v101\n"
        "v_cvt_pk_bf16_f32 v139, v102, v103\n"
        "v_mfma_f32_16x16x32_bf16 v[112:115], a[228:231], a[208:211], v[112:115]\n"
        "ds_read_b128 v[182:185], v251 offset:512\n"
        "v_mfma_f32_16x16x32_bf16 v[112:115], a[232:235], a[212:215], v[112:115]\n"
        "s_waitcnt lgkmcnt(9)\n"
        "v_mfma_f32_16x16x32_bf16 v[96:99], a[236:239], a[144:147], 0\n"
        "s_waitcnt lgkmcnt(8)\n"
        "v_mfma_f32_16x16x32_bf16 v[96:99], a[240:243], a[148:151], v[96:99]\n"
        "s_waitcnt lgkmcnt(7)\n"
        "v_mfma_f32_16x16x32_bf16 v[96:99], a[244:247], a[152:155], v[96:99]\n"
        "s_waitcnt lgkmcnt(4)\n"
        "ds_read_b128 v[202:205], v251 offset:4096\n"
        "v_mfma_f32_16x16x32_bf16 v[96:99], a[248:251], a[156:159], v[96:99]\n"
        "s_waitcnt lgkmcnt(3)\n"
        "v_mfma_f32_16x16x32_bf16 v[96:99], a[216:219], a[160:163], v[96:99]\n"
        "s_waitcnt lgkmcnt(2)\n"
        "v_mfma_f32_16x16x32_bf16 v[96:99], a[220:223], a[164:167], v[96:99]\n"
        "s_add_u32 s47, s47, 6291456\n"
        "v_cvt_pk_bf16_f32 v146, v112, v113\n"
        "v_cvt_pk_bf16_f32 v147, v114, v115\n"
        "v_mfma_f32_16x16x32_bf16 v[100:103], a[236:239], a[168:171], 0\n"
        "ds_read_b128 v[222:225], v251 offset:4608\n"
        "s_add_u32 s36, s36, 2097152\n"
        "v_mfma_f32_16x16x32_bf16 v[100:103], a[240:243], a[172:175], v[100:103]\n"
        "s_xor_b32 s48, s48, 8192\n"
        "v_cvt_pk_bf16_f32 v132, v96, v97\n"
        "v_cvt_pk_bf16_f32 v133, v98, v99\n"
        "v_mfma_f32_16x16x32_bf16 v[100:103], a[244:247], a[176:179], v[100:103]\n"
        "v_pk_min_u16 v162, v162, s26\n"
        "v_pk_min_u16 v163, v163, s26\n"
        "v_mfma_f32_16x16x32_bf16 v[100:103], a[248:251], a[180:183], v[100:103]\n"
        "v_pk_min_u16 v164, v164, s26\n"
        "v_pk_min_u16 v165, v165, s26\n"
        "v_mfma_f32_16x16x32_bf16 v[100:103], a[216:219], a[184:187], v[100:103]\n"
        "v_add_u32 v240, s31, v240\n"
        "v_add_u32 v241, s31, v241\n"
        "v_mfma_f32_16x16x32_bf16 v[100:103], a[220:223], a[188:191], v[100:103]\n"
        "v_mfma_f32_16x16x32_bf16 v[112:115], a[236:239], a[192:195], 0\n"
        "s_waitcnt lgkmcnt(2)\n"
        "v_pk_min_u16 v182, v182, s26\n"
        "v_pk_min_u16 v183, v183, s26\n"
        "v_mfma_f32_16x16x32_bf16 v[112:115], a[240:243], a[196:199], v[112:115]\n"
        "v_pk_min_u16 v184, v184, s26\n"
        "v_pk_min_u16 v185, v185, s26\n"
        "v_mfma_f32_16x16x32_bf16 v[112:115], a[244:247], a[200:203], v[112:115]\n"
        "v_mfma_f32_16x16x32_bf16 v[112:115], a[248:251], a[204:207], v[112:115]\n"
        "v_cvt_pk_bf16_f32 v140, v100, v101\n"
        "v_cvt_pk_bf16_f32 v141, v102, v103\n"
        "v_mfma_f32_16x16x32_bf16 v[112:115], a[216:219], a[208:211], v[112:115]\n"
        "s_waitcnt lgkmcnt(1)\n"
        "v_pk_min_u16 v202, v202, s26\n"
        "v_pk_min_u16 v203, v203, s26\n"
        "v_mfma_f32_16x16x32_bf16 v[112:115], a[220:223], a[212:215], v[112:115]\n"
        "v_pk_min_u16 v204, v204, s26\n"
        "v_pk_min_u16 v205, v205, s26\n"
        "v_mfma_f32_16x16x32_bf16 v[64:67], v[152:155], v[156:159], v[0:3]\n"
        "v_mfma_f32_16x16x32_bf16 v[64:67], v[168:171], v[160:163], v[64:67]\n"
        "v_mfma_f32_16x16x32_bf16 v[68:71], v[172:175], v[156:159], v[4:7]\n"
        "s_waitcnt lgkmcnt(0)\n"
        "v_pk_min_u16 v222, v222, s26\n"
        "v_pk_min_u16 v223, v223, s26\n"
        "v_mfma_f32_16x16x32_bf16 v[68:71], v[188:191], v[164:167], v[68:71]\n"
        "ds_read_b128 v[162:165], v252 offset:0\n"
        "v_cvt_pk_bf16_f32 v148, v112, v113\n"
        "v_mfma_f32_16x16x32_bf16 v[80:83], v[152:155], v[176:179], v[16:19]\n"
        "v_cvt_pk_bf16_f32 v149, v114, v115\n"
        "v_pk_min_u16 v224, v224, s26\n"
        "v_mfma_f32_16x16x32_bf16 v[80:83], v[168:171], v[180:183], v[80:83]\n"
        "v_pk_min_u16 v225, v225, s26\n"
        "v_max3_f32 v232, v64, v65, v66\n"
        "v_mfma_f32_16x16x32_bf16 v[84:87], v[172:175], v[176:179], v[20:23]\n"
        "v_max3_f32 v232, v232, v67, v68\n"
        "v_max3_f32 v232, v232, v69, v70\n"
        "v_mfma_f32_16x16x32_bf16 v[84:87], v[188:191], v[184:187], v[84:87]\n"
        "ds_read_b128 v[182:185], v252 offset:512\n"
        "v_mfma_f32_16x16x32_bf16 v[96:99], v[152:155], v[196:199], v[32:35]\n"
        "v_max3_f32 v233, v80, v81, v82\n"
        "v_mfma_f32_16x16x32_bf16 v[96:99], v[168:171], v[200:203], v[96:99]\n"
        "v_mfma_f32_16x16x32_bf16 v[100:103], v[172:175], v[196:199], v[36:39]\n"
        "v_mfma_f32_16x16x32_bf16 v[100:103], v[188:191], v[204:207], v[100:103]\n"
        "ds_read_b128 v[202:205], v252 offset:4096\n"
        "v_mfma_f32_16x16x32_bf16 v[112:115], v[152:155], v[216:219], v[48:51]\n"
        "s_waitcnt lgkmcnt(2)\n"
        "v_pk_min_u16 v162, v162, s26\n"
        "v_pk_min_u16 v163, v163, s26\n"
        "v_mfma_f32_16x16x32_bf16 v[112:115], v[168:171], v[220:223], v[112:115]\n"
        "v_pk_min_u16 v164, v164, s26\n"
        "v_pk_min_u16 v165, v165, s26\n"
        "v_mfma_f32_16x16x32_bf16 v[116:119], v[172:175], v[216:219], v[52:55]\n"
        "v_max3_f32 v233, v233, v83, v84\n"
        "v_max3_f32 v233, v233, v85, v86\n"
        "v_mfma_f32_16x16x32_bf16 v[116:119], v[188:191], v[224:227], v[116:119]\n"
        "ds_read_b128 v[222:225], v252 offset:4608\n"
        "v_max3_f32 v234, v96, v97, v98\n"
        "v_mfma_f32_16x16x32_bf16 v[72:75], v[192:195], v[156:159], v[8:11]\n"
        "s_waitcnt lgkmcnt(2)\n"
        "v_pk_min_u16 v182, v182, s26\n"
        "v_pk_min_u16 v183, v183, s26\n"
        "v_mfma_f32_16x16x32_bf16 v[72:75], v[208:211], v[160:163], v[72:75]\n"
        "v_pk_min_u16 v184, v184, s26\n"
        "v_pk_min_u16 v185, v185, s26\n"
        "v_mfma_f32_16x16x32_bf16 v[76:79], v[212:215], v[156:159], v[12:15]\n"
        "v_max3_f32 v234, v234, v99, v100\n"
        "v_max3_f32 v234, v234, v101, v102\n"
        "v_mfma_f32_16x16x32_bf16 v[76:79], v[228:231], v[164:167], v[76:79]\n"
        "v_max3_f32 v235, v112, v113, v114\n"
        "v_max3_f32 v235, v235, v115, v116\n"
        "v_mfma_f32_16x16x32_bf16 v[88:91], v[192:195], v[176:179], v[24:27]\n"
        "s_barrier\n"
        "ds_read_b128 v[152:155], v247 offset:0\n"
        "ds_read_b128 v[160:163], v247 offset:4096\n"
        "v_mfma_f32_16x16x32_bf16 v[88:91], v[208:211], v[180:183], v[88:91]\n"
        "ds_read_b128 v[156:159], v247 offset:256\n"
        "ds_read_b128 v[164:167], v247 offset:4352\n"
        "v_mfma_f32_16x16x32_bf16 v[92:95], v[212:215], v[176:179], v[28:31]\n"
        "ds_read_b128 a[216:219], v248 offset:0\n"
        "ds_read_b128 a[220:223], v248 offset:6144\n"
        "v_mfma_f32_16x16x32_bf16 v[92:95], v[228:231], v[184:187], v[92:95]\n"
        "s_waitcnt lgkmcnt(7)\n"
        "v_pk_min_u16 v202, v202, s26\n"
        "v_pk_min_u16 v203, v203, s26\n"
        "v_mfma_f32_16x16x32_bf16 v[104:107], v[192:195], v[196:199], v[40:43]\n"
        "ds_read_b128 a[224:227], v254 offset:16\n"
        "ds_read_b128 a[228:231], v254 offset:6160\n"
        "v_mfma_f32_16x16x32_bf16 v[104:107], v[208:211], v[200:203], v[104:107]\n"
        "v_pk_min_u16 v204, v204, s26\n"
        "v_pk_min_u16 v205, v205, s26\n"
        "v_mfma_f32_16x16x32_bf16 v[108:111], v[212:215], v[196:199], v[44:47]\n"
        "s_waitcnt lgkmcnt(8)\n"
        "v_pk_min_u16 v222, v222, s26\n"
        "v_pk_min_u16 v223, v223, s26\n"
        "v_mfma_f32_16x16x32_bf16 v[108:111], v[228:231], v[204:207], v[108:111]\n"
        "v_pk_min_u16 v224, v224, s26\n"
        "v_pk_min_u16 v225, v225, s26\n"
        "v_mfma_f32_16x16x32_bf16 v[120:123], v[192:195], v[216:219], v[56:59]\n"
        "ds_read_b128 a[232:235], v248 offset:32\n"
        "v_max3_f32 v232, v232, v71, v72\n"
        "v_mfma_f32_16x16x32_bf16 v[120:123], v[208:211], v[220:223], v[120:123]\n"
        "v_max3_f32 v232, v232, v73, v74\n"
        "v_max3_f32 v232, v232, v75, v76\n"
        "v_mfma_f32_16x16x32_bf16 v[124:127], v[212:215], v[216:219], v[60:63]\n"
        "v_max3_f32 v232, v232, v77, v78\n"
        "v_max_f32 v232, v232, v79\n"
        "v_mfma_f32_16x16x32_bf16 v[124:127], v[228:231], v[224:227], v[124:127]\n"
        "s_waitcnt lgkmcnt(4)\n"
        "v_max3_f32 v233, v233, v87, v88\n"
        "v_max3_f32 v233, v233, v89, v90\n"
        "v_mfma_f32_16x16x32_bf16 v[172:175], v[152:155], a[216:219], 0\n"
        "s_waitcnt lgkmcnt(3)\n"
        "v_max3_f32 v233, v233, v91, v92\n"
        "v_max3_f32 v233, v233, v93, v94\n"
        "v_mfma_f32_16x16x32_bf16 v[172:175], v[160:163], a[220:223], v[172:175]\n"
        "v_max_f32 v233, v233, v95\n"
        "v_max3_f32 v234, v234, v103, v104\n"
        "v_mfma_f32_16x16x32_bf16 v[180:183], v[164:167], a[220:223], 0\n"
        "ds_read_b128 a[220:223], v248 offset:6176\n"
        "ds_read_b128 a[236:239], v247 offset:8192\n"
        "v_mfma_f32_16x16x32_bf16 v[180:183], v[156:159], a[216:219], v[180:183]\n"
        "s_waitcnt lgkmcnt(4)\n"
        "ds_read_b128 a[216:219], v254 offset:48\n"
        "ds_read_b128 a[244:247], v247 offset:12288\n"
        "v_mfma_f32_16x16x32_bf16 v[184:187], v[156:159], a[224:227], 0\n"
        "s_waitcnt lgkmcnt(5)\n"
        "ds_read_b128 a[240:243], v247 offset:8448\n"
        "ds_read_b128 a[248:251], v247 offset:12544\n"
        "v_mfma_f32_16x16x32_bf16 v[184:187], v[164:167], a[228:231], v[184:187]\n"
        "v_permlane32_swap_b32 v232, v233\n"
        "v_mfma_f32_16x16x32_bf16 v[176:179], v[160:163], a[228:231], 0\n"
        "ds_read_b128 a[228:231], v254 offset:6192\n"
        "v_max_f32 v232, v232, v233\n"
        "v_mfma_f32_16x16x32_bf16 v[176:179], v[152:155], a[224:227], v[176:179]\n"
        "s_waitcnt lgkmcnt(7)\n"
        "ds_read_b128 a[224:227], v248 offset:64\n"
        "v_mov_b32 v233, v232\n"
        "v_mfma_f32_16x16x32_bf16 v[212:215], v[152:155], a[232:235], 0\n"
        "s_waitcnt lgkmcnt(7)\n"
        "v_max3_f32 v234, v234, v105, v106\n"
        "v_max3_f32 v234, v234, v107, v108\n"
        "v_mfma_f32_16x16x32_bf16 v[212:215], v[160:163], a[220:223], v[212:215]\n"
        "v_permlane16_swap_b32 v232, v233\n"
        "v_mfma_f32_16x16x32_bf16 v[192:195], v[164:167], a[220:223], 0\n"
        "ds_read_b128 a[220:223], v248 offset:6208\n"
        "v_max_f32 v232, v232, v233\n"
        "v_mfma_f32_16x16x32_bf16 v[192:195], v[156:159], a[232:235], v[192:195]\n"
        "s_waitcnt lgkmcnt(6)\n"
        "ds_read_b128 a[232:235], v254 offset:80\n"
        "v_mul_f32 v236, s25, v232\n"
        "v_mfma_f32_16x16x32_bf16 v[196:199], v[156:159], a[216:219], 0\n"
        "s_waitcnt lgkmcnt(3)\n"
        "v_mul_f32 v237, s25, v232\n"
        "v_max3_f32 v234, v234, v109, v110\n"
        "v_mfma_f32_16x16x32_bf16 v[196:199], v[164:167], a[228:231], v[196:199]\n"
        "v_permlane32_swap_b32 v236, v237\n"
        "v_mfma_f32_16x16x32_bf16 v[216:219], v[160:163], a[228:231], 0\n"
        "ds_read_b128 a[228:231], v254 offset:6224\n"
        "v_max_f32 v234, v234, v111\n"
        "v_mfma_f32_16x16x32_bf16 v[216:219], v[152:155], a[216:219], v[216:219]\n"
        "s_waitcnt lgkmcnt(3)\n"
        "ds_read_b128 a[216:219], v248 offset:36864\n"
        "v_max3_f32 v235, v235, v117, v118\n"
        "v_mfma_f32_16x16x32_bf16 v[220:223], v[152:155], a[224:227], 0\n"
        "s_waitcnt lgkmcnt(3)\n"
        "v_max3_f32 v235, v235, v119, v120\n"
        "v_max3_f32 v235, v235, v121, v122\n"
        "v_mfma_f32_16x16x32_bf16 v[220:223], v[160:163], a[220:223], v[220:223]\n"
        "v_max3_f32 v235, v235, v123, v124\n"
        "v_max3_f32 v235, v235, v125, v126\n"
        "v_mfma_f32_16x16x32_bf16 v[200:203], v[164:167], a[220:223], 0\n"
        "ds_read_b128 a[220:223], v248 offset:43008\n"
        "v_max_f32 v235, v235, v127\n"
        "v_mfma_f32_16x16x32_bf16 v[200:203], v[156:159], a[224:227], v[200:203]\n"
        "s_waitcnt lgkmcnt(3)\n"
        "ds_read_b128 a[224:227], v254 offset:36880\n"
        "v_fma_f32 v64, v64, s24, v236\n"
        "v_mfma_f32_16x16x32_bf16 v[204:207], v[156:159], a[232:235], 0\n"
        "s_waitcnt lgkmcnt(3)\n"
        "v_permlane32_swap_b32 v234, v235\n"
        "v_mfma_f32_16x16x32_bf16 v[204:207], v[164:167], a[228:231], v[204:207]\n"
        "v_max_f32 v234, v234, v235\n"
        "v_mov_b32 v235, v234\n"
        "v_mfma_f32_16x16x32_bf16 v[224:227], v[160:163], a[228:231], 0\n"
        "ds_read_b128 a[228:231], v254 offset:43024\n"
        "v_fma_f32 v65, v65, s24, v236\n"
        "v_mfma_f32_16x16x32_bf16 v[224:227], v[152:155], a[232:235], v[224:227]\n"
        "s_waitcnt lgkmcnt(3)\n"
        "ds_read_b128 a[232:235], v248 offset:36896\n"
        "v_fma_f32 v66, v66, s24, v236\n"
        "v_mfma_f32_16x16x32_bf16 v[172:175], a[236:239], a[216:219], v[172:175]\n"
        "s_waitcnt lgkmcnt(3)\n"
        "v_permlane16_swap_b32 v234, v235\n"
        "v_mfma_f32_16x16x32_bf16 v[172:175], a[244:247], a[220:223], v[172:175]\n"
        "v_max_f32 v234, v234, v235\n"
        "v_mul_f32 v238, s25, v234\n"
        "v_mfma_f32_16x16x32_bf16 v[180:183], a[248:251], a[220:223], v[180:183]\n"
        "ds_read_b128 a[220:223], v248 offset:43040\n"
        "ds_read_b128 v[152:155], v247 offset:16384\n"
        "v_mfma_f32_16x16x32_bf16 v[180:183], a[240:243], a[216:219], v[180:183]\n"
        "s_waitcnt lgkmcnt(4)\n"
        "ds_read_b128 a[216:219], v254 offset:36912\n"
        "ds_read_b128 v[160:163], v247 offset:20480\n"
        "v_mfma_f32_16x16x32_bf16 v[184:187], a[240:243], a[224:227], v[184:187]\n"
        "s_waitcnt lgkmcnt(5)\n"
        "ds_read_b128 v[156:159], v247 offset:16640\n"
        "ds_read_b128 v[164:167], v247 offset:20736\n"
        "v_mfma_f32_16x16x32_bf16 v[184:187], a[248:251], a[228:231], v[184:187]\n"
        "v_mul_f32 v239, s25, v234\n"
        "v_fma_f32 v67, v67, s24, v236\n"
        "v_mfma_f32_16x16x32_bf16 v[176:179], a[244:247], a[228:231], v[176:179]\n"
        "ds_read_b128 a[228:231], v254 offset:43056\n"
        "v_fma_f32 v68, v68, s24, v236\n"
        "v_mfma_f32_16x16x32_bf16 v[176:179], a[236:239], a[224:227], v[176:179]\n"
        "s_waitcnt lgkmcnt(7)\n"
        "ds_read_b128 a[224:227], v248 offset:36928\n"
        "v_fma_f32 v69, v69, s24, v236\n"
        "v_mfma_f32_16x16x32_bf16 v[212:215], a[236:239], a[232:235], v[212:215]\n"
        "s_waitcnt lgkmcnt(7)\n"
        "v_permlane32_swap_b32 v238, v239\n"
        "v_mfma_f32_16x16x32_bf16 v[212:215], a[244:247], a[220:223], v[212:215]\n"
        "v_exp_f32 v64, v64\n"
        "v_mfma_f32_16x16x32_bf16 v[192:195], a[248:251], a[220:223], v[192:195]\n"
        "ds_read_b128 a[220:223], v248 offset:43072\n"
        "v_fma_f32 v70, v70, s24, v236\n"
        "v_mfma_f32_16x16x32_bf16 v[192:195], a[240:243], a[232:235], v[192:195]\n"
        "s_waitcnt lgkmcnt(6)\n"
        "ds_read_b128 a[232:235], v254 offset:36944\n"
        "v_fma_f32 v71, v71, s24, v236\n"
        "v_mfma_f32_16x16x32_bf16 v[196:199], a[240:243], a[216:219], v[196:199]\n"
        "s_waitcnt lgkmcnt(3)\n"
        "v_exp_f32 v65, v65\n"
        "v_mfma_f32_16x16x32_bf16 v[196:199], a[248:251], a[228:231], v[196:199]\n"
        "v_exp_f32 v66, v66\n"
        "v_mfma_f32_16x16x32_bf16 v[216:219], a[244:247], a[228:231], v[216:219]\n"
        "ds_read_b128 a[228:231], v254 offset:43088\n"
        "v_cvt_pk_bf16_f32 v64, v64, v65\n"
        "v_mfma_f32_16x16x32_bf16 v[216:219], a[236:239], a[216:219], v[216:219]\n"
        "s_waitcnt lgkmcnt(3)\n"
        "ds_read_b128 a[216:219], v248 offset:12288\n"
        "v_fma_f32 v72, v72, s24, v236\n"
        "v_mfma_f32_16x16x32_bf16 v[220:223], a[236:239], a[224:227], v[220:223]\n"
        "s_waitcnt lgkmcnt(3)\n"
        "v_exp_f32 v67, v67\n"
        "v_mfma_f32_16x16x32_bf16 v[220:223], a[244:247], a[220:223], v[220:223]\n"
        "v_exp_f32 v68, v68\n"
        "v_mfma_f32_16x16x32_bf16 v[200:203], a[248:251], a[220:223], v[200:203]\n"
        "ds_read_b128 a[220:223], v248 offset:49152\n"
        "v_cvt_pk_bf16_f32 v65, v66, v67\n"
        "v_mfma_f32_16x16x32_bf16 v[200:203], a[240:243], a[224:227], v[200:203]\n"
        "s_waitcnt lgkmcnt(3)\n"
        "ds_read_b128 a[224:227], v254 offset:12304\n"
        "v_fma_f32 v73, v73, s24, v236\n"
        "v_mfma_f32_16x16x32_bf16 v[204:207], a[240:243], a[232:235], v[204:207]\n"
        "s_waitcnt lgkmcnt(3)\n"
        "v_exp_f32 v69, v69\n"
        "v_mfma_f32_16x16x32_bf16 v[204:207], a[248:251], a[228:231], v[204:207]\n"
        "v_exp_f32 v70, v70\n"
        "v_mfma_f32_16x16x32_bf16 v[224:227], a[244:247], a[228:231], v[224:227]\n"
        "ds_read_b128 a[228:231], v254 offset:49168\n"
        "v_cvt_pk_bf16_f32 v66, v68, v69\n"
        "v_mfma_f32_16x16x32_bf16 v[224:227], a[236:239], a[232:235], v[224:227]\n"
        "s_waitcnt lgkmcnt(3)\n"
        "ds_read_b128 a[232:235], v248 offset:12320\n"
        "v_fma_f32 v74, v74, s24, v236\n"
        "v_mfma_f32_16x16x32_bf16 v[172:175], v[152:155], a[216:219], v[172:175]\n"
        "s_waitcnt lgkmcnt(3)\n"
        "v_exp_f32 v71, v71\n"
        "v_mfma_f32_16x16x32_bf16 v[172:175], v[160:163], a[220:223], v[172:175]\n"
        "v_exp_f32 v72, v72\n"
        "v_mfma_f32_16x16x32_bf16 v[180:183], v[164:167], a[220:223], v[180:183]\n"
        "ds_read_b128 a[220:223], v248 offset:49184\n"
        "v_cvt_pk_bf16_f32 v67, v70, v71\n"
        "v_mfma_f32_16x16x32_bf16 v[180:183], v[156:159], a[216:219], v[180:183]\n"
        "s_waitcnt lgkmcnt(3)\n"
        "ds_read_b128 a[216:219], v254 offset:12336\n"
        "v_fma_f32 v75, v75, s24, v236\n"
        "v_mfma_f32_16x16x32_bf16 v[184:187], v[156:159], a[224:227], v[184:187]\n"
        "s_waitcnt lgkmcnt(3)\n"
        "v_exp_f32 v73, v73\n"
        "v_mfma_f32_16x16x32_bf16 v[184:187], v[164:167], a[228:231], v[184:187]\n"
        "v_cvt_pk_bf16_f32 v68, v72, v73\n"
        "v_xor_b32 v251, 0x2000, v251\n"
        "v_mfma_f32_16x16x32_bf16 v[176:179], v[160:163], a[228:231], v[176:179]\n"
        "ds_read_b128 a[228:231], v254 offset:49200\n"
        "v_xor_b32 v252, 0x2000, v252\n"
        "v_mfma_f32_16x16x32_bf16 v[176:179], v[152:155], a[224:227], v[176:179]\n"
        "s_waitcnt lgkmcnt(3)\n"
        "ds_read_b128 a[224:227], v248 offset:12352\n"
        "v_add_u32 v245, s45, v245\n"
        "v_mfma_f32_16x16x32_bf16 v[212:215], v[152:155], a[232:235], v[212:215]\n"
        "s_waitcnt lgkmcnt(3)\n"
        "s_waitcnt vmcnt(0)\n"
        "s_barrier\n"
        "v_exp_f32 v74, v74\n"
        "v_mfma_f32_16x16x32_bf16 v[212:215], v[160:163], a[220:223], v[212:215]\n"
        "v_exp_f32 v75, v75\n"
        "v_mfma_f32_16x16x32_bf16 v[192:195], v[164:167], a[220:223], v[192:195]\n"
        "ds_read_b128 a[220:223], v248 offset:49216\n"
        "ds_read_b128 a[236:239], v240 offset:0\n"
        "v_mfma_f32_16x16x32_bf16 v[192:195], v[156:159], a[232:235], v[192:195]\n"
        "s_waitcnt lgkmcnt(4)\n"
        "ds_read_b128 a[232:235], v254 offset:12368\n"
        "ds_read_b128 a[240:243], v241 offset:0\n"
        "v_mfma_f32_16x16x32_bf16 v[196:199], v[156:159], a[216:219], v[196:199]\n"
        "s_waitcnt lgkmcnt(5)\n"
        "ds_read_b128 a[244:247], v240 offset:128\n"
        "v_cvt_pk_bf16_f32 v69, v74, v75\n"
        "v_mfma_f32_16x16x32_bf16 v[196:199], v[164:167], a[228:231], v[196:199]\n"
        "ds_read_b128 a[248:251], v241 offset:128\n"
        "v_add_u32 v246, s45, v246\n"
        "v_mfma_f32_16x16x32_bf16 v[216:219], v[160:163], a[228:231], v[216:219]\n"
        "ds_read_b128 a[228:231], v254 offset:49232\n"
        "v_add_u32 v247, s45, v247\n"
        "v_mfma_f32_16x16x32_bf16 v[216:219], v[152:155], a[216:219], v[216:219]\n"
        "s_waitcnt lgkmcnt(7)\n"
        "ds_read_b128 a[216:219], v240 offset:256\n"
        "v_mfma_f32_16x16x32_bf16 v[220:223], v[152:155], a[224:227], v[220:223]\n"
        "s_waitcnt lgkmcnt(7)\n"
        "v_mfma_f32_16x16x32_bf16 v[220:223], v[160:163], a[220:223], v[220:223]\n"
        "v_mfma_f32_16x16x32_bf16 v[200:203], v[164:167], a[220:223], v[200:203]\n"
        "ds_read_b128 a[220:223], v241 offset:256\n"
        "v_mfma_f32_16x16x32_bf16 v[200:203], v[156:159], a[224:227], v[200:203]\n"
        "s_waitcnt lgkmcnt(6)\n"
        "v_mfma_f32_16x16x32_bf16 v[204:207], v[156:159], a[232:235], v[204:207]\n"
        "s_waitcnt lgkmcnt(2)\n"
        "v_mfma_f32_16x16x32_bf16 v[204:207], v[164:167], a[228:231], v[204:207]\n"
        "v_mfma_f32_16x16x32_bf16 v[224:227], v[160:163], a[228:231], v[224:227]\n"
        "v_mfma_f32_16x16x32_bf16 v[224:227], v[152:155], a[232:235], v[224:227]\n"
        "s_waitcnt lgkmcnt(0)\n"
        ".p2align 6\n"
        "10:\n"
        "v_mfma_f32_16x16x32_bf16 v[162:165], a[72:75], a[236:239], 0\n"
        "v_fma_f32 v76, v76, s24, v236\n"
        "v_fma_f32 v77, v77, s24, v236\n"
        "v_mfma_f32_16x16x32_bf16 v[162:165], a[76:79], a[240:243], v[162:165]\n"
        "s_waitcnt lgkmcnt(3)\n"
        "s_add_u32 s41, s30, s40\n"
        "v_cvt_pk_bf16_f32 v232, v172, v176\n"
        "v_cvt_pk_bf16_f32 v233, v212, v216\n"
        "v_mfma_f32_16x16x32_bf16 v[162:165], a[80:83], a[244:247], v[162:165]\n"
        "s_waitcnt lgkmcnt(2)\n"
        "v_cvt_pk_bf16_f32 v234, v220, v224\n"
        "v_fma_f32 v78, v78, s24, v236\n"
        "v_mfma_f32_16x16x32_bf16 v[162:165], a[84:87], a[248:251], v[162:165]\n"
        "s_waitcnt lgkmcnt(1)\n"
        "buffer_store_dwordx3 v[232:234], v250, s[16:19], s38 offen offset:0 sc1\n"
        "v_mfma_f32_16x16x32_bf16 v[162:165], a[88:91], a[216:219], v[162:165]\n"
        "s_waitcnt lgkmcnt(0)\n"
        "v_exp_f32 v76, v76\n"
        "v_mfma_f32_16x16x32_bf16 v[162:165], a[92:95], a[220:223], v[162:165]\n"
        "v_exp_f32 v77, v77\n"
        "v_mfma_f32_16x16x32_bf16 v[72:75], a[116:119], a[220:223], 0\n"
        "v_fma_f32 v79, v79, s24, v236\n"
        "v_cvt_pk_bf16_f32 v70, v76, v77\n"
        "v_mfma_f32_16x16x32_bf16 v[72:75], a[112:115], a[216:219], v[72:75]\n"
        "v_exp_f32 v78, v78\n"
        "v_mfma_f32_16x16x32_bf16 v[72:75], a[108:111], a[248:251], v[72:75]\n"
        "v_exp_f32 v79, v79\n"
        "v_mfma_f32_16x16x32_bf16 v[72:75], a[104:107], a[244:247], v[72:75]\n"
        "v_cvt_pk_bf16_f32 v71, v78, v79\n"
        "v_cvt_pk_bf16_f32 v232, v173, v177\n"
        "v_mfma_f32_16x16x32_bf16 v[72:75], a[100:103], a[240:243], v[72:75]\n"
        "v_cvt_pk_bf16_f32 v233, v213, v217\n"
        "v_cvt_pk_bf16_f32 v234, v221, v225\n"
        "v_mfma_f32_16x16x32_bf16 v[72:75], a[96:99], a[236:239], v[72:75]\n"
        "buffer_store_dwordx3 v[232:234], v250, s[16:19], s38 offen offset:384 sc1\n"
        "v_mfma_f32_16x16x32_bf16 v[76:79], a[120:123], a[236:239], 0\n"
        "s_add_u32 s35, s47, 786432\n"
        "v_cvt_pk_bf16_f32 v152, v162, v163\n"
        "v_cvt_pk_bf16_f32 v153, v164, v165\n"
        "v_mfma_f32_16x16x32_bf16 v[76:79], a[124:127], a[240:243], v[76:79]\n"
        "s_sub_u32 s46, s35, 100663296\n"
        "v_fma_f32 v80, v80, s24, v237\n"
        "v_cvt_pk_bf16_f32 v154, v72, v73\n"
        "v_mfma_f32_16x16x32_bf16 v[76:79], a[128:131], a[244:247], v[76:79]\n"
        "s_cmp_lt_u32 s46, 786432\n"
        "v_exp_f32 v80, v80\n"
        "v_mfma_f32_16x16x32_bf16 v[76:79], a[132:135], a[248:251], v[76:79]\n"
        "s_cselect_b32 s35, s46, s35\n"
        "v_cvt_pk_bf16_f32 v155, v74, v75\n"
        "v_cvt_pk_bf16_f32 v232, v174, v178\n"
        "v_mfma_f32_16x16x32_bf16 v[76:79], a[136:139], a[216:219], v[76:79]\n"
        "v_cvt_pk_bf16_f32 v233, v214, v218\n"
        "v_cvt_pk_bf16_f32 v234, v222, v226\n"
        "v_mfma_f32_16x16x32_bf16 v[76:79], a[140:143], a[220:223], v[76:79]\n"
        "v_fma_f32 v81, v81, s24, v237\n"
        "v_fma_f32 v82, v82, s24, v237\n"
        "v_mfma_f32_16x16x32_bf16 v[162:165], a[20:23], a[220:223], 0\n"
        "v_exp_f32 v81, v81\n"
        "v_mfma_f32_16x16x32_bf16 v[162:165], a[16:19], a[216:219], v[162:165]\n"
        "v_exp_f32 v82, v82\n"
        "v_mfma_f32_16x16x32_bf16 v[162:165], a[12:15], a[248:251], v[162:165]\n"
        "buffer_store_dwordx3 v[232:234], v250, s[16:19], s38 offen offset:768 sc1\n"
        "v_mfma_f32_16x16x32_bf16 v[162:165], a[8:11], a[244:247], v[162:165]\n"
        "v_cvt_pk_bf16_f32 v168, v76, v77\n"
        "v_cvt_pk_bf16_f32 v169, v78, v79\n"
        "v_mfma_f32_16x16x32_bf16 v[162:165], a[4:7], a[240:243], v[162:165]\n"
        "v_cvt_pk_bf16_f32 v80, v80, v81\n"
        "v_fma_f32 v83, v83, s24, v237\n"
        "v_mfma_f32_16x16x32_bf16 v[162:165], a[0:3], a[236:239], v[162:165]\n"
        "v_exp_f32 v83, v83\n"
        "v_mfma_f32_16x16x32_bf16 v[72:75], a[24:27], a[236:239], 0\n"
        "v_cvt_pk_bf16_f32 v232, v175, v179\n"
        "v_cvt_pk_bf16_f32 v233, v215, v219\n"
        "v_mfma_f32_16x16x32_bf16 v[72:75], a[28:31], a[240:243], v[72:75]\n"
        "v_cvt_pk_bf16_f32 v234, v223, v227\n"
        "v_cvt_pk_bf16_f32 v81, v82, v83\n"
        "v_mfma_f32_16x16x32_bf16 v[72:75], a[32:35], a[244:247], v[72:75]\n"
        "v_cvt_pk_bf16_f32 v156, v162, v163\n"
        "v_cvt_pk_bf16_f32 v157, v164, v165\n"
        "v_mfma_f32_16x16x32_bf16 v[72:75], a[36:39], a[248:251], v[72:75]\n"
        "v_fma_f32 v84, v84, s24, v237\n"
        "v_fma_f32 v85, v85, s24, v237\n"
        "v_mfma_f32_16x16x32_bf16 v[72:75], a[40:43], a[216:219], v[72:75]\n"
        "v_exp_f32 v84, v84\n"
        "v_mfma_f32_16x16x32_bf16 v[72:75], a[44:47], a[220:223], v[72:75]\n"
        "buffer_store_dwordx3 v[232:234], v250, s[16:19], s38 offen offset:1152 sc1\n"
        "v_mfma_f32_16x16x32_bf16 v[76:79], a[68:71], a[220:223], 0\n"
        "v_exp_f32 v85, v85\n"
        "v_mfma_f32_16x16x32_bf16 v[76:79], a[64:67], a[216:219], v[76:79]\n"
        "v_cvt_pk_bf16_f32 v82, v84, v85\n"
        "v_fma_f32 v86, v86, s24, v237\n"
        "v_mfma_f32_16x16x32_bf16 v[76:79], a[60:63], a[248:251], v[76:79]\n"
        "ds_read_b128 a[224:227], v240 offset:1536\n"
        "v_cvt_pk_bf16_f32 v158, v72, v73\n"
        "v_mfma_f32_16x16x32_bf16 v[76:79], a[56:59], a[244:247], v[76:79]\n"
        "ds_read_b128 a[228:231], v241 offset:1536\n"
        "v_cvt_pk_bf16_f32 v159, v74, v75\n"
        "v_mfma_f32_16x16x32_bf16 v[76:79], a[52:55], a[240:243], v[76:79]\n"
        "ds_read_b128 a[232:235], v240 offset:1664\n"
        "v_cvt_pk_bf16_f32 v232, v180, v184\n"
        "v_mfma_f32_16x16x32_bf16 v[76:79], a[48:51], a[236:239], v[76:79]\n"
        "v_cvt_pk_bf16_f32 v233, v192, v196\n"
        "v_cvt_pk_bf16_f32 v234, v200, v204\n"
        "v_mfma_f32_16x16x32_bf16 v[162:165], a[236:239], a[144:147], 0\n"
        "v_exp_f32 v86, v86\n"
        "v_mfma_f32_16x16x32_bf16 v[162:165], a[240:243], a[148:151], v[162:165]\n"
        "v_fma_f32 v87, v87, s24, v237\n"
        "v_fma_f32 v88, v88, s24, v237\n"
        "v_mfma_f32_16x16x32_bf16 v[162:165], a[244:247], a[152:155], v[162:165]\n"
        "buffer_store_dwordx3 v[232:234], v250, s[16:19], s39 offen offset:0 sc1\n"
        "v_mfma_f32_16x16x32_bf16 v[162:165], a[248:251], a[156:159], v[162:165]\n"
        "v_exp_f32 v87, v87\n"
        "v_mfma_f32_16x16x32_bf16 v[162:165], a[216:219], a[160:163], v[162:165]\n"
        "v_cvt_pk_bf16_f32 v160, v76, v77\n"
        "v_cvt_pk_bf16_f32 v161, v78, v79\n"
        "v_mfma_f32_16x16x32_bf16 v[162:165], a[220:223], a[164:167], v[162:165]\n"
        "v_cvt_pk_bf16_f32 v166, v76, v77\n"
        "v_cvt_pk_bf16_f32 v167, v78, v79\n"
        "v_mfma_f32_16x16x32_bf16 v[72:75], a[220:223], a[188:191], 0\n"
        "v_exp_f32 v88, v88\n"
        "v_mfma_f32_16x16x32_bf16 v[72:75], a[216:219], a[184:187], v[72:75]\n"
        "v_cvt_pk_bf16_f32 v232, v181, v185\n"
        "v_cvt_pk_bf16_f32 v233, v193, v197\n"
        "v_mfma_f32_16x16x32_bf16 v[72:75], a[248:251], a[180:183], v[72:75]\n"
        "v_cvt_pk_bf16_f32 v234, v201, v205\n"
        "v_cvt_pk_bf16_f32 v212, v162, v163\n"
        "v_mfma_f32_16x16x32_bf16 v[72:75], a[244:247], a[176:179], v[72:75]\n"
        "v_cvt_pk_bf16_f32 v213, v164, v165\n"
        "v_cvt_pk_bf16_f32 v83, v86, v87\n"
        "v_mfma_f32_16x16x32_bf16 v[72:75], a[240:243], a[172:175], v[72:75]\n"
        "v_fma_f32 v89, v89, s24, v237\n"
        "v_fma_f32 v90, v90, s24, v237\n"
        "v_mfma_f32_16x16x32_bf16 v[72:75], a[236:239], a[168:171], v[72:75]\n"
        "buffer_store_dwordx3 v[232:234], v250, s[16:19], s39 offen offset:384 sc1\n"
        "v_mfma_f32_16x16x32_bf16 v[76:79], a[236:239], a[192:195], 0\n"
        "ds_read_b128 a[236:239], v241 offset:1664\n"
        "v_fma_f32 v91, v91, s24, v237\n"
        "v_mfma_f32_16x16x32_bf16 v[76:79], a[240:243], a[196:199], v[76:79]\n"
        "ds_read_b128 a[240:243], v240 offset:1792\n"
        "v_fma_f32 v92, v92, s24, v237\n"
        "v_mfma_f32_16x16x32_bf16 v[76:79], a[244:247], a[200:203], v[76:79]\n"
        "ds_read_b128 a[244:247], v241 offset:1792\n"
        "v_cvt_pk_bf16_f32 v214, v72, v73\n"
        "v_mfma_f32_16x16x32_bf16 v[76:79], a[248:251], a[204:207], v[76:79]\n"
        "v_exp_f32 v89, v89\n"
        "v_mfma_f32_16x16x32_bf16 v[76:79], a[216:219], a[208:211], v[76:79]\n"
        "v_cvt_pk_bf16_f32 v215, v74, v75\n"
        "v_cvt_pk_bf16_f32 v232, v182, v186\n"
        "v_mfma_f32_16x16x32_bf16 v[76:79], a[220:223], a[212:215], v[76:79]\n"
        "s_waitcnt lgkmcnt(5)\n"
        "v_cvt_pk_bf16_f32 v233, v194, v198\n"
        "v_cvt_pk_bf16_f32 v234, v202, v206\n"
        "v_mfma_f32_16x16x32_bf16 v[162:165], a[72:75], a[224:227], 0\n"
        "s_waitcnt lgkmcnt(4)\n"
        "v_exp_f32 v90, v90\n"
        "v_mfma_f32_16x16x32_bf16 v[162:165], a[76:79], a[228:231], v[162:165]\n"
        "s_waitcnt lgkmcnt(3)\n"
        "v_exp_f32 v91, v91\n"
        "v_mfma_f32_16x16x32_bf16 v[162:165], a[80:83], a[232:235], v[162:165]\n"
        "s_waitcnt lgkmcnt(2)\n"
        "buffer_store_dwordx3 v[232:234], v250, s[16:19], s39 offen offset:768 sc1\n"
        "v_mfma_f32_16x16x32_bf16 v[162:165], a[84:87], a[236:239], v[162:165]\n"
        "s_waitcnt lgkmcnt(1)\n"
        "v_exp_f32 v92, v92\n"
        "v_mfma_f32_16x16x32_bf16 v[162:165], a[88:91], a[240:243], v[162:165]\n"
        "s_waitcnt lgkmcnt(0)\n"
        "v_cvt_pk_bf16_f32 v216, v76, v77\n"
        "v_cvt_pk_bf16_f32 v217, v78, v79\n"
        "v_mfma_f32_16x16x32_bf16 v[162:165], a[92:95], a[244:247], v[162:165]\n"
        "v_cvt_pk_bf16_f32 v232, v183, v187\n"
        "v_cvt_pk_bf16_f32 v233, v195, v199\n"
        "v_mfma_f32_16x16x32_bf16 v[72:75], a[116:119], a[244:247], 0\n"
        "v_cvt_pk_bf16_f32 v234, v203, v207\n"
        "v_cvt_pk_bf16_f32 v84, v88, v89\n"
        "v_mfma_f32_16x16x32_bf16 v[72:75], a[112:115], a[240:243], v[72:75]\n"
        "v_cvt_pk_bf16_f32 v85, v90, v91\n"
        "v_fma_f32 v93, v93, s24, v237\n"
        "v_mfma_f32_16x16x32_bf16 v[72:75], a[108:111], a[236:239], v[72:75]\n"
        "v_exp_f32 v93, v93\n"
        "v_mfma_f32_16x16x32_bf16 v[72:75], a[104:107], a[232:235], v[72:75]\n"
        "v_cvt_pk_bf16_f32 v172, v162, v163\n"
        "v_cvt_pk_bf16_f32 v173, v164, v165\n"
        "v_mfma_f32_16x16x32_bf16 v[72:75], a[100:103], a[228:231], v[72:75]\n"
        "v_cvt_pk_bf16_f32 v86, v92, v93\n"
        "v_fma_f32 v94, v94, s24, v237\n"
        "v_mfma_f32_16x16x32_bf16 v[72:75], a[96:99], a[224:227], v[72:75]\n"
        "buffer_store_dwordx3 v[232:234], v250, s[16:19], s39 offen offset:1152 sc1\n"
        "v_mfma_f32_16x16x32_bf16 v[76:79], a[120:123], a[224:227], 0\n"
        "v_exp_f32 v94, v94\n"
        "v_mfma_f32_16x16x32_bf16 v[76:79], a[124:127], a[228:231], v[76:79]\n"
        "v_fma_f32 v95, v95, s24, v237\n"
        "v_fma_f32 v96, v96, s24, v238\n"
        "v_mfma_f32_16x16x32_bf16 v[76:79], a[128:131], a[232:235], v[76:79]\n"
        "v_exp_f32 v95, v95\n"
        "v_mfma_f32_16x16x32_bf16 v[76:79], a[132:135], a[236:239], v[76:79]\n"
        "v_exp_f32 v96, v96\n"
        "v_mfma_f32_16x16x32_bf16 v[76:79], a[136:139], a[240:243], v[76:79]\n"
        "v_cvt_pk_bf16_f32 v174, v72, v73\n"
        "v_cvt_pk_bf16_f32 v175, v74, v75\n"
        "v_mfma_f32_16x16x32_bf16 v[76:79], a[140:143], a[244:247], v[76:79]\n"
        "v_cvt_pk_bf16_f32 v87, v94, v95\n"
        "v_fma_f32 v97, v97, s24, v238\n"
        "v_mfma_f32_16x16x32_bf16 v[162:165], a[20:23], a[244:247], 0\n"
        "s_mov_b32 m0, s48\n"
        "v_exp_f32 v97, v97\n"
        "v_mfma_f32_16x16x32_bf16 v[162:165], a[16:19], a[240:243], v[162:165]\n"
        "v_cvt_pk_bf16_f32 v96, v96, v97\n"
        "v_fma_f32 v98, v98, s24, v238\n"
        "v_mfma_f32_16x16x32_bf16 v[162:165], a[12:15], a[236:239], v[162:165]\n"
        "buffer_load_dwordx4 v249, s[12:15], s36 offen offset:0 lds\n"
        "v_mfma_f32_16x16x32_bf16 v[162:165], a[8:11], a[232:235], v[162:165]\n"
        "v_exp_f32 v98, v98\n"
        "v_mfma_f32_16x16x32_bf16 v[162:165], a[4:7], a[228:231], v[162:165]\n"
        "v_cvt_pk_bf16_f32 v190, v76, v77\n"
        "v_cvt_pk_bf16_f32 v191, v78, v79\n"
        "v_mfma_f32_16x16x32_bf16 v[162:165], a[0:3], a[224:227], v[162:165]\n"
        "v_fma_f32 v99, v99, s24, v238\n"
        "v_fma_f32 v100, v100, s24, v238\n"
        "v_mfma_f32_16x16x32_bf16 v[72:75], a[24:27], a[224:227], 0\n"
        "v_exp_f32 v99, v99\n"
        "v_mfma_f32_16x16x32_bf16 v[72:75], a[28:31], a[228:231], v[72:75]\n"
        "v_exp_f32 v100, v100\n"
        "v_mfma_f32_16x16x32_bf16 v[72:75], a[32:35], a[232:235], v[72:75]\n"
        "v_cvt_pk_bf16_f32 v97, v98, v99\n"
        "v_cvt_pk_bf16_f32 v176, v162, v163\n"
        "v_mfma_f32_16x16x32_bf16 v[72:75], a[36:39], a[236:239], v[72:75]\n"
        "v_cvt_pk_bf16_f32 v177, v164, v165\n"
        "v_fma_f32 v101, v101, s24, v238\n"
        "v_mfma_f32_16x16x32_bf16 v[72:75], a[40:43], a[240:243], v[72:75]\n"
        "v_exp_f32 v101, v101\n"
        "v_mfma_f32_16x16x32_bf16 v[72:75], a[44:47], a[244:247], v[72:75]\n"
        "v_cvt_pk_bf16_f32 v98, v100, v101\n"
        "v_fma_f32 v102, v102, s24, v238\n"
        "v_mfma_f32_16x16x32_bf16 v[76:79], a[68:71], a[244:247], 0\n"
        "v_xor_b32 v253, 32, v249\n"
        "v_fma_f32 v103, v103, s24, v238\n"
        "v_mfma_f32_16x16x32_bf16 v[76:79], a[64:67], a[240:243], v[76:79]\n"
        "v_exp_f32 v102, v102\n"
        "v_mfma_f32_16x16x32_bf16 v[76:79], a[60:63], a[236:239], v[76:79]\n"
        "ds_read_b128 a[248:251], v240 offset:12288\n"
        "v_cvt_pk_bf16_f32 v178, v72, v73\n"
        "v_mfma_f32_16x16x32_bf16 v[76:79], a[56:59], a[232:235], v[76:79]\n"
        "ds_read_b128 a[216:219], v241 offset:12288\n"
        "v_cvt_pk_bf16_f32 v179, v74, v75\n"
        "v_mfma_f32_16x16x32_bf16 v[76:79], a[52:55], a[228:231], v[76:79]\n"
        "buffer_load_dwordx4 v253, s[12:15], s36 offen offset:1024 lds\n"
        "v_mfma_f32_16x16x32_bf16 v[76:79], a[48:51], a[224:227], v[76:79]\n"
        "ds_read_b128 a[220:223], v240 offset:12416\n"
        "v_fma_f32 v104, v104, s24, v238\n"
        "v_mfma_f32_16x16x32_bf16 v[162:165], a[224:227], a[144:147], 0\n"
        "v_exp_f32 v103, v103\n"
        "v_mfma_f32_16x16x32_bf16 v[162:165], a[228:231], a[148:151], v[162:165]\n"
        "v_exp_f32 v104, v104\n"
        "v_mfma_f32_16x16x32_bf16 v[162:165], a[232:235], a[152:155], v[162:165]\n"
        "v_cvt_pk_bf16_f32 v99, v102, v103\n"
        "v_cvt_pk_bf16_f32 v180, v76, v77\n"
        "v_mfma_f32_16x16x32_bf16 v[162:165], a[236:239], a[156:159], v[162:165]\n"
        "v_cvt_pk_bf16_f32 v181, v78, v79\n"
        "v_cvt_pk_bf16_f32 v186, v76, v77\n"
        "v_mfma_f32_16x16x32_bf16 v[162:165], a[240:243], a[160:163], v[162:165]\n"
        "v_cvt_pk_bf16_f32 v187, v78, v79\n"
        "v_fma_f32 v105, v105, s24, v238\n"
        "v_mfma_f32_16x16x32_bf16 v[162:165], a[244:247], a[164:167], v[162:165]\n"
        "v_exp_f32 v105, v105\n"
        "v_mfma_f32_16x16x32_bf16 v[72:75], a[244:247], a[188:191], 0\n"
        "v_cvt_pk_bf16_f32 v100, v104, v105\n"
        "v_fma_f32 v106, v106, s24, v238\n"
        "v_mfma_f32_16x16x32_bf16 v[72:75], a[240:243], a[184:187], v[72:75]\n"
        "v_exp_f32 v106, v106\n"
        "v_mfma_f32_16x16x32_bf16 v[72:75], a[236:239], a[180:183], v[72:75]\n"
        "v_fma_f32 v107, v107, s24, v238\n"
        "v_cvt_pk_bf16_f32 v218, v162, v163\n"
        "v_mfma_f32_16x16x32_bf16 v[72:75], a[232:235], a[176:179], v[72:75]\n"
        "v_exp_f32 v107, v107\n"
        "v_mfma_f32_16x16x32_bf16 v[72:75], a[228:231], a[172:175], v[72:75]\n"
        "s_add_u32 m0, s41, 0\n"
        "v_cvt_pk_bf16_f32 v219, v164, v165\n"
        "v_cvt_pk_bf16_f32 v101, v106, v107\n"
        "v_mfma_f32_16x16x32_bf16 v[72:75], a[224:227], a[168:171], v[72:75]\n"
        "v_fma_f32 v108, v108, s24, v238\n"
        "v_fma_f32 v109, v109, s24, v238\n"
        "v_mfma_f32_16x16x32_bf16 v[76:79], a[224:227], a[192:195], 0\n"
        "ds_read_b128 a[224:227], v241 offset:12416\n"
        "v_fma_f32 v110, v110, s24, v238\n"
        "v_mfma_f32_16x16x32_bf16 v[76:79], a[228:231], a[196:199], v[76:79]\n"
        "ds_read_b128 a[228:231], v240 offset:12544\n"
        "v_fma_f32 v111, v111, s24, v238\n"
        "v_mfma_f32_16x16x32_bf16 v[76:79], a[232:235], a[200:203], v[76:79]\n"
        "buffer_load_dwordx4 v242, s[8:11], s47 offen lds\n"
        "s_sub_u32 s31, s30, s28\n"
        "v_mfma_f32_16x16x32_bf16 v[76:79], a[236:239], a[204:207], v[76:79]\n"
        "ds_read_b128 a[232:235], v241 offset:12544\n"
        "s_sub_u32 s45, s28, s29\n"
        "v_cvt_pk_bf16_f32 v220, v72, v73\n"
        "v_mfma_f32_16x16x32_bf16 v[76:79], a[240:243], a[208:211], v[76:79]\n"
        "s_add_u32 s28, s28, s31\n"
        "v_cvt_pk_bf16_f32 v221, v74, v75\n"
        "v_fma_f32 v112, v112, s24, v239\n"
        "v_mfma_f32_16x16x32_bf16 v[76:79], a[244:247], a[212:215], v[76:79]\n"
        "s_waitcnt lgkmcnt(5)\n"
        "v_exp_f32 v108, v108\n"
        "v_mfma_f32_16x16x32_bf16 v[162:165], a[72:75], a[248:251], 0\n"
        "s_waitcnt lgkmcnt(4)\n"
        "v_exp_f32 v109, v109\n"
        "v_mfma_f32_16x16x32_bf16 v[162:165], a[76:79], a[216:219], v[162:165]\n"
        "s_waitcnt lgkmcnt(3)\n"
        "v_exp_f32 v110, v110\n"
        "v_mfma_f32_16x16x32_bf16 v[162:165], a[80:83], a[220:223], v[162:165]\n"
        "s_waitcnt lgkmcnt(2)\n"
        "v_exp_f32 v111, v111\n"
        "v_mfma_f32_16x16x32_bf16 v[162:165], a[84:87], a[224:227], v[162:165]\n"
        "s_waitcnt lgkmcnt(1)\n"
        "v_exp_f32 v112, v112\n"
        "v_mfma_f32_16x16x32_bf16 v[162:165], a[88:91], a[228:231], v[162:165]\n"
        "s_waitcnt lgkmcnt(0)\n"
        "v_cvt_pk_bf16_f32 v222, v76, v77\n"
        "v_cvt_pk_bf16_f32 v223, v78, v79\n"
        "v_mfma_f32_16x16x32_bf16 v[162:165], a[92:95], a[232:235], v[162:165]\n"
        "s_mov_b32 s30, s29\n"
        "v_cvt_pk_bf16_f32 v102, v108, v109\n"
        "v_cvt_pk_bf16_f32 v103, v110, v111\n"
        "v_mfma_f32_16x16x32_bf16 v[72:75], a[116:119], a[232:235], 0\n"
        "s_add_u32 m0, s41, 1024\n"
        "v_fma_f32 v113, v113, s24, v239\n"
        "v_fma_f32 v114, v114, s24, v239\n"
        "v_mfma_f32_16x16x32_bf16 v[72:75], a[112:115], a[228:231], v[72:75]\n"
        "s_add_u32 s29, s29, s45\n"
        "v_exp_f32 v113, v113\n"
        "v_mfma_f32_16x16x32_bf16 v[72:75], a[108:111], a[224:227], v[72:75]\n"
        "buffer_load_dwordx4 v243, s[8:11], s47 offen lds\n"
        "v_mfma_f32_16x16x32_bf16 v[72:75], a[104:107], a[220:223], v[72:75]\n"
        "v_exp_f32 v114, v114\n"
        "v_mfma_f32_16x16x32_bf16 v[72:75], a[100:103], a[216:219], v[72:75]\n"
        "v_cvt_pk_bf16_f32 v192, v162, v163\n"
        "v_cvt_pk_bf16_f32 v193, v164, v165\n"
        "v_mfma_f32_16x16x32_bf16 v[72:75], a[96:99], a[248:251], v[72:75]\n"
        "v_cvt_pk_bf16_f32 v112, v112, v113\n"
        "v_fma_f32 v115, v115, s24, v239\n"
        "v_mfma_f32_16x16x32_bf16 v[76:79], a[120:123], a[248:251], 0\n"
        "v_exp_f32 v115, v115\n"
        "v_mfma_f32_16x16x32_bf16 v[76:79], a[124:127], a[216:219], v[76:79]\n"
        "v_cvt_pk_bf16_f32 v113, v114, v115\n"
        "v_fma_f32 v116, v116, s24, v239\n"
        "v_mfma_f32_16x16x32_bf16 v[76:79], a[128:131], a[220:223], v[76:79]\n"
        "v_exp_f32 v116, v116\n"
        "v_mfma_f32_16x16x32_bf16 v[76:79], a[132:135], a[224:227], v[76:79]\n"
        "v_cvt_pk_bf16_f32 v194, v72, v73\n"
        "v_cvt_pk_bf16_f32 v195, v74, v75\n"
        "v_mfma_f32_16x16x32_bf16 v[76:79], a[136:139], a[228:231], v[76:79]\n"
        "v_fma_f32 v117, v117, s24, v239\n"
        "v_fma_f32 v118, v118, s24, v239\n"
        "v_mfma_f32_16x16x32_bf16 v[76:79], a[140:143], a[232:235], v[76:79]\n"
        "v_exp_f32 v117, v117\n"
        "v_mfma_f32_16x16x32_bf16 v[162:165], a[20:23], a[232:235], 0\n"
        "v_exp_f32 v118, v118\n"
        "v_mfma_f32_16x16x32_bf16 v[162:165], a[16:19], a[228:231], v[162:165]\n"
        "v_cvt_pk_bf16_f32 v114, v116, v117\n"
        "v_fma_f32 v119, v119, s24, v239\n"
        "v_mfma_f32_16x16x32_bf16 v[162:165], a[12:15], a[224:227], v[162:165]\n"
        "s_add_u32 m0, s41, 2048\n"
        "v_exp_f32 v119, v119\n"
        "v_mfma_f32_16x16x32_bf16 v[162:165], a[8:11], a[220:223], v[162:165]\n"
        "v_cvt_pk_bf16_f32 v208, v76, v77\n"
        "v_cvt_pk_bf16_f32 v209, v78, v79\n"
        "v_mfma_f32_16x16x32_bf16 v[162:165], a[4:7], a[216:219], v[162:165]\n"
        "buffer_load_dwordx4 v244, s[8:11], s47 offen lds\n"
        "v_mfma_f32_16x16x32_bf16 v[162:165], a[0:3], a[248:251], v[162:165]\n"
        "v_cvt_pk_bf16_f32 v115, v118, v119\n"
        "v_fma_f32 v120, v120, s24, v239\n"
        "v_mfma_f32_16x16x32_bf16 v[72:75], a[24:27], a[248:251], 0\n"
        "v_exp_f32 v120, v120\n"
        "v_mfma_f32_16x16x32_bf16 v[72:75], a[28:31], a[216:219], v[72:75]\n"
        "v_fma_f32 v121, v121, s24, v239\n"
        "v_fma_f32 v122, v122, s24, v239\n"
        "v_mfma_f32_16x16x32_bf16 v[72:75], a[32:35], a[220:223], v[72:75]\n"
        "v_exp_f32 v121, v121\n"
        "v_mfma_f32_16x16x32_bf16 v[72:75], a[36:39], a[224:227], v[72:75]\n"
        "v_exp_f32 v122, v122\n"
        "v_mfma_f32_16x16x32_bf16 v[72:75], a[40:43], a[228:231], v[72:75]\n"
        "v_cvt_pk_bf16_f32 v196, v162, v163\n"
        "v_cvt_pk_bf16_f32 v197, v164, v165\n"
        "v_mfma_f32_16x16x32_bf16 v[72:75], a[44:47], a[232:235], v[72:75]\n"
        "v_cvt_pk_bf16_f32 v116, v120, v121\n"
        "v_fma_f32 v123, v123, s24, v239\n"
        "v_mfma_f32_16x16x32_bf16 v[76:79], a[68:71], a[232:235], 0\n"
        "v_exp_f32 v123, v123\n"
        "v_mfma_f32_16x16x32_bf16 v[76:79], a[64:67], a[228:231], v[76:79]\n"
        "v_cvt_pk_bf16_f32 v117, v122, v123\n"
        "v_fma_f32 v124, v124, s24, v239\n"
        "v_mfma_f32_16x16x32_bf16 v[76:79], a[60:63], a[224:227], v[76:79]\n"
        "v_exp_f32 v124, v124\n"
        "v_mfma_f32_16x16x32_bf16 v[76:79], a[56:59], a[220:223], v[76:79]\n"
        "v_cvt_pk_bf16_f32 v198, v72, v73\n"
        "v_cvt_pk_bf16_f32 v199, v74, v75\n"
        "v_mfma_f32_16x16x32_bf16 v[76:79], a[52:55], a[216:219], v[76:79]\n"
        "s_add_u32 m0, s41, 12288\n"
        "v_fma_f32 v125, v125, s24, v239\n"
        "v_fma_f32 v126, v126, s24, v239\n"
        "v_mfma_f32_16x16x32_bf16 v[76:79], a[48:51], a[248:251], v[76:79]\n"
        "v_exp_f32 v125, v125\n"
        "v_mfma_f32_16x16x32_bf16 v[182:185], a[252:255], v[64:67], 0\n"
        "buffer_load_dwordx4 v242, s[8:11], s35 offen lds\n"
        "v_mfma_f32_16x16x32_bf16 v[182:185], a[252:255], v[68:71], v[182:185]\n"
        "v_exp_f32 v126, v126\n"
        "v_mfma_f32_16x16x32_bf16 v[202:205], v[132:135], v[68:71], 0\n"
        "v_cvt_pk_bf16_f32 v118, v124, v125\n"
        "v_fma_f32 v127, v127, s24, v239\n"
        "v_mfma_f32_16x16x32_bf16 v[202:205], v[128:131], v[64:67], v[202:205]\n"
        "v_exp_f32 v127, v127\n"
        "v_mfma_f32_16x16x32_bf16 v[88:91], v[136:139], v[64:67], 0\n"
        "v_cvt_pk_bf16_f32 v200, v76, v77\n"
        "v_cvt_pk_bf16_f32 v201, v78, v79\n"
        "v_mfma_f32_16x16x32_bf16 v[88:91], v[140:143], v[68:71], v[88:91]\n"
        "v_cvt_pk_bf16_f32 v206, v76, v77\n"
        "v_cvt_pk_bf16_f32 v207, v78, v79\n"
        "v_mfma_f32_16x16x32_bf16 v[92:95], v[148:151], v[68:71], 0\n"
        "s_cmp_eq_u32 s37, 100663296\n"
        "v_rcp_f32 v182, v182\n"
        "v_mfma_f32_16x16x32_bf16 v[92:95], v[144:147], v[64:67], v[92:95]\n"
        "s_cselect_b32 s46, 0, s37\n"
        "v_cvt_pk_bf16_f32 v119, v126, v127\n"
        "v_mul_f32 v202, v182, v202\n"
        "v_mfma_f32_16x16x32_bf16 v[64:67], a[252:255], v[80:83], 0\n"
        "s_add_u32 s38, s46, s43\n"
        "v_mul_f32 v203, v182, v203\n"
        "v_mul_f32 v204, v182, v204\n"
        "v_mfma_f32_16x16x32_bf16 v[64:67], a[252:255], v[84:87], v[64:67]\n"
        "s_add_u32 s39, s46, s44\n"
        "v_mul_f32 v205, v182, v205\n"
        "v_cvt_pk_bf16_f32 v202, v202, v203\n"
        "v_mfma_f32_16x16x32_bf16 v[68:71], v[132:135], v[84:87], 0\n"
        "s_add_u32 s37, s37, 6291456\n"
        "v_cvt_pk_bf16_f32 v203, v204, v205\n"
        "v_mul_f32 v88, v182, v88\n"
        "v_mfma_f32_16x16x32_bf16 v[68:71], v[128:131], v[80:83], v[68:71]\n"
        "v_rcp_f32 v64, v64\n"
        "v_mfma_f32_16x16x32_bf16 v[72:75], v[136:139], v[80:83], 0\n"
        "s_add_u32 m0, s41, 13312\n"
        "v_mul_f32 v89, v182, v89\n"
        "v_mul_f32 v90, v182, v90\n"
        "v_mfma_f32_16x16x32_bf16 v[72:75], v[140:143], v[84:87], v[72:75]\n"
        "v_mul_f32 v91, v182, v91\n"
        "v_cvt_pk_bf16_f32 v204, v88, v89\n"
        "v_mfma_f32_16x16x32_bf16 v[162:165], v[148:151], v[84:87], 0\n"
        "buffer_load_dwordx4 v243, s[8:11], s35 offen lds\n"
        "v_mfma_f32_16x16x32_bf16 v[162:165], v[144:147], v[80:83], v[162:165]\n"
        "v_cvt_pk_bf16_f32 v205, v90, v91\n"
        "v_mul_f32 v92, v182, v92\n"
        "v_mfma_f32_16x16x32_bf16 v[80:83], a[252:255], v[96:99], 0\n"
        "ds_write_b128 v245, v[202:205] offset:0\n"
        "v_mfma_f32_16x16x32_bf16 v[80:83], a[252:255], v[100:103], v[80:83]\n"
        "v_mul_f32 v93, v182, v93\n"
        "v_mul_f32 v94, v182, v94\n"
        "v_mfma_f32_16x16x32_bf16 v[84:87], v[132:135], v[100:103], 0\n"
        "v_mul_f32 v95, v182, v95\n"
        "v_cvt_pk_bf16_f32 v92, v92, v93\n"
        "v_mfma_f32_16x16x32_bf16 v[84:87], v[128:131], v[96:99], v[84:87]\n"
        "v_cvt_pk_bf16_f32 v93, v94, v95\n"
        "v_mul_f32 v68, v64, v68\n"
        "v_mfma_f32_16x16x32_bf16 v[76:79], v[136:139], v[96:99], 0\n"
        "v_rcp_f32 v80, v80\n"
        "v_mfma_f32_16x16x32_bf16 v[76:79], v[140:143], v[100:103], v[76:79]\n"
        "ds_write_b64 v246, v[92:93] offset:0\n"
        "v_mfma_f32_16x16x32_bf16 v[104:107], v[148:151], v[100:103], 0\n"
        "v_mul_f32 v69, v64, v69\n"
        "v_mul_f32 v70, v64, v70\n"
        "v_mfma_f32_16x16x32_bf16 v[104:107], v[144:147], v[96:99], v[104:107]\n"
        "v_mul_f32 v71, v64, v71\n"
        "v_cvt_pk_bf16_f32 v68, v68, v69\n"
        "v_mfma_f32_16x16x32_bf16 v[96:99], a[252:255], v[112:115], 0\n"
        "v_cvt_pk_bf16_f32 v69, v70, v71\n"
        "v_mul_f32 v72, v64, v72\n"
        "v_mfma_f32_16x16x32_bf16 v[96:99], a[252:255], v[116:119], v[96:99]\n"
        "v_mul_f32 v73, v64, v73\n"
        "v_mul_f32 v74, v64, v74\n"
        "v_mfma_f32_16x16x32_bf16 v[108:111], v[132:135], v[116:119], 0\n"
        "s_add_u32 m0, s41, 14336\n"
        "v_mul_f32 v75, v64, v75\n"
        "v_cvt_pk_bf16_f32 v70, v72, v73\n"
        "v_mfma_f32_16x16x32_bf16 v[108:111], v[128:131], v[112:115], v[108:111]\n"
        "v_mov_b32 v128, v212\n"
        "v_mov_b32 v129, v213\n"
        "v_mfma_f32_16x16x32_bf16 v[120:123], v[136:139], v[112:115], 0\n"
        "buffer_load_dwordx4 v244, s[8:11], s35 offen lds\n"
        "v_mfma_f32_16x16x32_bf16 v[120:123], v[140:143], v[116:119], v[120:123]\n"
        "ds_read_b128 a[236:239], v240 offset:13824\n"
        "v_mov_b32 v136, v214\n"
        "v_mfma_f32_16x16x32_bf16 v[124:127], v[148:151], v[116:119], 0\n"
        "ds_read_b128 a[240:243], v241 offset:13824\n"
        "v_mov_b32 v137, v215\n"
        "v_mfma_f32_16x16x32_bf16 v[124:127], v[144:147], v[112:115], v[124:127]\n"
        "ds_read_b128 a[244:247], v240 offset:13952\n"
        "v_mov_b32 v144, v216\n"
        "v_mfma_f32_16x16x32_bf16 v[100:103], a[248:251], a[144:147], 0\n"
        "v_mov_b32 v145, v217\n"
        "v_cvt_pk_bf16_f32 v71, v74, v75\n"
        "v_mfma_f32_16x16x32_bf16 v[100:103], a[216:219], a[148:151], v[100:103]\n"
        "ds_write_b128 v245, v[68:71] offset:256\n"
        "v_mfma_f32_16x16x32_bf16 v[100:103], a[220:223], a[152:155], v[100:103]\n"
        "v_mov_b32 v130, v218\n"
        "v_mov_b32 v131, v219\n"
        "v_mfma_f32_16x16x32_bf16 v[100:103], a[224:227], a[156:159], v[100:103]\n"
        "v_mov_b32 v138, v220\n"
        "v_mov_b32 v139, v221\n"
        "v_mfma_f32_16x16x32_bf16 v[100:103], a[228:231], a[160:163], v[100:103]\n"
        "v_mul_f32 v162, v64, v162\n"
        "v_mul_f32 v163, v64, v163\n"
        "v_mfma_f32_16x16x32_bf16 v[100:103], a[232:235], a[164:167], v[100:103]\n"
        "v_mul_f32 v164, v64, v164\n"
        "v_mul_f32 v165, v64, v165\n"
        "v_mfma_f32_16x16x32_bf16 v[112:115], a[232:235], a[188:191], 0\n"
        "v_mov_b32 v146, v222\n"
        "v_mov_b32 v147, v223\n"
        "v_mfma_f32_16x16x32_bf16 v[112:115], a[228:231], a[184:187], v[112:115]\n"
        "v_cvt_pk_bf16_f32 v162, v162, v163\n"
        "v_cvt_pk_bf16_f32 v163, v164, v165\n"
        "v_mfma_f32_16x16x32_bf16 v[112:115], a[224:227], a[180:183], v[112:115]\n"
        "ds_write_b64 v246, v[162:163] offset:256\n"
        "v_mfma_f32_16x16x32_bf16 v[112:115], a[220:223], a[176:179], v[112:115]\n"
        "v_cvt_pk_bf16_f32 v132, v100, v101\n"
        "v_cvt_pk_bf16_f32 v133, v102, v103\n"
        "v_mfma_f32_16x16x32_bf16 v[112:115], a[216:219], a[172:175], v[112:115]\n"
        "v_mul_f32 v84, v80, v84\n"
        "v_mul_f32 v85, v80, v85\n"
        "v_mfma_f32_16x16x32_bf16 v[112:115], a[248:251], a[168:171], v[112:115]\n"
        "v_rcp_f32 v96, v96\n"
        "v_mfma_f32_16x16x32_bf16 v[116:119], a[248:251], a[192:195], 0\n"
        "ds_read_b128 a[248:251], v241 offset:13952\n"
        "v_mul_f32 v86, v80, v86\n"
        "v_mfma_f32_16x16x32_bf16 v[116:119], a[216:219], a[196:199], v[116:119]\n"
        "ds_read_b128 a[216:219], v240 offset:14080\n"
        "v_mul_f32 v87, v80, v87\n"
        "v_mfma_f32_16x16x32_bf16 v[116:119], a[220:223], a[200:203], v[116:119]\n"
        "ds_read_b128 a[220:223], v241 offset:14080\n"
        "v_cvt_pk_bf16_f32 v140, v112, v113\n"
        "v_mfma_f32_16x16x32_bf16 v[116:119], a[224:227], a[204:207], v[116:119]\n"
        "v_cvt_pk_bf16_f32 v141, v114, v115\n"
        "v_cvt_pk_bf16_f32 v84, v84, v85\n"
        "v_mfma_f32_16x16x32_bf16 v[116:119], a[228:231], a[208:211], v[116:119]\n"
        "v_cvt_pk_bf16_f32 v85, v86, v87\n"
        "v_mul_f32 v76, v80, v76\n"
        "v_mfma_f32_16x16x32_bf16 v[116:119], a[232:235], a[212:215], v[116:119]\n"
        "s_waitcnt lgkmcnt(7)\n"
        "v_mul_f32 v77, v80, v77\n"
        "v_mul_f32 v78, v80, v78\n"
        "v_mfma_f32_16x16x32_bf16 v[100:103], a[236:239], a[144:147], 0\n"
        "s_waitcnt lgkmcnt(6)\n"
        "v_mul_f32 v79, v80, v79\n"
        "v_cvt_pk_bf16_f32 v86, v76, v77\n"
        "v_mfma_f32_16x16x32_bf16 v[100:103], a[240:243], a[148:151], v[100:103]\n"
        "s_waitcnt lgkmcnt(5)\n"
        "v_cvt_pk_bf16_f32 v87, v78, v79\n"
        "v_mul_f32 v104, v80, v104\n"
        "v_mfma_f32_16x16x32_bf16 v[100:103], a[244:247], a[152:155], v[100:103]\n"
        "s_waitcnt lgkmcnt(2)\n"
        "ds_write_b128 v245, v[84:87] offset:512\n"
        "v_mfma_f32_16x16x32_bf16 v[100:103], a[248:251], a[156:159], v[100:103]\n"
        "s_waitcnt lgkmcnt(2)\n"
        "v_cvt_pk_bf16_f32 v148, v116, v117\n"
        "v_cvt_pk_bf16_f32 v149, v118, v119\n"
        "v_mfma_f32_16x16x32_bf16 v[100:103], a[216:219], a[160:163], v[100:103]\n"
        "s_waitcnt lgkmcnt(1)\n"
        "v_mul_f32 v105, v80, v105\n"
        "v_mul_f32 v106, v80, v106\n"
        "v_mfma_f32_16x16x32_bf16 v[100:103], a[220:223], a[164:167], v[100:103]\n"
        "v_mul_f32 v107, v80, v107\n"
        "v_cvt_pk_bf16_f32 v104, v104, v105\n"
        "v_mfma_f32_16x16x32_bf16 v[112:115], a[220:223], a[188:191], 0\n"
        "v_cvt_pk_bf16_f32 v105, v106, v107\n"
        "v_mul_f32 v108, v96, v108\n"
        "v_mfma_f32_16x16x32_bf16 v[112:115], a[216:219], a[184:187], v[112:115]\n"
        "ds_write_b64 v246, v[104:105] offset:512\n"
        "v_mfma_f32_16x16x32_bf16 v[112:115], a[248:251], a[180:183], v[112:115]\n"
        "v_cvt_pk_bf16_f32 v134, v100, v101\n"
        "v_cvt_pk_bf16_f32 v135, v102, v103\n"
        "v_mfma_f32_16x16x32_bf16 v[112:115], a[244:247], a[176:179], v[112:115]\n"
        "v_mul_f32 v109, v96, v109\n"
        "v_mul_f32 v110, v96, v110\n"
        "v_mfma_f32_16x16x32_bf16 v[112:115], a[240:243], a[172:175], v[112:115]\n"
        "v_mul_f32 v111, v96, v111\n"
        "v_cvt_pk_bf16_f32 v108, v108, v109\n"
        "v_mfma_f32_16x16x32_bf16 v[112:115], a[236:239], a[168:171], v[112:115]\n"
        "v_cvt_pk_bf16_f32 v109, v110, v111\n"
        "v_mul_f32 v120, v96, v120\n"
        "v_mfma_f32_16x16x32_bf16 v[116:119], a[236:239], a[192:195], 0\n"
        "v_mul_f32 v121, v96, v121\n"
        "v_mul_f32 v122, v96, v122\n"
        "v_mfma_f32_16x16x32_bf16 v[116:119], a[240:243], a[196:199], v[116:119]\n"
        "v_mul_f32 v123, v96, v123\n"
        "v_cvt_pk_bf16_f32 v110, v120, v121\n"
        "v_mfma_f32_16x16x32_bf16 v[116:119], a[244:247], a[200:203], v[116:119]\n"
        "v_cvt_pk_bf16_f32 v142, v112, v113\n"
        "v_cvt_pk_bf16_f32 v143, v114, v115\n"
        "v_mfma_f32_16x16x32_bf16 v[116:119], a[248:251], a[204:207], v[116:119]\n"
        "v_cvt_pk_bf16_f32 v111, v122, v123\n"
        "v_mul_f32 v124, v96, v124\n"
        "v_mfma_f32_16x16x32_bf16 v[116:119], a[216:219], a[208:211], v[116:119]\n"
        "ds_write_b128 v245, v[108:111] offset:768\n"
        "v_mfma_f32_16x16x32_bf16 v[116:119], a[220:223], a[212:215], v[116:119]\n"
        "v_mul_f32 v125, v96, v125\n"
        "v_mul_f32 v126, v96, v126\n"
        "v_mfma_f32_16x16x32_bf16 v[100:103], a[20:23], a[220:223], 0\n"
        "v_mul_f32 v127, v96, v127\n"
        "v_cvt_pk_bf16_f32 v124, v124, v125\n"
        "v_mfma_f32_16x16x32_bf16 v[100:103], a[16:19], a[216:219], v[100:103]\n"
        "v_cvt_pk_bf16_f32 v125, v126, v127\n"
        "v_mfma_f32_16x16x32_bf16 v[100:103], a[12:15], a[248:251], v[100:103]\n"
        "v_cvt_pk_bf16_f32 v150, v116, v117\n"
        "v_cvt_pk_bf16_f32 v151, v118, v119\n"
        "v_mfma_f32_16x16x32_bf16 v[100:103], a[8:11], a[244:247], v[100:103]\n"
        "ds_write_b64 v246, v[124:125] offset:768\n"
        "v_mfma_f32_16x16x32_bf16 v[100:103], a[4:7], a[240:243], v[100:103]\n"
        "v_mfma_f32_16x16x32_bf16 v[100:103], a[0:3], a[236:239], v[100:103]\n"
        "v_mfma_f32_16x16x32_bf16 v[112:115], a[24:27], a[236:239], 0\n"
        "v_mfma_f32_16x16x32_bf16 v[112:115], a[28:31], a[240:243], v[112:115]\n"
        "v_mfma_f32_16x16x32_bf16 v[112:115], a[32:35], a[244:247], v[112:115]\n"
        "v_mfma_f32_16x16x32_bf16 v[112:115], a[36:39], a[248:251], v[112:115]\n"
        "v_mfma_f32_16x16x32_bf16 v[112:115], a[40:43], a[216:219], v[112:115]\n"
        "v_mfma_f32_16x16x32_bf16 v[112:115], a[44:47], a[220:223], v[112:115]\n"
        "v_mfma_f32_16x16x32_bf16 v[116:119], a[68:71], a[220:223], 0\n"
        "ds_read_b128 v[162:165], v251 offset:0\n"
        "v_cvt_pk_bf16_f32 v216, v100, v101\n"
        "v_mfma_f32_16x16x32_bf16 v[116:119], a[64:67], a[216:219], v[116:119]\n"
        "v_cvt_pk_bf16_f32 v217, v102, v103\n"
        "v_mfma_f32_16x16x32_bf16 v[116:119], a[60:63], a[248:251], v[116:119]\n"
        "v_mfma_f32_16x16x32_bf16 v[116:119], a[56:59], a[244:247], v[116:119]\n"
        "v_mfma_f32_16x16x32_bf16 v[116:119], a[52:55], a[240:243], v[116:119]\n"
        "ds_read_b128 v[182:185], v251 offset:512\n"
        "v_cvt_pk_bf16_f32 v218, v112, v113\n"
        "v_mfma_f32_16x16x32_bf16 v[116:119], a[48:51], a[236:239], v[116:119]\n"
        "v_cvt_pk_bf16_f32 v219, v114, v115\n"
        "v_mfma_f32_16x16x32_bf16 v[100:103], a[72:75], a[236:239], 0\n"
        "s_add_u32 s47, s47, 6291456\n"
        "v_mfma_f32_16x16x32_bf16 v[100:103], a[76:79], a[240:243], v[100:103]\n"
        "s_add_u32 s36, s36, 2097152\n"
        "v_mfma_f32_16x16x32_bf16 v[100:103], a[80:83], a[244:247], v[100:103]\n"
        "ds_read_b128 v[202:205], v251 offset:4096\n"
        "s_xor_b32 s48, s48, 8192\n"
        "v_cvt_pk_bf16_f32 v220, v116, v117\n"
        "v_mfma_f32_16x16x32_bf16 v[100:103], a[84:87], a[248:251], v[100:103]\n"
        "v_cvt_pk_bf16_f32 v221, v118, v119\n"
        "v_cvt_pk_bf16_f32 v226, v116, v117\n"
        "v_mfma_f32_16x16x32_bf16 v[100:103], a[88:91], a[216:219], v[100:103]\n"
        "v_cvt_pk_bf16_f32 v227, v118, v119\n"
        "v_mfma_f32_16x16x32_bf16 v[100:103], a[92:95], a[220:223], v[100:103]\n"
        "v_mfma_f32_16x16x32_bf16 v[112:115], a[116:119], a[220:223], 0\n"
        "ds_read_b128 v[222:225], v251 offset:4608\n"
        "v_mfma_f32_16x16x32_bf16 v[112:115], a[112:115], a[216:219], v[112:115]\n"
        "v_mfma_f32_16x16x32_bf16 v[112:115], a[108:111], a[248:251], v[112:115]\n"
        "s_waitcnt lgkmcnt(3)\n"
        "v_pk_min_u16 v162, v162, s26\n"
        "v_pk_min_u16 v163, v163, s26\n"
        "v_mfma_f32_16x16x32_bf16 v[112:115], a[104:107], a[244:247], v[112:115]\n"
        "v_pk_min_u16 v164, v164, s26\n"
        "v_cvt_pk_bf16_f32 v212, v100, v101\n"
        "v_mfma_f32_16x16x32_bf16 v[112:115], a[100:103], a[240:243], v[112:115]\n"
        "v_cvt_pk_bf16_f32 v213, v102, v103\n"
        "v_pk_min_u16 v165, v165, s26\n"
        "v_mfma_f32_16x16x32_bf16 v[112:115], a[96:99], a[236:239], v[112:115]\n"
        "v_add_u32 v240, s31, v240\n"
        "v_add_u32 v241, s31, v241\n"
        "v_mfma_f32_16x16x32_bf16 v[116:119], a[120:123], a[236:239], 0\n"
        "s_waitcnt lgkmcnt(2)\n"
        "v_pk_min_u16 v182, v182, s26\n"
        "v_pk_min_u16 v183, v183, s26\n"
        "v_mfma_f32_16x16x32_bf16 v[116:119], a[124:127], a[240:243], v[116:119]\n"
        "v_pk_min_u16 v184, v184, s26\n"
        "v_pk_min_u16 v185, v185, s26\n"
        "v_mfma_f32_16x16x32_bf16 v[116:119], a[128:131], a[244:247], v[116:119]\n"
        "v_cvt_pk_bf16_f32 v214, v112, v113\n"
        "v_cvt_pk_bf16_f32 v215, v114, v115\n"
        "v_mfma_f32_16x16x32_bf16 v[116:119], a[132:135], a[248:251], v[116:119]\n"
        "v_mfma_f32_16x16x32_bf16 v[116:119], a[136:139], a[216:219], v[116:119]\n"
        "s_waitcnt lgkmcnt(1)\n"
        "v_pk_min_u16 v202, v202, s26\n"
        "v_pk_min_u16 v203, v203, s26\n"
        "v_mfma_f32_16x16x32_bf16 v[116:119], a[140:143], a[220:223], v[116:119]\n"
        "v_pk_min_u16 v204, v204, s26\n"
        "v_pk_min_u16 v205, v205, s26\n"
        "v_mfma_f32_16x16x32_bf16 v[64:67], v[152:155], v[156:159], v[0:3]\n"
        "v_mfma_f32_16x16x32_bf16 v[64:67], v[168:171], v[160:163], v[64:67]\n"
        "v_mfma_f32_16x16x32_bf16 v[68:71], v[172:175], v[156:159], v[4:7]\n"
        "s_waitcnt lgkmcnt(0)\n"
        "v_pk_min_u16 v222, v222, s26\n"
        "v_pk_min_u16 v223, v223, s26\n"
        "v_mfma_f32_16x16x32_bf16 v[68:71], v[188:191], v[164:167], v[68:71]\n"
        "ds_read_b128 v[162:165], v252 offset:0\n"
        "v_cvt_pk_bf16_f32 v230, v116, v117\n"
        "v_mfma_f32_16x16x32_bf16 v[80:83], v[152:155], v[176:179], v[16:19]\n"
        "v_cvt_pk_bf16_f32 v231, v118, v119\n"
        "v_pk_min_u16 v224, v224, s26\n"
        "v_mfma_f32_16x16x32_bf16 v[80:83], v[168:171], v[180:183], v[80:83]\n"
        "v_pk_min_u16 v225, v225, s26\n"
        "v_max3_f32 v232, v64, v65, v66\n"
        "v_mfma_f32_16x16x32_bf16 v[84:87], v[172:175], v[176:179], v[20:23]\n"
        "v_max3_f32 v232, v232, v67, v68\n"
        "v_max3_f32 v232, v232, v69, v70\n"
        "v_mfma_f32_16x16x32_bf16 v[84:87], v[188:191], v[184:187], v[84:87]\n"
        "ds_read_b128 v[182:185], v252 offset:512\n"
        "v_mfma_f32_16x16x32_bf16 v[96:99], v[152:155], v[196:199], v[32:35]\n"
        "v_max3_f32 v233, v80, v81, v82\n"
        "v_mfma_f32_16x16x32_bf16 v[96:99], v[168:171], v[200:203], v[96:99]\n"
        "v_mfma_f32_16x16x32_bf16 v[100:103], v[172:175], v[196:199], v[36:39]\n"
        "v_mfma_f32_16x16x32_bf16 v[100:103], v[188:191], v[204:207], v[100:103]\n"
        "ds_read_b128 v[202:205], v252 offset:4096\n"
        "v_mfma_f32_16x16x32_bf16 v[112:115], v[152:155], v[216:219], v[48:51]\n"
        "s_waitcnt lgkmcnt(2)\n"
        "v_pk_min_u16 v162, v162, s26\n"
        "v_pk_min_u16 v163, v163, s26\n"
        "v_mfma_f32_16x16x32_bf16 v[112:115], v[168:171], v[220:223], v[112:115]\n"
        "v_pk_min_u16 v164, v164, s26\n"
        "v_pk_min_u16 v165, v165, s26\n"
        "v_mfma_f32_16x16x32_bf16 v[116:119], v[172:175], v[216:219], v[52:55]\n"
        "v_max3_f32 v233, v233, v83, v84\n"
        "v_max3_f32 v233, v233, v85, v86\n"
        "v_mfma_f32_16x16x32_bf16 v[116:119], v[188:191], v[224:227], v[116:119]\n"
        "ds_read_b128 v[222:225], v252 offset:4608\n"
        "v_max3_f32 v234, v96, v97, v98\n"
        "v_mfma_f32_16x16x32_bf16 v[72:75], v[192:195], v[156:159], v[8:11]\n"
        "s_waitcnt lgkmcnt(2)\n"
        "v_pk_min_u16 v182, v182, s26\n"
        "v_pk_min_u16 v183, v183, s26\n"
        "v_mfma_f32_16x16x32_bf16 v[72:75], v[208:211], v[160:163], v[72:75]\n"
        "v_pk_min_u16 v184, v184, s26\n"
        "v_pk_min_u16 v185, v185, s26\n"
        "v_mfma_f32_16x16x32_bf16 v[76:79], v[212:215], v[156:159], v[12:15]\n"
        "v_max3_f32 v234, v234, v99, v100\n"
        "v_max3_f32 v234, v234, v101, v102\n"
        "v_mfma_f32_16x16x32_bf16 v[76:79], v[228:231], v[164:167], v[76:79]\n"
        "v_max3_f32 v235, v112, v113, v114\n"
        "v_max3_f32 v235, v235, v115, v116\n"
        "v_mfma_f32_16x16x32_bf16 v[88:91], v[192:195], v[176:179], v[24:27]\n"
        "s_barrier\n"
        "ds_read_b128 v[152:155], v247 offset:0\n"
        "s_waitcnt lgkmcnt(2)\n"
        "v_pk_min_u16 v202, v202, s26\n"
        "v_mfma_f32_16x16x32_bf16 v[88:91], v[208:211], v[180:183], v[88:91]\n"
        "ds_read_b128 v[160:163], v247 offset:4096\n"
        "v_pk_min_u16 v203, v203, s26\n"
        "v_mfma_f32_16x16x32_bf16 v[92:95], v[212:215], v[176:179], v[28:31]\n"
        "ds_read_b128 v[156:159], v247 offset:256\n"
        "v_pk_min_u16 v204, v204, s26\n"
        "v_mfma_f32_16x16x32_bf16 v[92:95], v[228:231], v[184:187], v[92:95]\n"
        "ds_read_b128 v[164:167], v247 offset:4352\n"
        "v_pk_min_u16 v205, v205, s26\n"
        "v_mfma_f32_16x16x32_bf16 v[104:107], v[192:195], v[196:199], v[40:43]\n"
        "ds_read_b128 a[216:219], v248 offset:0\n"
        "s_waitcnt lgkmcnt(5)\n"
        "v_pk_min_u16 v222, v222, s26\n"
        "v_mfma_f32_16x16x32_bf16 v[104:107], v[208:211], v[200:203], v[104:107]\n"
        "ds_read_b128 a[220:223], v248 offset:6144\n"
        "v_pk_min_u16 v223, v223, s26\n"
        "v_mfma_f32_16x16x32_bf16 v[108:111], v[212:215], v[196:199], v[44:47]\n"
        "ds_read_b128 a[224:227], v254 offset:16\n"
        "v_pk_min_u16 v224, v224, s26\n"
        "v_mfma_f32_16x16x32_bf16 v[108:111], v[228:231], v[204:207], v[108:111]\n"
        "ds_read_b128 a[228:231], v254 offset:6160\n"
        "v_pk_min_u16 v225, v225, s26\n"
        "v_mfma_f32_16x16x32_bf16 v[120:123], v[192:195], v[216:219], v[56:59]\n"
        "ds_read_b128 a[232:235], v248 offset:32\n"
        "v_max3_f32 v232, v232, v71, v72\n"
        "v_mfma_f32_16x16x32_bf16 v[120:123], v[208:211], v[220:223], v[120:123]\n"
        "v_max3_f32 v232, v232, v73, v74\n"
        "v_max3_f32 v232, v232, v75, v76\n"
        "v_mfma_f32_16x16x32_bf16 v[124:127], v[212:215], v[216:219], v[60:63]\n"
        "v_max3_f32 v232, v232, v77, v78\n"
        "v_max_f32 v232, v232, v79\n"
        "v_mfma_f32_16x16x32_bf16 v[124:127], v[228:231], v[224:227], v[124:127]\n"
        "s_waitcnt lgkmcnt(4)\n"
        "v_max3_f32 v233, v233, v87, v88\n"
        "v_max3_f32 v233, v233, v89, v90\n"
        "v_mfma_f32_16x16x32_bf16 v[172:175], v[152:155], a[216:219], 0\n"
        "s_waitcnt lgkmcnt(3)\n"
        "v_max3_f32 v233, v233, v91, v92\n"
        "v_max3_f32 v233, v233, v93, v94\n"
        "v_mfma_f32_16x16x32_bf16 v[172:175], v[160:163], a[220:223], v[172:175]\n"
        "v_max_f32 v233, v233, v95\n"
        "v_max3_f32 v234, v234, v103, v104\n"
        "v_mfma_f32_16x16x32_bf16 v[180:183], v[164:167], a[220:223], 0\n"
        "ds_read_b128 a[220:223], v248 offset:6176\n"
        "v_max3_f32 v234, v234, v105, v106\n"
        "v_mfma_f32_16x16x32_bf16 v[180:183], v[156:159], a[216:219], v[180:183]\n"
        "s_waitcnt lgkmcnt(3)\n"
        "ds_read_b128 a[216:219], v254 offset:48\n"
        "v_max3_f32 v234, v234, v107, v108\n"
        "v_mfma_f32_16x16x32_bf16 v[184:187], v[156:159], a[224:227], 0\n"
        "s_waitcnt lgkmcnt(3)\n"
        "ds_read_b128 a[236:239], v247 offset:8192\n"
        "v_max3_f32 v234, v234, v109, v110\n"
        "v_mfma_f32_16x16x32_bf16 v[184:187], v[164:167], a[228:231], v[184:187]\n"
        "ds_read_b128 a[244:247], v247 offset:12288\n"
        "v_max_f32 v234, v234, v111\n"
        "v_mfma_f32_16x16x32_bf16 v[176:179], v[160:163], a[228:231], 0\n"
        "ds_read_b128 a[228:231], v254 offset:6192\n"
        "v_max3_f32 v235, v235, v117, v118\n"
        "v_mfma_f32_16x16x32_bf16 v[176:179], v[152:155], a[224:227], v[176:179]\n"
        "s_waitcnt lgkmcnt(5)\n"
        "ds_read_b128 a[224:227], v248 offset:64\n"
        "v_max3_f32 v235, v235, v119, v120\n"
        "v_mfma_f32_16x16x32_bf16 v[212:215], v[152:155], a[232:235], 0\n"
        "s_waitcnt lgkmcnt(5)\n"
        "ds_read_b128 a[240:243], v247 offset:8448\n"
        "v_max3_f32 v235, v235, v121, v122\n"
        "v_mfma_f32_16x16x32_bf16 v[212:215], v[160:163], a[220:223], v[212:215]\n"
        "ds_read_b128 a[248:251], v247 offset:12544\n"
        "v_max3_f32 v235, v235, v123, v124\n"
        "v_mfma_f32_16x16x32_bf16 v[192:195], v[164:167], a[220:223], 0\n"
        "ds_read_b128 a[220:223], v248 offset:6208\n"
        "v_max3_f32 v235, v235, v125, v126\n"
        "v_mfma_f32_16x16x32_bf16 v[192:195], v[156:159], a[232:235], v[192:195]\n"
        "s_waitcnt lgkmcnt(7)\n"
        "ds_read_b128 a[232:235], v254 offset:80\n"
        "v_max_f32 v235, v235, v127\n"
        "v_mfma_f32_16x16x32_bf16 v[196:199], v[156:159], a[216:219], 0\n"
        "s_waitcnt lgkmcnt(5)\n"
        "v_permlane32_swap_b32 v232, v233\n"
        "v_mfma_f32_16x16x32_bf16 v[196:199], v[164:167], a[228:231], v[196:199]\n"
        "v_max_f32 v232, v232, v233\n"
        "v_mov_b32 v233, v232\n"
        "v_mfma_f32_16x16x32_bf16 v[216:219], v[160:163], a[228:231], 0\n"
        "ds_read_b128 a[228:231], v254 offset:6224\n"
        "v_xor_b32 v251, 0x2000, v251\n"
        "v_mfma_f32_16x16x32_bf16 v[216:219], v[152:155], a[216:219], v[216:219]\n"
        "s_waitcnt lgkmcnt(5)\n"
        "ds_read_b128 a[216:219], v248 offset:36864\n"
        "v_xor_b32 v252, 0x2000, v252\n"
        "v_mfma_f32_16x16x32_bf16 v[220:223], v[152:155], a[224:227], 0\n"
        "s_waitcnt lgkmcnt(3)\n"
        "v_permlane16_swap_b32 v232, v233\n"
        "v_mfma_f32_16x16x32_bf16 v[220:223], v[160:163], a[220:223], v[220:223]\n"
        "v_max_f32 v232, v232, v233\n"
        "v_mul_f32 v236, s25, v232\n"
        "v_mfma_f32_16x16x32_bf16 v[200:203], v[164:167], a[220:223], 0\n"
        "ds_read_b128 a[220:223], v248 offset:43008\n"
        "v_mul_f32 v237, s25, v232\n"
        "v_mfma_f32_16x16x32_bf16 v[200:203], v[156:159], a[224:227], v[200:203]\n"
        "s_waitcnt lgkmcnt(3)\n"
        "ds_read_b128 a[224:227], v254 offset:36880\n"
        "v_add_u32 v245, s45, v245\n"
        "v_mfma_f32_16x16x32_bf16 v[204:207], v[156:159], a[232:235], 0\n"
        "s_waitcnt lgkmcnt(3)\n"
        "v_permlane32_swap_b32 v236, v237\n"
        "v_mfma_f32_16x16x32_bf16 v[204:207], v[164:167], a[228:231], v[204:207]\n"
        "v_permlane32_swap_b32 v234, v235\n"
        "v_mfma_f32_16x16x32_bf16 v[224:227], v[160:163], a[228:231], 0\n"
        "ds_read_b128 a[228:231], v254 offset:43024\n"
        "v_max_f32 v234, v234, v235\n"
        "v_mfma_f32_16x16x32_bf16 v[224:227], v[152:155], a[232:235], v[224:227]\n"
        "s_waitcnt lgkmcnt(3)\n"
        "ds_read_b128 a[232:235], v248 offset:36896\n"
        "v_mov_b32 v235, v234\n"
        "v_mfma_f32_16x16x32_bf16 v[172:175], a[236:239], a[216:219], v[172:175]\n"
        "s_waitcnt lgkmcnt(3)\n"
        "v_fma_f32 v64, v64, s24, v236\n"
        "v_fma_f32 v65, v65, s24, v236\n"
        "v_mfma_f32_16x16x32_bf16 v[172:175], a[244:247], a[220:223], v[172:175]\n"
        "v_permlane16_swap_b32 v234, v235\n"
        "v_mfma_f32_16x16x32_bf16 v[180:183], a[248:251], a[220:223], v[180:183]\n"
        "ds_read_b128 a[220:223], v248 offset:43040\n"
        "v_max_f32 v234, v234, v235\n"
        "v_mfma_f32_16x16x32_bf16 v[180:183], a[240:243], a[216:219], v[180:183]\n"
        "s_waitcnt lgkmcnt(3)\n"
        "ds_read_b128 a[216:219], v254 offset:36912\n"
        "v_mul_f32 v238, s25, v234\n"
        "v_mfma_f32_16x16x32_bf16 v[184:187], a[240:243], a[224:227], v[184:187]\n"
        "s_waitcnt lgkmcnt(3)\n"
        "ds_read_b128 v[152:155], v247 offset:16384\n"
        "v_mul_f32 v239, s25, v234\n"
        "v_mfma_f32_16x16x32_bf16 v[184:187], a[248:251], a[228:231], v[184:187]\n"
        "ds_read_b128 v[160:163], v247 offset:20480\n"
        "v_fma_f32 v66, v66, s24, v236\n"
        "v_mfma_f32_16x16x32_bf16 v[176:179], a[244:247], a[228:231], v[176:179]\n"
        "ds_read_b128 a[228:231], v254 offset:43056\n"
        "v_fma_f32 v67, v67, s24, v236\n"
        "v_mfma_f32_16x16x32_bf16 v[176:179], a[236:239], a[224:227], v[176:179]\n"
        "s_waitcnt lgkmcnt(5)\n"
        "ds_read_b128 a[224:227], v248 offset:36928\n"
        "v_fma_f32 v68, v68, s24, v236\n"
        "v_mfma_f32_16x16x32_bf16 v[212:215], a[236:239], a[232:235], v[212:215]\n"
        "s_waitcnt lgkmcnt(5)\n"
        "ds_read_b128 v[156:159], v247 offset:16640\n"
        "v_fma_f32 v69, v69, s24, v236\n"
        "v_mfma_f32_16x16x32_bf16 v[212:215], a[244:247], a[220:223], v[212:215]\n"
        "ds_read_b128 v[164:167], v247 offset:20736\n"
        "v_fma_f32 v70, v70, s24, v236\n"
        "v_mfma_f32_16x16x32_bf16 v[192:195], a[248:251], a[220:223], v[192:195]\n"
        "ds_read_b128 a[220:223], v248 offset:43072\n"
        "v_fma_f32 v71, v71, s24, v236\n"
        "v_mfma_f32_16x16x32_bf16 v[192:195], a[240:243], a[232:235], v[192:195]\n"
        "s_waitcnt lgkmcnt(7)\n"
        "ds_read_b128 a[232:235], v254 offset:36944\n"
        "v_fma_f32 v72, v72, s24, v236\n"
        "v_mfma_f32_16x16x32_bf16 v[196:199], a[240:243], a[216:219], v[196:199]\n"
        "s_waitcnt lgkmcnt(5)\n"
        "v_permlane32_swap_b32 v238, v239\n"
        "v_mfma_f32_16x16x32_bf16 v[196:199], a[248:251], a[228:231], v[196:199]\n"
        "v_exp_f32 v64, v64\n"
        "v_mfma_f32_16x16x32_bf16 v[216:219], a[244:247], a[228:231], v[216:219]\n"
        "ds_read_b128 a[228:231], v254 offset:43088\n"
        "v_fma_f32 v73, v73, s24, v236\n"
        "v_mfma_f32_16x16x32_bf16 v[216:219], a[236:239], a[216:219], v[216:219]\n"
        "s_waitcnt lgkmcnt(5)\n"
        "ds_read_b128 a[216:219], v248 offset:12288\n"
        "v_fma_f32 v74, v74, s24, v236\n"
        "v_mfma_f32_16x16x32_bf16 v[220:223], a[236:239], a[224:227], v[220:223]\n"
        "s_waitcnt lgkmcnt(3)\n"
        "v_exp_f32 v65, v65\n"
        "v_mfma_f32_16x16x32_bf16 v[220:223], a[244:247], a[220:223], v[220:223]\n"
        "v_exp_f32 v66, v66\n"
        "v_mfma_f32_16x16x32_bf16 v[200:203], a[248:251], a[220:223], v[200:203]\n"
        "ds_read_b128 a[220:223], v248 offset:49152\n"
        "v_cvt_pk_bf16_f32 v64, v64, v65\n"
        "v_mfma_f32_16x16x32_bf16 v[200:203], a[240:243], a[224:227], v[200:203]\n"
        "s_waitcnt lgkmcnt(3)\n"
        "ds_read_b128 a[224:227], v254 offset:12304\n"
        "v_fma_f32 v75, v75, s24, v236\n"
        "v_mfma_f32_16x16x32_bf16 v[204:207], a[240:243], a[232:235], v[204:207]\n"
        "s_waitcnt lgkmcnt(3)\n"
        "v_exp_f32 v67, v67\n"
        "v_mfma_f32_16x16x32_bf16 v[204:207], a[248:251], a[228:231], v[204:207]\n"
        "v_exp_f32 v68, v68\n"
        "v_mfma_f32_16x16x32_bf16 v[224:227], a[244:247], a[228:231], v[224:227]\n"
        "ds_read_b128 a[228:231], v254 offset:49168\n"
        "v_cvt_pk_bf16_f32 v65, v66, v67\n"
        "v_mfma_f32_16x16x32_bf16 v[224:227], a[236:239], a[232:235], v[224:227]\n"
        "s_waitcnt lgkmcnt(3)\n"
        "ds_read_b128 a[232:235], v248 offset:12320\n"
        "v_add_u32 v246, s45, v246\n"
        "v_mfma_f32_16x16x32_bf16 v[172:175], v[152:155], a[216:219], v[172:175]\n"
        "s_waitcnt lgkmcnt(3)\n"
        "v_exp_f32 v69, v69\n"
        "v_mfma_f32_16x16x32_bf16 v[172:175], v[160:163], a[220:223], v[172:175]\n"
        "v_cvt_pk_bf16_f32 v66, v68, v69\n"
        "v_add_u32 v247, s45, v247\n"
        "v_mfma_f32_16x16x32_bf16 v[180:183], v[164:167], a[220:223], v[180:183]\n"
        "ds_read_b128 a[220:223], v248 offset:49184\n"
        "v_mfma_f32_16x16x32_bf16 v[180:183], v[156:159], a[216:219], v[180:183]\n"
        "s_waitcnt lgkmcnt(3)\n"
        "ds_read_b128 a[216:219], v254 offset:12336\n"
        "v_mfma_f32_16x16x32_bf16 v[184:187], v[156:159], a[224:227], v[184:187]\n"
        "s_waitcnt lgkmcnt(3)\n"
        "v_exp_f32 v70, v70\n"
        "v_mfma_f32_16x16x32_bf16 v[184:187], v[164:167], a[228:231], v[184:187]\n"
        "v_exp_f32 v71, v71\n"
        "v_mfma_f32_16x16x32_bf16 v[176:179], v[160:163], a[228:231], v[176:179]\n"
        "ds_read_b128 a[228:231], v254 offset:49200\n"
        "v_cvt_pk_bf16_f32 v67, v70, v71\n"
        "v_mfma_f32_16x16x32_bf16 v[176:179], v[152:155], a[224:227], v[176:179]\n"
        "s_waitcnt lgkmcnt(3)\n"
        "ds_read_b128 a[224:227], v248 offset:12352\n"
        "v_mfma_f32_16x16x32_bf16 v[212:215], v[152:155], a[232:235], v[212:215]\n"
        "s_waitcnt lgkmcnt(3)\n"
        "s_waitcnt vmcnt(0)\n"
        "s_barrier\n"
        "v_exp_f32 v72, v72\n"
        "v_mfma_f32_16x16x32_bf16 v[212:215], v[160:163], a[220:223], v[212:215]\n"
        "v_exp_f32 v73, v73\n"
        "v_mfma_f32_16x16x32_bf16 v[192:195], v[164:167], a[220:223], v[192:195]\n"
        "ds_read_b128 a[220:223], v248 offset:49216\n"
        "v_cvt_pk_bf16_f32 v68, v72, v73\n"
        "v_mfma_f32_16x16x32_bf16 v[192:195], v[156:159], a[232:235], v[192:195]\n"
        "s_waitcnt lgkmcnt(3)\n"
        "ds_read_b128 a[232:235], v254 offset:12368\n"
        "v_mfma_f32_16x16x32_bf16 v[196:199], v[156:159], a[216:219], v[196:199]\n"
        "s_waitcnt lgkmcnt(3)\n"
        "ds_read_b128 a[236:239], v240 offset:0\n"
        "v_mfma_f32_16x16x32_bf16 v[196:199], v[164:167], a[228:231], v[196:199]\n"
        "ds_read_b128 a[240:243], v241 offset:0\n"
        "v_mfma_f32_16x16x32_bf16 v[216:219], v[160:163], a[228:231], v[216:219]\n"
        "ds_read_b128 a[228:231], v254 offset:49232\n"
        "v_mfma_f32_16x16x32_bf16 v[216:219], v[152:155], a[216:219], v[216:219]\n"
        "s_waitcnt lgkmcnt(5)\n"
        "ds_read_b128 a[244:247], v240 offset:128\n"
        "v_mfma_f32_16x16x32_bf16 v[220:223], v[152:155], a[224:227], v[220:223]\n"
        "s_waitcnt lgkmcnt(5)\n"
        "ds_read_b128 a[248:251], v241 offset:128\n"
        "v_mfma_f32_16x16x32_bf16 v[220:223], v[160:163], a[220:223], v[220:223]\n"
        "ds_read_b128 a[216:219], v240 offset:256\n"
        "v_mfma_f32_16x16x32_bf16 v[200:203], v[164:167], a[220:223], v[200:203]\n"
        "ds_read_b128 a[220:223], v241 offset:256\n"
        "v_mfma_f32_16x16x32_bf16 v[200:203], v[156:159], a[224:227], v[200:203]\n"
        "s_waitcnt lgkmcnt(7)\n"
        "v_exp_f32 v74, v74\n"
        "v_mfma_f32_16x16x32_bf16 v[204:207], v[156:159], a[232:235], v[204:207]\n"
        "s_waitcnt lgkmcnt(4)\n"
        "v_exp_f32 v75, v75\n"
        "v_mfma_f32_16x16x32_bf16 v[204:207], v[164:167], a[228:231], v[204:207]\n"
        "s_sub_u32 s27, s27, 1\n"
        "v_cvt_pk_bf16_f32 v69, v74, v75\n"
        "v_mfma_f32_16x16x32_bf16 v[224:227], v[160:163], a[228:231], v[224:227]\n"
        "s_cmp_lg_u32 s27, 0\n"
        "v_mfma_f32_16x16x32_bf16 v[224:227], v[152:155], a[232:235], v[224:227]\n"
        "s_cbranch_scc1 10b\n"
        "20:\n"
        "s_waitcnt lgkmcnt(0)\n"
        "s_nop 7\n"
        "v_mfma_f32_16x16x32_bf16 v[48:51], a[252:255], v[64:67], 0\n"
        "s_and_b32 s46, s42, 1\n"
        "s_lshl_b32 s31, s46, 8\n"
        "s_mul_i32 s45, s46, 786432\n"
        "s_sub_u32 s37, s37, s45\n"
        "s_cmp_eq_u32 s46, 0\n"
        "s_cselect_b32 s43, s43, s44\n"
        "s_add_u32 s28, s37, 786432\n"
        "s_cmp_eq_u32 s28, 100663296\n"
        "s_cselect_b32 s28, 0, s28\n"
        "v_fma_f32 v76, v76, s24, v236\n"
        "v_exp_f32 v76, v76\n"
        "v_fma_f32 v77, v77, s24, v236\n"
        "v_exp_f32 v77, v77\n"
        "v_fma_f32 v78, v78, s24, v236\n"
        "v_cvt_pk_bf16_f32 v70, v76, v77\n"
        "v_exp_f32 v78, v78\n"
        "v_fma_f32 v79, v79, s24, v236\n"
        "v_exp_f32 v79, v79\n"
        "v_subrev_u32 v247, s31, v247\n"
        "v_cvt_pk_bf16_f32 v71, v78, v79\n"
        "s_nop 1\n"
        "v_mfma_f32_16x16x32_bf16 v[48:51], a[252:255], v[68:71], v[48:51]\n"
        "v_cvt_pk_bf16_f32 v232, v172, v176\n"
        "v_cvt_pk_bf16_f32 v233, v212, v216\n"
        "v_cvt_pk_bf16_f32 v234, v220, v224\n"
        "v_fma_f32 v80, v80, s24, v237\n"
        "v_exp_f32 v80, v80\n"
        "v_mfma_f32_16x16x32_bf16 v[52:55], v[132:135], v[68:71], 0\n"
        "buffer_store_dwordx3 v[232:234], v250, s[16:19], s38 offen offset:0 sc1\n"
        "ds_read_b128 a[216:219], v248 offset:0\n"
        "v_mfma_f32_16x16x32_bf16 v[52:55], v[128:131], v[64:67], v[52:55]\n"
        "ds_read_b128 a[220:223], v248 offset:6144\n"
        "v_cvt_pk_bf16_f32 v232, v173, v177\n"
        "v_cvt_pk_bf16_f32 v233, v213, v217\n"
        "v_cvt_pk_bf16_f32 v234, v221, v225\n"
        "v_fma_f32 v81, v81, s24, v237\n"
        "v_fma_f32 v82, v82, s24, v237\n"
        "v_mfma_f32_16x16x32_bf16 v[56:59], v[136:139], v[64:67], 0\n"
        "buffer_store_dwordx3 v[232:234], v250, s[16:19], s38 offen offset:384 sc1\n"
        "ds_read_b128 a[224:227], v248 offset:36864\n"
        "v_mfma_f32_16x16x32_bf16 v[56:59], v[140:143], v[68:71], v[56:59]\n"
        "ds_read_b128 a[228:231], v248 offset:43008\n"
        "v_cvt_pk_bf16_f32 v232, v174, v178\n"
        "v_cvt_pk_bf16_f32 v233, v214, v218\n"
        "v_cvt_pk_bf16_f32 v234, v222, v226\n"
        "v_exp_f32 v81, v81\n"
        "v_mfma_f32_16x16x32_bf16 v[60:63], v[148:151], v[68:71], 0\n"
        "buffer_store_dwordx3 v[232:234], v250, s[16:19], s38 offen offset:768 sc1\n"
        "ds_read_b128 a[232:235], v248 offset:12288\n"
        "v_mfma_f32_16x16x32_bf16 v[60:63], v[144:147], v[64:67], v[60:63]\n"
        "ds_read_b128 a[236:239], v248 offset:49152\n"
        "v_cvt_pk_bf16_f32 v232, v175, v179\n"
        "v_cvt_pk_bf16_f32 v233, v215, v219\n"
        "v_cvt_pk_bf16_f32 v234, v223, v227\n"
        "v_cvt_pk_bf16_f32 v80, v80, v81\n"
        "v_fma_f32 v83, v83, s24, v237\n"
        "v_exp_f32 v82, v82\n"
        "v_exp_f32 v83, v83\n"
        "v_fma_f32 v84, v84, s24, v237\n"
        "v_cvt_pk_bf16_f32 v81, v82, v83\n"
        "v_exp_f32 v84, v84\n"
        "v_fma_f32 v85, v85, s24, v237\n"
        "v_exp_f32 v85, v85\n"
        "v_fma_f32 v86, v86, s24, v237\n"
        "v_cvt_pk_bf16_f32 v82, v84, v85\n"
        "v_exp_f32 v86, v86\n"
        "v_fma_f32 v87, v87, s24, v237\n"
        "v_exp_f32 v87, v87\n"
        "v_fma_f32 v88, v88, s24, v237\n"
        "v_cvt_pk_bf16_f32 v83, v86, v87\n"
        "s_nop 1\n"
        "v_mfma_f32_16x16x32_bf16 v[168:171], a[252:255], v[80:83], 0\n"
        "buffer_store_dwordx3 v[232:234], v250, s[16:19], s38 offen offset:1152 sc1\n"
        "ds_read_b128 a[240:243], v254 offset:16\n"
        "v_exp_f32 v88, v88\n"
        "v_fma_f32 v89, v89, s24, v237\n"
        "v_exp_f32 v89, v89\n"
        "v_fma_f32 v90, v90, s24, v237\n"
        "v_cvt_pk_bf16_f32 v84, v88, v89\n"
        "v_exp_f32 v90, v90\n"
        "v_fma_f32 v91, v91, s24, v237\n"
        "v_exp_f32 v91, v91\n"
        "v_fma_f32 v92, v92, s24, v237\n"
        "v_cvt_pk_bf16_f32 v85, v90, v91\n"
        "v_exp_f32 v92, v92\n"
        "v_fma_f32 v93, v93, s24, v237\n"
        "v_exp_f32 v93, v93\n"
        "v_fma_f32 v94, v94, s24, v237\n"
        "v_cvt_pk_bf16_f32 v86, v92, v93\n"
        "v_exp_f32 v94, v94\n"
        "v_fma_f32 v95, v95, s24, v237\n"
        "v_exp_f32 v95, v95\n"
        "v_cvt_pk_bf16_f32 v232, v180, v184\n"
        "v_cvt_pk_bf16_f32 v87, v94, v95\n"
        "s_nop 1\n"
        "v_mfma_f32_16x16x32_bf16 v[168:171], a[252:255], v[84:87], v[168:171]\n"
        "ds_read_b128 a[244:247], v254 offset:6160\n"
        "v_cvt_pk_bf16_f32 v233, v192, v196\n"
        "v_cvt_pk_bf16_f32 v234, v200, v204\n"
        "v_rcp_f32 v48, v48\n"
        "v_fma_f32 v96, v96, s24, v238\n"
        "v_mfma_f32_16x16x32_bf16 v[188:191], v[132:135], v[84:87], 0\n"
        "buffer_store_dwordx3 v[232:234], v250, s[16:19], s39 offen offset:0 sc1\n"
        "ds_read_b128 a[248:251], v254 offset:36880\n"
        "v_mfma_f32_16x16x32_bf16 v[188:191], v[128:131], v[80:83], v[188:191]\n"
        "v_cvt_pk_bf16_f32 v232, v181, v185\n"
        "v_cvt_pk_bf16_f32 v233, v193, v197\n"
        "v_cvt_pk_bf16_f32 v234, v201, v205\n"
        "v_mul_f32 v52, v48, v52\n"
        "v_mul_f32 v53, v48, v53\n"
        "v_mul_f32 v54, v48, v54\n"
        "v_mfma_f32_16x16x32_bf16 v[208:211], v[136:139], v[80:83], 0\n"
        "buffer_store_dwordx3 v[232:234], v250, s[16:19], s39 offen offset:384 sc1\n"
        "v_mul_f32 v55, v48, v55\n"
        "v_mfma_f32_16x16x32_bf16 v[208:211], v[140:143], v[84:87], v[208:211]\n"
        "v_cvt_pk_bf16_f32 v232, v182, v186\n"
        "v_cvt_pk_bf16_f32 v233, v194, v198\n"
        "v_cvt_pk_bf16_f32 v234, v202, v206\n"
        "v_cvt_pk_bf16_f32 v52, v52, v53\n"
        "v_cvt_pk_bf16_f32 v53, v54, v55\n"
        "v_mul_f32 v56, v48, v56\n"
        "v_mfma_f32_16x16x32_bf16 v[228:231], v[148:151], v[84:87], 0\n"
        "buffer_store_dwordx3 v[232:234], v250, s[16:19], s39 offen offset:768 sc1\n"
        "v_mul_f32 v57, v48, v57\n"
        "v_mfma_f32_16x16x32_bf16 v[228:231], v[144:147], v[80:83], v[228:231]\n"
        "v_cvt_pk_bf16_f32 v232, v183, v187\n"
        "v_cvt_pk_bf16_f32 v233, v195, v199\n"
        "v_cvt_pk_bf16_f32 v234, v203, v207\n"
        "v_mul_f32 v58, v48, v58\n"
        "v_mul_f32 v59, v48, v59\n"
        "v_cvt_pk_bf16_f32 v54, v56, v57\n"
        "v_cvt_pk_bf16_f32 v55, v58, v59\n"
        "v_mul_f32 v60, v48, v60\n"
        "v_mul_f32 v61, v48, v61\n"
        "v_mul_f32 v62, v48, v62\n"
        "v_mul_f32 v63, v48, v63\n"
        "v_cvt_pk_bf16_f32 v60, v60, v61\n"
        "v_cvt_pk_bf16_f32 v61, v62, v63\n"
        "ds_write_b128 v245, v[52:55] offset:0\n"
        "ds_write_b64 v246, v[60:61] offset:0\n"
        "v_rcp_f32 v168, v168\n"
        "v_exp_f32 v96, v96\n"
        "v_mul_f32 v188, v168, v188\n"
        "v_mul_f32 v189, v168, v189\n"
        "v_mul_f32 v190, v168, v190\n"
        "v_mul_f32 v191, v168, v191\n"
        "v_cvt_pk_bf16_f32 v188, v188, v189\n"
        "v_cvt_pk_bf16_f32 v189, v190, v191\n"
        "v_mul_f32 v208, v168, v208\n"
        "v_mul_f32 v209, v168, v209\n"
        "v_mul_f32 v210, v168, v210\n"
        "v_mul_f32 v211, v168, v211\n"
        "v_cvt_pk_bf16_f32 v190, v208, v209\n"
        "v_cvt_pk_bf16_f32 v191, v210, v211\n"
        "v_mul_f32 v228, v168, v228\n"
        "v_mul_f32 v229, v168, v229\n"
        "v_mul_f32 v230, v168, v230\n"
        "v_mul_f32 v231, v168, v231\n"
        "v_cvt_pk_bf16_f32 v228, v228, v229\n"
        "v_cvt_pk_bf16_f32 v229, v230, v231\n"
        "ds_write_b128 v245, v[188:191] offset:256\n"
        "ds_write_b64 v246, v[228:229] offset:256\n"
        "s_waitcnt lgkmcnt(0)\n"
        "s_barrier\n"
        "ds_read_b128 v[152:155], v247 offset:0\n"
        "s_waitcnt lgkmcnt(0)\n"
        "v_mfma_f32_16x16x32_bf16 v[0:3], v[152:155], a[216:219], 0\n"
        "buffer_store_dwordx3 v[232:234], v250, s[16:19], s39 offen offset:1152 sc1\n"
        "ds_read_b128 v[156:159], v247 offset:4096\n"
        "s_waitcnt lgkmcnt(0)\n"
        "v_mfma_f32_16x16x32_bf16 v[0:3], v[156:159], a[220:223], v[0:3]\n"
        "ds_read_b128 v[172:175], v247 offset:8192\n"
        "ds_read_b128 v[176:179], v247 offset:12288\n"
        "ds_read_b128 v[212:215], v247 offset:16384\n"
        "ds_read_b128 v[216:219], v247 offset:20480\n"
        "s_waitcnt lgkmcnt(3)\n"
        "v_mfma_f32_16x16x32_bf16 v[0:3], v[172:175], a[224:227], v[0:3]\n"
        "s_waitcnt lgkmcnt(2)\n"
        "ds_read_b128 a[216:219], v254 offset:43024\n"
        "ds_read_b128 a[220:223], v254 offset:12304\n"
        "v_fma_f32 v97, v97, s24, v238\n"
        "v_fma_f32 v98, v98, s24, v238\n"
        "v_mfma_f32_16x16x32_bf16 v[0:3], v[176:179], a[228:231], v[0:3]\n"
        "s_waitcnt lgkmcnt(3)\n"
        "ds_read_b128 a[224:227], v254 offset:49168\n"
        "ds_read_b128 a[228:231], v248 offset:32\n"
        "v_exp_f32 v97, v97\n"
        "v_mfma_f32_16x16x32_bf16 v[0:3], v[212:215], a[232:235], v[0:3]\n"
        "s_waitcnt lgkmcnt(4)\n"
        "ds_read_b128 a[232:235], v248 offset:6176\n"
        "v_exp_f32 v98, v98\n"
        "v_cvt_pk_bf16_f32 v96, v96, v97\n"
        "v_mfma_f32_16x16x32_bf16 v[0:3], v[216:219], a[236:239], v[0:3]\n"
        "ds_read_b128 a[236:239], v248 offset:36896\n"
        "v_fma_f32 v99, v99, s24, v238\n"
        "v_exp_f32 v99, v99\n"
        "v_mfma_f32_16x16x32_bf16 v[4:7], v[152:155], a[240:243], 0\n"
        "ds_read_b128 a[240:243], v248 offset:43040\n"
        "v_cvt_pk_bf16_f32 v97, v98, v99\n"
        "v_fma_f32 v100, v100, s24, v238\n"
        "v_fma_f32 v101, v101, s24, v238\n"
        "v_mfma_f32_16x16x32_bf16 v[4:7], v[156:159], a[244:247], v[4:7]\n"
        "ds_read_b128 a[244:247], v248 offset:12320\n"
        "v_exp_f32 v100, v100\n"
        "v_fma_f32 v102, v102, s24, v238\n"
        "v_mfma_f32_16x16x32_bf16 v[4:7], v[172:175], a[248:251], v[4:7]\n"
        "s_waitcnt lgkmcnt(7)\n"
        "ds_read_b128 a[248:251], v248 offset:49184\n"
        "v_exp_f32 v101, v101\n"
        "v_fma_f32 v103, v103, s24, v238\n"
        "v_mfma_f32_16x16x32_bf16 v[4:7], v[176:179], a[216:219], v[4:7]\n"
        "s_waitcnt lgkmcnt(7)\n"
        "v_cvt_pk_bf16_f32 v98, v100, v101\n"
        "v_exp_f32 v102, v102\n"
        "v_fma_f32 v104, v104, s24, v238\n"
        "v_mfma_f32_16x16x32_bf16 v[4:7], v[212:215], a[220:223], v[4:7]\n"
        "s_waitcnt lgkmcnt(6)\n"
        "v_exp_f32 v103, v103\n"
        "v_exp_f32 v104, v104\n"
        "v_mfma_f32_16x16x32_bf16 v[4:7], v[216:219], a[224:227], v[4:7]\n"
        "s_waitcnt lgkmcnt(5)\n"
        "v_cvt_pk_bf16_f32 v99, v102, v103\n"
        "v_fma_f32 v105, v105, s24, v238\n"
        "v_exp_f32 v105, v105\n"
        "v_mfma_f32_16x16x32_bf16 v[8:11], v[152:155], a[228:231], 0\n"
        "s_waitcnt lgkmcnt(4)\n"
        "ds_read_b128 a[216:219], v254 offset:48\n"
        "v_cvt_pk_bf16_f32 v100, v104, v105\n"
        "v_fma_f32 v106, v106, s24, v238\n"
        "v_fma_f32 v107, v107, s24, v238\n"
        "v_mfma_f32_16x16x32_bf16 v[8:11], v[156:159], a[232:235], v[8:11]\n"
        "s_waitcnt lgkmcnt(4)\n"
        "ds_read_b128 a[220:223], v254 offset:6192\n"
        "v_exp_f32 v106, v106\n"
        "v_fma_f32 v108, v108, s24, v238\n"
        "v_mfma_f32_16x16x32_bf16 v[8:11], v[172:175], a[236:239], v[8:11]\n"
        "s_waitcnt lgkmcnt(4)\n"
        "ds_read_b128 a[224:227], v254 offset:36912\n"
        "v_exp_f32 v107, v107\n"
        "v_fma_f32 v109, v109, s24, v238\n"
        "v_mfma_f32_16x16x32_bf16 v[8:11], v[176:179], a[240:243], v[8:11]\n"
        "s_waitcnt lgkmcnt(4)\n"
        "ds_read_b128 a[228:231], v254 offset:43056\n"
        "v_cvt_pk_bf16_f32 v101, v106, v107\n"
        "v_exp_f32 v108, v108\n"
        "v_mfma_f32_16x16x32_bf16 v[8:11], v[212:215], a[244:247], v[8:11]\n"
        "s_waitcnt lgkmcnt(4)\n"
        "ds_read_b128 a[232:235], v254 offset:12336\n"
        "v_exp_f32 v109, v109\n"
        "v_fma_f32 v110, v110, s24, v238\n"
        "v_mfma_f32_16x16x32_bf16 v[8:11], v[216:219], a[248:251], v[8:11]\n"
        "ds_read_b128 a[236:239], v254 offset:49200\n"
        "s_add_u32 s38, s37, s43\n"
        "v_cvt_pk_bf16_f32 v102, v108, v109\n"
        "v_exp_f32 v110, v110\n"
        "v_mfma_f32_16x16x32_bf16 v[64:67], a[252:255], v[96:99], 0\n"
        "ds_read_b128 a[240:243], v248 offset:64\n"
        "s_add_u32 s39, s28, s43\n"
        "v_fma_f32 v111, v111, s24, v238\n"
        "v_exp_f32 v111, v111\n"
        "v_fma_f32 v112, v112, s24, v239\n"
        "v_cvt_pk_bf16_f32 v103, v110, v111\n"
        "s_nop 1\n"
        "v_mfma_f32_16x16x32_bf16 v[64:67], a[252:255], v[100:103], v[64:67]\n"
        "ds_read_b128 a[244:247], v248 offset:6208\n"
        "v_exp_f32 v112, v112\n"
        "v_fma_f32 v113, v113, s24, v239\n"
        "v_mfma_f32_16x16x32_bf16 v[68:71], v[132:135], v[100:103], 0\n"
        "ds_read_b128 a[248:251], v248 offset:36928\n"
        "v_exp_f32 v113, v113\n"
        "v_fma_f32 v114, v114, s24, v239\n"
        "v_mfma_f32_16x16x32_bf16 v[68:71], v[128:131], v[96:99], v[68:71]\n"
        "v_rcp_f32 v64, v64\n"
        "v_exp_f32 v114, v114\n"
        "v_mfma_f32_16x16x32_bf16 v[72:75], v[136:139], v[96:99], 0\n"
        "v_cvt_pk_bf16_f32 v112, v112, v113\n"
        "v_fma_f32 v115, v115, s24, v239\n"
        "v_exp_f32 v115, v115\n"
        "v_mfma_f32_16x16x32_bf16 v[72:75], v[140:143], v[100:103], v[72:75]\n"
        "v_cvt_pk_bf16_f32 v113, v114, v115\n"
        "v_mul_f32 v68, v64, v68\n"
        "v_mul_f32 v69, v64, v69\n"
        "v_mul_f32 v70, v64, v70\n"
        "v_mfma_f32_16x16x32_bf16 v[76:79], v[148:151], v[100:103], 0\n"
        "v_mul_f32 v71, v64, v71\n"
        "v_cvt_pk_bf16_f32 v68, v68, v69\n"
        "v_cvt_pk_bf16_f32 v69, v70, v71\n"
        "v_mul_f32 v72, v64, v72\n"
        "v_mfma_f32_16x16x32_bf16 v[76:79], v[144:147], v[96:99], v[76:79]\n"
        "s_waitcnt lgkmcnt(8)\n"
        "v_mul_f32 v73, v64, v73\n"
        "v_mul_f32 v74, v64, v74\n"
        "v_mul_f32 v75, v64, v75\n"
        "v_cvt_pk_bf16_f32 v70, v72, v73\n"
        "v_mfma_f32_16x16x32_bf16 v[12:15], v[152:155], a[216:219], 0\n"
        "s_waitcnt lgkmcnt(7)\n"
        "ds_read_b128 a[216:219], v248 offset:43072\n"
        "v_cvt_pk_bf16_f32 v71, v74, v75\n"
        "v_fma_f32 v116, v116, s24, v239\n"
        "v_mul_f32 v76, v64, v76\n"
        "v_mfma_f32_16x16x32_bf16 v[12:15], v[156:159], a[220:223], v[12:15]\n"
        "s_waitcnt lgkmcnt(7)\n"
        "ds_read_b128 a[220:223], v248 offset:12352\n"
        "v_mul_f32 v77, v64, v77\n"
        "v_mul_f32 v78, v64, v78\n"
        "v_mul_f32 v79, v64, v79\n"
        "v_mfma_f32_16x16x32_bf16 v[12:15], v[172:175], a[224:227], v[12:15]\n"
        "s_waitcnt lgkmcnt(7)\n"
        "ds_write_b128 v245, v[68:71] offset:512\n"
        "v_mfma_f32_16x16x32_bf16 v[12:15], v[176:179], a[228:231], v[12:15]\n"
        "s_waitcnt lgkmcnt(7)\n"
        "ds_read_b128 a[224:227], v248 offset:49216\n"
        "ds_read_b128 a[228:231], v254 offset:80\n"
        "v_cvt_pk_bf16_f32 v76, v76, v77\n"
        "v_cvt_pk_bf16_f32 v77, v78, v79\n"
        "v_mfma_f32_16x16x32_bf16 v[12:15], v[212:215], a[232:235], v[12:15]\n"
        "s_waitcnt lgkmcnt(8)\n"
        "ds_read_b128 a[232:235], v254 offset:6224\n"
        "v_exp_f32 v116, v116\n"
        "v_fma_f32 v117, v117, s24, v239\n"
        "v_mfma_f32_16x16x32_bf16 v[12:15], v[216:219], a[236:239], v[12:15]\n"
        "s_waitcnt lgkmcnt(8)\n"
        "ds_write_b64 v246, v[76:77] offset:512\n"
        "ds_read_b128 a[236:239], v254 offset:36944\n"
        "v_mfma_f32_16x16x32_bf16 v[16:19], v[152:155], a[240:243], 0\n"
        "s_waitcnt lgkmcnt(9)\n"
        "ds_read_b128 a[240:243], v254 offset:43088\n"
        "v_exp_f32 v117, v117\n"
        "v_fma_f32 v118, v118, s24, v239\n"
        "v_mfma_f32_16x16x32_bf16 v[16:19], v[156:159], a[244:247], v[16:19]\n"
        "s_waitcnt lgkmcnt(9)\n"
        "ds_read_b128 a[244:247], v254 offset:12368\n"
        "v_exp_f32 v118, v118\n"
        "v_cvt_pk_bf16_f32 v114, v116, v117\n"
        "v_mfma_f32_16x16x32_bf16 v[16:19], v[172:175], a[248:251], v[16:19]\n"
        "s_waitcnt lgkmcnt(9)\n"
        "ds_read_b128 a[248:251], v254 offset:49232\n"
        "v_fma_f32 v119, v119, s24, v239\n"
        "v_exp_f32 v119, v119\n"
        "v_mfma_f32_16x16x32_bf16 v[16:19], v[176:179], a[216:219], v[16:19]\n"
        "s_waitcnt lgkmcnt(9)\n"
        "v_cvt_pk_bf16_f32 v115, v118, v119\n"
        "v_fma_f32 v120, v120, s24, v239\n"
        "v_exp_f32 v120, v120\n"
        "v_mfma_f32_16x16x32_bf16 v[16:19], v[212:215], a[220:223], v[16:19]\n"
        "s_waitcnt lgkmcnt(7)\n"
        "v_fma_f32 v121, v121, s24, v239\n"
        "v_exp_f32 v121, v121\n"
        "v_fma_f32 v122, v122, s24, v239\n"
        "v_mfma_f32_16x16x32_bf16 v[16:19], v[216:219], a[224:227], v[16:19]\n"
        "s_waitcnt lgkmcnt(6)\n"
        "v_cvt_pk_bf16_f32 v116, v120, v121\n"
        "v_exp_f32 v122, v122\n"
        "v_fma_f32 v123, v123, s24, v239\n"
        "v_mfma_f32_16x16x32_bf16 v[20:23], v[152:155], a[228:231], 0\n"
        "s_waitcnt lgkmcnt(5)\n"
        "ds_read_b128 a[216:219], v248 offset:0\n"
        "v_exp_f32 v123, v123\n"
        "v_fma_f32 v124, v124, s24, v239\n"
        "v_mfma_f32_16x16x32_bf16 v[20:23], v[156:159], a[232:235], v[20:23]\n"
        "s_waitcnt lgkmcnt(4)\n"
        "ds_read_b128 a[220:223], v248 offset:6144\n"
        "v_cvt_pk_bf16_f32 v117, v122, v123\n"
        "v_exp_f32 v124, v124\n"
        "v_mfma_f32_16x16x32_bf16 v[20:23], v[172:175], a[236:239], v[20:23]\n"
        "s_waitcnt lgkmcnt(4)\n"
        "ds_read_b128 a[224:227], v248 offset:36864\n"
        "v_fma_f32 v125, v125, s24, v239\n"
        "v_exp_f32 v125, v125\n"
        "v_mfma_f32_16x16x32_bf16 v[20:23], v[176:179], a[240:243], v[20:23]\n"
        "s_waitcnt lgkmcnt(4)\n"
        "ds_read_b128 a[228:231], v248 offset:43008\n"
        "v_cvt_pk_bf16_f32 v118, v124, v125\n"
        "v_fma_f32 v126, v126, s24, v239\n"
        "v_fma_f32 v127, v127, s24, v239\n"
        "v_mfma_f32_16x16x32_bf16 v[20:23], v[212:215], a[244:247], v[20:23]\n"
        "s_waitcnt lgkmcnt(4)\n"
        "ds_read_b128 a[232:235], v248 offset:12288\n"
        "v_exp_f32 v126, v126\n"
        "v_cvt_pk_bf16_f32 v192, v0, v4\n"
        "v_mfma_f32_16x16x32_bf16 v[20:23], v[216:219], a[248:251], v[20:23]\n"
        "ds_read_b128 a[236:239], v248 offset:49152\n"
        "v_exp_f32 v127, v127\n"
        "v_cvt_pk_bf16_f32 v193, v8, v12\n"
        "v_mfma_f32_16x16x32_bf16 v[80:83], a[252:255], v[112:115], 0\n"
        "ds_read_b128 a[240:243], v254 offset:16\n"
        "v_cvt_pk_bf16_f32 v119, v126, v127\n"
        "v_cvt_pk_bf16_f32 v196, v1, v5\n"
        "v_cvt_pk_bf16_f32 v197, v9, v13\n"
        "v_mfma_f32_16x16x32_bf16 v[80:83], a[252:255], v[116:119], v[80:83]\n"
        "ds_read_b128 a[244:247], v254 offset:6160\n"
        "v_cvt_pk_bf16_f32 v194, v16, v20\n"
        "v_cvt_pk_bf16_f32 v198, v17, v21\n"
        "v_cvt_pk_bf16_f32 v200, v2, v6\n"
        "v_mfma_f32_16x16x32_bf16 v[84:87], v[132:135], v[116:119], 0\n"
        "buffer_store_dwordx3 v[192:194], v250, s[16:19], s38 offen offset:0 sc1\n"
        "v_mfma_f32_16x16x32_bf16 v[84:87], v[128:131], v[112:115], v[84:87]\n"
        "ds_read_b128 a[248:251], v254 offset:36880\n"
        "v_rcp_f32 v80, v80\n"
        "v_cvt_pk_bf16_f32 v201, v10, v14\n"
        "v_mfma_f32_16x16x32_bf16 v[88:91], v[136:139], v[112:115], 0\n"
        "v_cvt_pk_bf16_f32 v202, v18, v22\n"
        "v_cvt_pk_bf16_f32 v204, v3, v7\n"
        "v_cvt_pk_bf16_f32 v205, v11, v15\n"
        "v_cvt_pk_bf16_f32 v206, v19, v23\n"
        "v_mfma_f32_16x16x32_bf16 v[88:91], v[140:143], v[116:119], v[88:91]\n"
        "buffer_store_dwordx3 v[196:198], v250, s[16:19], s38 offen offset:384 sc1\n"
        "v_mfma_f32_16x16x32_bf16 v[92:95], v[148:151], v[116:119], 0\n"
        "v_mul_f32 v84, v80, v84\n"
        "v_mul_f32 v85, v80, v85\n"
        "v_mul_f32 v86, v80, v86\n"
        "v_mul_f32 v87, v80, v87\n"
        "v_mfma_f32_16x16x32_bf16 v[92:95], v[144:147], v[112:115], v[92:95]\n"
        "v_cvt_pk_bf16_f32 v84, v84, v85\n"
        "v_cvt_pk_bf16_f32 v85, v86, v87\n"
        "v_mul_f32 v88, v80, v88\n"
        "v_mul_f32 v89, v80, v89\n"
        "v_mul_f32 v90, v80, v90\n"
        "v_mul_f32 v91, v80, v91\n"
        "v_cvt_pk_bf16_f32 v86, v88, v89\n"
        "v_cvt_pk_bf16_f32 v87, v90, v91\n"
        "v_mul_f32 v92, v80, v92\n"
        "v_mul_f32 v93, v80, v93\n"
        "v_mul_f32 v94, v80, v94\n"
        "v_mul_f32 v95, v80, v95\n"
        "v_cvt_pk_bf16_f32 v92, v92, v93\n"
        "v_cvt_pk_bf16_f32 v93, v94, v95\n"
        "ds_write_b128 v245, v[84:87] offset:768\n"
        "ds_write_b64 v246, v[92:93] offset:768\n"
        "s_waitcnt lgkmcnt(0)\n"
        "s_barrier\n"
        "ds_read_b128 v[128:131], v247 offset:512\n"
        "s_waitcnt lgkmcnt(0)\n"
        "v_mfma_f32_16x16x32_bf16 v[24:27], v[128:131], a[216:219], 0\n"
        "ds_read_b128 v[132:135], v247 offset:4608\n"
        "ds_read_b128 v[136:139], v247 offset:8704\n"
        "s_waitcnt lgkmcnt(1)\n"
        "v_mfma_f32_16x16x32_bf16 v[24:27], v[132:135], a[220:223], v[24:27]\n"
        "s_waitcnt lgkmcnt(0)\n"
        "ds_read_b128 v[140:143], v247 offset:12800\n"
        "ds_read_b128 v[144:147], v247 offset:16896\n"
        "v_mfma_f32_16x16x32_bf16 v[24:27], v[136:139], a[224:227], v[24:27]\n"
        "s_waitcnt lgkmcnt(1)\n"
        "ds_read_b128 v[148:151], v247 offset:20992\n"
        "ds_read_b128 a[216:219], v254 offset:43024\n"
        "v_mfma_f32_16x16x32_bf16 v[24:27], v[140:143], a[228:231], v[24:27]\n"
        "s_waitcnt lgkmcnt(2)\n"
        "buffer_store_dwordx3 v[200:202], v250, s[16:19], s38 offen offset:768 sc1\n"
        "v_mfma_f32_16x16x32_bf16 v[24:27], v[144:147], a[232:235], v[24:27]\n"
        "s_waitcnt lgkmcnt(1)\n"
        "ds_read_b128 a[220:223], v254 offset:12304\n"
        "ds_read_b128 a[224:227], v254 offset:49168\n"
        "v_mfma_f32_16x16x32_bf16 v[24:27], v[148:151], a[236:239], v[24:27]\n"
        "ds_read_b128 a[228:231], v248 offset:32\n"
        "ds_read_b128 a[232:235], v248 offset:6176\n"
        "v_mfma_f32_16x16x32_bf16 v[28:31], v[128:131], a[240:243], 0\n"
        "buffer_store_dwordx3 v[204:206], v250, s[16:19], s38 offen offset:1152 sc1\n"
        "v_mfma_f32_16x16x32_bf16 v[28:31], v[132:135], a[244:247], v[28:31]\n"
        "ds_read_b128 a[236:239], v248 offset:36896\n"
        "ds_read_b128 a[240:243], v248 offset:43040\n"
        "v_mfma_f32_16x16x32_bf16 v[28:31], v[136:139], a[248:251], v[28:31]\n"
        "s_waitcnt lgkmcnt(6)\n"
        "ds_read_b128 a[244:247], v248 offset:12320\n"
        "ds_read_b128 a[248:251], v248 offset:49184\n"
        "v_mfma_f32_16x16x32_bf16 v[28:31], v[140:143], a[216:219], v[28:31]\n"
        "s_waitcnt lgkmcnt(7)\n"
        "ds_read_b128 a[216:219], v254 offset:48\n"
        "v_mfma_f32_16x16x32_bf16 v[28:31], v[144:147], a[220:223], v[28:31]\n"
        "s_waitcnt lgkmcnt(7)\n"
        "ds_read_b128 a[220:223], v254 offset:6192\n"
        "v_mfma_f32_16x16x32_bf16 v[28:31], v[148:151], a[224:227], v[28:31]\n"
        "s_waitcnt lgkmcnt(7)\n"
        "ds_read_b128 a[224:227], v254 offset:36912\n"
        "v_mfma_f32_16x16x32_bf16 v[32:35], v[128:131], a[228:231], 0\n"
        "s_waitcnt lgkmcnt(7)\n"
        "ds_read_b128 a[228:231], v254 offset:43056\n"
        "v_mfma_f32_16x16x32_bf16 v[32:35], v[132:135], a[232:235], v[32:35]\n"
        "s_waitcnt lgkmcnt(7)\n"
        "ds_read_b128 a[232:235], v254 offset:12336\n"
        "v_mfma_f32_16x16x32_bf16 v[32:35], v[136:139], a[236:239], v[32:35]\n"
        "s_waitcnt lgkmcnt(7)\n"
        "ds_read_b128 a[236:239], v254 offset:49200\n"
        "v_mfma_f32_16x16x32_bf16 v[32:35], v[140:143], a[240:243], v[32:35]\n"
        "s_waitcnt lgkmcnt(7)\n"
        "ds_read_b128 a[240:243], v248 offset:64\n"
        "v_cvt_pk_bf16_f32 v180, v24, v28\n"
        "v_mfma_f32_16x16x32_bf16 v[32:35], v[144:147], a[244:247], v[32:35]\n"
        "s_waitcnt lgkmcnt(7)\n"
        "ds_read_b128 a[244:247], v248 offset:6208\n"
        "v_cvt_pk_bf16_f32 v184, v25, v29\n"
        "v_mfma_f32_16x16x32_bf16 v[32:35], v[148:151], a[248:251], v[32:35]\n"
        "s_waitcnt lgkmcnt(7)\n"
        "ds_read_b128 a[248:251], v248 offset:36928\n"
        "v_cvt_pk_bf16_f32 v220, v26, v30\n"
        "v_mfma_f32_16x16x32_bf16 v[36:39], v[128:131], a[216:219], 0\n"
        "s_waitcnt lgkmcnt(7)\n"
        "ds_read_b128 a[216:219], v248 offset:43072\n"
        "v_cvt_pk_bf16_f32 v224, v27, v31\n"
        "v_mfma_f32_16x16x32_bf16 v[36:39], v[132:135], a[220:223], v[36:39]\n"
        "s_waitcnt lgkmcnt(7)\n"
        "ds_read_b128 a[220:223], v248 offset:12352\n"
        "v_mfma_f32_16x16x32_bf16 v[36:39], v[136:139], a[224:227], v[36:39]\n"
        "s_waitcnt lgkmcnt(7)\n"
        "ds_read_b128 a[224:227], v248 offset:49216\n"
        "v_mfma_f32_16x16x32_bf16 v[36:39], v[140:143], a[228:231], v[36:39]\n"
        "s_waitcnt lgkmcnt(7)\n"
        "ds_read_b128 a[228:231], v254 offset:80\n"
        "v_mfma_f32_16x16x32_bf16 v[36:39], v[144:147], a[232:235], v[36:39]\n"
        "s_waitcnt lgkmcnt(7)\n"
        "ds_read_b128 a[232:235], v254 offset:6224\n"
        "v_mfma_f32_16x16x32_bf16 v[36:39], v[148:151], a[236:239], v[36:39]\n"
        "s_waitcnt lgkmcnt(7)\n"
        "ds_read_b128 a[236:239], v254 offset:36944\n"
        "v_mfma_f32_16x16x32_bf16 v[40:43], v[128:131], a[240:243], 0\n"
        "s_waitcnt lgkmcnt(7)\n"
        "ds_read_b128 a[240:243], v254 offset:43088\n"
        "v_mfma_f32_16x16x32_bf16 v[40:43], v[132:135], a[244:247], v[40:43]\n"
        "s_waitcnt lgkmcnt(7)\n"
        "ds_read_b128 a[244:247], v254 offset:12368\n"
        "v_mfma_f32_16x16x32_bf16 v[40:43], v[136:139], a[248:251], v[40:43]\n"
        "s_waitcnt lgkmcnt(7)\n"
        "ds_read_b128 a[248:251], v254 offset:49232\n"
        "v_mfma_f32_16x16x32_bf16 v[40:43], v[140:143], a[216:219], v[40:43]\n"
        "s_waitcnt lgkmcnt(7)\n"
        "v_cvt_pk_bf16_f32 v181, v32, v36\n"
        "v_cvt_pk_bf16_f32 v185, v33, v37\n"
        "v_mfma_f32_16x16x32_bf16 v[40:43], v[144:147], a[220:223], v[40:43]\n"
        "s_waitcnt lgkmcnt(6)\n"
        "v_cvt_pk_bf16_f32 v221, v34, v38\n"
        "v_cvt_pk_bf16_f32 v225, v35, v39\n"
        "v_mfma_f32_16x16x32_bf16 v[40:43], v[148:151], a[224:227], v[40:43]\n"
        "s_waitcnt lgkmcnt(5)\n"
        "v_mfma_f32_16x16x32_bf16 v[44:47], v[128:131], a[228:231], 0\n"
        "s_waitcnt lgkmcnt(4)\n"
        "v_mfma_f32_16x16x32_bf16 v[44:47], v[132:135], a[232:235], v[44:47]\n"
        "s_waitcnt lgkmcnt(3)\n"
        "v_mfma_f32_16x16x32_bf16 v[44:47], v[136:139], a[236:239], v[44:47]\n"
        "s_waitcnt lgkmcnt(2)\n"
        "v_mfma_f32_16x16x32_bf16 v[44:47], v[140:143], a[240:243], v[44:47]\n"
        "s_waitcnt lgkmcnt(1)\n"
        "v_mfma_f32_16x16x32_bf16 v[44:47], v[144:147], a[244:247], v[44:47]\n"
        "s_waitcnt lgkmcnt(0)\n"
        "v_mfma_f32_16x16x32_bf16 v[44:47], v[148:151], a[248:251], v[44:47]\n"
        "s_nop 7\n"
        "v_cvt_pk_bf16_f32 v182, v40, v44\n"
        "buffer_store_dwordx3 v[180:182], v250, s[16:19], s39 offen offset:0 sc1\n"
        "v_cvt_pk_bf16_f32 v186, v41, v45\n"
        "buffer_store_dwordx3 v[184:186], v250, s[16:19], s39 offen offset:384 sc1\n"
        "v_cvt_pk_bf16_f32 v222, v42, v46\n"
        "buffer_store_dwordx3 v[220:222], v250, s[16:19], s39 offen offset:768 sc1\n"
        "v_cvt_pk_bf16_f32 v226, v43, v47\n"
        "buffer_store_dwordx3 v[224:226], v250, s[16:19], s39 offen offset:1152 sc1\n"
        : "+{v240}"(xr[0]), "+{v241}"(xr[1]), "+{v251}"(mr[0]), "+{v252}"(mr[1]), "+{v242}"(dm[0]), "+{v243}"(dm[1]), "+{v244}"(dm[2]), "+{v245}"(ow), "+{v246}"(ow2), "+{v247}"(ord), "+{v248}"(wor), "+{v249}"(ms), "+{v250}"(st), "+{v142}"(l16), "+{v170}"(id0), "+{v171}"(id1), "+{v139}"(rs), "+{v144}"(rr), "+{v140}"(oa), "+{v141}"(ob), "+{v147}"(od), "+{v145}"(rbs), "+{v146}"(rbw), "+{v254}"(wor2), "+{v255}"(ml), "+{v148}"(pf0), "+{v149}"(pf1), "+{v128}"(wsrc[0]), "+{v129}"(wsrc[1]), "+{v130}"(wsrc[2]), "+{v131}"(wrd[0]), "+{v132}"(wrd[1]), "+{v133}"(wrd[2]), "+{s47}"(ya), "+{s36}"(mk), "+{s37}"(raw), "+{s43}"(x0), "+{s44}"(x1)
        : "{s[8:11]}"(srdx), "{s[12:15]}"(srdm), "{s[16:19]}"(srdo), "{s[20:23]}"(srdw), "{s[52:55]}"(srdwo), "{s[56:59]}"(srdrp), "{s[60:63]}"(srdrb), "{s42}"(wv), "s"(ldsb)
        : "v0", "v1", "v2", "v3", "v4", "v5", "v6", "v7", "v8", "v9", "v10", "v11", "v12", "v13", "v14", "v15", "v16", "v17", "v18", "v19", "v20", "v21", "v22", "v23", "v24", "v25", "v26", "v27", "v28", "v29", "v30", "v31", "v32", "v33", "v34", "v35", "v36", "v37", "v38", "v39", "v40", "v41", "v42", "v43", "v44", "v45", "v46", "v47", "v48", "v49", "v50", "v51", "v52", "v53", "v54", "v55", "v56", "v57", "v58", "v59", "v60", "v61", "v62", "v63", "v64", "v65", "v66", "v67", "v68", "v69", "v70", "v71", "v72", "v73", "v74", "v75", "v76", "v77", "v78", "v79", "v80", "v81", "v82", "v83", "v84", "v85", "v86", "v87", "v88", "v89", "v90", "v91", "v92", "v93", "v94", "v95", "v96", "v97", "v98", "v99", "v100", "v101", "v102", "v103", "v104", "v105", "v106", "v107", "v108", "v109", "v110", "v111", "v112", "v113", "v114", "v115", "v116", "v117", "v118", "v119", "v120", "v121", "v122", "v123", "v124", "v125", "v126", "v127", "v134", "v135", "v136", "v137", "v138", "v143", "v150", "v151", "v152", "v153", "v154", "v155", "v156", "v157", "v158", "v159", "v160", "v161", "v162", "v163", "v164", "v165", "v166", "v167", "v168", "v169", "v172", "v173", "v174", "v175", "v176", "v177", "v178", "v179", "v180", "v181", "v182", "v183", "v184", "v185", "v186", "v187", "v188", "v189", "v190", "v191", "v192", "v193", "v194", "v195", "v196", "v197", "v198", "v199", "v200", "v201", "v202", "v203", "v204", "v205", "v206", "v207", "v208", "v209", "v210", "v211", "v212", "v213", "v214", "v215", "v216", "v217", "v218", "v219", "v220", "v221", "v222", "v223", "v224", "v225", "v226", "v227", "v228", "v229", "v230", "v231", "v232", "v233", "v234", "v235", "v236", "v237", "v238", "v239", "v253", "a0", "a1", "a2", "a3", "a4", "a5", "a6", "a7", "a8", "a9", "a10", "a11", "a12", "a13", "a14", "a15", "a16", "a17", "a18", "a19", "a20", "a21", "a22", "a23", "a24", "a25", "a26", "a27", "a28", "a29", "a30", "a31", "a32", "a33", "a34", "a35", "a36", "a37", "a38", "a39", "a40", "a41", "a42", "a43", "a44", "a45", "a46", "a47", "a48", "a49", "a50", "a51", "a52", "a53", "a54", "a55", "a56", "a57", "a58", "a59", "a60", "a61", "a62", "a63", "a64", "a65", "a66", "a67", "a68", "a69", "a70", "a71", "a72", "a73", "a74", "a75", "a76", "a77", "a78", "a79", "a80", "a81", "a82", "a83", "a84", "a85", "a86", "a87", "a88", "a89", "a90", "a91", "a92", "a93", "a94", "a95", "a96", "a97", "a98", "a99", "a100", "a101", "a102", "a103", "a104", "a105", "a106", "a107", "a108", "a109", "a110", "a111", "a112", "a113", "a114", "a115", "a116", "a117", "a118", "a119", "a120", "a121", "a122", "a123", "a124", "a125", "a126", "a127", "a128", "a129", "a130", "a131", "a132", "a133", "a134", "a135", "a136", "a137", "a138", "a139", "a140", "a141", "a142", "a143", "a144", "a145", "a146", "a147", "a148", "a149", "a150", "a151", "a152", "a153", "a154", "a155", "a156", "a157", "a158", "a159", "a160", "a161", "a162", "a163", "a164", "a165", "a166", "a167", "a168", "a169", "a170", "a171", "a172", "a173", "a174", "a175", "a176", "a177", "a178", "a179", "a180", "a181", "a182", "a183", "a184", "a185", "a186", "a187", "a188", "a189", "a190", "a191", "a192", "a193", "a194", "a195", "a196", "a197", "a198", "a199", "a200", "a201", "a202", "a203", "a204", "a205", "a206", "a207", "a208", "a209", "a210", "a211", "a212", "a213", "a214", "a215", "a216", "a217", "a218", "a219", "a220", "a221", "a222", "a223", "a224", "a225", "a226", "a227", "a228", "a229", "a230", "a231", "a232", "a233", "a234", "a235", "a236", "a237", "a238", "a239", "a240", "a241", "a242", "a243", "a244", "a245", "a246", "a247", "a248", "a249", "a250", "a251", "a252", "a253", "a254", "a255", "s24", "s25", "s26", "s27", "s28", "s29", "s30", "s31", "s34", "s35", "s38", "s39", "s40", "s41", "s45", "s46", "s48", "s49", "s50", "s51", "s64", "s65", "s66", "s67", "vcc", "scc", "memory");
}


__global__ __launch_bounds__(256, 1) __attribute__((aligned(4096))) void win_attn_kernel(const char* __restrict__ x, const char* __restrict__ wqkv,
                                                          const char* __restrict__ wo, const char* __restrict__ rpb,
                                                          const char* __restrict__ rpi, const char* __restrict__ mask,
                                                          char* __restrict__ out, char* ws) {
    __shared__ __attribute__((aligned(16))) char smem[163840];
    unsigned tid = threadIdx.x, w, nit;
    unsigned long long hist;
    const unsigned ldsb = (unsigned)(size_t)smem;
    asm volatile(
        "s_load_dwordx2 s[8:9], s[0:1], 0\n"
        "s_load_dwordx2 s[20:21], s[0:1], 8\n"
        "s_load_dwordx2 s[52:53], s[0:1], 16\n"
        "s_load_dwordx2 s[60:61], s[0:1], 24\n"
        "s_load_dwordx2 s[56:57], s[0:1], 32\n"
        "s_load_dwordx2 s[12:13], s[0:1], 40\n"
        "s_load_dwordx2 s[16:17], s[0:1], 48\n"
        "v_readfirstlane_b32 s42, v0\n"
        "v_and_b32 v1, 63, v0\n"
        "v_lshlrev_b32 v145, 3, v0\n"
        "s_lshr_b32 s42, s42, 6\n"
        "s_bfe_u32 s41, s2, 0x50003\n"
        "s_lshl2_add_u32 s41, s41, s42\n"
        "s_lshl_b32 s49, s41, 8\n"
        "v_and_b32 v10, 1, v0\n"
        "v_lshl_add_u32 v10, v10, 7, s49\n"
        "v_min_u32 v10, 32256, v10\n"
        "s_getpc_b64 s[38:39]\n"
        "global_load_dword v66, v10, s[38:39]\n"
        "v_bfe_u32 v2, v0, 4, 2\n"
        "s_and_b32 s51, s42, 1\n"
        "s_lshl_b32 s66, s51, 3\n"
        "v_xor_b32 v11, v1, v2\n"
        "v_xor_b32 v11, s66, v11\n"
        "v_lshlrev_b32 v139, 4, v11\n"
        "v_xor_b32 v144, 64, v139\n"
        "s_lshl_b32 s34, s42, 12\n"
        "s_mul_i32 s40, s42, 3072\n"
        "s_and_b32 s31, s2, 63\n"
        "s_lshl_b32 s31, s31, 3\n"
        "s_add_u32 s31, s31, 4\n"
        "s_lshr_b32 s45, s2, 6\n"
        "s_lshl_b32 s45, s45, 3\n"
        "s_add_u32 s47, s45, s42\n"
        "s_add_u32 s47, s47, 68\n"
        "s_mul_i32 s47, s47, 196608\n"
        "s_lshl_b32 s46, s42, 1\n"
        "s_lshl_b32 s49, s41, 11\n"
        "v_lshl_add_u32 v148, v1, 7, s49\n"
        "v_lshl_or_b32 v148, v2, 28, v148\n"
        "s_mov_b32 s62, 1800\n"
        "s_mov_b32 s63, 0x20000\n"
        "s_mov_b32 s58, 16384\n"
        "s_mov_b32 s59, 0x20000\n"
        "s_mov_b32 s22, 221184\n"
        "s_mov_b32 s23, 0x20000\n"
        "s_mov_b32 s10, 100663296\n"
        "s_mov_b32 s11, 0x20000\n"
        "v_mul_u32_u24 v15, 171, v1\n"
        "v_lshrrev_b32 v15, 12, v15\n"
        "v_mul_u32_u24 v13, 24, v15\n"
        "v_sub_u32 v18, v1, v13\n"
        "v_bfe_u32 v21, v15, 1, 1\n"
        "v_add_u32 v13, s31, v15\n"
        "v_and_b32 v13, 0x1ff, v13\n"
        "v_mul_u32_u24 v13, 384, v13\n"
        "v_bitop3_b32 v14, v21, v18, s46 bitop3:0x36\n"
        "v_lshl_add_u32 v242, v14, 4, v13\n"
        "v_add_u32 v12, 64, v1\n"
        "v_mul_u32_u24 v16, 171, v12\n"
        "v_lshrrev_b32 v16, 12, v16\n"
        "v_mul_u32_u24 v13, 24, v16\n"
        "v_sub_u32 v19, v12, v13\n"
        "v_bfe_u32 v22, v16, 1, 1\n"
        "v_add_u32 v13, s31, v16\n"
        "v_and_b32 v13, 0x1ff, v13\n"
        "v_mul_u32_u24 v13, 384, v13\n"
        "v_bitop3_b32 v14, v22, v19, s46 bitop3:0x36\n"
        "v_lshl_add_u32 v243, v14, 4, v13\n"
        "v_add_u32 v12, 128, v1\n"
        "v_mul_u32_u24 v17, 171, v12\n"
        "v_lshrrev_b32 v17, 12, v17\n"
        "v_mul_u32_u24 v13, 24, v17\n"
        "v_sub_u32 v20, v12, v13\n"
        "v_bfe_u32 v23, v17, 1, 1\n"
        "v_add_u32 v13, s31, v17\n"
        "v_and_b32 v13, 0x1ff, v13\n"
        "v_mul_u32_u24 v13, 384, v13\n"
        "v_bitop3_b32 v14, v23, v20, s46 bitop3:0x36\n"
        "v_lshl_add_u32 v244, v14, 4, v13\n"
        "s_mov_b32 s14, 33554432\n"
        "s_mov_b32 s15, 0x20000\n"
        "v_bfe_u32 v4, v0, 4, 1\n"
        "s_lshl_b32 s66, s51, 2\n"
        "v_bitop3_b32 v11, v4, v1, s66 bitop3:0x36\n"
        "v_lshlrev_b32 v249, 4, v11\n"
        "s_waitcnt lgkmcnt(0)\n"
        "buffer_load_dword v64, v148, s[20:23], 0 offen\n"
        "buffer_load_dwordx2 v[28:29], v145, s[60:63], 0 offen\n"
        "s_add_u32 m0, s34, 122880\n"
        "s_mov_b32 s64, s34\n"
        "buffer_load_dwordx4 v139, s[56:59], s64 offen lds\n"
        "v_xor_b32 v149, 32, v249\n"
        "s_lshl_b32 s48, s42, 11\n"
        "s_lshl_b32 s36, s2, 13\n"
        "s_add_u32 s36, s36, s48\n"
        "s_add_u32 s36, s36, 4194304\n"
        "s_add_u32 s48, s48, 147456\n"
        "s_mov_b32 s54, 73728\n"
        "s_mov_b32 s55, 0x20000\n"
        "s_mov_b32 s18, 100663296\n"
        "s_mov_b32 s19, 0x20000\n"
        "s_mul_i32 s65, s42, 9216\n"
        "v_lshl_or_b32 v13, v21, 1, s66\n"
        "buffer_load_dwordx4 v139, s[56:59], s64 offen offset:1024 lds\n"
        "v_xor_b32 v13, v13, v18\n"
        "v_mul_u32_u24 v14, 1152, v15\n"
        "v_lshl_add_u32 v128, v13, 4, v14\n"
        "v_lshl_or_b32 v13, v22, 1, s66\n"
        "v_xor_b32 v13, v13, v19\n"
        "v_mul_u32_u24 v14, 1152, v16\n"
        "v_lshl_add_u32 v129, v13, 4, v14\n"
        "v_subrev_u32 v129, 1024, v129\n"
        "v_lshl_or_b32 v13, v23, 1, s66\n"
        "v_xor_b32 v13, v13, v20\n"
        "v_mul_u32_u24 v14, 1152, v17\n"
        "v_lshl_add_u32 v130, v13, 4, v14\n"
        "buffer_load_dwordx4 v144, s[56:59], s64 offen offset:2048 lds\n"
        "v_subrev_u32 v130, 2048, v130\n"
        "v_bfe_u32 v6, v0, 2, 2\n"
        "v_and_b32 v7, 3, v0\n"
        "v_lshlrev_b32 v150, 4, v1\n"
        "v_lshl_add_u32 v8, v6, 3, v7\n"
        "v_and_b32 v9, 0xf0, v150\n"
        "v_lshlrev_b32 v146, 2, v0\n"
        "v_add_u32 v146, 139264, v146\n"
        "v_lshlrev_b32 v10, 5, v2\n"
        "v_xor_b32 v10, v10, v9\n"
        "v_lshl_add_u32 v10, v8, 8, v10\n"
        "v_add_u32 v140, 122880, v10\n"
        "buffer_load_dwordx4 v144, s[56:59], s64 offen offset:3072 lds\n"
        "v_xor_b32 v141, 16, v140\n"
        "v_xor_b32 v142, 128, v140\n"
        "v_xor_b32 v143, 144, v140\n"
        "s_lshl_b32 s67, s42, 10\n"
        "s_add_u32 s67, s67, 139264\n"
        "v_bfe_u32 v10, v0, 1, 3\n"
        "v_xor_b32 v10, v10, v2\n"
        "v_lshlrev_b32 v10, 4, v10\n"
        "v_mul_u32_u24 v11, 384, v8\n"
        "v_add_u32 v11, 73728, v11\n"
        "v_add_u32 v240, v10, v11\n"
        "v_xor_b32 v241, 64, v240\n"
        "s_add_u32 m0, s40, 73728\n"
        "s_sub_u32 s64, s47, 12582912\n"
        "buffer_load_dwordx4 v242, s[8:11], s64 offen nt lds\n"
        "v_lshl_add_u32 v10, v8, 7, v10\n"
        "v_add_u32 v251, 147456, v10\n"
        "v_xor_b32 v252, 64, v251\n"
        "v_lshl_add_u32 v10, v2, 3, v6\n"
        "v_mul_u32_u24 v10, 384, v10\n"
        "v_and_b32 v11, 1, v0\n"
        "v_lshl_add_u32 v10, v11, 3, v10\n"
        "v_and_b32 v12, 2, v6\n"
        "v_lshl_or_b32 v12, v4, 2, v12\n"
        "s_mul_i32 s35, s42, 6\n"
        "v_bfe_u32 v13, v0, 1, 1\n"
        "v_add_u32 v13, s35, v13\n"
        "s_add_u32 m0, s40, 74752\n"
        "s_sub_u32 s64, s47, 12582912\n"
        "buffer_load_dwordx4 v243, s[8:11], s64 offen nt lds\n"
        "v_xor_b32 v14, v13, v12\n"
        "v_lshl_add_u32 v131, v14, 4, v10\n"
        "v_add_u32 v136, 98304, v131\n"
        "v_add_u32 v13, 2, v13\n"
        "v_xor_b32 v14, v13, v12\n"
        "v_lshl_add_u32 v132, v14, 4, v10\n"
        "v_add_u32 v137, 98304, v132\n"
        "v_add_u32 v13, 2, v13\n"
        "v_xor_b32 v14, v13, v12\n"
        "v_lshl_add_u32 v133, v14, 4, v10\n"
        "v_add_u32 v138, 98304, v133\n"
        "v_lshlrev_b32 v10, 4, v11\n"
        "s_add_u32 m0, s40, 75776\n"
        "s_sub_u32 s64, s47, 12582912\n"
        "buffer_load_dwordx4 v244, s[8:11], s64 offen nt lds\n"
        "v_mov_b32 v12, 0x3fb9\n"
        "v_cmp_eq_u32 vcc, v6, v2\n"
        "v_lshlrev_b32 v10, v10, v12\n"
        "v_bfe_i32 v13, v0, 0, 1\n"
        "v_cndmask_b32 v10, 0, v10, vcc\n"
        "v_cmp_gt_u32 vcc, 2, v7\n"
        "v_and_b32 v13, 0xaaaaaaaa, v13\n"
        "v_or_b32 v255, 0x44444444, v13\n"
        "v_cndmask_b32 v170, 0, v10, vcc\n"
        "v_xor_b32 v171, v170, v10\n"
        "v_mov_b64 v[188:189], v[170:171]\n"
        "v_mov_b64 v[210:211], v[170:171]\n"
        "s_add_u32 m0, s40, 86016\n"
        "s_sub_u32 s64, s47, 11796480\n"
        "buffer_load_dwordx4 v242, s[8:11], s64 offen nt lds\n"
        "v_mov_b64 v[228:229], v[170:171]\n"
        "v_bfe_u32 v5, v0, 5, 1\n"
        "v_and_b32 v3, 15, v0\n"
        "v_lshl_add_u32 v10, v2, 10, v9\n"
        "s_add_u32 s49, s34, 73728\n"
        "v_add_u32 v245, s49, v10\n"
        "s_lshl_b32 s49, s51, 9\n"
        "s_add_u32 s49, s49, 73728\n"
        "v_add_u32 v247, s49, v10\n"
        "v_lshl_add_u32 v10, v5, 10, v9\n"
        "v_lshl_add_u32 v10, v4, 3, v10\n"
        "s_lshl_b32 s49, s42, 11\n"
        "s_add_u32 m0, s40, 87040\n"
        "s_sub_u32 s64, s47, 11796480\n"
        "buffer_load_dwordx4 v243, s[8:11], s64 offen nt lds\n"
        "s_add_u32 s49, s49, 90112\n"
        "v_add_u32 v246, s49, v10\n"
        "s_lshr_b32 s35, s42, 1\n"
        "s_mul_i32 s49, s35, 18432\n"
        "v_mul_u32_u24 v12, 1536, v2\n"
        "v_add_u32 v12, s49, v12\n"
        "v_mad_u32_u24 v13, v9, 6, v12\n"
        "v_lshl_add_u32 v248, v4, 4, v13\n"
        "v_mad_i32_i24 v254, v4, -16, v13\n"
        "v_mul_u32_u24 v10, 196608, v2\n"
        "v_lshl_add_u32 v10, v3, 2, v10\n"
        "s_lshl_b32 s49, s35, 6\n"
        "s_add_u32 m0, s40, 88064\n"
        "s_sub_u32 s64, s47, 11796480\n"
        "buffer_load_dwordx4 v244, s[8:11], s64 offen nt lds\n"
        "s_add_u32 s49, s49, 128\n"
        "v_add_u32 v250, s49, v10\n"
        "s_mul_i32 s26, s35, 192\n"
        "s_sub_u32 s26, s26, 128\n"
        "s_mul_i32 s43, s31, 384\n"
        "s_add_u32 s44, s31, 4\n"
        "s_and_b32 s44, s44, 511\n"
        "s_mul_i32 s44, s44, 384\n"
        "s_add_u32 s37, s45, s66\n"
        "s_add_u32 s37, s37, 4\n"
        "s_mul_i32 s37, s37, 196608\n"
        "s_mul_i32 s51, s51, 36864\n"
        "s_mov_b32 m0, s48\n"
        "s_sub_u32 s64, s36, 4194304\n"
        "buffer_load_dwordx4 v249, s[12:15], s64 offen nt lds\n"
        "s_mul_i32 s66, s35, 18432\n"
        "s_add_u32 s66, s66, s51\n"
        "v_add_u32 v12, s51, v12\n"
        "v_bfrev_b32 v14, v0\n"
        "v_bfe_u32 v10, v14, 28, 2\n"
        "v_mad_u32_u24 v10, v10, 6, v7\n"
        "v_bfe_u32 v13, v0, 1, 3\n"
        "v_mad_u32_u24 v13, v13, 6, v11\n"
        "v_add_u32 v13, 4, v13\n"
        "v_xor_b32 v10, v10, v4\n"
        "v_xor_b32 v13, v13, v4\n"
        "v_lshl_add_u32 v236, v10, 4, v12\n"
        "buffer_load_dwordx4 v149, s[12:15], s64 offen offset:1024 nt lds\n"
        "v_lshl_add_u32 v237, v13, 4, v12\n"
        "v_lshrrev_b32 v10, 2, v1\n"
        "v_mul_u32_u24 v10, 192, v10\n"
        "v_add_u32 v10, s66, v10\n"
        "v_lshlrev_b32 v13, 5, v4\n"
        "v_add_u32 v12, v10, v13\n"
        "v_sub_u32 v13, v10, v13\n"
        "v_lshrrev_b32 v14, 30, v14\n"
        "v_lshl_add_u32 v234, v14, 3, v12\n"
        "v_lshl_add_u32 v235, v14, 3, v13\n"
        "v_lshl_add_u32 v238, v7, 3, v12\n"
        "v_lshl_add_u32 v239, v7, 3, v13\n"
        "s_load_dwordx2 s[4:5], s[0:1], 56\n"
        "s_lshl_b32 s46, s35, 8\n"
        "s_mul_i32 s35, s35, -192\n"
        "s_movk_i32 s49, 1536\n"
        "v_mul_u32_u24 v10, 171, v1\n"
        "v_lshrrev_b32 v10, 11, v10\n"
        "v_mad_i32_i24 v13, v10, -12, v1\n"
        "v_lshrrev_b32 v13, 3, v13\n"
        "v_mul_u32_u24 v14, 192, v10\n"
        "v_add_u32 v14, s46, v14\n"
        "v_mad_i32_i24 v14, v13, s35, v14\n"
        "v_and_b32 v13, 4, v10\n"
        "v_lshlrev_b32 v13, 3, v13\n"
        "v_xad_u32 v124, v150, v13, v14\n"
        "v_lshrrev_b32 v13, 2, v10\n"
        "v_and_or_b32 v13, v10, 8, v13\n"
        "v_mad_u32_u24 v127, v13, s49, v124\n"
        "v_add_u32 v12, 64, v1\n"
        "v_mul_u32_u24 v10, 171, v12\n"
        "v_lshrrev_b32 v10, 11, v10\n"
        "v_mad_i32_i24 v13, v10, -12, v12\n"
        "v_lshrrev_b32 v13, 3, v13\n"
        "v_mul_u32_u24 v14, 192, v10\n"
        "v_add_u32 v14, s46, v14\n"
        "v_mad_i32_i24 v14, v13, s35, v14\n"
        "v_and_b32 v13, 4, v10\n"
        "v_lshlrev_b32 v13, 3, v13\n"
        "v_xad_u32 v125, v150, v13, v14\n"
        "v_lshrrev_b32 v13, 2, v10\n"
        "v_and_or_b32 v13, v10, 8, v13\n"
        "v_mad_u32_u24 v232, v13, s49, v125\n"
        "v_add_u32 v12, 128, v1\n"
        "v_mul_u32_u24 v10, 171, v12\n"
        "v_lshrrev_b32 v10, 11, v10\n"
        "v_mad_i32_i24 v13, v10, -12, v12\n"
        "v_lshrrev_b32 v13, 3, v13\n"
        "v_mul_u32_u24 v14, 192, v10\n"
        "v_add_u32 v14, s46, v14\n"
        "v_mad_i32_i24 v14, v13, s35, v14\n"
        "v_and_b32 v13, 4, v10\n"
        "v_lshlrev_b32 v13, 3, v13\n"
        "v_xad_u32 v126, v150, v13, v14\n"
        "v_lshrrev_b32 v13, 2, v10\n"
        "v_and_or_b32 v13, v10, 8, v13\n"
        "v_mad_u32_u24 v233, v13, s49, v126\n"
        "s_mov_b32 s24, 0x3fb8aa3b\n"
        "s_mov_b32 s25, 0x4f800000\n"
        "s_mov_b32 s50, 0x3e553b95\n"
        "s_mov_b32 s27, 0\n"
        "s_mov_b32 s28, 98304\n"
        "s_mov_b32 s30, 73728\n"
        "s_mov_b32 s29, 122880\n"
        "v_mov_b32 v253, 0x3f803f80\n"
        "s_nop 0\n"
        "v_accvgpr_write_b32 a252, v253\n"
        "v_accvgpr_write_b32 a253, v253\n"
        "v_accvgpr_write_b32 a254, v253\n"
        "v_accvgpr_write_b32 a255, v253\n"
        "s_waitcnt vmcnt(12)\n"
        "v_lshlrev_b32 v32, 16, v28\n"
        "v_and_b32 v33, 0xffff0000, v28\n"
        "v_lshlrev_b32 v34, 16, v29\n"
        "v_and_b32 v35, 0xffff0000, v29\n"
        "v_mul_f32 v32, s24, v32\n"
        "v_mul_f32 v33, s24, v33\n"
        "v_mul_f32 v34, s24, v34\n"
        "v_mul_f32 v35, s24, v35\n"
        "ds_write_b32 v146, v32 offset:0\n"
        "ds_write_b32 v146, v33 offset:1024\n"
        "ds_write_b32 v146, v34 offset:2048\n"
        "ds_write_b32 v146, v35 offset:3072\n"
        "s_cmp_eq_u32 s42, 0\n"
        "s_cselect_b32 s3, 4, 0\n"
        "s_cmp_eq_u32 s2, 0\n"
        "s_cselect_b32 s6, s3, 0\n"
        "s_mov_b32 s7, 0x20000\n"
        "v_mov_b32 v67, 0\n"
        "s_add_u32 s24, s42, 4\n"
        "s_mul_i32 s24, s24, 196608\n"
        "s_lshl_b32 s34, s42, 11\n"
        "s_and_b32 s37, s42, 1\n"
        "s_add_u32 s37, s37, 1\n"
        "s_mul_i32 s37, s37, 786432\n"
        "s_waitcnt vmcnt(8) lgkmcnt(0)\n"
        "s_barrier\n"
        "s_mov_b32 s56, s2\n"
        "s_add_u32 s57, s2, 256\n"
        "s_add_u32 s58, s2, 512\n"
        "s_mov_b32 s59, -1\n"
        "s_mul_i32 s60, s2, 0x01010101\n"
        "s_mov_b32 s61, s60\n"
        "ds_read_b128 v[0:3], v140 offset:0\n"
        "ds_read_b128 v[4:7], v141 offset:0\n"
        "ds_read_b128 v[8:11], v142 offset:0\n"
        "ds_read_b128 v[12:15], v143 offset:0\n"
        "ds_read_b128 v[16:19], v140 offset:1024\n"
        "ds_read_b128 v[20:23], v141 offset:1024\n"
        "ds_read_b128 v[24:27], v142 offset:1024\n"
        "ds_read_b128 v[28:31], v143 offset:1024\n"
        "ds_read_b128 v[32:35], v140 offset:8192\n"
        "ds_read_b128 v[36:39], v141 offset:8192\n"
        "ds_read_b128 v[40:43], v142 offset:8192\n"
        "ds_read_b128 v[44:47], v143 offset:8192\n"
        "ds_read_b128 v[48:51], v140 offset:9216\n"
        "ds_read_b128 v[52:55], v141 offset:9216\n"
        "ds_read_b128 v[56:59], v142 offset:9216\n"
        "ds_read_b128 v[60:63], v143 offset:9216\n"
        "s_add_u32 m0, s40, 0\n"
        "s_add_u32 s64, s65, 0\n"
        "buffer_load_dwordx4 v128, s[20:23], s64 offen lds\n"
        "s_waitcnt lgkmcnt(15)\n"
        "v_lshl_add_u32 v0, v0, 2, s67\n"
        "v_lshl_add_u32 v1, v1, 2, s67\n"
        "v_lshl_add_u32 v2, v2, 2, s67\n"
        "v_lshl_add_u32 v3, v3, 2, s67\n"
        "s_waitcnt lgkmcnt(14)\n"
        "v_lshl_add_u32 v4, v4, 2, s67\n"
        "v_lshl_add_u32 v5, v5, 2, s67\n"
        "v_lshl_add_u32 v6, v6, 2, s67\n"
        "v_lshl_add_u32 v7, v7, 2, s67\n"
        "ds_read_b32 v0, v0\n"
        "ds_read_b32 v1, v1\n"
        "ds_read_b32 v2, v2\n"
        "ds_read_b32 v3, v3\n"
        "ds_read_b32 v4, v4\n"
        "ds_read_b32 v5, v5\n"
        "ds_read_b32 v6, v6\n"
        "ds_read_b32 v7, v7\n"
        "buffer_load_dwordx4 v129, s[20:23], s64 offen offset:1024 lds\n"
        "s_waitcnt lgkmcnt(15)\n"
        "v_lshl_add_u32 v8, v8, 2, s67\n"
        "v_lshl_add_u32 v9, v9, 2, s67\n"
        "v_lshl_add_u32 v10, v10, 2, s67\n"
        "v_lshl_add_u32 v11, v11, 2, s67\n"
        "v_lshl_add_u32 v12, v12, 2, s67\n"
        "v_lshl_add_u32 v13, v13, 2, s67\n"
        "v_lshl_add_u32 v14, v14, 2, s67\n"
        "v_lshl_add_u32 v15, v15, 2, s67\n"
        "ds_read_b32 v8, v8\n"
        "ds_read_b32 v9, v9\n"
        "ds_read_b32 v10, v10\n"
        "ds_read_b32 v11, v11\n"
        "ds_read_b32 v12, v12\n"
        "ds_read_b32 v13, v13\n"
        "ds_read_b32 v14, v14\n"
        "ds_read_b32 v15, v15\n"
        "buffer_load_dwordx4 v130, s[20:23], s64 offen offset:2048 lds\n"
        "v_lshl_add_u32 v16, v16, 2, s67\n"
        "v_lshl_add_u32 v17, v17, 2, s67\n"
        "v_lshl_add_u32 v18, v18, 2, s67\n"
        "v_lshl_add_u32 v19, v19, 2, s67\n"
        "v_lshl_add_u32 v20, v20, 2, s67\n"
        "v_lshl_add_u32 v21, v21, 2, s67\n"
        "v_lshl_add_u32 v22, v22, 2, s67\n"
        "v_lshl_add_u32 v23, v23, 2, s67\n"
        "ds_read_b32 v16, v16\n"
        "ds_read_b32 v17, v17\n"
        "ds_read_b32 v18, v18\n"
        "ds_read_b32 v19, v19\n"
        "ds_read_b32 v20, v20\n"
        "ds_read_b32 v21, v21\n"
        "ds_read_b32 v22, v22\n"
        "ds_read_b32 v23, v23\n"
        "s_add_u32 m0, s40, 12288\n"
        "s_add_u32 s64, s65, 36864\n"
        "buffer_load_dwordx4 v128, s[20:23], s64 offen lds\n"
        "v_lshl_add_u32 v24, v24, 2, s67\n"
        "v_lshl_add_u32 v25, v25, 2, s67\n"
        "v_lshl_add_u32 v26, v26, 2, s67\n"
        "v_lshl_add_u32 v27, v27, 2, s67\n"
        "v_lshl_add_u32 v28, v28, 2, s67\n"
        "v_lshl_add_u32 v29, v29, 2, s67\n"
        "v_lshl_add_u32 v30, v30, 2, s67\n"
        "v_lshl_add_u32 v31, v31, 2, s67\n"
        "ds_read_b32 v24, v24\n"
        "ds_read_b32 v25, v25\n"
        "ds_read_b32 v26, v26\n"
        "ds_read_b32 v27, v27\n"
        "ds_read_b32 v28, v28\n"
        "ds_read_b32 v29, v29\n"
        "ds_read_b32 v30, v30\n"
        "ds_read_b32 v31, v31\n"
        "buffer_load_dwordx4 v129, s[20:23], s64 offen offset:1024 lds\n"
        "buffer_load_dwordx4 v130, s[20:23], s64 offen offset:2048 lds\n"
        "s_mov_b64 exec, 0\n"
        "s_cmp_eq_u32 s42, 0\n"
        "s_cbranch_scc1 41f\n"
        "s_cmp_eq_u32 s42, 1\n"
        "s_cbranch_scc1 42f\n"
        "s_cmp_eq_u32 s42, 2\n"
        "s_cbranch_scc1 43f\n"
        "s_branch 40f\n"
        ".fill 281, 4, 0xbf800000\n"
        "40:\n"
        "s_mov_b64 exec, -1\n"
        "s_add_u32 m0, s40, 24576\n"
        "s_add_u32 s64, s65, 73728\n"
        "buffer_load_dwordx4 v128, s[20:23], s64 offen lds\n"
        "v_lshl_add_u32 v32, v32, 2, s67\n"
        "v_lshl_add_u32 v33, v33, 2, s67\n"
        "v_lshl_add_u32 v34, v34, 2, s67\n"
        "v_lshl_add_u32 v35, v35, 2, s67\n"
        "s_waitcnt lgkmcnt(15)\n"
        "v_lshl_add_u32 v36, v36, 2, s67\n"
        "v_lshl_add_u32 v37, v37, 2, s67\n"
        "v_lshl_add_u32 v38, v38, 2, s67\n"
        "v_lshl_add_u32 v39, v39, 2, s67\n"
        "ds_read_b32 v32, v32\n"
        "ds_read_b32 v33, v33\n"
        "ds_read_b32 v34, v34\n"
        "ds_read_b32 v35, v35\n"
        "ds_read_b32 v36, v36\n"
        "ds_read_b32 v37, v37\n"
        "ds_read_b32 v38, v38\n"
        "ds_read_b32 v39, v39\n"
        "buffer_load_dwordx4 v129, s[20:23], s64 offen offset:1024 lds\n"
        "v_lshl_add_u32 v40, v40, 2, s67\n"
        "v_lshl_add_u32 v41, v41, 2, s67\n"
        "v_lshl_add_u32 v42, v42, 2, s67\n"
        "v_lshl_add_u32 v43, v43, 2, s67\n"
        "v_lshl_add_u32 v44, v44, 2, s67\n"
        "v_lshl_add_u32 v45, v45, 2, s67\n"
        "v_lshl_add_u32 v46, v46, 2, s67\n"
        "v_lshl_add_u32 v47, v47, 2, s67\n"
        "ds_read_b32 v40, v40\n"
        "ds_read_b32 v41, v41\n"
        "ds_read_b32 v42, v42\n"
        "ds_read_b32 v43, v43\n"
        "ds_read_b32 v44, v44\n"
        "ds_read_b32 v45, v45\n"
        "ds_read_b32 v46, v46\n"
        "ds_read_b32 v47, v47\n"
        "buffer_load_dwordx4 v130, s[20:23], s64 offen offset:2048 lds\n"
        "v_lshl_add_u32 v48, v48, 2, s67\n"
        "v_lshl_add_u32 v49, v49, 2, s67\n"
        "v_lshl_add_u32 v50, v50, 2, s67\n"
        "v_lshl_add_u32 v51, v51, 2, s67\n"
        "v_lshl_add_u32 v52, v52, 2, s67\n"
        "v_lshl_add_u32 v53, v53, 2, s67\n"
        "v_lshl_add_u32 v54, v54, 2, s67\n"
        "v_lshl_add_u32 v55, v55, 2, s67\n"
        "ds_read_b32 v48, v48\n"
        "ds_read_b32 v49, v49\n"
        "ds_read_b32 v50, v50\n"
        "ds_read_b32 v51, v51\n"
        "ds_read_b32 v52, v52\n"
        "ds_read_b32 v53, v53\n"
        "ds_read_b32 v54, v54\n"
        "ds_read_b32 v55, v55\n"
        "s_add_u32 m0, s40, 36864\n"
        "s_add_u32 s64, s65, 110592\n"
        "buffer_load_dwordx4 v128, s[20:23], s64 offen lds\n"
        "v_lshl_add_u32 v56, v56, 2, s67\n"
        "v_lshl_add_u32 v57, v57, 2, s67\n"
        "v_lshl_add_u32 v58, v58, 2, s67\n"
        "v_lshl_add_u32 v59, v59, 2, s67\n"
        "v_lshl_add_u32 v60, v60, 2, s67\n"
        "v_lshl_add_u32 v61, v61, 2, s67\n"
        "v_lshl_add_u32 v62, v62, 2, s67\n"
        "v_lshl_add_u32 v63, v63, 2, s67\n"
        "ds_read_b32 v56, v56\n"
        "ds_read_b32 v57, v57\n"
        "ds_read_b32 v58, v58\n"
        "ds_read_b32 v59, v59\n"
        "ds_read_b32 v60, v60\n"
        "ds_read_b32 v61, v61\n"
        "ds_read_b32 v62, v62\n"
        "ds_read_b32 v63, v63\n"
        "buffer_load_dwordx4 v129, s[20:23], s64 offen offset:1024 lds\n"
        "buffer_load_dwordx4 v130, s[20:23], s64 offen offset:2048 lds\n"
        "s_waitcnt vmcnt(14)\n"
        "s_barrier\n"
        "s_add_u32 m0, s40, 49152\n"
        "s_add_u32 s64, s65, 147456\n"
        "buffer_load_dwordx4 v128, s[20:23], s64 offen lds\n"
        "ds_read_b128 a[144:147], v240 offset:0\n"
        "ds_read_b128 a[148:151], v241 offset:0\n"
        "ds_read_b128 a[160:163], v240 offset:1536\n"
        "ds_read_b128 a[164:167], v241 offset:1536\n"
        "buffer_load_dwordx4 v129, s[20:23], s64 offen offset:1024 lds\n"
        "ds_read_b128 a[176:179], v240 offset:12288\n"
        "ds_read_b128 a[180:183], v241 offset:12288\n"
        "ds_read_b128 a[192:195], v240 offset:13824\n"
        "ds_read_b128 a[196:199], v241 offset:13824\n"
        "buffer_load_dwordx4 v130, s[20:23], s64 offen offset:2048 lds\n"
        "ds_read_b128 a[152:155], v240 offset:128\n"
        "ds_read_b128 a[156:159], v241 offset:128\n"
        "ds_read_b128 a[168:171], v240 offset:1664\n"
        "ds_read_b128 a[172:175], v241 offset:1664\n"
        "s_add_u32 m0, s40, 61440\n"
        "s_add_u32 s64, s65, 184320\n"
        "buffer_load_dwordx4 v128, s[20:23], s64 offen lds\n"
        "ds_read_b128 a[184:187], v240 offset:12416\n"
        "ds_read_b128 a[188:191], v241 offset:12416\n"
        "ds_read_b128 a[200:203], v240 offset:13952\n"
        "ds_read_b128 a[204:207], v241 offset:13952\n"
        "buffer_load_dwordx4 v129, s[20:23], s64 offen offset:1024 lds\n"
        "ds_read_b128 a[216:219], v240 offset:256\n"
        "ds_read_b128 a[220:223], v241 offset:256\n"
        "ds_read_b128 a[224:227], v240 offset:1792\n"
        "ds_read_b128 a[228:231], v241 offset:1792\n"
        "buffer_load_dwordx4 v130, s[20:23], s64 offen offset:2048 lds\n"
        "ds_read_b128 a[232:235], v240 offset:12544\n"
        "ds_read_b128 a[236:239], v241 offset:12544\n"
        "ds_read_b128 a[240:243], v240 offset:14080\n"
        "ds_read_b128 a[244:247], v241 offset:14080\n"
        "s_lshr_b32 s67, s42, 1\n"
        "s_add_u32 s67, s67, s40\n"
        "s_sub_u32 s67, -2, s67\n"
        "s_waitcnt vmcnt(12)\n"
        "s_barrier\n"
        "ds_read_b64_tr_b16 a[0:1], v131 offset:0\n"
        "ds_read_b64_tr_b16 a[2:3], v131 offset:1536\n"
        "ds_read_b64_tr_b16 a[4:5], v131 offset:12288\n"
        "ds_read_b64_tr_b16 a[6:7], v131 offset:13824\n"
        "ds_read_b64_tr_b16 a[24:25], v132 offset:0\n"
        "ds_read_b64_tr_b16 a[26:27], v132 offset:1536\n"
        "ds_read_b64_tr_b16 a[28:29], v132 offset:12288\n"
        "ds_read_b64_tr_b16 a[30:31], v132 offset:13824\n"
        "ds_read_b64_tr_b16 a[48:49], v133 offset:0\n"
        "ds_read_b64_tr_b16 a[50:51], v133 offset:1536\n"
        "ds_read_b64_tr_b16 a[52:53], v133 offset:12288\n"
        "ds_read_b64_tr_b16 a[54:55], v133 offset:13824\n"
        "s_waitcnt lgkmcnt(10)\n"
        "v_mfma_f32_16x16x32_bf16 v[96:99], a[0:3], a[144:147], 0\n"
        "s_waitcnt lgkmcnt(8)\n"
        "s_add_u32 m0, s40, 98304\n"
        "s_add_u32 s64, s65, 384\n"
        "v_mfma_f32_16x16x32_bf16 v[96:99], a[4:7], a[148:151], v[96:99]\n"
        "s_waitcnt lgkmcnt(4)\n"
        "buffer_load_dwordx4 v128, s[20:23], s64 offen lds\n"
        "v_mfma_f32_16x16x32_bf16 v[100:103], a[28:31], a[148:151], 0\n"
        "v_mfma_f32_16x16x32_bf16 v[100:103], a[24:27], a[144:147], v[100:103]\n"
        "s_waitcnt lgkmcnt(2)\n"
        "v_mfma_f32_16x16x32_bf16 v[104:107], a[48:51], a[144:147], 0\n"
        "s_waitcnt lgkmcnt(0)\n"
        "v_mfma_f32_16x16x32_bf16 v[104:107], a[52:55], a[148:151], v[104:107]\n"
        "buffer_load_dwordx4 v129, s[20:23], s64 offen offset:1024 lds\n"
        "v_mfma_f32_16x16x32_bf16 v[108:111], a[4:7], a[164:167], 0\n"
        "v_mfma_f32_16x16x32_bf16 v[108:111], a[0:3], a[160:163], v[108:111]\n"
        "v_mfma_f32_16x16x32_bf16 v[112:115], a[24:27], a[160:163], 0\n"
        "v_mfma_f32_16x16x32_bf16 v[112:115], a[28:31], a[164:167], v[112:115]\n"
        "buffer_load_dwordx4 v130, s[20:23], s64 offen offset:2048 lds\n"
        "v_mfma_f32_16x16x32_bf16 v[116:119], a[52:55], a[164:167], 0\n"
        "s_add_u32 m0, s40, 110592\n"
        "v_mfma_f32_16x16x32_bf16 v[116:119], a[48:51], a[160:163], v[116:119]\n"
        "s_add_u32 s64, s65, 37248\n"
        "v_mfma_f32_16x16x32_bf16 v[120:123], a[0:3], a[176:179], 0\n"
        "v_mfma_f32_16x16x32_bf16 v[120:123], a[4:7], a[180:183], v[120:123]\n"
        "s_waitcnt vmcnt(9)\n"
        "buffer_load_dwordx4 v128, s[20:23], s64 offen lds\n"
        "v_mfma_f32_16x16x32_bf16 v[92:95], a[28:31], a[180:183], 0\n"
        "s_barrier\n"
        "ds_read_b64_tr_b16 a[8:9], v131 offset:24576\n"
        "v_mfma_f32_16x16x32_bf16 v[92:95], a[24:27], a[176:179], v[92:95]\n"
        "ds_read_b64_tr_b16 a[10:11], v131 offset:26112\n"
        "v_mfma_f32_16x16x32_bf16 v[152:155], a[48:51], a[176:179], 0\n"
        "ds_read_b64_tr_b16 a[12:13], v131 offset:36864\n"
        "v_mfma_f32_16x16x32_bf16 v[152:155], a[52:55], a[180:183], v[152:155]\n"
        "buffer_load_dwordx4 v129, s[20:23], s64 offen offset:1024 lds\n"
        "v_mfma_f32_16x16x32_bf16 v[172:175], a[4:7], a[196:199], 0\n"
        "ds_read_b64_tr_b16 a[14:15], v131 offset:38400\n"
        "ds_read_b64_tr_b16 a[32:33], v132 offset:24576\n"
        "v_mfma_f32_16x16x32_bf16 v[172:175], a[0:3], a[192:195], v[172:175]\n"
        "ds_read_b64_tr_b16 a[34:35], v132 offset:26112\n"
        "v_mfma_f32_16x16x32_bf16 v[192:195], a[24:27], a[192:195], 0\n"
        "ds_read_b64_tr_b16 a[36:37], v132 offset:36864\n"
        "v_mfma_f32_16x16x32_bf16 v[192:195], a[28:31], a[196:199], v[192:195]\n"
        "buffer_load_dwordx4 v130, s[20:23], s64 offen offset:2048 lds\n"
        "v_mfma_f32_16x16x32_bf16 v[212:215], a[52:55], a[196:199], 0\n"
        "s_add_u32 m0, s40, 0\n"
        "ds_read_b64_tr_b16 a[38:39], v132 offset:38400\n"
        "ds_read_b64_tr_b16 a[56:57], v133 offset:24576\n"
        "v_mfma_f32_16x16x32_bf16 v[212:215], a[48:51], a[192:195], v[212:215]\n"
        "s_waitcnt lgkmcnt(7)\n"
        "s_add_u32 s64, s65, 74112\n"
        "ds_read_b64_tr_b16 a[58:59], v133 offset:26112\n"
        "v_mfma_f32_16x16x32_bf16 v[96:99], a[8:11], a[152:155], v[96:99]\n"
        "s_waitcnt lgkmcnt(6)\n"
        "ds_read_b64_tr_b16 a[60:61], v133 offset:36864\n"
        "v_mfma_f32_16x16x32_bf16 v[96:99], a[12:15], a[156:159], v[96:99]\n"
        "s_waitcnt lgkmcnt(3)\n"
        "ds_read_b64_tr_b16 a[62:63], v133 offset:38400\n"
        "v_mfma_f32_16x16x32_bf16 v[100:103], a[36:39], a[156:159], v[100:103]\n"
        "buffer_load_dwordx4 v128, s[20:23], s64 offen lds\n"
        "v_mfma_f32_16x16x32_bf16 v[100:103], a[32:35], a[152:155], v[100:103]\n"
        "s_waitcnt lgkmcnt(2)\n"
        "v_mfma_f32_16x16x32_bf16 v[104:107], a[56:59], a[152:155], v[104:107]\n"
        "s_waitcnt lgkmcnt(0)\n"
        "v_mfma_f32_16x16x32_bf16 v[104:107], a[60:63], a[156:159], v[104:107]\n"
        "buffer_load_dwordx4 v129, s[20:23], s64 offen offset:1024 lds\n"
        "v_mfma_f32_16x16x32_bf16 v[108:111], a[12:15], a[172:175], v[108:111]\n"
        "v_mfma_f32_16x16x32_bf16 v[108:111], a[8:11], a[168:171], v[108:111]\n"
        "buffer_atomic_swap v67, v249, s[4:7], 0 offen sc1\n"
        "v_mfma_f32_16x16x32_bf16 v[112:115], a[32:35], a[168:171], v[112:115]\n"
        "v_mfma_f32_16x16x32_bf16 v[112:115], a[36:39], a[172:175], v[112:115]\n"
        "buffer_load_dwordx4 v130, s[20:23], s64 offen offset:2048 lds\n"
        "v_mfma_f32_16x16x32_bf16 v[116:119], a[60:63], a[172:175], v[116:119]\n"
        "s_add_u32 m0, s40, 12288\n"
        "v_mfma_f32_16x16x32_bf16 v[116:119], a[56:59], a[168:171], v[116:119]\n"
        "s_add_u32 s64, s65, 110976\n"
        "v_mfma_f32_16x16x32_bf16 v[120:123], a[8:11], a[184:187], v[120:123]\n"
        "s_waitcnt vmcnt(10)\n"
        "v_mfma_f32_16x16x32_bf16 v[120:123], a[12:15], a[188:191], v[120:123]\n"
        "s_barrier\n"
        "buffer_load_dwordx4 v128, s[20:23], s64 offen lds\n"
        "v_mfma_f32_16x16x32_bf16 v[92:95], a[36:39], a[188:191], v[92:95]\n"
        "ds_read_b64_tr_b16 a[16:17], v131 offset:49152\n"
        "ds_read_b64_tr_b16 a[18:19], v131 offset:50688\n"
        "v_mfma_f32_16x16x32_bf16 v[92:95], a[32:35], a[184:187], v[92:95]\n"
        "ds_read_b64_tr_b16 a[20:21], v131 offset:61440\n"
        "v_mfma_f32_16x16x32_bf16 v[152:155], a[56:59], a[184:187], v[152:155]\n"
        "ds_read_b64_tr_b16 a[22:23], v131 offset:62976\n"
        "v_mfma_f32_16x16x32_bf16 v[152:155], a[60:63], a[188:191], v[152:155]\n"
        "buffer_load_dwordx4 v129, s[20:23], s64 offen offset:1024 lds\n"
        "v_mfma_f32_16x16x32_bf16 v[172:175], a[12:15], a[204:207], v[172:175]\n"
        "ds_read_b64_tr_b16 a[40:41], v132 offset:49152\n"
        "ds_read_b64_tr_b16 a[42:43], v132 offset:50688\n"
        "v_mfma_f32_16x16x32_bf16 v[172:175], a[8:11], a[200:203], v[172:175]\n"
        "ds_read_b64_tr_b16 a[44:45], v132 offset:61440\n"
        "v_mfma_f32_16x16x32_bf16 v[192:195], a[32:35], a[200:203], v[192:195]\n"
        "ds_read_b64_tr_b16 a[46:47], v132 offset:62976\n"
        "v_mfma_f32_16x16x32_bf16 v[192:195], a[36:39], a[204:207], v[192:195]\n"
        "buffer_load_dwordx4 v130, s[20:23], s64 offen offset:2048 lds\n"
        "v_mfma_f32_16x16x32_bf16 v[212:215], a[60:63], a[204:207], v[212:215]\n"
        "s_add_u32 m0, s40, 24576\n"
        "ds_read_b64_tr_b16 a[64:65], v133 offset:49152\n"
        "ds_read_b64_tr_b16 a[66:67], v133 offset:50688\n"
        "v_mfma_f32_16x16x32_bf16 v[212:215], a[56:59], a[200:203], v[212:215]\n"
        "s_waitcnt lgkmcnt(8)\n"
        "s_add_u32 s64, s65, 147840\n"
        "ds_read_b64_tr_b16 a[68:69], v133 offset:61440\n"
        "v_mfma_f32_16x16x32_bf16 v[96:99], a[16:19], a[216:219], v[96:99]\n"
        "s_waitcnt lgkmcnt(7)\n"
        "ds_read_b64_tr_b16 a[70:71], v133 offset:62976\n"
        "v_mfma_f32_16x16x32_bf16 v[96:99], a[20:23], a[220:223], v[96:99]\n"
        "s_waitcnt lgkmcnt(4)\n"
        "buffer_load_dwordx4 v128, s[20:23], s64 offen lds\n"
        "v_mfma_f32_16x16x32_bf16 v[100:103], a[44:47], a[220:223], v[100:103]\n"
        "v_mfma_f32_16x16x32_bf16 v[100:103], a[40:43], a[216:219], v[100:103]\n"
        "s_waitcnt lgkmcnt(2)\n"
        "v_mfma_f32_16x16x32_bf16 v[104:107], a[64:67], a[216:219], v[104:107]\n"
        "s_waitcnt lgkmcnt(0)\n"
        "v_mfma_f32_16x16x32_bf16 v[104:107], a[68:71], a[220:223], v[104:107]\n"
        "buffer_load_dwordx4 v129, s[20:23], s64 offen offset:1024 lds\n"
        "v_mfma_f32_16x16x32_bf16 v[108:111], a[20:23], a[228:231], v[108:111]\n"
        "v_mfma_f32_16x16x32_bf16 v[108:111], a[16:19], a[224:227], v[108:111]\n"
        "v_mul_f32 v96, s50, v96\n"
        "v_mul_f32 v97, s50, v97\n"
        "v_mfma_f32_16x16x32_bf16 v[112:115], a[40:43], a[224:227], v[112:115]\n"
        "v_mul_f32 v98, s50, v98\n"
        "v_mul_f32 v99, s50, v99\n"
        "v_mfma_f32_16x16x32_bf16 v[112:115], a[44:47], a[228:231], v[112:115]\n"
        "buffer_load_dwordx4 v130, s[20:23], s64 offen offset:2048 lds\n"
        "v_mfma_f32_16x16x32_bf16 v[116:119], a[68:71], a[228:231], v[116:119]\n"
        "s_add_u32 m0, s40, 36864\n"
        "v_cvt_pk_bf16_f32 v156, v96, v97\n"
        "v_cvt_pk_bf16_f32 v157, v98, v99\n"
        "v_mfma_f32_16x16x32_bf16 v[116:119], a[64:67], a[224:227], v[116:119]\n"
        "s_add_u32 s64, s65, 184704\n"
        "v_mul_f32 v100, s50, v100\n"
        "v_mul_f32 v101, s50, v101\n"
        "v_mfma_f32_16x16x32_bf16 v[120:123], a[16:19], a[232:235], v[120:123]\n"
        "s_waitcnt vmcnt(10)\n"
        "s_barrier\n"
        "ds_read_b64_tr_b16 a[72:73], v136 offset:0\n"
        "v_mul_f32 v102, s50, v102\n"
        "v_mfma_f32_16x16x32_bf16 v[120:123], a[20:23], a[236:239], v[120:123]\n"
        "buffer_load_dwordx4 v128, s[20:23], s64 offen lds\n"
        "v_mfma_f32_16x16x32_bf16 v[92:95], a[44:47], a[236:239], v[92:95]\n"
        "ds_read_b64_tr_b16 a[74:75], v136 offset:1536\n"
        "ds_read_b64_tr_b16 a[76:77], v136 offset:12288\n"
        "v_mfma_f32_16x16x32_bf16 v[92:95], a[40:43], a[232:235], v[92:95]\n"
        "ds_read_b64_tr_b16 a[78:79], v136 offset:13824\n"
        "v_mul_f32 v103, s50, v103\n"
        "v_mfma_f32_16x16x32_bf16 v[152:155], a[64:67], a[232:235], v[152:155]\n"
        "ds_read_b64_tr_b16 a[96:97], v137 offset:0\n"
        "v_cvt_pk_bf16_f32 v158, v100, v101\n"
        "v_mfma_f32_16x16x32_bf16 v[152:155], a[68:71], a[236:239], v[152:155]\n"
        "buffer_load_dwordx4 v129, s[20:23], s64 offen offset:1024 lds\n"
        "v_mfma_f32_16x16x32_bf16 v[172:175], a[20:23], a[244:247], v[172:175]\n"
        "ds_read_b64_tr_b16 a[98:99], v137 offset:1536\n"
        "ds_read_b64_tr_b16 a[100:101], v137 offset:12288\n"
        "v_mfma_f32_16x16x32_bf16 v[172:175], a[16:19], a[240:243], v[172:175]\n"
        "ds_read_b64_tr_b16 a[102:103], v137 offset:13824\n"
        "v_cvt_pk_bf16_f32 v159, v102, v103\n"
        "v_mfma_f32_16x16x32_bf16 v[192:195], a[40:43], a[240:243], v[192:195]\n"
        "ds_read_b64_tr_b16 a[120:121], v138 offset:0\n"
        "v_mul_f32 v104, s50, v104\n"
        "v_mfma_f32_16x16x32_bf16 v[192:195], a[44:47], a[244:247], v[192:195]\n"
        "buffer_load_dwordx4 v130, s[20:23], s64 offen offset:2048 lds\n"
        "v_mfma_f32_16x16x32_bf16 v[212:215], a[68:71], a[244:247], v[212:215]\n"
        "s_add_u32 m0, s40, 49152\n"
        "ds_read_b64_tr_b16 a[122:123], v138 offset:1536\n"
        "ds_read_b64_tr_b16 a[124:125], v138 offset:12288\n"
        "v_mfma_f32_16x16x32_bf16 v[212:215], a[64:67], a[240:243], v[212:215]\n"
        "s_waitcnt lgkmcnt(9)\n"
        "s_add_u32 s64, s65, 768\n"
        "ds_read_b64_tr_b16 a[126:127], v138 offset:13824\n"
        "v_mul_f32 v105, s50, v105\n"
        "v_mfma_f32_16x16x32_bf16 v[64:67], a[72:75], a[144:147], 0\n"
        "s_waitcnt lgkmcnt(8)\n"
        "v_mul_f32 v106, s50, v106\n"
        "v_mul_f32 v107, s50, v107\n"
        "v_mfma_f32_16x16x32_bf16 v[64:67], a[76:79], a[148:151], v[64:67]\n"
        "s_waitcnt lgkmcnt(4)\n"
        "buffer_load_dwordx4 v128, s[20:23], s64 offen lds\n"
        "v_mfma_f32_16x16x32_bf16 v[68:71], a[100:103], a[148:151], 0\n"
        "v_cvt_pk_bf16_f32 v160, v104, v105\n"
        "v_cvt_pk_bf16_f32 v161, v106, v107\n"
        "v_mfma_f32_16x16x32_bf16 v[68:71], a[96:99], a[144:147], v[68:71]\n"
        "s_waitcnt lgkmcnt(2)\n"
        "v_mul_f32 v108, s50, v108\n"
        "v_mul_f32 v109, s50, v109\n"
        "v_mfma_f32_16x16x32_bf16 v[72:75], a[120:123], a[144:147], 0\n"
        "s_waitcnt lgkmcnt(0)\n"
        "v_mov_b64 v[166:167], v[160:161]\n"
        "v_mul_f32 v110, s50, v110\n"
        "v_mfma_f32_16x16x32_bf16 v[72:75], a[124:127], a[148:151], v[72:75]\n"
        "buffer_load_dwordx4 v129, s[20:23], s64 offen offset:1024 lds\n"
        "v_mfma_f32_16x16x32_bf16 v[76:79], a[76:79], a[164:167], 0\n"
        "v_mul_f32 v111, s50, v111\n"
        "v_cvt_pk_bf16_f32 v176, v108, v109\n"
        "v_mfma_f32_16x16x32_bf16 v[76:79], a[72:75], a[160:163], v[76:79]\n"
        "v_cvt_pk_bf16_f32 v177, v110, v111\n"
        "v_mul_f32 v112, s50, v112\n"
        "v_mfma_f32_16x16x32_bf16 v[80:83], a[96:99], a[160:163], 0\n"
        "v_mul_f32 v113, s50, v113\n"
        "v_mul_f32 v114, s50, v114\n"
        "v_mfma_f32_16x16x32_bf16 v[80:83], a[100:103], a[164:167], v[80:83]\n"
        "s_waitcnt vmcnt(8)\n"
        "buffer_load_dwordx4 v130, s[20:23], s64 offen offset:2048 lds\n"
        "v_mfma_f32_16x16x32_bf16 v[84:87], a[124:127], a[164:167], 0\n"
        "s_barrier\n"
        "ds_read_b64_tr_b16 a[80:81], v131 offset:0\n"
        "v_mul_f32 v115, s50, v115\n"
        "v_mfma_f32_16x16x32_bf16 v[84:87], a[120:123], a[160:163], v[84:87]\n"
        "s_add_u32 m0, s40, 61440\n"
        "ds_read_b64_tr_b16 a[82:83], v131 offset:1536\n"
        "v_cvt_pk_bf16_f32 v178, v112, v113\n"
        "v_mfma_f32_16x16x32_bf16 v[88:91], a[72:75], a[176:179], 0\n"
        "s_add_u32 s64, s65, 37632\n"
        "ds_read_b64_tr_b16 a[84:85], v131 offset:12288\n"
        "v_cvt_pk_bf16_f32 v179, v114, v115\n"
        "v_mfma_f32_16x16x32_bf16 v[88:91], a[76:79], a[180:183], v[88:91]\n"
        "buffer_load_dwordx4 v128, s[20:23], s64 offen lds\n"
        "v_mfma_f32_16x16x32_bf16 v[108:111], a[100:103], a[180:183], 0\n"
        "ds_read_b64_tr_b16 a[86:87], v131 offset:13824\n"
        "ds_read_b64_tr_b16 a[104:105], v132 offset:0\n"
        "v_mfma_f32_16x16x32_bf16 v[108:111], a[96:99], a[176:179], v[108:111]\n"
        "ds_read_b64_tr_b16 a[106:107], v132 offset:1536\n"
        "v_mul_f32 v116, s50, v116\n"
        "v_mfma_f32_16x16x32_bf16 v[112:115], a[120:123], a[176:179], 0\n"
        "ds_read_b64_tr_b16 a[108:109], v132 offset:12288\n"
        "v_mul_f32 v117, s50, v117\n"
        "v_mfma_f32_16x16x32_bf16 v[112:115], a[124:127], a[180:183], v[112:115]\n"
        "buffer_load_dwordx4 v129, s[20:23], s64 offen offset:1024 lds\n"
        "v_mfma_f32_16x16x32_bf16 v[96:99], a[76:79], a[196:199], 0\n"
        "ds_read_b64_tr_b16 a[110:111], v132 offset:13824\n"
        "ds_read_b64_tr_b16 a[128:129], v133 offset:0\n"
        "v_mfma_f32_16x16x32_bf16 v[96:99], a[72:75], a[192:195], v[96:99]\n"
        "ds_read_b64_tr_b16 a[130:131], v133 offset:1536\n"
        "v_mul_f32 v118, s50, v118\n"
        "v_mfma_f32_16x16x32_bf16 v[100:103], a[96:99], a[192:195], 0\n"
        "ds_read_b64_tr_b16 a[132:133], v133 offset:12288\n"
        "v_mul_f32 v119, s50, v119\n"
        "v_mfma_f32_16x16x32_bf16 v[100:103], a[100:103], a[196:199], v[100:103]\n"
        "buffer_load_dwordx4 v130, s[20:23], s64 offen offset:2048 lds\n"
        "v_mfma_f32_16x16x32_bf16 v[104:107], a[124:127], a[196:199], 0\n"
        "ds_read_b64_tr_b16 a[134:135], v133 offset:13824\n"
        "v_cvt_pk_bf16_f32 v180, v116, v117\n"
        "v_mfma_f32_16x16x32_bf16 v[104:107], a[120:123], a[192:195], v[104:107]\n"
        "s_waitcnt lgkmcnt(10)\n"
        "v_cvt_pk_bf16_f32 v181, v118, v119\n"
        "v_mul_f32 v120, s50, v120\n"
        "v_mfma_f32_16x16x32_bf16 v[64:67], a[80:83], a[152:155], v[64:67]\n"
        "s_waitcnt lgkmcnt(8)\n"
        "v_mul_f32 v121, s50, v121\n"
        "v_mov_b64 v[186:187], v[180:181]\n"
        "v_mfma_f32_16x16x32_bf16 v[64:67], a[84:87], a[156:159], v[64:67]\n"
        "s_waitcnt lgkmcnt(4)\n"
        "v_mul_f32 v122, s50, v122\n"
        "v_mul_f32 v123, s50, v123\n"
        "v_mfma_f32_16x16x32_bf16 v[68:71], a[108:111], a[156:159], v[68:71]\n"
        "v_cvt_pk_bf16_f32 v196, v120, v121\n"
        "v_cvt_pk_bf16_f32 v197, v122, v123\n"
        "v_mfma_f32_16x16x32_bf16 v[68:71], a[104:107], a[152:155], v[68:71]\n"
        "s_waitcnt lgkmcnt(2)\n"
        "v_mul_f32 v92, s50, v92\n"
        "v_mul_f32 v93, s50, v93\n"
        "v_mfma_f32_16x16x32_bf16 v[72:75], a[128:131], a[152:155], v[72:75]\n"
        "s_waitcnt lgkmcnt(0)\n"
        "v_mul_f32 v94, s50, v94\n"
        "v_mul_f32 v95, s50, v95\n"
        "v_mfma_f32_16x16x32_bf16 v[72:75], a[132:135], a[156:159], v[72:75]\n"
        "v_cvt_pk_bf16_f32 v198, v92, v93\n"
        "v_cvt_pk_bf16_f32 v199, v94, v95\n"
        "v_mfma_f32_16x16x32_bf16 v[76:79], a[84:87], a[172:175], v[76:79]\n"
        "v_mul_f32 v152, s50, v152\n"
        "v_mul_f32 v153, s50, v153\n"
        "v_mfma_f32_16x16x32_bf16 v[76:79], a[80:83], a[168:171], v[76:79]\n"
        "v_mul_f32 v154, s50, v154\n"
        "v_mul_f32 v155, s50, v155\n"
        "v_mfma_f32_16x16x32_bf16 v[80:83], a[104:107], a[168:171], v[80:83]\n"
        "s_waitcnt vmcnt(6)\n"
        "v_cvt_pk_bf16_f32 v200, v152, v153\n"
        "v_cvt_pk_bf16_f32 v201, v154, v155\n"
        "v_mfma_f32_16x16x32_bf16 v[80:83], a[108:111], a[172:175], v[80:83]\n"
        "s_barrier\n"
        "s_add_u32 m0, s40, 0\n"
        "ds_read_b64_tr_b16 a[88:89], v131 offset:24576\n"
        "v_mfma_f32_16x16x32_bf16 v[84:87], a[132:135], a[172:175], v[84:87]\n"
        "s_add_u32 s64, s65, 74496\n"
        "ds_read_b64_tr_b16 a[90:91], v131 offset:26112\n"
        "v_mul_f32 v172, s50, v172\n"
        "v_mfma_f32_16x16x32_bf16 v[84:87], a[128:131], a[168:171], v[84:87]\n"
        "ds_read_b64_tr_b16 a[92:93], v131 offset:36864\n"
        "v_mul_f32 v173, s50, v173\n"
        "v_mfma_f32_16x16x32_bf16 v[88:91], a[80:83], a[184:187], v[88:91]\n"
        "buffer_load_dwordx4 v128, s[20:23], s64 offen lds\n"
        "v_mfma_f32_16x16x32_bf16 v[88:91], a[84:87], a[188:191], v[88:91]\n"
        "ds_read_b64_tr_b16 a[94:95], v131 offset:38400\n"
        "ds_read_b64_tr_b16 a[112:113], v132 offset:24576\n"
        "v_mfma_f32_16x16x32_bf16 v[108:111], a[108:111], a[188:191], v[108:111]\n"
        "ds_read_b64_tr_b16 a[114:115], v132 offset:26112\n"
        "v_mov_b64 v[206:207], v[200:201]\n"
        "v_mfma_f32_16x16x32_bf16 v[108:111], a[104:107], a[184:187], v[108:111]\n"
        "ds_read_b64_tr_b16 a[116:117], v132 offset:36864\n"
        "v_mul_f32 v174, s50, v174\n"
        "v_mfma_f32_16x16x32_bf16 v[112:115], a[128:131], a[184:187], v[112:115]\n"
        "buffer_load_dwordx4 v129, s[20:23], s64 offen offset:1024 lds\n"
        "v_mfma_f32_16x16x32_bf16 v[112:115], a[132:135], a[188:191], v[112:115]\n"
        "ds_read_b64_tr_b16 a[118:119], v132 offset:38400\n"
        "ds_read_b64_tr_b16 a[136:137], v133 offset:24576\n"
        "v_mfma_f32_16x16x32_bf16 v[96:99], a[84:87], a[204:207], v[96:99]\n"
        "ds_read_b64_tr_b16 a[138:139], v133 offset:26112\n"
        "v_mul_f32 v175, s50, v175\n"
        "v_mfma_f32_16x16x32_bf16 v[96:99], a[80:83], a[200:203], v[96:99]\n"
        "ds_read_b64_tr_b16 a[140:141], v133 offset:36864\n"
        "v_cvt_pk_bf16_f32 v216, v172, v173\n"
        "v_mfma_f32_16x16x32_bf16 v[100:103], a[104:107], a[200:203], v[100:103]\n"
        "buffer_load_dwordx4 v130, s[20:23], s64 offen offset:2048 lds\n"
        "v_mfma_f32_16x16x32_bf16 v[100:103], a[108:111], a[204:207], v[100:103]\n"
        "s_add_u32 m0, s40, 12288\n"
        "ds_read_b64_tr_b16 a[142:143], v133 offset:38400\n"
        "v_cvt_pk_bf16_f32 v217, v174, v175\n"
        "v_mfma_f32_16x16x32_bf16 v[104:107], a[132:135], a[204:207], v[104:107]\n"
        "s_add_u32 s64, s65, 111360\n"
        "v_mul_f32 v192, s50, v192\n"
        "v_mul_f32 v193, s50, v193\n"
        "v_mfma_f32_16x16x32_bf16 v[104:107], a[128:131], a[200:203], v[104:107]\n"
        "s_waitcnt lgkmcnt(10)\n"
        "buffer_load_dwordx4 v128, s[20:23], s64 offen lds\n"
        "v_mfma_f32_16x16x32_bf16 v[64:67], a[88:91], a[216:219], v[64:67]\n"
        "s_waitcnt lgkmcnt(8)\n"
        "v_mul_f32 v194, s50, v194\n"
        "v_mul_f32 v195, s50, v195\n"
        "v_mfma_f32_16x16x32_bf16 v[64:67], a[92:95], a[220:223], v[64:67]\n"
        "s_waitcnt lgkmcnt(4)\n"
        "v_cvt_pk_bf16_f32 v218, v192, v193\n"
        "v_cvt_pk_bf16_f32 v219, v194, v195\n"
        "v_mfma_f32_16x16x32_bf16 v[68:71], a[116:119], a[220:223], v[68:71]\n"
        "v_mul_f32 v212, s50, v212\n"
        "v_mul_f32 v213, s50, v213\n"
        "v_mfma_f32_16x16x32_bf16 v[68:71], a[112:115], a[216:219], v[68:71]\n"
        "s_waitcnt lgkmcnt(2)\n"
        "buffer_load_dwordx4 v129, s[20:23], s64 offen offset:1024 lds\n"
        "v_mfma_f32_16x16x32_bf16 v[72:75], a[136:139], a[216:219], v[72:75]\n"
        "s_waitcnt lgkmcnt(0)\n"
        "v_mul_f32 v214, s50, v214\n"
        "v_mul_f32 v215, s50, v215\n"
        "v_mfma_f32_16x16x32_bf16 v[72:75], a[140:143], a[220:223], v[72:75]\n"
        "v_cvt_pk_bf16_f32 v220, v212, v213\n"
        "v_cvt_pk_bf16_f32 v221, v214, v215\n"
        "v_mfma_f32_16x16x32_bf16 v[76:79], a[92:95], a[228:231], v[76:79]\n"
        "v_mov_b64 v[226:227], v[220:221]\n"
        "v_cvt_pk_bf16_f32 v152, v64, v65\n"
        "v_mfma_f32_16x16x32_bf16 v[76:79], a[88:91], a[224:227], v[76:79]\n"
        "buffer_load_dwordx4 v130, s[20:23], s64 offen offset:2048 lds\n"
        "v_mfma_f32_16x16x32_bf16 v[80:83], a[112:115], a[224:227], v[80:83]\n"
        "v_cvt_pk_bf16_f32 v153, v66, v67\n"
        "v_cvt_pk_bf16_f32 v154, v68, v69\n"
        "v_mfma_f32_16x16x32_bf16 v[80:83], a[116:119], a[228:231], v[80:83]\n"
        "v_cvt_pk_bf16_f32 v155, v70, v71\n"
        "v_cvt_pk_bf16_f32 v168, v72, v73\n"
        "v_mfma_f32_16x16x32_bf16 v[84:87], a[140:143], a[228:231], v[84:87]\n"
        "v_cvt_pk_bf16_f32 v169, v74, v75\n"
        "v_cvt_pk_bf16_f32 v172, v76, v77\n"
        "v_mfma_f32_16x16x32_bf16 v[84:87], a[136:139], a[224:227], v[84:87]\n"
        "s_waitcnt vmcnt(6)\n"
        "ds_read_b128 v[162:165], v251 offset:0\n"
        "v_cvt_pk_bf16_f32 v173, v78, v79\n"
        "v_mfma_f32_16x16x32_bf16 v[88:91], a[88:91], a[232:235], v[88:91]\n"
        "s_barrier\n"
        "s_add_u32 m0, s40, 24576\n"
        "ds_read_b64_tr_b16 a[144:145], v131 offset:49152\n"
        "v_mfma_f32_16x16x32_bf16 v[88:91], a[92:95], a[236:239], v[88:91]\n"
        "s_add_u32 s64, s65, 148224\n"
        "ds_read_b64_tr_b16 a[146:147], v131 offset:50688\n"
        "v_cvt_pk_bf16_f32 v174, v80, v81\n"
        "v_mfma_f32_16x16x32_bf16 v[108:111], a[116:119], a[236:239], v[108:111]\n"
        "ds_read_b64_tr_b16 a[148:149], v131 offset:61440\n"
        "v_cvt_pk_bf16_f32 v175, v82, v83\n"
        "v_mfma_f32_16x16x32_bf16 v[108:111], a[112:115], a[232:235], v[108:111]\n"
        "buffer_load_dwordx4 v128, s[20:23], s64 offen lds\n"
        "v_mfma_f32_16x16x32_bf16 v[112:115], a[136:139], a[232:235], v[112:115]\n"
        "ds_read_b64_tr_b16 a[150:151], v131 offset:62976\n"
        "ds_read_b64_tr_b16 a[168:169], v132 offset:49152\n"
        "v_mfma_f32_16x16x32_bf16 v[112:115], a[140:143], a[236:239], v[112:115]\n"
        "ds_read_b64_tr_b16 a[170:171], v132 offset:50688\n"
        "v_cvt_pk_bf16_f32 v190, v84, v85\n"
        "v_mfma_f32_16x16x32_bf16 v[96:99], a[92:95], a[244:247], v[96:99]\n"
        "ds_read_b64_tr_b16 a[172:173], v132 offset:61440\n"
        "ds_read_b128 v[182:185], v251 offset:512\n"
        "v_mfma_f32_16x16x32_bf16 v[96:99], a[88:91], a[240:243], v[96:99]\n"
        "buffer_load_dwordx4 v129, s[20:23], s64 offen offset:1024 lds\n"
        "v_mfma_f32_16x16x32_bf16 v[100:103], a[112:115], a[240:243], v[100:103]\n"
        "ds_read_b64_tr_b16 a[174:175], v132 offset:62976\n"
        "ds_read_b64_tr_b16 a[192:193], v133 offset:49152\n"
        "v_mfma_f32_16x16x32_bf16 v[100:103], a[116:119], a[244:247], v[100:103]\n"
        "ds_read_b64_tr_b16 a[194:195], v133 offset:50688\n"
        "v_cvt_pk_bf16_f32 v191, v86, v87\n"
        "v_mfma_f32_16x16x32_bf16 v[104:107], a[140:143], a[244:247], v[104:107]\n"
        "ds_read_b64_tr_b16 a[196:197], v133 offset:61440\n"
        "ds_read_b128 v[202:205], v251 offset:4096\n"
        "v_mfma_f32_16x16x32_bf16 v[104:107], a[136:139], a[240:243], v[104:107]\n"
        "buffer_load_dwordx4 v130, s[20:23], s64 offen offset:2048 lds\n"
        "v_mfma_f32_16x16x32_bf16 v[64:67], v[152:155], v[156:159], v[0:3]\n"
        "s_waitcnt lgkmcnt(13)\n"
        "s_add_u32 m0, s40, 36864\n"
        "ds_read_b64_tr_b16 a[198:199], v133 offset:62976\n"
        "v_cvt_pk_bf16_f32 v192, v88, v89\n"
        "v_mfma_f32_16x16x32_bf16 v[64:67], v[168:171], v[160:163], v[64:67]\n"
        "s_add_u32 s64, s65, 185088\n"
        "v_cvt_pk_bf16_f32 v193, v90, v91\n"
        "v_cvt_pk_bf16_f32 v194, v108, v109\n"
        "v_mfma_f32_16x16x32_bf16 v[68:71], v[172:175], v[156:159], v[4:7]\n"
        "buffer_load_dwordx4 v128, s[20:23], s64 offen lds\n"
        "v_mfma_f32_16x16x32_bf16 v[68:71], v[188:191], v[164:167], v[68:71]\n"
        "ds_read_b128 v[222:225], v251 offset:4608\n"
        "v_cvt_pk_bf16_f32 v195, v110, v111\n"
        "v_mfma_f32_16x16x32_bf16 v[72:75], v[152:155], v[176:179], v[16:19]\n"
        "s_waitcnt lgkmcnt(7)\n"
        "v_cvt_pk_bf16_f32 v208, v112, v113\n"
        "v_cvt_pk_bf16_f32 v209, v114, v115\n"
        "v_mfma_f32_16x16x32_bf16 v[72:75], v[168:171], v[180:183], v[72:75]\n"
        "v_cvt_pk_bf16_f32 v212, v96, v97\n"
        "v_cvt_pk_bf16_f32 v213, v98, v99\n"
        "v_mfma_f32_16x16x32_bf16 v[76:79], v[172:175], v[176:179], v[20:23]\n"
        "buffer_load_dwordx4 v129, s[20:23], s64 offen offset:1024 lds\n"
        "v_mfma_f32_16x16x32_bf16 v[76:79], v[188:191], v[184:187], v[76:79]\n"
        "ds_read_b128 v[162:165], v252 offset:0\n"
        "v_cvt_pk_bf16_f32 v214, v100, v101\n"
        "v_mfma_f32_16x16x32_bf16 v[80:83], v[152:155], v[196:199], v[32:35]\n"
        "s_waitcnt lgkmcnt(3)\n"
        "ds_read_b128 v[182:185], v252 offset:512\n"
        "v_cvt_pk_bf16_f32 v215, v102, v103\n"
        "v_mfma_f32_16x16x32_bf16 v[80:83], v[168:171], v[200:203], v[80:83]\n"
        "41:\n"
        "s_cbranch_execz 44f\n"
        "v_cvt_pk_bf16_f32 v230, v104, v105\n"
        "v_cvt_pk_bf16_f32 v231, v106, v107\n"
        "v_mfma_f32_16x16x32_bf16 v[84:87], v[172:175], v[196:199], v[36:39]\n"
        "buffer_load_dwordx4 v130, s[20:23], s64 offen offset:2048 lds\n"
        "v_mfma_f32_16x16x32_bf16 v[84:87], v[188:191], v[204:207], v[84:87]\n"
        "s_add_u32 m0, s40, 98304\n"
        "ds_read_b128 v[202:205], v252 offset:4096\n"
        "v_mfma_f32_16x16x32_bf16 v[88:91], v[152:155], v[216:219], v[48:51]\n"
        "s_waitcnt vmcnt(6) lgkmcnt(3)\n"
        "s_barrier\n"
        "ds_read_b64_tr_b16 a[152:153], v131 offset:0\n"
        "ds_read_b128 a[248:251], v240 offset:0\n"
        "v_mfma_f32_16x16x32_bf16 v[88:91], v[168:171], v[220:223], v[88:91]\n"
        "ds_read_b64_tr_b16 a[154:155], v131 offset:1536\n"
        "ds_read_b128 a[216:219], v241 offset:0\n"
        "v_mfma_f32_16x16x32_bf16 v[92:95], v[172:175], v[216:219], v[52:55]\n"
        "ds_read_b64_tr_b16 a[156:157], v131 offset:12288\n"
        "ds_read_b128 a[220:223], v240 offset:128\n"
        "v_mfma_f32_16x16x32_bf16 v[92:95], v[188:191], v[224:227], v[92:95]\n"
        "buffer_load_dword v148, s[52:55], 0 offen lds\n"
        "v_mfma_f32_16x16x32_bf16 v[108:111], v[212:215], v[156:159], v[12:15]\n"
        "s_waitcnt lgkmcnt(8)\n"
        "s_add_u32 m0, s40, 98304\n"
        "s_sub_u32 s64, s47, 6291456\n"
        "ds_read_b64_tr_b16 a[158:159], v131 offset:13824\n"
        "v_mfma_f32_16x16x32_bf16 v[108:111], v[228:231], v[164:167], v[108:111]\n"
        "ds_read_b128 a[224:227], v241 offset:128\n"
        "ds_read_b64_tr_b16 a[176:177], v132 offset:0\n"
        "v_mfma_f32_16x16x32_bf16 v[112:115], v[212:215], v[176:179], v[28:31]\n"
        "s_waitcnt lgkmcnt(10)\n"
        "buffer_load_dwordx4 v242, s[8:11], s64 offen lds\n"
        "v_mfma_f32_16x16x32_bf16 v[112:115], v[228:231], v[184:187], v[112:115]\n"
        "s_add_u32 m0, s40, 99328\n"
        "s_sub_u32 s64, s47, 6291456\n"
        "ds_read_b64_tr_b16 a[178:179], v132 offset:1536\n"
        "v_mfma_f32_16x16x32_bf16 v[116:119], v[212:215], v[196:199], v[44:47]\n"
        "s_waitcnt lgkmcnt(10)\n"
        "ds_read_b64_tr_b16 a[180:181], v132 offset:12288\n"
        "ds_read_b64_tr_b16 a[182:183], v132 offset:13824\n"
        "v_mfma_f32_16x16x32_bf16 v[116:119], v[228:231], v[204:207], v[116:119]\n"
        "s_waitcnt lgkmcnt(10)\n"
        "ds_read_b64_tr_b16 a[200:201], v133 offset:0\n"
        "ds_read_b64_tr_b16 a[202:203], v133 offset:1536\n"
        "v_mfma_f32_16x16x32_bf16 v[120:123], a[248:251], a[144:147], 0\n"
        "s_waitcnt lgkmcnt(10)\n"
        "buffer_load_dwordx4 v243, s[8:11], s64 offen lds\n"
        "v_mfma_f32_16x16x32_bf16 v[120:123], a[216:219], a[148:151], v[120:123]\n"
        "s_waitcnt lgkmcnt(8)\n"
        "s_add_u32 m0, s40, 100352\n"
        "s_sub_u32 s64, s47, 6291456\n"
        "ds_read_b64_tr_b16 a[204:205], v133 offset:12288\n"
        "v_mfma_f32_16x16x32_bf16 v[120:123], a[220:223], a[152:155], v[120:123]\n"
        "s_waitcnt lgkmcnt(7)\n"
        "ds_read_b64_tr_b16 a[206:207], v133 offset:13824\n"
        "v_mfma_f32_16x16x32_bf16 v[120:123], a[224:227], a[156:159], v[120:123]\n"
        "s_waitcnt lgkmcnt(4)\n"
        "buffer_load_dwordx4 v244, s[8:11], s64 offen lds\n"
        "v_mfma_f32_16x16x32_bf16 v[96:99], a[224:227], a[180:183], 0\n"
        "s_add_u32 m0, s40, 110592\n"
        "ds_read_b128 a[228:231], v240 offset:1536\n"
        "v_mfma_f32_16x16x32_bf16 v[96:99], a[220:223], a[176:179], v[96:99]\n"
        "s_sub_u32 s64, s47, 5505024\n"
        "ds_read_b128 a[232:235], v241 offset:1536\n"
        "v_mfma_f32_16x16x32_bf16 v[96:99], a[216:219], a[172:175], v[96:99]\n"
        "buffer_load_dwordx4 v242, s[8:11], s64 offen lds\n"
        "v_mfma_f32_16x16x32_bf16 v[96:99], a[248:251], a[168:171], v[96:99]\n"
        "s_add_u32 m0, s40, 111616\n"
        "ds_read_b128 a[236:239], v240 offset:1664\n"
        "ds_read_b128 a[240:243], v241 offset:1664\n"
        "v_mfma_f32_16x16x32_bf16 v[100:103], a[248:251], a[192:195], 0\n"
        "s_sub_u32 s64, s47, 5505024\n"
        "v_exp_f32 v64, v64\n"
        "v_mfma_f32_16x16x32_bf16 v[100:103], a[216:219], a[196:199], v[100:103]\n"
        "s_waitcnt lgkmcnt(6)\n"
        "v_exp_f32 v65, v65\n"
        "v_mfma_f32_16x16x32_bf16 v[100:103], a[220:223], a[200:203], v[100:103]\n"
        "s_waitcnt lgkmcnt(4)\n"
        "buffer_load_dwordx4 v243, s[8:11], s64 offen lds\n"
        "v_mfma_f32_16x16x32_bf16 v[100:103], a[224:227], a[204:207], v[100:103]\n"
        "s_add_u32 m0, s40, 112640\n"
        "v_exp_f32 v66, v66\n"
        "v_exp_f32 v68, v68\n"
        "v_cvt_pk_bf16_f32 v64, v64, v65\n"
        "v_exp_f32 v67, v67\n"
        "v_exp_f32 v69, v69\n"
        "v_cvt_pk_bf16_f32 v65, v66, v67\n"
        "v_exp_f32 v70, v70\n"
        "v_cvt_pk_bf16_f32 v66, v68, v69\n"
        "v_exp_f32 v71, v71\n"
        "v_exp_f32 v72, v72\n"
        "v_cvt_pk_bf16_f32 v67, v70, v71\n"
        "v_mfma_f32_16x16x32_bf16 v[68:71], v[192:195], v[156:159], v[8:11]\n"
        "s_sub_u32 s64, s47, 5505024\n"
        "v_exp_f32 v73, v73\n"
        "v_mfma_f32_16x16x32_bf16 v[68:71], v[208:211], v[160:163], v[68:71]\n"
        "s_waitcnt lgkmcnt(0)\n"
        "v_exp_f32 v74, v74\n"
        "v_mfma_f32_16x16x32_bf16 v[104:107], a[240:243], a[156:159], 0\n"
        "buffer_load_dwordx4 v244, s[8:11], s64 offen lds\n"
        "v_mfma_f32_16x16x32_bf16 v[104:107], a[236:239], a[152:155], v[104:107]\n"
        "s_xor_b32 m0, s48, 8192\n"
        "ds_read_b128 a[244:247], v240 offset:12288\n"
        "v_cvt_pk_bf16_f32 v72, v72, v73\n"
        "v_mfma_f32_16x16x32_bf16 v[104:107], a[232:235], a[148:151], v[104:107]\n"
        "s_sub_u32 s64, s36, 2097152\n"
        "v_exp_f32 v75, v75\n"
        "v_mfma_f32_16x16x32_bf16 v[104:107], a[228:231], a[144:147], v[104:107]\n"
        "s_waitcnt vmcnt(7)\n"
        "ds_read_b128 a[248:251], v241 offset:12288\n"
        "v_cvt_pk_bf16_f32 v73, v74, v75\n"
        "v_mfma_f32_16x16x32_bf16 v[156:159], a[228:231], a[168:171], 0\n"
        "s_barrier\n"
        "ds_read_b64_tr_b16 a[160:161], v131 offset:24576\n"
        "v_mfma_f32_16x16x32_bf16 v[156:159], a[232:235], a[172:175], v[156:159]\n"
        "buffer_load_dwordx4 v249, s[12:15], s64 offen lds\n"
        "v_mfma_f32_16x16x32_bf16 v[156:159], a[236:239], a[176:179], v[156:159]\n"
        "ds_read_b64_tr_b16 a[162:163], v131 offset:26112\n"
        "ds_read_b64_tr_b16 a[164:165], v131 offset:36864\n"
        "v_mfma_f32_16x16x32_bf16 v[156:159], a[240:243], a[180:183], v[156:159]\n"
        "ds_read_b128 a[216:219], v240 offset:12416\n"
        "ds_read_b64_tr_b16 a[166:167], v131 offset:38400\n"
        "v_mfma_f32_16x16x32_bf16 v[160:163], a[240:243], a[204:207], 0\n"
        "ds_read_b128 a[220:223], v241 offset:12416\n"
        "ds_read_b64_tr_b16 a[184:185], v132 offset:24576\n"
        "v_mfma_f32_16x16x32_bf16 v[160:163], a[236:239], a[200:203], v[160:163]\n"
        "buffer_load_dwordx4 v149, s[12:15], s64 offen offset:1024 lds\n"
        "v_mfma_f32_16x16x32_bf16 v[160:163], a[232:235], a[196:199], v[160:163]\n"
        "ds_read_b64_tr_b16 a[186:187], v132 offset:26112\n"
        "ds_read_b64_tr_b16 a[188:189], v132 offset:36864\n"
        "v_mfma_f32_16x16x32_bf16 v[160:163], a[228:231], a[192:195], v[160:163]\n"
        "ds_read_b64_tr_b16 a[190:191], v132 offset:38400\n"
        "v_exp_f32 v76, v76\n"
        "v_exp_f32 v77, v77\n"
        "v_exp_f32 v78, v78\n"
        "v_cvt_pk_bf16_f32 v74, v76, v77\n"
        "v_exp_f32 v79, v79\n"
        "v_exp_f32 v80, v80\n"
        "v_cvt_pk_bf16_f32 v75, v78, v79\n"
        "v_mfma_f32_16x16x32_bf16 v[76:79], v[192:195], v[176:179], v[24:27]\n"
        "ds_read_b64_tr_b16 a[208:209], v133 offset:24576\n"
        "v_mfma_f32_16x16x32_bf16 v[76:79], v[208:211], v[180:183], v[76:79]\n"
        "s_waitcnt lgkmcnt(12)\n"
        "ds_read_b64_tr_b16 a[210:211], v133 offset:26112\n"
        "v_mfma_f32_16x16x32_bf16 v[164:167], a[244:247], a[144:147], 0\n"
        "s_waitcnt lgkmcnt(12)\n"
        "ds_read_b64_tr_b16 a[212:213], v133 offset:36864\n"
        "v_mfma_f32_16x16x32_bf16 v[164:167], a[248:251], a[148:151], v[164:167]\n"
        "s_waitcnt lgkmcnt(9)\n"
        "ds_read_b64_tr_b16 a[214:215], v133 offset:38400\n"
        "v_mfma_f32_16x16x32_bf16 v[164:167], a[216:219], a[152:155], v[164:167]\n"
        "s_waitcnt lgkmcnt(8)\n"
        "v_exp_f32 v81, v81\n"
        "v_mfma_f32_16x16x32_bf16 v[164:167], a[220:223], a[156:159], v[164:167]\n"
        "v_exp_f32 v82, v82\n"
        "v_mfma_f32_16x16x32_bf16 v[176:179], a[220:223], a[180:183], 0\n"
        "ds_read_b128 a[224:227], v240 offset:13824\n"
        "v_cvt_pk_bf16_f32 v80, v80, v81\n"
        "v_mfma_f32_16x16x32_bf16 v[176:179], a[216:219], a[176:179], v[176:179]\n"
        "ds_read_b128 a[228:231], v241 offset:13824\n"
        "v_mfma_f32_16x16x32_bf16 v[176:179], a[248:251], a[172:175], v[176:179]\n"
        "ds_read_b128 a[232:235], v240 offset:13952\n"
        "v_mfma_f32_16x16x32_bf16 v[176:179], a[244:247], a[168:171], v[176:179]\n"
        "ds_read_b128 a[236:239], v241 offset:13952\n"
        "v_mfma_f32_16x16x32_bf16 v[180:183], a[244:247], a[192:195], 0\n"
        "v_exp_f32 v83, v83\n"
        "v_mfma_f32_16x16x32_bf16 v[180:183], a[248:251], a[196:199], v[180:183]\n"
        "ds_read_b128 v[222:225], v252 offset:4608\n"
        "v_cvt_pk_bf16_f32 v81, v82, v83\n"
        "v_mfma_f32_16x16x32_bf16 v[180:183], a[216:219], a[200:203], v[180:183]\n"
        "v_exp_f32 v84, v84\n"
        "v_mfma_f32_16x16x32_bf16 v[180:183], a[220:223], a[204:207], v[180:183]\n"
        "v_exp_f32 v85, v85\n"
        "v_exp_f32 v86, v86\n"
        "v_cvt_pk_bf16_f32 v82, v84, v85\n"
        "v_exp_f32 v87, v87\n"
        "v_exp_f32 v88, v88\n"
        "v_cvt_pk_bf16_f32 v83, v86, v87\n"
        "v_mfma_f32_16x16x32_bf16 v[84:87], v[192:195], v[196:199], v[40:43]\n"
        "v_exp_f32 v89, v89\n"
        "v_mfma_f32_16x16x32_bf16 v[84:87], v[208:211], v[200:203], v[84:87]\n"
        "s_waitcnt lgkmcnt(1)\n"
        "v_exp_f32 v90, v90\n"
        "v_mfma_f32_16x16x32_bf16 v[184:187], a[236:239], a[156:159], 0\n"
        "ds_read_b128 a[240:243], v240 offset:256\n"
        "v_cvt_pk_bf16_f32 v88, v88, v89\n"
        "v_mfma_f32_16x16x32_bf16 v[184:187], a[232:235], a[152:155], v[184:187]\n"
        "v_exp_f32 v91, v91\n"
        "v_mfma_f32_16x16x32_bf16 v[184:187], a[228:231], a[148:151], v[184:187]\n"
        "ds_read_b128 a[244:247], v241 offset:256\n"
        "v_cvt_pk_bf16_f32 v89, v90, v91\n"
        "v_mfma_f32_16x16x32_bf16 v[184:187], a[224:227], a[144:147], v[184:187]\n"
        "v_exp_f32 v92, v92\n"
        "v_mfma_f32_16x16x32_bf16 v[196:199], a[224:227], a[168:171], 0\n"
        "v_exp_f32 v93, v93\n"
        "v_mfma_f32_16x16x32_bf16 v[196:199], a[228:231], a[172:175], v[196:199]\n"
        "v_exp_f32 v94, v94\n"
        "v_mfma_f32_16x16x32_bf16 v[196:199], a[232:235], a[176:179], v[196:199]\n"
        "ds_read_b128 a[248:251], v240 offset:1792\n"
        "v_cvt_pk_bf16_f32 v90, v92, v93\n"
        "v_mfma_f32_16x16x32_bf16 v[196:199], a[236:239], a[180:183], v[196:199]\n"
        "v_exp_f32 v95, v95\n"
        "v_mfma_f32_16x16x32_bf16 v[200:203], a[236:239], a[204:207], 0\n"
        "ds_read_b128 a[216:219], v241 offset:1792\n"
        "v_cvt_pk_bf16_f32 v91, v94, v95\n"
        "v_mfma_f32_16x16x32_bf16 v[200:203], a[232:235], a[200:203], v[200:203]\n"
        "v_exp_f32 v108, v108\n"
        "v_mfma_f32_16x16x32_bf16 v[200:203], a[228:231], a[196:199], v[200:203]\n"
        "v_exp_f32 v109, v109\n"
        "v_mfma_f32_16x16x32_bf16 v[200:203], a[224:227], a[192:195], v[200:203]\n"
        "v_exp_f32 v110, v110\n"
        "v_exp_f32 v111, v111\n"
        "v_exp_f32 v70, v70\n"
        "v_exp_f32 v69, v69\n"
        "v_exp_f32 v68, v68\n"
        "v_exp_f32 v71, v71\n"
        "v_cvt_pk_bf16_f32 v68, v68, v69\n"
        "v_cvt_pk_bf16_f32 v69, v70, v71\n"
        "v_cvt_pk_bf16_f32 v70, v108, v109\n"
        "v_cvt_pk_bf16_f32 v71, v110, v111\n"
        "v_mfma_f32_16x16x32_bf16 v[108:111], v[192:195], v[216:219], v[56:59]\n"
        "s_waitcnt lgkmcnt(4)\n"
        "v_exp_f32 v112, v112\n"
        "v_mfma_f32_16x16x32_bf16 v[108:111], v[208:211], v[220:223], v[108:111]\n"
        "v_exp_f32 v113, v113\n"
        "v_exp_f32 v114, v114\n"
        "v_exp_f32 v115, v115\n"
        "v_exp_f32 v78, v78\n"
        "v_exp_f32 v77, v77\n"
        "v_exp_f32 v76, v76\n"
        "v_exp_f32 v79, v79\n"
        "v_cvt_pk_bf16_f32 v76, v76, v77\n"
        "v_cvt_pk_bf16_f32 v77, v78, v79\n"
        "v_cvt_pk_bf16_f32 v78, v112, v113\n"
        "v_cvt_pk_bf16_f32 v79, v114, v115\n"
        "v_mfma_f32_16x16x32_bf16 v[112:115], v[212:215], v[216:219], v[60:63]\n"
        "v_exp_f32 v116, v116\n"
        "v_mfma_f32_16x16x32_bf16 v[112:115], v[228:231], v[224:227], v[112:115]\n"
        "s_waitcnt lgkmcnt(3)\n"
        "v_exp_f32 v117, v117\n"
        "v_mfma_f32_16x16x32_bf16 v[120:123], a[240:243], a[160:163], v[120:123]\n"
        "s_waitcnt lgkmcnt(2)\n"
        "v_exp_f32 v118, v118\n"
        "v_mfma_f32_16x16x32_bf16 v[120:123], a[244:247], a[164:167], v[120:123]\n"
        "v_exp_f32 v119, v119\n"
        "v_mfma_f32_16x16x32_bf16 v[96:99], a[244:247], a[188:191], v[96:99]\n"
        "ds_read_b128 a[220:223], v240 offset:12544\n"
        "v_mfma_f32_16x16x32_bf16 v[96:99], a[240:243], a[184:187], v[96:99]\n"
        "ds_read_b128 a[224:227], v241 offset:12544\n"
        "v_mfma_f32_16x16x32_bf16 v[100:103], a[240:243], a[208:211], v[100:103]\n"
        "v_exp_f32 v84, v84\n"
        "v_mfma_f32_16x16x32_bf16 v[100:103], a[244:247], a[212:215], v[100:103]\n"
        "s_waitcnt lgkmcnt(2)\n"
        "v_exp_f32 v85, v85\n"
        "v_mfma_f32_16x16x32_bf16 v[104:107], a[216:219], a[164:167], v[104:107]\n"
        "v_exp_f32 v86, v86\n"
        "v_mfma_f32_16x16x32_bf16 v[104:107], a[248:251], a[160:163], v[104:107]\n"
        "v_cvt_pk_bf16_f32 v84, v84, v85\n"
        "v_cvt_pk_bf16_f32 v128, v120, v121\n"
        "v_mfma_f32_16x16x32_bf16 v[156:159], a[248:251], a[184:187], v[156:159]\n"
        "ds_read_b128 a[228:231], v240 offset:14080\n"
        "v_cvt_pk_bf16_f32 v129, v122, v123\n"
        "v_mfma_f32_16x16x32_bf16 v[156:159], a[216:219], a[188:191], v[156:159]\n"
        "ds_read_b128 a[232:235], v241 offset:14080\n"
        "v_cvt_pk_bf16_f32 v136, v96, v97\n"
        "v_mfma_f32_16x16x32_bf16 v[160:163], a[216:219], a[212:215], v[160:163]\n"
        "v_exp_f32 v87, v87\n"
        "v_mfma_f32_16x16x32_bf16 v[160:163], a[248:251], a[208:211], v[160:163]\n"
        "s_waitcnt lgkmcnt(3)\n"
        "v_cvt_pk_bf16_f32 v85, v86, v87\n"
        "v_cvt_pk_bf16_f32 v86, v116, v117\n"
        "v_mfma_f32_16x16x32_bf16 v[164:167], a[220:223], a[160:163], v[164:167]\n"
        "s_waitcnt lgkmcnt(2)\n"
        "v_cvt_pk_bf16_f32 v87, v118, v119\n"
        "v_cvt_pk_bf16_f32 v137, v98, v99\n"
        "v_mfma_f32_16x16x32_bf16 v[164:167], a[224:227], a[164:167], v[164:167]\n"
        "v_exp_f32 v108, v108\n"
        "v_mfma_f32_16x16x32_bf16 v[176:179], a[224:227], a[188:191], v[176:179]\n"
        "v_exp_f32 v109, v109\n"
        "v_mfma_f32_16x16x32_bf16 v[176:179], a[220:223], a[184:187], v[176:179]\n"
        "v_exp_f32 v110, v110\n"
        "v_mfma_f32_16x16x32_bf16 v[180:183], a[220:223], a[208:211], v[180:183]\n"
        "v_cvt_pk_bf16_f32 v92, v108, v109\n"
        "v_cvt_pk_bf16_f32 v144, v100, v101\n"
        "v_mfma_f32_16x16x32_bf16 v[180:183], a[224:227], a[212:215], v[180:183]\n"
        "s_waitcnt lgkmcnt(0)\n"
        "v_exp_f32 v111, v111\n"
        "v_mfma_f32_16x16x32_bf16 v[184:187], a[232:235], a[164:167], v[184:187]\n"
        "v_cvt_pk_bf16_f32 v93, v110, v111\n"
        "v_cvt_pk_bf16_f32 v145, v102, v103\n"
        "v_mfma_f32_16x16x32_bf16 v[184:187], a[228:231], a[160:163], v[184:187]\n"
        "v_exp_f32 v112, v112\n"
        "v_mfma_f32_16x16x32_bf16 v[196:199], a[228:231], a[184:187], v[196:199]\n"
        "v_exp_f32 v113, v113\n"
        "v_mfma_f32_16x16x32_bf16 v[196:199], a[232:235], a[188:191], v[196:199]\n"
        "v_exp_f32 v114, v114\n"
        "v_mfma_f32_16x16x32_bf16 v[200:203], a[232:235], a[212:215], v[200:203]\n"
        "v_cvt_pk_bf16_f32 v94, v112, v113\n"
        "v_cvt_pk_bf16_f32 v130, v104, v105\n"
        "v_mfma_f32_16x16x32_bf16 v[200:203], a[228:231], a[208:211], v[200:203]\n"
        "v_exp_f32 v115, v115\n"
        "v_mov_b64 v[120:121], 0\n"
        "s_nop 1\n"
        "v_smfmac_f32_16x16x64_bf16 v[120:123], a[252:255], v[64:71], v255\n"
        "v_cvt_pk_bf16_f32 v95, v114, v115\n"
        "v_cvt_pk_bf16_f32 v131, v106, v107\n"
        "s_nop 1\n"
        "v_mfma_f32_16x16x32_bf16 v[116:119], v[128:131], v[64:67], 0\n"
        "v_cvt_pk_bf16_f32 v138, v156, v157\n"
        "v_cvt_pk_bf16_f32 v139, v158, v159\n"
        "v_cvt_pk_bf16_f32 v132, v164, v165\n"
        "v_cvt_pk_bf16_f32 v133, v166, v167\n"
        "v_cvt_pk_bf16_f32 v134, v184, v185\n"
        "v_cvt_pk_bf16_f32 v135, v186, v187\n"
        "s_nop 1\n"
        "v_mfma_f32_16x16x32_bf16 v[116:119], v[132:135], v[68:71], v[116:119]\n"
        "v_cvt_pk_bf16_f32 v146, v160, v161\n"
        "v_cvt_pk_bf16_f32 v147, v162, v163\n"
        "v_cvt_pk_bf16_f32 v140, v176, v177\n"
        "v_cvt_pk_bf16_f32 v141, v178, v179\n"
        "v_cvt_pk_bf16_f32 v142, v196, v197\n"
        "v_cvt_pk_bf16_f32 v143, v198, v199\n"
        "s_nop 1\n"
        "v_mfma_f32_16x16x32_bf16 v[152:155], v[140:143], v[68:71], 0\n"
        "v_mov_b64 v[172:173], 0\n"
        "v_cvt_pk_bf16_f32 v148, v180, v181\n"
        "v_mfma_f32_16x16x32_bf16 v[152:155], v[136:139], v[64:67], v[152:155]\n"
        "v_cvt_pk_bf16_f32 v149, v182, v183\n"
        "v_cvt_pk_bf16_f32 v150, v200, v201\n"
        "v_mfma_f32_16x16x32_bf16 v[160:163], v[144:147], v[64:67], 0\n"
        "v_cvt_pk_bf16_f32 v151, v202, v203\n"
        "v_mov_b64 v[192:193], 0\n"
        "s_nop 0\n"
        "v_mfma_f32_16x16x32_bf16 v[160:163], v[148:151], v[68:71], v[160:163]\n"
        "v_add_f32 v120, v120, v121\n"
        "v_add_u32 v240, 24576, v240\n"
        "v_smfmac_f32_16x16x64_bf16 v[172:175], a[252:255], v[72:79], v255\n"
        "v_add_u32 v241, 24576, v241\n"
        "v_mfma_f32_16x16x32_bf16 v[176:179], v[128:131], v[72:75], 0\n"
        "v_rcp_f32 v121, v120\n"
        "v_mfma_f32_16x16x32_bf16 v[176:179], v[132:135], v[76:79], v[176:179]\n"
        "v_mov_b64 v[212:213], 0\n"
        "v_mul_f32 v116, v121, v116\n"
        "v_mfma_f32_16x16x32_bf16 v[180:183], v[140:143], v[76:79], 0\n"
        "v_mul_f32 v117, v121, v117\n"
        "v_mul_f32 v118, v121, v118\n"
        "v_mfma_f32_16x16x32_bf16 v[180:183], v[136:139], v[72:75], v[180:183]\n"
        "v_mul_f32 v119, v121, v119\n"
        "v_cvt_pk_bf16_f32 v116, v116, v117\n"
        "v_mfma_f32_16x16x32_bf16 v[184:187], v[144:147], v[72:75], 0\n"
        "v_cvt_pk_bf16_f32 v117, v118, v119\n"
        "v_mul_f32 v152, v121, v152\n"
        "v_mfma_f32_16x16x32_bf16 v[184:187], v[148:151], v[76:79], v[184:187]\n"
        "s_waitcnt vmcnt(5)\n"
        "v_mul_f32 v153, v121, v153\n"
        "v_maximum3_f32 v253, v253, v120, v121\n"
        "v_smfmac_f32_16x16x64_bf16 v[192:195], a[252:255], v[80:87], v255\n"
        "s_barrier\n"
        "ds_read_b128 a[236:239], v240 offset:0\n"
        "v_mul_f32 v154, v121, v154\n"
        "v_mfma_f32_16x16x32_bf16 v[196:199], v[128:131], v[80:83], 0\n"
        "s_add_u32 m0, s66, 0\n"
        "ds_read_b128 a[240:243], v241 offset:0\n"
        "v_mul_f32 v155, v121, v155\n"
        "v_mfma_f32_16x16x32_bf16 v[196:199], v[132:135], v[84:87], v[196:199]\n"
        "s_add_u32 s64, s51, 0\n"
        "v_cvt_pk_bf16_f32 v118, v152, v153\n"
        "v_cvt_pk_bf16_f32 v119, v154, v155\n"
        "v_mfma_f32_16x16x32_bf16 v[200:203], v[140:143], v[84:87], 0\n"
        "ds_read_b128 a[244:247], v240 offset:128\n"
        "v_mul_f32 v160, v121, v160\n"
        "v_mfma_f32_16x16x32_bf16 v[200:203], v[136:139], v[80:83], v[200:203]\n"
        "buffer_load_dwordx4 v124, s[52:55], s64 offen lds\n"
        "v_mfma_f32_16x16x32_bf16 v[204:207], v[144:147], v[80:83], 0\n"
        "ds_read_b128 a[248:251], v241 offset:128\n"
        "v_mul_f32 v161, v121, v161\n"
        "v_mfma_f32_16x16x32_bf16 v[204:207], v[148:151], v[84:87], v[204:207]\n"
        "ds_read_b128 a[216:219], v240 offset:256\n"
        "v_mul_f32 v162, v121, v162\n"
        "v_smfmac_f32_16x16x64_bf16 v[212:215], a[252:255], v[88:95], v255\n"
        "ds_read_b128 a[220:223], v241 offset:256\n"
        "v_mul_f32 v163, v121, v163\n"
        "v_mfma_f32_16x16x32_bf16 v[216:219], v[128:131], v[88:91], 0\n"
        "ds_write_b128 v245, v[116:119] offset:0\n"
        "v_mfma_f32_16x16x32_bf16 v[216:219], v[132:135], v[92:95], v[216:219]\n"
        "v_cvt_pk_bf16_f32 v160, v160, v161\n"
        "v_cvt_pk_bf16_f32 v161, v162, v163\n"
        "v_mfma_f32_16x16x32_bf16 v[220:223], v[140:143], v[92:95], 0\n"
        "v_add_f32 v172, v172, v173\n"
        "v_add_f32 v192, v192, v193\n"
        "v_mfma_f32_16x16x32_bf16 v[220:223], v[136:139], v[88:91], v[220:223]\n"
        "v_rcp_f32 v173, v172\n"
        "v_mfma_f32_16x16x32_bf16 v[224:227], v[144:147], v[88:91], 0\n"
        "ds_write_b64 v246, v[160:161] offset:0\n"
        "v_mfma_f32_16x16x32_bf16 v[224:227], v[148:151], v[92:95], v[224:227]\n"
        "s_waitcnt lgkmcnt(7)\n"
        "v_mul_f32 v176, v173, v176\n"
        "v_mul_f32 v177, v173, v177\n"
        "v_mfma_f32_16x16x32_bf16 v[96:99], a[72:75], a[236:239], 0\n"
        "s_waitcnt lgkmcnt(6)\n"
        "v_mul_f32 v178, v173, v178\n"
        "v_mul_f32 v179, v173, v179\n"
        "v_mfma_f32_16x16x32_bf16 v[96:99], a[76:79], a[240:243], v[96:99]\n"
        "s_waitcnt lgkmcnt(5)\n"
        "buffer_load_dwordx4 v125, s[52:55], s64 offen offset:1024 lds\n"
        "v_mfma_f32_16x16x32_bf16 v[96:99], a[80:83], a[244:247], v[96:99]\n"
        "s_waitcnt lgkmcnt(4)\n"
        "v_cvt_pk_bf16_f32 v176, v176, v177\n"
        "v_cvt_pk_bf16_f32 v177, v178, v179\n"
        "v_mfma_f32_16x16x32_bf16 v[96:99], a[84:87], a[248:251], v[96:99]\n"
        "s_waitcnt lgkmcnt(3)\n"
        "v_rcp_f32 v193, v192\n"
        "v_mfma_f32_16x16x32_bf16 v[96:99], a[88:91], a[216:219], v[96:99]\n"
        "s_waitcnt lgkmcnt(2)\n"
        "v_mul_f32 v180, v173, v180\n"
        "v_mul_f32 v181, v173, v181\n"
        "v_mfma_f32_16x16x32_bf16 v[96:99], a[92:95], a[220:223], v[96:99]\n"
        "s_add_u32 s41, s29, s40\n"
        "v_maximum3_f32 v253, v253, v172, v173\n"
        "v_mul_f32 v182, v173, v182\n"
        "v_mfma_f32_16x16x32_bf16 v[100:103], a[116:119], a[220:223], 0\n"
        "s_lshr_b32 s47, s58, 6\n"
        "v_mul_f32 v183, v173, v183\n"
        "v_cvt_pk_bf16_f32 v178, v180, v181\n"
        "v_mfma_f32_16x16x32_bf16 v[100:103], a[112:115], a[216:219], v[100:103]\n"
        "s_mul_i32 s47, s47, 1572864\n"
        "v_cvt_pk_bf16_f32 v179, v182, v183\n"
        "v_cvt_pk_bf16_f32 v152, v96, v97\n"
        "v_mfma_f32_16x16x32_bf16 v[100:103], a[108:111], a[248:251], v[100:103]\n"
        "s_sub_u32 s31, s28, s30\n"
        "buffer_load_dwordx4 v126, s[52:55], s64 offen offset:2048 lds\n"
        "v_mfma_f32_16x16x32_bf16 v[100:103], a[104:107], a[244:247], v[100:103]\n"
        "s_sub_u32 s45, s29, s28\n"
        "v_cvt_pk_bf16_f32 v153, v98, v99\n"
        "v_mul_f32 v184, v173, v184\n"
        "v_mfma_f32_16x16x32_bf16 v[100:103], a[100:103], a[240:243], v[100:103]\n"
        "s_mov_b32 s29, s30\n"
        "ds_write_b128 v245, v[176:179] offset:256\n"
        "v_mfma_f32_16x16x32_bf16 v[100:103], a[96:99], a[236:239], v[100:103]\n"
        "s_mov_b32 s30, s28\n"
        "v_mul_f32 v185, v173, v185\n"
        "v_mul_f32 v186, v173, v186\n"
        "v_mfma_f32_16x16x32_bf16 v[104:107], a[120:123], a[236:239], 0\n"
        "s_add_u32 s28, s28, s45\n"
        "v_mul_f32 v187, v173, v187\n"
        "v_cvt_pk_bf16_f32 v184, v184, v185\n"
        "v_mfma_f32_16x16x32_bf16 v[104:107], a[124:127], a[240:243], v[104:107]\n"
        "s_add_u32 s47, s47, s24\n"
        "v_cvt_pk_bf16_f32 v185, v186, v187\n"
        "v_cvt_pk_bf16_f32 v154, v100, v101\n"
        "v_mfma_f32_16x16x32_bf16 v[104:107], a[128:131], a[244:247], v[104:107]\n"
        "s_add_u32 m0, s66, 3072\n"
        "v_cvt_pk_bf16_f32 v155, v102, v103\n"
        "v_mul_f32 v196, v193, v196\n"
        "v_mfma_f32_16x16x32_bf16 v[104:107], a[132:135], a[248:251], v[104:107]\n"
        "s_add_u32 s64, s51, 6144\n"
        "ds_write_b64 v246, v[184:185] offset:256\n"
        "v_mfma_f32_16x16x32_bf16 v[104:107], a[136:139], a[216:219], v[104:107]\n"
        "s_lshl_b32 s36, s58, 13\n"
        "v_mul_f32 v197, v193, v197\n"
        "v_mul_f32 v198, v193, v198\n"
        "v_mfma_f32_16x16x32_bf16 v[104:107], a[140:143], a[220:223], v[104:107]\n"
        "s_add_u32 s36, s36, s34\n"
        "v_mul_f32 v199, v193, v199\n"
        "v_cvt_pk_bf16_f32 v196, v196, v197\n"
        "v_mfma_f32_16x16x32_bf16 v[96:99], a[20:23], a[220:223], 0\n"
        "s_sub_u32 s62, s58, s57\n"
        "v_cvt_pk_bf16_f32 v197, v198, v199\n"
        "v_mul_f32 v200, v193, v200\n"
        "v_mfma_f32_16x16x32_bf16 v[96:99], a[16:19], a[216:219], v[96:99]\n"
        "s_and_b32 s62, s62, 63\n"
        "buffer_load_dwordx4 v124, s[52:55], s64 offen lds\n"
        "v_mfma_f32_16x16x32_bf16 v[96:99], a[12:15], a[248:251], v[96:99]\n"
        "s_mul_i32 s62, s62, 3072\n"
        "v_cvt_pk_bf16_f32 v168, v104, v105\n"
        "v_cvt_pk_bf16_f32 v169, v106, v107\n"
        "v_mfma_f32_16x16x32_bf16 v[96:99], a[8:11], a[244:247], v[96:99]\n"
        "s_add_u32 s35, s47, 786432\n"
        "v_mul_f32 v201, v193, v201\n"
        "v_maximum3_f32 v253, v253, v192, v193\n"
        "v_mfma_f32_16x16x32_bf16 v[96:99], a[4:7], a[240:243], v[96:99]\n"
        "s_sub_u32 s46, s35, 100663296\n"
        "v_mul_f32 v202, v193, v202\n"
        "v_mul_f32 v203, v193, v203\n"
        "v_mfma_f32_16x16x32_bf16 v[96:99], a[0:3], a[236:239], v[96:99]\n"
        "s_cmp_lt_u32 s46, 786432\n"
        "v_cvt_pk_bf16_f32 v198, v200, v201\n"
        "v_cvt_pk_bf16_f32 v199, v202, v203\n"
        "v_mfma_f32_16x16x32_bf16 v[100:103], a[24:27], a[236:239], 0\n"
        "s_cselect_b32 s35, s46, s35\n"
        "ds_write_b128 v245, v[196:199] offset:512\n"
        "v_mfma_f32_16x16x32_bf16 v[100:103], a[28:31], a[240:243], v[100:103]\n"
        "v_mul_f32 v204, v193, v204\n"
        "v_mul_f32 v96, s50, v96\n"
        "v_mfma_f32_16x16x32_bf16 v[100:103], a[32:35], a[244:247], v[100:103]\n"
        "v_mul_f32 v97, s50, v97\n"
        "v_mul_f32 v98, s50, v98\n"
        "v_mfma_f32_16x16x32_bf16 v[100:103], a[36:39], a[248:251], v[100:103]\n"
        "v_mul_f32 v99, s50, v99\n"
        "v_cvt_pk_bf16_f32 v156, v96, v97\n"
        "v_mfma_f32_16x16x32_bf16 v[100:103], a[40:43], a[216:219], v[100:103]\n"
        "buffer_load_dwordx4 v125, s[52:55], s64 offen offset:1024 lds\n"
        "v_mfma_f32_16x16x32_bf16 v[100:103], a[44:47], a[220:223], v[100:103]\n"
        "v_cvt_pk_bf16_f32 v157, v98, v99\n"
        "v_mul_f32 v205, v193, v205\n"
        "v_mfma_f32_16x16x32_bf16 v[104:107], a[68:71], a[220:223], 0\n"
        "v_mul_f32 v206, v193, v206\n"
        "v_mul_f32 v207, v193, v207\n"
        "v_mfma_f32_16x16x32_bf16 v[104:107], a[64:67], a[216:219], v[104:107]\n"
        "v_cvt_pk_bf16_f32 v204, v204, v205\n"
        "v_cvt_pk_bf16_f32 v205, v206, v207\n"
        "v_mfma_f32_16x16x32_bf16 v[104:107], a[60:63], a[248:251], v[104:107]\n"
        "v_mul_f32 v100, s50, v100\n"
        "v_mul_f32 v101, s50, v101\n"
        "v_mfma_f32_16x16x32_bf16 v[104:107], a[56:59], a[244:247], v[104:107]\n"
        "v_mul_f32 v102, s50, v102\n"
        "v_mul_f32 v103, s50, v103\n"
        "v_mfma_f32_16x16x32_bf16 v[104:107], a[52:55], a[240:243], v[104:107]\n"
        "v_cvt_pk_bf16_f32 v158, v100, v101\n"
        "v_cvt_pk_bf16_f32 v159, v102, v103\n"
        "v_mfma_f32_16x16x32_bf16 v[104:107], a[48:51], a[236:239], v[104:107]\n"
        "buffer_load_dwordx4 v126, s[52:55], s64 offen offset:2048 lds\n"
        "v_mfma_f32_16x16x32_bf16 v[96:99], a[236:239], a[144:147], 0\n"
        "ds_write_b64 v246, v[204:205] offset:512\n"
        "v_mfma_f32_16x16x32_bf16 v[96:99], a[240:243], a[148:151], v[96:99]\n"
        "v_add_f32 v212, v212, v213\n"
        "v_xor_b32 v251, 0x2000, v251\n"
        "v_mfma_f32_16x16x32_bf16 v[96:99], a[244:247], a[152:155], v[96:99]\n"
        "v_rcp_f32 v213, v212\n"
        "v_mfma_f32_16x16x32_bf16 v[96:99], a[248:251], a[156:159], v[96:99]\n"
        "v_mul_f32 v104, s50, v104\n"
        "v_mul_f32 v105, s50, v105\n"
        "v_mfma_f32_16x16x32_bf16 v[96:99], a[216:219], a[160:163], v[96:99]\n"
        "s_lshl_b64 s[60:61], s[60:61], 8\n"
        "v_mul_f32 v106, s50, v106\n"
        "v_mul_f32 v107, s50, v107\n"
        "v_mfma_f32_16x16x32_bf16 v[96:99], a[220:223], a[164:167], v[96:99]\n"
        "s_add_u32 m0, s66, 6144\n"
        "v_cvt_pk_bf16_f32 v160, v104, v105\n"
        "v_cvt_pk_bf16_f32 v161, v106, v107\n"
        "v_mfma_f32_16x16x32_bf16 v[100:103], a[220:223], a[188:191], 0\n"
        "s_add_u32 s64, s51, 18432\n"
        "v_mov_b64 v[166:167], v[160:161]\n"
        "v_mul_f32 v216, v213, v216\n"
        "v_mfma_f32_16x16x32_bf16 v[100:103], a[216:219], a[184:187], v[100:103]\n"
        "s_and_b32 s63, s58, 255\n"
        "v_mul_f32 v217, v213, v217\n"
        "v_mul_f32 v218, v213, v218\n"
        "v_mfma_f32_16x16x32_bf16 v[100:103], a[248:251], a[180:183], v[100:103]\n"
        "s_or_b32 s60, s60, s63\n"
        "buffer_load_dwordx4 v124, s[52:55], s64 offen lds\n"
        "v_mfma_f32_16x16x32_bf16 v[100:103], a[244:247], a[176:179], v[100:103]\n"
        "s_add_u32 s27, s27, 1\n"
        "ds_read_b128 a[224:227], v240 offset:1536\n"
        "ds_read_b128 a[228:231], v241 offset:1536\n"
        "v_mfma_f32_16x16x32_bf16 v[100:103], a[240:243], a[172:175], v[100:103]\n"
        "s_add_u32 s63, s58, 256\n"
        "ds_read_b128 a[232:235], v240 offset:1664\n"
        "v_mul_f32 v219, v213, v219\n"
        "v_mfma_f32_16x16x32_bf16 v[100:103], a[236:239], a[168:171], v[100:103]\n"
        "s_cmp_lt_u32 s63, 3840\n"
        "v_cvt_pk_bf16_f32 v216, v216, v217\n"
        "v_cvt_pk_bf16_f32 v217, v218, v219\n"
        "v_mfma_f32_16x16x32_bf16 v[104:107], a[236:239], a[192:195], 0\n"
        "s_cselect_b32 s63, s63, -1\n"
        "ds_read_b128 a[236:239], v241 offset:1664\n"
        "v_cvt_pk_bf16_f32 v128, v96, v97\n"
        "v_mfma_f32_16x16x32_bf16 v[104:107], a[240:243], a[196:199], v[104:107]\n"
        "s_cmp_lt_u32 s27, 1024\n"
        "ds_read_b128 a[240:243], v240 offset:1792\n"
        "v_cvt_pk_bf16_f32 v129, v98, v99\n"
        "v_mfma_f32_16x16x32_bf16 v[104:107], a[244:247], a[200:203], v[104:107]\n"
        "s_cselect_b32 s49, 4096, 0\n"
        "ds_read_b128 a[244:247], v241 offset:1792\n"
        "v_mul_f32 v220, v213, v220\n"
        "v_mfma_f32_16x16x32_bf16 v[104:107], a[248:251], a[204:207], v[104:107]\n"
        "v_mul_f32 v221, v213, v221\n"
        "v_maximum3_f32 v253, v253, v212, v213\n"
        "v_mfma_f32_16x16x32_bf16 v[104:107], a[216:219], a[208:211], v[104:107]\n"
        "v_mul_f32 v222, v213, v222\n"
        "v_mul_f32 v223, v213, v223\n"
        "v_mfma_f32_16x16x32_bf16 v[104:107], a[220:223], a[212:215], v[104:107]\n"
        "s_waitcnt lgkmcnt(5)\n"
        "buffer_load_dwordx4 v125, s[52:55], s64 offen offset:1024 lds\n"
        "v_mfma_f32_16x16x32_bf16 v[96:99], a[72:75], a[224:227], 0\n"
        "s_waitcnt lgkmcnt(4)\n"
        "v_cvt_pk_bf16_f32 v218, v220, v221\n"
        "v_cvt_pk_bf16_f32 v219, v222, v223\n"
        "v_mfma_f32_16x16x32_bf16 v[96:99], a[76:79], a[228:231], v[96:99]\n"
        "s_waitcnt lgkmcnt(3)\n"
        "v_xor_b32 v252, 0x2000, v252\n"
        "v_mul_f32 v224, v213, v224\n"
        "v_mfma_f32_16x16x32_bf16 v[96:99], a[80:83], a[232:235], v[96:99]\n"
        "s_waitcnt lgkmcnt(2)\n"
        "ds_write_b128 v245, v[216:219] offset:768\n"
        "v_mfma_f32_16x16x32_bf16 v[96:99], a[84:87], a[236:239], v[96:99]\n"
        "s_waitcnt lgkmcnt(2)\n"
        "v_mul_f32 v225, v213, v225\n"
        "v_cvt_pk_bf16_f32 v136, v100, v101\n"
        "v_mfma_f32_16x16x32_bf16 v[96:99], a[88:91], a[240:243], v[96:99]\n"
        "s_waitcnt lgkmcnt(1)\n"
        "v_cvt_pk_bf16_f32 v137, v102, v103\n"
        "v_mul_f32 v226, v213, v226\n"
        "v_mfma_f32_16x16x32_bf16 v[96:99], a[92:95], a[244:247], v[96:99]\n"
        "s_waitcnt vmcnt(8)\n"
        "v_mul_f32 v227, v213, v227\n"
        "v_cvt_pk_bf16_f32 v224, v224, v225\n"
        "v_mfma_f32_16x16x32_bf16 v[100:103], a[116:119], a[244:247], 0\n"
        "s_barrier\n"
        "v_cvt_pk_bf16_f32 v225, v226, v227\n"
        "v_cvt_pk_bf16_f32 v144, v104, v105\n"
        "v_mfma_f32_16x16x32_bf16 v[100:103], a[112:115], a[240:243], v[100:103]\n"
        "42:\n"
        "s_cbranch_execz 45f\n"
        "ds_write_b64 v246, v[224:225] offset:768\n"
        "v_mfma_f32_16x16x32_bf16 v[100:103], a[108:111], a[236:239], v[100:103]\n"
        "v_cvt_pk_bf16_f32 v145, v106, v107\n"
        "v_cvt_pk_bf16_f32 v172, v96, v97\n"
        "v_mfma_f32_16x16x32_bf16 v[100:103], a[104:107], a[232:235], v[100:103]\n"
        "v_cvt_pk_bf16_f32 v173, v98, v99\n"
        "v_add_u32 v245, s31, v245\n"
        "v_mfma_f32_16x16x32_bf16 v[100:103], a[100:103], a[228:231], v[100:103]\n"
        "buffer_load_dwordx4 v126, s[52:55], s64 offen offset:2048 lds\n"
        "v_mfma_f32_16x16x32_bf16 v[100:103], a[96:99], a[224:227], v[100:103]\n"
        "s_waitcnt vmcnt(3)\n"
        "ds_read_b64_tr_b16 v[88:89], v234 offset:0\n"
        "ds_read_b64_tr_b16 v[90:91], v234 offset:3072\n"
        "v_mfma_f32_16x16x32_bf16 v[104:107], a[120:123], a[224:227], 0\n"
        "ds_read_b64_tr_b16 v[92:93], v235 offset:32\n"
        "v_add_u32 v246, s31, v246\n"
        "v_mfma_f32_16x16x32_bf16 v[104:107], a[124:127], a[228:231], v[104:107]\n"
        "ds_read_b64_tr_b16 v[94:95], v235 offset:3104\n"
        "v_mfma_f32_16x16x32_bf16 v[104:107], a[128:131], a[232:235], v[104:107]\n"
        "s_add_u32 m0, s66, 9216\n"
        "ds_read_b64_tr_b16 v[120:121], v234 offset:64\n"
        "ds_read_b128 v[162:165], v251 offset:0\n"
        "v_mfma_f32_16x16x32_bf16 v[104:107], a[132:135], a[236:239], v[104:107]\n"
        "s_add_u32 s64, s51, 24576\n"
        "ds_read_b64_tr_b16 v[122:123], v234 offset:3136\n"
        "v_cvt_pk_bf16_f32 v174, v100, v101\n"
        "v_mfma_f32_16x16x32_bf16 v[104:107], a[136:139], a[240:243], v[104:107]\n"
        "ds_read_b64_tr_b16 v[216:217], v235 offset:96\n"
        "v_cvt_pk_bf16_f32 v175, v102, v103\n"
        "v_mfma_f32_16x16x32_bf16 v[104:107], a[140:143], a[244:247], v[104:107]\n"
        "ds_read_b64_tr_b16 v[218:219], v235 offset:3168\n"
        "v_mfma_f32_16x16x32_bf16 v[96:99], a[20:23], a[244:247], 0\n"
        "buffer_load_dwordx4 v124, s[52:55], s64 offen lds\n"
        "v_mfma_f32_16x16x32_bf16 v[96:99], a[16:19], a[240:243], v[96:99]\n"
        "ds_read_b64_tr_b16 v[220:221], v238 offset:128\n"
        "ds_read_b64_tr_b16 v[222:223], v238 offset:3200\n"
        "v_mfma_f32_16x16x32_bf16 v[96:99], a[12:15], a[236:239], v[96:99]\n"
        "ds_read_b64_tr_b16 v[224:225], v239 offset:160\n"
        "v_cvt_pk_bf16_f32 v190, v104, v105\n"
        "v_mfma_f32_16x16x32_bf16 v[96:99], a[8:11], a[232:235], v[96:99]\n"
        "ds_read_b64_tr_b16 v[226:227], v239 offset:3232\n"
        "v_cvt_pk_bf16_f32 v191, v106, v107\n"
        "v_mfma_f32_16x16x32_bf16 v[96:99], a[4:7], a[228:231], v[96:99]\n"
        "s_waitcnt lgkmcnt(11)\n"
        "ds_write_b128 v236, v[88:91] offset:0\n"
        "v_mfma_f32_16x16x32_bf16 v[96:99], a[0:3], a[224:227], v[96:99]\n"
        "v_mfma_f32_16x16x32_bf16 v[64:67], v[152:155], v[156:159], v[0:3]\n"
        "s_waitcnt lgkmcnt(8)\n"
        "v_mfma_f32_16x16x32_bf16 v[64:67], v[168:171], v[160:163], v[64:67]\n"
        "v_mfma_f32_16x16x32_bf16 v[68:71], v[172:175], v[156:159], v[4:7]\n"
        "buffer_load_dwordx4 v125, s[52:55], s64 offen offset:1024 lds\n"
        "v_mfma_f32_16x16x32_bf16 v[68:71], v[188:191], v[164:167], v[68:71]\n"
        "ds_write_b128 v236, v[92:95] offset:384\n"
        "v_mfma_f32_16x16x32_bf16 v[100:103], a[24:27], a[224:227], 0\n"
        "v_mfma_f32_16x16x32_bf16 v[100:103], a[28:31], a[228:231], v[100:103]\n"
        "v_mul_f32 v96, s50, v96\n"
        "v_mul_f32 v97, s50, v97\n"
        "v_mfma_f32_16x16x32_bf16 v[100:103], a[32:35], a[232:235], v[100:103]\n"
        "v_mul_f32 v98, s50, v98\n"
        "v_mul_f32 v99, s50, v99\n"
        "v_mfma_f32_16x16x32_bf16 v[100:103], a[36:39], a[236:239], v[100:103]\n"
        "v_cvt_pk_bf16_f32 v176, v96, v97\n"
        "v_cvt_pk_bf16_f32 v177, v98, v99\n"
        "v_mfma_f32_16x16x32_bf16 v[100:103], a[40:43], a[240:243], v[100:103]\n"
        "s_waitcnt lgkmcnt(8)\n"
        "ds_write_b128 v236, v[120:123] offset:768\n"
        "v_mfma_f32_16x16x32_bf16 v[100:103], a[44:47], a[244:247], v[100:103]\n"
        "v_exp_f32 v64, v64\n"
        "v_mfma_f32_16x16x32_bf16 v[104:107], a[68:71], a[244:247], 0\n"
        "v_exp_f32 v65, v65\n"
        "v_mfma_f32_16x16x32_bf16 v[104:107], a[64:67], a[240:243], v[104:107]\n"
        "v_exp_f32 v66, v66\n"
        "v_mfma_f32_16x16x32_bf16 v[104:107], a[60:63], a[236:239], v[104:107]\n"
        "buffer_load_dwordx4 v126, s[52:55], s64 offen offset:2048 lds\n"
        "v_mfma_f32_16x16x32_bf16 v[104:107], a[56:59], a[232:235], v[104:107]\n"
        "ds_read_b128 v[182:185], v251 offset:512\n"
        "v_mul_f32 v100, s50, v100\n"
        "v_mfma_f32_16x16x32_bf16 v[104:107], a[52:55], a[228:231], v[104:107]\n"
        "v_mul_f32 v101, s50, v101\n"
        "v_mul_f32 v102, s50, v102\n"
        "v_mfma_f32_16x16x32_bf16 v[104:107], a[48:51], a[224:227], v[104:107]\n"
        "v_mul_f32 v103, s50, v103\n"
        "v_cvt_pk_bf16_f32 v178, v100, v101\n"
        "v_mfma_f32_16x16x32_bf16 v[96:99], a[224:227], a[144:147], 0\n"
        "ds_read_b128 a[248:251], v240 offset:12288\n"
        "v_cvt_pk_bf16_f32 v179, v102, v103\n"
        "v_mfma_f32_16x16x32_bf16 v[96:99], a[228:231], a[148:151], v[96:99]\n"
        "s_add_u32 m0, s66, 12288\n"
        "s_waitcnt lgkmcnt(9)\n"
        "ds_write_b128 v236, v[216:219] offset:1152\n"
        "v_mfma_f32_16x16x32_bf16 v[96:99], a[232:235], a[152:155], v[96:99]\n"
        "s_add_u32 s64, s51, 12288\n"
        "v_mul_f32 v104, s50, v104\n"
        "v_mul_f32 v105, s50, v105\n"
        "v_mfma_f32_16x16x32_bf16 v[96:99], a[236:239], a[156:159], v[96:99]\n"
        "v_mul_f32 v106, s50, v106\n"
        "v_mul_f32 v107, s50, v107\n"
        "v_mfma_f32_16x16x32_bf16 v[96:99], a[240:243], a[160:163], v[96:99]\n"
        "v_cvt_pk_bf16_f32 v180, v104, v105\n"
        "v_cvt_pk_bf16_f32 v181, v106, v107\n"
        "v_mfma_f32_16x16x32_bf16 v[96:99], a[244:247], a[164:167], v[96:99]\n"
        "buffer_load_dwordx4 v127, s[52:55], s64 offen lds\n"
        "v_mfma_f32_16x16x32_bf16 v[72:75], v[152:155], v[176:179], v[16:19]\n"
        "s_waitcnt lgkmcnt(2)\n"
        "ds_write_b128 v237, v[220:223] offset:0\n"
        "v_mfma_f32_16x16x32_bf16 v[72:75], v[168:171], v[180:183], v[72:75]\n"
        "v_mov_b64 v[186:187], v[180:181]\n"
        "v_cvt_pk_bf16_f32 v64, v64, v65\n"
        "v_mfma_f32_16x16x32_bf16 v[76:79], v[172:175], v[176:179], v[20:23]\n"
        "v_exp_f32 v67, v67\n"
        "v_mfma_f32_16x16x32_bf16 v[76:79], v[188:191], v[184:187], v[76:79]\n"
        "v_cvt_pk_bf16_f32 v130, v96, v97\n"
        "v_cvt_pk_bf16_f32 v131, v98, v99\n"
        "v_mfma_f32_16x16x32_bf16 v[100:103], a[244:247], a[188:191], 0\n"
        "ds_write_b128 v237, v[224:227] offset:768\n"
        "v_mfma_f32_16x16x32_bf16 v[100:103], a[240:243], a[184:187], v[100:103]\n"
        "v_exp_f32 v68, v68\n"
        "v_mfma_f32_16x16x32_bf16 v[100:103], a[236:239], a[180:183], v[100:103]\n"
        "v_exp_f32 v69, v69\n"
        "v_mfma_f32_16x16x32_bf16 v[100:103], a[232:235], a[176:179], v[100:103]\n"
        "v_exp_f32 v70, v70\n"
        "v_mfma_f32_16x16x32_bf16 v[100:103], a[228:231], a[172:175], v[100:103]\n"
        "buffer_load_dwordx4 v232, s[52:55], s64 offen offset:1024 lds\n"
        "v_mfma_f32_16x16x32_bf16 v[100:103], a[224:227], a[168:171], v[100:103]\n"
        "ds_read_b128 a[216:219], v241 offset:12288\n"
        "ds_read_b128 a[220:223], v240 offset:12416\n"
        "v_mfma_f32_16x16x32_bf16 v[104:107], a[224:227], a[192:195], 0\n"
        "ds_read_b128 a[224:227], v241 offset:12416\n"
        "v_cvt_pk_bf16_f32 v65, v66, v67\n"
        "v_mfma_f32_16x16x32_bf16 v[104:107], a[228:231], a[196:199], v[104:107]\n"
        "ds_read_b128 a[228:231], v240 offset:12544\n"
        "v_cvt_pk_bf16_f32 v66, v68, v69\n"
        "v_mfma_f32_16x16x32_bf16 v[104:107], a[232:235], a[200:203], v[104:107]\n"
        "ds_read_b128 a[232:235], v241 offset:12544\n"
        "v_cvt_pk_bf16_f32 v138, v100, v101\n"
        "v_mfma_f32_16x16x32_bf16 v[104:107], a[236:239], a[204:207], v[104:107]\n"
        "v_cvt_pk_bf16_f32 v139, v102, v103\n"
        "v_mfma_f32_16x16x32_bf16 v[104:107], a[240:243], a[208:211], v[104:107]\n"
        "v_exp_f32 v71, v71\n"
        "v_mfma_f32_16x16x32_bf16 v[104:107], a[244:247], a[212:215], v[104:107]\n"
        "s_waitcnt lgkmcnt(8)\n"
        "v_exp_f32 v72, v72\n"
        "v_mfma_f32_16x16x32_bf16 v[96:99], a[72:75], a[248:251], 0\n"
        "s_waitcnt lgkmcnt(4)\n"
        "buffer_load_dwordx4 v233, s[52:55], s64 offen offset:2048 lds\n"
        "v_mfma_f32_16x16x32_bf16 v[96:99], a[76:79], a[216:219], v[96:99]\n"
        "s_waitcnt lgkmcnt(3)\n"
        "v_exp_f32 v73, v73\n"
        "v_mfma_f32_16x16x32_bf16 v[96:99], a[80:83], a[220:223], v[96:99]\n"
        "s_waitcnt lgkmcnt(2)\n"
        "v_exp_f32 v74, v74\n"
        "v_mfma_f32_16x16x32_bf16 v[96:99], a[84:87], a[224:227], v[96:99]\n"
        "s_waitcnt lgkmcnt(1)\n"
        "v_cvt_pk_bf16_f32 v146, v104, v105\n"
        "v_cvt_pk_bf16_f32 v147, v106, v107\n"
        "v_mfma_f32_16x16x32_bf16 v[96:99], a[88:91], a[228:231], v[96:99]\n"
        "s_waitcnt vmcnt(3) lgkmcnt(0)\n"
        "ds_read_b64_tr_b16 v[88:89], v234 offset:6144\n"
        "v_cvt_pk_bf16_f32 v67, v70, v71\n"
        "v_mfma_f32_16x16x32_bf16 v[96:99], a[92:95], a[232:235], v[96:99]\n"
        "ds_read_b64_tr_b16 v[90:91], v234 offset:9216\n"
        "v_cvt_pk_bf16_f32 v72, v72, v73\n"
        "v_mfma_f32_16x16x32_bf16 v[100:103], a[116:119], a[232:235], 0\n"
        "s_add_u32 m0, s66, 15360\n"
        "ds_read_b64_tr_b16 v[92:93], v235 offset:6176\n"
        "v_mfma_f32_16x16x32_bf16 v[100:103], a[112:115], a[228:231], v[100:103]\n"
        "s_add_u32 s64, s51, 13824\n"
        "ds_read_b64_tr_b16 v[94:95], v235 offset:9248\n"
        "v_cvt_pk_bf16_f32 v192, v96, v97\n"
        "v_mfma_f32_16x16x32_bf16 v[100:103], a[108:111], a[224:227], v[100:103]\n"
        "ds_read_b64_tr_b16 v[120:121], v234 offset:6208\n"
        "v_cvt_pk_bf16_f32 v193, v98, v99\n"
        "v_mfma_f32_16x16x32_bf16 v[100:103], a[104:107], a[220:223], v[100:103]\n"
        "ds_read_b64_tr_b16 v[122:123], v234 offset:9280\n"
        "v_mfma_f32_16x16x32_bf16 v[100:103], a[100:103], a[216:219], v[100:103]\n"
        "buffer_load_dwordx4 v127, s[52:55], s64 offen lds\n"
        "v_mfma_f32_16x16x32_bf16 v[100:103], a[96:99], a[248:251], v[100:103]\n"
        "ds_read_b64_tr_b16 v[216:217], v235 offset:6240\n"
        "ds_read_b64_tr_b16 v[218:219], v235 offset:9312\n"
        "v_mfma_f32_16x16x32_bf16 v[104:107], a[120:123], a[248:251], 0\n"
        "ds_read_b64_tr_b16 v[220:221], v238 offset:6272\n"
        "v_mfma_f32_16x16x32_bf16 v[104:107], a[124:127], a[216:219], v[104:107]\n"
        "ds_read_b64_tr_b16 v[222:223], v238 offset:9344\n"
        "v_mfma_f32_16x16x32_bf16 v[104:107], a[128:131], a[220:223], v[104:107]\n"
        "ds_read_b64_tr_b16 v[224:225], v239 offset:6304\n"
        "v_cvt_pk_bf16_f32 v194, v100, v101\n"
        "v_mfma_f32_16x16x32_bf16 v[104:107], a[132:135], a[224:227], v[104:107]\n"
        "ds_read_b64_tr_b16 v[226:227], v239 offset:9376\n"
        "v_cvt_pk_bf16_f32 v195, v102, v103\n"
        "v_mfma_f32_16x16x32_bf16 v[104:107], a[136:139], a[228:231], v[104:107]\n"
        "s_waitcnt lgkmcnt(10)\n"
        "ds_write_b128 v236, v[88:91] offset:6144\n"
        "v_mfma_f32_16x16x32_bf16 v[104:107], a[140:143], a[232:235], v[104:107]\n"
        "v_exp_f32 v75, v75\n"
        "v_mfma_f32_16x16x32_bf16 v[96:99], a[20:23], a[232:235], 0\n"
        "v_exp_f32 v76, v76\n"
        "v_mfma_f32_16x16x32_bf16 v[96:99], a[16:19], a[228:231], v[96:99]\n"
        "v_exp_f32 v77, v77\n"
        "v_mfma_f32_16x16x32_bf16 v[96:99], a[12:15], a[224:227], v[96:99]\n"
        "buffer_load_dwordx4 v232, s[52:55], s64 offen offset:1024 lds\n"
        "v_mfma_f32_16x16x32_bf16 v[96:99], a[8:11], a[220:223], v[96:99]\n"
        "s_waitcnt lgkmcnt(9)\n"
        "ds_write_b128 v236, v[92:95] offset:6528\n"
        "v_mfma_f32_16x16x32_bf16 v[96:99], a[4:7], a[216:219], v[96:99]\n"
        "v_cvt_pk_bf16_f32 v208, v104, v105\n"
        "v_cvt_pk_bf16_f32 v209, v106, v107\n"
        "v_mfma_f32_16x16x32_bf16 v[96:99], a[0:3], a[248:251], v[96:99]\n"
        "v_exp_f32 v78, v78\n"
        "v_mfma_f32_16x16x32_bf16 v[100:103], a[24:27], a[248:251], 0\n"
        "v_exp_f32 v79, v79\n"
        "v_mfma_f32_16x16x32_bf16 v[100:103], a[28:31], a[216:219], v[100:103]\n"
        "ds_read_b128 v[162:165], v252 offset:0\n"
        "v_cvt_pk_bf16_f32 v73, v74, v75\n"
        "v_mfma_f32_16x16x32_bf16 v[100:103], a[32:35], a[220:223], v[100:103]\n"
        "ds_read_b128 v[182:185], v252 offset:512\n"
        "v_mul_f32 v96, s50, v96\n"
        "v_mfma_f32_16x16x32_bf16 v[100:103], a[36:39], a[224:227], v[100:103]\n"
        "buffer_load_dwordx4 v233, s[52:55], s64 offen offset:2048 lds\n"
        "v_mfma_f32_16x16x32_bf16 v[100:103], a[40:43], a[228:231], v[100:103]\n"
        "s_waitcnt lgkmcnt(10)\n"
        "ds_write_b128 v236, v[120:123] offset:6912\n"
        "v_mfma_f32_16x16x32_bf16 v[100:103], a[44:47], a[232:235], v[100:103]\n"
        "v_mul_f32 v97, s50, v97\n"
        "v_mul_f32 v98, s50, v98\n"
        "v_mfma_f32_16x16x32_bf16 v[104:107], a[68:71], a[232:235], 0\n"
        "v_mul_f32 v99, s50, v99\n"
        "v_cvt_pk_bf16_f32 v196, v96, v97\n"
        "v_mfma_f32_16x16x32_bf16 v[104:107], a[64:67], a[228:231], v[104:107]\n"
        "v_cvt_pk_bf16_f32 v197, v98, v99\n"
        "v_cvt_pk_bf16_f32 v74, v76, v77\n"
        "v_mfma_f32_16x16x32_bf16 v[104:107], a[60:63], a[224:227], v[104:107]\n"
        "s_waitcnt lgkmcnt(9)\n"
        "ds_write_b128 v236, v[216:219] offset:7296\n"
        "v_mfma_f32_16x16x32_bf16 v[104:107], a[56:59], a[220:223], v[104:107]\n"
        "v_mul_f32 v100, s50, v100\n"
        "v_mul_f32 v101, s50, v101\n"
        "v_mfma_f32_16x16x32_bf16 v[104:107], a[52:55], a[216:219], v[104:107]\n"
        "s_add_u32 m0, s41, 0\n"
        "v_mul_f32 v102, s50, v102\n"
        "v_mul_f32 v103, s50, v103\n"
        "v_mfma_f32_16x16x32_bf16 v[104:107], a[48:51], a[248:251], v[104:107]\n"
        "v_cvt_pk_bf16_f32 v198, v100, v101\n"
        "v_cvt_pk_bf16_f32 v199, v102, v103\n"
        "v_mfma_f32_16x16x32_bf16 v[68:71], v[192:195], v[156:159], v[8:11]\n"
        "s_waitcnt lgkmcnt(3)\n"
        "buffer_load_dwordx4 v242, s[8:11], s47 offen lds\n"
        "v_mfma_f32_16x16x32_bf16 v[68:71], v[208:211], v[160:163], v[68:71]\n"
        "ds_write_b128 v237, v[220:223] offset:6144\n"
        "v_mfma_f32_16x16x32_bf16 v[96:99], a[248:251], a[144:147], 0\n"
        "v_cvt_pk_bf16_f32 v75, v78, v79\n"
        "v_mul_f32 v104, s50, v104\n"
        "v_mfma_f32_16x16x32_bf16 v[96:99], a[216:219], a[148:151], v[96:99]\n"
        "s_add_u32 m0, s41, 1024\n"
        "v_mul_f32 v105, s50, v105\n"
        "v_mul_f32 v106, s50, v106\n"
        "v_mfma_f32_16x16x32_bf16 v[96:99], a[220:223], a[152:155], v[96:99]\n"
        "v_mul_f32 v107, s50, v107\n"
        "v_cvt_pk_bf16_f32 v200, v104, v105\n"
        "v_mfma_f32_16x16x32_bf16 v[96:99], a[224:227], a[156:159], v[96:99]\n"
        "buffer_load_dwordx4 v243, s[8:11], s47 offen lds\n"
        "v_mfma_f32_16x16x32_bf16 v[96:99], a[228:231], a[160:163], v[96:99]\n"
        "ds_read_b128 v[202:205], v251 offset:4096\n"
        "v_cvt_pk_bf16_f32 v201, v106, v107\n"
        "v_mfma_f32_16x16x32_bf16 v[96:99], a[232:235], a[164:167], v[96:99]\n"
        "ds_write_b128 v237, v[224:227] offset:6912\n"
        "v_mfma_f32_16x16x32_bf16 v[76:79], v[192:195], v[176:179], v[24:27]\n"
        "s_waitcnt lgkmcnt(5)\n"
        "s_add_u32 m0, s41, 2048\n"
        "v_mov_b64 v[206:207], v[200:201]\n"
        "v_mfma_f32_16x16x32_bf16 v[76:79], v[208:211], v[180:183], v[76:79]\n"
        "v_exp_f32 v68, v68\n"
        "v_mfma_f32_16x16x32_bf16 v[100:103], a[232:235], a[188:191], 0\n"
        "v_exp_f32 v69, v69\n"
        "v_mfma_f32_16x16x32_bf16 v[100:103], a[228:231], a[184:187], v[100:103]\n"
        "buffer_load_dwordx4 v244, s[8:11], s47 offen lds\n"
        "v_mfma_f32_16x16x32_bf16 v[100:103], a[224:227], a[180:183], v[100:103]\n"
        "v_cvt_pk_bf16_f32 v132, v96, v97\n"
        "v_cvt_pk_bf16_f32 v133, v98, v99\n"
        "v_mfma_f32_16x16x32_bf16 v[100:103], a[220:223], a[176:179], v[100:103]\n"
        "s_add_u32 m0, s41, 12288\n"
        "ds_read_b128 a[236:239], v240 offset:13824\n"
        "v_cvt_pk_bf16_f32 v68, v68, v69\n"
        "v_mfma_f32_16x16x32_bf16 v[100:103], a[216:219], a[172:175], v[100:103]\n"
        "s_waitcnt vmcnt(3)\n"
        "ds_read_b64_tr_b16 v[220:221], v234 offset:12288\n"
        "v_mfma_f32_16x16x32_bf16 v[100:103], a[248:251], a[168:171], v[100:103]\n"
        "ds_read_b64_tr_b16 v[222:223], v234 offset:15360\n"
        "v_mfma_f32_16x16x32_bf16 v[80:83], v[152:155], v[196:199], v[32:35]\n"
        "s_waitcnt lgkmcnt(4)\n"
        "buffer_load_dwordx4 v242, s[8:11], s35 offen lds\n"
        "v_mfma_f32_16x16x32_bf16 v[80:83], v[168:171], v[200:203], v[80:83]\n"
        "ds_read_b64_tr_b16 v[224:225], v235 offset:12320\n"
        "ds_read_b64_tr_b16 v[226:227], v235 offset:15392\n"
        "v_mfma_f32_16x16x32_bf16 v[104:107], a[248:251], a[192:195], 0\n"
        "s_add_u32 m0, s41, 13312\n"
        "ds_read_b128 a[240:243], v241 offset:13824\n"
        "ds_read_b64_tr_b16 v[88:89], v234 offset:12352\n"
        "v_mfma_f32_16x16x32_bf16 v[104:107], a[216:219], a[196:199], v[104:107]\n"
        "ds_read_b128 a[244:247], v240 offset:13952\n"
        "ds_read_b64_tr_b16 v[90:91], v234 offset:15424\n"
        "v_mfma_f32_16x16x32_bf16 v[104:107], a[220:223], a[200:203], v[104:107]\n"
        "ds_read_b128 a[248:251], v241 offset:13952\n"
        "ds_read_b64_tr_b16 v[92:93], v235 offset:12384\n"
        "v_mfma_f32_16x16x32_bf16 v[104:107], a[224:227], a[204:207], v[104:107]\n"
        "ds_read_b128 a[216:219], v240 offset:14080\n"
        "ds_read_b64_tr_b16 v[94:95], v235 offset:15456\n"
        "v_mfma_f32_16x16x32_bf16 v[104:107], a[228:231], a[208:211], v[104:107]\n"
        "buffer_load_dwordx4 v243, s[8:11], s35 offen lds\n"
        "v_mfma_f32_16x16x32_bf16 v[104:107], a[232:235], a[212:215], v[104:107]\n"
        "s_add_u32 m0, s41, 14336\n"
        "ds_read_b128 a[220:223], v241 offset:14080\n"
        "ds_read_b64_tr_b16 v[120:121], v238 offset:12416\n"
        "v_mfma_f32_16x16x32_bf16 v[84:87], v[172:175], v[196:199], v[36:39]\n"
        "ds_read_b64_tr_b16 v[122:123], v238 offset:15488\n"
        "ds_read_b64_tr_b16 v[124:125], v239 offset:12448\n"
        "v_mfma_f32_16x16x32_bf16 v[84:87], v[188:191], v[204:207], v[84:87]\n"
        "s_waitcnt lgkmcnt(15)\n"
        "ds_read_b64_tr_b16 v[126:127], v239 offset:15520\n"
        "v_cvt_pk_bf16_f32 v140, v100, v101\n"
        "v_mfma_f32_16x16x32_bf16 v[96:99], a[72:75], a[236:239], 0\n"
        "s_waitcnt lgkmcnt(12)\n"
        "buffer_load_dwordx4 v244, s[8:11], s35 offen lds\n"
        "v_mfma_f32_16x16x32_bf16 v[96:99], a[76:79], a[240:243], v[96:99]\n"
        "s_waitcnt lgkmcnt(10)\n"
        "v_cvt_pk_bf16_f32 v141, v102, v103\n"
        "v_cvt_pk_bf16_f32 v148, v104, v105\n"
        "v_mfma_f32_16x16x32_bf16 v[96:99], a[80:83], a[244:247], v[96:99]\n"
        "s_waitcnt lgkmcnt(8)\n"
        "s_mov_b32 m0, s48\n"
        "v_cvt_pk_bf16_f32 v149, v106, v107\n"
        "v_mfma_f32_16x16x32_bf16 v[96:99], a[84:87], a[248:251], v[96:99]\n"
        "s_waitcnt lgkmcnt(6)\n"
        "v_exp_f32 v70, v70\n"
        "v_mfma_f32_16x16x32_bf16 v[96:99], a[88:91], a[216:219], v[96:99]\n"
        "s_waitcnt lgkmcnt(4)\n"
        "buffer_load_dwordx4 v249, s[12:15], s36 offen offset:0 lds\n"
        "v_mfma_f32_16x16x32_bf16 v[96:99], a[92:95], a[220:223], v[96:99]\n"
        "ds_read_b128 v[202:205], v252 offset:4096\n"
        "v_xor_b32 v116, 32, v249\n"
        "v_mfma_f32_16x16x32_bf16 v[100:103], a[116:119], a[220:223], 0\n"
        "ds_write_b128 v236, v[220:223] offset:12288\n"
        "v_mfma_f32_16x16x32_bf16 v[100:103], a[112:115], a[216:219], v[100:103]\n"
        "v_exp_f32 v71, v71\n"
        "v_mfma_f32_16x16x32_bf16 v[100:103], a[108:111], a[248:251], v[100:103]\n"
        "v_exp_f32 v76, v76\n"
        "v_mfma_f32_16x16x32_bf16 v[100:103], a[104:107], a[244:247], v[100:103]\n"
        "v_cvt_pk_bf16_f32 v212, v96, v97\n"
        "v_cvt_pk_bf16_f32 v213, v98, v99\n"
        "v_mfma_f32_16x16x32_bf16 v[100:103], a[100:103], a[240:243], v[100:103]\n"
        "buffer_load_dwordx4 v116, s[12:15], s36 offen offset:1024 lds\n"
        "v_mfma_f32_16x16x32_bf16 v[100:103], a[96:99], a[236:239], v[100:103]\n"
        "ds_write_b128 v236, v[224:227] offset:12672\n"
        "v_mfma_f32_16x16x32_bf16 v[104:107], a[120:123], a[236:239], 0\n"
        "v_exp_f32 v77, v77\n"
        "v_mfma_f32_16x16x32_bf16 v[104:107], a[124:127], a[240:243], v[104:107]\n"
        "v_exp_f32 v78, v78\n"
        "v_mfma_f32_16x16x32_bf16 v[104:107], a[128:131], a[244:247], v[104:107]\n"
        "v_exp_f32 v79, v79\n"
        "v_mfma_f32_16x16x32_bf16 v[104:107], a[132:135], a[248:251], v[104:107]\n"
        "v_cvt_pk_bf16_f32 v214, v100, v101\n"
        "v_cvt_pk_bf16_f32 v215, v102, v103\n"
        "v_mfma_f32_16x16x32_bf16 v[104:107], a[136:139], a[216:219], v[104:107]\n"
        "ds_write_b128 v236, v[88:91] offset:13056\n"
        "v_mfma_f32_16x16x32_bf16 v[104:107], a[140:143], a[220:223], v[104:107]\n"
        "v_cvt_pk_bf16_f32 v69, v70, v71\n"
        "v_cvt_pk_bf16_f32 v76, v76, v77\n"
        "v_mfma_f32_16x16x32_bf16 v[96:99], a[20:23], a[220:223], 0\n"
        "v_exp_f32 v80, v80\n"
        "v_mfma_f32_16x16x32_bf16 v[96:99], a[16:19], a[216:219], v[96:99]\n"
        "v_exp_f32 v81, v81\n"
        "v_mfma_f32_16x16x32_bf16 v[96:99], a[12:15], a[248:251], v[96:99]\n"
        "s_lshr_b32 s46, s56, 6\n"
        "v_cvt_pk_bf16_f32 v230, v104, v105\n"
        "v_cvt_pk_bf16_f32 v231, v106, v107\n"
        "v_mfma_f32_16x16x32_bf16 v[96:99], a[8:11], a[244:247], v[96:99]\n"
        "s_mul_i32 s46, s46, 1572864\n"
        "ds_write_b128 v236, v[92:95] offset:13440\n"
        "v_mfma_f32_16x16x32_bf16 v[96:99], a[4:7], a[240:243], v[96:99]\n"
        "s_add_u32 s46, s46, s37\n"
        "v_cvt_pk_bf16_f32 v77, v78, v79\n"
        "v_cvt_pk_bf16_f32 v80, v80, v81\n"
        "v_mfma_f32_16x16x32_bf16 v[96:99], a[0:3], a[236:239], v[96:99]\n"
        "s_cmp_eq_u32 s46, 100663296\n"
        "v_exp_f32 v82, v82\n"
        "v_mfma_f32_16x16x32_bf16 v[108:111], v[212:215], v[156:159], v[12:15]\n"
        "s_cselect_b32 s46, 0, s46\n"
        "v_exp_f32 v83, v83\n"
        "v_mfma_f32_16x16x32_bf16 v[108:111], v[228:231], v[164:167], v[108:111]\n"
        "s_and_b32 s43, s56, 63\n"
        "v_exp_f32 v84, v84\n"
        "v_mfma_f32_16x16x32_bf16 v[112:115], v[212:215], v[176:179], v[28:31]\n"
        "s_mul_i32 s43, s43, 3072\n"
        "v_mul_f32 v96, s50, v96\n"
        "v_mul_f32 v97, s50, v97\n"
        "v_mfma_f32_16x16x32_bf16 v[112:115], v[228:231], v[184:187], v[112:115]\n"
        "s_add_u32 s44, s43, 3072\n"
        "v_mul_f32 v98, s50, v98\n"
        "v_mul_f32 v99, s50, v99\n"
        "v_mfma_f32_16x16x32_bf16 v[116:119], v[212:215], v[196:199], v[44:47]\n"
        "s_waitcnt lgkmcnt(4)\n"
        "ds_write_b128 v237, v[120:123] offset:12288\n"
        "v_mfma_f32_16x16x32_bf16 v[116:119], v[228:231], v[204:207], v[116:119]\n"
        "s_cmp_eq_u32 s44, 196608\n"
        "v_cvt_pk_bf16_f32 v216, v96, v97\n"
        "v_cvt_pk_bf16_f32 v217, v98, v99\n"
        "v_mfma_f32_16x16x32_bf16 v[100:103], a[24:27], a[236:239], 0\n"
        "s_cselect_b32 s44, 0, s44\n"
        "v_cvt_pk_bf16_f32 v81, v82, v83\n"
        "v_add_u32 v240, s45, v240\n"
        "v_mfma_f32_16x16x32_bf16 v[100:103], a[28:31], a[240:243], v[100:103]\n"
        "s_add_u32 s39, s46, s44\n"
        "v_exp_f32 v85, v85\n"
        "v_mfma_f32_16x16x32_bf16 v[100:103], a[32:35], a[244:247], v[100:103]\n"
        "s_add_u32 s43, s43, s46\n"
        "v_exp_f32 v86, v86\n"
        "v_mfma_f32_16x16x32_bf16 v[100:103], a[36:39], a[248:251], v[100:103]\n"
        "s_add_u32 s38, s43, 1536\n"
        "ds_read_b128 v[222:225], v251 offset:4608\n"
        "v_cvt_pk_bf16_f32 v82, v84, v85\n"
        "v_mfma_f32_16x16x32_bf16 v[100:103], a[40:43], a[216:219], v[100:103]\n"
        "ds_write_b128 v237, v[124:127] offset:13056\n"
        "v_mfma_f32_16x16x32_bf16 v[100:103], a[44:47], a[220:223], v[100:103]\n"
        "v_exp_f32 v87, v87\n"
        "v_add_u32 v241, s45, v241\n"
        "v_cvt_pk_bf16_f32 v83, v86, v87\n"
        "v_mfma_f32_16x16x32_bf16 v[84:87], v[192:195], v[196:199], v[40:43]\n"
        "v_exp_f32 v108, v108\n"
        "v_mfma_f32_16x16x32_bf16 v[84:87], v[208:211], v[200:203], v[84:87]\n"
        "v_exp_f32 v109, v109\n"
        "v_mfma_f32_16x16x32_bf16 v[104:107], a[68:71], a[220:223], 0\n"
        "v_mul_f32 v100, s50, v100\n"
        "v_mul_f32 v101, s50, v101\n"
        "v_mfma_f32_16x16x32_bf16 v[104:107], a[64:67], a[216:219], v[104:107]\n"
        "v_mul_f32 v102, s50, v102\n"
        "v_mul_f32 v103, s50, v103\n"
        "v_mfma_f32_16x16x32_bf16 v[104:107], a[60:63], a[248:251], v[104:107]\n"
        "v_cvt_pk_bf16_f32 v218, v100, v101\n"
        "v_cvt_pk_bf16_f32 v219, v102, v103\n"
        "v_mfma_f32_16x16x32_bf16 v[104:107], a[56:59], a[244:247], v[104:107]\n"
        "s_waitcnt lgkmcnt(0)\n"
        "v_exp_f32 v110, v110\n"
        "v_mfma_f32_16x16x32_bf16 v[104:107], a[52:55], a[240:243], v[104:107]\n"
        "s_barrier\n"
        "v_exp_f32 v111, v111\n"
        "v_mfma_f32_16x16x32_bf16 v[104:107], a[48:51], a[236:239], v[104:107]\n"
        "v_exp_f32 v112, v112\n"
        "v_mfma_f32_16x16x32_bf16 v[96:99], a[236:239], a[144:147], 0\n"
        "v_exp_f32 v113, v113\n"
        "v_mfma_f32_16x16x32_bf16 v[96:99], a[240:243], a[148:151], v[96:99]\n"
        "v_exp_f32 v114, v114\n"
        "v_mfma_f32_16x16x32_bf16 v[96:99], a[244:247], a[152:155], v[96:99]\n"
        "v_exp_f32 v115, v115\n"
        "v_mfma_f32_16x16x32_bf16 v[96:99], a[248:251], a[156:159], v[96:99]\n"
        "v_mul_f32 v104, s50, v104\n"
        "v_mul_f32 v105, s50, v105\n"
        "v_mfma_f32_16x16x32_bf16 v[96:99], a[216:219], a[160:163], v[96:99]\n"
        "v_mul_f32 v106, s50, v106\n"
        "v_mul_f32 v107, s50, v107\n"
        "v_mfma_f32_16x16x32_bf16 v[96:99], a[220:223], a[164:167], v[96:99]\n"
        "v_cvt_pk_bf16_f32 v220, v104, v105\n"
        "v_cvt_pk_bf16_f32 v221, v106, v107\n"
        "v_mfma_f32_16x16x32_bf16 v[88:91], v[152:155], v[216:219], v[48:51]\n"
        "ds_read_b128 v[156:159], v247 offset:0\n"
        "v_mov_b64 v[226:227], v[220:221]\n"
        "v_mfma_f32_16x16x32_bf16 v[88:91], v[168:171], v[220:223], v[88:91]\n"
        "ds_read_b128 v[164:167], v247 offset:4096\n"
        "v_cvt_pk_bf16_f32 v70, v108, v109\n"
        "v_mfma_f32_16x16x32_bf16 v[92:95], v[172:175], v[216:219], v[52:55]\n"
        "s_mov_b32 s56, s57\n"
        "ds_read_b128 v[160:163], v247 offset:256\n"
        "v_cvt_pk_bf16_f32 v134, v96, v97\n"
        "v_mfma_f32_16x16x32_bf16 v[92:95], v[188:191], v[224:227], v[92:95]\n"
        "s_mov_b32 s57, s58\n"
        "ds_read_b128 v[222:225], v252 offset:4608\n"
        "v_cvt_pk_bf16_f32 v135, v98, v99\n"
        "v_mfma_f32_16x16x32_bf16 v[100:103], a[220:223], a[188:191], 0\n"
        "s_xor_b32 s48, s48, 8192\n"
        "ds_read_b128 v[152:155], v247 offset:4352\n"
        "v_cvt_pk_bf16_f32 v71, v110, v111\n"
        "v_mfma_f32_16x16x32_bf16 v[100:103], a[216:219], a[184:187], v[100:103]\n"
        "v_exp_f32 v116, v116\n"
        "v_mfma_f32_16x16x32_bf16 v[100:103], a[248:251], a[180:183], v[100:103]\n"
        "v_exp_f32 v117, v117\n"
        "v_mfma_f32_16x16x32_bf16 v[100:103], a[244:247], a[176:179], v[100:103]\n"
        "v_exp_f32 v118, v118\n"
        "v_mfma_f32_16x16x32_bf16 v[100:103], a[240:243], a[172:175], v[100:103]\n"
        "v_exp_f32 v119, v119\n"
        "v_mfma_f32_16x16x32_bf16 v[100:103], a[236:239], a[168:171], v[100:103]\n"
        "v_exp_f32 v84, v84\n"
        "v_mfma_f32_16x16x32_bf16 v[104:107], a[236:239], a[192:195], 0\n"
        "ds_read_b128 a[236:239], v248 offset:0\n"
        "v_cvt_pk_bf16_f32 v78, v112, v113\n"
        "v_mfma_f32_16x16x32_bf16 v[104:107], a[240:243], a[196:199], v[104:107]\n"
        "ds_read_b128 a[240:243], v248 offset:6144\n"
        "v_cvt_pk_bf16_f32 v79, v114, v115\n"
        "v_mfma_f32_16x16x32_bf16 v[104:107], a[244:247], a[200:203], v[104:107]\n"
        "ds_read_b128 a[244:247], v254 offset:16\n"
        "v_cvt_pk_bf16_f32 v142, v100, v101\n"
        "v_mfma_f32_16x16x32_bf16 v[104:107], a[248:251], a[204:207], v[104:107]\n"
        "ds_read_b128 a[248:251], v254 offset:6160\n"
        "v_cvt_pk_bf16_f32 v143, v102, v103\n"
        "v_mfma_f32_16x16x32_bf16 v[104:107], a[216:219], a[208:211], v[104:107]\n"
        "v_exp_f32 v85, v85\n"
        "v_mfma_f32_16x16x32_bf16 v[104:107], a[220:223], a[212:215], v[104:107]\n"
        "v_exp_f32 v86, v86\n"
        "v_mfma_f32_16x16x32_bf16 v[108:111], v[192:195], v[216:219], v[56:59]\n"
        "s_waitcnt lgkmcnt(5)\n"
        "ds_read_b128 a[216:219], v248 offset:32\n"
        "v_cvt_pk_bf16_f32 v84, v84, v85\n"
        "v_mfma_f32_16x16x32_bf16 v[108:111], v[208:211], v[220:223], v[108:111]\n"
        "v_exp_f32 v87, v87\n"
        "v_mfma_f32_16x16x32_bf16 v[112:115], v[212:215], v[216:219], v[60:63]\n"
        "ds_read_b128 a[220:223], v248 offset:6176\n"
        "v_cvt_pk_bf16_f32 v150, v104, v105\n"
        "v_mfma_f32_16x16x32_bf16 v[112:115], v[228:231], v[224:227], v[112:115]\n"
        "s_waitcnt lgkmcnt(5)\n"
        "ds_read_b128 a[224:227], v254 offset:48\n"
        "v_cvt_pk_bf16_f32 v151, v106, v107\n"
        "v_mfma_f32_16x16x32_bf16 v[172:175], v[156:159], a[236:239], 0\n"
        "s_waitcnt lgkmcnt(5)\n"
        "ds_read_b128 a[228:231], v254 offset:6192\n"
        "v_cvt_pk_bf16_f32 v85, v86, v87\n"
        "v_mfma_f32_16x16x32_bf16 v[172:175], v[164:167], a[240:243], v[172:175]\n"
        "v_cvt_pk_bf16_f32 v86, v116, v117\n"
        "v_mov_b64 v[116:117], 0\n"
        "v_mfma_f32_16x16x32_bf16 v[200:203], v[152:155], a[240:243], 0\n"
        "ds_read_b128 a[232:235], v248 offset:64\n"
        "v_cvt_pk_bf16_f32 v87, v118, v119\n"
        "v_mfma_f32_16x16x32_bf16 v[200:203], v[160:163], a[236:239], v[200:203]\n"
        "v_exp_f32 v88, v88\n"
        "v_smfmac_f32_16x16x64_bf16 v[116:119], a[252:255], v[64:71], v255\n"
        "v_exp_f32 v89, v89\n"
        "v_mfma_f32_16x16x32_bf16 v[120:123], v[128:131], v[64:67], 0\n"
        "v_exp_f32 v90, v90\n"
        "v_mfma_f32_16x16x32_bf16 v[120:123], v[132:135], v[68:71], v[120:123]\n"
        "ds_read_b128 a[236:239], v248 offset:6208\n"
        "v_cvt_pk_bf16_f32 v88, v88, v89\n"
        "v_mfma_f32_16x16x32_bf16 v[124:127], v[140:143], v[68:71], 0\n"
        "v_exp_f32 v91, v91\n"
        "v_mfma_f32_16x16x32_bf16 v[124:127], v[136:139], v[64:67], v[124:127]\n"
        "v_cvt_pk_bf16_f32 v89, v90, v91\n"
        "v_add_f32 v116, v116, v117\n"
        "v_mfma_f32_16x16x32_bf16 v[96:99], v[144:147], v[64:67], 0\n"
        "v_exp_f32 v92, v92\n"
        "v_mfma_f32_16x16x32_bf16 v[96:99], v[148:151], v[68:71], v[96:99]\n"
        "s_waitcnt lgkmcnt(7)\n"
        "v_exp_f32 v93, v93\n"
        "v_mfma_f32_16x16x32_bf16 v[204:207], v[160:163], a[244:247], 0\n"
        "s_waitcnt lgkmcnt(6)\n"
        "v_exp_f32 v94, v94\n"
        "v_mfma_f32_16x16x32_bf16 v[204:207], v[152:155], a[248:251], v[204:207]\n"
        "ds_read_b128 a[240:243], v254 offset:80\n"
        "v_cvt_pk_bf16_f32 v90, v92, v93\n"
        "v_mfma_f32_16x16x32_bf16 v[176:179], v[164:167], a[248:251], 0\n"
        "v_exp_f32 v95, v95\n"
        "v_mfma_f32_16x16x32_bf16 v[176:179], v[156:159], a[244:247], v[176:179]\n"
        "s_waitcnt lgkmcnt(6)\n"
        "ds_read_b128 a[244:247], v254 offset:6224\n"
        "v_cvt_pk_bf16_f32 v91, v94, v95\n"
        "v_mfma_f32_16x16x32_bf16 v[180:183], v[156:159], a[216:219], 0\n"
        "s_waitcnt lgkmcnt(6)\n"
        "ds_read_b128 a[248:251], v248 offset:36864\n"
        "v_mov_b64 v[100:101], 0\n"
        "v_mfma_f32_16x16x32_bf16 v[180:183], v[164:167], a[220:223], v[180:183]\n"
        "v_exp_f32 v108, v108\n"
        "v_mfma_f32_16x16x32_bf16 v[212:215], v[152:155], a[220:223], 0\n"
        "v_exp_f32 v109, v109\n"
        "v_mfma_f32_16x16x32_bf16 v[212:215], v[160:163], a[216:219], v[212:215]\n"
        "s_waitcnt lgkmcnt(6)\n"
        "v_exp_f32 v110, v110\n"
        "v_mfma_f32_16x16x32_bf16 v[216:219], v[160:163], a[224:227], 0\n"
        "s_waitcnt lgkmcnt(5)\n"
        "v_rcp_f32 v117, v116\n"
        "v_mfma_f32_16x16x32_bf16 v[216:219], v[152:155], a[228:231], v[216:219]\n"
        "v_cvt_pk_bf16_f32 v92, v108, v109\n"
        "v_mul_f32 v120, v117, v120\n"
        "v_mfma_f32_16x16x32_bf16 v[184:187], v[164:167], a[228:231], 0\n"
        "v_exp_f32 v111, v111\n"
        "v_mfma_f32_16x16x32_bf16 v[184:187], v[156:159], a[224:227], v[184:187]\n"
        "s_waitcnt lgkmcnt(4)\n"
        "v_mul_f32 v121, v117, v121\n"
        "v_mul_f32 v122, v117, v122\n"
        "v_mfma_f32_16x16x32_bf16 v[192:195], v[156:159], a[232:235], 0\n"
        "s_waitcnt lgkmcnt(3)\n"
        "v_mul_f32 v123, v117, v123\n"
        "v_cvt_pk_bf16_f32 v93, v110, v111\n"
        "v_mfma_f32_16x16x32_bf16 v[192:195], v[164:167], a[236:239], v[192:195]\n"
        "v_exp_f32 v112, v112\n"
        "v_mfma_f32_16x16x32_bf16 v[220:223], v[152:155], a[236:239], 0\n"
        "v_cvt_pk_bf16_f32 v120, v120, v121\n"
        "v_cvt_pk_bf16_f32 v121, v122, v123\n"
        "v_mfma_f32_16x16x32_bf16 v[220:223], v[160:163], a[232:235], v[220:223]\n"
        "ds_read_b128 v[232:235], v247 offset:8192\n"
        "v_mul_f32 v124, v117, v124\n"
        "v_smfmac_f32_16x16x64_bf16 v[100:103], a[252:255], v[72:79], v255\n"
        "43:\n"
        "s_cbranch_execz 46f\n"
        "ds_read_b128 v[64:67], v247 offset:12288\n"
        "v_mul_f32 v125, v117, v125\n"
        "v_mfma_f32_16x16x32_bf16 v[104:107], v[128:131], v[72:75], 0\n"
        "ds_read_b128 v[236:239], v247 offset:8448\n"
        "v_maximum3_f32 v253, v253, v116, v117\n"
        "v_mfma_f32_16x16x32_bf16 v[104:107], v[132:135], v[76:79], v[104:107]\n"
        "ds_read_b128 v[68:71], v247 offset:12544\n"
        "ds_read_b128 a[216:219], v248 offset:43008\n"
        "v_mfma_f32_16x16x32_bf16 v[108:111], v[140:143], v[76:79], 0\n"
        "v_exp_f32 v113, v113\n"
        "v_mfma_f32_16x16x32_bf16 v[108:111], v[136:139], v[72:75], v[108:111]\n"
        "ds_write_b32 v245, v120 offset:0\n"
        "v_exp_f32 v114, v114\n"
        "v_cvt_pk_bf16_f32 v94, v112, v113\n"
        "v_exp_f32 v115, v115\n"
        "v_mul_f32 v126, v117, v126\n"
        "v_cvt_pk_bf16_f32 v95, v114, v115\n"
        "v_mfma_f32_16x16x32_bf16 v[112:115], v[144:147], v[72:75], 0\n"
        "ds_read_b128 a[220:223], v254 offset:36880\n"
        "v_mul_f32 v127, v117, v127\n"
        "v_mfma_f32_16x16x32_bf16 v[112:115], v[148:151], v[76:79], v[112:115]\n"
        "s_waitcnt lgkmcnt(9)\n"
        "ds_read_b128 a[224:227], v254 offset:43024\n"
        "v_cvt_pk_bf16_f32 v124, v124, v125\n"
        "v_mfma_f32_16x16x32_bf16 v[224:227], v[160:163], a[240:243], 0\n"
        "s_waitcnt lgkmcnt(9)\n"
        "ds_write_b32 v245, v121 offset:4\n"
        "v_mfma_f32_16x16x32_bf16 v[224:227], v[152:155], a[244:247], v[224:227]\n"
        "v_cvt_pk_bf16_f32 v125, v126, v127\n"
        "v_mul_f32 v96, v117, v96\n"
        "v_mfma_f32_16x16x32_bf16 v[196:199], v[164:167], a[244:247], 0\n"
        "ds_read_b128 a[228:231], v248 offset:36896\n"
        "v_mul_f32 v97, v117, v97\n"
        "v_mfma_f32_16x16x32_bf16 v[196:199], v[156:159], a[240:243], v[196:199]\n"
        "s_waitcnt lgkmcnt(9)\n"
        "ds_read_b128 a[232:235], v248 offset:43040\n"
        "v_mul_f32 v98, v117, v98\n"
        "v_mfma_f32_16x16x32_bf16 v[172:175], v[232:235], a[248:251], v[172:175]\n"
        "s_waitcnt lgkmcnt(6)\n"
        "ds_write_b32 v245, v124 offset:8\n"
        "v_mfma_f32_16x16x32_bf16 v[172:175], v[64:67], a[216:219], v[172:175]\n"
        "ds_write_b32 v245, v125 offset:12\n"
        "v_mfma_f32_16x16x32_bf16 v[200:203], v[68:71], a[216:219], v[200:203]\n"
        "ds_read_b128 a[236:239], v254 offset:36912\n"
        "v_mul_f32 v99, v117, v99\n"
        "v_mfma_f32_16x16x32_bf16 v[200:203], v[236:239], a[248:251], v[200:203]\n"
        "s_waitcnt lgkmcnt(7)\n"
        "ds_read_b128 a[240:243], v254 offset:43056\n"
        "v_cvt_pk_bf16_f32 v96, v96, v97\n"
        "v_mfma_f32_16x16x32_bf16 v[204:207], v[236:239], a[220:223], v[204:207]\n"
        "s_waitcnt lgkmcnt(7)\n"
        "v_cvt_pk_bf16_f32 v97, v98, v99\n"
        "v_add_f32 v100, v100, v101\n"
        "v_mfma_f32_16x16x32_bf16 v[204:207], v[68:71], a[224:227], v[204:207]\n"
        "ds_write_b32 v246, v96 offset:0\n"
        "v_mfma_f32_16x16x32_bf16 v[176:179], v[64:67], a[224:227], v[176:179]\n"
        "ds_write_b32 v246, v97 offset:4\n"
        "v_mfma_f32_16x16x32_bf16 v[176:179], v[232:235], a[220:223], v[176:179]\n"
        "s_waitcnt lgkmcnt(7)\n"
        "v_rcp_f32 v101, v100\n"
        "v_mfma_f32_16x16x32_bf16 v[180:183], v[232:235], a[228:231], v[180:183]\n"
        "s_waitcnt lgkmcnt(6)\n"
        "v_mul_f32 v104, v101, v104\n"
        "v_mul_f32 v105, v101, v105\n"
        "v_mfma_f32_16x16x32_bf16 v[180:183], v[64:67], a[232:235], v[180:183]\n"
        "v_mov_b64 v[116:117], 0\n"
        "v_mul_f32 v106, v101, v106\n"
        "v_mfma_f32_16x16x32_bf16 v[212:215], v[68:71], a[232:235], v[212:215]\n"
        "v_mul_f32 v107, v101, v107\n"
        "v_cvt_pk_bf16_f32 v104, v104, v105\n"
        "v_mfma_f32_16x16x32_bf16 v[212:215], v[236:239], a[228:231], v[212:215]\n"
        "s_waitcnt lgkmcnt(3)\n"
        "v_cvt_pk_bf16_f32 v105, v106, v107\n"
        "v_mul_f32 v108, v101, v108\n"
        "v_mfma_f32_16x16x32_bf16 v[216:219], v[236:239], a[236:239], v[216:219]\n"
        "s_waitcnt lgkmcnt(2)\n"
        "ds_write_b32 v245, v104 offset:256\n"
        "v_mfma_f32_16x16x32_bf16 v[216:219], v[68:71], a[240:243], v[216:219]\n"
        "ds_read_b128 a[244:247], v248 offset:36928\n"
        "v_mul_f32 v109, v101, v109\n"
        "v_mfma_f32_16x16x32_bf16 v[184:187], v[64:67], a[240:243], v[184:187]\n"
        "ds_read_b128 a[248:251], v248 offset:43072\n"
        "v_maximum3_f32 v253, v253, v100, v101\n"
        "v_mfma_f32_16x16x32_bf16 v[184:187], v[232:235], a[236:239], v[184:187]\n"
        "ds_write_b32 v245, v105 offset:260\n"
        "v_smfmac_f32_16x16x64_bf16 v[116:119], a[252:255], v[80:87], v255\n"
        "v_mul_f32 v110, v101, v110\n"
        "v_mul_f32 v111, v101, v111\n"
        "v_mfma_f32_16x16x32_bf16 v[120:123], v[128:131], v[80:83], 0\n"
        "ds_read_b128 a[216:219], v254 offset:36944\n"
        "v_cvt_pk_bf16_f32 v108, v108, v109\n"
        "v_mfma_f32_16x16x32_bf16 v[120:123], v[132:135], v[84:87], v[120:123]\n"
        "ds_read_b128 a[220:223], v254 offset:43088\n"
        "v_cvt_pk_bf16_f32 v109, v110, v111\n"
        "v_mfma_f32_16x16x32_bf16 v[124:127], v[140:143], v[84:87], 0\n"
        "ds_read_b128 v[156:159], v247 offset:16384\n"
        "v_mul_f32 v112, v101, v112\n"
        "v_mfma_f32_16x16x32_bf16 v[124:127], v[136:139], v[80:83], v[124:127]\n"
        "ds_read_b128 v[164:167], v247 offset:20480\n"
        "v_mul_f32 v113, v101, v113\n"
        "v_mfma_f32_16x16x32_bf16 v[96:99], v[144:147], v[80:83], 0\n"
        "ds_read_b128 v[160:163], v247 offset:16640\n"
        "ds_read_b128 a[224:227], v248 offset:12288\n"
        "v_mfma_f32_16x16x32_bf16 v[96:99], v[148:151], v[84:87], v[96:99]\n"
        "s_waitcnt lgkmcnt(8)\n"
        "ds_read_b128 v[152:155], v247 offset:20736\n"
        "ds_read_b128 a[228:231], v248 offset:49152\n"
        "v_mfma_f32_16x16x32_bf16 v[192:195], v[232:235], a[244:247], v[192:195]\n"
        "s_waitcnt lgkmcnt(9)\n"
        "ds_write_b32 v245, v108 offset:264\n"
        "v_mfma_f32_16x16x32_bf16 v[192:195], v[64:67], a[248:251], v[192:195]\n"
        "ds_write_b32 v245, v109 offset:268\n"
        "v_mfma_f32_16x16x32_bf16 v[220:223], v[68:71], a[248:251], v[220:223]\n"
        "ds_read_b128 a[232:235], v254 offset:12304\n"
        "v_mul_f32 v114, v101, v114\n"
        "v_mfma_f32_16x16x32_bf16 v[220:223], v[236:239], a[244:247], v[220:223]\n"
        "s_waitcnt lgkmcnt(10)\n"
        "ds_read_b128 a[236:239], v254 offset:49168\n"
        "v_mul_f32 v115, v101, v115\n"
        "v_mfma_f32_16x16x32_bf16 v[224:227], v[236:239], a[216:219], v[224:227]\n"
        "s_waitcnt lgkmcnt(10)\n"
        "ds_read_b128 a[240:243], v248 offset:12320\n"
        "v_cvt_pk_bf16_f32 v112, v112, v113\n"
        "v_mfma_f32_16x16x32_bf16 v[224:227], v[68:71], a[220:223], v[224:227]\n"
        "v_cvt_pk_bf16_f32 v113, v114, v115\n"
        "v_add_f32 v116, v116, v117\n"
        "v_mfma_f32_16x16x32_bf16 v[196:199], v[64:67], a[220:223], v[196:199]\n"
        "ds_write_b32 v246, v112 offset:256\n"
        "v_mfma_f32_16x16x32_bf16 v[196:199], v[232:235], a[216:219], v[196:199]\n"
        "s_waitcnt lgkmcnt(8)\n"
        "ds_read_b128 a[244:247], v248 offset:49184\n"
        "v_add_u32 v247, s31, v247\n"
        "v_mfma_f32_16x16x32_bf16 v[172:175], v[156:159], a[224:227], v[172:175]\n"
        "s_waitcnt lgkmcnt(7)\n"
        "ds_write_b32 v246, v113 offset:260\n"
        "v_mfma_f32_16x16x32_bf16 v[172:175], v[164:167], a[228:231], v[172:175]\n"
        "v_rcp_f32 v117, v116\n"
        "v_mfma_f32_16x16x32_bf16 v[200:203], v[152:155], a[228:231], v[200:203]\n"
        "ds_read_b128 a[248:251], v254 offset:12336\n"
        "v_mul_f32 v120, v117, v120\n"
        "v_mfma_f32_16x16x32_bf16 v[200:203], v[160:163], a[224:227], v[200:203]\n"
        "s_waitcnt lgkmcnt(6)\n"
        "ds_read_b128 a[216:219], v254 offset:49200\n"
        "v_mul_f32 v121, v117, v121\n"
        "v_mfma_f32_16x16x32_bf16 v[204:207], v[160:163], a[232:235], v[204:207]\n"
        "s_waitcnt lgkmcnt(6)\n"
        "v_mul_f32 v122, v117, v122\n"
        "v_mul_f32 v123, v117, v123\n"
        "v_mfma_f32_16x16x32_bf16 v[204:207], v[152:155], a[236:239], v[204:207]\n"
        "v_cvt_pk_bf16_f32 v120, v120, v121\n"
        "v_cvt_pk_bf16_f32 v121, v122, v123\n"
        "v_mfma_f32_16x16x32_bf16 v[176:179], v[164:167], a[236:239], v[176:179]\n"
        "ds_read_b128 a[220:223], v248 offset:12352\n"
        "v_mul_f32 v124, v117, v124\n"
        "v_mfma_f32_16x16x32_bf16 v[176:179], v[156:159], a[232:235], v[176:179]\n"
        "s_waitcnt lgkmcnt(6)\n"
        "ds_read_b128 a[224:227], v248 offset:49216\n"
        "v_mul_f32 v125, v117, v125\n"
        "v_mfma_f32_16x16x32_bf16 v[180:183], v[156:159], a[240:243], v[180:183]\n"
        "s_waitcnt lgkmcnt(5)\n"
        "ds_write_b32 v245, v120 offset:512\n"
        "v_mfma_f32_16x16x32_bf16 v[180:183], v[164:167], a[244:247], v[180:183]\n"
        "ds_write_b32 v245, v121 offset:516\n"
        "v_mfma_f32_16x16x32_bf16 v[212:215], v[152:155], a[244:247], v[212:215]\n"
        "ds_read_b128 a[228:231], v254 offset:12368\n"
        "v_maximum3_f32 v253, v253, v116, v117\n"
        "v_mfma_f32_16x16x32_bf16 v[212:215], v[160:163], a[240:243], v[212:215]\n"
        "s_waitcnt lgkmcnt(6)\n"
        "ds_read_b128 a[232:235], v254 offset:49232\n"
        "v_mul_f32 v126, v117, v126\n"
        "v_mfma_f32_16x16x32_bf16 v[216:219], v[160:163], a[248:251], v[216:219]\n"
        "s_waitcnt lgkmcnt(6)\n"
        "v_mul_f32 v127, v117, v127\n"
        "v_cvt_pk_bf16_f32 v124, v124, v125\n"
        "v_mfma_f32_16x16x32_bf16 v[216:219], v[152:155], a[216:219], v[216:219]\n"
        "v_cvt_pk_bf16_f32 v125, v126, v127\n"
        "v_mul_f32 v96, v117, v96\n"
        "v_mfma_f32_16x16x32_bf16 v[184:187], v[164:167], a[216:219], v[184:187]\n"
        "s_waitcnt vmcnt(0) lgkmcnt(5)\n"
        "ds_write_b32 v245, v124 offset:520\n"
        "v_mfma_f32_16x16x32_bf16 v[184:187], v[156:159], a[248:251], v[184:187]\n"
        "s_barrier\n"
        "ds_write_b32 v245, v125 offset:524\n"
        "v_mfma_f32_16x16x32_bf16 v[192:195], v[156:159], a[220:223], v[192:195]\n"
        "s_waitcnt lgkmcnt(6)\n"
        "v_mul_f32 v97, v117, v97\n"
        "v_mul_f32 v98, v117, v98\n"
        "v_mfma_f32_16x16x32_bf16 v[192:195], v[164:167], a[224:227], v[192:195]\n"
        "v_mul_f32 v99, v117, v99\n"
        "v_cvt_pk_bf16_f32 v96, v96, v97\n"
        "v_mfma_f32_16x16x32_bf16 v[220:223], v[152:155], a[224:227], v[220:223]\n"
        "v_cvt_pk_bf16_f32 v97, v98, v99\n"
        "v_mov_b64 v[80:81], 0\n"
        "v_mfma_f32_16x16x32_bf16 v[220:223], v[160:163], a[220:223], v[220:223]\n"
        "s_waitcnt lgkmcnt(3)\n"
        "ds_write_b32 v246, v96 offset:512\n"
        "v_mfma_f32_16x16x32_bf16 v[224:227], v[160:163], a[228:231], v[224:227]\n"
        "s_waitcnt lgkmcnt(3)\n"
        "ds_write_b32 v246, v97 offset:516\n"
        "v_mfma_f32_16x16x32_bf16 v[224:227], v[152:155], a[232:235], v[224:227]\n"
        "ds_read_b128 a[236:239], v240 offset:0\n"
        "v_mfma_f32_16x16x32_bf16 v[196:199], v[164:167], a[232:235], v[196:199]\n"
        "ds_read_b128 a[240:243], v241 offset:0\n"
        "v_mfma_f32_16x16x32_bf16 v[196:199], v[156:159], a[228:231], v[196:199]\n"
        "ds_read_b128 a[244:247], v240 offset:128\n"
        "v_smfmac_f32_16x16x64_bf16 v[80:83], a[252:255], v[88:95], v255\n"
        "ds_read_b128 a[248:251], v241 offset:128\n"
        "v_mfma_f32_16x16x32_bf16 v[84:87], v[128:131], v[88:91], 0\n"
        "s_min_u32 s58, s63, s59\n"
        "ds_read_b128 a[216:219], v240 offset:256\n"
        "v_mfma_f32_16x16x32_bf16 v[84:87], v[132:135], v[92:95], v[84:87]\n"
        "ds_read_b128 a[220:223], v241 offset:256\n"
        "v_mfma_f32_16x16x32_bf16 v[232:235], v[140:143], v[92:95], 0\n"
        "v_mfma_f32_16x16x32_bf16 v[232:235], v[136:139], v[88:91], v[232:235]\n"
        "v_mfma_f32_16x16x32_bf16 v[236:239], v[144:147], v[88:91], 0\n"
        "v_mfma_f32_16x16x32_bf16 v[236:239], v[148:151], v[92:95], v[236:239]\n"
        "s_waitcnt lgkmcnt(0)\n"
        "s_branch 11f\n"
        ".p2align 6\n"
        "10:\n"
        "v_mfma_f32_16x16x32_bf16 v[232:235], v[136:139], v[88:91], v[232:235]\n"
        "s_cmp_ge_u32 s58, 3584\n"
        "v_mov_b32 v120, 1\n"
        "v_mfma_f32_16x16x32_bf16 v[236:239], v[144:147], v[88:91], 0\n"
        "s_cselect_b32 s6, s3, 0\n"
        "buffer_atomic_add v120, v249, s[4:7], 0 offen sc0 sc1\n"
        "v_mfma_f32_16x16x32_bf16 v[236:239], v[148:151], v[92:95], v[236:239]\n"
        "11:\n"
        "s_waitcnt lgkmcnt(5)\n"
        "v_mfma_f32_16x16x32_bf16 v[96:99], a[72:75], a[236:239], 0\n"
        "s_waitcnt lgkmcnt(4)\n"
        "v_and_b32 v117, 60, v250\n"
        "v_add3_u32 v117, v250, v117, s26\n"
        "v_mfma_f32_16x16x32_bf16 v[96:99], a[76:79], a[240:243], v[96:99]\n"
        "s_waitcnt lgkmcnt(3)\n"
        "v_cvt_pk_bf16_f32 v108, v172, v176\n"
        "v_cvt_pk_bf16_f32 v109, v180, v184\n"
        "v_mfma_f32_16x16x32_bf16 v[96:99], a[80:83], a[244:247], v[96:99]\n"
        "s_waitcnt lgkmcnt(2)\n"
        "v_cvt_pk_bf16_f32 v110, v192, v196\n"
        "v_add_f32 v80, v80, v81\n"
        "v_mfma_f32_16x16x32_bf16 v[96:99], a[84:87], a[248:251], v[96:99]\n"
        "s_waitcnt lgkmcnt(1)\n"
        "buffer_store_dwordx2 v[108:109], v117, s[16:19], s38 offen offset:0 sc1\n"
        "v_mfma_f32_16x16x32_bf16 v[96:99], a[88:91], a[216:219], v[96:99]\n"
        "s_waitcnt lgkmcnt(0)\n"
        "v_rcp_f32 v81, v80\n"
        "v_mfma_f32_16x16x32_bf16 v[96:99], a[92:95], a[220:223], v[96:99]\n"
        "s_add_u32 s41, s29, s40\n"
        "v_cvt_pk_bf16_f32 v112, v173, v177\n"
        "v_cvt_pk_bf16_f32 v113, v181, v185\n"
        "v_mfma_f32_16x16x32_bf16 v[100:103], a[116:119], a[220:223], 0\n"
        "s_lshr_b32 s47, s58, 6\n"
        "v_mul_f32 v84, v81, v84\n"
        "v_mul_f32 v85, v81, v85\n"
        "v_mfma_f32_16x16x32_bf16 v[100:103], a[112:115], a[216:219], v[100:103]\n"
        "s_mul_i32 s47, s47, 1572864\n"
        "v_cvt_pk_bf16_f32 v152, v96, v97\n"
        "v_cvt_pk_bf16_f32 v153, v98, v99\n"
        "v_mfma_f32_16x16x32_bf16 v[100:103], a[108:111], a[248:251], v[100:103]\n"
        "s_add_u32 s47, s47, s24\n"
        "buffer_store_dword v110, v250, s[16:19], s38 offen offset:0 sc1\n"
        "v_mfma_f32_16x16x32_bf16 v[100:103], a[104:107], a[244:247], v[100:103]\n"
        "s_lshl_b32 s36, s58, 13\n"
        "v_cvt_pk_bf16_f32 v114, v193, v197\n"
        "v_mul_f32 v86, v81, v86\n"
        "v_mfma_f32_16x16x32_bf16 v[100:103], a[100:103], a[240:243], v[100:103]\n"
        "s_sub_u32 s31, s28, s30\n"
        "v_mul_f32 v87, v81, v87\n"
        "v_cvt_pk_bf16_f32 v84, v84, v85\n"
        "v_mfma_f32_16x16x32_bf16 v[100:103], a[96:99], a[236:239], v[100:103]\n"
        "s_sub_u32 s45, s29, s28\n"
        "ds_write_b32 v245, v84 offset:768\n"
        "v_mfma_f32_16x16x32_bf16 v[104:107], a[120:123], a[236:239], 0\n"
        "s_mov_b32 s29, s30\n"
        "v_cvt_pk_bf16_f32 v85, v86, v87\n"
        "v_mul_f32 v232, v81, v232\n"
        "v_mfma_f32_16x16x32_bf16 v[104:107], a[124:127], a[240:243], v[104:107]\n"
        "s_mov_b32 s30, s28\n"
        "buffer_store_dwordx2 v[112:113], v117, s[16:19], s38 offen offset:384 sc1\n"
        "v_mfma_f32_16x16x32_bf16 v[104:107], a[128:131], a[244:247], v[104:107]\n"
        "s_add_u32 s28, s28, s45\n"
        "v_cvt_pk_bf16_f32 v154, v100, v101\n"
        "v_cvt_pk_bf16_f32 v155, v102, v103\n"
        "v_mfma_f32_16x16x32_bf16 v[104:107], a[132:135], a[248:251], v[104:107]\n"
        "s_add_u32 s36, s36, s34\n"
        "ds_write_b32 v245, v85 offset:772\n"
        "v_mfma_f32_16x16x32_bf16 v[104:107], a[136:139], a[216:219], v[104:107]\n"
        "s_sub_u32 s62, s58, s57\n"
        "v_cvt_pk_bf16_f32 v108, v174, v178\n"
        "v_cvt_pk_bf16_f32 v109, v182, v186\n"
        "v_mfma_f32_16x16x32_bf16 v[104:107], a[140:143], a[220:223], v[104:107]\n"
        "s_and_b32 s62, s62, 63\n"
        "v_mul_f32 v233, v81, v233\n"
        "v_maximum3_f32 v253, v253, v80, v81\n"
        "v_mfma_f32_16x16x32_bf16 v[96:99], a[20:23], a[220:223], 0\n"
        "s_mul_i32 s62, s62, 3072\n"
        "buffer_store_dword v114, v250, s[16:19], s38 offen offset:384 sc1\n"
        "v_mfma_f32_16x16x32_bf16 v[96:99], a[16:19], a[216:219], v[96:99]\n"
        "s_add_u32 s35, s47, 786432\n"
        "v_cvt_pk_bf16_f32 v110, v194, v198\n"
        "v_mul_f32 v234, v81, v234\n"
        "v_mfma_f32_16x16x32_bf16 v[96:99], a[12:15], a[248:251], v[96:99]\n"
        "s_sub_u32 s46, s35, 100663296\n"
        "v_cvt_pk_bf16_f32 v168, v104, v105\n"
        "v_cvt_pk_bf16_f32 v169, v106, v107\n"
        "v_mfma_f32_16x16x32_bf16 v[96:99], a[8:11], a[244:247], v[96:99]\n"
        "s_cmp_lt_u32 s46, 786432\n"
        "v_mul_f32 v235, v81, v235\n"
        "v_cvt_pk_bf16_f32 v232, v232, v233\n"
        "v_mfma_f32_16x16x32_bf16 v[96:99], a[4:7], a[240:243], v[96:99]\n"
        "s_cselect_b32 s35, s46, s35\n"
        "ds_write_b32 v245, v232 offset:776\n"
        "v_mfma_f32_16x16x32_bf16 v[96:99], a[0:3], a[236:239], v[96:99]\n"
        "buffer_store_dwordx2 v[108:109], v117, s[16:19], s38 offen offset:768 sc1\n"
        "v_mfma_f32_16x16x32_bf16 v[100:103], a[24:27], a[236:239], 0\n"
        "v_cvt_pk_bf16_f32 v112, v175, v179\n"
        "v_cvt_pk_bf16_f32 v113, v183, v187\n"
        "v_mfma_f32_16x16x32_bf16 v[100:103], a[28:31], a[240:243], v[100:103]\n"
        "v_cvt_pk_bf16_f32 v233, v234, v235\n"
        "v_xor_b32 v251, 0x2000, v251\n"
        "v_mfma_f32_16x16x32_bf16 v[100:103], a[32:35], a[244:247], v[100:103]\n"
        "v_mul_f32 v96, s50, v96\n"
        "v_mul_f32 v97, s50, v97\n"
        "v_mfma_f32_16x16x32_bf16 v[100:103], a[36:39], a[248:251], v[100:103]\n"
        "v_mul_f32 v98, s50, v98\n"
        "v_mul_f32 v99, s50, v99\n"
        "v_mfma_f32_16x16x32_bf16 v[100:103], a[40:43], a[216:219], v[100:103]\n"
        "v_cvt_pk_bf16_f32 v156, v96, v97\n"
        "v_cvt_pk_bf16_f32 v157, v98, v99\n"
        "v_mfma_f32_16x16x32_bf16 v[100:103], a[44:47], a[220:223], v[100:103]\n"
        "buffer_store_dword v110, v250, s[16:19], s38 offen offset:768 sc1\n"
        "v_mfma_f32_16x16x32_bf16 v[104:107], a[68:71], a[220:223], 0\n"
        "ds_write_b32 v245, v233 offset:780\n"
        "v_mfma_f32_16x16x32_bf16 v[104:107], a[64:67], a[216:219], v[104:107]\n"
        "v_cvt_pk_bf16_f32 v114, v195, v199\n"
        "v_xor_b32 v252, 0x2000, v252\n"
        "v_mfma_f32_16x16x32_bf16 v[104:107], a[60:63], a[248:251], v[104:107]\n"
        "ds_read_b128 a[224:227], v240 offset:1536\n"
        "v_mul_f32 v100, s50, v100\n"
        "v_mfma_f32_16x16x32_bf16 v[104:107], a[56:59], a[244:247], v[104:107]\n"
        "v_mul_f32 v101, s50, v101\n"
        "v_mul_f32 v102, s50, v102\n"
        "v_mfma_f32_16x16x32_bf16 v[104:107], a[52:55], a[240:243], v[104:107]\n"
        "v_mul_f32 v103, s50, v103\n"
        "v_cvt_pk_bf16_f32 v158, v100, v101\n"
        "v_mfma_f32_16x16x32_bf16 v[104:107], a[48:51], a[236:239], v[104:107]\n"
        "buffer_store_dwordx2 v[112:113], v117, s[16:19], s38 offen offset:1152 sc1\n"
        "v_mfma_f32_16x16x32_bf16 v[96:99], a[236:239], a[144:147], 0\n"
        "ds_read_b128 a[228:231], v241 offset:1536\n"
        "v_cvt_pk_bf16_f32 v159, v102, v103\n"
        "v_mfma_f32_16x16x32_bf16 v[96:99], a[240:243], a[148:151], v[96:99]\n"
        "ds_read_b128 a[232:235], v240 offset:1664\n"
        "v_cvt_pk_bf16_f32 v108, v200, v204\n"
        "v_mfma_f32_16x16x32_bf16 v[96:99], a[244:247], a[152:155], v[96:99]\n"
        "v_mul_f32 v104, s50, v104\n"
        "v_mul_f32 v105, s50, v105\n"
        "v_mfma_f32_16x16x32_bf16 v[96:99], a[248:251], a[156:159], v[96:99]\n"
        "buffer_store_dword v114, v250, s[16:19], s38 offen offset:1152 sc1\n"
        "v_mfma_f32_16x16x32_bf16 v[96:99], a[216:219], a[160:163], v[96:99]\n"
        "s_lshl_b64 s[60:61], s[60:61], 8\n"
        "v_mul_f32 v106, s50, v106\n"
        "v_mul_f32 v107, s50, v107\n"
        "v_mfma_f32_16x16x32_bf16 v[96:99], a[220:223], a[164:167], v[96:99]\n"
        "s_and_b32 s63, s58, 255\n"
        "v_cvt_pk_bf16_f32 v160, v104, v105\n"
        "v_cvt_pk_bf16_f32 v161, v106, v107\n"
        "v_mfma_f32_16x16x32_bf16 v[100:103], a[220:223], a[188:191], 0\n"
        "s_or_b32 s60, s60, s63\n"
        "v_mov_b64 v[166:167], v[160:161]\n"
        "v_cvt_pk_bf16_f32 v109, v212, v216\n"
        "v_mfma_f32_16x16x32_bf16 v[100:103], a[216:219], a[184:187], v[100:103]\n"
        "s_add_u32 s27, s27, 1\n"
        "buffer_store_dwordx2 v[108:109], v117, s[16:19], s39 offen offset:0 sc1\n"
        "v_mfma_f32_16x16x32_bf16 v[100:103], a[248:251], a[180:183], v[100:103]\n"
        "s_add_u32 s63, s58, 256\n"
        "v_cvt_pk_bf16_f32 v128, v96, v97\n"
        "v_cvt_pk_bf16_f32 v129, v98, v99\n"
        "v_mfma_f32_16x16x32_bf16 v[100:103], a[244:247], a[176:179], v[100:103]\n"
        "s_cmp_lt_u32 s27, 1024\n"
        "v_cvt_pk_bf16_f32 v110, v220, v224\n"
        "v_cvt_pk_bf16_f32 v112, v201, v205\n"
        "v_mfma_f32_16x16x32_bf16 v[100:103], a[240:243], a[172:175], v[100:103]\n"
        "s_cselect_b32 s49, 4096, 0\n"
        "v_cvt_pk_bf16_f32 v113, v213, v217\n"
        "v_mul_f32 v236, v81, v236\n"
        "v_mfma_f32_16x16x32_bf16 v[100:103], a[236:239], a[168:171], v[100:103]\n"
        "s_cmp_lt_u32 s63, 3840\n"
        "v_mul_f32 v237, v81, v237\n"
        "v_mul_f32 v238, v81, v238\n"
        "v_mfma_f32_16x16x32_bf16 v[104:107], a[236:239], a[192:195], 0\n"
        "s_cselect_b32 s63, s63, -1\n"
        "ds_read_b128 a[236:239], v241 offset:1664\n"
        "v_mul_f32 v239, v81, v239\n"
        "v_mfma_f32_16x16x32_bf16 v[104:107], a[240:243], a[196:199], v[104:107]\n"
        "ds_read_b128 a[240:243], v240 offset:1792\n"
        "v_cvt_pk_bf16_f32 v236, v236, v237\n"
        "v_mfma_f32_16x16x32_bf16 v[104:107], a[244:247], a[200:203], v[104:107]\n"
        "ds_read_b128 a[244:247], v241 offset:1792\n"
        "v_cvt_pk_bf16_f32 v136, v100, v101\n"
        "v_mfma_f32_16x16x32_bf16 v[104:107], a[248:251], a[204:207], v[104:107]\n"
        "buffer_store_dword v110, v250, s[16:19], s39 offen offset:0 sc1\n"
        "v_mfma_f32_16x16x32_bf16 v[104:107], a[216:219], a[208:211], v[104:107]\n"
        "v_cvt_pk_bf16_f32 v137, v102, v103\n"
        "v_cvt_pk_bf16_f32 v237, v238, v239\n"
        "v_mfma_f32_16x16x32_bf16 v[104:107], a[220:223], a[212:215], v[104:107]\n"
        "s_waitcnt lgkmcnt(5)\n"
        "buffer_store_dwordx2 v[112:113], v117, s[16:19], s39 offen offset:384 sc1\n"
        "v_mfma_f32_16x16x32_bf16 v[96:99], a[72:75], a[224:227], 0\n"
        "s_waitcnt lgkmcnt(4)\n"
        "ds_write_b32 v246, v236 offset:768\n"
        "v_mfma_f32_16x16x32_bf16 v[96:99], a[76:79], a[228:231], v[96:99]\n"
        "s_waitcnt lgkmcnt(4)\n"
        "ds_write_b32 v246, v237 offset:772\n"
        "v_mfma_f32_16x16x32_bf16 v[96:99], a[80:83], a[232:235], v[96:99]\n"
        "s_waitcnt lgkmcnt(4)\n"
        "ds_read_b128 v[162:165], v251 offset:0\n"
        "v_cvt_pk_bf16_f32 v114, v221, v225\n"
        "v_mfma_f32_16x16x32_bf16 v[96:99], a[84:87], a[236:239], v[96:99]\n"
        "s_waitcnt lgkmcnt(4)\n"
        "v_cvt_pk_bf16_f32 v144, v104, v105\n"
        "v_cvt_pk_bf16_f32 v145, v106, v107\n"
        "v_mfma_f32_16x16x32_bf16 v[96:99], a[88:91], a[240:243], v[96:99]\n"
        "s_waitcnt lgkmcnt(3)\n"
        "buffer_store_dword v114, v250, s[16:19], s39 offen offset:384 sc1\n"
        "v_mfma_f32_16x16x32_bf16 v[96:99], a[92:95], a[244:247], v[96:99]\n"
        "v_cvt_pk_bf16_f32 v108, v202, v206\n"
        "v_cvt_pk_bf16_f32 v109, v214, v218\n"
        "v_mfma_f32_16x16x32_bf16 v[100:103], a[116:119], a[244:247], 0\n"
        "v_cvt_pk_bf16_f32 v110, v222, v226\n"
        "v_add_u32 v245, s31, v245\n"
        "v_mfma_f32_16x16x32_bf16 v[100:103], a[112:115], a[240:243], v[100:103]\n"
        "v_add_u32 v246, s31, v246\n"
        "v_add_u32 v242, s62, v242\n"
        "v_mfma_f32_16x16x32_bf16 v[100:103], a[108:111], a[236:239], v[100:103]\n"
        "v_cvt_pk_bf16_f32 v172, v96, v97\n"
        "v_cvt_pk_bf16_f32 v173, v98, v99\n"
        "v_mfma_f32_16x16x32_bf16 v[100:103], a[104:107], a[232:235], v[100:103]\n"
        "buffer_store_dwordx2 v[108:109], v117, s[16:19], s39 offen offset:768 sc1\n"
        "v_mfma_f32_16x16x32_bf16 v[100:103], a[100:103], a[228:231], v[100:103]\n"
        "v_cvt_pk_bf16_f32 v112, v203, v207\n"
        "v_cvt_pk_bf16_f32 v113, v215, v219\n"
        "v_mfma_f32_16x16x32_bf16 v[100:103], a[96:99], a[224:227], v[100:103]\n"
        "s_nop 0\n"
        "v_subrev_u32 v118, 196608, v242\n"
        "v_add_u32 v243, s62, v243\n"
        "v_mfma_f32_16x16x32_bf16 v[104:107], a[120:123], a[224:227], 0\n"
        "s_nop 0\n"
        "v_min_u32 v242, v242, v118\n"
        "v_subrev_u32 v119, 196608, v243\n"
        "v_mfma_f32_16x16x32_bf16 v[104:107], a[124:127], a[228:231], v[104:107]\n"
        "v_cvt_pk_bf16_f32 v174, v100, v101\n"
        "v_cvt_pk_bf16_f32 v175, v102, v103\n"
        "v_mfma_f32_16x16x32_bf16 v[104:107], a[128:131], a[232:235], v[104:107]\n"
        "buffer_store_dword v110, v250, s[16:19], s39 offen offset:768 sc1\n"
        "v_mfma_f32_16x16x32_bf16 v[104:107], a[132:135], a[236:239], v[104:107]\n"
        "v_cvt_pk_bf16_f32 v114, v223, v227\n"
        "v_min_u32 v243, v243, v119\n"
        "v_mfma_f32_16x16x32_bf16 v[104:107], a[136:139], a[240:243], v[104:107]\n"
        "v_add_u32 v244, s62, v244\n"
        "v_mfma_f32_16x16x32_bf16 v[104:107], a[140:143], a[244:247], v[104:107]\n"
        "s_nop 1\n"
        "v_subrev_u32 v118, 196608, v244\n"
        "v_mfma_f32_16x16x32_bf16 v[96:99], a[20:23], a[244:247], 0\n"
        "s_nop 1\n"
        "v_min_u32 v244, v244, v118\n"
        "v_mfma_f32_16x16x32_bf16 v[96:99], a[16:19], a[240:243], v[96:99]\n"
        "buffer_store_dwordx2 v[112:113], v117, s[16:19], s39 offen offset:1152 sc1\n"
        "v_mfma_f32_16x16x32_bf16 v[96:99], a[12:15], a[236:239], v[96:99]\n"
        "v_cvt_pk_bf16_f32 v190, v104, v105\n"
        "v_cvt_pk_bf16_f32 v191, v106, v107\n"
        "v_mfma_f32_16x16x32_bf16 v[96:99], a[8:11], a[232:235], v[96:99]\n"
        "v_mfma_f32_16x16x32_bf16 v[96:99], a[4:7], a[228:231], v[96:99]\n"
        "ds_read_b128 v[182:185], v251 offset:512\n"
        "v_mfma_f32_16x16x32_bf16 v[96:99], a[0:3], a[224:227], v[96:99]\n"
        "s_nop 2\n"
        "v_mfma_f32_16x16x32_bf16 v[64:67], v[152:155], v[156:159], v[0:3]\n"
        "s_waitcnt lgkmcnt(1)\n"
        "buffer_store_dword v114, v250, s[16:19], s39 offen offset:1152 sc1\n"
        "v_mfma_f32_16x16x32_bf16 v[64:67], v[168:171], v[160:163], v[64:67]\n"
        "s_nop 0\n"
        "v_mfma_f32_16x16x32_bf16 v[68:71], v[172:175], v[156:159], v[4:7]\n"
        "v_mul_f32 v96, s50, v96\n"
        "v_mul_f32 v97, s50, v97\n"
        "v_mfma_f32_16x16x32_bf16 v[68:71], v[188:191], v[164:167], v[68:71]\n"
        "v_mul_f32 v98, s50, v98\n"
        "v_mul_f32 v99, s50, v99\n"
        "v_mfma_f32_16x16x32_bf16 v[100:103], a[24:27], a[224:227], 0\n"
        "v_cvt_pk_bf16_f32 v176, v96, v97\n"
        "v_cvt_pk_bf16_f32 v177, v98, v99\n"
        "v_mfma_f32_16x16x32_bf16 v[100:103], a[28:31], a[228:231], v[100:103]\n"
        "v_exp_f32 v64, v64\n"
        "v_mfma_f32_16x16x32_bf16 v[100:103], a[32:35], a[232:235], v[100:103]\n"
        "s_add_u32 m0, s41, 0\n"
        "v_exp_f32 v65, v65\n"
        "v_mfma_f32_16x16x32_bf16 v[100:103], a[36:39], a[236:239], v[100:103]\n"
        "v_exp_f32 v66, v66\n"
        "v_mfma_f32_16x16x32_bf16 v[100:103], a[40:43], a[240:243], v[100:103]\n"
        "buffer_load_dwordx4 v242, s[8:11], s47 offen lds\n"
        "v_mfma_f32_16x16x32_bf16 v[100:103], a[44:47], a[244:247], v[100:103]\n"
        "v_exp_f32 v67, v67\n"
        "v_mfma_f32_16x16x32_bf16 v[104:107], a[68:71], a[244:247], 0\n"
        "v_exp_f32 v68, v68\n"
        "v_mfma_f32_16x16x32_bf16 v[104:107], a[64:67], a[240:243], v[104:107]\n"
        "s_add_u32 m0, s41, 1024\n"
        "v_exp_f32 v69, v69\n"
        "v_mfma_f32_16x16x32_bf16 v[104:107], a[60:63], a[236:239], v[104:107]\n"
        "v_exp_f32 v70, v70\n"
        "v_mfma_f32_16x16x32_bf16 v[104:107], a[56:59], a[232:235], v[104:107]\n"
        "buffer_load_dwordx4 v243, s[8:11], s47 offen lds\n"
        "v_mfma_f32_16x16x32_bf16 v[104:107], a[52:55], a[228:231], v[104:107]\n"
        "v_mul_f32 v100, s50, v100\n"
        "v_mul_f32 v101, s50, v101\n"
        "v_mfma_f32_16x16x32_bf16 v[104:107], a[48:51], a[224:227], v[104:107]\n"
        "v_mul_f32 v102, s50, v102\n"
        "v_mul_f32 v103, s50, v103\n"
        "v_mfma_f32_16x16x32_bf16 v[96:99], a[224:227], a[144:147], 0\n"
        "s_add_u32 m0, s41, 2048\n"
        "v_cvt_pk_bf16_f32 v178, v100, v101\n"
        "v_cvt_pk_bf16_f32 v179, v102, v103\n"
        "v_mfma_f32_16x16x32_bf16 v[96:99], a[228:231], a[148:151], v[96:99]\n"
        "ds_read_b128 a[248:251], v240 offset:12288\n"
        "v_mul_f32 v104, s50, v104\n"
        "v_mfma_f32_16x16x32_bf16 v[96:99], a[232:235], a[152:155], v[96:99]\n"
        "buffer_load_dwordx4 v244, s[8:11], s47 offen lds\n"
        "v_mfma_f32_16x16x32_bf16 v[96:99], a[236:239], a[156:159], v[96:99]\n"
        "v_mul_f32 v105, s50, v105\n"
        "v_mul_f32 v106, s50, v106\n"
        "v_mfma_f32_16x16x32_bf16 v[96:99], a[240:243], a[160:163], v[96:99]\n"
        "v_mul_f32 v107, s50, v107\n"
        "v_cvt_pk_bf16_f32 v180, v104, v105\n"
        "v_mfma_f32_16x16x32_bf16 v[96:99], a[244:247], a[164:167], v[96:99]\n"
        "s_add_u32 m0, s41, 12288\n"
        "ds_read_b128 a[216:219], v241 offset:12288\n"
        "v_cvt_pk_bf16_f32 v181, v106, v107\n"
        "v_mfma_f32_16x16x32_bf16 v[72:75], v[152:155], v[176:179], v[16:19]\n"
        "s_waitcnt lgkmcnt(2)\n"
        "ds_read_b128 a[220:223], v240 offset:12416\n"
        "v_mov_b64 v[186:187], v[180:181]\n"
        "v_mfma_f32_16x16x32_bf16 v[72:75], v[168:171], v[180:183], v[72:75]\n"
        "buffer_load_dwordx4 v242, s[8:11], s35 offen lds\n"
        "v_mfma_f32_16x16x32_bf16 v[76:79], v[172:175], v[176:179], v[20:23]\n"
        "v_cvt_pk_bf16_f32 v130, v96, v97\n"
        "v_cvt_pk_bf16_f32 v131, v98, v99\n"
        "v_mfma_f32_16x16x32_bf16 v[76:79], v[188:191], v[184:187], v[76:79]\n"
        "v_exp_f32 v71, v71\n"
        "v_mfma_f32_16x16x32_bf16 v[100:103], a[244:247], a[188:191], 0\n"
        "s_add_u32 m0, s41, 13312\n"
        "v_exp_f32 v72, v72\n"
        "v_mfma_f32_16x16x32_bf16 v[100:103], a[240:243], a[184:187], v[100:103]\n"
        "v_exp_f32 v73, v73\n"
        "v_mfma_f32_16x16x32_bf16 v[100:103], a[236:239], a[180:183], v[100:103]\n"
        "buffer_load_dwordx4 v243, s[8:11], s35 offen lds\n"
        "v_mfma_f32_16x16x32_bf16 v[100:103], a[232:235], a[176:179], v[100:103]\n"
        "v_exp_f32 v74, v74\n"
        "v_mfma_f32_16x16x32_bf16 v[100:103], a[228:231], a[172:175], v[100:103]\n"
        "v_exp_f32 v75, v75\n"
        "v_mfma_f32_16x16x32_bf16 v[100:103], a[224:227], a[168:171], v[100:103]\n"
        "s_add_u32 m0, s41, 14336\n"
        "v_exp_f32 v76, v76\n"
        "v_mfma_f32_16x16x32_bf16 v[104:107], a[224:227], a[192:195], 0\n"
        "ds_read_b128 a[224:227], v241 offset:12416\n"
        "v_cvt_pk_bf16_f32 v64, v64, v65\n"
        "v_mfma_f32_16x16x32_bf16 v[104:107], a[228:231], a[196:199], v[104:107]\n"
        "ds_read_b128 a[228:231], v240 offset:12544\n"
        "v_cvt_pk_bf16_f32 v65, v66, v67\n"
        "v_mfma_f32_16x16x32_bf16 v[104:107], a[232:235], a[200:203], v[104:107]\n"
        "ds_read_b128 a[232:235], v241 offset:12544\n"
        "v_cvt_pk_bf16_f32 v138, v100, v101\n"
        "v_mfma_f32_16x16x32_bf16 v[104:107], a[236:239], a[204:207], v[104:107]\n"
        "buffer_load_dwordx4 v244, s[8:11], s35 offen lds\n"
        "v_mfma_f32_16x16x32_bf16 v[104:107], a[240:243], a[208:211], v[104:107]\n"
        "s_mov_b32 m0, s48\n"
        "v_cvt_pk_bf16_f32 v139, v102, v103\n"
        "v_cvt_pk_bf16_f32 v66, v68, v69\n"
        "v_mfma_f32_16x16x32_bf16 v[104:107], a[244:247], a[212:215], v[104:107]\n"
        "s_waitcnt lgkmcnt(5)\n"
        "v_exp_f32 v77, v77\n"
        "v_mfma_f32_16x16x32_bf16 v[96:99], a[72:75], a[248:251], 0\n"
        "s_waitcnt lgkmcnt(4)\n"
        "buffer_load_dwordx4 v249, s[12:15], s36 offen offset:0 lds\n"
        "v_mfma_f32_16x16x32_bf16 v[96:99], a[76:79], a[216:219], v[96:99]\n"
        "s_waitcnt lgkmcnt(3)\n"
        "ds_read_b128 v[162:165], v252 offset:0\n"
        "v_xor_b32 v116, 32, v249\n"
        "v_mfma_f32_16x16x32_bf16 v[96:99], a[80:83], a[220:223], v[96:99]\n"
        "s_waitcnt lgkmcnt(3)\n"
        "ds_read_b128 v[182:185], v252 offset:512\n"
        "v_cvt_pk_bf16_f32 v146, v104, v105\n"
        "v_mfma_f32_16x16x32_bf16 v[96:99], a[84:87], a[224:227], v[96:99]\n"
        "s_waitcnt lgkmcnt(3)\n"
        "v_cvt_pk_bf16_f32 v147, v106, v107\n"
        "v_cvt_pk_bf16_f32 v67, v70, v71\n"
        "v_mfma_f32_16x16x32_bf16 v[96:99], a[88:91], a[228:231], v[96:99]\n"
        "s_waitcnt lgkmcnt(2)\n"
        "v_exp_f32 v78, v78\n"
        "v_mfma_f32_16x16x32_bf16 v[96:99], a[92:95], a[232:235], v[96:99]\n"
        "buffer_load_dwordx4 v116, s[12:15], s36 offen offset:1024 lds\n"
        "v_mfma_f32_16x16x32_bf16 v[100:103], a[116:119], a[232:235], 0\n"
        "v_exp_f32 v79, v79\n"
        "v_mfma_f32_16x16x32_bf16 v[100:103], a[112:115], a[228:231], v[100:103]\n"
        "v_cvt_pk_bf16_f32 v72, v72, v73\n"
        "v_cvt_pk_bf16_f32 v73, v74, v75\n"
        "v_mfma_f32_16x16x32_bf16 v[100:103], a[108:111], a[224:227], v[100:103]\n"
        "v_cvt_pk_bf16_f32 v74, v76, v77\n"
        "v_cvt_pk_bf16_f32 v192, v96, v97\n"
        "v_mfma_f32_16x16x32_bf16 v[100:103], a[104:107], a[220:223], v[100:103]\n"
        "v_cvt_pk_bf16_f32 v193, v98, v99\n"
        "v_cvt_pk_bf16_f32 v75, v78, v79\n"
        "v_mfma_f32_16x16x32_bf16 v[100:103], a[100:103], a[216:219], v[100:103]\n"
        "44:\n"
        "s_cbranch_execz 40b\n"
        "v_mfma_f32_16x16x32_bf16 v[100:103], a[96:99], a[248:251], v[100:103]\n"
        "s_nop 2\n"
        "v_mfma_f32_16x16x32_bf16 v[104:107], a[120:123], a[248:251], 0\n"
        "s_nop 2\n"
        "v_mfma_f32_16x16x32_bf16 v[104:107], a[124:127], a[216:219], v[104:107]\n"
        "v_cvt_pk_bf16_f32 v194, v100, v101\n"
        "v_cvt_pk_bf16_f32 v195, v102, v103\n"
        "v_mfma_f32_16x16x32_bf16 v[104:107], a[128:131], a[220:223], v[104:107]\n"
        "v_mfma_f32_16x16x32_bf16 v[104:107], a[132:135], a[224:227], v[104:107]\n"
        "v_mfma_f32_16x16x32_bf16 v[104:107], a[136:139], a[228:231], v[104:107]\n"
        "v_mfma_f32_16x16x32_bf16 v[104:107], a[140:143], a[232:235], v[104:107]\n"
        "s_nop 2\n"
        "v_mfma_f32_16x16x32_bf16 v[96:99], a[20:23], a[232:235], 0\n"
        "s_nop 2\n"
        "v_mfma_f32_16x16x32_bf16 v[96:99], a[16:19], a[228:231], v[96:99]\n"
        "v_cvt_pk_bf16_f32 v208, v104, v105\n"
        "v_cvt_pk_bf16_f32 v209, v106, v107\n"
        "v_mfma_f32_16x16x32_bf16 v[96:99], a[12:15], a[224:227], v[96:99]\n"
        "v_mfma_f32_16x16x32_bf16 v[96:99], a[8:11], a[220:223], v[96:99]\n"
        "v_mfma_f32_16x16x32_bf16 v[96:99], a[4:7], a[216:219], v[96:99]\n"
        "ds_read_b128 v[202:205], v251 offset:4096\n"
        "v_mfma_f32_16x16x32_bf16 v[96:99], a[0:3], a[248:251], v[96:99]\n"
        "s_nop 2\n"
        "v_mfma_f32_16x16x32_bf16 v[68:71], v[192:195], v[156:159], v[8:11]\n"
        "s_waitcnt lgkmcnt(2)\n"
        "s_nop 1\n"
        "v_mfma_f32_16x16x32_bf16 v[68:71], v[208:211], v[160:163], v[68:71]\n"
        "v_mfma_f32_16x16x32_bf16 v[76:79], v[192:195], v[176:179], v[24:27]\n"
        "s_waitcnt lgkmcnt(1)\n"
        "v_mul_f32 v96, s50, v96\n"
        "v_mul_f32 v97, s50, v97\n"
        "v_mfma_f32_16x16x32_bf16 v[76:79], v[208:211], v[180:183], v[76:79]\n"
        "v_mul_f32 v98, s50, v98\n"
        "v_mul_f32 v99, s50, v99\n"
        "v_mfma_f32_16x16x32_bf16 v[100:103], a[24:27], a[248:251], 0\n"
        "v_cvt_pk_bf16_f32 v196, v96, v97\n"
        "v_cvt_pk_bf16_f32 v197, v98, v99\n"
        "v_mfma_f32_16x16x32_bf16 v[100:103], a[28:31], a[216:219], v[100:103]\n"
        "v_exp_f32 v68, v68\n"
        "v_mfma_f32_16x16x32_bf16 v[100:103], a[32:35], a[220:223], v[100:103]\n"
        "v_exp_f32 v69, v69\n"
        "v_mfma_f32_16x16x32_bf16 v[100:103], a[36:39], a[224:227], v[100:103]\n"
        "v_exp_f32 v70, v70\n"
        "v_mfma_f32_16x16x32_bf16 v[100:103], a[40:43], a[228:231], v[100:103]\n"
        "v_exp_f32 v71, v71\n"
        "v_mfma_f32_16x16x32_bf16 v[100:103], a[44:47], a[232:235], v[100:103]\n"
        "v_exp_f32 v76, v76\n"
        "v_mfma_f32_16x16x32_bf16 v[104:107], a[68:71], a[232:235], 0\n"
        "v_exp_f32 v77, v77\n"
        "v_mfma_f32_16x16x32_bf16 v[104:107], a[64:67], a[228:231], v[104:107]\n"
        "v_exp_f32 v78, v78\n"
        "v_mfma_f32_16x16x32_bf16 v[104:107], a[60:63], a[224:227], v[104:107]\n"
        "v_exp_f32 v79, v79\n"
        "v_mfma_f32_16x16x32_bf16 v[104:107], a[56:59], a[220:223], v[104:107]\n"
        "v_mul_f32 v100, s50, v100\n"
        "v_mul_f32 v101, s50, v101\n"
        "v_mfma_f32_16x16x32_bf16 v[104:107], a[52:55], a[216:219], v[104:107]\n"
        "v_mul_f32 v102, s50, v102\n"
        "v_mul_f32 v103, s50, v103\n"
        "v_mfma_f32_16x16x32_bf16 v[104:107], a[48:51], a[248:251], v[104:107]\n"
        "v_cvt_pk_bf16_f32 v198, v100, v101\n"
        "v_cvt_pk_bf16_f32 v199, v102, v103\n"
        "v_mfma_f32_16x16x32_bf16 v[96:99], a[248:251], a[144:147], 0\n"
        "ds_read_b128 a[236:239], v240 offset:13824\n"
        "v_cvt_pk_bf16_f32 v68, v68, v69\n"
        "v_mfma_f32_16x16x32_bf16 v[96:99], a[216:219], a[148:151], v[96:99]\n"
        "ds_read_b128 a[240:243], v241 offset:13824\n"
        "v_cvt_pk_bf16_f32 v69, v70, v71\n"
        "v_mfma_f32_16x16x32_bf16 v[96:99], a[220:223], a[152:155], v[96:99]\n"
        "v_mul_f32 v104, s50, v104\n"
        "v_mul_f32 v105, s50, v105\n"
        "v_mfma_f32_16x16x32_bf16 v[96:99], a[224:227], a[156:159], v[96:99]\n"
        "v_mul_f32 v106, s50, v106\n"
        "v_mul_f32 v107, s50, v107\n"
        "v_mfma_f32_16x16x32_bf16 v[96:99], a[228:231], a[160:163], v[96:99]\n"
        "v_cvt_pk_bf16_f32 v200, v104, v105\n"
        "v_cvt_pk_bf16_f32 v201, v106, v107\n"
        "v_mfma_f32_16x16x32_bf16 v[96:99], a[232:235], a[164:167], v[96:99]\n"
        "ds_read_b128 a[244:247], v240 offset:13952\n"
        "v_mov_b64 v[206:207], v[200:201]\n"
        "v_mfma_f32_16x16x32_bf16 v[80:83], v[152:155], v[196:199], v[32:35]\n"
        "s_waitcnt lgkmcnt(3)\n"
        "v_cvt_pk_bf16_f32 v76, v76, v77\n"
        "v_cvt_pk_bf16_f32 v77, v78, v79\n"
        "v_mfma_f32_16x16x32_bf16 v[80:83], v[168:171], v[200:203], v[80:83]\n"
        "s_nop 0\n"
        "v_mfma_f32_16x16x32_bf16 v[84:87], v[172:175], v[196:199], v[36:39]\n"
        "v_cvt_pk_bf16_f32 v132, v96, v97\n"
        "v_cvt_pk_bf16_f32 v133, v98, v99\n"
        "v_mfma_f32_16x16x32_bf16 v[84:87], v[188:191], v[204:207], v[84:87]\n"
        "ds_read_b128 v[202:205], v252 offset:4096\n"
        "v_lshrrev_b32 v121, 6, v250\n"
        "v_mfma_f32_16x16x32_bf16 v[100:103], a[232:235], a[188:191], 0\n"
        "v_add3_u32 v121, v121, v245, s67\n"
        "v_mfma_f32_16x16x32_bf16 v[100:103], a[228:231], a[184:187], v[100:103]\n"
        "v_exp_f32 v80, v80\n"
        "v_mfma_f32_16x16x32_bf16 v[100:103], a[224:227], a[180:183], v[100:103]\n"
        "v_exp_f32 v81, v81\n"
        "v_mfma_f32_16x16x32_bf16 v[100:103], a[220:223], a[176:179], v[100:103]\n"
        "v_exp_f32 v82, v82\n"
        "v_mfma_f32_16x16x32_bf16 v[100:103], a[216:219], a[172:175], v[100:103]\n"
        "v_exp_f32 v83, v83\n"
        "v_mfma_f32_16x16x32_bf16 v[100:103], a[248:251], a[168:171], v[100:103]\n"
        "v_exp_f32 v84, v84\n"
        "v_mfma_f32_16x16x32_bf16 v[104:107], a[248:251], a[192:195], 0\n"
        "ds_read_b128 a[248:251], v241 offset:13952\n"
        "v_cvt_pk_bf16_f32 v80, v80, v81\n"
        "v_mfma_f32_16x16x32_bf16 v[104:107], a[216:219], a[196:199], v[104:107]\n"
        "ds_read_b128 a[216:219], v240 offset:14080\n"
        "v_cvt_pk_bf16_f32 v81, v82, v83\n"
        "v_mfma_f32_16x16x32_bf16 v[104:107], a[220:223], a[200:203], v[104:107]\n"
        "ds_read_b128 a[220:223], v241 offset:14080\n"
        "v_cvt_pk_bf16_f32 v140, v100, v101\n"
        "v_mfma_f32_16x16x32_bf16 v[104:107], a[224:227], a[204:207], v[104:107]\n"
        "v_cvt_pk_bf16_f32 v141, v102, v103\n"
        "v_mfma_f32_16x16x32_bf16 v[104:107], a[228:231], a[208:211], v[104:107]\n"
        "v_exp_f32 v85, v85\n"
        "v_mfma_f32_16x16x32_bf16 v[104:107], a[232:235], a[212:215], v[104:107]\n"
        "s_waitcnt lgkmcnt(6)\n"
        "v_exp_f32 v86, v86\n"
        "v_mfma_f32_16x16x32_bf16 v[96:99], a[72:75], a[236:239], 0\n"
        "s_waitcnt lgkmcnt(5)\n"
        "v_cvt_pk_bf16_f32 v82, v84, v85\n"
        "v_mfma_f32_16x16x32_bf16 v[96:99], a[76:79], a[240:243], v[96:99]\n"
        "s_waitcnt lgkmcnt(4)\n"
        "v_exp_f32 v87, v87\n"
        "v_mfma_f32_16x16x32_bf16 v[96:99], a[80:83], a[244:247], v[96:99]\n"
        "s_waitcnt lgkmcnt(2)\n"
        "v_cvt_pk_bf16_f32 v83, v86, v87\n"
        "v_mfma_f32_16x16x32_bf16 v[96:99], a[84:87], a[248:251], v[96:99]\n"
        "s_waitcnt lgkmcnt(1)\n"
        "v_cvt_pk_bf16_f32 v148, v104, v105\n"
        "v_cvt_pk_bf16_f32 v149, v106, v107\n"
        "v_mfma_f32_16x16x32_bf16 v[96:99], a[88:91], a[216:219], v[96:99]\n"
        "s_waitcnt lgkmcnt(0)\n"
        "v_mfma_f32_16x16x32_bf16 v[96:99], a[92:95], a[220:223], v[96:99]\n"
        "s_nop 2\n"
        "v_mfma_f32_16x16x32_bf16 v[84:87], v[192:195], v[196:199], v[40:43]\n"
        "s_nop 1\n"
        "v_mfma_f32_16x16x32_bf16 v[84:87], v[208:211], v[200:203], v[84:87]\n"
        "v_mfma_f32_16x16x32_bf16 v[100:103], a[116:119], a[220:223], 0\n"
        "v_cvt_pk_bf16_f32 v212, v96, v97\n"
        "v_cvt_pk_bf16_f32 v213, v98, v99\n"
        "v_mfma_f32_16x16x32_bf16 v[100:103], a[112:115], a[216:219], v[100:103]\n"
        "v_add_u32 v240, s45, v240\n"
        "v_add_u32 v241, s45, v241\n"
        "v_mfma_f32_16x16x32_bf16 v[100:103], a[108:111], a[248:251], v[100:103]\n"
        "v_mfma_f32_16x16x32_bf16 v[100:103], a[104:107], a[244:247], v[100:103]\n"
        "v_exp_f32 v84, v84\n"
        "v_mfma_f32_16x16x32_bf16 v[100:103], a[100:103], a[240:243], v[100:103]\n"
        "v_exp_f32 v85, v85\n"
        "v_mfma_f32_16x16x32_bf16 v[100:103], a[96:99], a[236:239], v[100:103]\n"
        "v_exp_f32 v86, v86\n"
        "v_mfma_f32_16x16x32_bf16 v[104:107], a[120:123], a[236:239], 0\n"
        "v_exp_f32 v87, v87\n"
        "v_mfma_f32_16x16x32_bf16 v[104:107], a[124:127], a[240:243], v[104:107]\n"
        "v_cvt_pk_bf16_f32 v84, v84, v85\n"
        "v_cvt_pk_bf16_f32 v85, v86, v87\n"
        "v_mfma_f32_16x16x32_bf16 v[104:107], a[128:131], a[244:247], v[104:107]\n"
        "s_waitcnt vmcnt(24)\n"
        "v_readfirstlane_b32 s59, v120\n"
        "v_mfma_f32_16x16x32_bf16 v[104:107], a[132:135], a[248:251], v[104:107]\n"
        "v_cvt_pk_bf16_f32 v214, v100, v101\n"
        "v_cvt_pk_bf16_f32 v215, v102, v103\n"
        "v_mfma_f32_16x16x32_bf16 v[104:107], a[136:139], a[216:219], v[104:107]\n"
        "s_min_u32 s59, s59, 256\n"
        "v_mfma_f32_16x16x32_bf16 v[104:107], a[140:143], a[220:223], v[104:107]\n"
        "s_add_u32 s59, s59, 3840\n"
        "s_nop 0\n"
        "v_mov_b32 v120, s59\n"
        "v_mfma_f32_16x16x32_bf16 v[96:99], a[20:23], a[220:223], 0\n"
        "s_lshr_b32 s46, s56, 6\n"
        "s_nop 1\n"
        "v_mfma_f32_16x16x32_bf16 v[96:99], a[16:19], a[216:219], v[96:99]\n"
        "s_mul_i32 s46, s46, 1572864\n"
        "v_cvt_pk_bf16_f32 v230, v104, v105\n"
        "v_cvt_pk_bf16_f32 v231, v106, v107\n"
        "v_mfma_f32_16x16x32_bf16 v[96:99], a[12:15], a[248:251], v[96:99]\n"
        "s_barrier\n"
        "v_mfma_f32_16x16x32_bf16 v[96:99], a[8:11], a[244:247], v[96:99]\n"
        "s_add_u32 s46, s46, s37\n"
        "ds_write_b32 v121, v120 offset:768\n"
        "v_mfma_f32_16x16x32_bf16 v[96:99], a[4:7], a[240:243], v[96:99]\n"
        "s_cmp_eq_u32 s46, 100663296\n"
        "v_mfma_f32_16x16x32_bf16 v[96:99], a[0:3], a[236:239], v[96:99]\n"
        "s_cselect_b32 s46, 0, s46\n"
        "s_nop 1\n"
        "v_mfma_f32_16x16x32_bf16 v[108:111], v[212:215], v[156:159], v[12:15]\n"
        "s_and_b32 s43, s56, 63\n"
        "ds_read_b128 v[222:225], v251 offset:4608\n"
        "v_mfma_f32_16x16x32_bf16 v[108:111], v[228:231], v[164:167], v[108:111]\n"
        "s_mul_i32 s43, s43, 3072\n"
        "v_mul_f32 v96, s50, v96\n"
        "v_mul_f32 v97, s50, v97\n"
        "v_mfma_f32_16x16x32_bf16 v[112:115], v[212:215], v[176:179], v[28:31]\n"
        "s_add_u32 s44, s43, 3072\n"
        "v_mul_f32 v98, s50, v98\n"
        "v_mul_f32 v99, s50, v99\n"
        "v_mfma_f32_16x16x32_bf16 v[112:115], v[228:231], v[184:187], v[112:115]\n"
        "s_cmp_eq_u32 s44, 196608\n"
        "v_cvt_pk_bf16_f32 v216, v96, v97\n"
        "v_cvt_pk_bf16_f32 v217, v98, v99\n"
        "v_mfma_f32_16x16x32_bf16 v[116:119], v[212:215], v[196:199], v[44:47]\n"
        "s_cselect_b32 s44, 0, s44\n"
        "v_exp_f32 v108, v108\n"
        "v_mfma_f32_16x16x32_bf16 v[116:119], v[228:231], v[204:207], v[116:119]\n"
        "s_add_u32 s39, s46, s44\n"
        "v_exp_f32 v109, v109\n"
        "v_mfma_f32_16x16x32_bf16 v[100:103], a[24:27], a[236:239], 0\n"
        "s_add_u32 s43, s43, s46\n"
        "v_exp_f32 v110, v110\n"
        "v_mfma_f32_16x16x32_bf16 v[100:103], a[28:31], a[240:243], v[100:103]\n"
        "s_add_u32 s38, s43, 1536\n"
        "v_exp_f32 v111, v111\n"
        "v_mfma_f32_16x16x32_bf16 v[100:103], a[32:35], a[244:247], v[100:103]\n"
        "v_exp_f32 v112, v112\n"
        "v_mfma_f32_16x16x32_bf16 v[100:103], a[36:39], a[248:251], v[100:103]\n"
        "v_exp_f32 v113, v113\n"
        "v_mfma_f32_16x16x32_bf16 v[100:103], a[40:43], a[216:219], v[100:103]\n"
        "v_exp_f32 v114, v114\n"
        "v_mfma_f32_16x16x32_bf16 v[100:103], a[44:47], a[220:223], v[100:103]\n"
        "v_exp_f32 v115, v115\n"
        "v_mfma_f32_16x16x32_bf16 v[104:107], a[68:71], a[220:223], 0\n"
        "v_exp_f32 v116, v116\n"
        "v_mfma_f32_16x16x32_bf16 v[104:107], a[64:67], a[216:219], v[104:107]\n"
        "v_exp_f32 v117, v117\n"
        "v_mfma_f32_16x16x32_bf16 v[104:107], a[60:63], a[248:251], v[104:107]\n"
        "v_exp_f32 v118, v118\n"
        "v_mfma_f32_16x16x32_bf16 v[104:107], a[56:59], a[244:247], v[104:107]\n"
        "v_mul_f32 v100, s50, v100\n"
        "v_mul_f32 v101, s50, v101\n"
        "v_mfma_f32_16x16x32_bf16 v[104:107], a[52:55], a[240:243], v[104:107]\n"
        "v_mul_f32 v102, s50, v102\n"
        "v_mul_f32 v103, s50, v103\n"
        "v_mfma_f32_16x16x32_bf16 v[104:107], a[48:51], a[236:239], v[104:107]\n"
        "v_cvt_pk_bf16_f32 v218, v100, v101\n"
        "v_cvt_pk_bf16_f32 v219, v102, v103\n"
        "v_mfma_f32_16x16x32_bf16 v[96:99], a[236:239], a[144:147], 0\n"
        "v_exp_f32 v119, v119\n"
        "v_mfma_f32_16x16x32_bf16 v[96:99], a[240:243], a[148:151], v[96:99]\n"
        "v_cvt_pk_bf16_f32 v70, v108, v109\n"
        "v_cvt_pk_bf16_f32 v71, v110, v111\n"
        "v_mfma_f32_16x16x32_bf16 v[96:99], a[244:247], a[152:155], v[96:99]\n"
        "v_mul_f32 v104, s50, v104\n"
        "v_mul_f32 v105, s50, v105\n"
        "v_mfma_f32_16x16x32_bf16 v[96:99], a[248:251], a[156:159], v[96:99]\n"
        "v_mul_f32 v106, s50, v106\n"
        "v_mul_f32 v107, s50, v107\n"
        "v_mfma_f32_16x16x32_bf16 v[96:99], a[216:219], a[160:163], v[96:99]\n"
        "v_cvt_pk_bf16_f32 v220, v104, v105\n"
        "v_cvt_pk_bf16_f32 v221, v106, v107\n"
        "v_mfma_f32_16x16x32_bf16 v[96:99], a[220:223], a[164:167], v[96:99]\n"
        "ds_read_b128 v[156:159], v247 offset:0\n"
        "v_mov_b64 v[226:227], v[220:221]\n"
        "v_mfma_f32_16x16x32_bf16 v[88:91], v[152:155], v[216:219], v[48:51]\n"
        "s_waitcnt lgkmcnt(1)\n"
        "ds_read_b128 v[164:167], v247 offset:4096\n"
        "v_cvt_pk_bf16_f32 v78, v112, v113\n"
        "v_mfma_f32_16x16x32_bf16 v[88:91], v[168:171], v[220:223], v[88:91]\n"
        "ds_read_b128 v[160:163], v247 offset:256\n"
        "v_cvt_pk_bf16_f32 v79, v114, v115\n"
        "v_mfma_f32_16x16x32_bf16 v[92:95], v[172:175], v[216:219], v[52:55]\n"
        "s_mov_b32 s56, s57\n"
        "ds_read_b128 v[152:155], v247 offset:4352\n"
        "v_cvt_pk_bf16_f32 v134, v96, v97\n"
        "v_mfma_f32_16x16x32_bf16 v[92:95], v[188:191], v[224:227], v[92:95]\n"
        "s_mov_b32 s57, s58\n"
        "ds_read_b128 v[222:225], v252 offset:4608\n"
        "v_cvt_pk_bf16_f32 v135, v98, v99\n"
        "v_mfma_f32_16x16x32_bf16 v[100:103], a[220:223], a[188:191], 0\n"
        "s_xor_b32 s48, s48, 8192\n"
        "v_exp_f32 v88, v88\n"
        "v_mfma_f32_16x16x32_bf16 v[100:103], a[216:219], a[184:187], v[100:103]\n"
        "v_exp_f32 v89, v89\n"
        "v_mfma_f32_16x16x32_bf16 v[100:103], a[248:251], a[180:183], v[100:103]\n"
        "v_exp_f32 v90, v90\n"
        "v_mfma_f32_16x16x32_bf16 v[100:103], a[244:247], a[176:179], v[100:103]\n"
        "v_exp_f32 v91, v91\n"
        "v_mfma_f32_16x16x32_bf16 v[100:103], a[240:243], a[172:175], v[100:103]\n"
        "v_exp_f32 v92, v92\n"
        "v_mfma_f32_16x16x32_bf16 v[100:103], a[236:239], a[168:171], v[100:103]\n"
        "v_exp_f32 v93, v93\n"
        "v_mfma_f32_16x16x32_bf16 v[104:107], a[236:239], a[192:195], 0\n"
        "ds_read_b128 a[236:239], v248 offset:0\n"
        "v_cvt_pk_bf16_f32 v86, v116, v117\n"
        "v_mfma_f32_16x16x32_bf16 v[104:107], a[240:243], a[196:199], v[104:107]\n"
        "ds_read_b128 a[240:243], v248 offset:6144\n"
        "v_cvt_pk_bf16_f32 v87, v118, v119\n"
        "v_mfma_f32_16x16x32_bf16 v[104:107], a[244:247], a[200:203], v[104:107]\n"
        "ds_read_b128 a[244:247], v254 offset:16\n"
        "v_mov_b64 v[116:117], 0\n"
        "v_mfma_f32_16x16x32_bf16 v[104:107], a[248:251], a[204:207], v[104:107]\n"
        "v_cvt_pk_bf16_f32 v142, v100, v101\n"
        "v_cvt_pk_bf16_f32 v143, v102, v103\n"
        "v_mfma_f32_16x16x32_bf16 v[104:107], a[216:219], a[208:211], v[104:107]\n"
        "ds_read_b128 a[248:251], v254 offset:6160\n"
        "v_cvt_pk_bf16_f32 v88, v88, v89\n"
        "v_mfma_f32_16x16x32_bf16 v[104:107], a[220:223], a[212:215], v[104:107]\n"
        "ds_read_b128 a[216:219], v248 offset:32\n"
        "v_cvt_pk_bf16_f32 v89, v90, v91\n"
        "v_mfma_f32_16x16x32_bf16 v[108:111], v[192:195], v[216:219], v[56:59]\n"
        "s_waitcnt lgkmcnt(5)\n"
        "ds_read_b128 a[220:223], v248 offset:6176\n"
        "v_cvt_pk_bf16_f32 v90, v92, v93\n"
        "v_mfma_f32_16x16x32_bf16 v[108:111], v[208:211], v[220:223], v[108:111]\n"
        "v_exp_f32 v94, v94\n"
        "v_mfma_f32_16x16x32_bf16 v[112:115], v[212:215], v[216:219], v[60:63]\n"
        "ds_read_b128 a[224:227], v254 offset:48\n"
        "v_cvt_pk_bf16_f32 v150, v104, v105\n"
        "v_mfma_f32_16x16x32_bf16 v[112:115], v[228:231], v[224:227], v[112:115]\n"
        "s_waitcnt lgkmcnt(6)\n"
        "ds_read_b128 a[228:231], v254 offset:6192\n"
        "v_cvt_pk_bf16_f32 v151, v106, v107\n"
        "v_mfma_f32_16x16x32_bf16 v[172:175], v[156:159], a[236:239], 0\n"
        "s_waitcnt lgkmcnt(6)\n"
        "v_exp_f32 v95, v95\n"
        "v_mfma_f32_16x16x32_bf16 v[172:175], v[164:167], a[240:243], v[172:175]\n"
        "ds_read_b128 a[232:235], v248 offset:64\n"
        "v_cvt_pk_bf16_f32 v91, v94, v95\n"
        "v_mfma_f32_16x16x32_bf16 v[200:203], v[152:155], a[240:243], 0\n"
        "v_exp_f32 v108, v108\n"
        "v_mfma_f32_16x16x32_bf16 v[200:203], v[160:163], a[236:239], v[200:203]\n"
        "v_exp_f32 v109, v109\n"
        "v_smfmac_f32_16x16x64_bf16 v[116:119], a[252:255], v[64:71], v255\n"
        "ds_read_b128 a[236:239], v248 offset:6208\n"
        "v_cvt_pk_bf16_f32 v92, v108, v109\n"
        "v_mfma_f32_16x16x32_bf16 v[120:123], v[128:131], v[64:67], 0\n"
        "v_exp_f32 v110, v110\n"
        "v_mfma_f32_16x16x32_bf16 v[120:123], v[132:135], v[68:71], v[120:123]\n"
        "v_exp_f32 v111, v111\n"
        "v_mfma_f32_16x16x32_bf16 v[124:127], v[140:143], v[68:71], 0\n"
        "v_exp_f32 v112, v112\n"
        "v_mfma_f32_16x16x32_bf16 v[124:127], v[136:139], v[64:67], v[124:127]\n"
        "v_exp_f32 v113, v113\n"
        "v_mfma_f32_16x16x32_bf16 v[96:99], v[144:147], v[64:67], 0\n"
        "v_exp_f32 v114, v114\n"
        "v_mfma_f32_16x16x32_bf16 v[96:99], v[148:151], v[68:71], v[96:99]\n"
        "s_waitcnt lgkmcnt(7)\n"
        "ds_read_b128 a[240:243], v254 offset:80\n"
        "v_add_f32 v116, v116, v117\n"
        "v_mfma_f32_16x16x32_bf16 v[204:207], v[160:163], a[244:247], 0\n"
        "s_waitcnt lgkmcnt(7)\n"
        "v_rcp_f32 v117, v116\n"
        "v_mfma_f32_16x16x32_bf16 v[204:207], v[152:155], a[248:251], v[204:207]\n"
        "v_exp_f32 v115, v115\n"
        "v_mfma_f32_16x16x32_bf16 v[176:179], v[164:167], a[248:251], 0\n"
        "v_mul_f32 v120, v117, v120\n"
        "v_mul_f32 v121, v117, v121\n"
        "v_mfma_f32_16x16x32_bf16 v[176:179], v[156:159], a[244:247], v[176:179]\n"
        "s_waitcnt lgkmcnt(6)\n"
        "ds_read_b128 a[244:247], v254 offset:6224\n"
        "v_mul_f32 v122, v117, v122\n"
        "v_mfma_f32_16x16x32_bf16 v[180:183], v[156:159], a[216:219], 0\n"
        "s_waitcnt lgkmcnt(6)\n"
        "ds_read_b128 a[248:251], v248 offset:36864\n"
        "v_mov_b64 v[100:101], 0\n"
        "v_mfma_f32_16x16x32_bf16 v[180:183], v[164:167], a[220:223], v[180:183]\n"
        "ds_read_b128 v[232:235], v247 offset:8192\n"
        "v_mul_f32 v123, v117, v123\n"
        "v_mfma_f32_16x16x32_bf16 v[212:215], v[152:155], a[220:223], 0\n"
        "ds_read_b128 v[64:67], v247 offset:12288\n"
        "v_cvt_pk_bf16_f32 v93, v110, v111\n"
        "v_mfma_f32_16x16x32_bf16 v[212:215], v[160:163], a[216:219], v[212:215]\n"
        "s_waitcnt lgkmcnt(8)\n"
        "ds_read_b128 v[236:239], v247 offset:8448\n"
        "v_cvt_pk_bf16_f32 v120, v120, v121\n"
        "v_mfma_f32_16x16x32_bf16 v[216:219], v[160:163], a[224:227], 0\n"
        "s_waitcnt lgkmcnt(8)\n"
        "ds_read_b128 v[68:71], v247 offset:12544\n"
        "v_cvt_pk_bf16_f32 v121, v122, v123\n"
        "v_mfma_f32_16x16x32_bf16 v[216:219], v[152:155], a[228:231], v[216:219]\n"
        "ds_write_b32 v245, v120 offset:0\n"
        "v_mfma_f32_16x16x32_bf16 v[184:187], v[164:167], a[228:231], 0\n"
        "ds_write_b32 v245, v121 offset:4\n"
        "v_mfma_f32_16x16x32_bf16 v[184:187], v[156:159], a[224:227], v[184:187]\n"
        "s_waitcnt lgkmcnt(10)\n"
        "ds_read_b128 a[216:219], v248 offset:43008\n"
        "v_cvt_pk_bf16_f32 v94, v112, v113\n"
        "v_mfma_f32_16x16x32_bf16 v[192:195], v[156:159], a[232:235], 0\n"
        "s_waitcnt lgkmcnt(10)\n"
        "v_mul_f32 v124, v117, v124\n"
        "v_mul_f32 v125, v117, v125\n"
        "v_mfma_f32_16x16x32_bf16 v[192:195], v[164:167], a[236:239], v[192:195]\n"
        "v_maximum3_f32 v253, v253, v116, v117\n"
        "v_mul_f32 v126, v117, v126\n"
        "v_mfma_f32_16x16x32_bf16 v[220:223], v[152:155], a[236:239], 0\n"
        "v_mul_f32 v127, v117, v127\n"
        "v_cvt_pk_bf16_f32 v95, v114, v115\n"
        "v_mfma_f32_16x16x32_bf16 v[220:223], v[160:163], a[232:235], v[220:223]\n"
        "ds_read_b128 a[220:223], v254 offset:36880\n"
        "v_cvt_pk_bf16_f32 v124, v124, v125\n"
        "v_smfmac_f32_16x16x64_bf16 v[100:103], a[252:255], v[72:79], v255\n"
        "ds_read_b128 a[224:227], v254 offset:43024\n"
        "v_cvt_pk_bf16_f32 v125, v126, v127\n"
        "v_mfma_f32_16x16x32_bf16 v[104:107], v[128:131], v[72:75], 0\n"
        "ds_write_b32 v245, v124 offset:8\n"
        "v_mfma_f32_16x16x32_bf16 v[104:107], v[132:135], v[76:79], v[104:107]\n"
        "ds_write_b32 v245, v125 offset:12\n"
        "v_mfma_f32_16x16x32_bf16 v[108:111], v[140:143], v[76:79], 0\n"
        "v_mul_f32 v96, v117, v96\n"
        "v_mul_f32 v97, v117, v97\n"
        "v_mfma_f32_16x16x32_bf16 v[108:111], v[136:139], v[72:75], v[108:111]\n"
        "v_mul_f32 v98, v117, v98\n"
        "v_mul_f32 v99, v117, v99\n"
        "v_mfma_f32_16x16x32_bf16 v[112:115], v[144:147], v[72:75], 0\n"
        "ds_read_b128 a[228:231], v248 offset:36896\n"
        "v_cvt_pk_bf16_f32 v96, v96, v97\n"
        "v_mfma_f32_16x16x32_bf16 v[112:115], v[148:151], v[76:79], v[112:115]\n"
        "s_waitcnt lgkmcnt(14)\n"
        "ds_read_b128 a[232:235], v248 offset:43040\n"
        "v_cvt_pk_bf16_f32 v97, v98, v99\n"
        "v_mfma_f32_16x16x32_bf16 v[224:227], v[160:163], a[240:243], 0\n"
        "s_waitcnt lgkmcnt(14)\n"
        "ds_write_b32 v246, v96 offset:0\n"
        "v_mfma_f32_16x16x32_bf16 v[224:227], v[152:155], a[244:247], v[224:227]\n"
        "ds_write_b32 v246, v97 offset:4\n"
        "v_mfma_f32_16x16x32_bf16 v[196:199], v[164:167], a[244:247], 0\n"
        "ds_read_b128 a[236:239], v254 offset:36912\n"
        "v_add_f32 v100, v100, v101\n"
        "v_mfma_f32_16x16x32_bf16 v[196:199], v[156:159], a[240:243], v[196:199]\n"
        "s_waitcnt lgkmcnt(15)\n"
        "v_rcp_f32 v101, v100\n"
        "v_mfma_f32_16x16x32_bf16 v[172:175], v[232:235], a[248:251], v[172:175]\n"
        "s_waitcnt lgkmcnt(9)\n"
        "ds_read_b128 a[240:243], v254 offset:43056\n"
        "v_mul_f32 v104, v101, v104\n"
        "v_mfma_f32_16x16x32_bf16 v[172:175], v[64:67], a[216:219], v[172:175]\n"
        "ds_read_b128 a[244:247], v248 offset:36928\n"
        "v_mul_f32 v105, v101, v105\n"
        "v_mfma_f32_16x16x32_bf16 v[200:203], v[68:71], a[216:219], v[200:203]\n"
        "v_mul_f32 v106, v101, v106\n"
        "v_mul_f32 v107, v101, v107\n"
        "v_mfma_f32_16x16x32_bf16 v[200:203], v[236:239], a[248:251], v[200:203]\n"
        "s_waitcnt lgkmcnt(10)\n"
        "ds_read_b128 a[248:251], v248 offset:43072\n"
        "v_cvt_pk_bf16_f32 v104, v104, v105\n"
        "v_mfma_f32_16x16x32_bf16 v[204:207], v[236:239], a[220:223], v[204:207]\n"
        "s_waitcnt lgkmcnt(10)\n"
        "ds_read_b128 a[216:219], v254 offset:36944\n"
        "v_mov_b64 v[116:117], 0\n"
        "v_mfma_f32_16x16x32_bf16 v[204:207], v[68:71], a[224:227], v[204:207]\n"
        "v_cvt_pk_bf16_f32 v105, v106, v107\n"
        "v_mul_f32 v108, v101, v108\n"
        "v_mfma_f32_16x16x32_bf16 v[176:179], v[64:67], a[224:227], v[176:179]\n"
        "ds_write_b32 v245, v104 offset:256\n"
        "v_mfma_f32_16x16x32_bf16 v[176:179], v[232:235], a[220:223], v[176:179]\n"
        "s_waitcnt lgkmcnt(9)\n"
        "ds_write_b32 v245, v105 offset:260\n"
        "v_mfma_f32_16x16x32_bf16 v[180:183], v[232:235], a[228:231], v[180:183]\n"
        "s_waitcnt lgkmcnt(9)\n"
        "ds_read_b128 a[220:223], v254 offset:43088\n"
        "v_mul_f32 v109, v101, v109\n"
        "v_mfma_f32_16x16x32_bf16 v[180:183], v[64:67], a[232:235], v[180:183]\n"
        "v_maximum3_f32 v253, v253, v100, v101\n"
        "v_mul_f32 v110, v101, v110\n"
        "v_mfma_f32_16x16x32_bf16 v[212:215], v[68:71], a[232:235], v[212:215]\n"
        "v_mul_f32 v111, v101, v111\n"
        "v_cvt_pk_bf16_f32 v108, v108, v109\n"
        "v_mfma_f32_16x16x32_bf16 v[212:215], v[236:239], a[228:231], v[212:215]\n"
        "s_waitcnt lgkmcnt(7)\n"
        "ds_read_b128 v[156:159], v247 offset:16384\n"
        "v_cvt_pk_bf16_f32 v109, v110, v111\n"
        "v_mfma_f32_16x16x32_bf16 v[216:219], v[236:239], a[236:239], v[216:219]\n"
        "s_waitcnt lgkmcnt(7)\n"
        "ds_write_b32 v245, v108 offset:264\n"
        "v_mfma_f32_16x16x32_bf16 v[216:219], v[68:71], a[240:243], v[216:219]\n"
        "ds_write_b32 v245, v109 offset:268\n"
        "v_mfma_f32_16x16x32_bf16 v[184:187], v[64:67], a[240:243], v[184:187]\n"
        "v_mul_f32 v112, v101, v112\n"
        "v_mul_f32 v113, v101, v113\n"
        "v_mfma_f32_16x16x32_bf16 v[184:187], v[232:235], a[236:239], v[184:187]\n"
        "v_mul_f32 v114, v101, v114\n"
        "v_mul_f32 v115, v101, v115\n"
        "v_smfmac_f32_16x16x64_bf16 v[116:119], a[252:255], v[80:87], v255\n"
        "ds_read_b128 v[164:167], v247 offset:20480\n"
        "v_cvt_pk_bf16_f32 v112, v112, v113\n"
        "v_mfma_f32_16x16x32_bf16 v[120:123], v[128:131], v[80:83], 0\n"
        "ds_read_b128 v[160:163], v247 offset:16640\n"
        "v_cvt_pk_bf16_f32 v113, v114, v115\n"
        "v_mfma_f32_16x16x32_bf16 v[120:123], v[132:135], v[84:87], v[120:123]\n"
        "ds_write_b32 v246, v112 offset:256\n"
        "v_mfma_f32_16x16x32_bf16 v[124:127], v[140:143], v[84:87], 0\n"
        "ds_write_b32 v246, v113 offset:260\n"
        "v_mfma_f32_16x16x32_bf16 v[124:127], v[136:139], v[80:83], v[124:127]\n"
        "ds_read_b128 a[224:227], v248 offset:12288\n"
        "v_add_f32 v116, v116, v117\n"
        "v_mfma_f32_16x16x32_bf16 v[96:99], v[144:147], v[80:83], 0\n"
        "ds_read_b128 v[152:155], v247 offset:20736\n"
        "v_mfma_f32_16x16x32_bf16 v[96:99], v[148:151], v[84:87], v[96:99]\n"
        "s_waitcnt lgkmcnt(14)\n"
        "ds_read_b128 a[228:231], v248 offset:49152\n"
        "v_mfma_f32_16x16x32_bf16 v[192:195], v[232:235], a[244:247], v[192:195]\n"
        "s_waitcnt lgkmcnt(14)\n"
        "v_rcp_f32 v117, v116\n"
        "v_mfma_f32_16x16x32_bf16 v[192:195], v[64:67], a[248:251], v[192:195]\n"
        "ds_read_b128 a[232:235], v254 offset:12304\n"
        "v_mul_f32 v120, v117, v120\n"
        "v_mfma_f32_16x16x32_bf16 v[220:223], v[68:71], a[248:251], v[220:223]\n"
        "ds_read_b128 a[236:239], v254 offset:49168\n"
        "v_mul_f32 v121, v117, v121\n"
        "v_mfma_f32_16x16x32_bf16 v[220:223], v[236:239], a[244:247], v[220:223]\n"
        "s_waitcnt lgkmcnt(15)\n"
        "ds_read_b128 a[240:243], v248 offset:12320\n"
        "v_mul_f32 v122, v117, v122\n"
        "v_mfma_f32_16x16x32_bf16 v[224:227], v[236:239], a[216:219], v[224:227]\n"
        "s_waitcnt lgkmcnt(13)\n"
        "ds_read_b128 a[244:247], v248 offset:49184\n"
        "v_mul_f32 v123, v117, v123\n"
        "v_mfma_f32_16x16x32_bf16 v[224:227], v[68:71], a[220:223], v[224:227]\n"
        "ds_read_b128 a[248:251], v254 offset:12336\n"
        "v_cvt_pk_bf16_f32 v120, v120, v121\n"
        "v_mfma_f32_16x16x32_bf16 v[196:199], v[64:67], a[220:223], v[196:199]\n"
        "ds_read_b128 a[220:223], v248 offset:12352\n"
        "v_cvt_pk_bf16_f32 v121, v122, v123\n"
        "v_mfma_f32_16x16x32_bf16 v[196:199], v[232:235], a[216:219], v[196:199]\n"
        "s_waitcnt lgkmcnt(8)\n"
        "ds_write_b32 v245, v120 offset:512\n"
        "v_mfma_f32_16x16x32_bf16 v[172:175], v[156:159], a[224:227], v[172:175]\n"
        "s_waitcnt lgkmcnt(7)\n"
        "ds_read_b128 a[216:219], v254 offset:49200\n"
        "v_mul_f32 v124, v117, v124\n"
        "v_mfma_f32_16x16x32_bf16 v[172:175], v[164:167], a[228:231], v[172:175]\n"
        "ds_write_b32 v245, v121 offset:516\n"
        "v_mfma_f32_16x16x32_bf16 v[200:203], v[152:155], a[228:231], v[200:203]\n"
        "ds_read_b128 a[228:231], v254 offset:12368\n"
        "v_mul_f32 v125, v117, v125\n"
        "v_mfma_f32_16x16x32_bf16 v[200:203], v[160:163], a[224:227], v[200:203]\n"
        "s_waitcnt lgkmcnt(9)\n"
        "ds_read_b128 a[224:227], v248 offset:49216\n"
        "v_maximum3_f32 v253, v253, v116, v117\n"
        "v_mfma_f32_16x16x32_bf16 v[204:207], v[160:163], a[232:235], v[204:207]\n"
        "s_waitcnt lgkmcnt(9)\n"
        "v_mul_f32 v126, v117, v126\n"
        "v_mul_f32 v127, v117, v127\n"
        "v_mfma_f32_16x16x32_bf16 v[204:207], v[152:155], a[236:239], v[204:207]\n"
        "v_cvt_pk_bf16_f32 v124, v124, v125\n"
        "v_cvt_pk_bf16_f32 v125, v126, v127\n"
        "v_mfma_f32_16x16x32_bf16 v[176:179], v[164:167], a[236:239], v[176:179]\n"
        "ds_write_b32 v245, v124 offset:520\n"
        "v_mfma_f32_16x16x32_bf16 v[176:179], v[156:159], a[232:235], v[176:179]\n"
        "s_waitcnt lgkmcnt(9)\n"
        "ds_write_b32 v245, v125 offset:524\n"
        "v_mfma_f32_16x16x32_bf16 v[180:183], v[156:159], a[240:243], v[180:183]\n"
        "s_waitcnt lgkmcnt(9)\n"
        "ds_read_b128 a[232:235], v254 offset:49232\n"
        "v_mul_f32 v96, v117, v96\n"
        "v_mfma_f32_16x16x32_bf16 v[180:183], v[164:167], a[244:247], v[180:183]\n"
        "v_mul_f32 v97, v117, v97\n"
        "v_mul_f32 v98, v117, v98\n"
        "v_mfma_f32_16x16x32_bf16 v[212:215], v[152:155], a[244:247], v[212:215]\n"
        "v_mul_f32 v99, v117, v99\n"
        "v_cvt_pk_bf16_f32 v96, v96, v97\n"
        "v_mfma_f32_16x16x32_bf16 v[212:215], v[160:163], a[240:243], v[212:215]\n"
        "s_waitcnt lgkmcnt(9)\n"
        "v_cvt_pk_bf16_f32 v97, v98, v99\n"
        "v_add_u32 v247, s31, v247\n"
        "v_mfma_f32_16x16x32_bf16 v[216:219], v[160:163], a[248:251], v[216:219]\n"
        "s_waitcnt lgkmcnt(6)\n"
        "v_mov_b64 v[80:81], 0\n"
        "v_mfma_f32_16x16x32_bf16 v[216:219], v[152:155], a[216:219], v[216:219]\n"
        "ds_write_b32 v246, v96 offset:512\n"
        "v_mfma_f32_16x16x32_bf16 v[184:187], v[164:167], a[216:219], v[184:187]\n"
        "s_waitcnt vmcnt(0)\n"
        "ds_write_b32 v246, v97 offset:516\n"
        "v_mfma_f32_16x16x32_bf16 v[184:187], v[156:159], a[248:251], v[184:187]\n"
        "s_barrier\n"
        "v_mfma_f32_16x16x32_bf16 v[192:195], v[156:159], a[220:223], v[192:195]\n"
        "s_waitcnt lgkmcnt(5)\n"
        "ds_read_b32 v108, v245 offset:768\n"
        "v_mfma_f32_16x16x32_bf16 v[192:195], v[164:167], a[224:227], v[192:195]\n"
        "ds_read_b128 a[236:239], v240 offset:0\n"
        "v_mfma_f32_16x16x32_bf16 v[220:223], v[152:155], a[224:227], v[220:223]\n"
        "ds_read_b128 a[240:243], v241 offset:0\n"
        "v_mfma_f32_16x16x32_bf16 v[220:223], v[160:163], a[220:223], v[220:223]\n"
        "ds_read_b128 a[244:247], v240 offset:128\n"
        "v_mfma_f32_16x16x32_bf16 v[224:227], v[160:163], a[228:231], v[224:227]\n"
        "s_waitcnt lgkmcnt(6)\n"
        "ds_read_b128 a[248:251], v241 offset:128\n"
        "v_mfma_f32_16x16x32_bf16 v[224:227], v[152:155], a[232:235], v[224:227]\n"
        "ds_read_b128 a[216:219], v240 offset:256\n"
        "v_mfma_f32_16x16x32_bf16 v[196:199], v[164:167], a[232:235], v[196:199]\n"
        "ds_read_b128 a[220:223], v241 offset:256\n"
        "v_mfma_f32_16x16x32_bf16 v[196:199], v[156:159], a[228:231], v[196:199]\n"
        "v_smfmac_f32_16x16x64_bf16 v[80:83], a[252:255], v[88:95], v255\n"
        "s_waitcnt lgkmcnt(6)\n"
        "v_readfirstlane_b32 s59, v108\n"
        "v_mfma_f32_16x16x32_bf16 v[84:87], v[128:131], v[88:91], 0\n"
        "v_mfma_f32_16x16x32_bf16 v[84:87], v[132:135], v[92:95], v[84:87]\n"
        "s_min_u32 s58, s63, s59\n"
        "s_cmp_lt_u32 s58, s49\n"
        "v_mfma_f32_16x16x32_bf16 v[232:235], v[140:143], v[92:95], 0\n"
        "s_cbranch_scc1 10b\n"
        "v_mfma_f32_16x16x32_bf16 v[232:235], v[136:139], v[88:91], v[232:235]\n"
        "v_mfma_f32_16x16x32_bf16 v[236:239], v[144:147], v[88:91], 0\n"
        "v_mfma_f32_16x16x32_bf16 v[236:239], v[148:151], v[92:95], v[236:239]\n"
        "20:\n"
        "s_waitcnt lgkmcnt(0)\n"
        "v_mfma_f32_16x16x32_bf16 v[96:99], a[72:75], a[236:239], 0\n"
        "v_and_b32 v242, 60, v250\n"
        "v_add3_u32 v242, v250, v242, s26\n"
        "v_mfma_f32_16x16x32_bf16 v[96:99], a[76:79], a[240:243], v[96:99]\n"
        "v_cvt_pk_bf16_f32 v108, v172, v176\n"
        "v_cvt_pk_bf16_f32 v109, v180, v184\n"
        "v_mfma_f32_16x16x32_bf16 v[96:99], a[80:83], a[244:247], v[96:99]\n"
        "v_cvt_pk_bf16_f32 v110, v192, v196\n"
        "v_add_f32 v80, v80, v81\n"
        "v_mfma_f32_16x16x32_bf16 v[96:99], a[84:87], a[248:251], v[96:99]\n"
        "buffer_store_dwordx2 v[108:109], v242, s[16:19], s38 offen offset:0 sc1\n"
        "v_mfma_f32_16x16x32_bf16 v[96:99], a[88:91], a[216:219], v[96:99]\n"
        "v_rcp_f32 v81, v80\n"
        "v_mfma_f32_16x16x32_bf16 v[96:99], a[92:95], a[220:223], v[96:99]\n"
        "45:\n"
        "s_cbranch_execz 40b\n"
        "v_cvt_pk_bf16_f32 v112, v173, v177\n"
        "v_cvt_pk_bf16_f32 v113, v181, v185\n"
        "v_mfma_f32_16x16x32_bf16 v[100:103], a[116:119], a[220:223], 0\n"
        "v_mul_f32 v84, v81, v84\n"
        "v_mul_f32 v85, v81, v85\n"
        "v_mfma_f32_16x16x32_bf16 v[100:103], a[112:115], a[216:219], v[100:103]\n"
        "v_mul_f32 v86, v81, v86\n"
        "v_mul_f32 v87, v81, v87\n"
        "v_mfma_f32_16x16x32_bf16 v[100:103], a[108:111], a[248:251], v[100:103]\n"
        "s_sub_u32 s31, s29, s30\n"
        "buffer_store_dword v110, v250, s[16:19], s38 offen offset:0 sc1\n"
        "v_mfma_f32_16x16x32_bf16 v[100:103], a[104:107], a[244:247], v[100:103]\n"
        "v_cvt_pk_bf16_f32 v152, v96, v97\n"
        "v_cvt_pk_bf16_f32 v153, v98, v99\n"
        "v_mfma_f32_16x16x32_bf16 v[100:103], a[100:103], a[240:243], v[100:103]\n"
        "v_cvt_pk_bf16_f32 v114, v193, v197\n"
        "v_cvt_pk_bf16_f32 v84, v84, v85\n"
        "v_mfma_f32_16x16x32_bf16 v[100:103], a[96:99], a[236:239], v[100:103]\n"
        "ds_write_b32 v245, v84 offset:768\n"
        "v_mfma_f32_16x16x32_bf16 v[104:107], a[120:123], a[236:239], 0\n"
        "v_cvt_pk_bf16_f32 v85, v86, v87\n"
        "v_mul_f32 v232, v81, v232\n"
        "v_mfma_f32_16x16x32_bf16 v[104:107], a[124:127], a[240:243], v[104:107]\n"
        "buffer_store_dwordx2 v[112:113], v242, s[16:19], s38 offen offset:384 sc1\n"
        "v_mfma_f32_16x16x32_bf16 v[104:107], a[128:131], a[244:247], v[104:107]\n"
        "ds_write_b32 v245, v85 offset:772\n"
        "v_mfma_f32_16x16x32_bf16 v[104:107], a[132:135], a[248:251], v[104:107]\n"
        "v_cvt_pk_bf16_f32 v154, v100, v101\n"
        "v_cvt_pk_bf16_f32 v155, v102, v103\n"
        "v_mfma_f32_16x16x32_bf16 v[104:107], a[136:139], a[216:219], v[104:107]\n"
        "v_cvt_pk_bf16_f32 v108, v174, v178\n"
        "v_cvt_pk_bf16_f32 v109, v182, v186\n"
        "v_mfma_f32_16x16x32_bf16 v[104:107], a[140:143], a[220:223], v[104:107]\n"
        "v_mul_f32 v233, v81, v233\n"
        "v_maximum3_f32 v253, v253, v80, v81\n"
        "v_mfma_f32_16x16x32_bf16 v[96:99], a[20:23], a[220:223], 0\n"
        "buffer_store_dword v114, v250, s[16:19], s38 offen offset:384 sc1\n"
        "v_mfma_f32_16x16x32_bf16 v[96:99], a[16:19], a[216:219], v[96:99]\n"
        "v_cvt_pk_bf16_f32 v110, v194, v198\n"
        "v_mul_f32 v234, v81, v234\n"
        "v_mfma_f32_16x16x32_bf16 v[96:99], a[12:15], a[248:251], v[96:99]\n"
        "v_cvt_pk_bf16_f32 v168, v104, v105\n"
        "v_cvt_pk_bf16_f32 v169, v106, v107\n"
        "v_mfma_f32_16x16x32_bf16 v[96:99], a[8:11], a[244:247], v[96:99]\n"
        "v_mul_f32 v235, v81, v235\n"
        "v_cvt_pk_bf16_f32 v232, v232, v233\n"
        "v_mfma_f32_16x16x32_bf16 v[96:99], a[4:7], a[240:243], v[96:99]\n"
        "ds_write_b32 v245, v232 offset:776\n"
        "v_mfma_f32_16x16x32_bf16 v[96:99], a[0:3], a[236:239], v[96:99]\n"
        "buffer_store_dwordx2 v[108:109], v242, s[16:19], s38 offen offset:768 sc1\n"
        "v_mfma_f32_16x16x32_bf16 v[100:103], a[24:27], a[236:239], 0\n"
        "v_cvt_pk_bf16_f32 v112, v175, v179\n"
        "v_cvt_pk_bf16_f32 v113, v183, v187\n"
        "v_mfma_f32_16x16x32_bf16 v[100:103], a[28:31], a[240:243], v[100:103]\n"
        "v_cvt_pk_bf16_f32 v233, v234, v235\n"
        "v_xor_b32 v251, 0x2000, v251\n"
        "v_mfma_f32_16x16x32_bf16 v[100:103], a[32:35], a[244:247], v[100:103]\n"
        "v_mul_f32 v96, s50, v96\n"
        "v_mul_f32 v97, s50, v97\n"
        "v_mfma_f32_16x16x32_bf16 v[100:103], a[36:39], a[248:251], v[100:103]\n"
        "v_mul_f32 v98, s50, v98\n"
        "v_mul_f32 v99, s50, v99\n"
        "v_mfma_f32_16x16x32_bf16 v[100:103], a[40:43], a[216:219], v[100:103]\n"
        "buffer_store_dword v110, v250, s[16:19], s38 offen offset:768 sc1\n"
        "v_mfma_f32_16x16x32_bf16 v[100:103], a[44:47], a[220:223], v[100:103]\n"
        "v_cvt_pk_bf16_f32 v156, v96, v97\n"
        "v_cvt_pk_bf16_f32 v157, v98, v99\n"
        "v_mfma_f32_16x16x32_bf16 v[104:107], a[68:71], a[220:223], 0\n"
        "ds_write_b32 v245, v233 offset:780\n"
        "v_mfma_f32_16x16x32_bf16 v[104:107], a[64:67], a[216:219], v[104:107]\n"
        "v_cvt_pk_bf16_f32 v114, v195, v199\n"
        "v_xor_b32 v252, 0x2000, v252\n"
        "v_mfma_f32_16x16x32_bf16 v[104:107], a[60:63], a[248:251], v[104:107]\n"
        "v_mul_f32 v100, s50, v100\n"
        "v_mul_f32 v101, s50, v101\n"
        "v_mfma_f32_16x16x32_bf16 v[104:107], a[56:59], a[244:247], v[104:107]\n"
        "buffer_store_dwordx2 v[112:113], v242, s[16:19], s38 offen offset:1152 sc1\n"
        "v_mfma_f32_16x16x32_bf16 v[104:107], a[52:55], a[240:243], v[104:107]\n"
        "v_mul_f32 v102, s50, v102\n"
        "v_mul_f32 v103, s50, v103\n"
        "v_mfma_f32_16x16x32_bf16 v[104:107], a[48:51], a[236:239], v[104:107]\n"
        "v_cvt_pk_bf16_f32 v158, v100, v101\n"
        "v_cvt_pk_bf16_f32 v159, v102, v103\n"
        "v_mfma_f32_16x16x32_bf16 v[96:99], a[236:239], a[144:147], 0\n"
        "ds_read_b128 a[224:227], v240 offset:1536\n"
        "v_cvt_pk_bf16_f32 v108, v200, v204\n"
        "v_mfma_f32_16x16x32_bf16 v[96:99], a[240:243], a[148:151], v[96:99]\n"
        "ds_read_b128 a[228:231], v241 offset:1536\n"
        "v_cvt_pk_bf16_f32 v109, v212, v216\n"
        "v_mfma_f32_16x16x32_bf16 v[96:99], a[244:247], a[152:155], v[96:99]\n"
        "buffer_store_dword v114, v250, s[16:19], s38 offen offset:1152 sc1\n"
        "v_mfma_f32_16x16x32_bf16 v[96:99], a[248:251], a[156:159], v[96:99]\n"
        "v_mul_f32 v104, s50, v104\n"
        "v_mul_f32 v105, s50, v105\n"
        "v_mfma_f32_16x16x32_bf16 v[96:99], a[216:219], a[160:163], v[96:99]\n"
        "v_mul_f32 v106, s50, v106\n"
        "v_mul_f32 v107, s50, v107\n"
        "v_mfma_f32_16x16x32_bf16 v[96:99], a[220:223], a[164:167], v[96:99]\n"
        "v_cvt_pk_bf16_f32 v160, v104, v105\n"
        "v_cvt_pk_bf16_f32 v161, v106, v107\n"
        "v_mfma_f32_16x16x32_bf16 v[100:103], a[220:223], a[188:191], 0\n"
        "ds_read_b128 a[232:235], v240 offset:1664\n"
        "v_mov_b64 v[166:167], v[160:161]\n"
        "v_mfma_f32_16x16x32_bf16 v[100:103], a[216:219], a[184:187], v[100:103]\n"
        "buffer_store_dwordx2 v[108:109], v242, s[16:19], s39 offen offset:0 sc1\n"
        "v_mfma_f32_16x16x32_bf16 v[100:103], a[248:251], a[180:183], v[100:103]\n"
        "v_cvt_pk_bf16_f32 v128, v96, v97\n"
        "v_cvt_pk_bf16_f32 v129, v98, v99\n"
        "v_mfma_f32_16x16x32_bf16 v[100:103], a[244:247], a[176:179], v[100:103]\n"
        "v_cvt_pk_bf16_f32 v110, v220, v224\n"
        "v_cvt_pk_bf16_f32 v112, v201, v205\n"
        "v_mfma_f32_16x16x32_bf16 v[100:103], a[240:243], a[172:175], v[100:103]\n"
        "v_cvt_pk_bf16_f32 v113, v213, v217\n"
        "v_mul_f32 v236, v81, v236\n"
        "v_mfma_f32_16x16x32_bf16 v[100:103], a[236:239], a[168:171], v[100:103]\n"
        "v_mul_f32 v237, v81, v237\n"
        "v_mul_f32 v238, v81, v238\n"
        "v_mfma_f32_16x16x32_bf16 v[104:107], a[236:239], a[192:195], 0\n"
        "buffer_store_dword v110, v250, s[16:19], s39 offen offset:0 sc1\n"
        "v_mfma_f32_16x16x32_bf16 v[104:107], a[240:243], a[196:199], v[104:107]\n"
        "ds_read_b128 a[236:239], v241 offset:1664\n"
        "ds_read_b128 a[240:243], v240 offset:1792\n"
        "v_mfma_f32_16x16x32_bf16 v[104:107], a[244:247], a[200:203], v[104:107]\n"
        "ds_read_b128 a[244:247], v241 offset:1792\n"
        "v_cvt_pk_bf16_f32 v136, v100, v101\n"
        "v_mfma_f32_16x16x32_bf16 v[104:107], a[248:251], a[204:207], v[104:107]\n"
        "v_cvt_pk_bf16_f32 v137, v102, v103\n"
        "v_mul_f32 v239, v81, v239\n"
        "v_mfma_f32_16x16x32_bf16 v[104:107], a[216:219], a[208:211], v[104:107]\n"
        "v_cvt_pk_bf16_f32 v236, v236, v237\n"
        "v_cvt_pk_bf16_f32 v237, v238, v239\n"
        "v_mfma_f32_16x16x32_bf16 v[104:107], a[220:223], a[212:215], v[104:107]\n"
        "s_waitcnt lgkmcnt(5)\n"
        "buffer_store_dwordx2 v[112:113], v242, s[16:19], s39 offen offset:384 sc1\n"
        "v_mfma_f32_16x16x32_bf16 v[96:99], a[72:75], a[224:227], 0\n"
        "s_waitcnt lgkmcnt(4)\n"
        "ds_write_b32 v246, v236 offset:768\n"
        "v_mfma_f32_16x16x32_bf16 v[96:99], a[76:79], a[228:231], v[96:99]\n"
        "s_waitcnt lgkmcnt(4)\n"
        "ds_write_b32 v246, v237 offset:772\n"
        "v_mfma_f32_16x16x32_bf16 v[96:99], a[80:83], a[232:235], v[96:99]\n"
        "s_waitcnt lgkmcnt(4)\n"
        "ds_read_b128 v[162:165], v251 offset:0\n"
        "v_cvt_pk_bf16_f32 v114, v221, v225\n"
        "v_mfma_f32_16x16x32_bf16 v[96:99], a[84:87], a[236:239], v[96:99]\n"
        "s_waitcnt lgkmcnt(4)\n"
        "v_cvt_pk_bf16_f32 v144, v104, v105\n"
        "v_cvt_pk_bf16_f32 v145, v106, v107\n"
        "v_mfma_f32_16x16x32_bf16 v[96:99], a[88:91], a[240:243], v[96:99]\n"
        "s_waitcnt lgkmcnt(3)\n"
        "buffer_store_dword v114, v250, s[16:19], s39 offen offset:384 sc1\n"
        "v_mfma_f32_16x16x32_bf16 v[96:99], a[92:95], a[244:247], v[96:99]\n"
        "v_cvt_pk_bf16_f32 v108, v202, v206\n"
        "v_cvt_pk_bf16_f32 v109, v214, v218\n"
        "v_mfma_f32_16x16x32_bf16 v[100:103], a[116:119], a[244:247], 0\n"
        "v_cvt_pk_bf16_f32 v110, v222, v226\n"
        "v_add_u32 v245, s31, v245\n"
        "v_mfma_f32_16x16x32_bf16 v[100:103], a[112:115], a[240:243], v[100:103]\n"
        "v_add_u32 v246, s31, v246\n"
        "v_mfma_f32_16x16x32_bf16 v[100:103], a[108:111], a[236:239], v[100:103]\n"
        "v_cvt_pk_bf16_f32 v172, v96, v97\n"
        "v_cvt_pk_bf16_f32 v173, v98, v99\n"
        "v_mfma_f32_16x16x32_bf16 v[100:103], a[104:107], a[232:235], v[100:103]\n"
        "buffer_store_dwordx2 v[108:109], v242, s[16:19], s39 offen offset:768 sc1\n"
        "v_mfma_f32_16x16x32_bf16 v[100:103], a[100:103], a[228:231], v[100:103]\n"
        "v_cvt_pk_bf16_f32 v112, v203, v207\n"
        "v_cvt_pk_bf16_f32 v113, v215, v219\n"
        "v_mfma_f32_16x16x32_bf16 v[100:103], a[96:99], a[224:227], v[100:103]\n"
        "s_nop 2\n"
        "v_mfma_f32_16x16x32_bf16 v[104:107], a[120:123], a[224:227], 0\n"
        "s_nop 2\n"
        "v_mfma_f32_16x16x32_bf16 v[104:107], a[124:127], a[228:231], v[104:107]\n"
        "v_cvt_pk_bf16_f32 v174, v100, v101\n"
        "v_cvt_pk_bf16_f32 v175, v102, v103\n"
        "v_mfma_f32_16x16x32_bf16 v[104:107], a[128:131], a[232:235], v[104:107]\n"
        "buffer_store_dword v110, v250, s[16:19], s39 offen offset:768 sc1\n"
        "v_mfma_f32_16x16x32_bf16 v[104:107], a[132:135], a[236:239], v[104:107]\n"
        "v_cvt_pk_bf16_f32 v114, v223, v227\n"
        "v_mfma_f32_16x16x32_bf16 v[104:107], a[136:139], a[240:243], v[104:107]\n"
        "v_mfma_f32_16x16x32_bf16 v[104:107], a[140:143], a[244:247], v[104:107]\n"
        "s_nop 2\n"
        "v_mfma_f32_16x16x32_bf16 v[96:99], a[20:23], a[244:247], 0\n"
        "ds_read_b128 a[248:251], v240 offset:12288\n"
        "s_nop 0\n"
        "v_mfma_f32_16x16x32_bf16 v[96:99], a[16:19], a[240:243], v[96:99]\n"
        "buffer_store_dwordx2 v[112:113], v242, s[16:19], s39 offen offset:1152 sc1\n"
        "v_mfma_f32_16x16x32_bf16 v[96:99], a[12:15], a[236:239], v[96:99]\n"
        "ds_read_b128 a[216:219], v241 offset:12288\n"
        "v_cvt_pk_bf16_f32 v190, v104, v105\n"
        "v_mfma_f32_16x16x32_bf16 v[96:99], a[8:11], a[232:235], v[96:99]\n"
        "ds_read_b128 a[220:223], v240 offset:12416\n"
        "v_cvt_pk_bf16_f32 v191, v106, v107\n"
        "v_mfma_f32_16x16x32_bf16 v[96:99], a[4:7], a[228:231], v[96:99]\n"
        "ds_read_b128 v[182:185], v251 offset:512\n"
        "v_mfma_f32_16x16x32_bf16 v[96:99], a[0:3], a[224:227], v[96:99]\n"
        "s_nop 2\n"
        "v_mfma_f32_16x16x32_bf16 v[0:3], v[152:155], v[156:159], v[0:3]\n"
        "s_waitcnt lgkmcnt(4)\n"
        "buffer_store_dword v114, v250, s[16:19], s39 offen offset:1152 sc1\n"
        "v_mfma_f32_16x16x32_bf16 v[0:3], v[168:171], v[160:163], v[0:3]\n"
        "s_nop 0\n"
        "v_mfma_f32_16x16x32_bf16 v[4:7], v[172:175], v[156:159], v[4:7]\n"
        "v_mul_f32 v96, s50, v96\n"
        "v_mul_f32 v97, s50, v97\n"
        "v_mfma_f32_16x16x32_bf16 v[4:7], v[188:191], v[164:167], v[4:7]\n"
        "v_mul_f32 v98, s50, v98\n"
        "v_mul_f32 v99, s50, v99\n"
        "v_mfma_f32_16x16x32_bf16 v[100:103], a[24:27], a[224:227], 0\n"
        "v_cvt_pk_bf16_f32 v176, v96, v97\n"
        "v_cvt_pk_bf16_f32 v177, v98, v99\n"
        "v_mfma_f32_16x16x32_bf16 v[100:103], a[28:31], a[228:231], v[100:103]\n"
        "v_exp_f32 v0, v0\n"
        "v_mfma_f32_16x16x32_bf16 v[100:103], a[32:35], a[232:235], v[100:103]\n"
        "v_exp_f32 v1, v1\n"
        "v_mfma_f32_16x16x32_bf16 v[100:103], a[36:39], a[236:239], v[100:103]\n"
        "v_exp_f32 v2, v2\n"
        "v_mfma_f32_16x16x32_bf16 v[100:103], a[40:43], a[240:243], v[100:103]\n"
        "v_exp_f32 v3, v3\n"
        "v_mfma_f32_16x16x32_bf16 v[100:103], a[44:47], a[244:247], v[100:103]\n"
        "v_exp_f32 v4, v4\n"
        "v_mfma_f32_16x16x32_bf16 v[104:107], a[68:71], a[244:247], 0\n"
        "ds_read_b128 a[244:247], v241 offset:12416\n"
        "v_cvt_pk_bf16_f32 v64, v0, v1\n"
        "v_mfma_f32_16x16x32_bf16 v[104:107], a[64:67], a[240:243], v[104:107]\n"
        "ds_read_b128 a[240:243], v240 offset:12544\n"
        "v_cvt_pk_bf16_f32 v65, v2, v3\n"
        "v_mfma_f32_16x16x32_bf16 v[104:107], a[60:63], a[236:239], v[104:107]\n"
        "s_barrier\n"
        "ds_read_b128 a[236:239], v241 offset:12544\n"
        "v_mul_f32 v100, s50, v100\n"
        "v_mfma_f32_16x16x32_bf16 v[104:107], a[56:59], a[232:235], v[104:107]\n"
        "v_mul_f32 v101, s50, v101\n"
        "v_mul_f32 v102, s50, v102\n"
        "v_mfma_f32_16x16x32_bf16 v[104:107], a[52:55], a[228:231], v[104:107]\n"
        "v_mul_f32 v103, s50, v103\n"
        "v_cvt_pk_bf16_f32 v178, v100, v101\n"
        "v_mfma_f32_16x16x32_bf16 v[104:107], a[48:51], a[224:227], v[104:107]\n"
        "s_waitcnt lgkmcnt(6)\n"
        "v_cvt_pk_bf16_f32 v179, v102, v103\n"
        "v_mfma_f32_16x16x32_bf16 v[96:99], a[72:75], a[248:251], 0\n"
        "s_waitcnt lgkmcnt(5)\n"
        "v_exp_f32 v5, v5\n"
        "v_mfma_f32_16x16x32_bf16 v[96:99], a[76:79], a[216:219], v[96:99]\n"
        "s_waitcnt lgkmcnt(4)\n"
        "v_exp_f32 v6, v6\n"
        "v_mfma_f32_16x16x32_bf16 v[96:99], a[80:83], a[220:223], v[96:99]\n"
        "s_waitcnt lgkmcnt(2)\n"
        "v_exp_f32 v7, v7\n"
        "v_mfma_f32_16x16x32_bf16 v[96:99], a[84:87], a[244:247], v[96:99]\n"
        "s_waitcnt lgkmcnt(1)\n"
        "v_mul_f32 v104, s50, v104\n"
        "v_mul_f32 v105, s50, v105\n"
        "v_mfma_f32_16x16x32_bf16 v[96:99], a[88:91], a[240:243], v[96:99]\n"
        "s_waitcnt lgkmcnt(0)\n"
        "v_mul_f32 v106, s50, v106\n"
        "v_mul_f32 v107, s50, v107\n"
        "v_mfma_f32_16x16x32_bf16 v[96:99], a[92:95], a[236:239], v[96:99]\n"
        "v_cvt_pk_bf16_f32 v180, v104, v105\n"
        "v_cvt_pk_bf16_f32 v181, v106, v107\n"
        "v_mfma_f32_16x16x32_bf16 v[16:19], v[152:155], v[176:179], v[16:19]\n"
        "ds_read_b128 v[162:165], v252 offset:0\n"
        "v_mov_b64 v[186:187], v[180:181]\n"
        "v_mfma_f32_16x16x32_bf16 v[16:19], v[168:171], v[180:183], v[16:19]\n"
        "s_lshr_b32 s46, s56, 6\n"
        "v_cvt_pk_bf16_f32 v66, v4, v5\n"
        "v_cvt_pk_bf16_f32 v192, v96, v97\n"
        "v_mfma_f32_16x16x32_bf16 v[20:23], v[172:175], v[176:179], v[20:23]\n"
        "s_mul_i32 s46, s46, 1572864\n"
        "v_cvt_pk_bf16_f32 v193, v98, v99\n"
        "v_cvt_pk_bf16_f32 v67, v6, v7\n"
        "v_mfma_f32_16x16x32_bf16 v[20:23], v[188:191], v[184:187], v[20:23]\n"
        "s_add_u32 s46, s46, s37\n"
        "v_exp_f32 v16, v16\n"
        "v_mfma_f32_16x16x32_bf16 v[100:103], a[116:119], a[236:239], 0\n"
        "s_cmp_eq_u32 s46, 100663296\n"
        "ds_read_b128 v[182:185], v252 offset:512\n"
        "v_add_u32 v243, s31, v247\n"
        "v_mfma_f32_16x16x32_bf16 v[100:103], a[112:115], a[240:243], v[100:103]\n"
        "s_cselect_b32 s46, 0, s46\n"
        "v_exp_f32 v17, v17\n"
        "v_mfma_f32_16x16x32_bf16 v[100:103], a[108:111], a[244:247], v[100:103]\n"
        "s_and_b32 s43, s56, 63\n"
        "v_exp_f32 v18, v18\n"
        "v_mfma_f32_16x16x32_bf16 v[100:103], a[104:107], a[220:223], v[100:103]\n"
        "s_mul_i32 s43, s43, 3072\n"
        "v_exp_f32 v19, v19\n"
        "v_mfma_f32_16x16x32_bf16 v[100:103], a[100:103], a[216:219], v[100:103]\n"
        "s_add_u32 s44, s43, 3072\n"
        "v_exp_f32 v20, v20\n"
        "v_mfma_f32_16x16x32_bf16 v[100:103], a[96:99], a[248:251], v[100:103]\n"
        "s_cmp_eq_u32 s44, 196608\n"
        "v_exp_f32 v21, v21\n"
        "v_mfma_f32_16x16x32_bf16 v[104:107], a[120:123], a[248:251], 0\n"
        "s_cselect_b32 s44, 0, s44\n"
        "v_exp_f32 v22, v22\n"
        "v_mfma_f32_16x16x32_bf16 v[104:107], a[124:127], a[216:219], v[104:107]\n"
        "s_add_u32 s39, s46, s44\n"
        "v_exp_f32 v23, v23\n"
        "v_mfma_f32_16x16x32_bf16 v[104:107], a[128:131], a[220:223], v[104:107]\n"
        "s_add_u32 s43, s43, s46\n"
        "v_cvt_pk_bf16_f32 v194, v100, v101\n"
        "v_cvt_pk_bf16_f32 v195, v102, v103\n"
        "v_mfma_f32_16x16x32_bf16 v[104:107], a[132:135], a[244:247], v[104:107]\n"
        "s_add_u32 s38, s43, 1536\n"
        "v_cvt_pk_bf16_f32 v72, v16, v17\n"
        "v_cvt_pk_bf16_f32 v73, v18, v19\n"
        "v_mfma_f32_16x16x32_bf16 v[104:107], a[136:139], a[240:243], v[104:107]\n"
        "s_lshl_b32 s29, s42, 8\n"
        "v_cvt_pk_bf16_f32 v74, v20, v21\n"
        "v_cvt_pk_bf16_f32 v75, v22, v23\n"
        "v_mfma_f32_16x16x32_bf16 v[104:107], a[140:143], a[236:239], v[104:107]\n"
        "s_add_u32 s29, s29, 147456\n"
        "v_mbcnt_lo_u32_b32 v244, -1, 0\n"
        "v_mbcnt_hi_u32_b32 v244, -1, v244\n"
        "v_mfma_f32_16x16x32_bf16 v[96:99], a[20:23], a[236:239], 0\n"
        "s_mov_b32 s47, 147456\n"
        "ds_read_b128 a[232:235], v240 offset:13824\n"
        "v_lshl_add_u32 v249, v244, 4, s47\n"
        "v_mfma_f32_16x16x32_bf16 v[96:99], a[16:19], a[240:243], v[96:99]\n"
        "ds_read_b128 a[228:231], v241 offset:13824\n"
        "v_cvt_pk_bf16_f32 v208, v104, v105\n"
        "v_mfma_f32_16x16x32_bf16 v[96:99], a[12:15], a[244:247], v[96:99]\n"
        "ds_read_b128 a[224:227], v240 offset:13952\n"
        "v_cvt_pk_bf16_f32 v209, v106, v107\n"
        "v_mfma_f32_16x16x32_bf16 v[96:99], a[8:11], a[220:223], v[96:99]\n"
        "v_lshl_add_u32 v244, v244, 2, s29\n"
        "v_mfma_f32_16x16x32_bf16 v[96:99], a[4:7], a[216:219], v[96:99]\n"
        "ds_read_b128 v[202:205], v251 offset:4096\n"
        "v_mfma_f32_16x16x32_bf16 v[96:99], a[0:3], a[248:251], v[96:99]\n"
        "s_nop 2\n"
        "v_mfma_f32_16x16x32_bf16 v[8:11], v[192:195], v[156:159], v[8:11]\n"
        "s_waitcnt lgkmcnt(5)\n"
        "s_nop 1\n"
        "v_mfma_f32_16x16x32_bf16 v[8:11], v[208:211], v[160:163], v[8:11]\n"
        "v_mfma_f32_16x16x32_bf16 v[24:27], v[192:195], v[176:179], v[24:27]\n"
        "s_waitcnt lgkmcnt(4)\n"
        "v_mul_f32 v96, s50, v96\n"
        "v_mul_f32 v97, s50, v97\n"
        "v_mfma_f32_16x16x32_bf16 v[24:27], v[208:211], v[180:183], v[24:27]\n"
        "v_mul_f32 v98, s50, v98\n"
        "v_mul_f32 v99, s50, v99\n"
        "v_mfma_f32_16x16x32_bf16 v[100:103], a[24:27], a[248:251], 0\n"
        "v_cvt_pk_bf16_f32 v196, v96, v97\n"
        "v_cvt_pk_bf16_f32 v197, v98, v99\n"
        "v_mfma_f32_16x16x32_bf16 v[100:103], a[28:31], a[216:219], v[100:103]\n"
        "v_exp_f32 v8, v8\n"
        "v_mfma_f32_16x16x32_bf16 v[100:103], a[32:35], a[220:223], v[100:103]\n"
        "v_exp_f32 v9, v9\n"
        "v_mfma_f32_16x16x32_bf16 v[100:103], a[36:39], a[244:247], v[100:103]\n"
        "v_exp_f32 v10, v10\n"
        "v_mfma_f32_16x16x32_bf16 v[100:103], a[40:43], a[240:243], v[100:103]\n"
        "s_lshr_b32 s46, s57, 6\n"
        "v_exp_f32 v11, v11\n"
        "v_mfma_f32_16x16x32_bf16 v[100:103], a[44:47], a[236:239], v[100:103]\n"
        "s_mul_i32 s46, s46, 1572864\n"
        "v_exp_f32 v24, v24\n"
        "v_mfma_f32_16x16x32_bf16 v[104:107], a[68:71], a[236:239], 0\n"
        "s_add_u32 s46, s46, s37\n"
        "ds_read_b128 a[236:239], v241 offset:13952\n"
        "v_cvt_pk_bf16_f32 v68, v8, v9\n"
        "v_mfma_f32_16x16x32_bf16 v[104:107], a[64:67], a[240:243], v[104:107]\n"
        "s_cmp_eq_u32 s46, 100663296\n"
        "ds_read_b128 a[240:243], v240 offset:14080\n"
        "v_mul_f32 v100, s50, v100\n"
        "v_mfma_f32_16x16x32_bf16 v[104:107], a[60:63], a[244:247], v[104:107]\n"
        "s_cselect_b32 s46, 0, s46\n"
        "ds_read_b128 a[244:247], v241 offset:14080\n"
        "v_mul_f32 v101, s50, v101\n"
        "v_mfma_f32_16x16x32_bf16 v[104:107], a[56:59], a[220:223], v[104:107]\n"
        "s_and_b32 s43, s57, 63\n"
        "v_mul_f32 v102, s50, v102\n"
        "v_mul_f32 v103, s50, v103\n"
        "v_mfma_f32_16x16x32_bf16 v[104:107], a[52:55], a[216:219], v[104:107]\n"
        "s_mul_i32 s43, s43, 3072\n"
        "v_cvt_pk_bf16_f32 v198, v100, v101\n"
        "v_cvt_pk_bf16_f32 v199, v102, v103\n"
        "v_mfma_f32_16x16x32_bf16 v[104:107], a[48:51], a[248:251], v[104:107]\n"
        "s_waitcnt lgkmcnt(6)\n"
        "v_exp_f32 v25, v25\n"
        "v_mfma_f32_16x16x32_bf16 v[96:99], a[72:75], a[232:235], 0\n"
        "s_waitcnt lgkmcnt(5)\n"
        "v_cvt_pk_bf16_f32 v69, v10, v11\n"
        "v_cvt_pk_bf16_f32 v76, v24, v25\n"
        "v_mfma_f32_16x16x32_bf16 v[96:99], a[76:79], a[228:231], v[96:99]\n"
        "s_waitcnt lgkmcnt(4)\n"
        "v_exp_f32 v26, v26\n"
        "v_mfma_f32_16x16x32_bf16 v[96:99], a[80:83], a[224:227], v[96:99]\n"
        "s_waitcnt lgkmcnt(2)\n"
        "v_exp_f32 v27, v27\n"
        "v_mfma_f32_16x16x32_bf16 v[96:99], a[84:87], a[236:239], v[96:99]\n"
        "s_waitcnt lgkmcnt(1)\n"
        "v_mul_f32 v104, s50, v104\n"
        "v_mul_f32 v105, s50, v105\n"
        "v_mfma_f32_16x16x32_bf16 v[96:99], a[88:91], a[240:243], v[96:99]\n"
        "s_waitcnt lgkmcnt(0)\n"
        "v_mul_f32 v106, s50, v106\n"
        "v_mul_f32 v107, s50, v107\n"
        "v_mfma_f32_16x16x32_bf16 v[96:99], a[92:95], a[244:247], v[96:99]\n"
        "s_add_u32 s44, s43, 3072\n"
        "v_cvt_pk_bf16_f32 v200, v104, v105\n"
        "v_cvt_pk_bf16_f32 v201, v106, v107\n"
        "v_mfma_f32_16x16x32_bf16 v[32:35], v[152:155], v[196:199], v[32:35]\n"
        "v_mov_b64 v[206:207], v[200:201]\n"
        "v_cvt_pk_bf16_f32 v77, v26, v27\n"
        "v_mfma_f32_16x16x32_bf16 v[32:35], v[168:171], v[200:203], v[32:35]\n"
        "s_cmp_eq_u32 s44, 196608\n"
        "v_cvt_pk_bf16_f32 v212, v96, v97\n"
        "v_cvt_pk_bf16_f32 v213, v98, v99\n"
        "v_mfma_f32_16x16x32_bf16 v[36:39], v[172:175], v[196:199], v[36:39]\n"
        "s_cselect_b32 s44, 0, s44\n"
        "v_mfma_f32_16x16x32_bf16 v[36:39], v[188:191], v[204:207], v[36:39]\n"
        "s_add_u32 s36, s46, s44\n"
        "ds_read_b128 v[202:205], v252 offset:4096\n"
        "v_mfma_f32_16x16x32_bf16 v[100:103], a[116:119], a[244:247], 0\n"
        "s_add_u32 s43, s43, s46\n"
        "v_exp_f32 v32, v32\n"
        "v_mfma_f32_16x16x32_bf16 v[100:103], a[112:115], a[240:243], v[100:103]\n"
        "s_add_u32 s35, s43, 1536\n"
        "v_exp_f32 v33, v33\n"
        "v_mfma_f32_16x16x32_bf16 v[100:103], a[108:111], a[236:239], v[100:103]\n"
        "v_exp_f32 v34, v34\n"
        "v_mfma_f32_16x16x32_bf16 v[100:103], a[104:107], a[224:227], v[100:103]\n"
        "ds_read_b128 a[72:75], v248 offset:0\n"
        "v_cvt_pk_bf16_f32 v80, v32, v33\n"
        "v_mfma_f32_16x16x32_bf16 v[100:103], a[100:103], a[228:231], v[100:103]\n"
        "v_exp_f32 v35, v35\n"
        "v_mfma_f32_16x16x32_bf16 v[100:103], a[96:99], a[232:235], v[100:103]\n"
        "ds_read_b128 a[76:79], v248 offset:6144\n"
        "v_cvt_pk_bf16_f32 v81, v34, v35\n"
        "v_mfma_f32_16x16x32_bf16 v[40:43], v[192:195], v[196:199], v[40:43]\n"
        "s_waitcnt lgkmcnt(2)\n"
        "v_exp_f32 v36, v36\n"
        "v_mfma_f32_16x16x32_bf16 v[40:43], v[208:211], v[200:203], v[40:43]\n"
        "v_exp_f32 v37, v37\n"
        "v_mfma_f32_16x16x32_bf16 v[104:107], a[120:123], a[232:235], 0\n"
        "v_exp_f32 v38, v38\n"
        "v_mfma_f32_16x16x32_bf16 v[104:107], a[124:127], a[228:231], v[104:107]\n"
        "v_cvt_pk_bf16_f32 v214, v100, v101\n"
        "v_cvt_pk_bf16_f32 v215, v102, v103\n"
        "v_mfma_f32_16x16x32_bf16 v[104:107], a[128:131], a[224:227], v[104:107]\n"
        "ds_read_b128 a[80:83], v248 offset:36864\n"
        "v_cvt_pk_bf16_f32 v82, v36, v37\n"
        "v_mfma_f32_16x16x32_bf16 v[104:107], a[132:135], a[236:239], v[104:107]\n"
        "v_exp_f32 v39, v39\n"
        "v_mfma_f32_16x16x32_bf16 v[104:107], a[136:139], a[240:243], v[104:107]\n"
        "ds_read_b128 a[84:87], v248 offset:43008\n"
        "v_cvt_pk_bf16_f32 v83, v38, v39\n"
        "v_mfma_f32_16x16x32_bf16 v[104:107], a[140:143], a[244:247], v[104:107]\n"
        "v_exp_f32 v40, v40\n"
        "v_mfma_f32_16x16x32_bf16 v[96:99], a[20:23], a[244:247], 0\n"
        "v_exp_f32 v41, v41\n"
        "v_mfma_f32_16x16x32_bf16 v[96:99], a[16:19], a[240:243], v[96:99]\n"
        "v_exp_f32 v42, v42\n"
        "v_mfma_f32_16x16x32_bf16 v[96:99], a[12:15], a[236:239], v[96:99]\n"
        "ds_read_b128 a[88:91], v248 offset:12288\n"
        "v_cvt_pk_bf16_f32 v84, v40, v41\n"
        "v_mfma_f32_16x16x32_bf16 v[96:99], a[8:11], a[224:227], v[96:99]\n"
        "v_cvt_pk_bf16_f32 v230, v104, v105\n"
        "v_cvt_pk_bf16_f32 v231, v106, v107\n"
        "v_mfma_f32_16x16x32_bf16 v[96:99], a[4:7], a[228:231], v[96:99]\n"
        "v_exp_f32 v43, v43\n"
        "v_mfma_f32_16x16x32_bf16 v[96:99], a[0:3], a[232:235], v[96:99]\n"
        "ds_read_b128 a[92:95], v248 offset:49152\n"
        "v_cvt_pk_bf16_f32 v85, v42, v43\n"
        "v_mfma_f32_16x16x32_bf16 v[12:15], v[212:215], v[156:159], v[12:15]\n"
        "ds_read_b128 v[222:225], v251 offset:4608\n"
        "s_nop 0\n"
        "v_mfma_f32_16x16x32_bf16 v[12:15], v[228:231], v[164:167], v[12:15]\n"
        "ds_read_b128 a[96:99], v254 offset:6160\n"
        "v_mfma_f32_16x16x32_bf16 v[28:31], v[212:215], v[176:179], v[28:31]\n"
        "v_mul_f32 v96, s50, v96\n"
        "v_mul_f32 v97, s50, v97\n"
        "v_mfma_f32_16x16x32_bf16 v[28:31], v[228:231], v[184:187], v[28:31]\n"
        "v_mul_f32 v98, s50, v98\n"
        "v_mul_f32 v99, s50, v99\n"
        "v_mfma_f32_16x16x32_bf16 v[44:47], v[212:215], v[196:199], v[44:47]\n"
        "v_cvt_pk_bf16_f32 v216, v96, v97\n"
        "v_cvt_pk_bf16_f32 v217, v98, v99\n"
        "v_mfma_f32_16x16x32_bf16 v[44:47], v[228:231], v[204:207], v[44:47]\n"
        "v_exp_f32 v12, v12\n"
        "v_mfma_f32_16x16x32_bf16 v[100:103], a[24:27], a[232:235], 0\n"
        "v_exp_f32 v13, v13\n"
        "v_mfma_f32_16x16x32_bf16 v[100:103], a[28:31], a[228:231], v[100:103]\n"
        "v_exp_f32 v14, v14\n"
        "v_mfma_f32_16x16x32_bf16 v[100:103], a[32:35], a[224:227], v[100:103]\n"
        "ds_read_b128 a[100:103], v254 offset:16\n"
        "v_cvt_pk_bf16_f32 v70, v12, v13\n"
        "v_mfma_f32_16x16x32_bf16 v[100:103], a[36:39], a[236:239], v[100:103]\n"
        "v_exp_f32 v15, v15\n"
        "v_mfma_f32_16x16x32_bf16 v[100:103], a[40:43], a[240:243], v[100:103]\n"
        "ds_read_b128 a[104:107], v254 offset:43024\n"
        "v_cvt_pk_bf16_f32 v71, v14, v15\n"
        "v_mfma_f32_16x16x32_bf16 v[100:103], a[44:47], a[244:247], v[100:103]\n"
        "v_exp_f32 v28, v28\n"
        "v_mfma_f32_16x16x32_bf16 v[104:107], a[68:71], a[244:247], 0\n"
        "v_exp_f32 v29, v29\n"
        "v_mfma_f32_16x16x32_bf16 v[104:107], a[64:67], a[240:243], v[104:107]\n"
        "v_exp_f32 v30, v30\n"
        "v_mfma_f32_16x16x32_bf16 v[104:107], a[60:63], a[236:239], v[104:107]\n"
        "ds_read_b128 a[108:111], v254 offset:36880\n"
        "v_cvt_pk_bf16_f32 v78, v28, v29\n"
        "v_mfma_f32_16x16x32_bf16 v[104:107], a[56:59], a[224:227], v[104:107]\n"
        "v_mul_f32 v100, s50, v100\n"
        "v_mul_f32 v101, s50, v101\n"
        "v_mfma_f32_16x16x32_bf16 v[104:107], a[52:55], a[228:231], v[104:107]\n"
        "v_mul_f32 v102, s50, v102\n"
        "v_mul_f32 v103, s50, v103\n"
        "v_mfma_f32_16x16x32_bf16 v[104:107], a[48:51], a[232:235], v[104:107]\n"
        "v_cvt_pk_bf16_f32 v218, v100, v101\n"
        "v_cvt_pk_bf16_f32 v219, v102, v103\n"
        "v_mfma_f32_16x16x32_bf16 v[96:99], a[232:235], a[144:147], 0\n"
        "v_exp_f32 v31, v31\n"
        "v_mfma_f32_16x16x32_bf16 v[96:99], a[228:231], a[148:151], v[96:99]\n"
        "ds_read_b128 a[112:115], v254 offset:49168\n"
        "v_cvt_pk_bf16_f32 v79, v30, v31\n"
        "v_mfma_f32_16x16x32_bf16 v[96:99], a[224:227], a[152:155], v[96:99]\n"
        "v_mul_f32 v104, s50, v104\n"
        "v_mul_f32 v105, s50, v105\n"
        "v_mfma_f32_16x16x32_bf16 v[96:99], a[236:239], a[156:159], v[96:99]\n"
        "v_mul_f32 v106, s50, v106\n"
        "v_mul_f32 v107, s50, v107\n"
        "v_mfma_f32_16x16x32_bf16 v[96:99], a[240:243], a[160:163], v[96:99]\n"
        "v_cvt_pk_bf16_f32 v220, v104, v105\n"
        "v_cvt_pk_bf16_f32 v221, v106, v107\n"
        "v_mfma_f32_16x16x32_bf16 v[96:99], a[244:247], a[164:167], v[96:99]\n"
        "ds_read_b128 a[220:223], v240 offset:12288\n"
        "v_mov_b64 v[226:227], v[220:221]\n"
        "v_mfma_f32_16x16x32_bf16 v[48:51], v[152:155], v[216:219], v[48:51]\n"
        "s_waitcnt lgkmcnt(6)\n"
        "v_exp_f32 v44, v44\n"
        "v_mfma_f32_16x16x32_bf16 v[48:51], v[168:171], v[220:223], v[48:51]\n"
        "v_exp_f32 v45, v45\n"
        "v_mfma_f32_16x16x32_bf16 v[52:55], v[172:175], v[216:219], v[52:55]\n"
        "v_exp_f32 v46, v46\n"
        "v_mfma_f32_16x16x32_bf16 v[52:55], v[188:191], v[224:227], v[52:55]\n"
        "ds_read_b128 v[222:225], v252 offset:4608\n"
        "v_cvt_pk_bf16_f32 v134, v96, v97\n"
        "v_mfma_f32_16x16x32_bf16 v[100:103], a[244:247], a[188:191], 0\n"
        "ds_read_b128 a[216:219], v241 offset:12288\n"
        "v_cvt_pk_bf16_f32 v135, v98, v99\n"
        "v_mfma_f32_16x16x32_bf16 v[100:103], a[240:243], a[184:187], v[100:103]\n"
        "ds_read_b128 a[248:251], v240 offset:12416\n"
        "v_cvt_pk_bf16_f32 v86, v44, v45\n"
        "v_mfma_f32_16x16x32_bf16 v[100:103], a[236:239], a[180:183], v[100:103]\n"
        "v_exp_f32 v47, v47\n"
        "v_mfma_f32_16x16x32_bf16 v[100:103], a[224:227], a[176:179], v[100:103]\n"
        "ds_read_b128 a[116:119], v254 offset:12304\n"
        "v_cvt_pk_bf16_f32 v87, v46, v47\n"
        "v_mfma_f32_16x16x32_bf16 v[100:103], a[228:231], a[172:175], v[100:103]\n"
        "v_exp_f32 v48, v48\n"
        "v_mfma_f32_16x16x32_bf16 v[100:103], a[232:235], a[168:171], v[100:103]\n"
        "v_exp_f32 v49, v49\n"
        "v_mfma_f32_16x16x32_bf16 v[56:59], v[192:195], v[216:219], v[56:59]\n"
        "s_waitcnt lgkmcnt(3)\n"
        "v_exp_f32 v50, v50\n"
        "v_mfma_f32_16x16x32_bf16 v[56:59], v[208:211], v[220:223], v[56:59]\n"
        "ds_read_b128 a[120:123], v248 offset:32\n"
        "v_cvt_pk_bf16_f32 v88, v48, v49\n"
        "v_mfma_f32_16x16x32_bf16 v[60:63], v[212:215], v[216:219], v[60:63]\n"
        "v_exp_f32 v51, v51\n"
        "v_mfma_f32_16x16x32_bf16 v[60:63], v[228:231], v[224:227], v[60:63]\n"
        "v_cvt_pk_bf16_f32 v142, v100, v101\n"
        "v_cvt_pk_bf16_f32 v143, v102, v103\n"
        "v_mfma_f32_16x16x32_bf16 v[104:107], a[232:235], a[192:195], 0\n"
        "ds_read_b128 a[232:235], v241 offset:12416\n"
        "v_cvt_pk_bf16_f32 v89, v50, v51\n"
        "v_mfma_f32_16x16x32_bf16 v[104:107], a[228:231], a[196:199], v[104:107]\n"
        "ds_read_b128 a[228:231], v240 offset:12544\n"
        "v_mfma_f32_16x16x32_bf16 v[104:107], a[224:227], a[200:203], v[104:107]\n"
        "ds_read_b128 a[224:227], v241 offset:12544\n"
        "v_mfma_f32_16x16x32_bf16 v[104:107], a[236:239], a[204:207], v[104:107]\n"
        "v_exp_f32 v52, v52\n"
        "v_mfma_f32_16x16x32_bf16 v[104:107], a[240:243], a[208:211], v[104:107]\n"
        "v_exp_f32 v53, v53\n"
        "v_mfma_f32_16x16x32_bf16 v[104:107], a[244:247], a[212:215], v[104:107]\n"
        "v_exp_f32 v54, v54\n"
        "v_mfma_f32_16x16x32_bf16 v[96:99], a[220:223], a[144:147], 0\n"
        "s_waitcnt lgkmcnt(6)\n"
        "ds_read_b128 a[236:239], v240 offset:1536\n"
        "v_cvt_pk_bf16_f32 v90, v52, v53\n"
        "v_mfma_f32_16x16x32_bf16 v[96:99], a[216:219], a[148:151], v[96:99]\n"
        "s_waitcnt lgkmcnt(6)\n"
        "v_exp_f32 v55, v55\n"
        "v_mfma_f32_16x16x32_bf16 v[96:99], a[248:251], a[152:155], v[96:99]\n"
        "s_waitcnt lgkmcnt(3)\n"
        "ds_read_b128 a[240:243], v241 offset:1536\n"
        "v_cvt_pk_bf16_f32 v150, v104, v105\n"
        "v_mfma_f32_16x16x32_bf16 v[96:99], a[232:235], a[156:159], v[96:99]\n"
        "s_waitcnt lgkmcnt(3)\n"
        "ds_read_b128 a[244:247], v240 offset:1664\n"
        "v_cvt_pk_bf16_f32 v151, v106, v107\n"
        "v_mfma_f32_16x16x32_bf16 v[96:99], a[228:231], a[160:163], v[96:99]\n"
        "s_waitcnt lgkmcnt(3)\n"
        "ds_read_b128 a[124:127], v248 offset:6176\n"
        "v_cvt_pk_bf16_f32 v91, v54, v55\n"
        "v_mfma_f32_16x16x32_bf16 v[96:99], a[224:227], a[164:167], v[96:99]\n"
        "v_exp_f32 v56, v56\n"
        "v_mfma_f32_16x16x32_bf16 v[100:103], a[224:227], a[188:191], 0\n"
        "v_exp_f32 v57, v57\n"
        "v_mfma_f32_16x16x32_bf16 v[100:103], a[228:231], a[184:187], v[100:103]\n"
        "v_exp_f32 v58, v58\n"
        "v_mfma_f32_16x16x32_bf16 v[100:103], a[232:235], a[180:183], v[100:103]\n"
        "46:\n"
        "s_cbranch_execz 40b\n"
        "ds_read_b128 v[32:35], v247 offset:0\n"
        "v_cvt_pk_bf16_f32 v92, v56, v57\n"
        "v_mfma_f32_16x16x32_bf16 v[100:103], a[248:251], a[176:179], v[100:103]\n"
        "v_cvt_pk_bf16_f32 v132, v96, v97\n"
        "v_cvt_pk_bf16_f32 v133, v98, v99\n"
        "v_mfma_f32_16x16x32_bf16 v[100:103], a[216:219], a[172:175], v[100:103]\n"
        "v_exp_f32 v59, v59\n"
        "v_mfma_f32_16x16x32_bf16 v[100:103], a[220:223], a[168:171], v[100:103]\n"
        "ds_read_b128 v[156:159], v247 offset:4096\n"
        "v_cvt_pk_bf16_f32 v93, v58, v59\n"
        "v_mfma_f32_16x16x32_bf16 v[104:107], a[220:223], a[192:195], 0\n"
        "ds_read_b128 a[220:223], v241 offset:1664\n"
        "v_mfma_f32_16x16x32_bf16 v[104:107], a[216:219], a[196:199], v[104:107]\n"
        "ds_read_b128 a[216:219], v240 offset:1792\n"
        "v_mfma_f32_16x16x32_bf16 v[104:107], a[248:251], a[200:203], v[104:107]\n"
        "ds_read_b128 a[248:251], v241 offset:1792\n"
        "v_cvt_pk_bf16_f32 v140, v100, v101\n"
        "v_mfma_f32_16x16x32_bf16 v[104:107], a[232:235], a[204:207], v[104:107]\n"
        "ds_read_b128 v[160:163], v247 offset:8192\n"
        "v_cvt_pk_bf16_f32 v141, v102, v103\n"
        "v_mfma_f32_16x16x32_bf16 v[104:107], a[228:231], a[208:211], v[104:107]\n"
        "v_exp_f32 v60, v60\n"
        "v_mfma_f32_16x16x32_bf16 v[104:107], a[224:227], a[212:215], v[104:107]\n"
        "s_waitcnt lgkmcnt(9)\n"
        "v_exp_f32 v61, v61\n"
        "v_mfma_f32_16x16x32_bf16 v[96:99], a[236:239], a[144:147], 0\n"
        "s_waitcnt lgkmcnt(8)\n"
        "v_exp_f32 v62, v62\n"
        "v_mfma_f32_16x16x32_bf16 v[96:99], a[240:243], a[148:151], v[96:99]\n"
        "s_waitcnt lgkmcnt(7)\n"
        "ds_read_b128 v[164:167], v247 offset:12288\n"
        "v_cvt_pk_bf16_f32 v94, v60, v61\n"
        "v_mfma_f32_16x16x32_bf16 v[96:99], a[244:247], a[152:155], v[96:99]\n"
        "s_waitcnt lgkmcnt(4)\n"
        "v_exp_f32 v63, v63\n"
        "v_mfma_f32_16x16x32_bf16 v[96:99], a[220:223], a[156:159], v[96:99]\n"
        "s_waitcnt lgkmcnt(3)\n"
        "v_cvt_pk_bf16_f32 v148, v104, v105\n"
        "v_cvt_pk_bf16_f32 v149, v106, v107\n"
        "v_mfma_f32_16x16x32_bf16 v[96:99], a[216:219], a[160:163], v[96:99]\n"
        "s_waitcnt lgkmcnt(2)\n"
        "ds_read_b128 v[152:155], v247 offset:16384\n"
        "v_cvt_pk_bf16_f32 v95, v62, v63\n"
        "v_mfma_f32_16x16x32_bf16 v[96:99], a[248:251], a[164:167], v[96:99]\n"
        "ds_read_b128 v[168:171], v247 offset:20480\n"
        "s_nop 0\n"
        "v_mfma_f32_16x16x32_bf16 v[100:103], a[248:251], a[188:191], 0\n"
        "ds_read_b128 a[128:131], v248 offset:36896\n"
        "s_nop 0\n"
        "v_mfma_f32_16x16x32_bf16 v[100:103], a[216:219], a[184:187], v[100:103]\n"
        "ds_read_b128 a[132:135], v248 offset:43040\n"
        "v_mfma_f32_16x16x32_bf16 v[100:103], a[220:223], a[180:183], v[100:103]\n"
        "ds_read_b128 a[136:139], v248 offset:12320\n"
        "v_cvt_pk_bf16_f32 v130, v96, v97\n"
        "v_mfma_f32_16x16x32_bf16 v[100:103], a[244:247], a[176:179], v[100:103]\n"
        "ds_read_b128 a[140:143], v248 offset:49184\n"
        "v_cvt_pk_bf16_f32 v131, v98, v99\n"
        "v_mfma_f32_16x16x32_bf16 v[100:103], a[240:243], a[172:175], v[100:103]\n"
        "ds_read_b128 a[0:3], v254 offset:6192\n"
        "v_mfma_f32_16x16x32_bf16 v[100:103], a[236:239], a[168:171], v[100:103]\n"
        "ds_read_b128 a[4:7], v254 offset:48\n"
        "v_mfma_f32_16x16x32_bf16 v[104:107], a[236:239], a[192:195], 0\n"
        "ds_read_b128 a[8:11], v254 offset:43056\n"
        "v_mov_b64 v[116:117], 0\n"
        "v_mfma_f32_16x16x32_bf16 v[104:107], a[240:243], a[196:199], v[104:107]\n"
        "ds_read_b128 a[12:15], v254 offset:36912\n"
        "s_nop 0\n"
        "v_mfma_f32_16x16x32_bf16 v[104:107], a[244:247], a[200:203], v[104:107]\n"
        "ds_read_b128 a[16:19], v254 offset:49200\n"
        "v_cvt_pk_bf16_f32 v138, v100, v101\n"
        "v_mfma_f32_16x16x32_bf16 v[104:107], a[220:223], a[204:207], v[104:107]\n"
        "ds_read_b128 a[20:23], v254 offset:12336\n"
        "v_cvt_pk_bf16_f32 v139, v102, v103\n"
        "v_mfma_f32_16x16x32_bf16 v[104:107], a[216:219], a[208:211], v[104:107]\n"
        "ds_read_b128 a[24:27], v248 offset:64\n"
        "v_mfma_f32_16x16x32_bf16 v[104:107], a[248:251], a[212:215], v[104:107]\n"
        "ds_read_b128 a[28:31], v248 offset:6208\n"
        "v_mfma_f32_16x16x32_bf16 v[172:175], v[32:35], a[72:75], 0\n"
        "ds_read_b128 a[32:35], v248 offset:36928\n"
        "v_mfma_f32_16x16x32_bf16 v[172:175], v[156:159], a[76:79], v[172:175]\n"
        "s_waitcnt lgkmcnt(15)\n"
        "ds_read_b128 a[36:39], v248 offset:43072\n"
        "v_mfma_f32_16x16x32_bf16 v[172:175], v[160:163], a[80:83], v[172:175]\n"
        "s_waitcnt lgkmcnt(15)\n"
        "ds_read_b128 a[40:43], v248 offset:12352\n"
        "v_mfma_f32_16x16x32_bf16 v[172:175], v[164:167], a[84:87], v[172:175]\n"
        "s_waitcnt lgkmcnt(15)\n"
        "v_cvt_pk_bf16_f32 v146, v104, v105\n"
        "v_cvt_pk_bf16_f32 v147, v106, v107\n"
        "v_mfma_f32_16x16x32_bf16 v[172:175], v[152:155], a[88:91], v[172:175]\n"
        "s_waitcnt lgkmcnt(15)\n"
        "ds_read_b128 a[44:47], v248 offset:49216\n"
        "v_mfma_f32_16x16x32_bf16 v[172:175], v[168:171], a[92:95], v[172:175]\n"
        "ds_read_b128 a[48:51], v254 offset:6224\n"
        "v_smfmac_f32_16x16x64_bf16 v[116:119], a[252:255], v[64:71], v255\n"
        "ds_read_b128 a[52:55], v254 offset:80\n"
        "v_mfma_f32_16x16x32_bf16 v[120:123], v[128:131], v[64:67], 0\n"
        "ds_read_b128 a[56:59], v254 offset:43088\n"
        "v_mfma_f32_16x16x32_bf16 v[120:123], v[132:135], v[68:71], v[120:123]\n"
        "ds_read_b128 a[60:63], v254 offset:36944\n"
        "v_mfma_f32_16x16x32_bf16 v[124:127], v[140:143], v[68:71], 0\n"
        "ds_read_b128 a[64:67], v254 offset:49232\n"
        "v_mfma_f32_16x16x32_bf16 v[124:127], v[136:139], v[64:67], v[124:127]\n"
        "ds_read_b128 a[68:71], v254 offset:12368\n"
        "v_add_f32 v116, v116, v117\n"
        "v_mfma_f32_16x16x32_bf16 v[232:235], v[144:147], v[64:67], 0\n"
        "v_rcp_f32 v117, v116\n"
        "v_mfma_f32_16x16x32_bf16 v[232:235], v[148:151], v[68:71], v[232:235]\n"
        "v_mul_f32 v120, v117, v120\n"
        "v_mul_f32 v121, v117, v121\n"
        "v_mfma_f32_16x16x32_bf16 v[176:179], v[156:159], a[96:99], 0\n"
        "v_mul_f32 v122, v117, v122\n"
        "v_mul_f32 v123, v117, v123\n"
        "v_mfma_f32_16x16x32_bf16 v[176:179], v[32:35], a[100:103], v[176:179]\n"
        "v_cvt_pk_bf16_f32 v120, v120, v121\n"
        "v_cvt_pk_bf16_f32 v121, v122, v123\n"
        "v_mfma_f32_16x16x32_bf16 v[176:179], v[164:167], a[104:107], v[176:179]\n"
        "ds_write_b32 v245, v120 offset:0\n"
        "v_mfma_f32_16x16x32_bf16 v[176:179], v[160:163], a[108:111], v[176:179]\n"
        "ds_write_b32 v245, v121 offset:4\n"
        "v_mfma_f32_16x16x32_bf16 v[176:179], v[168:171], a[112:115], v[176:179]\n"
        "v_mul_f32 v124, v117, v124\n"
        "v_mul_f32 v125, v117, v125\n"
        "v_mfma_f32_16x16x32_bf16 v[176:179], v[152:155], a[116:119], v[176:179]\n"
        "v_maximum3_f32 v253, v253, v116, v117\n"
        "v_mul_f32 v126, v117, v126\n"
        "v_mfma_f32_16x16x32_bf16 v[180:183], v[32:35], a[120:123], 0\n"
        "v_mov_b64 v[100:101], 0\n"
        "v_mul_f32 v127, v117, v127\n"
        "v_mfma_f32_16x16x32_bf16 v[180:183], v[156:159], a[124:127], v[180:183]\n"
        "s_waitcnt lgkmcnt(15)\n"
        "ds_read_b128 v[0:3], v247 offset:256\n"
        "v_cvt_pk_bf16_f32 v124, v124, v125\n"
        "v_mfma_f32_16x16x32_bf16 v[180:183], v[160:163], a[128:131], v[180:183]\n"
        "s_waitcnt lgkmcnt(15)\n"
        "ds_read_b128 v[4:7], v247 offset:4352\n"
        "v_cvt_pk_bf16_f32 v125, v126, v127\n"
        "v_mfma_f32_16x16x32_bf16 v[180:183], v[164:167], a[132:135], v[180:183]\n"
        "s_waitcnt lgkmcnt(15)\n"
        "ds_write_b32 v245, v124 offset:8\n"
        "v_mfma_f32_16x16x32_bf16 v[180:183], v[152:155], a[136:139], v[180:183]\n"
        "s_waitcnt lgkmcnt(15)\n"
        "ds_write_b32 v245, v125 offset:12\n"
        "v_mfma_f32_16x16x32_bf16 v[180:183], v[168:171], a[140:143], v[180:183]\n"
        "s_waitcnt lgkmcnt(15)\n"
        "ds_read_b128 v[8:11], v247 offset:8448\n"
        "v_mul_f32 v232, v117, v232\n"
        "v_mfma_f32_16x16x32_bf16 v[184:187], v[156:159], a[0:3], 0\n"
        "s_waitcnt lgkmcnt(15)\n"
        "ds_read_b128 v[16:19], v247 offset:12544\n"
        "v_mul_f32 v233, v117, v233\n"
        "v_mfma_f32_16x16x32_bf16 v[184:187], v[32:35], a[4:7], v[184:187]\n"
        "s_waitcnt lgkmcnt(15)\n"
        "ds_read_b128 v[20:23], v247 offset:16640\n"
        "v_mul_f32 v234, v117, v234\n"
        "v_mfma_f32_16x16x32_bf16 v[184:187], v[164:167], a[8:11], v[184:187]\n"
        "s_waitcnt lgkmcnt(15)\n"
        "ds_read_b128 v[12:15], v247 offset:20736\n"
        "v_mul_f32 v235, v117, v235\n"
        "v_mfma_f32_16x16x32_bf16 v[184:187], v[160:163], a[12:15], v[184:187]\n"
        "s_waitcnt lgkmcnt(15)\n"
        "v_cvt_pk_bf16_f32 v232, v232, v233\n"
        "v_cvt_pk_bf16_f32 v233, v234, v235\n"
        "v_mfma_f32_16x16x32_bf16 v[184:187], v[168:171], a[16:19], v[184:187]\n"
        "s_waitcnt lgkmcnt(15)\n"
        "ds_write_b32 v246, v232 offset:0\n"
        "v_mfma_f32_16x16x32_bf16 v[184:187], v[152:155], a[20:23], v[184:187]\n"
        "v_cvt_pk_bf16_f32 v188, v172, v176\n"
        "v_cvt_pk_bf16_f32 v208, v173, v177\n"
        "v_smfmac_f32_16x16x64_bf16 v[100:103], a[252:255], v[80:87], v255\n"
        "ds_write_b32 v246, v233 offset:4\n"
        "v_mfma_f32_16x16x32_bf16 v[104:107], v[128:131], v[80:83], 0\n"
        "v_cvt_pk_bf16_f32 v228, v174, v178\n"
        "s_nop 0\n"
        "v_mfma_f32_16x16x32_bf16 v[104:107], v[132:135], v[84:87], v[104:107]\n"
        "v_cvt_pk_bf16_f32 v189, v180, v184\n"
        "v_cvt_pk_bf16_f32 v209, v181, v185\n"
        "v_mfma_f32_16x16x32_bf16 v[108:111], v[140:143], v[84:87], 0\n"
        "buffer_store_dwordx2 v[188:189], v242, s[16:19], s38 offen offset:0 sc1\n"
        "v_mfma_f32_16x16x32_bf16 v[108:111], v[136:139], v[80:83], v[108:111]\n"
        "v_cvt_pk_bf16_f32 v229, v182, v186\n"
        "v_cvt_pk_bf16_f32 v236, v175, v179\n"
        "v_mfma_f32_16x16x32_bf16 v[112:115], v[144:147], v[80:83], 0\n"
        "v_cvt_pk_bf16_f32 v237, v183, v187\n"
        "v_add_f32 v100, v100, v101\n"
        "v_mfma_f32_16x16x32_bf16 v[112:115], v[148:151], v[84:87], v[112:115]\n"
        "s_waitcnt lgkmcnt(15)\n"
        "buffer_store_dwordx2 v[208:209], v242, s[16:19], s38 offen offset:384 sc1\n"
        "v_mfma_f32_16x16x32_bf16 v[192:195], v[32:35], a[24:27], 0\n"
        "s_waitcnt lgkmcnt(15)\n"
        "v_rcp_f32 v101, v100\n"
        "v_mfma_f32_16x16x32_bf16 v[192:195], v[156:159], a[28:31], v[192:195]\n"
        "s_waitcnt lgkmcnt(15)\n"
        "v_mul_f32 v104, v101, v104\n"
        "v_mul_f32 v105, v101, v105\n"
        "v_mfma_f32_16x16x32_bf16 v[192:195], v[160:163], a[32:35], v[192:195]\n"
        "s_waitcnt lgkmcnt(15)\n"
        "buffer_store_dwordx2 v[228:229], v242, s[16:19], s38 offen offset:768 sc1\n"
        "v_mfma_f32_16x16x32_bf16 v[192:195], v[164:167], a[36:39], v[192:195]\n"
        "s_waitcnt lgkmcnt(15)\n"
        "v_mul_f32 v106, v101, v106\n"
        "v_mul_f32 v107, v101, v107\n"
        "v_mfma_f32_16x16x32_bf16 v[192:195], v[152:155], a[40:43], v[192:195]\n"
        "s_waitcnt lgkmcnt(15)\n"
        "v_cvt_pk_bf16_f32 v104, v104, v105\n"
        "v_cvt_pk_bf16_f32 v105, v106, v107\n"
        "v_mfma_f32_16x16x32_bf16 v[192:195], v[168:171], a[44:47], v[192:195]\n"
        "s_waitcnt lgkmcnt(15)\n"
        "buffer_store_dwordx2 v[236:237], v242, s[16:19], s38 offen offset:1152 sc1\n"
        "v_mfma_f32_16x16x32_bf16 v[196:199], v[156:159], a[48:51], 0\n"
        "s_waitcnt lgkmcnt(15)\n"
        "v_mov_b64 v[48:49], 0\n"
        "v_mul_f32 v108, v101, v108\n"
        "v_mfma_f32_16x16x32_bf16 v[196:199], v[32:35], a[52:55], v[196:199]\n"
        "s_waitcnt lgkmcnt(15)\n"
        "ds_write_b32 v245, v104 offset:512\n"
        "v_mfma_f32_16x16x32_bf16 v[196:199], v[164:167], a[56:59], v[196:199]\n"
        "s_waitcnt lgkmcnt(15)\n"
        "ds_write_b32 v245, v105 offset:516\n"
        "v_mfma_f32_16x16x32_bf16 v[196:199], v[160:163], a[60:63], v[196:199]\n"
        "s_waitcnt lgkmcnt(15)\n"
        "v_mul_f32 v109, v101, v109\n"
        "v_maximum3_f32 v253, v253, v100, v101\n"
        "v_mfma_f32_16x16x32_bf16 v[196:199], v[168:171], a[64:67], v[196:199]\n"
        "s_waitcnt lgkmcnt(14)\n"
        "v_mul_f32 v110, v101, v110\n"
        "v_mul_f32 v111, v101, v111\n"
        "v_mfma_f32_16x16x32_bf16 v[196:199], v[152:155], a[68:71], v[196:199]\n"
        "s_waitcnt lgkmcnt(10)\n"
        "v_cvt_pk_bf16_f32 v108, v108, v109\n"
        "v_cvt_pk_bf16_f32 v109, v110, v111\n"
        "v_mfma_f32_16x16x32_bf16 v[200:203], v[4:7], a[76:79], 0\n"
        "ds_write_b32 v245, v108 offset:520\n"
        "v_mfma_f32_16x16x32_bf16 v[200:203], v[0:3], a[72:75], v[200:203]\n"
        "s_waitcnt lgkmcnt(7)\n"
        "ds_write_b32 v245, v109 offset:524\n"
        "v_mfma_f32_16x16x32_bf16 v[200:203], v[16:19], a[84:87], v[200:203]\n"
        "v_mul_f32 v112, v101, v112\n"
        "v_cvt_pk_bf16_f32 v190, v192, v196\n"
        "v_mfma_f32_16x16x32_bf16 v[200:203], v[8:11], a[80:83], v[200:203]\n"
        "s_waitcnt lgkmcnt(6)\n"
        "buffer_store_dword v190, v250, s[16:19], s38 offen offset:0 sc1\n"
        "v_mfma_f32_16x16x32_bf16 v[200:203], v[12:15], a[92:95], v[200:203]\n"
        "v_mul_f32 v113, v101, v113\n"
        "v_cvt_pk_bf16_f32 v210, v193, v197\n"
        "v_mfma_f32_16x16x32_bf16 v[200:203], v[20:23], a[88:91], v[200:203]\n"
        "v_mul_f32 v114, v101, v114\n"
        "v_mul_f32 v115, v101, v115\n"
        "v_smfmac_f32_16x16x64_bf16 v[48:51], a[252:255], v[72:79], v255\n"
        "buffer_store_dword v210, v250, s[16:19], s38 offen offset:384 sc1\n"
        "v_mfma_f32_16x16x32_bf16 v[52:55], v[128:131], v[72:75], 0\n"
        "v_cvt_pk_bf16_f32 v112, v112, v113\n"
        "v_cvt_pk_bf16_f32 v113, v114, v115\n"
        "v_mfma_f32_16x16x32_bf16 v[52:55], v[132:135], v[76:79], v[52:55]\n"
        "ds_write_b32 v246, v112 offset:512\n"
        "v_mfma_f32_16x16x32_bf16 v[56:59], v[140:143], v[76:79], 0\n"
        "v_cvt_pk_bf16_f32 v230, v194, v198\n"
        "v_cvt_pk_bf16_f32 v238, v195, v199\n"
        "v_mfma_f32_16x16x32_bf16 v[56:59], v[136:139], v[72:75], v[56:59]\n"
        "buffer_store_dword v230, v250, s[16:19], s38 offen offset:768 sc1\n"
        "v_mfma_f32_16x16x32_bf16 v[60:63], v[144:147], v[72:75], 0\n"
        "ds_write_b32 v246, v113 offset:516\n"
        "v_mfma_f32_16x16x32_bf16 v[60:63], v[148:151], v[76:79], v[60:63]\n"
        "buffer_store_dword v238, v250, s[16:19], s38 offen offset:1152 sc1\n"
        "v_mfma_f32_16x16x32_bf16 v[204:207], v[0:3], a[100:103], 0\n"
        "v_add_f32 v48, v48, v49\n"
        "v_mfma_f32_16x16x32_bf16 v[204:207], v[4:7], a[96:99], v[204:207]\n"
        "v_rcp_f32 v49, v48\n"
        "v_mfma_f32_16x16x32_bf16 v[204:207], v[8:11], a[108:111], v[204:207]\n"
        "v_mul_f32 v52, v49, v52\n"
        "v_mul_f32 v53, v49, v53\n"
        "v_mfma_f32_16x16x32_bf16 v[204:207], v[16:19], a[104:107], v[204:207]\n"
        "v_mul_f32 v54, v49, v54\n"
        "v_mul_f32 v55, v49, v55\n"
        "v_mfma_f32_16x16x32_bf16 v[204:207], v[20:23], a[116:119], v[204:207]\n"
        "v_cvt_pk_bf16_f32 v52, v52, v53\n"
        "v_cvt_pk_bf16_f32 v53, v54, v55\n"
        "v_mfma_f32_16x16x32_bf16 v[204:207], v[12:15], a[112:115], v[204:207]\n"
        "ds_write_b32 v245, v52 offset:256\n"
        "v_mfma_f32_16x16x32_bf16 v[212:215], v[4:7], a[124:127], 0\n"
        "v_mov_b64 v[64:65], 0\n"
        "v_mul_f32 v56, v49, v56\n"
        "v_mfma_f32_16x16x32_bf16 v[212:215], v[0:3], a[120:123], v[212:215]\n"
        "ds_write_b32 v245, v53 offset:260\n"
        "v_mfma_f32_16x16x32_bf16 v[212:215], v[16:19], a[132:135], v[212:215]\n"
        "v_mul_f32 v57, v49, v57\n"
        "v_maximum3_f32 v253, v253, v48, v49\n"
        "v_mfma_f32_16x16x32_bf16 v[212:215], v[8:11], a[128:131], v[212:215]\n"
        "v_mul_f32 v58, v49, v58\n"
        "v_mul_f32 v59, v49, v59\n"
        "v_mfma_f32_16x16x32_bf16 v[212:215], v[12:15], a[140:143], v[212:215]\n"
        "v_cvt_pk_bf16_f32 v56, v56, v57\n"
        "v_cvt_pk_bf16_f32 v57, v58, v59\n"
        "v_mfma_f32_16x16x32_bf16 v[212:215], v[20:23], a[136:139], v[212:215]\n"
        "ds_write_b32 v245, v56 offset:264\n"
        "v_mfma_f32_16x16x32_bf16 v[216:219], v[0:3], a[4:7], 0\n"
        "ds_write_b32 v245, v57 offset:268\n"
        "v_mfma_f32_16x16x32_bf16 v[216:219], v[4:7], a[0:3], v[216:219]\n"
        "v_mul_f32 v60, v49, v60\n"
        "v_mul_f32 v61, v49, v61\n"
        "v_mfma_f32_16x16x32_bf16 v[216:219], v[8:11], a[12:15], v[216:219]\n"
        "v_mul_f32 v62, v49, v62\n"
        "v_mul_f32 v63, v49, v63\n"
        "v_mfma_f32_16x16x32_bf16 v[216:219], v[16:19], a[8:11], v[216:219]\n"
        "v_cvt_pk_bf16_f32 v60, v60, v61\n"
        "v_cvt_pk_bf16_f32 v61, v62, v63\n"
        "v_mfma_f32_16x16x32_bf16 v[216:219], v[20:23], a[20:23], v[216:219]\n"
        "ds_write_b32 v246, v60 offset:256\n"
        "v_mfma_f32_16x16x32_bf16 v[216:219], v[12:15], a[16:19], v[216:219]\n"
        "v_cvt_pk_bf16_f32 v188, v200, v204\n"
        "v_cvt_pk_bf16_f32 v208, v201, v205\n"
        "v_smfmac_f32_16x16x64_bf16 v[64:67], a[252:255], v[88:95], v255\n"
        "ds_write_b32 v246, v61 offset:260\n"
        "v_mfma_f32_16x16x32_bf16 v[68:71], v[128:131], v[88:91], 0\n"
        "v_cvt_pk_bf16_f32 v228, v202, v206\n"
        "s_nop 0\n"
        "v_mfma_f32_16x16x32_bf16 v[68:71], v[132:135], v[92:95], v[68:71]\n"
        "s_waitcnt lgkmcnt(6)\n"
        "v_cvt_pk_bf16_f32 v189, v212, v216\n"
        "v_cvt_pk_bf16_f32 v209, v213, v217\n"
        "v_mfma_f32_16x16x32_bf16 v[80:83], v[140:143], v[92:95], 0\n"
        "s_barrier\n"
        "buffer_store_dwordx2 v[188:189], v242, s[16:19], s39 offen offset:0 sc1\n"
        "v_mfma_f32_16x16x32_bf16 v[80:83], v[136:139], v[88:91], v[80:83]\n"
        "ds_read_b128 a[168:171], v243 offset:0\n"
        "v_cvt_pk_bf16_f32 v229, v214, v218\n"
        "v_mfma_f32_16x16x32_bf16 v[84:87], v[144:147], v[88:91], 0\n"
        "ds_read_b128 a[172:175], v243 offset:4096\n"
        "v_cvt_pk_bf16_f32 v236, v203, v207\n"
        "v_mfma_f32_16x16x32_bf16 v[84:87], v[148:151], v[92:95], v[84:87]\n"
        "buffer_store_dwordx2 v[208:209], v242, s[16:19], s39 offen offset:384 sc1\n"
        "v_mfma_f32_16x16x32_bf16 v[220:223], v[4:7], a[28:31], 0\n"
        "ds_read_b128 a[176:179], v243 offset:8192\n"
        "v_cvt_pk_bf16_f32 v237, v215, v219\n"
        "v_mfma_f32_16x16x32_bf16 v[220:223], v[0:3], a[24:27], v[220:223]\n"
        "ds_read_b128 a[180:183], v243 offset:12288\n"
        "v_add_f32 v64, v64, v65\n"
        "v_mfma_f32_16x16x32_bf16 v[220:223], v[16:19], a[36:39], v[220:223]\n"
        "buffer_store_dwordx2 v[228:229], v242, s[16:19], s39 offen offset:768 sc1\n"
        "v_mfma_f32_16x16x32_bf16 v[220:223], v[8:11], a[32:35], v[220:223]\n"
        "ds_read_b128 a[184:187], v243 offset:16384\n"
        "v_mfma_f32_16x16x32_bf16 v[220:223], v[12:15], a[44:47], v[220:223]\n"
        "v_rcp_f32 v65, v64\n"
        "v_mfma_f32_16x16x32_bf16 v[220:223], v[20:23], a[40:43], v[220:223]\n"
        "buffer_store_dwordx2 v[236:237], v242, s[16:19], s39 offen offset:1152 sc1\n"
        "v_mfma_f32_16x16x32_bf16 v[224:227], v[0:3], a[52:55], 0\n"
        "ds_read_b128 a[188:191], v243 offset:20480\n"
        "v_mul_f32 v68, v65, v68\n"
        "v_mfma_f32_16x16x32_bf16 v[224:227], v[4:7], a[48:51], v[224:227]\n"
        "v_mul_f32 v69, v65, v69\n"
        "v_mul_f32 v70, v65, v70\n"
        "v_mfma_f32_16x16x32_bf16 v[224:227], v[8:11], a[60:63], v[224:227]\n"
        "v_mul_f32 v71, v65, v71\n"
        "v_cvt_pk_bf16_f32 v68, v68, v69\n"
        "v_mfma_f32_16x16x32_bf16 v[224:227], v[16:19], a[56:59], v[224:227]\n"
        "v_cvt_pk_bf16_f32 v69, v70, v71\n"
        "v_mul_f32 v80, v65, v80\n"
        "v_mfma_f32_16x16x32_bf16 v[224:227], v[20:23], a[68:71], v[224:227]\n"
        "ds_write_b32 v245, v68 offset:768\n"
        "v_mfma_f32_16x16x32_bf16 v[224:227], v[12:15], a[64:67], v[224:227]\n"
        "s_waitcnt lgkmcnt(6)\n"
        "ds_write_b32 v245, v69 offset:772\n"
        "v_mfma_f32_16x16x32_bf16 v[24:27], a[168:171], a[72:75], 0\n"
        "s_waitcnt lgkmcnt(6)\n"
        "v_mul_f32 v81, v65, v81\n"
        "v_maximum3_f32 v253, v253, v64, v65\n"
        "v_mfma_f32_16x16x32_bf16 v[24:27], a[172:175], a[76:79], v[24:27]\n"
        "s_waitcnt lgkmcnt(5)\n"
        "v_mul_f32 v82, v65, v82\n"
        "v_mul_f32 v83, v65, v83\n"
        "v_mfma_f32_16x16x32_bf16 v[24:27], a[176:179], a[80:83], v[24:27]\n"
        "s_waitcnt lgkmcnt(4)\n"
        "v_cvt_pk_bf16_f32 v80, v80, v81\n"
        "v_cvt_pk_bf16_f32 v81, v82, v83\n"
        "v_mfma_f32_16x16x32_bf16 v[24:27], a[180:183], a[84:87], v[24:27]\n"
        "s_waitcnt lgkmcnt(3)\n"
        "v_cvt_pk_bf16_f32 v190, v220, v224\n"
        "v_mul_f32 v84, v65, v84\n"
        "v_mfma_f32_16x16x32_bf16 v[24:27], a[184:187], a[88:91], v[24:27]\n"
        "s_waitcnt lgkmcnt(2)\n"
        "buffer_store_dword v190, v250, s[16:19], s39 offen offset:0 sc1\n"
        "v_mfma_f32_16x16x32_bf16 v[24:27], a[188:191], a[92:95], v[24:27]\n"
        "ds_write_b32 v245, v80 offset:776\n"
        "v_mfma_f32_16x16x32_bf16 v[28:31], a[172:175], a[96:99], 0\n"
        "ds_write_b32 v245, v81 offset:780\n"
        "v_mfma_f32_16x16x32_bf16 v[28:31], a[168:171], a[100:103], v[28:31]\n"
        "v_mul_f32 v85, v65, v85\n"
        "v_cvt_pk_bf16_f32 v210, v221, v225\n"
        "v_mfma_f32_16x16x32_bf16 v[28:31], a[180:183], a[104:107], v[28:31]\n"
        "buffer_store_dword v210, v250, s[16:19], s39 offen offset:384 sc1\n"
        "v_mfma_f32_16x16x32_bf16 v[28:31], a[176:179], a[108:111], v[28:31]\n"
        "v_mul_f32 v86, v65, v86\n"
        "v_mul_f32 v87, v65, v87\n"
        "v_mfma_f32_16x16x32_bf16 v[28:31], a[188:191], a[112:115], v[28:31]\n"
        "v_cvt_pk_bf16_f32 v84, v84, v85\n"
        "v_cvt_pk_bf16_f32 v85, v86, v87\n"
        "v_mfma_f32_16x16x32_bf16 v[28:31], a[184:187], a[116:119], v[28:31]\n"
        "ds_write_b32 v246, v84 offset:768\n"
        "v_mfma_f32_16x16x32_bf16 v[32:35], a[168:171], a[120:123], 0\n"
        "v_cvt_pk_bf16_f32 v230, v222, v226\n"
        "v_cvt_pk_bf16_f32 v238, v223, v227\n"
        "v_mfma_f32_16x16x32_bf16 v[32:35], a[172:175], a[124:127], v[32:35]\n"
        "buffer_store_dword v230, v250, s[16:19], s39 offen offset:768 sc1\n"
        "v_mfma_f32_16x16x32_bf16 v[32:35], a[176:179], a[128:131], v[32:35]\n"
        "buffer_store_dword v238, v250, s[16:19], s39 offen offset:1152 sc1\n"
        "v_mfma_f32_16x16x32_bf16 v[32:35], a[180:183], a[132:135], v[32:35]\n"
        "ds_write_b32 v246, v85 offset:772\n"
        "v_mfma_f32_16x16x32_bf16 v[32:35], a[184:187], a[136:139], v[32:35]\n"
        "ds_write_b32 v244, v253\n"
        "v_mfma_f32_16x16x32_bf16 v[32:35], a[188:191], a[140:143], v[32:35]\n"
        "v_cvt_pk_bf16_f32 v172, v24, v28\n"
        "v_mfma_f32_16x16x32_bf16 v[36:39], a[172:175], a[0:3], 0\n"
        "v_mfma_f32_16x16x32_bf16 v[36:39], a[168:171], a[4:7], v[36:39]\n"
        "v_mfma_f32_16x16x32_bf16 v[36:39], a[180:183], a[8:11], v[36:39]\n"
        "v_cvt_pk_bf16_f32 v176, v25, v29\n"
        "v_mfma_f32_16x16x32_bf16 v[36:39], a[176:179], a[12:15], v[36:39]\n"
        "v_mfma_f32_16x16x32_bf16 v[36:39], a[188:191], a[16:19], v[36:39]\n"
        "s_waitcnt lgkmcnt(0)\n"
        "v_mfma_f32_16x16x32_bf16 v[36:39], a[184:187], a[20:23], v[36:39]\n"
        "s_barrier\n"
        "ds_read_b128 a[192:195], v243 offset:256\n"
        "v_cvt_pk_bf16_f32 v180, v26, v30\n"
        "v_mfma_f32_16x16x32_bf16 v[40:43], a[168:171], a[24:27], 0\n"
        "ds_read_b128 a[196:199], v243 offset:4352\n"
        "s_nop 0\n"
        "v_mfma_f32_16x16x32_bf16 v[40:43], a[172:175], a[28:31], v[40:43]\n"
        "ds_read_b128 a[200:203], v243 offset:8448\n"
        "v_cvt_pk_bf16_f32 v173, v32, v36\n"
        "v_mfma_f32_16x16x32_bf16 v[40:43], a[176:179], a[32:35], v[40:43]\n"
        "ds_read_b128 a[204:207], v243 offset:12544\n"
        "v_cvt_pk_bf16_f32 v177, v33, v37\n"
        "v_mfma_f32_16x16x32_bf16 v[40:43], a[180:183], a[36:39], v[40:43]\n"
        "buffer_store_dwordx2 v[172:173], v242, s[16:19], s35 offen offset:0 sc1\n"
        "v_mfma_f32_16x16x32_bf16 v[40:43], a[184:187], a[40:43], v[40:43]\n"
        "ds_read_b128 a[208:211], v243 offset:16640\n"
        "v_cvt_pk_bf16_f32 v181, v34, v38\n"
        "v_mfma_f32_16x16x32_bf16 v[40:43], a[188:191], a[44:47], v[40:43]\n"
        "ds_read_b128 a[212:215], v243 offset:20736\n"
        "v_cvt_pk_bf16_f32 v184, v27, v31\n"
        "v_mfma_f32_16x16x32_bf16 v[44:47], a[172:175], a[48:51], 0\n"
        "buffer_store_dwordx2 v[176:177], v242, s[16:19], s35 offen offset:384 sc1\n"
        "v_mfma_f32_16x16x32_bf16 v[44:47], a[168:171], a[52:55], v[44:47]\n"
        "ds_read_b128 v[100:103], v249\n"
        "v_cvt_pk_bf16_f32 v185, v35, v39\n"
        "v_mfma_f32_16x16x32_bf16 v[44:47], a[180:183], a[56:59], v[44:47]\n"
        "v_mfma_f32_16x16x32_bf16 v[44:47], a[176:179], a[60:63], v[44:47]\n"
        "buffer_store_dwordx2 v[180:181], v242, s[16:19], s35 offen offset:768 sc1\n"
        "v_mfma_f32_16x16x32_bf16 v[44:47], a[188:191], a[64:67], v[44:47]\n"
        "v_mfma_f32_16x16x32_bf16 v[44:47], a[184:187], a[68:71], v[44:47]\n"
        "s_waitcnt lgkmcnt(5)\n"
        "s_nop 1\n"
        "v_mfma_f32_16x16x32_bf16 v[0:3], a[196:199], a[76:79], 0\n"
        "buffer_store_dwordx2 v[184:185], v242, s[16:19], s35 offen offset:1152 sc1\n"
        "v_mfma_f32_16x16x32_bf16 v[0:3], a[192:195], a[72:75], v[0:3]\n"
        "s_waitcnt lgkmcnt(3)\n"
        "s_nop 1\n"
        "v_mfma_f32_16x16x32_bf16 v[0:3], a[204:207], a[84:87], v[0:3]\n"
        "v_cvt_pk_bf16_f32 v174, v40, v44\n"
        "v_cvt_pk_bf16_f32 v178, v41, v45\n"
        "v_mfma_f32_16x16x32_bf16 v[0:3], a[200:203], a[80:83], v[0:3]\n"
        "s_waitcnt lgkmcnt(1)\n"
        "buffer_store_dword v174, v250, s[16:19], s35 offen offset:0 sc1\n"
        "v_mfma_f32_16x16x32_bf16 v[0:3], a[212:215], a[92:95], v[0:3]\n"
        "v_cvt_pk_bf16_f32 v182, v42, v46\n"
        "v_cvt_pk_bf16_f32 v186, v43, v47\n"
        "v_mfma_f32_16x16x32_bf16 v[0:3], a[208:211], a[88:91], v[0:3]\n"
        "v_mfma_f32_16x16x32_bf16 v[4:7], a[192:195], a[100:103], 0\n"
        "buffer_store_dword v178, v250, s[16:19], s35 offen offset:384 sc1\n"
        "v_mfma_f32_16x16x32_bf16 v[4:7], a[196:199], a[96:99], v[4:7]\n"
        "s_waitcnt lgkmcnt(0)\n"
        "v_maximum3_f32 v100, v100, v101, v102\n"
        "v_maximum3_f32 v100, v100, v103, v103\n"
        "v_mfma_f32_16x16x32_bf16 v[4:7], a[200:203], a[108:111], v[4:7]\n"
        "v_cmp_nge_f32 vcc, s25, v100\n"
        "v_mfma_f32_16x16x32_bf16 v[4:7], a[204:207], a[104:107], v[4:7]\n"
        "buffer_store_dword v182, v250, s[16:19], s35 offen offset:768 sc1\n"
        "v_mfma_f32_16x16x32_bf16 v[4:7], a[208:211], a[116:119], v[4:7]\n"
        "v_mfma_f32_16x16x32_bf16 v[4:7], a[212:215], a[112:115], v[4:7]\n"
        "v_mfma_f32_16x16x32_bf16 v[8:11], a[196:199], a[124:127], 0\n"
        "buffer_store_dword v186, v250, s[16:19], s35 offen offset:1152 sc1\n"
        "v_mfma_f32_16x16x32_bf16 v[8:11], a[192:195], a[120:123], v[8:11]\n"
        "v_mfma_f32_16x16x32_bf16 v[8:11], a[204:207], a[132:135], v[8:11]\n"
        "v_mfma_f32_16x16x32_bf16 v[8:11], a[200:203], a[128:131], v[8:11]\n"
        "v_mfma_f32_16x16x32_bf16 v[8:11], a[212:215], a[140:143], v[8:11]\n"
        "v_mfma_f32_16x16x32_bf16 v[8:11], a[208:211], a[136:139], v[8:11]\n"
        "v_mfma_f32_16x16x32_bf16 v[12:15], a[192:195], a[4:7], 0\n"
        "v_cvt_pk_bf16_f32 v192, v0, v4\n"
        "v_mfma_f32_16x16x32_bf16 v[12:15], a[196:199], a[0:3], v[12:15]\n"
        "v_cvt_pk_bf16_f32 v196, v1, v5\n"
        "v_mfma_f32_16x16x32_bf16 v[12:15], a[200:203], a[12:15], v[12:15]\n"
        "v_mfma_f32_16x16x32_bf16 v[12:15], a[204:207], a[8:11], v[12:15]\n"
        "v_cvt_pk_bf16_f32 v200, v2, v6\n"
        "v_mfma_f32_16x16x32_bf16 v[12:15], a[208:211], a[20:23], v[12:15]\n"
        "v_mfma_f32_16x16x32_bf16 v[12:15], a[212:215], a[16:19], v[12:15]\n"
        "v_cvt_pk_bf16_f32 v204, v3, v7\n"
        "s_nop 0\n"
        "v_mfma_f32_16x16x32_bf16 v[16:19], a[196:199], a[28:31], 0\n"
        "s_nop 2\n"
        "v_mfma_f32_16x16x32_bf16 v[16:19], a[192:195], a[24:27], v[16:19]\n"
        "v_mfma_f32_16x16x32_bf16 v[16:19], a[204:207], a[36:39], v[16:19]\n"
        "v_cvt_pk_bf16_f32 v193, v8, v12\n"
        "v_cvt_pk_bf16_f32 v197, v9, v13\n"
        "v_mfma_f32_16x16x32_bf16 v[16:19], a[200:203], a[32:35], v[16:19]\n"
        "buffer_store_dwordx2 v[192:193], v242, s[16:19], s36 offen offset:0 sc1\n"
        "v_mfma_f32_16x16x32_bf16 v[16:19], a[212:215], a[44:47], v[16:19]\n"
        "v_cvt_pk_bf16_f32 v201, v10, v14\n"
        "v_cvt_pk_bf16_f32 v205, v11, v15\n"
        "v_mfma_f32_16x16x32_bf16 v[16:19], a[208:211], a[40:43], v[16:19]\n"
        "buffer_store_dwordx2 v[196:197], v242, s[16:19], s36 offen offset:384 sc1\n"
        "v_mfma_f32_16x16x32_bf16 v[20:23], a[192:195], a[52:55], 0\n"
        "v_mfma_f32_16x16x32_bf16 v[20:23], a[196:199], a[48:51], v[20:23]\n"
        "buffer_store_dwordx2 v[200:201], v242, s[16:19], s36 offen offset:768 sc1\n"
        "v_mfma_f32_16x16x32_bf16 v[20:23], a[200:203], a[60:63], v[20:23]\n"
        "v_mfma_f32_16x16x32_bf16 v[20:23], a[204:207], a[56:59], v[20:23]\n"
        "buffer_store_dwordx2 v[204:205], v242, s[16:19], s36 offen offset:1152 sc1\n"
        "v_mfma_f32_16x16x32_bf16 v[20:23], a[208:211], a[68:71], v[20:23]\n"
        "v_mfma_f32_16x16x32_bf16 v[20:23], a[212:215], a[64:67], v[20:23]\n"
        "s_nop 2\n"
        "s_nop 4\n"
        "v_cvt_pk_bf16_f32 v194, v16, v20\n"
        "buffer_store_dword v194, v250, s[16:19], s36 offen offset:0 sc1\n"
        "v_cvt_pk_bf16_f32 v198, v17, v21\n"
        "buffer_store_dword v198, v250, s[16:19], s36 offen offset:384 sc1\n"
        "v_cvt_pk_bf16_f32 v202, v18, v22\n"
        "buffer_store_dword v202, v250, s[16:19], s36 offen offset:768 sc1\n"
        "v_cvt_pk_bf16_f32 v206, v19, v23\n"
        "buffer_store_dword v206, v250, s[16:19], s36 offen offset:1152 sc1\n"
        "s_cmp_lg_u64 vcc, 0\n"
        "s_cbranch_scc1 30f\n"
        "s_endpgm\n"
        "30:\n"
        : "+{v0}"(tid), "=&{s42}"(w), "=&{s[60:61]}"(hist), "=&{s27}"(nit)
        : "{s[0:1]}"(__builtin_amdgcn_kernarg_segment_ptr()), "{s2}"(blockIdx.x), "s"(ldsb)
        : "v1", "v2", "v3", "v4", "v5", "v6", "v7", "v8", "v9", "v10", "v11", "v12", "v13", "v14", "v15", "v16", "v17", "v18", "v19", "v20", "v21", "v22", "v23", "v24", "v25", "v26", "v27", "v28", "v29", "v30", "v31", "v32", "v33", "v34", "v35", "v36", "v37", "v38", "v39", "v40", "v41", "v42", "v43", "v44", "v45", "v46", "v47", "v48", "v49", "v50", "v51", "v52", "v53", "v54", "v55", "v56", "v57", "v58", "v59", "v60", "v61", "v62", "v63", "v64", "v65", "v66", "v67", "v68", "v69", "v70", "v71", "v72", "v73", "v74", "v75", "v76", "v77", "v78", "v79", "v80", "v81", "v82", "v83", "v84", "v85", "v86", "v87", "v88", "v89", "v90", "v91", "v92", "v93", "v94", "v95", "v96", "v97", "v98", "v99", "v100", "v101", "v102", "v103", "v104", "v105", "v106", "v107", "v108", "v109", "v110", "v111", "v112", "v113", "v114", "v115", "v116", "v117", "v118", "v119", "v120", "v121", "v122", "v123", "v124", "v125", "v126", "v127", "v128", "v129", "v130", "v131", "v132", "v133", "v134", "v135", "v136", "v137", "v138", "v139", "v140", "v141", "v142", "v143", "v144", "v145", "v146", "v147", "v148", "v149", "v150", "v151", "v152", "v153", "v154", "v155", "v156", "v157", "v158", "v159", "v160", "v161", "v162", "v163", "v164", "v165", "v166", "v167", "v168", "v169", "v170", "v171", "v172", "v173", "v174", "v175", "v176", "v177", "v178", "v179", "v180", "v181", "v182", "v183", "v184", "v185", "v186", "v187", "v188", "v189", "v190", "v191", "v192", "v193", "v194", "v195", "v196", "v197", "v198", "v199", "v200", "v201", "v202", "v203", "v204", "v205", "v206", "v207", "v208", "v209", "v210", "v211", "v212", "v213", "v214", "v215", "v216", "v217", "v218", "v219", "v220", "v221", "v222", "v223", "v224", "v225", "v226", "v227", "v228", "v229", "v230", "v231", "v232", "v233", "v234", "v235", "v236", "v237", "v238", "v239", "v240", "v241", "v242", "v243", "v244", "v245", "v246", "v247", "v248", "v249", "v250", "v251", "v252", "v253", "v254", "v255", "a0", "a1", "a2", "a3", "a4", "a5", "a6", "a7", "a8", "a9", "a10", "a11", "a12", "a13", "a14", "a15", "a16", "a17", "a18", "a19", "a20", "a21", "a22", "a23", "a24", "a25", "a26", "a27", "a28", "a29", "a30", "a31", "a32", "a33", "a34", "a35", "a36", "a37", "a38", "a39", "a40", "a41", "a42", "a43", "a44", "a45", "a46", "a47", "a48", "a49", "a50", "a51", "a52", "a53", "a54", "a55", "a56", "a57", "a58", "a59", "a60", "a61", "a62", "a63", "a64", "a65", "a66", "a67", "a68", "a69", "a70", "a71", "a72", "a73", "a74", "a75", "a76", "a77", "a78", "a79", "a80", "a81", "a82", "a83", "a84", "a85", "a86", "a87", "a88", "a89", "a90", "a91", "a92", "a93", "a94", "a95", "a96", "a97", "a98", "a99", "a100", "a101", "a102", "a103", "a104", "a105", "a106", "a107", "a108", "a109", "a110", "a111", "a112", "a113", "a114", "a115", "a116", "a117", "a118", "a119", "a120", "a121", "a122", "a123", "a124", "a125", "a126", "a127", "a128", "a129", "a130", "a131", "a132", "a133", "a134", "a135", "a136", "a137", "a138", "a139", "a140", "a141", "a142", "a143", "a144", "a145", "a146", "a147", "a148", "a149", "a150", "a151", "a152", "a153", "a154", "a155", "a156", "a157", "a158", "a159", "a160", "a161", "a162", "a163", "a164", "a165", "a166", "a167", "a168", "a169", "a170", "a171", "a172", "a173", "a174", "a175", "a176", "a177", "a178", "a179", "a180", "a181", "a182", "a183", "a184", "a185", "a186", "a187", "a188", "a189", "a190", "a191", "a192", "a193", "a194", "a195", "a196", "a197", "a198", "a199", "a200", "a201", "a202", "a203", "a204", "a205", "a206", "a207", "a208", "a209", "a210", "a211", "a212", "a213", "a214", "a215", "a216", "a217", "a218", "a219", "a220", "a221", "a222", "a223", "a224", "a225", "a226", "a227", "a228", "a229", "a230", "a231", "a232", "a233", "a234", "a235", "a236", "a237", "a238", "a239", "a240", "a241", "a242", "a243", "a244", "a245", "a246", "a247", "a248", "a249", "a250", "a251", "a252", "a253", "a254", "a255", "s3", "s4", "s5", "s6", "s7", "s8", "s9", "s10", "s11", "s12", "s13", "s14", "s15", "s16", "s17", "s18", "s19", "s20", "s21", "s22", "s23", "s24", "s25", "s26", "s28", "s29", "s30", "s31", "s34", "s35", "s36", "s37", "s38", "s39", "s40", "s41", "s43", "s44", "s45", "s46", "s47", "s48", "s49", "s50", "s51", "s52", "s53", "s54", "s55", "s56", "s57", "s58", "s59", "s62", "s63", "s64", "s65", "s66", "s67", "vcc", "scc", "memory");
    const bool all = nit > 21u;
    for (unsigned i = 0; i < (all ? 256u : 9u); ++i) {
        const unsigned b = all ? i : i ? (unsigned)(hist >> (8u * (i - 1u))) & 255u : blockIdx.x;
        if (!all && i && b == blockIdx.x) continue;
        unsigned l2, w2 = w;
        asm volatile("s_waitcnt vmcnt(0) lgkmcnt(0)\n s_barrier\n v_mbcnt_lo_u32_b32 %0, -1, 0\n v_mbcnt_hi_u32_b32 %0, -1, %0\n s_nop 1"
                     : "=v"(l2), "+s"(w2) :: "memory");
        guarded_pass(x, wqkv, wo, rpb, rpi, mask, out, ldsb, l2, w2, b);
    }
}


extern "C" void kernel_launch(void* const* d_in, const int* in_sizes, int n_in, void* d_out, int out_size, void* d_ws,
                              size_t ws_size, hipStream_t stream) {
    win_attn_kernel<<<256, 256, 0, stream>>>((const char*)d_in[0], (const char*)d_in[1], (const char*)d_in[2],
                                             (const char*)d_in[3], (const char*)d_in[4], (const char*)d_in[5],
                                             (char*)d_out, (char*)d_ws);

}
```

```cpp
#include <hip/hip_runtime.h>
#include <hip/hip_bf16.h>

typedef unsigned int u32x4 __attribute__((ext_vector_type(4)));

__device__ __forceinline__ void guarded_pass(const char* x, const char* wqkv, const char* wo, const char* rpb, const char* rpi,
                                             const char* mask, char* out, unsigned ldsb, const unsigned lane, const unsigned w, const unsigned bid) {
    const unsigned wx = bid & 63u, wy0 = bid >> 6;
    unsigned dm[3], wsrc[3];
#pragma unroll
    for (int p = 0; p < 3; ++p) {
        unsigned gch = 64u * p + lane, t = gch / 24u, c1 = gch % 24u;
        dm[p] = ((8u * wx + 4u + t) & 511u) * 384u + ((c1 ^ (((t >> 1) & 1u) | (w << 1))) << 4);
        wsrc[p] = t * 1152u + ((c1 ^ (2u * (((t >> 1) & 1u) | ((w & 1u) << 1)))) << 4) - 1024u * p;
    }
    u32x4 srdx = {(unsigned)(size_t)x, (unsigned)((size_t)x >> 32) & 0xffffu, 100663296u, 0x20000u};
    u32x4 srdw = {(unsigned)(size_t)wqkv, (unsigned)((size_t)wqkv >> 32) & 0xffffu, 221184u, 0x20000u};
    unsigned ya = (8u * wy0 + 36u + w) * 196608u;
    unsigned wv = w;
    unsigned w3 = w * 3072u, w9 = w * 9216u;
    __builtin_amdgcn_sched_barrier(0);
    asm volatile(
        "s_nop 4\n"
        "s_add_u32 m0, s40, 0\n"
        "s_add_u32 s64, s65, 0\n"
        "buffer_load_dwordx4 v128, s[20:23], s64 offen lds\n"
        "buffer_load_dwordx4 v129, s[20:23], s64 offen offset:1024 lds\n"
        "buffer_load_dwordx4 v130, s[20:23], s64 offen offset:2048 lds\n"
        "s_add_u32 m0, s40, 12288\n"
        "s_add_u32 s64, s65, 36864\n"
        "buffer_load_dwordx4 v128, s[20:23], s64 offen lds\n"
        "buffer_load_dwordx4 v129, s[20:23], s64 offen offset:1024 lds\n"
        "buffer_load_dwordx4 v130, s[20:23], s64 offen offset:2048 lds\n"
        "s_add_u32 m0, s40, 24576\n"
        "s_add_u32 s64, s65, 73728\n"
        "buffer_load_dwordx4 v128, s[20:23], s64 offen lds\n"
        "buffer_load_dwordx4 v129, s[20:23], s64 offen offset:1024 lds\n"
        "buffer_load_dwordx4 v130, s[20:23], s64 offen offset:2048 lds\n"
        :: "{v242}"(dm[0]), "{v243}"(dm[1]), "{v244}"(dm[2]), "{v128}"(wsrc[0]), "{v129}"(wsrc[1]), "{v130}"(wsrc[2]), "{s[8:11]}"(srdx), "{s[20:23]}"(srdw), "{s47}"(ya), "{s40}"(w3), "{s65}"(w9), "s"(ldsb)
        : "s64", "scc", "memory");
    __builtin_amdgcn_sched_barrier(0);

    const unsigned rho = lane & 15u, g = lane >> 4;
    const unsigned cg = w >> 1, T = w & 1u;
    const unsigned ql = 8u * (rho >> 2) + (rho & 3u);
    unsigned xr[2], mr[2];
#pragma unroll
    for (int j = 0; j < 2; ++j) {
        xr[j] = 73728u + ql * 384u + (((4u * j + g) ^ (rho >> 1)) << 4);
        mr[j] = 147456u + 8192u + ql * 128u + (((4u * j + g) ^ (rho >> 1)) << 4);
    }
    unsigned ow = 122880u + (4u * w + g) * 1024u + rho * 16u;
    unsigned ow2 = 122880u + (16u + 2u * w + (g >> 1)) * 1024u + rho * 16u + (g & 1u) * 8u;
    unsigned ord = 122880u + g * 1024u + (32u * T + rho) * 16u;
    __builtin_amdgcn_sched_barrier(0);
    asm volatile(
        "s_nop 4\n"
        "s_add_u32 m0, s40, 36864\n"
        "s_add_u32 s64, s65, 110592\n"
        "buffer_load_dwordx4 v128, s[20:23], s64 offen lds\n"
        "buffer_load_dwordx4 v129, s[20:23], s64 offen offset:1024 lds\n"
        "buffer_load_dwordx4 v130, s[20:23], s64 offen offset:2048 lds\n"
        :: "{v242}"(dm[0]), "{v243}"(dm[1]), "{v244}"(dm[2]), "{v128}"(wsrc[0]), "{v129}"(wsrc[1]), "{v130}"(wsrc[2]), "{s[8:11]}"(srdx), "{s[20:23]}"(srdw), "{s47}"(ya), "{s40}"(w3), "{s65}"(w9), "s"(ldsb)
        : "s64", "scc", "memory");
    __builtin_amdgcn_sched_barrier(0);
    unsigned wor = 0u + cg * 18432u + g * 1536u + rho * 96u + (g & 1u) * 16u;
    unsigned wor2 = 0u + cg * 18432u + g * 1536u + rho * 96u - (g & 1u) * 16u;
    unsigned ml = 147456u + w * 2048u + lane * 16u;
    unsigned ms = (lane >> 3) * 128u + (((lane & 7u) ^ (((lane >> 4) & 1u) | ((w & 1u) << 2))) << 4);
    unsigned st = g * 196608u + cg * 192u + rho * 12u;
    unsigned l16 = lane * 16u;
    unsigned id0 = (rho == 4u * g ? 0x3f80u : 0u) | (rho == 4u * g + 1u ? 0x3f800000u : 0u);
    unsigned id1 = (rho == 4u * g + 2u ? 0x3f80u : 0u) | (rho == 4u * g + 3u ? 0x3f800000u : 0u);
    unsigned x0 = (8u * wx + 4u) * 384u, x1 = ((8u * wx + 8u) & 511u) * 384u;
    u32x4 srdm = {(unsigned)(size_t)mask, (unsigned)((size_t)mask >> 32) & 0xffffu, 33554432u, 0x20000u};
    u32x4 srdo = {(unsigned)(size_t)out, (unsigned)((size_t)out >> 32) & 0xffffu, 100663296u, 0x20000u};
    u32x4 srdwo = {(unsigned)(size_t)wo, (unsigned)((size_t)wo >> 32) & 0xffffu, 73728u, 0x20000u};
    u32x4 srdrp = {(unsigned)(size_t)rpi, (unsigned)((size_t)rpi >> 32) & 0xffffu, 16384u, 0x20000u};
    u32x4 srdrb = {(unsigned)(size_t)rpb, (unsigned)((size_t)rpb >> 32) & 0xffffu, 1800u, 0x20000u};
    __builtin_amdgcn_sched_barrier(0);
    asm volatile(
        "s_nop 4\n"
        "s_add_u32 m0, s40, 49152\n"
        "s_add_u32 s64, s65, 147456\n"
        "buffer_load_dwordx4 v128, s[20:23], s64 offen lds\n"
        "buffer_load_dwordx4 v129, s[20:23], s64 offen offset:1024 lds\n"
        "buffer_load_dwordx4 v130, s[20:23], s64 offen offset:2048 lds\n"
        :: "{v242}"(dm[0]), "{v243}"(dm[1]), "{v244}"(dm[2]), "{v128}"(wsrc[0]), "{v129}"(wsrc[1]), "{v130}"(wsrc[2]), "{s[8:11]}"(srdx), "{s[20:23]}"(srdw), "{s47}"(ya), "{s40}"(w3), "{s65}"(w9), "s"(ldsb)
        : "s64", "scc", "memory");
    __builtin_amdgcn_sched_barrier(0);
    const unsigned q4 = (lane >> 2) & 3u, p4 = lane & 3u;
    unsigned wrd[3];
#pragma unroll
    for (int p = 0; p < 3; ++p) {
        unsigned c = 6u * w + 2u * p + (p4 >> 1);
        wrd[p] = (8u * g + q4) * 384u + ((c ^ (2u * ((q4 >> 1) | ((g & 1u) << 1)))) << 4) + (p4 & 1u) * 8u;
    }
    unsigned od = cg * 18432u + g * 1536u + (((48u * T + rho) ^ (g & 1u)) << 4);
    unsigned rbs = lane * 8u + 2u * w, rbw = 151552u + w * 1024u + lane * 4u;
    const unsigned pfi = ((bid >> 3) & 31u) * 4u + w;
    unsigned pf0 = pfi * 2048u + lane * 16u, pf1 = pfi * 1024u + lane * 16u;
    __builtin_amdgcn_sched_barrier(0);
    asm volatile(
        "s_nop 4\n"
        "s_add_u32 m0, s40, 61440\n"
        "s_add_u32 s64, s65, 184320\n"
        "buffer_load_dwordx4 v128, s[20:23], s64 offen lds\n"
        "buffer_load_dwordx4 v129, s[20:23], s64 offen offset:1024 lds\n"
        "buffer_load_dwordx4 v130, s[20:23], s64 offen offset:2048 lds\n"
        :: "{v242}"(dm[0]), "{v243}"(dm[1]), "{v244}"(dm[2]), "{v128}"(wsrc[0]), "{v129}"(wsrc[1]), "{v130}"(wsrc[2]), "{s[8:11]}"(srdx), "{s[20:23]}"(srdw), "{s47}"(ya), "{s40}"(w3), "{s65}"(w9), "s"(ldsb)
        : "s64", "scc", "memory");
    __builtin_amdgcn_sched_barrier(0);
    unsigned rs = g * 256u + ((rho ^ (g | ((w & 1u) << 3))) << 4);
    unsigned rr = 135168u + ql * 256u + (((2u * g) ^ rho) << 4);
    unsigned oa = (4u * g + q4) * 384u + 96u * w + p4 * 8u;
    unsigned ob = (48u * (g >> 1) + 8u * (g & 1u) + q4) * 384u + 96u * w + p4 * 8u;
    unsigned mk = (bid + 256u) * 8192u + w * 2048u;
    unsigned raw = (8u * wy0 + 4u + 4u * T) * 196608u;
    asm volatile(
        "s_nop 4\n"
        "s_mul_i32 s40, s42, 3072\n"
        "s_mul_i32 s65, s42, 9216\n"
        "s_lshl_b32 s48, s42, 11\n"
        "s_add_u32 s48, s48, 147456\n"
        "s_mov_b32 s24, 0x3fb8aa3b\n"
        "s_mov_b32 s25, 0xbfb8aa3b\n"
        "s_mov_b32 s26, 0xff7fff7f\n"
        "s_mov_b32 s49, 0x3e13cd3a\n"
        "s_mov_b32 s50, 0x3e13cd3a\n"
        "s_mov_b32 s51, 0x3e13cd3a\n"
        "s_mov_b32 s27, 14\n"
        "s_mov_b32 s28, 73728\n"
        "s_mov_b32 s29, 122880\n"
        "s_mov_b32 s30, 98304\n"
        "s_mov_b32 s38, 0x80000000\n"
        "s_mov_b32 s39, 0x80000000\n"
        "s_lshl_b32 s67, s42, 10\n"
        "s_add_u32 s67, s67, 151552\n"
        "v_mov_b32 v188, v170\n"
        "v_mov_b32 v189, v171\n"
        "v_mov_b32 v210, v170\n"
        "v_mov_b32 v211, v171\n"
        "v_mov_b32 v228, v170\n"
        "v_mov_b32 v229, v171\n"
        "v_accvgpr_write_b32 a252, v140\n"
        "v_add_u32 v136, 98304, v131\n"
        "v_accvgpr_write_b32 a253, v141\n"
        "v_add_u32 v137, 98304, v132\n"
        "v_accvgpr_write_b32 a254, v147\n"
        "v_add_u32 v138, 98304, v133\n"
        "v_mov_b32 v150, v240\n"
        "v_mov_b32 v151, v241\n"
        "v_xor_b32 v253, 64, v139\n"
        "v_xor_b32 v232, 0, v144\n"
        "v_xor_b32 v233, 16, v144\n"
        "v_xor_b32 v234, 128, v144\n"
        "v_xor_b32 v235, 144, v144\n"
        "s_lshl_b32 s46, s42, 12\n"
        "buffer_load_dwordx4 v[64:67], v148, s[20:23], 0 offen\n"
        "buffer_load_dwordx4 v[68:71], v148, s[20:23], 0 offen offset:1024\n"
        "buffer_load_dwordx4 v[72:75], v149, s[52:55], 0 offen\n"
        "s_waitcnt vmcnt(12)\n"
        "s_barrier\n"
        "s_add_u32 m0, s40, 73728\n"
        "ds_read_b64_tr_b16 v[76:77], v131 offset:0\n"
        "ds_read_b64_tr_b16 v[78:79], v131 offset:1536\n"
        "ds_read_b64_tr_b16 v[80:81], v131 offset:12288\n"
        "ds_read_b64_tr_b16 v[82:83], v131 offset:13824\n"
        "ds_read_b64_tr_b16 v[84:85], v131 offset:24576\n"
        "ds_read_b64_tr_b16 v[86:87], v131 offset:26112\n"
        "ds_read_b64_tr_b16 v[88:89], v132 offset:0\n"
        "ds_read_b64_tr_b16 v[90:91], v132 offset:1536\n"
        "ds_read_b64_tr_b16 v[92:93], v132 offset:12288\n"
        "ds_read_b64_tr_b16 v[94:95], v132 offset:13824\n"
        "ds_read_b64_tr_b16 v[104:105], v132 offset:24576\n"
        "ds_read_b64_tr_b16 v[106:107], v132 offset:26112\n"
        "ds_read_b64_tr_b16 v[108:109], v133 offset:0\n"
        "ds_read_b64_tr_b16 v[110:111], v133 offset:1536\n"
        "ds_read_b64_tr_b16 v[116:117], v133 offset:12288\n"
        "ds_read_b64_tr_b16 v[118:119], v133 offset:13824\n"
        "ds_read_b64_tr_b16 v[120:121], v133 offset:24576\n"
        "ds_read_b64_tr_b16 v[122:123], v133 offset:26112\n"
        "s_sub_u32 s64, s47, 6291456\n"
        "buffer_load_dwordx4 v242, s[8:11], s64 offen lds\n"
        "s_add_u32 m0, s40, 74752\n"
        "s_sub_u32 s64, s47, 6291456\n"
        "buffer_load_dwordx4 v243, s[8:11], s64 offen lds\n"
        "s_add_u32 m0, s40, 75776\n"
        "s_sub_u32 s64, s47, 6291456\n"
        "buffer_load_dwordx4 v244, s[8:11], s64 offen lds\n"
        "s_add_u32 m0, s40, 86016\n"
        "s_sub_u32 s64, s47, 5505024\n"
        "buffer_load_dwordx4 v242, s[8:11], s64 offen lds\n"
        "s_add_u32 m0, s40, 87040\n"
        "s_sub_u32 s64, s47, 5505024\n"
        "buffer_load_dwordx4 v243, s[8:11], s64 offen lds\n"
        "s_add_u32 m0, s40, 88064\n"
        "s_sub_u32 s64, s47, 5505024\n"
        "buffer_load_dwordx4 v244, s[8:11], s64 offen lds\n"
        "s_waitcnt lgkmcnt(15)\n"
        "v_lshlrev_b32 v0, 16, v76\n"
        "v_lshlrev_b32 v2, 16, v77\n"
        "v_lshlrev_b32 v4, 16, v78\n"
        "v_lshlrev_b32 v6, 16, v79\n"
        "v_and_b32 v1, 0xffff0000, v76\n"
        "v_and_b32 v3, 0xffff0000, v77\n"
        "v_and_b32 v5, 0xffff0000, v78\n"
        "v_and_b32 v7, 0xffff0000, v79\n"
        "v_pk_mul_f32 v[0:1], v[0:1], s[50:51]\n"
        "v_pk_mul_f32 v[2:3], v[2:3], s[50:51]\n"
        "v_pk_mul_f32 v[4:5], v[4:5], s[50:51]\n"
        "v_pk_mul_f32 v[6:7], v[6:7], s[50:51]\n"
        "v_cvt_pk_bf16_f32 v0, v0, v1\n"
        "v_cvt_pk_bf16_f32 v2, v2, v3\n"
        "v_cvt_pk_bf16_f32 v4, v4, v5\n"
        "v_cvt_pk_bf16_f32 v6, v6, v7\n"
        "v_accvgpr_write_b32 a0, v0\n"
        "v_accvgpr_write_b32 a1, v2\n"
        "v_accvgpr_write_b32 a2, v4\n"
        "v_accvgpr_write_b32 a3, v6\n"
        "v_lshlrev_b32 v8, 16, v80\n"
        "v_lshlrev_b32 v10, 16, v81\n"
        "s_waitcnt lgkmcnt(14)\n"
        "v_lshlrev_b32 v12, 16, v82\n"
        "v_lshlrev_b32 v14, 16, v83\n"
        "v_and_b32 v9, 0xffff0000, v80\n"
        "v_and_b32 v11, 0xffff0000, v81\n"
        "v_and_b32 v13, 0xffff0000, v82\n"
        "v_and_b32 v15, 0xffff0000, v83\n"
        "v_pk_mul_f32 v[8:9], v[8:9], s[50:51]\n"
        "v_pk_mul_f32 v[10:11], v[10:11], s[50:51]\n"
        "v_pk_mul_f32 v[12:13], v[12:13], s[50:51]\n"
        "v_pk_mul_f32 v[14:15], v[14:15], s[50:51]\n"
        "v_cvt_pk_bf16_f32 v8, v8, v9\n"
        "v_cvt_pk_bf16_f32 v10, v10, v11\n"
        "v_cvt_pk_bf16_f32 v12, v12, v13\n"
        "v_cvt_pk_bf16_f32 v14, v14, v15\n"
        "v_accvgpr_write_b32 a4, v8\n"
        "v_accvgpr_write_b32 a5, v10\n"
        "v_accvgpr_write_b32 a6, v12\n"
        "v_accvgpr_write_b32 a7, v14\n"
        "s_waitcnt lgkmcnt(13)\n"
        "v_lshlrev_b32 v0, 16, v84\n"
        "v_lshlrev_b32 v2, 16, v85\n"
        "s_waitcnt lgkmcnt(12)\n"
        "v_lshlrev_b32 v4, 16, v86\n"
        "v_lshlrev_b32 v6, 16, v87\n"
        "v_and_b32 v1, 0xffff0000, v84\n"
        "v_and_b32 v3, 0xffff0000, v85\n"
        "v_and_b32 v5, 0xffff0000, v86\n"
        "v_and_b32 v7, 0xffff0000, v87\n"
        "v_pk_mul_f32 v[0:1], v[0:1], s[50:51]\n"
        "v_pk_mul_f32 v[2:3], v[2:3], s[50:51]\n"
        "v_pk_mul_f32 v[4:5], v[4:5], s[50:51]\n"
        "v_pk_mul_f32 v[6:7], v[6:7], s[50:51]\n"
        "v_cvt_pk_bf16_f32 v0, v0, v1\n"
        "v_cvt_pk_bf16_f32 v2, v2, v3\n"
        "v_cvt_pk_bf16_f32 v4, v4, v5\n"
        "v_cvt_pk_bf16_f32 v6, v6, v7\n"
        "v_accvgpr_write_b32 a8, v0\n"
        "v_accvgpr_write_b32 a9, v2\n"
        "v_accvgpr_write_b32 a10, v4\n"
        "v_accvgpr_write_b32 a11, v6\n"
        "s_waitcnt lgkmcnt(11)\n"
        "v_lshlrev_b32 v8, 16, v88\n"
        "v_lshlrev_b32 v10, 16, v89\n"
        "s_waitcnt lgkmcnt(10)\n"
        "v_lshlrev_b32 v12, 16, v90\n"
        "v_lshlrev_b32 v14, 16, v91\n"
        "buffer_load_ushort v236, v145, s[60:63], 0 offen offset:0\n"
        "buffer_load_ushort v237, v145, s[60:63], 0 offen offset:512\n"
        "buffer_load_ushort v238, v145, s[60:63], 0 offen offset:1024\n"
        "buffer_load_ushort v239, v145, s[60:63], 0 offen offset:1536\n"
        "v_and_b32 v9, 0xffff0000, v88\n"
        "v_and_b32 v11, 0xffff0000, v89\n"
        "v_and_b32 v13, 0xffff0000, v90\n"
        "v_and_b32 v15, 0xffff0000, v91\n"
        "v_pk_mul_f32 v[8:9], v[8:9], s[50:51]\n"
        "v_pk_mul_f32 v[10:11], v[10:11], s[50:51]\n"
        "v_pk_mul_f32 v[12:13], v[12:13], s[50:51]\n"
        "v_pk_mul_f32 v[14:15], v[14:15], s[50:51]\n"
        "s_add_u32 m0, s46, 135168\n"
        "v_cvt_pk_bf16_f32 v8, v8, v9\n"
        "v_cvt_pk_bf16_f32 v10, v10, v11\n"
        "v_cvt_pk_bf16_f32 v12, v12, v13\n"
        "v_cvt_pk_bf16_f32 v14, v14, v15\n"
        "s_mov_b32 s64, s46\n"
        "v_accvgpr_write_b32 a24, v8\n"
        "v_accvgpr_write_b32 a25, v10\n"
        "v_accvgpr_write_b32 a26, v12\n"
        "v_accvgpr_write_b32 a27, v14\n"
        "buffer_load_dwordx4 v139, s[56:59], s64 offen lds\n"
        "s_waitcnt lgkmcnt(9)\n"
        "v_lshlrev_b32 v0, 16, v92\n"
        "v_lshlrev_b32 v2, 16, v93\n"
        "s_waitcnt lgkmcnt(8)\n"
        "v_lshlrev_b32 v4, 16, v94\n"
        "v_lshlrev_b32 v6, 16, v95\n"
        "s_waitcnt lgkmcnt(0)\n"
        "s_waitcnt vmcnt(14)\n"
        "s_barrier\n"
        "v_and_b32 v1, 0xffff0000, v92\n"
        "v_and_b32 v3, 0xffff0000, v93\n"
        "v_and_b32 v5, 0xffff0000, v94\n"
        "v_and_b32 v7, 0xffff0000, v95\n"
        "ds_read_b64_tr_b16 v[152:153], v131 offset:36864\n"
        "ds_read_b64_tr_b16 v[154:155], v131 offset:38400\n"
        "ds_read_b64_tr_b16 v[172:173], v131 offset:49152\n"
        "ds_read_b64_tr_b16 v[174:175], v131 offset:50688\n"
        "ds_read_b64_tr_b16 v[192:193], v131 offset:61440\n"
        "ds_read_b64_tr_b16 v[194:195], v131 offset:62976\n"
        "ds_read_b64_tr_b16 v[212:213], v132 offset:36864\n"
        "ds_read_b64_tr_b16 v[214:215], v132 offset:38400\n"
        "ds_read_b64_tr_b16 v[156:157], v132 offset:49152\n"
        "ds_read_b64_tr_b16 v[158:159], v132 offset:50688\n"
        "ds_read_b64_tr_b16 v[176:177], v132 offset:61440\n"
        "ds_read_b64_tr_b16 v[178:179], v132 offset:62976\n"
        "ds_read_b64_tr_b16 v[196:197], v133 offset:36864\n"
        "ds_read_b64_tr_b16 v[198:199], v133 offset:38400\n"
        "ds_read_b64_tr_b16 v[216:217], v133 offset:49152\n"
        "ds_read_b64_tr_b16 v[218:219], v133 offset:50688\n"
        "ds_read_b64_tr_b16 v[124:125], v133 offset:61440\n"
        "ds_read_b64_tr_b16 v[126:127], v133 offset:62976\n"
        "v_pk_mul_f32 v[0:1], v[0:1], s[50:51]\n"
        "v_pk_mul_f32 v[2:3], v[2:3], s[50:51]\n"
        "v_pk_mul_f32 v[4:5], v[4:5], s[50:51]\n"
        "v_pk_mul_f32 v[6:7], v[6:7], s[50:51]\n"
        "v_cvt_pk_bf16_f32 v0, v0, v1\n"
        "v_cvt_pk_bf16_f32 v2, v2, v3\n"
        "v_cvt_pk_bf16_f32 v4, v4, v5\n"
        "v_cvt_pk_bf16_f32 v6, v6, v7\n"
        "buffer_load_dwordx4 v139, s[56:59], s64 offen offset:1024 lds\n"
        "v_accvgpr_write_b32 a28, v0\n"
        "v_accvgpr_write_b32 a29, v2\n"
        "v_accvgpr_write_b32 a30, v4\n"
        "v_accvgpr_write_b32 a31, v6\n"
        "v_lshlrev_b32 v8, 16, v104\n"
        "v_lshlrev_b32 v10, 16, v105\n"
        "v_lshlrev_b32 v12, 16, v106\n"
        "v_lshlrev_b32 v14, 16, v107\n"
        "v_and_b32 v9, 0xffff0000, v104\n"
        "v_and_b32 v11, 0xffff0000, v105\n"
        "v_and_b32 v13, 0xffff0000, v106\n"
        "v_and_b32 v15, 0xffff0000, v107\n"
        "v_pk_mul_f32 v[8:9], v[8:9], s[50:51]\n"
        "v_pk_mul_f32 v[10:11], v[10:11], s[50:51]\n"
        "v_pk_mul_f32 v[12:13], v[12:13], s[50:51]\n"
        "v_pk_mul_f32 v[14:15], v[14:15], s[50:51]\n"
        "buffer_load_dwordx4 v253, s[56:59], s64 offen offset:2048 lds\n"
        "v_cvt_pk_bf16_f32 v8, v8, v9\n"
        "v_cvt_pk_bf16_f32 v10, v10, v11\n"
        "v_cvt_pk_bf16_f32 v12, v12, v13\n"
        "v_cvt_pk_bf16_f32 v14, v14, v15\n"
        "v_accvgpr_write_b32 a32, v8\n"
        "v_accvgpr_write_b32 a33, v10\n"
        "v_accvgpr_write_b32 a34, v12\n"
        "v_accvgpr_write_b32 a35, v14\n"
        "v_lshlrev_b32 v0, 16, v108\n"
        "v_lshlrev_b32 v2, 16, v109\n"
        "v_lshlrev_b32 v4, 16, v110\n"
        "v_lshlrev_b32 v6, 16, v111\n"
        "v_and_b32 v1, 0xffff0000, v108\n"
        "v_and_b32 v3, 0xffff0000, v109\n"
        "v_and_b32 v5, 0xffff0000, v110\n"
        "v_and_b32 v7, 0xffff0000, v111\n"
        "buffer_load_dwordx4 v253, s[56:59], s64 offen offset:3072 lds\n"
        "v_pk_mul_f32 v[0:1], v[0:1], s[50:51]\n"
        "v_pk_mul_f32 v[2:3], v[2:3], s[50:51]\n"
        "v_pk_mul_f32 v[4:5], v[4:5], s[50:51]\n"
        "v_pk_mul_f32 v[6:7], v[6:7], s[50:51]\n"
        "v_cvt_pk_bf16_f32 v0, v0, v1\n"
        "v_cvt_pk_bf16_f32 v2, v2, v3\n"
        "v_cvt_pk_bf16_f32 v4, v4, v5\n"
        "v_cvt_pk_bf16_f32 v6, v6, v7\n"
        "v_accvgpr_write_b32 a48, v0\n"
        "v_accvgpr_write_b32 a49, v2\n"
        "v_accvgpr_write_b32 a50, v4\n"
        "v_accvgpr_write_b32 a51, v6\n"
        "v_lshlrev_b32 v8, 16, v116\n"
        "v_lshlrev_b32 v10, 16, v117\n"
        "v_lshlrev_b32 v12, 16, v118\n"
        "v_lshlrev_b32 v14, 16, v119\n"
        "v_and_b32 v9, 0xffff0000, v116\n"
        "v_and_b32 v11, 0xffff0000, v117\n"
        "v_and_b32 v13, 0xffff0000, v118\n"
        "v_and_b32 v15, 0xffff0000, v119\n"
        "v_pk_mul_f32 v[8:9], v[8:9], s[50:51]\n"
        "v_pk_mul_f32 v[10:11], v[10:11], s[50:51]\n"
        "v_pk_mul_f32 v[12:13], v[12:13], s[50:51]\n"
        "v_pk_mul_f32 v[14:15], v[14:15], s[50:51]\n"
        "v_cvt_pk_bf16_f32 v8, v8, v9\n"
        "v_cvt_pk_bf16_f32 v10, v10, v11\n"
        "v_cvt_pk_bf16_f32 v12, v12, v13\n"
        "v_cvt_pk_bf16_f32 v14, v14, v15\n"
        "v_accvgpr_write_b32 a52, v8\n"
        "v_accvgpr_write_b32 a53, v10\n"
        "v_accvgpr_write_b32 a54, v12\n"
        "v_accvgpr_write_b32 a55, v14\n"
        "v_lshlrev_b32 v0, 16, v120\n"
        "v_lshlrev_b32 v2, 16, v121\n"
        "v_lshlrev_b32 v4, 16, v122\n"
        "v_lshlrev_b32 v6, 16, v123\n"
        "v_and_b32 v1, 0xffff0000, v120\n"
        "v_and_b32 v3, 0xffff0000, v121\n"
        "v_and_b32 v5, 0xffff0000, v122\n"
        "v_and_b32 v7, 0xffff0000, v123\n"
        "v_pk_mul_f32 v[0:1], v[0:1], s[50:51]\n"
        "v_pk_mul_f32 v[2:3], v[2:3], s[50:51]\n"
        "v_pk_mul_f32 v[4:5], v[4:5], s[50:51]\n"
        "v_pk_mul_f32 v[6:7], v[6:7], s[50:51]\n"
        "v_cvt_pk_bf16_f32 v0, v0, v1\n"
        "v_cvt_pk_bf16_f32 v2, v2, v3\n"
        "v_cvt_pk_bf16_f32 v4, v4, v5\n"
        "v_cvt_pk_bf16_f32 v6, v6, v7\n"
        "v_accvgpr_write_b32 a56, v0\n"
        "v_accvgpr_write_b32 a57, v2\n"
        "v_accvgpr_write_b32 a58, v4\n"
        "v_accvgpr_write_b32 a59, v6\n"
        "s_waitcnt lgkmcnt(15)\n"
        "v_lshlrev_b32 v8, 16, v152\n"
        "v_lshlrev_b32 v10, 16, v153\n"
        "v_lshlrev_b32 v12, 16, v154\n"
        "v_lshlrev_b32 v14, 16, v155\n"
        "v_and_b32 v9, 0xffff0000, v152\n"
        "v_and_b32 v11, 0xffff0000, v153\n"
        "v_and_b32 v13, 0xffff0000, v154\n"
        "v_and_b32 v15, 0xffff0000, v155\n"
        "v_pk_mul_f32 v[8:9], v[8:9], s[50:51]\n"
        "v_pk_mul_f32 v[10:11], v[10:11], s[50:51]\n"
        "v_pk_mul_f32 v[12:13], v[12:13], s[50:51]\n"
        "v_pk_mul_f32 v[14:15], v[14:15], s[50:51]\n"
        "v_cvt_pk_bf16_f32 v8, v8, v9\n"
        "v_cvt_pk_bf16_f32 v10, v10, v11\n"
        "v_cvt_pk_bf16_f32 v12, v12, v13\n"
        "v_cvt_pk_bf16_f32 v14, v14, v15\n"
        "v_accvgpr_write_b32 a12, v8\n"
        "v_accvgpr_write_b32 a13, v10\n"
        "v_accvgpr_write_b32 a14, v12\n"
        "v_accvgpr_write_b32 a15, v14\n"
        "v_lshlrev_b32 v0, 16, v172\n"
        "v_lshlrev_b32 v2, 16, v173\n"
        "s_waitcnt lgkmcnt(14)\n"
        "v_lshlrev_b32 v4, 16, v174\n"
        "v_lshlrev_b32 v6, 16, v175\n"
        "v_and_b32 v1, 0xffff0000, v172\n"
        "v_and_b32 v3, 0xffff0000, v173\n"
        "v_and_b32 v5, 0xffff0000, v174\n"
        "v_and_b32 v7, 0xffff0000, v175\n"
        "v_pk_mul_f32 v[0:1], v[0:1], s[50:51]\n"
        "v_pk_mul_f32 v[2:3], v[2:3], s[50:51]\n"
        "v_pk_mul_f32 v[4:5], v[4:5], s[50:51]\n"
        "v_pk_mul_f32 v[6:7], v[6:7], s[50:51]\n"
        "v_cvt_pk_bf16_f32 v0, v0, v1\n"
        "v_cvt_pk_bf16_f32 v2, v2, v3\n"
        "v_cvt_pk_bf16_f32 v4, v4, v5\n"
        "v_cvt_pk_bf16_f32 v6, v6, v7\n"
        "v_accvgpr_write_b32 a16, v0\n"
        "v_accvgpr_write_b32 a17, v2\n"
        "v_accvgpr_write_b32 a18, v4\n"
        "v_accvgpr_write_b32 a19, v6\n"
        "s_waitcnt lgkmcnt(13)\n"
        "v_lshlrev_b32 v8, 16, v192\n"
        "v_lshlrev_b32 v10, 16, v193\n"
        "s_waitcnt lgkmcnt(12)\n"
        "v_lshlrev_b32 v12, 16, v194\n"
        "v_lshlrev_b32 v14, 16, v195\n"
        "v_and_b32 v9, 0xffff0000, v192\n"
        "v_and_b32 v11, 0xffff0000, v193\n"
        "v_and_b32 v13, 0xffff0000, v194\n"
        "v_and_b32 v15, 0xffff0000, v195\n"
        "v_pk_mul_f32 v[8:9], v[8:9], s[50:51]\n"
        "v_pk_mul_f32 v[10:11], v[10:11], s[50:51]\n"
        "v_pk_mul_f32 v[12:13], v[12:13], s[50:51]\n"
        "v_pk_mul_f32 v[14:15], v[14:15], s[50:51]\n"
        "v_cvt_pk_bf16_f32 v8, v8, v9\n"
        "v_cvt_pk_bf16_f32 v10, v10, v11\n"
        "v_cvt_pk_bf16_f32 v12, v12, v13\n"
        "v_cvt_pk_bf16_f32 v14, v14, v15\n"
        "v_accvgpr_write_b32 a20, v8\n"
        "v_accvgpr_write_b32 a21, v10\n"
        "v_accvgpr_write_b32 a22, v12\n"
        "v_accvgpr_write_b32 a23, v14\n"
        "s_waitcnt lgkmcnt(11)\n"
        "v_lshlrev_b32 v0, 16, v212\n"
        "v_lshlrev_b32 v2, 16, v213\n"
        "s_waitcnt lgkmcnt(10)\n"
        "v_lshlrev_b32 v4, 16, v214\n"
        "v_lshlrev_b32 v6, 16, v215\n"
        "v_and_b32 v1, 0xffff0000, v212\n"
        "v_and_b32 v3, 0xffff0000, v213\n"
        "v_and_b32 v5, 0xffff0000, v214\n"
        "v_and_b32 v7, 0xffff0000, v215\n"
        "v_pk_mul_f32 v[0:1], v[0:1], s[50:51]\n"
        "v_pk_mul_f32 v[2:3], v[2:3], s[50:51]\n"
        "v_pk_mul_f32 v[4:5], v[4:5], s[50:51]\n"
        "v_pk_mul_f32 v[6:7], v[6:7], s[50:51]\n"
        "v_cvt_pk_bf16_f32 v0, v0, v1\n"
        "v_cvt_pk_bf16_f32 v2, v2, v3\n"
        "v_cvt_pk_bf16_f32 v4, v4, v5\n"
        "v_cvt_pk_bf16_f32 v6, v6, v7\n"
        "s_add_u32 m0, s40, 98304\n"
        "v_accvgpr_write_b32 a36, v0\n"
        "v_accvgpr_write_b32 a37, v2\n"
        "v_accvgpr_write_b32 a38, v4\n"
        "v_accvgpr_write_b32 a39, v6\n"
        "s_add_u32 s64, s65, 384\n"
        "s_waitcnt lgkmcnt(9)\n"
        "v_lshlrev_b32 v8, 16, v156\n"
        "v_lshlrev_b32 v10, 16, v157\n"
        "s_waitcnt lgkmcnt(8)\n"
        "v_lshlrev_b32 v12, 16, v158\n"
        "v_lshlrev_b32 v14, 16, v159\n"
        "buffer_load_dwordx4 v128, s[20:23], s64 offen lds\n"
        "v_and_b32 v9, 0xffff0000, v156\n"
        "v_and_b32 v11, 0xffff0000, v157\n"
        "v_and_b32 v13, 0xffff0000, v158\n"
        "v_and_b32 v15, 0xffff0000, v159\n"
        "v_pk_mul_f32 v[8:9], v[8:9], s[50:51]\n"
        "v_pk_mul_f32 v[10:11], v[10:11], s[50:51]\n"
        "v_pk_mul_f32 v[12:13], v[12:13], s[50:51]\n"
        "v_pk_mul_f32 v[14:15], v[14:15], s[50:51]\n"
        "v_cvt_pk_bf16_f32 v8, v8, v9\n"
        "v_cvt_pk_bf16_f32 v10, v10, v11\n"
        "v_cvt_pk_bf16_f32 v12, v12, v13\n"
        "v_cvt_pk_bf16_f32 v14, v14, v15\n"
        "v_accvgpr_write_b32 a40, v8\n"
        "v_accvgpr_write_b32 a41, v10\n"
        "v_accvgpr_write_b32 a42, v12\n"
        "v_accvgpr_write_b32 a43, v14\n"
        "buffer_load_dwordx4 v129, s[20:23], s64 offen offset:1024 lds\n"
        "s_waitcnt lgkmcnt(7)\n"
        "v_lshlrev_b32 v0, 16, v176\n"
        "v_lshlrev_b32 v2, 16, v177\n"
        "s_waitcnt lgkmcnt(6)\n"
        "v_lshlrev_b32 v4, 16, v178\n"
        "v_lshlrev_b32 v6, 16, v179\n"
        "v_and_b32 v1, 0xffff0000, v176\n"
        "v_and_b32 v3, 0xffff0000, v177\n"
        "v_and_b32 v5, 0xffff0000, v178\n"
        "v_and_b32 v7, 0xffff0000, v179\n"
        "v_pk_mul_f32 v[0:1], v[0:1], s[50:51]\n"
        "v_pk_mul_f32 v[2:3], v[2:3], s[50:51]\n"
        "v_pk_mul_f32 v[4:5], v[4:5], s[50:51]\n"
        "v_pk_mul_f32 v[6:7], v[6:7], s[50:51]\n"
        "v_cvt_pk_bf16_f32 v0, v0, v1\n"
        "v_cvt_pk_bf16_f32 v2, v2, v3\n"
        "v_cvt_pk_bf16_f32 v4, v4, v5\n"
        "v_cvt_pk_bf16_f32 v6, v6, v7\n"
        "buffer_load_dwordx4 v130, s[20:23], s64 offen offset:2048 lds\n"
        "v_accvgpr_write_b32 a44, v0\n"
        "v_accvgpr_write_b32 a45, v2\n"
        "v_accvgpr_write_b32 a46, v4\n"
        "v_accvgpr_write_b32 a47, v6\n"
        "s_waitcnt lgkmcnt(5)\n"
        "v_lshlrev_b32 v8, 16, v196\n"
        "v_lshlrev_b32 v10, 16, v197\n"
        "s_waitcnt lgkmcnt(4)\n"
        "v_lshlrev_b32 v12, 16, v198\n"
        "v_lshlrev_b32 v14, 16, v199\n"
        "s_add_u32 m0, s40, 110592\n"
        "v_and_b32 v9, 0xffff0000, v196\n"
        "v_and_b32 v11, 0xffff0000, v197\n"
        "v_and_b32 v13, 0xffff0000, v198\n"
        "v_and_b32 v15, 0xffff0000, v199\n"
        "s_add_u32 s64, s65, 37248\n"
        "v_pk_mul_f32 v[8:9], v[8:9], s[50:51]\n"
        "v_pk_mul_f32 v[10:11], v[10:11], s[50:51]\n"
        "v_pk_mul_f32 v[12:13], v[12:13], s[50:51]\n"
        "v_pk_mul_f32 v[14:15], v[14:15], s[50:51]\n"
        "buffer_load_dwordx4 v128, s[20:23], s64 offen lds\n"
        "v_cvt_pk_bf16_f32 v8, v8, v9\n"
        "v_cvt_pk_bf16_f32 v10, v10, v11\n"
        "v_cvt_pk_bf16_f32 v12, v12, v13\n"
        "v_cvt_pk_bf16_f32 v14, v14, v15\n"
        "v_accvgpr_write_b32 a60, v8\n"
        "v_accvgpr_write_b32 a61, v10\n"
        "v_accvgpr_write_b32 a62, v12\n"
        "v_accvgpr_write_b32 a63, v14\n"
        "s_waitcnt lgkmcnt(3)\n"
        "v_lshlrev_b32 v0, 16, v216\n"
        "v_lshlrev_b32 v2, 16, v217\n"
        "s_waitcnt lgkmcnt(2)\n"
        "v_lshlrev_b32 v4, 16, v218\n"
        "v_lshlrev_b32 v6, 16, v219\n"
        "v_and_b32 v1, 0xffff0000, v216\n"
        "v_and_b32 v3, 0xffff0000, v217\n"
        "v_and_b32 v5, 0xffff0000, v218\n"
        "v_and_b32 v7, 0xffff0000, v219\n"
        "buffer_load_dwordx4 v129, s[20:23], s64 offen offset:1024 lds\n"
        "v_pk_mul_f32 v[0:1], v[0:1], s[50:51]\n"
        "v_pk_mul_f32 v[2:3], v[2:3], s[50:51]\n"
        "v_pk_mul_f32 v[4:5], v[4:5], s[50:51]\n"
        "v_pk_mul_f32 v[6:7], v[6:7], s[50:51]\n"
        "v_cvt_pk_bf16_f32 v0, v0, v1\n"
        "v_cvt_pk_bf16_f32 v2, v2, v3\n"
        "v_cvt_pk_bf16_f32 v4, v4, v5\n"
        "v_cvt_pk_bf16_f32 v6, v6, v7\n"
        "v_accvgpr_write_b32 a64, v0\n"
        "v_accvgpr_write_b32 a65, v2\n"
        "v_accvgpr_write_b32 a66, v4\n"
        "v_accvgpr_write_b32 a67, v6\n"
        "s_waitcnt lgkmcnt(1)\n"
        "v_lshlrev_b32 v8, 16, v124\n"
        "v_lshlrev_b32 v10, 16, v125\n"
        "s_waitcnt lgkmcnt(0)\n"
        "v_lshlrev_b32 v12, 16, v126\n"
        "v_lshlrev_b32 v14, 16, v127\n"
        "buffer_load_dwordx4 v130, s[20:23], s64 offen offset:2048 lds\n"
        "v_and_b32 v9, 0xffff0000, v124\n"
        "v_and_b32 v11, 0xffff0000, v125\n"
        "v_and_b32 v13, 0xffff0000, v126\n"
        "v_and_b32 v15, 0xffff0000, v127\n"
        "v_pk_mul_f32 v[8:9], v[8:9], s[50:51]\n"
        "v_pk_mul_f32 v[10:11], v[10:11], s[50:51]\n"
        "v_pk_mul_f32 v[12:13], v[12:13], s[50:51]\n"
        "v_pk_mul_f32 v[14:15], v[14:15], s[50:51]\n"
        "s_add_u32 m0, s40, 122880\n"
        "v_cvt_pk_bf16_f32 v8, v8, v9\n"
        "v_cvt_pk_bf16_f32 v10, v10, v11\n"
        "v_cvt_pk_bf16_f32 v12, v12, v13\n"
        "v_cvt_pk_bf16_f32 v14, v14, v15\n"
        "s_add_u32 s64, s65, 74112\n"
        "v_accvgpr_write_b32 a68, v8\n"
        "v_accvgpr_write_b32 a69, v10\n"
        "v_accvgpr_write_b32 a70, v12\n"
        "v_accvgpr_write_b32 a71, v14\n"
        "buffer_load_dwordx4 v128, s[20:23], s64 offen lds\n"
        "buffer_load_dwordx4 v129, s[20:23], s64 offen offset:1024 lds\n"
        "buffer_load_dwordx4 v130, s[20:23], s64 offen offset:2048 lds\n"
        "s_waitcnt vmcnt(20)\n"
        "s_barrier\n"
        "ds_read_b128 a[224:227], v150 offset:0\n"
        "s_waitcnt lgkmcnt(0)\n"
        "v_mfma_f32_16x16x32_bf16 v[96:99], a[0:3], a[224:227], 0\n"
        "ds_read_b128 a[228:231], v151 offset:0\n"
        "ds_read_b128 a[232:235], v150 offset:128\n"
        "s_add_u32 m0, s40, 0\n"
        "s_add_u32 s64, s65, 110976\n"
        "s_waitcnt lgkmcnt(1)\n"
        "v_mfma_f32_16x16x32_bf16 v[96:99], a[4:7], a[228:231], v[96:99]\n"
        "s_waitcnt lgkmcnt(0)\n"
        "ds_read_b128 a[236:239], v151 offset:128\n"
        "ds_read_b128 a[240:243], v150 offset:256\n"
        "v_mfma_f32_16x16x32_bf16 v[96:99], a[8:11], a[232:235], v[96:99]\n"
        "s_waitcnt lgkmcnt(1)\n"
        "ds_read_b128 a[244:247], v151 offset:256\n"
        "buffer_load_dwordx4 v128, s[20:23], s64 offen lds\n"
        "v_mfma_f32_16x16x32_bf16 v[96:99], a[12:15], a[236:239], v[96:99]\n"
        "s_waitcnt lgkmcnt(1)\n"
        "ds_read_b128 a[248:251], v150 offset:1536\n"
        "ds_read_b128 a[216:219], v151 offset:1536\n"
        "v_mfma_f32_16x16x32_bf16 v[96:99], a[16:19], a[240:243], v[96:99]\n"
        "s_waitcnt lgkmcnt(2)\n"
        "ds_read_b128 a[220:223], v150 offset:1664\n"
        "v_mfma_f32_16x16x32_bf16 v[96:99], a[20:23], a[244:247], v[96:99]\n"
        "buffer_load_dwordx4 v129, s[20:23], s64 offen offset:1024 lds\n"
        "v_mfma_f32_16x16x32_bf16 v[100:103], a[24:27], a[224:227], 0\n"
        "v_mfma_f32_16x16x32_bf16 v[100:103], a[28:31], a[228:231], v[100:103]\n"
        "v_mfma_f32_16x16x32_bf16 v[100:103], a[32:35], a[232:235], v[100:103]\n"
        "v_mfma_f32_16x16x32_bf16 v[100:103], a[36:39], a[236:239], v[100:103]\n"
        "buffer_load_dwordx4 v130, s[20:23], s64 offen offset:2048 lds\n"
        "v_mfma_f32_16x16x32_bf16 v[100:103], a[40:43], a[240:243], v[100:103]\n"
        "s_waitcnt vmcnt(19)\n"
        "v_lshlrev_b32 v236, 16, v236\n"
        "v_cvt_pk_bf16_f32 v156, v96, v97\n"
        "v_mfma_f32_16x16x32_bf16 v[100:103], a[44:47], a[244:247], v[100:103]\n"
        "s_add_u32 m0, s40, 12288\n"
        "ds_write_b32 v146, v236 offset:0\n"
        "v_mfma_f32_16x16x32_bf16 v[112:115], a[48:51], a[224:227], 0\n"
        "ds_read_b128 a[224:227], v151 offset:1664\n"
        "s_add_u32 s64, s65, 147840\n"
        "v_cvt_pk_bf16_f32 v157, v98, v99\n"
        "s_waitcnt vmcnt(18)\n"
        "v_lshlrev_b32 v237, 16, v237\n"
        "v_mfma_f32_16x16x32_bf16 v[112:115], a[52:55], a[228:231], v[112:115]\n"
        "ds_read_b128 a[228:231], v150 offset:1792\n"
        "buffer_load_dwordx4 v128, s[20:23], s64 offen lds\n"
        "v_cvt_pk_bf16_f32 v158, v100, v101\n"
        "v_mfma_f32_16x16x32_bf16 v[112:115], a[56:59], a[232:235], v[112:115]\n"
        "ds_read_b128 a[232:235], v151 offset:1792\n"
        "ds_write_b32 v146, v237 offset:256\n"
        "v_mfma_f32_16x16x32_bf16 v[112:115], a[60:63], a[236:239], v[112:115]\n"
        "v_cvt_pk_bf16_f32 v159, v102, v103\n"
        "s_waitcnt vmcnt(18)\n"
        "v_lshlrev_b32 v238, 16, v238\n"
        "s_waitcnt vmcnt(17)\n"
        "v_lshlrev_b32 v239, 16, v239\n"
        "v_mfma_f32_16x16x32_bf16 v[112:115], a[64:67], a[240:243], v[112:115]\n"
        "v_mfma_f32_16x16x32_bf16 v[112:115], a[68:71], a[244:247], v[112:115]\n"
        "s_waitcnt lgkmcnt(7)\n"
        "ds_write_b32 v146, v238 offset:512\n"
        "buffer_load_dwordx4 v129, s[20:23], s64 offen offset:1024 lds\n"
        "v_mfma_f32_16x16x32_bf16 v[96:99], a[0:3], a[248:251], 0\n"
        "s_waitcnt lgkmcnt(7)\n"
        "ds_write_b32 v146, v239 offset:768\n"
        "s_add_u32 s35, s47, 786432\n"
        "s_sub_u32 s46, s35, 100663296\n"
        "s_cmp_lt_u32 s46, 786432\n"
        "s_waitcnt vmcnt(5)\n"
        "s_barrier\n"
        "v_mfma_f32_16x16x32_bf16 v[96:99], a[4:7], a[216:219], v[96:99]\n"
        "s_waitcnt lgkmcnt(7)\n"
        "s_cselect_b32 s35, s46, s35\n"
        "s_add_u32 s41, s30, s40\n"
        "ds_read_b64_tr_b16 a[72:73], v136 offset:0\n"
        "ds_read_b64_tr_b16 a[74:75], v136 offset:1536\n"
        "v_mfma_f32_16x16x32_bf16 v[96:99], a[8:11], a[220:223], v[96:99]\n"
        "s_waitcnt lgkmcnt(7)\n"
        "ds_read_b64_tr_b16 a[76:77], v136 offset:12288\n"
        "ds_read_b64_tr_b16 a[78:79], v136 offset:13824\n"
        "v_cvt_pk_bf16_f32 v160, v112, v113\n"
        "v_mfma_f32_16x16x32_bf16 v[96:99], a[12:15], a[224:227], v[96:99]\n"
        "s_waitcnt lgkmcnt(8)\n"
        "buffer_load_dwordx4 v130, s[20:23], s64 offen offset:2048 lds\n"
        "ds_read_b64_tr_b16 a[80:81], v136 offset:24576\n"
        "v_cvt_pk_bf16_f32 v161, v114, v115\n"
        "v_mfma_f32_16x16x32_bf16 v[96:99], a[16:19], a[228:231], v[96:99]\n"
        "s_waitcnt lgkmcnt(8)\n"
        "ds_read_b64_tr_b16 a[82:83], v136 offset:26112\n"
        "ds_read_b64_tr_b16 a[96:97], v137 offset:0\n"
        "v_cvt_pk_bf16_f32 v166, v112, v113\n"
        "v_mfma_f32_16x16x32_bf16 v[96:99], a[20:23], a[232:235], v[96:99]\n"
        "s_add_u32 m0, s40, 24576\n"
        "ds_read_b64_tr_b16 a[98:99], v137 offset:1536\n"
        "ds_read_b128 a[236:239], v150 offset:12288\n"
        "v_cvt_pk_bf16_f32 v167, v114, v115\n"
        "v_mfma_f32_16x16x32_bf16 v[100:103], a[24:27], a[248:251], 0\n"
        "s_add_u32 s64, s65, 184704\n"
        "ds_read_b64_tr_b16 a[100:101], v137 offset:12288\n"
        "ds_read_b128 a[240:243], v151 offset:12288\n"
        "v_cvt_pk_bf16_f32 v176, v96, v97\n"
        "v_mfma_f32_16x16x32_bf16 v[100:103], a[28:31], a[216:219], v[100:103]\n"
        "buffer_load_dwordx4 v128, s[20:23], s64 offen lds\n"
        "ds_read_b64_tr_b16 a[102:103], v137 offset:13824\n"
        "v_cvt_pk_bf16_f32 v177, v98, v99\n"
        "v_mfma_f32_16x16x32_bf16 v[100:103], a[32:35], a[220:223], v[100:103]\n"
        "ds_read_b128 v[0:3], v232 offset:0\n"
        "ds_read_b128 a[244:247], v150 offset:12416\n"
        "v_mfma_f32_16x16x32_bf16 v[100:103], a[36:39], a[224:227], v[100:103]\n"
        "ds_read_b64_tr_b16 a[104:105], v137 offset:24576\n"
        "ds_read_b64_tr_b16 a[106:107], v137 offset:26112\n"
        "v_mfma_f32_16x16x32_bf16 v[100:103], a[40:43], a[228:231], v[100:103]\n"
        "ds_read_b64_tr_b16 a[120:121], v138 offset:0\n"
        "ds_read_b64_tr_b16 a[122:123], v138 offset:1536\n"
        "s_waitcnt lgkmcnt(5)\n"
        "v_lshl_add_u32 v0, v0, 2, s67\n"
        "v_mfma_f32_16x16x32_bf16 v[100:103], a[44:47], a[232:235], v[100:103]\n"
        "buffer_load_dwordx4 v129, s[20:23], s64 offen offset:1024 lds\n"
        "ds_read_b64_tr_b16 a[124:125], v138 offset:12288\n"
        "v_lshl_add_u32 v1, v1, 2, s67\n"
        "v_mfma_f32_16x16x32_bf16 v[112:115], a[48:51], a[248:251], 0\n"
        "ds_read_b128 a[248:251], v151 offset:12416\n"
        "ds_read_b128 v[16:19], v232 offset:1024\n"
        "v_lshl_add_u32 v2, v2, 2, s67\n"
        "v_mfma_f32_16x16x32_bf16 v[112:115], a[52:55], a[216:219], v[112:115]\n"
        "ds_read_b128 a[216:219], v150 offset:12544\n"
        "ds_read_b64_tr_b16 a[126:127], v138 offset:13824\n"
        "v_cvt_pk_bf16_f32 v178, v100, v101\n"
        "v_mfma_f32_16x16x32_bf16 v[112:115], a[56:59], a[220:223], v[112:115]\n"
        "ds_read_b128 a[220:223], v151 offset:12544\n"
        "ds_read_b64_tr_b16 a[128:129], v138 offset:24576\n"
        "v_cvt_pk_bf16_f32 v179, v102, v103\n"
        "v_mfma_f32_16x16x32_bf16 v[112:115], a[60:63], a[224:227], v[112:115]\n"
        "buffer_load_dwordx4 v130, s[20:23], s64 offen offset:2048 lds\n"
        "ds_read_b64_tr_b16 a[130:131], v138 offset:26112\n"
        "v_lshl_add_u32 v3, v3, 2, s67\n"
        "v_mfma_f32_16x16x32_bf16 v[112:115], a[64:67], a[228:231], v[112:115]\n"
        "ds_read_b128 v[32:35], v232 offset:8192\n"
        "ds_read_b128 v[48:51], v232 offset:9216\n"
        "s_waitcnt lgkmcnt(7)\n"
        "v_lshl_add_u32 v16, v16, 2, s67\n"
        "v_mfma_f32_16x16x32_bf16 v[112:115], a[68:71], a[232:235], v[112:115]\n"
        "ds_read_b128 a[224:227], v150 offset:13824\n"
        "ds_read_b128 a[228:231], v151 offset:13824\n"
        "v_xor_b32 v253, 32, v249\n"
        "v_mfma_f32_16x16x32_bf16 v[96:99], a[0:3], a[236:239], 0\n"
        "ds_read_b128 a[232:235], v150 offset:13952\n"
        "ds_read_b32 v0, v0\n"
        "v_lshl_add_u32 v17, v17, 2, s67\n"
        "v_mfma_f32_16x16x32_bf16 v[96:99], a[4:7], a[240:243], v[96:99]\n"
        "s_xor_b32 m0, s48, 8192\n"
        "ds_read_b32 v1, v1\n"
        "ds_read_b32 v2, v2\n"
        "v_cvt_pk_bf16_f32 v180, v112, v113\n"
        "v_mfma_f32_16x16x32_bf16 v[96:99], a[8:11], a[244:247], v[96:99]\n"
        "s_sub_u32 s64, s36, 2097152\n"
        "ds_read_b128 v[4:7], v233 offset:0\n"
        "ds_read_b32 v3, v3\n"
        "v_cvt_pk_bf16_f32 v181, v114, v115\n"
        "v_mfma_f32_16x16x32_bf16 v[96:99], a[12:15], a[248:251], v[96:99]\n"
        "s_waitcnt lgkmcnt(14)\n"
        "buffer_load_dwordx4 v249, s[12:15], s64 offen lds\n"
        "ds_read_b32 v16, v16\n"
        "v_cvt_pk_bf16_f32 v186, v112, v113\n"
        "v_mfma_f32_16x16x32_bf16 v[96:99], a[16:19], a[216:219], v[96:99]\n"
        "s_waitcnt lgkmcnt(13)\n"
        "ds_read_b32 v17, v17\n"
        "v_cvt_pk_bf16_f32 v187, v114, v115\n"
        "v_lshl_add_u32 v18, v18, 2, s67\n"
        "v_mfma_f32_16x16x32_bf16 v[96:99], a[20:23], a[220:223], v[96:99]\n"
        "ds_read_b32 v18, v18\n"
        "v_lshl_add_u32 v19, v19, 2, s67\n"
        "s_waitcnt lgkmcnt(12)\n"
        "v_lshl_add_u32 v32, v32, 2, s67\n"
        "v_mfma_f32_16x16x32_bf16 v[100:103], a[24:27], a[236:239], 0\n"
        "s_barrier\n"
        "ds_read_b32 v19, v19\n"
        "ds_read_b32 v32, v32\n"
        "v_lshl_add_u32 v33, v33, 2, s67\n"
        "v_mfma_f32_16x16x32_bf16 v[100:103], a[28:31], a[240:243], v[100:103]\n"
        "buffer_load_dwordx4 v253, s[12:15], s64 offen offset:1024 lds\n"
        "ds_read_b128 v[20:23], v233 offset:1024\n"
        "v_cvt_pk_bf16_f32 v196, v96, v97\n"
        "v_mfma_f32_16x16x32_bf16 v[100:103], a[32:35], a[244:247], v[100:103]\n"
        "ds_read_b32 v33, v33\n"
        "v_cvt_pk_bf16_f32 v197, v98, v99\n"
        "v_lshl_add_u32 v34, v34, 2, s67\n"
        "v_mfma_f32_16x16x32_bf16 v[100:103], a[36:39], a[248:251], v[100:103]\n"
        "ds_read_b32 v34, v34\n"
        "v_lshl_add_u32 v35, v35, 2, s67\n"
        "s_waitcnt lgkmcnt(15)\n"
        "v_lshl_add_u32 v48, v48, 2, s67\n"
        "v_mfma_f32_16x16x32_bf16 v[100:103], a[40:43], a[216:219], v[100:103]\n"
        "s_add_u32 m0, s41, 0\n"
        "ds_read_b32 v35, v35\n"
        "ds_read_b32 v48, v48\n"
        "v_lshl_add_u32 v49, v49, 2, s67\n"
        "v_mfma_f32_16x16x32_bf16 v[100:103], a[44:47], a[220:223], v[100:103]\n"
        "buffer_load_dwordx4 v242, s[8:11], s47 offen lds\n"
        "ds_read_b32 v49, v49\n"
        "v_mfma_f32_16x16x32_bf16 v[112:115], a[48:51], a[236:239], 0\n"
        "ds_read_b128 a[236:239], v151 offset:13952\n"
        "ds_read_b128 v[36:39], v233 offset:8192\n"
        "v_lshl_add_u32 v50, v50, 2, s67\n"
        "v_mfma_f32_16x16x32_bf16 v[112:115], a[52:55], a[240:243], v[112:115]\n"
        "ds_read_b128 a[240:243], v150 offset:14080\n"
        "ds_read_b32 v50, v50\n"
        "v_cvt_pk_bf16_f32 v198, v100, v101\n"
        "v_mfma_f32_16x16x32_bf16 v[112:115], a[56:59], a[244:247], v[112:115]\n"
        "ds_read_b128 a[244:247], v151 offset:14080\n"
        "v_cvt_pk_bf16_f32 v199, v102, v103\n"
        "v_lshl_add_u32 v51, v51, 2, s67\n"
        "v_mfma_f32_16x16x32_bf16 v[112:115], a[60:63], a[248:251], v[112:115]\n"
        "s_add_u32 m0, s41, 1024\n"
        "ds_read_b128 a[248:251], v150 offset:0\n"
        "ds_read_b32 v51, v51\n"
        "s_waitcnt lgkmcnt(15)\n"
        "v_lshl_add_u32 v4, v4, 2, s67\n"
        "v_mfma_f32_16x16x32_bf16 v[112:115], a[64:67], a[216:219], v[112:115]\n"
        "buffer_load_dwordx4 v243, s[8:11], s47 offen lds\n"
        "ds_read_b128 a[216:219], v151 offset:0\n"
        "v_mfma_f32_16x16x32_bf16 v[112:115], a[68:71], a[220:223], v[112:115]\n"
        "ds_read_b128 v[52:55], v233 offset:9216\n"
        "ds_read_b128 a[220:223], v150 offset:128\n"
        "v_lshl_add_u32 v5, v5, 2, s67\n"
        "v_mfma_f32_16x16x32_bf16 v[96:99], a[0:3], a[224:227], 0\n"
        "ds_read_b32 v4, v4\n"
        "ds_read_b32 v5, v5\n"
        "v_lshl_add_u32 v6, v6, 2, s67\n"
        "v_mfma_f32_16x16x32_bf16 v[96:99], a[4:7], a[228:231], v[96:99]\n"
        "ds_read_b32 v6, v6\n"
        "v_cvt_pk_bf16_f32 v200, v112, v113\n"
        "v_cvt_pk_bf16_f32 v201, v114, v115\n"
        "v_mfma_f32_16x16x32_bf16 v[96:99], a[8:11], a[232:235], v[96:99]\n"
        "s_waitcnt lgkmcnt(12)\n"
        "s_add_u32 m0, s41, 2048\n"
        "v_cvt_pk_bf16_f32 v206, v112, v113\n"
        "v_cvt_pk_bf16_f32 v207, v114, v115\n"
        "v_lshl_add_u32 v7, v7, 2, s67\n"
        "v_mfma_f32_16x16x32_bf16 v[96:99], a[12:15], a[236:239], v[96:99]\n"
        "s_waitcnt lgkmcnt(10)\n"
        "buffer_load_dwordx4 v244, s[8:11], s47 offen lds\n"
        "ds_read_b32 v7, v7\n"
        "v_mfma_f32_16x16x32_bf16 v[96:99], a[16:19], a[240:243], v[96:99]\n"
        "s_waitcnt lgkmcnt(9)\n"
        "ds_read_b128 v[8:11], v234 offset:0\n"
        "v_lshl_add_u32 v20, v20, 2, s67\n"
        "v_lshl_add_u32 v21, v21, 2, s67\n"
        "v_mfma_f32_16x16x32_bf16 v[96:99], a[20:23], a[244:247], v[96:99]\n"
        "ds_read_b32 v20, v20\n"
        "ds_read_b32 v21, v21\n"
        "v_lshl_add_u32 v22, v22, 2, s67\n"
        "v_mfma_f32_16x16x32_bf16 v[100:103], a[24:27], a[224:227], 0\n"
        "s_waitcnt vmcnt(5)\n"
        "s_barrier\n"
        "ds_read_b32 v22, v22\n"
        "v_lshl_add_u32 v23, v23, 2, s67\n"
        "v_lshl_add_u32 v36, v36, 2, s67\n"
        "v_mfma_f32_16x16x32_bf16 v[100:103], a[28:31], a[228:231], v[100:103]\n"
        "s_add_u32 m0, s41, 12288\n"
        "ds_read_b64_tr_b16 a[84:85], v131 offset:0\n"
        "ds_read_b64_tr_b16 a[86:87], v131 offset:1536\n"
        "ds_read_b64_tr_b16 a[88:89], v131 offset:12288\n"
        "v_mfma_f32_16x16x32_bf16 v[100:103], a[32:35], a[232:235], v[100:103]\n"
        "ds_read_b64_tr_b16 a[90:91], v131 offset:13824\n"
        "ds_read_b64_tr_b16 a[92:93], v131 offset:24576\n"
        "ds_read_b64_tr_b16 a[94:95], v131 offset:26112\n"
        "s_add_u32 s47, s47, 6291456\n"
        "v_mfma_f32_16x16x32_bf16 v[100:103], a[36:39], a[236:239], v[100:103]\n"
        "ds_read_b64_tr_b16 a[108:109], v132 offset:0\n"
        "ds_read_b64_tr_b16 a[110:111], v132 offset:1536\n"
        "ds_read_b64_tr_b16 a[112:113], v132 offset:12288\n"
        "s_sub_u32 s31, s30, s28\n"
        "v_mfma_f32_16x16x32_bf16 v[100:103], a[40:43], a[240:243], v[100:103]\n"
        "ds_read_b64_tr_b16 a[114:115], v132 offset:13824\n"
        "ds_read_b64_tr_b16 a[116:117], v132 offset:24576\n"
        "ds_read_b64_tr_b16 a[118:119], v132 offset:26112\n"
        "s_sub_u32 s45, s28, s29\n"
        "v_mfma_f32_16x16x32_bf16 v[100:103], a[44:47], a[244:247], v[100:103]\n"
        "ds_read_b64_tr_b16 a[132:133], v133 offset:0\n"
        "ds_read_b64_tr_b16 a[134:135], v133 offset:1536\n"
        "ds_read_b64_tr_b16 a[136:137], v133 offset:12288\n"
        "s_add_u32 s28, s28, s31\n"
        "v_mfma_f32_16x16x32_bf16 v[112:115], a[48:51], a[224:227], 0\n"
        "ds_read_b64_tr_b16 a[138:139], v133 offset:13824\n"
        "ds_read_b64_tr_b16 a[140:141], v133 offset:24576\n"
        "ds_read_b64_tr_b16 a[142:143], v133 offset:26112\n"
        "s_mov_b32 s30, s29\n"
        "v_mfma_f32_16x16x32_bf16 v[112:115], a[52:55], a[228:231], v[112:115]\n"
        "buffer_load_dwordx4 v242, s[8:11], s35 offen lds\n"
        "ds_read_b128 a[224:227], v151 offset:128\n"
        "s_add_u32 m0, s41, 13312\n"
        "v_mfma_f32_16x16x32_bf16 v[112:115], a[56:59], a[232:235], v[112:115]\n"
        "ds_read_b128 a[228:231], v150 offset:256\n"
        "buffer_load_dwordx4 v243, s[8:11], s35 offen lds\n"
        "s_add_u32 s29, s29, s45\n"
        "v_mfma_f32_16x16x32_bf16 v[112:115], a[60:63], a[236:239], v[112:115]\n"
        "ds_read_b128 a[232:235], v151 offset:256\n"
        "ds_read_b128 v[24:27], v234 offset:1024\n"
        "v_cvt_pk_bf16_f32 v216, v96, v97\n"
        "v_mfma_f32_16x16x32_bf16 v[112:115], a[64:67], a[240:243], v[112:115]\n"
        "ds_read_b32 v23, v23\n"
        "ds_read_b128 v[40:43], v234 offset:8192\n"
        "v_cvt_pk_bf16_f32 v217, v98, v99\n"
        "v_mfma_f32_16x16x32_bf16 v[112:115], a[68:71], a[244:247], v[112:115]\n"
        "s_waitcnt lgkmcnt(15)\n"
        "s_add_u32 m0, s41, 14336\n"
        "ds_read_b32 v36, v36\n"
        "ds_read_b128 a[236:239], v150 offset:1536\n"
        "v_cvt_pk_bf16_f32 v218, v100, v101\n"
        "v_mfma_f32_16x16x32_bf16 v[96:99], a[72:75], a[248:251], 0\n"
        "s_waitcnt lgkmcnt(15)\n"
        "buffer_load_dwordx4 v244, s[8:11], s35 offen lds\n"
        "ds_read_b128 a[240:243], v151 offset:1536\n"
        "v_mfma_f32_16x16x32_bf16 v[96:99], a[76:79], a[216:219], v[96:99]\n"
        "s_waitcnt lgkmcnt(15)\n"
        "ds_read_b128 a[244:247], v150 offset:1664\n"
        "ds_read_b128 v[56:59], v234 offset:9216\n"
        "v_cvt_pk_bf16_f32 v219, v102, v103\n"
        "v_mfma_f32_16x16x32_bf16 v[96:99], a[80:83], a[220:223], v[96:99]\n"
        "s_waitcnt lgkmcnt(10)\n"
        "v_cvt_pk_bf16_f32 v220, v112, v113\n"
        "v_cvt_pk_bf16_f32 v221, v114, v115\n"
        "v_cvt_pk_bf16_f32 v226, v112, v113\n"
        "v_mfma_f32_16x16x32_bf16 v[96:99], a[84:87], a[224:227], v[96:99]\n"
        "s_waitcnt lgkmcnt(9)\n"
        "s_add_u32 m0, s40, 36864\n"
        "v_cvt_pk_bf16_f32 v227, v114, v115\n"
        "v_lshl_add_u32 v37, v37, 2, s67\n"
        "v_lshl_add_u32 v38, v38, 2, s67\n"
        "v_mfma_f32_16x16x32_bf16 v[96:99], a[88:91], a[228:231], v[96:99]\n"
        "s_waitcnt lgkmcnt(8)\n"
        "s_add_u32 s64, s65, 768\n"
        "ds_read_b32 v37, v37\n"
        "ds_read_b32 v38, v38\n"
        "v_lshl_add_u32 v39, v39, 2, s67\n"
        "v_mfma_f32_16x16x32_bf16 v[96:99], a[92:95], a[232:235], v[96:99]\n"
        "buffer_load_dwordx4 v128, s[20:23], s64 offen lds\n"
        "ds_read_b32 v39, v39\n"
        "v_lshl_add_u32 v52, v52, 2, s67\n"
        "v_mfma_f32_16x16x32_bf16 v[100:103], a[96:99], a[248:251], 0\n"
        "ds_read_b32 v52, v52\n"
        "ds_read_b128 v[12:15], v235 offset:0\n"
        "v_lshl_add_u32 v53, v53, 2, s67\n"
        "v_mfma_f32_16x16x32_bf16 v[100:103], a[100:103], a[216:219], v[100:103]\n"
        "ds_read_b32 v53, v53\n"
        "v_cvt_pk_bf16_f32 v152, v96, v97\n"
        "v_cvt_pk_bf16_f32 v153, v98, v99\n"
        "v_mfma_f32_16x16x32_bf16 v[100:103], a[104:107], a[220:223], v[100:103]\n"
        "v_lshl_add_u32 v54, v54, 2, s67\n"
        "v_lshl_add_u32 v55, v55, 2, s67\n"
        "v_lshl_add_u32 v8, v8, 2, s67\n"
        "v_mfma_f32_16x16x32_bf16 v[100:103], a[108:111], a[224:227], v[100:103]\n"
        "ds_read_b32 v54, v54\n"
        "ds_read_b32 v55, v55\n"
        "v_lshl_add_u32 v9, v9, 2, s67\n"
        "v_mfma_f32_16x16x32_bf16 v[100:103], a[112:115], a[228:231], v[100:103]\n"
        "buffer_load_dwordx4 v129, s[20:23], s64 offen offset:1024 lds\n"
        "ds_read_b32 v8, v8\n"
        "v_lshl_add_u32 v10, v10, 2, s67\n"
        "v_mfma_f32_16x16x32_bf16 v[100:103], a[116:119], a[232:235], v[100:103]\n"
        "ds_read_b32 v9, v9\n"
        "ds_read_b32 v10, v10\n"
        "v_lshl_add_u32 v11, v11, 2, s67\n"
        "v_mfma_f32_16x16x32_bf16 v[112:115], a[120:123], a[248:251], 0\n"
        "ds_read_b128 a[248:251], v151 offset:1664\n"
        "ds_read_b32 v11, v11\n"
        "s_waitcnt lgkmcnt(15)\n"
        "v_lshl_add_u32 v24, v24, 2, s67\n"
        "v_mfma_f32_16x16x32_bf16 v[112:115], a[124:127], a[216:219], v[112:115]\n"
        "ds_read_b128 a[216:219], v150 offset:1792\n"
        "ds_read_b128 v[28:31], v235 offset:1024\n"
        "v_cvt_pk_bf16_f32 v154, v100, v101\n"
        "v_mfma_f32_16x16x32_bf16 v[112:115], a[128:131], a[220:223], v[112:115]\n"
        "ds_read_b128 a[220:223], v151 offset:1792\n"
        "ds_read_b32 v24, v24\n"
        "v_cvt_pk_bf16_f32 v155, v102, v103\n"
        "v_mfma_f32_16x16x32_bf16 v[112:115], a[132:135], a[224:227], v[112:115]\n"
        "buffer_load_dwordx4 v130, s[20:23], s64 offen offset:2048 lds\n"
        "ds_read_b128 a[224:227], v150 offset:12288\n"
        "v_lshl_add_u32 v25, v25, 2, s67\n"
        "v_mfma_f32_16x16x32_bf16 v[112:115], a[136:139], a[228:231], v[112:115]\n"
        "ds_read_b32 v25, v25\n"
        "ds_read_b128 v[44:47], v235 offset:8192\n"
        "v_lshl_add_u32 v26, v26, 2, s67\n"
        "v_mfma_f32_16x16x32_bf16 v[112:115], a[140:143], a[232:235], v[112:115]\n"
        "ds_read_b32 v26, v26\n"
        "ds_read_b128 a[228:231], v151 offset:12288\n"
        "v_lshl_add_u32 v27, v27, 2, s67\n"
        "v_mfma_f32_16x16x32_bf16 v[96:99], a[72:75], a[236:239], 0\n"
        "s_add_u32 m0, s40, 49152\n"
        "ds_read_b32 v27, v27\n"
        "ds_read_b128 a[232:235], v150 offset:12416\n"
        "v_lshl_add_u32 v40, v40, 2, s67\n"
        "v_mfma_f32_16x16x32_bf16 v[96:99], a[76:79], a[240:243], v[96:99]\n"
        "s_waitcnt lgkmcnt(15)\n"
        "s_add_u32 s64, s65, 37632\n"
        "ds_read_b32 v40, v40\n"
        "v_cvt_pk_bf16_f32 v168, v112, v113\n"
        "v_cvt_pk_bf16_f32 v169, v114, v115\n"
        "v_mfma_f32_16x16x32_bf16 v[96:99], a[80:83], a[244:247], v[96:99]\n"
        "s_waitcnt lgkmcnt(13)\n"
        "buffer_load_dwordx4 v128, s[20:23], s64 offen lds\n"
        "v_lshl_add_u32 v41, v41, 2, s67\n"
        "v_lshl_add_u32 v42, v42, 2, s67\n"
        "v_mfma_f32_16x16x32_bf16 v[96:99], a[84:87], a[248:251], v[96:99]\n"
        "s_waitcnt lgkmcnt(11)\n"
        "ds_read_b32 v41, v41\n"
        "ds_read_b32 v42, v42\n"
        "v_lshl_add_u32 v43, v43, 2, s67\n"
        "v_mfma_f32_16x16x32_bf16 v[96:99], a[88:91], a[216:219], v[96:99]\n"
        "s_waitcnt lgkmcnt(11)\n"
        "ds_read_b32 v43, v43\n"
        "ds_read_b128 v[60:63], v235 offset:9216\n"
        "v_lshl_add_u32 v56, v56, 2, s67\n"
        "v_mfma_f32_16x16x32_bf16 v[96:99], a[92:95], a[220:223], v[96:99]\n"
        "ds_read_b32 v56, v56\n"
        "v_lshl_add_u32 v57, v57, 2, s67\n"
        "v_lshl_add_u32 v58, v58, 2, s67\n"
        "v_mfma_f32_16x16x32_bf16 v[100:103], a[96:99], a[236:239], 0\n"
        "ds_read_b32 v57, v57\n"
        "ds_read_b32 v58, v58\n"
        "v_lshl_add_u32 v59, v59, 2, s67\n"
        "v_mfma_f32_16x16x32_bf16 v[100:103], a[100:103], a[240:243], v[100:103]\n"
        "buffer_load_dwordx4 v129, s[20:23], s64 offen offset:1024 lds\n"
        "ds_read_b32 v59, v59\n"
        "v_cvt_pk_bf16_f32 v172, v96, v97\n"
        "v_mfma_f32_16x16x32_bf16 v[100:103], a[104:107], a[244:247], v[100:103]\n"
        "v_cvt_pk_bf16_f32 v173, v98, v99\n"
        "v_lshl_add_u32 v12, v12, 2, s67\n"
        "v_lshl_add_u32 v13, v13, 2, s67\n"
        "v_mfma_f32_16x16x32_bf16 v[100:103], a[108:111], a[248:251], v[100:103]\n"
        "ds_read_b32 v12, v12\n"
        "ds_read_b32 v13, v13\n"
        "v_lshl_add_u32 v14, v14, 2, s67\n"
        "v_mfma_f32_16x16x32_bf16 v[100:103], a[112:115], a[216:219], v[100:103]\n"
        "ds_read_b32 v14, v14\n"
        "v_lshl_add_u32 v15, v15, 2, s67\n"
        "v_lshl_add_u32 v28, v28, 2, s67\n"
        "v_mfma_f32_16x16x32_bf16 v[100:103], a[116:119], a[220:223], v[100:103]\n"
        "ds_read_b32 v15, v15\n"
        "ds_read_b32 v28, v28\n"
        "v_lshl_add_u32 v29, v29, 2, s67\n"
        "v_mfma_f32_16x16x32_bf16 v[112:115], a[120:123], a[236:239], 0\n"
        "ds_read_b128 a[236:239], v151 offset:12416\n"
        "buffer_load_dwordx4 v130, s[20:23], s64 offen offset:2048 lds\n"
        "v_lshl_add_u32 v30, v30, 2, s67\n"
        "v_mfma_f32_16x16x32_bf16 v[112:115], a[124:127], a[240:243], v[112:115]\n"
        "ds_read_b128 a[240:243], v150 offset:12544\n"
        "ds_read_b32 v29, v29\n"
        "v_cvt_pk_bf16_f32 v174, v100, v101\n"
        "v_mfma_f32_16x16x32_bf16 v[112:115], a[128:131], a[244:247], v[112:115]\n"
        "ds_read_b128 a[244:247], v151 offset:12544\n"
        "ds_read_b32 v30, v30\n"
        "v_cvt_pk_bf16_f32 v175, v102, v103\n"
        "v_mfma_f32_16x16x32_bf16 v[112:115], a[132:135], a[248:251], v[112:115]\n"
        "s_add_u32 m0, s40, 61440\n"
        "ds_read_b128 a[248:251], v150 offset:13824\n"
        "v_lshl_add_u32 v31, v31, 2, s67\n"
        "s_waitcnt lgkmcnt(15)\n"
        "v_lshl_add_u32 v44, v44, 2, s67\n"
        "v_mfma_f32_16x16x32_bf16 v[112:115], a[136:139], a[216:219], v[112:115]\n"
        "s_add_u32 s64, s65, 74496\n"
        "ds_read_b32 v31, v31\n"
        "ds_read_b128 a[216:219], v151 offset:13824\n"
        "v_lshl_add_u32 v45, v45, 2, s67\n"
        "v_mfma_f32_16x16x32_bf16 v[112:115], a[140:143], a[220:223], v[112:115]\n"
        "buffer_load_dwordx4 v128, s[20:23], s64 offen lds\n"
        "ds_read_b128 a[220:223], v150 offset:13952\n"
        "v_lshl_add_u32 v46, v46, 2, s67\n"
        "v_mfma_f32_16x16x32_bf16 v[96:99], a[72:75], a[224:227], 0\n"
        "ds_read_b32 v44, v44\n"
        "ds_read_b32 v45, v45\n"
        "v_lshl_add_u32 v47, v47, 2, s67\n"
        "v_mfma_f32_16x16x32_bf16 v[96:99], a[76:79], a[228:231], v[96:99]\n"
        "ds_read_b32 v46, v46\n"
        "ds_read_b32 v47, v47\n"
        "v_cvt_pk_bf16_f32 v190, v112, v113\n"
        "v_mfma_f32_16x16x32_bf16 v[96:99], a[80:83], a[232:235], v[96:99]\n"
        "s_waitcnt lgkmcnt(12)\n"
        "v_cvt_pk_bf16_f32 v191, v114, v115\n"
        "v_lshl_add_u32 v60, v60, 2, s67\n"
        "v_lshl_add_u32 v61, v61, 2, s67\n"
        "v_mfma_f32_16x16x32_bf16 v[96:99], a[84:87], a[236:239], v[96:99]\n"
        "s_waitcnt lgkmcnt(11)\n"
        "ds_read_b32 v60, v60\n"
        "ds_read_b32 v61, v61\n"
        "v_lshl_add_u32 v62, v62, 2, s67\n"
        "v_mfma_f32_16x16x32_bf16 v[96:99], a[88:91], a[240:243], v[96:99]\n"
        "s_waitcnt lgkmcnt(11)\n"
        "buffer_load_dwordx4 v129, s[20:23], s64 offen offset:1024 lds\n"
        "ds_read_b32 v62, v62\n"
        "v_lshl_add_u32 v63, v63, 2, s67\n"
        "v_mfma_f32_16x16x32_bf16 v[96:99], a[92:95], a[244:247], v[96:99]\n"
        "ds_read_b32 v63, v63\n"
        "v_add_u32 v240, s31, v240\n"
        "v_add_u32 v241, s31, v241\n"
        "v_mfma_f32_16x16x32_bf16 v[100:103], a[96:99], a[224:227], 0\n"
        "v_add_u32 v245, s45, v245\n"
        "v_add_u32 v246, s45, v246\n"
        "v_add_u32 v247, s45, v247\n"
        "v_mfma_f32_16x16x32_bf16 v[100:103], a[100:103], a[228:231], v[100:103]\n"
        "v_cvt_pk_bf16_f32 v192, v96, v97\n"
        "v_cvt_pk_bf16_f32 v193, v98, v99\n"
        "v_mfma_f32_16x16x32_bf16 v[100:103], a[104:107], a[232:235], v[100:103]\n"
        "v_mfma_f32_16x16x32_bf16 v[100:103], a[108:111], a[236:239], v[100:103]\n"
        "buffer_load_dwordx4 v130, s[20:23], s64 offen offset:2048 lds\n"
        "v_mfma_f32_16x16x32_bf16 v[100:103], a[112:115], a[240:243], v[100:103]\n"
        "v_mfma_f32_16x16x32_bf16 v[100:103], a[116:119], a[244:247], v[100:103]\n"
        "v_mfma_f32_16x16x32_bf16 v[112:115], a[120:123], a[224:227], 0\n"
        "ds_read_b128 a[224:227], v151 offset:13952\n"
        "v_mfma_f32_16x16x32_bf16 v[112:115], a[124:127], a[228:231], v[112:115]\n"
        "ds_read_b128 a[228:231], v150 offset:14080\n"
        "v_mfma_f32_16x16x32_bf16 v[112:115], a[128:131], a[232:235], v[112:115]\n"
        "ds_read_b128 a[232:235], v151 offset:14080\n"
        "v_mfma_f32_16x16x32_bf16 v[112:115], a[132:135], a[236:239], v[112:115]\n"
        "v_mfma_f32_16x16x32_bf16 v[112:115], a[136:139], a[240:243], v[112:115]\n"
        "v_cvt_pk_bf16_f32 v194, v100, v101\n"
        "v_cvt_pk_bf16_f32 v195, v102, v103\n"
        "v_mfma_f32_16x16x32_bf16 v[112:115], a[140:143], a[244:247], v[112:115]\n"
        "s_waitcnt lgkmcnt(14)\n"
        "s_waitcnt vmcnt(15)\n"
        "s_barrier\n"
        "ds_read_b128 v[162:165], v251 offset:0\n"
        "ds_read_b128 v[182:185], v251 offset:512\n"
        "v_mfma_f32_16x16x32_bf16 v[96:99], a[72:75], a[248:251], 0\n"
        "s_waitcnt lgkmcnt(14)\n"
        "s_add_u32 m0, s40, 0\n"
        "v_mfma_f32_16x16x32_bf16 v[96:99], a[76:79], a[216:219], v[96:99]\n"
        "s_waitcnt lgkmcnt(13)\n"
        "s_add_u32 s64, s65, 111360\n"
        "v_mfma_f32_16x16x32_bf16 v[96:99], a[80:83], a[220:223], v[96:99]\n"
        "s_waitcnt lgkmcnt(4)\n"
        "buffer_load_dwordx4 v128, s[20:23], s64 offen lds\n"
        "ds_read_b128 v[202:205], v251 offset:4096\n"
        "v_cvt_pk_bf16_f32 v208, v112, v113\n"
        "v_mfma_f32_16x16x32_bf16 v[96:99], a[84:87], a[224:227], v[96:99]\n"
        "s_waitcnt lgkmcnt(4)\n"
        "v_cvt_pk_bf16_f32 v209, v114, v115\n"
        "v_mfma_f32_16x16x32_bf16 v[96:99], a[88:91], a[228:231], v[96:99]\n"
        "s_waitcnt lgkmcnt(3)\n"
        "v_mfma_f32_16x16x32_bf16 v[96:99], a[92:95], a[232:235], v[96:99]\n"
        "v_mfma_f32_16x16x32_bf16 v[100:103], a[96:99], a[248:251], 0\n"
        "ds_read_b128 v[222:225], v251 offset:4608\n"
        "v_mfma_f32_16x16x32_bf16 v[100:103], a[100:103], a[216:219], v[100:103]\n"
        "buffer_load_dwordx4 v129, s[20:23], s64 offen offset:1024 lds\n"
        "v_mfma_f32_16x16x32_bf16 v[100:103], a[104:107], a[220:223], v[100:103]\n"
        "s_waitcnt lgkmcnt(3)\n"
        "v_pk_min_u16 v162, v162, s26\n"
        "v_pk_min_u16 v163, v163, s26\n"
        "v_pk_min_u16 v164, v164, s26\n"
        "v_mfma_f32_16x16x32_bf16 v[100:103], a[108:111], a[224:227], v[100:103]\n"
        "v_cvt_pk_bf16_f32 v212, v96, v97\n"
        "v_cvt_pk_bf16_f32 v213, v98, v99\n"
        "v_pk_min_u16 v165, v165, s26\n"
        "v_mfma_f32_16x16x32_bf16 v[100:103], a[112:115], a[228:231], v[100:103]\n"
        "v_mfma_f32_16x16x32_bf16 v[100:103], a[116:119], a[232:235], v[100:103]\n"
        "v_mfma_f32_16x16x32_bf16 v[112:115], a[120:123], a[248:251], 0\n"
        "buffer_load_dwordx4 v130, s[20:23], s64 offen offset:2048 lds\n"
        "s_waitcnt lgkmcnt(2)\n"
        "v_pk_min_u16 v182, v182, s26\n"
        "v_pk_min_u16 v183, v183, s26\n"
        "v_mfma_f32_16x16x32_bf16 v[112:115], a[124:127], a[216:219], v[112:115]\n"
        "v_pk_min_u16 v184, v184, s26\n"
        "v_pk_min_u16 v185, v185, s26\n"
        "v_mfma_f32_16x16x32_bf16 v[112:115], a[128:131], a[220:223], v[112:115]\n"
        "v_cvt_pk_bf16_f32 v214, v100, v101\n"
        "v_cvt_pk_bf16_f32 v215, v102, v103\n"
        "v_mfma_f32_16x16x32_bf16 v[112:115], a[132:135], a[224:227], v[112:115]\n"
        "s_add_u32 m0, s40, 12288\n"
        "v_mfma_f32_16x16x32_bf16 v[112:115], a[136:139], a[228:231], v[112:115]\n"
        "s_add_u32 s64, s65, 148224\n"
        "s_waitcnt lgkmcnt(1)\n"
        "v_pk_min_u16 v202, v202, s26\n"
        "v_pk_min_u16 v203, v203, s26\n"
        "v_pk_min_u16 v204, v204, s26\n"
        "v_mfma_f32_16x16x32_bf16 v[112:115], a[140:143], a[232:235], v[112:115]\n"
        "buffer_load_dwordx4 v128, s[20:23], s64 offen lds\n"
        "v_pk_min_u16 v205, v205, s26\n"
        "v_mfma_f32_16x16x32_bf16 v[64:67], v[152:155], v[156:159], v[0:3]\n"
        "v_mfma_f32_16x16x32_bf16 v[64:67], v[168:171], v[160:163], v[64:67]\n"
        "v_mfma_f32_16x16x32_bf16 v[68:71], v[172:175], v[156:159], v[4:7]\n"
        "s_waitcnt lgkmcnt(0)\n"
        "v_pk_min_u16 v222, v222, s26\n"
        "v_pk_min_u16 v223, v223, s26\n"
        "v_pk_min_u16 v224, v224, s26\n"
        "v_mfma_f32_16x16x32_bf16 v[68:71], v[188:191], v[164:167], v[68:71]\n"
        "ds_read_b128 v[162:165], v252 offset:0\n"
        "v_cvt_pk_bf16_f32 v230, v112, v113\n"
        "v_cvt_pk_bf16_f32 v231, v114, v115\n"
        "v_mfma_f32_16x16x32_bf16 v[80:83], v[152:155], v[176:179], v[16:19]\n"
        "buffer_load_dwordx4 v129, s[20:23], s64 offen offset:1024 lds\n"
        "v_pk_min_u16 v225, v225, s26\n"
        "v_max3_f32 v232, v64, v65, v66\n"
        "v_mfma_f32_16x16x32_bf16 v[80:83], v[168:171], v[180:183], v[80:83]\n"
        "v_max3_f32 v232, v232, v67, v68\n"
        "v_max3_f32 v232, v232, v69, v70\n"
        "v_mfma_f32_16x16x32_bf16 v[84:87], v[172:175], v[176:179], v[20:23]\n"
        "v_mfma_f32_16x16x32_bf16 v[84:87], v[188:191], v[184:187], v[84:87]\n"
        "ds_read_b128 v[182:185], v252 offset:512\n"
        "v_mfma_f32_16x16x32_bf16 v[96:99], v[152:155], v[196:199], v[32:35]\n"
        "v_mfma_f32_16x16x32_bf16 v[96:99], v[168:171], v[200:203], v[96:99]\n"
        "buffer_load_dwordx4 v130, s[20:23], s64 offen offset:2048 lds\n"
        "v_max3_f32 v233, v80, v81, v82\n"
        "v_mfma_f32_16x16x32_bf16 v[100:103], v[172:175], v[196:199], v[36:39]\n"
        "v_mfma_f32_16x16x32_bf16 v[100:103], v[188:191], v[204:207], v[100:103]\n"
        "ds_read_b128 v[202:205], v252 offset:4096\n"
        "s_waitcnt vmcnt(15)\n"
        "s_barrier\n"
        "v_max3_f32 v233, v233, v83, v84\n"
        "v_max3_f32 v233, v233, v85, v86\n"
        "v_mfma_f32_16x16x32_bf16 v[112:115], v[152:155], v[216:219], v[48:51]\n"
        "s_add_u32 m0, s40, 24576\n"
        "ds_read_b128 a[236:239], v240 offset:0\n"
        "s_waitcnt lgkmcnt(3)\n"
        "v_pk_min_u16 v162, v162, s26\n"
        "v_pk_min_u16 v163, v163, s26\n"
        "v_mfma_f32_16x16x32_bf16 v[112:115], v[168:171], v[220:223], v[112:115]\n"
        "s_add_u32 s64, s65, 185088\n"
        "ds_read_b128 a[240:243], v241 offset:0\n"
        "v_pk_min_u16 v164, v164, s26\n"
        "v_pk_min_u16 v165, v165, s26\n"
        "v_mfma_f32_16x16x32_bf16 v[116:119], v[172:175], v[216:219], v[52:55]\n"
        "buffer_load_dwordx4 v128, s[20:23], s64 offen lds\n"
        "ds_read_b128 a[244:247], v240 offset:128\n"
        "v_max3_f32 v234, v96, v97, v98\n"
        "v_mfma_f32_16x16x32_bf16 v[116:119], v[188:191], v[224:227], v[116:119]\n"
        "ds_read_b128 v[222:225], v252 offset:4608\n"
        "ds_read_b128 a[248:251], v241 offset:128\n"
        "v_max3_f32 v234, v234, v99, v100\n"
        "v_mfma_f32_16x16x32_bf16 v[72:75], v[192:195], v[156:159], v[8:11]\n"
        "ds_read_b128 a[216:219], v240 offset:256\n"
        "s_waitcnt lgkmcnt(7)\n"
        "v_pk_min_u16 v182, v182, s26\n"
        "v_pk_min_u16 v183, v183, s26\n"
        "v_mfma_f32_16x16x32_bf16 v[72:75], v[208:211], v[160:163], v[72:75]\n"
        "ds_read_b128 a[220:223], v241 offset:256\n"
        "v_pk_min_u16 v184, v184, s26\n"
        "v_pk_min_u16 v185, v185, s26\n"
        "v_mfma_f32_16x16x32_bf16 v[76:79], v[212:215], v[156:159], v[12:15]\n"
        "v_max3_f32 v234, v234, v101, v102\n"
        "v_max3_f32 v235, v112, v113, v114\n"
        "v_max3_f32 v235, v235, v115, v116\n"
        "v_mfma_f32_16x16x32_bf16 v[76:79], v[228:231], v[164:167], v[76:79]\n"
        "buffer_load_dwordx4 v129, s[20:23], s64 offen offset:1024 lds\n"
        "v_max3_f32 v232, v232, v71, v72\n"
        "v_max3_f32 v232, v232, v73, v74\n"
        "v_mfma_f32_16x16x32_bf16 v[88:91], v[192:195], v[176:179], v[24:27]\n"
        "s_waitcnt lgkmcnt(7)\n"
        "v_pk_min_u16 v202, v202, s26\n"
        "v_pk_min_u16 v203, v203, s26\n"
        "v_pk_min_u16 v204, v204, s26\n"
        "v_mfma_f32_16x16x32_bf16 v[88:91], v[208:211], v[180:183], v[88:91]\n"
        "v_pk_min_u16 v205, v205, s26\n"
        "v_max3_f32 v232, v232, v75, v76\n"
        "v_max3_f32 v232, v232, v77, v78\n"
        "v_mfma_f32_16x16x32_bf16 v[92:95], v[212:215], v[176:179], v[28:31]\n"
        "v_max_f32 v232, v232, v79\n"
        "v_max3_f32 v235, v235, v117, v118\n"
        "v_xor_b32 v251, 0x2000, v251\n"
        "v_mfma_f32_16x16x32_bf16 v[92:95], v[228:231], v[184:187], v[92:95]\n"
        "v_max3_f32 v233, v233, v87, v88\n"
        "v_max3_f32 v233, v233, v89, v90\n"
        "v_xor_b32 v252, 0x2000, v252\n"
        "v_mfma_f32_16x16x32_bf16 v[104:107], v[192:195], v[196:199], v[40:43]\n"
        "buffer_load_dwordx4 v130, s[20:23], s64 offen offset:2048 lds\n"
        "s_waitcnt lgkmcnt(3)\n"
        "v_pk_min_u16 v222, v222, s26\n"
        "v_pk_min_u16 v223, v223, s26\n"
        "v_mfma_f32_16x16x32_bf16 v[104:107], v[208:211], v[200:203], v[104:107]\n"
        "v_pk_min_u16 v224, v224, s26\n"
        "v_pk_min_u16 v225, v225, s26\n"
        "v_max3_f32 v233, v233, v91, v92\n"
        "v_mfma_f32_16x16x32_bf16 v[108:111], v[212:215], v[196:199], v[44:47]\n"
        "s_waitcnt vmcnt(9)\n"
        "s_barrier\n"
        "v_max3_f32 v233, v233, v93, v94\n"
        "v_max_f32 v233, v233, v95\n"
        "v_mfma_f32_16x16x32_bf16 v[108:111], v[228:231], v[204:207], v[108:111]\n"
        "ds_read_b64_tr_b16 a[144:145], v131 offset:36864\n"
        "ds_read_b64_tr_b16 a[146:147], v131 offset:38400\n"
        "v_max3_f32 v234, v234, v103, v104\n"
        "v_mfma_f32_16x16x32_bf16 v[120:123], v[192:195], v[216:219], v[56:59]\n"
        "s_mov_b32 m0, s48\n"
        "s_xor_b32 s48, s48, 8192\n"
        "ds_read_b64_tr_b16 a[148:149], v131 offset:49152\n"
        "ds_read_b64_tr_b16 a[150:151], v131 offset:50688\n"
        "v_mfma_f32_16x16x32_bf16 v[120:123], v[208:211], v[220:223], v[120:123]\n"
        "buffer_load_dwordx4 v249, s[12:15], s36 offen offset:0 lds\n"
        "ds_read_b64_tr_b16 a[152:153], v131 offset:61440\n"
        "v_mfma_f32_16x16x32_bf16 v[124:127], v[212:215], v[216:219], v[60:63]\n"
        "ds_read_b64_tr_b16 a[154:155], v131 offset:62976\n"
        "ds_read_b64_tr_b16 a[168:169], v132 offset:36864\n"
        "v_xor_b32 v253, 32, v249\n"
        "v_mfma_f32_16x16x32_bf16 v[124:127], v[228:231], v[224:227], v[124:127]\n"
        "ds_read_b64_tr_b16 a[170:171], v132 offset:38400\n"
        "ds_read_b64_tr_b16 a[172:173], v132 offset:49152\n"
        "v_max3_f32 v234, v234, v105, v106\n"
        "v_mfma_f32_16x16x32_bf16 v[212:215], a[72:75], a[236:239], 0\n"
        "ds_read_b64_tr_b16 a[174:175], v132 offset:50688\n"
        "ds_read_b64_tr_b16 a[176:177], v132 offset:61440\n"
        "v_max3_f32 v234, v234, v107, v108\n"
        "v_mfma_f32_16x16x32_bf16 v[212:215], a[76:79], a[240:243], v[212:215]\n"
        "ds_read_b64_tr_b16 a[178:179], v132 offset:62976\n"
        "ds_read_b64_tr_b16 a[192:193], v133 offset:36864\n"
        "v_max3_f32 v234, v234, v109, v110\n"
        "v_mfma_f32_16x16x32_bf16 v[212:215], a[80:83], a[244:247], v[212:215]\n"
        "s_waitcnt lgkmcnt(15)\n"
        "buffer_load_dwordx4 v253, s[12:15], s36 offen offset:1024 lds\n"
        "s_add_u32 s36, s36, 2097152\n"
        "ds_read_b64_tr_b16 a[194:195], v133 offset:38400\n"
        "v_mfma_f32_16x16x32_bf16 v[212:215], a[84:87], a[248:251], v[212:215]\n"
        "s_waitcnt lgkmcnt(15)\n"
        "ds_read_b64_tr_b16 a[196:197], v133 offset:49152\n"
        "ds_read_b64_tr_b16 a[198:199], v133 offset:50688\n"
        "v_max_f32 v234, v234, v111\n"
        "v_mfma_f32_16x16x32_bf16 v[212:215], a[88:91], a[216:219], v[212:215]\n"
        "s_waitcnt lgkmcnt(15)\n"
        "ds_read_b64_tr_b16 a[200:201], v133 offset:61440\n"
        "ds_read_b64_tr_b16 a[202:203], v133 offset:62976\n"
        "v_max3_f32 v235, v235, v119, v120\n"
        "v_mfma_f32_16x16x32_bf16 v[212:215], a[92:95], a[220:223], v[212:215]\n"
        "v_permlane32_swap_b32 v232, v233\n"
        "v_max_f32 v232, v232, v233\n"
        "v_mfma_f32_16x16x32_bf16 v[216:219], a[96:99], a[236:239], 0\n"
        "v_mov_b32 v233, v232\n"
        "v_max3_f32 v235, v235, v121, v122\n"
        "v_max3_f32 v235, v235, v123, v124\n"
        "v_mfma_f32_16x16x32_bf16 v[216:219], a[100:103], a[240:243], v[216:219]\n"
        "v_permlane16_swap_b32 v232, v233\n"
        "v_max_f32 v232, v232, v233\n"
        "v_mfma_f32_16x16x32_bf16 v[216:219], a[104:107], a[244:247], v[216:219]\n"
        "v_mul_f32 v236, s25, v232\n"
        "v_mul_f32 v237, s25, v232\n"
        "v_max3_f32 v235, v235, v125, v126\n"
        "v_mfma_f32_16x16x32_bf16 v[216:219], a[108:111], a[248:251], v[216:219]\n"
        "v_permlane32_swap_b32 v236, v237\n"
        "v_max_f32 v235, v235, v127\n"
        "v_mfma_f32_16x16x32_bf16 v[216:219], a[112:115], a[216:219], v[216:219]\n"
        "v_cvt_pk_bf16_f32 v152, v212, v213\n"
        "v_permlane32_swap_b32 v234, v235\n"
        "v_mfma_f32_16x16x32_bf16 v[216:219], a[116:119], a[220:223], v[216:219]\n"
        "v_max_f32 v234, v234, v235\n"
        "v_mov_b32 v235, v234\n"
        "v_cvt_pk_bf16_f32 v153, v214, v215\n"
        "v_mfma_f32_16x16x32_bf16 v[162:165], a[120:123], a[236:239], 0\n"
        "v_permlane16_swap_b32 v234, v235\n"
        "v_max_f32 v234, v234, v235\n"
        "v_mfma_f32_16x16x32_bf16 v[162:165], a[124:127], a[240:243], v[162:165]\n"
        "v_mul_f32 v238, s25, v234\n"
        "v_mul_f32 v239, s25, v234\n"
        "v_cvt_pk_bf16_f32 v154, v216, v217\n"
        "v_mfma_f32_16x16x32_bf16 v[162:165], a[128:131], a[244:247], v[162:165]\n"
        "ds_read_b128 a[224:227], v240 offset:1536\n"
        "v_permlane32_swap_b32 v238, v239\n"
        "v_mfma_f32_16x16x32_bf16 v[162:165], a[132:135], a[248:251], v[162:165]\n"
        "ds_read_b128 a[228:231], v241 offset:1536\n"
        "v_cvt_pk_bf16_f32 v155, v218, v219\n"
        "v_fma_f32 v64, v64, s24, v236\n"
        "v_mfma_f32_16x16x32_bf16 v[162:165], a[136:139], a[216:219], v[162:165]\n"
        "ds_read_b128 a[232:235], v240 offset:1664\n"
        "v_exp_f32 v64, v64\n"
        "v_mfma_f32_16x16x32_bf16 v[162:165], a[140:143], a[220:223], v[162:165]\n"
        "v_fma_f32 v65, v65, s24, v236\n"
        "v_exp_f32 v65, v65\n"
        "v_mfma_f32_16x16x32_bf16 v[212:215], a[0:3], a[236:239], 0\n"
        "v_cvt_pk_bf16_f32 v64, v64, v65\n"
        "v_fma_f32 v66, v66, s24, v236\n"
        "v_fma_f32 v67, v67, s24, v236\n"
        "v_mfma_f32_16x16x32_bf16 v[212:215], a[4:7], a[240:243], v[212:215]\n"
        "v_exp_f32 v66, v66\n"
        "v_cvt_pk_bf16_f32 v168, v162, v163\n"
        "v_mfma_f32_16x16x32_bf16 v[212:215], a[8:11], a[244:247], v[212:215]\n"
        "v_exp_f32 v67, v67\n"
        "v_cvt_pk_bf16_f32 v169, v164, v165\n"
        "v_mfma_f32_16x16x32_bf16 v[212:215], a[12:15], a[248:251], v[212:215]\n"
        "v_cvt_pk_bf16_f32 v65, v66, v67\n"
        "v_fma_f32 v68, v68, s24, v236\n"
        "v_fma_f32 v69, v69, s24, v236\n"
        "v_mfma_f32_16x16x32_bf16 v[212:215], a[16:19], a[216:219], v[212:215]\n"
        "v_exp_f32 v68, v68\n"
        "v_fma_f32 v70, v70, s24, v236\n"
        "v_mfma_f32_16x16x32_bf16 v[212:215], a[20:23], a[220:223], v[212:215]\n"
        "v_exp_f32 v69, v69\n"
        "v_fma_f32 v71, v71, s24, v236\n"
        "v_mfma_f32_16x16x32_bf16 v[216:219], a[24:27], a[236:239], 0\n"
        "s_waitcnt lgkmcnt(3)\n"
        "s_waitcnt vmcnt(2)\n"
        "s_barrier\n"
        "v_exp_f32 v70, v70\n"
        "v_cvt_pk_bf16_f32 v66, v68, v69\n"
        "v_mfma_f32_16x16x32_bf16 v[216:219], a[28:31], a[240:243], v[216:219]\n"
        "ds_read_b64_tr_b16 a[156:157], v131 offset:0\n"
        "s_add_u32 m0, s65, 36864\n"
        "ds_read_b64_tr_b16 a[158:159], v131 offset:1536\n"
        "v_cvt_pk_bf16_f32 v156, v212, v213\n"
        "v_mfma_f32_16x16x32_bf16 v[216:219], a[32:35], a[244:247], v[216:219]\n"
        "s_add_u32 s64, s65, 36864\n"
        "ds_read_b64_tr_b16 a[160:161], v131 offset:12288\n"
        "ds_read_b64_tr_b16 a[162:163], v131 offset:13824\n"
        "v_cvt_pk_bf16_f32 v157, v214, v215\n"
        "v_mfma_f32_16x16x32_bf16 v[216:219], a[36:39], a[248:251], v[216:219]\n"
        "buffer_load_dwordx4 v142, s[52:55], s64 offen lds\n"
        "ds_read_b64_tr_b16 a[164:165], v131 offset:24576\n"
        "v_fma_f32 v72, v72, s24, v236\n"
        "v_mfma_f32_16x16x32_bf16 v[216:219], a[40:43], a[216:219], v[216:219]\n"
        "ds_read_b64_tr_b16 a[166:167], v131 offset:26112\n"
        "ds_read_b64_tr_b16 a[180:181], v132 offset:0\n"
        "v_fma_f32 v73, v73, s24, v236\n"
        "v_mfma_f32_16x16x32_bf16 v[216:219], a[44:47], a[220:223], v[216:219]\n"
        "ds_read_b64_tr_b16 a[182:183], v132 offset:1536\n"
        "ds_read_b64_tr_b16 a[184:185], v132 offset:12288\n"
        "v_fma_f32 v74, v74, s24, v236\n"
        "v_mfma_f32_16x16x32_bf16 v[162:165], a[48:51], a[236:239], 0\n"
        "ds_read_b128 a[236:239], v241 offset:1664\n"
        "ds_read_b64_tr_b16 a[186:187], v132 offset:13824\n"
        "v_fma_f32 v75, v75, s24, v236\n"
        "v_mfma_f32_16x16x32_bf16 v[162:165], a[52:55], a[240:243], v[162:165]\n"
        "ds_read_b128 a[240:243], v240 offset:1792\n"
        "ds_read_b64_tr_b16 a[188:189], v132 offset:24576\n"
        "v_cvt_pk_bf16_f32 v158, v216, v217\n"
        "v_mfma_f32_16x16x32_bf16 v[162:165], a[56:59], a[244:247], v[162:165]\n"
        "ds_read_b128 a[244:247], v241 offset:1792\n"
        "buffer_load_dwordx4 v142, s[52:55], s64 offen offset:1024 lds\n"
        "v_cvt_pk_bf16_f32 v159, v218, v219\n"
        "v_mfma_f32_16x16x32_bf16 v[162:165], a[60:63], a[248:251], v[162:165]\n"
        "ds_read_b64_tr_b16 a[190:191], v132 offset:26112\n"
        "ds_read_b64_tr_b16 a[204:205], v133 offset:0\n"
        "v_fma_f32 v76, v76, s24, v236\n"
        "v_mfma_f32_16x16x32_bf16 v[162:165], a[64:67], a[216:219], v[162:165]\n"
        "ds_read_b64_tr_b16 a[206:207], v133 offset:1536\n"
        "ds_read_b64_tr_b16 a[208:209], v133 offset:12288\n"
        "v_fma_f32 v77, v77, s24, v236\n"
        "v_mfma_f32_16x16x32_bf16 v[162:165], a[68:71], a[220:223], v[162:165]\n"
        "s_waitcnt lgkmcnt(15)\n"
        "ds_read_b64_tr_b16 a[210:211], v133 offset:13824\n"
        "ds_read_b64_tr_b16 a[212:213], v133 offset:24576\n"
        "v_fma_f32 v78, v78, s24, v236\n"
        "v_mfma_f32_16x16x32_bf16 v[212:215], a[72:75], a[224:227], 0\n"
        "s_waitcnt lgkmcnt(15)\n"
        "ds_read_b64_tr_b16 a[214:215], v133 offset:26112\n"
        "v_exp_f32 v71, v71\n"
        "v_mfma_f32_16x16x32_bf16 v[212:215], a[76:79], a[228:231], v[212:215]\n"
        "s_waitcnt lgkmcnt(15)\n"
        "buffer_load_dwordx4 v142, s[52:55], s64 offen offset:2048 lds\n"
        "v_exp_f32 v72, v72\n"
        "v_mfma_f32_16x16x32_bf16 v[212:215], a[80:83], a[232:235], v[212:215]\n"
        "s_waitcnt lgkmcnt(11)\n"
        "v_exp_f32 v73, v73\n"
        "v_cvt_pk_bf16_f32 v160, v162, v163\n"
        "v_mfma_f32_16x16x32_bf16 v[212:215], a[84:87], a[236:239], v[212:215]\n"
        "s_waitcnt lgkmcnt(9)\n"
        "v_exp_f32 v74, v74\n"
        "v_cvt_pk_bf16_f32 v161, v164, v165\n"
        "v_mfma_f32_16x16x32_bf16 v[212:215], a[88:91], a[240:243], v[212:215]\n"
        "s_waitcnt lgkmcnt(7)\n"
        "s_add_u32 m0, s65, 39936\n"
        "v_cvt_pk_bf16_f32 v166, v162, v163\n"
        "v_cvt_pk_bf16_f32 v167, v164, v165\n"
        "v_cvt_pk_bf16_f32 v67, v70, v71\n"
        "v_mfma_f32_16x16x32_bf16 v[212:215], a[92:95], a[244:247], v[212:215]\n"
        "s_add_u32 s64, s65, 39936\n"
        "v_exp_f32 v75, v75\n"
        "v_cvt_pk_bf16_f32 v68, v72, v73\n"
        "v_mfma_f32_16x16x32_bf16 v[216:219], a[96:99], a[224:227], 0\n"
        "buffer_load_dwordx4 v142, s[52:55], s64 offen lds\n"
        "v_exp_f32 v76, v76\n"
        "v_mfma_f32_16x16x32_bf16 v[216:219], a[100:103], a[228:231], v[216:219]\n"
        "v_exp_f32 v77, v77\n"
        "v_cvt_pk_bf16_f32 v172, v212, v213\n"
        "v_mfma_f32_16x16x32_bf16 v[216:219], a[104:107], a[232:235], v[216:219]\n"
        "v_exp_f32 v78, v78\n"
        "v_cvt_pk_bf16_f32 v173, v214, v215\n"
        "v_mfma_f32_16x16x32_bf16 v[216:219], a[108:111], a[236:239], v[216:219]\n"
        "v_cvt_pk_bf16_f32 v69, v74, v75\n"
        "v_cvt_pk_bf16_f32 v70, v76, v77\n"
        "v_fma_f32 v79, v79, s24, v236\n"
        "v_mfma_f32_16x16x32_bf16 v[216:219], a[112:115], a[240:243], v[216:219]\n"
        "s_waitcnt lgkmcnt(0)\n"
        "s_barrier\n"
        "v_exp_f32 v79, v79\n"
        "v_fma_f32 v80, v80, s24, v237\n"
        "v_mfma_f32_16x16x32_bf16 v[216:219], a[116:119], a[244:247], v[216:219]\n"
        "buffer_load_dwordx4 v142, s[52:55], s64 offen offset:1024 lds\n"
        "v_exp_f32 v80, v80\n"
        "v_mfma_f32_16x16x32_bf16 v[162:165], a[120:123], a[224:227], 0\n"
        "v_cvt_pk_bf16_f32 v71, v78, v79\n"
        "v_fma_f32 v81, v81, s24, v237\n"
        "v_fma_f32 v82, v82, s24, v237\n"
        "v_mfma_f32_16x16x32_bf16 v[162:165], a[124:127], a[228:231], v[162:165]\n"
        "v_exp_f32 v81, v81\n"
        "v_cvt_pk_bf16_f32 v174, v216, v217\n"
        "v_mfma_f32_16x16x32_bf16 v[162:165], a[128:131], a[232:235], v[162:165]\n"
        "ds_read_b128 a[248:251], v240 offset:12288\n"
        "v_exp_f32 v82, v82\n"
        "v_mfma_f32_16x16x32_bf16 v[162:165], a[132:135], a[236:239], v[162:165]\n"
        "ds_read_b128 a[216:219], v241 offset:12288\n"
        "v_cvt_pk_bf16_f32 v175, v218, v219\n"
        "v_cvt_pk_bf16_f32 v80, v80, v81\n"
        "v_mfma_f32_16x16x32_bf16 v[162:165], a[136:139], a[240:243], v[162:165]\n"
        "buffer_load_dwordx4 v142, s[52:55], s64 offen offset:2048 lds\n"
        "ds_read_b128 a[220:223], v240 offset:12416\n"
        "v_fma_f32 v83, v83, s24, v237\n"
        "v_mfma_f32_16x16x32_bf16 v[162:165], a[140:143], a[244:247], v[162:165]\n"
        "v_exp_f32 v83, v83\n"
        "v_fma_f32 v84, v84, s24, v237\n"
        "v_mfma_f32_16x16x32_bf16 v[212:215], a[0:3], a[224:227], 0\n"
        "v_exp_f32 v84, v84\n"
        "v_cvt_pk_bf16_f32 v81, v82, v83\n"
        "v_mfma_f32_16x16x32_bf16 v[212:215], a[4:7], a[228:231], v[212:215]\n"
        "s_add_u32 m0, s65, 43008\n"
        "v_fma_f32 v85, v85, s24, v237\n"
        "v_exp_f32 v85, v85\n"
        "v_mfma_f32_16x16x32_bf16 v[212:215], a[8:11], a[232:235], v[212:215]\n"
        "s_add_u32 s64, s65, 43008\n"
        "v_cvt_pk_bf16_f32 v190, v162, v163\n"
        "v_cvt_pk_bf16_f32 v191, v164, v165\n"
        "v_cvt_pk_bf16_f32 v82, v84, v85\n"
        "v_mfma_f32_16x16x32_bf16 v[212:215], a[12:15], a[236:239], v[212:215]\n"
        "buffer_load_dwordx4 v142, s[52:55], s64 offen lds\n"
        "v_fma_f32 v86, v86, s24, v237\n"
        "v_fma_f32 v87, v87, s24, v237\n"
        "v_mfma_f32_16x16x32_bf16 v[212:215], a[16:19], a[240:243], v[212:215]\n"
        "v_exp_f32 v86, v86\n"
        "v_fma_f32 v88, v88, s24, v237\n"
        "v_mfma_f32_16x16x32_bf16 v[212:215], a[20:23], a[244:247], v[212:215]\n"
        "v_exp_f32 v87, v87\n"
        "v_fma_f32 v89, v89, s24, v237\n"
        "v_mfma_f32_16x16x32_bf16 v[216:219], a[24:27], a[224:227], 0\n"
        "v_exp_f32 v88, v88\n"
        "v_cvt_pk_bf16_f32 v83, v86, v87\n"
        "v_mfma_f32_16x16x32_bf16 v[216:219], a[28:31], a[228:231], v[216:219]\n"
        "v_exp_f32 v89, v89\n"
        "v_fma_f32 v90, v90, s24, v237\n"
        "v_mfma_f32_16x16x32_bf16 v[216:219], a[32:35], a[232:235], v[216:219]\n"
        "buffer_load_dwordx4 v142, s[52:55], s64 offen offset:1024 lds\n"
        "v_exp_f32 v90, v90\n"
        "v_mfma_f32_16x16x32_bf16 v[216:219], a[36:39], a[236:239], v[216:219]\n"
        "v_cvt_pk_bf16_f32 v176, v212, v213\n"
        "v_cvt_pk_bf16_f32 v177, v214, v215\n"
        "v_cvt_pk_bf16_f32 v84, v88, v89\n"
        "v_mfma_f32_16x16x32_bf16 v[216:219], a[40:43], a[240:243], v[216:219]\n"
        "v_fma_f32 v91, v91, s24, v237\n"
        "v_exp_f32 v91, v91\n"
        "v_mfma_f32_16x16x32_bf16 v[216:219], a[44:47], a[244:247], v[216:219]\n"
        "v_cvt_pk_bf16_f32 v85, v90, v91\n"
        "v_fma_f32 v92, v92, s24, v237\n"
        "v_fma_f32 v93, v93, s24, v237\n"
        "v_mfma_f32_16x16x32_bf16 v[162:165], a[48:51], a[224:227], 0\n"
        "ds_read_b128 a[224:227], v241 offset:12416\n"
        "v_exp_f32 v92, v92\n"
        "v_mfma_f32_16x16x32_bf16 v[162:165], a[52:55], a[228:231], v[162:165]\n"
        "ds_read_b128 a[228:231], v240 offset:12544\n"
        "buffer_load_dwordx4 v142, s[52:55], s64 offen offset:2048 lds\n"
        "v_cvt_pk_bf16_f32 v178, v216, v217\n"
        "v_mfma_f32_16x16x32_bf16 v[162:165], a[56:59], a[232:235], v[162:165]\n"
        "ds_read_b128 a[232:235], v241 offset:12544\n"
        "v_exp_f32 v93, v93\n"
        "v_mfma_f32_16x16x32_bf16 v[162:165], a[60:63], a[236:239], v[162:165]\n"
        "v_cvt_pk_bf16_f32 v179, v218, v219\n"
        "v_cvt_pk_bf16_f32 v86, v92, v93\n"
        "v_fma_f32 v94, v94, s24, v237\n"
        "v_mfma_f32_16x16x32_bf16 v[162:165], a[64:67], a[240:243], v[162:165]\n"
        "s_add_u32 m0, s65, 0\n"
        "v_exp_f32 v94, v94\n"
        "v_fma_f32 v95, v95, s24, v237\n"
        "v_mfma_f32_16x16x32_bf16 v[162:165], a[68:71], a[244:247], v[162:165]\n"
        "s_waitcnt lgkmcnt(5)\n"
        "s_add_u32 s64, s65, 0\n"
        "v_exp_f32 v95, v95\n"
        "v_fma_f32 v96, v96, s24, v238\n"
        "v_mfma_f32_16x16x32_bf16 v[72:75], a[72:75], a[248:251], 0\n"
        "s_waitcnt lgkmcnt(4)\n"
        "buffer_load_dwordx4 v142, s[52:55], s64 offen lds\n"
        "v_exp_f32 v96, v96\n"
        "v_mfma_f32_16x16x32_bf16 v[72:75], a[76:79], a[216:219], v[72:75]\n"
        "s_waitcnt lgkmcnt(3)\n"
        "v_cvt_pk_bf16_f32 v87, v94, v95\n"
        "v_cvt_pk_bf16_f32 v180, v162, v163\n"
        "v_cvt_pk_bf16_f32 v181, v164, v165\n"
        "v_mfma_f32_16x16x32_bf16 v[72:75], a[80:83], a[220:223], v[72:75]\n"
        "s_waitcnt lgkmcnt(2)\n"
        "v_cvt_pk_bf16_f32 v186, v162, v163\n"
        "v_cvt_pk_bf16_f32 v187, v164, v165\n"
        "v_fma_f32 v97, v97, s24, v238\n"
        "v_mfma_f32_16x16x32_bf16 v[72:75], a[84:87], a[224:227], v[72:75]\n"
        "s_waitcnt lgkmcnt(1)\n"
        "v_exp_f32 v97, v97\n"
        "v_fma_f32 v98, v98, s24, v238\n"
        "v_mfma_f32_16x16x32_bf16 v[72:75], a[88:91], a[228:231], v[72:75]\n"
        "s_waitcnt lgkmcnt(0)\n"
        "v_exp_f32 v98, v98\n"
        "v_accvgpr_read_b32 v232, a252\n"
        "v_mfma_f32_16x16x32_bf16 v[72:75], a[92:95], a[232:235], v[72:75]\n"
        "buffer_load_dwordx4 v142, s[52:55], s64 offen offset:1024 lds\n"
        "v_accvgpr_read_b32 v233, a253\n"
        "v_accvgpr_read_b32 v234, a254\n"
        "v_mfma_f32_16x16x32_bf16 v[76:79], a[96:99], a[248:251], 0\n"
        "v_mov_b32 v235, 0x3f803f80\n"
        "v_accvgpr_write_b32 a252, v235\n"
        "v_accvgpr_write_b32 a253, v235\n"
        "v_mfma_f32_16x16x32_bf16 v[76:79], a[100:103], a[216:219], v[76:79]\n"
        "v_cvt_pk_bf16_f32 v192, v72, v73\n"
        "v_cvt_pk_bf16_f32 v193, v74, v75\n"
        "v_accvgpr_write_b32 a254, v235\n"
        "v_mfma_f32_16x16x32_bf16 v[76:79], a[104:107], a[220:223], v[76:79]\n"
        "v_accvgpr_write_b32 a255, v235\n"
        "v_cvt_pk_bf16_f32 v96, v96, v97\n"
        "v_fma_f32 v99, v99, s24, v238\n"
        "v_mfma_f32_16x16x32_bf16 v[76:79], a[108:111], a[224:227], v[76:79]\n"
        "v_exp_f32 v99, v99\n"
        "v_fma_f32 v100, v100, s24, v238\n"
        "v_mfma_f32_16x16x32_bf16 v[76:79], a[112:115], a[228:231], v[76:79]\n"
        "buffer_load_dwordx4 v142, s[52:55], s64 offen offset:2048 lds\n"
        "v_exp_f32 v100, v100\n"
        "v_mfma_f32_16x16x32_bf16 v[76:79], a[116:119], a[232:235], v[76:79]\n"
        "v_cvt_pk_bf16_f32 v97, v98, v99\n"
        "v_fma_f32 v101, v101, s24, v238\n"
        "v_fma_f32 v102, v102, s24, v238\n"
        "v_mfma_f32_16x16x32_bf16 v[162:165], a[120:123], a[248:251], 0\n"
        "ds_read_b128 a[236:239], v150 offset:0\n"
        "v_exp_f32 v101, v101\n"
        "v_mfma_f32_16x16x32_bf16 v[162:165], a[124:127], a[216:219], v[162:165]\n"
        "s_add_u32 m0, s65, 3072\n"
        "ds_read_b128 a[240:243], v151 offset:0\n"
        "v_exp_f32 v102, v102\n"
        "v_mfma_f32_16x16x32_bf16 v[162:165], a[128:131], a[220:223], v[162:165]\n"
        "s_add_u32 s64, s65, 3072\n"
        "s_waitcnt vmcnt(3)\n"
        "s_barrier\n"
        "ds_read_b64_tr_b16 v[212:213], v232 offset:36864\n"
        "ds_read_b128 a[244:247], v150 offset:128\n"
        "v_mfma_f32_16x16x32_bf16 v[162:165], a[132:135], a[224:227], v[162:165]\n"
        "buffer_load_dwordx4 v142, s[52:55], s64 offen lds\n"
        "v_cvt_pk_bf16_f32 v194, v76, v77\n"
        "v_cvt_pk_bf16_f32 v195, v78, v79\n"
        "v_mfma_f32_16x16x32_bf16 v[162:165], a[136:139], a[228:231], v[162:165]\n"
        "ds_read_b64_tr_b16 v[214:215], v232 offset:43008\n"
        "v_cvt_pk_bf16_f32 v98, v100, v101\n"
        "v_fma_f32 v103, v103, s24, v238\n"
        "v_mfma_f32_16x16x32_bf16 v[162:165], a[140:143], a[232:235], v[162:165]\n"
        "v_exp_f32 v103, v103\n"
        "v_fma_f32 v104, v104, s24, v238\n"
        "v_mfma_f32_16x16x32_bf16 v[72:75], a[0:3], a[248:251], 0\n"
        "ds_read_b64_tr_b16 v[216:217], v232 offset:55296\n"
        "v_exp_f32 v104, v104\n"
        "v_mfma_f32_16x16x32_bf16 v[72:75], a[4:7], a[216:219], v[72:75]\n"
        "v_cvt_pk_bf16_f32 v99, v102, v103\n"
        "v_fma_f32 v105, v105, s24, v238\n"
        "v_cvt_pk_bf16_f32 v208, v162, v163\n"
        "v_mfma_f32_16x16x32_bf16 v[72:75], a[8:11], a[220:223], v[72:75]\n"
        "buffer_load_dwordx4 v142, s[52:55], s64 offen offset:1024 lds\n"
        "ds_read_b64_tr_b16 v[218:219], v232 offset:61440\n"
        "v_cvt_pk_bf16_f32 v209, v164, v165\n"
        "v_mfma_f32_16x16x32_bf16 v[72:75], a[12:15], a[224:227], v[72:75]\n"
        "v_exp_f32 v105, v105\n"
        "v_fma_f32 v106, v106, s24, v238\n"
        "v_mfma_f32_16x16x32_bf16 v[72:75], a[16:19], a[228:231], v[72:75]\n"
        "ds_read_b64_tr_b16 v[220:221], v233 offset:49152\n"
        "v_exp_f32 v106, v106\n"
        "v_mfma_f32_16x16x32_bf16 v[72:75], a[20:23], a[232:235], v[72:75]\n"
        "v_cvt_pk_bf16_f32 v100, v104, v105\n"
        "v_fma_f32 v107, v107, s24, v238\n"
        "v_fma_f32 v108, v108, s24, v238\n"
        "v_mfma_f32_16x16x32_bf16 v[76:79], a[24:27], a[248:251], 0\n"
        "ds_read_b64_tr_b16 v[222:223], v233 offset:50688\n"
        "v_exp_f32 v107, v107\n"
        "v_mfma_f32_16x16x32_bf16 v[76:79], a[28:31], a[216:219], v[76:79]\n"
        "buffer_load_dwordx4 v142, s[52:55], s64 offen offset:2048 lds\n"
        "v_exp_f32 v108, v108\n"
        "v_mfma_f32_16x16x32_bf16 v[76:79], a[32:35], a[220:223], v[76:79]\n"
        "ds_read_b64_tr_b16 v[224:225], v232 offset:36896\n"
        "v_cvt_pk_bf16_f32 v196, v72, v73\n"
        "v_cvt_pk_bf16_f32 v197, v74, v75\n"
        "v_mfma_f32_16x16x32_bf16 v[76:79], a[36:39], a[224:227], v[76:79]\n"
        "v_cvt_pk_bf16_f32 v101, v106, v107\n"
        "v_fma_f32 v109, v109, s24, v238\n"
        "v_fma_f32 v110, v110, s24, v238\n"
        "v_mfma_f32_16x16x32_bf16 v[76:79], a[40:43], a[228:231], v[76:79]\n"
        "s_add_u32 m0, s65, 6144\n"
        "ds_read_b64_tr_b16 v[226:227], v232 offset:43040\n"
        "v_exp_f32 v109, v109\n"
        "v_mfma_f32_16x16x32_bf16 v[76:79], a[44:47], a[232:235], v[76:79]\n"
        "s_add_u32 s64, s65, 6144\n"
        "v_exp_f32 v110, v110\n"
        "v_cvt_pk_bf16_f32 v102, v108, v109\n"
        "v_mfma_f32_16x16x32_bf16 v[162:165], a[48:51], a[248:251], 0\n"
        "ds_read_b128 a[248:251], v151 offset:128\n"
        "buffer_load_dwordx4 v142, s[52:55], s64 offen lds\n"
        "v_fma_f32 v111, v111, s24, v238\n"
        "v_mfma_f32_16x16x32_bf16 v[162:165], a[52:55], a[216:219], v[162:165]\n"
        "ds_read_b128 a[216:219], v150 offset:256\n"
        "ds_read_b64_tr_b16 v[182:183], v232 offset:55328\n"
        "v_cvt_pk_bf16_f32 v198, v76, v77\n"
        "v_mfma_f32_16x16x32_bf16 v[162:165], a[56:59], a[220:223], v[162:165]\n"
        "ds_read_b128 a[220:223], v151 offset:256\n"
        "ds_read_b64_tr_b16 v[184:185], v232 offset:61472\n"
        "s_sub_u32 s31, s30, s28\n"
        "v_cvt_pk_bf16_f32 v199, v78, v79\n"
        "v_mfma_f32_16x16x32_bf16 v[162:165], a[60:63], a[224:227], v[162:165]\n"
        "s_sub_u32 s45, s28, s29\n"
        "v_exp_f32 v111, v111\n"
        "v_fma_f32 v112, v112, s24, v239\n"
        "v_mfma_f32_16x16x32_bf16 v[162:165], a[64:67], a[228:231], v[162:165]\n"
        "ds_read_b64_tr_b16 v[202:203], v233 offset:49184\n"
        "ds_read_b128 a[228:231], v150 offset:1536\n"
        "s_add_u32 s28, s28, s31\n"
        "v_cvt_pk_bf16_f32 v103, v110, v111\n"
        "v_mfma_f32_16x16x32_bf16 v[162:165], a[68:71], a[232:235], v[162:165]\n"
        "s_waitcnt lgkmcnt(15)\n"
        "buffer_load_dwordx4 v142, s[52:55], s64 offen offset:1024 lds\n"
        "ds_read_b128 a[232:235], v151 offset:1536\n"
        "v_fma_f32 v113, v113, s24, v239\n"
        "v_mfma_f32_16x16x32_bf16 v[72:75], a[236:239], a[144:147], 0\n"
        "s_waitcnt lgkmcnt(15)\n"
        "ds_read_b64_tr_b16 v[204:205], v233 offset:50720\n"
        "v_exp_f32 v112, v112\n"
        "v_mfma_f32_16x16x32_bf16 v[72:75], a[240:243], a[148:151], v[72:75]\n"
        "s_waitcnt lgkmcnt(15)\n"
        "v_exp_f32 v113, v113\n"
        "v_cvt_pk_bf16_f32 v200, v162, v163\n"
        "v_mfma_f32_16x16x32_bf16 v[72:75], a[244:247], a[152:155], v[72:75]\n"
        "s_waitcnt lgkmcnt(8)\n"
        "ds_read_b64_tr_b16 v[88:89], v232 offset:36928\n"
        "v_cvt_pk_bf16_f32 v201, v164, v165\n"
        "v_cvt_pk_bf16_f32 v206, v162, v163\n"
        "v_mfma_f32_16x16x32_bf16 v[72:75], a[248:251], a[156:159], v[72:75]\n"
        "s_waitcnt lgkmcnt(8)\n"
        "v_cvt_pk_bf16_f32 v207, v164, v165\n"
        "v_cvt_pk_bf16_f32 v112, v112, v113\n"
        "v_fma_f32 v114, v114, s24, v239\n"
        "v_mfma_f32_16x16x32_bf16 v[72:75], a[216:219], a[160:163], v[72:75]\n"
        "s_waitcnt lgkmcnt(6)\n"
        "buffer_load_dwordx4 v142, s[52:55], s64 offen offset:2048 lds\n"
        "s_add_u32 s41, s30, s40\n"
        "s_add_u32 s35, s47, 786432\n"
        "s_sub_u32 s46, s35, 100663296\n"
        "s_cmp_lt_u32 s46, 786432\n"
        "v_mfma_f32_16x16x32_bf16 v[72:75], a[220:223], a[164:167], v[72:75]\n"
        "s_cselect_b32 s35, s46, s35\n"
        "ds_read_b64_tr_b16 v[90:91], v232 offset:43072\n"
        "v_exp_f32 v114, v114\n"
        "v_mfma_f32_16x16x32_bf16 v[76:79], a[236:239], a[168:171], 0\n"
        "ds_read_b64_tr_b16 v[92:93], v232 offset:55360\n"
        "v_fma_f32 v115, v115, s24, v239\n"
        "v_fma_f32 v116, v116, s24, v239\n"
        "v_mfma_f32_16x16x32_bf16 v[76:79], a[240:243], a[172:175], v[76:79]\n"
        "v_exp_f32 v115, v115\n"
        "v_cvt_pk_bf16_f32 v128, v72, v73\n"
        "v_mfma_f32_16x16x32_bf16 v[76:79], a[244:247], a[176:179], v[76:79]\n"
        "ds_read_b64_tr_b16 v[94:95], v232 offset:61504\n"
        "v_exp_f32 v116, v116\n"
        "v_mfma_f32_16x16x32_bf16 v[76:79], a[248:251], a[180:183], v[76:79]\n"
        "s_mov_b32 m0, s48\n"
        "v_cvt_pk_bf16_f32 v129, v74, v75\n"
        "v_cvt_pk_bf16_f32 v113, v114, v115\n"
        "v_fma_f32 v117, v117, s24, v239\n"
        "v_mfma_f32_16x16x32_bf16 v[76:79], a[216:219], a[184:187], v[76:79]\n"
        "buffer_load_dwordx4 v249, s[12:15], s36 offen offset:0 lds\n"
        "ds_read_b64_tr_b16 v[104:105], v233 offset:49216\n"
        "v_mfma_f32_16x16x32_bf16 v[76:79], a[220:223], a[188:191], v[76:79]\n"
        "v_exp_f32 v117, v117\n"
        "v_fma_f32 v118, v118, s24, v239\n"
        "v_mfma_f32_16x16x32_bf16 v[162:165], a[236:239], a[192:195], 0\n"
        "ds_read_b128 a[236:239], v150 offset:1664\n"
        "ds_read_b64_tr_b16 v[106:107], v233 offset:50752\n"
        "v_cvt_pk_bf16_f32 v114, v116, v117\n"
        "v_mfma_f32_16x16x32_bf16 v[162:165], a[240:243], a[196:199], v[162:165]\n"
        "ds_read_b128 a[240:243], v151 offset:1664\n"
        "ds_read_b128 a[224:227], v151 offset:12288\n"
        "v_xor_b32 v253, 32, v249\n"
        "v_mfma_f32_16x16x32_bf16 v[162:165], a[244:247], a[200:203], v[162:165]\n"
        "ds_read_b128 a[244:247], v150 offset:1792\n"
        "v_exp_f32 v118, v118\n"
        "v_mfma_f32_16x16x32_bf16 v[162:165], a[248:251], a[204:207], v[162:165]\n"
        "ds_read_b128 a[248:251], v151 offset:1792\n"
        "buffer_load_dwordx4 v253, s[12:15], s36 offen offset:1024 lds\n"
        "s_mov_b32 s30, s29\n"
        "v_mfma_f32_16x16x32_bf16 v[162:165], a[216:219], a[208:211], v[162:165]\n"
        "s_waitcnt lgkmcnt(4)\n"
        "s_barrier\n"
        "ds_write_b128 v234, v[212:215] offset:36864\n"
        "v_mfma_f32_16x16x32_bf16 v[162:165], a[220:223], a[212:215], v[162:165]\n"
        "s_add_u32 s29, s29, s45\n"
        "ds_read_b128 a[220:223], v150 offset:12288\n"
        "v_cvt_pk_bf16_f32 v136, v76, v77\n"
        "v_mfma_f32_16x16x32_bf16 v[72:75], a[228:231], a[144:147], 0\n"
        "v_cvt_pk_bf16_f32 v137, v78, v79\n"
        "v_fma_f32 v119, v119, s24, v239\n"
        "v_mfma_f32_16x16x32_bf16 v[72:75], a[232:235], a[148:151], v[72:75]\n"
        "s_add_u32 m0, s41, 0\n"
        "v_exp_f32 v119, v119\n"
        "v_mfma_f32_16x16x32_bf16 v[72:75], a[236:239], a[152:155], v[72:75]\n"
        "s_waitcnt lgkmcnt(5)\n"
        "buffer_load_dwordx4 v242, s[8:11], s47 offen lds\n"
        "v_mfma_f32_16x16x32_bf16 v[72:75], a[240:243], a[156:159], v[72:75]\n"
        "s_waitcnt lgkmcnt(3)\n"
        "ds_write_b128 v234, v[216:219] offset:43008\n"
        "v_mfma_f32_16x16x32_bf16 v[72:75], a[244:247], a[160:163], v[72:75]\n"
        "s_waitcnt lgkmcnt(3)\n"
        "v_cvt_pk_bf16_f32 v144, v162, v163\n"
        "v_cvt_pk_bf16_f32 v145, v164, v165\n"
        "v_mfma_f32_16x16x32_bf16 v[72:75], a[248:251], a[164:167], v[72:75]\n"
        "v_cvt_pk_bf16_f32 v115, v118, v119\n"
        "v_fma_f32 v120, v120, s24, v239\n"
        "v_mfma_f32_16x16x32_bf16 v[76:79], a[228:231], a[168:171], 0\n"
        "ds_write_b128 v234, v[220:223] offset:49152\n"
        "v_mfma_f32_16x16x32_bf16 v[76:79], a[232:235], a[172:175], v[76:79]\n"
        "v_exp_f32 v120, v120\n"
        "v_mfma_f32_16x16x32_bf16 v[76:79], a[236:239], a[176:179], v[76:79]\n"
        "v_fma_f32 v121, v121, s24, v239\n"
        "v_cvt_pk_bf16_f32 v130, v72, v73\n"
        "v_mfma_f32_16x16x32_bf16 v[76:79], a[240:243], a[180:183], v[76:79]\n"
        "ds_write_b128 v234, v[224:227] offset:37120\n"
        "v_mfma_f32_16x16x32_bf16 v[76:79], a[244:247], a[184:187], v[76:79]\n"
        "v_cvt_pk_bf16_f32 v131, v74, v75\n"
        "v_fma_f32 v122, v122, s24, v239\n"
        "v_mfma_f32_16x16x32_bf16 v[76:79], a[248:251], a[188:191], v[76:79]\n"
        "v_exp_f32 v121, v121\n"
        "v_mfma_f32_16x16x32_bf16 v[162:165], a[228:231], a[192:195], 0\n"
        "ds_read_b128 a[228:231], v150 offset:12416\n"
        "v_cvt_pk_bf16_f32 v116, v120, v121\n"
        "v_mfma_f32_16x16x32_bf16 v[162:165], a[232:235], a[196:199], v[162:165]\n"
        "ds_read_b128 a[232:235], v151 offset:12416\n"
        "ds_read_b128 a[216:219], v151 offset:13824\n"
        "v_mfma_f32_16x16x32_bf16 v[162:165], a[236:239], a[200:203], v[162:165]\n"
        "ds_write_b128 v234, v[182:185] offset:43264\n"
        "v_mfma_f32_16x16x32_bf16 v[162:165], a[240:243], a[204:207], v[162:165]\n"
        "ds_read_b128 a[236:239], v150 offset:12544\n"
        "ds_read_b128 a[240:243], v151 offset:12544\n"
        "v_mfma_f32_16x16x32_bf16 v[162:165], a[244:247], a[208:211], v[162:165]\n"
        "v_cvt_pk_bf16_f32 v138, v76, v77\n"
        "v_cvt_pk_bf16_f32 v139, v78, v79\n"
        "v_mfma_f32_16x16x32_bf16 v[162:165], a[248:251], a[212:215], v[162:165]\n"
        "s_waitcnt lgkmcnt(9)\n"
        "ds_write_b128 v234, v[202:205] offset:49408\n"
        "v_mfma_f32_16x16x32_bf16 v[72:75], a[220:223], a[144:147], 0\n"
        "ds_read_b128 a[248:251], v150 offset:13824\n"
        "v_fma_f32 v123, v123, s24, v239\n"
        "v_mfma_f32_16x16x32_bf16 v[72:75], a[224:227], a[148:151], v[72:75]\n"
        "s_waitcnt lgkmcnt(7)\n"
        "v_exp_f32 v122, v122\n"
        "v_mfma_f32_16x16x32_bf16 v[72:75], a[228:231], a[152:155], v[72:75]\n"
        "s_waitcnt lgkmcnt(6)\n"
        "ds_write_b128 v234, v[88:91] offset:37376\n"
        "v_mfma_f32_16x16x32_bf16 v[72:75], a[232:235], a[156:159], v[72:75]\n"
        "s_waitcnt lgkmcnt(4)\n"
        "v_exp_f32 v123, v123\n"
        "v_mfma_f32_16x16x32_bf16 v[72:75], a[236:239], a[160:163], v[72:75]\n"
        "s_waitcnt lgkmcnt(3)\n"
        "v_cvt_pk_bf16_f32 v146, v162, v163\n"
        "v_cvt_pk_bf16_f32 v147, v164, v165\n"
        "v_mfma_f32_16x16x32_bf16 v[72:75], a[240:243], a[164:167], v[72:75]\n"
        "ds_write_b128 v234, v[92:95] offset:43520\n"
        "v_mfma_f32_16x16x32_bf16 v[76:79], a[220:223], a[168:171], 0\n"
        "v_cvt_pk_bf16_f32 v117, v122, v123\n"
        "v_fma_f32 v124, v124, s24, v239\n"
        "v_mfma_f32_16x16x32_bf16 v[76:79], a[224:227], a[172:175], v[76:79]\n"
        "v_fma_f32 v125, v125, s24, v239\n"
        "v_fma_f32 v126, v126, s24, v239\n"
        "v_mfma_f32_16x16x32_bf16 v[76:79], a[228:231], a[176:179], v[76:79]\n"
        "ds_write_b128 v234, v[104:107] offset:49664\n"
        "s_waitcnt vmcnt(3)\n"
        "s_barrier\n"
        "v_mfma_f32_16x16x32_bf16 v[76:79], a[232:235], a[180:183], v[76:79]\n"
        "ds_read_b64_tr_b16 v[108:109], v232 offset:0\n"
        "v_fma_f32 v127, v127, s24, v239\n"
        "v_mfma_f32_16x16x32_bf16 v[76:79], a[236:239], a[184:187], v[76:79]\n"
        "ds_read_b64_tr_b16 v[110:111], v232 offset:6144\n"
        "v_cvt_pk_bf16_f32 v132, v72, v73\n"
        "v_mfma_f32_16x16x32_bf16 v[76:79], a[240:243], a[188:191], v[76:79]\n"
        "v_exp_f32 v124, v124\n"
        "v_mfma_f32_16x16x32_bf16 v[162:165], a[220:223], a[192:195], 0\n"
        "ds_read_b128 a[220:223], v150 offset:13952\n"
        "ds_read_b64_tr_b16 v[120:121], v232 offset:18432\n"
        "v_mfma_f32_16x16x32_bf16 v[162:165], a[224:227], a[196:199], v[162:165]\n"
        "ds_read_b128 a[224:227], v151 offset:13952\n"
        "v_cvt_pk_bf16_f32 v133, v74, v75\n"
        "v_mfma_f32_16x16x32_bf16 v[162:165], a[228:231], a[200:203], v[162:165]\n"
        "ds_read_b128 a[228:231], v150 offset:14080\n"
        "ds_read_b64_tr_b16 v[122:123], v232 offset:24576\n"
        "v_mfma_f32_16x16x32_bf16 v[162:165], a[232:235], a[204:207], v[162:165]\n"
        "ds_read_b128 a[232:235], v151 offset:14080\n"
        "v_cvt_pk_bf16_f32 v140, v76, v77\n"
        "v_mfma_f32_16x16x32_bf16 v[162:165], a[236:239], a[208:211], v[162:165]\n"
        "ds_read_b128 a[236:239], v240 offset:13824\n"
        "ds_read_b128 a[244:247], v240 offset:13952\n"
        "v_mfma_f32_16x16x32_bf16 v[162:165], a[240:243], a[212:215], v[162:165]\n"
        "s_waitcnt lgkmcnt(13)\n"
        "ds_read_b128 a[240:243], v241 offset:13824\n"
        "v_cvt_pk_bf16_f32 v141, v78, v79\n"
        "v_mfma_f32_16x16x32_bf16 v[72:75], a[248:251], a[144:147], 0\n"
        "v_exp_f32 v125, v125\n"
        "v_mfma_f32_16x16x32_bf16 v[72:75], a[216:219], a[148:151], v[72:75]\n"
        "s_waitcnt lgkmcnt(8)\n"
        "v_cvt_pk_bf16_f32 v118, v124, v125\n"
        "v_mfma_f32_16x16x32_bf16 v[72:75], a[220:223], a[152:155], v[72:75]\n"
        "s_waitcnt lgkmcnt(6)\n"
        "ds_read_b64_tr_b16 v[124:125], v233 offset:12288\n"
        "ds_read_b64_tr_b16 v[222:223], v232 offset:32\n"
        "v_mfma_f32_16x16x32_bf16 v[72:75], a[224:227], a[156:159], v[72:75]\n"
        "s_waitcnt lgkmcnt(7)\n"
        "v_exp_f32 v126, v126\n"
        "v_mfma_f32_16x16x32_bf16 v[72:75], a[228:231], a[160:163], v[72:75]\n"
        "s_waitcnt lgkmcnt(5)\n"
        "ds_read_b64_tr_b16 v[224:225], v232 offset:6176\n"
        "v_cvt_pk_bf16_f32 v148, v162, v163\n"
        "v_mfma_f32_16x16x32_bf16 v[72:75], a[232:235], a[164:167], v[72:75]\n"
        "v_exp_f32 v127, v127\n"
        "v_mfma_f32_16x16x32_bf16 v[76:79], a[248:251], a[168:171], 0\n"
        "ds_read_b64_tr_b16 v[182:183], v232 offset:18464\n"
        "v_cvt_pk_bf16_f32 v119, v126, v127\n"
        "v_mfma_f32_16x16x32_bf16 v[76:79], a[216:219], a[172:175], v[76:79]\n"
        "ds_read_b64_tr_b16 v[126:127], v233 offset:13824\n"
        "v_cvt_pk_bf16_f32 v149, v164, v165\n"
        "v_mfma_f32_16x16x32_bf16 v[76:79], a[220:223], a[176:179], v[76:79]\n"
        "ds_read_b64_tr_b16 v[184:185], v232 offset:24608\n"
        "v_cvt_pk_bf16_f32 v134, v72, v73\n"
        "v_mfma_f32_16x16x32_bf16 v[76:79], a[224:227], a[180:183], v[76:79]\n"
        "v_cvt_pk_bf16_f32 v135, v74, v75\n"
        "v_mfma_f32_16x16x32_bf16 v[76:79], a[228:231], a[184:187], v[76:79]\n"
        "ds_read_b64_tr_b16 v[202:203], v233 offset:12320\n"
        "v_mfma_f32_16x16x32_bf16 v[76:79], a[232:235], a[188:191], v[76:79]\n"
        "v_mfma_f32_16x16x32_bf16 v[162:165], a[248:251], a[192:195], 0\n"
        "ds_read_b128 a[248:251], v241 offset:13952\n"
        "ds_read_b64_tr_b16 v[204:205], v233 offset:13856\n"
        "v_mfma_f32_16x16x32_bf16 v[162:165], a[216:219], a[196:199], v[162:165]\n"
        "ds_read_b128 a[216:219], v240 offset:14080\n"
        "v_mfma_f32_16x16x32_bf16 v[162:165], a[220:223], a[200:203], v[162:165]\n"
        "ds_read_b128 a[220:223], v241 offset:14080\n"
        "ds_read_b64_tr_b16 v[88:89], v232 offset:64\n"
        "v_mfma_f32_16x16x32_bf16 v[162:165], a[224:227], a[204:207], v[162:165]\n"
        "v_cvt_pk_bf16_f32 v142, v76, v77\n"
        "v_cvt_pk_bf16_f32 v143, v78, v79\n"
        "v_mfma_f32_16x16x32_bf16 v[162:165], a[228:231], a[208:211], v[162:165]\n"
        "ds_read_b64_tr_b16 v[90:91], v232 offset:6208\n"
        "v_mfma_f32_16x16x32_bf16 v[162:165], a[232:235], a[212:215], v[162:165]\n"
        "s_waitcnt lgkmcnt(15)\n"
        "v_mfma_f32_16x16x32_bf16 v[72:75], a[72:75], a[236:239], 0\n"
        "s_waitcnt lgkmcnt(13)\n"
        "ds_read_b64_tr_b16 v[92:93], v232 offset:18496\n"
        "v_mfma_f32_16x16x32_bf16 v[72:75], a[76:79], a[240:243], v[72:75]\n"
        "v_mfma_f32_16x16x32_bf16 v[72:75], a[80:83], a[244:247], v[72:75]\n"
        "s_waitcnt lgkmcnt(6)\n"
        "ds_read_b64_tr_b16 v[94:95], v232 offset:24640\n"
        "v_mfma_f32_16x16x32_bf16 v[72:75], a[84:87], a[248:251], v[72:75]\n"
        "s_waitcnt lgkmcnt(5)\n"
        "v_mfma_f32_16x16x32_bf16 v[72:75], a[88:91], a[216:219], v[72:75]\n"
        "s_waitcnt lgkmcnt(4)\n"
        "ds_read_b64_tr_b16 v[104:105], v233 offset:12352\n"
        "v_cvt_pk_bf16_f32 v150, v162, v163\n"
        "v_mfma_f32_16x16x32_bf16 v[72:75], a[92:95], a[220:223], v[72:75]\n"
        "v_cvt_pk_bf16_f32 v151, v164, v165\n"
        "v_mfma_f32_16x16x32_bf16 v[76:79], a[96:99], a[236:239], 0\n"
        "s_add_u32 m0, s41, 1024\n"
        "ds_read_b64_tr_b16 v[106:107], v233 offset:13888\n"
        "v_mfma_f32_16x16x32_bf16 v[76:79], a[100:103], a[240:243], v[76:79]\n"
        "v_mfma_f32_16x16x32_bf16 v[76:79], a[104:107], a[244:247], v[76:79]\n"
        "buffer_load_dwordx4 v243, s[8:11], s47 offen lds\n"
        "s_waitcnt lgkmcnt(0)\n"
        "s_barrier\n"
        "v_mfma_f32_16x16x32_bf16 v[76:79], a[108:111], a[248:251], v[76:79]\n"
        "ds_write_b128 v234, v[108:111] offset:0\n"
        "v_mfma_f32_16x16x32_bf16 v[76:79], a[112:115], a[216:219], v[76:79]\n"
        "v_cvt_pk_bf16_f32 v212, v72, v73\n"
        "v_cvt_pk_bf16_f32 v213, v74, v75\n"
        "v_mfma_f32_16x16x32_bf16 v[76:79], a[116:119], a[220:223], v[76:79]\n"
        "v_mfma_f32_16x16x32_bf16 v[162:165], a[120:123], a[236:239], 0\n"
        "ds_write_b128 v234, v[120:123] offset:6144\n"
        "v_mfma_f32_16x16x32_bf16 v[162:165], a[124:127], a[240:243], v[162:165]\n"
        "v_mfma_f32_16x16x32_bf16 v[162:165], a[128:131], a[244:247], v[162:165]\n"
        "v_mfma_f32_16x16x32_bf16 v[162:165], a[132:135], a[248:251], v[162:165]\n"
        "ds_write_b128 v234, v[124:127] offset:12288\n"
        "v_mfma_f32_16x16x32_bf16 v[162:165], a[136:139], a[216:219], v[162:165]\n"
        "v_mfma_f32_16x16x32_bf16 v[162:165], a[140:143], a[220:223], v[162:165]\n"
        "v_cvt_pk_bf16_f32 v214, v76, v77\n"
        "v_cvt_pk_bf16_f32 v215, v78, v79\n"
        "v_mfma_f32_16x16x32_bf16 v[72:75], a[0:3], a[236:239], 0\n"
        "ds_write_b128 v234, v[222:225] offset:256\n"
        "v_mfma_f32_16x16x32_bf16 v[72:75], a[4:7], a[240:243], v[72:75]\n"
        "v_mfma_f32_16x16x32_bf16 v[72:75], a[8:11], a[244:247], v[72:75]\n"
        "s_add_u32 m0, s41, 2048\n"
        "v_mfma_f32_16x16x32_bf16 v[72:75], a[12:15], a[248:251], v[72:75]\n"
        "ds_write_b128 v234, v[182:185] offset:6400\n"
        "v_mfma_f32_16x16x32_bf16 v[72:75], a[16:19], a[216:219], v[72:75]\n"
        "buffer_load_dwordx4 v244, s[8:11], s47 offen lds\n"
        "v_mfma_f32_16x16x32_bf16 v[72:75], a[20:23], a[220:223], v[72:75]\n"
        "v_cvt_pk_bf16_f32 v230, v162, v163\n"
        "v_cvt_pk_bf16_f32 v231, v164, v165\n"
        "v_mfma_f32_16x16x32_bf16 v[76:79], a[24:27], a[236:239], 0\n"
        "ds_write_b128 v234, v[202:205] offset:12544\n"
        "v_mfma_f32_16x16x32_bf16 v[76:79], a[28:31], a[240:243], v[76:79]\n"
        "v_mfma_f32_16x16x32_bf16 v[76:79], a[32:35], a[244:247], v[76:79]\n"
        "v_mfma_f32_16x16x32_bf16 v[76:79], a[36:39], a[248:251], v[76:79]\n"
        "ds_write_b128 v234, v[88:91] offset:512\n"
        "v_mfma_f32_16x16x32_bf16 v[76:79], a[40:43], a[216:219], v[76:79]\n"
        "v_cvt_pk_bf16_f32 v216, v72, v73\n"
        "v_cvt_pk_bf16_f32 v217, v74, v75\n"
        "v_mfma_f32_16x16x32_bf16 v[76:79], a[44:47], a[220:223], v[76:79]\n"
        "v_mfma_f32_16x16x32_bf16 v[162:165], a[48:51], a[236:239], 0\n"
        "ds_write_b128 v234, v[92:95] offset:6656\n"
        "v_mfma_f32_16x16x32_bf16 v[162:165], a[52:55], a[240:243], v[162:165]\n"
        "v_mfma_f32_16x16x32_bf16 v[162:165], a[56:59], a[244:247], v[162:165]\n"
        "v_mfma_f32_16x16x32_bf16 v[162:165], a[60:63], a[248:251], v[162:165]\n"
        "ds_write_b128 v234, v[104:107] offset:12800\n"
        "v_mfma_f32_16x16x32_bf16 v[162:165], a[64:67], a[216:219], v[162:165]\n"
        "s_add_u32 m0, s41, 12288\n"
        "v_cvt_pk_bf16_f32 v218, v76, v77\n"
        "v_cvt_pk_bf16_f32 v219, v78, v79\n"
        "v_mfma_f32_16x16x32_bf16 v[162:165], a[68:71], a[220:223], v[162:165]\n"
        "v_mfma_f32_16x16x32_bf16 v[88:91], a[252:255], v[64:67], 0\n"
        "buffer_load_dwordx4 v242, s[8:11], s35 offen lds\n"
        "v_mfma_f32_16x16x32_bf16 v[88:91], a[252:255], v[68:71], v[88:91]\n"
        "v_mfma_f32_16x16x32_bf16 v[92:95], v[132:135], v[68:71], 0\n"
        "v_mfma_f32_16x16x32_bf16 v[92:95], v[128:131], v[64:67], v[92:95]\n"
        "v_mfma_f32_16x16x32_bf16 v[182:185], v[136:139], v[64:67], 0\n"
        "v_mfma_f32_16x16x32_bf16 v[182:185], v[140:143], v[68:71], v[182:185]\n"
        "v_mfma_f32_16x16x32_bf16 v[202:205], v[148:151], v[68:71], 0\n"
        "s_cmp_eq_u32 s37, 100663296\n"
        "v_cvt_pk_bf16_f32 v220, v162, v163\n"
        "v_cvt_pk_bf16_f32 v221, v164, v165\n"
        "v_mfma_f32_16x16x32_bf16 v[202:205], v[144:147], v[64:67], v[202:205]\n"
        "s_cselect_b32 s46, 0, s37\n"
        "v_cvt_pk_bf16_f32 v226, v162, v163\n"
        "v_cvt_pk_bf16_f32 v227, v164, v165\n"
        "v_mfma_f32_16x16x32_bf16 v[72:75], a[252:255], v[80:83], 0\n"
        "s_add_u32 s38, s46, s43\n"
        "v_rcp_f32 v88, v88\n"
        "v_mfma_f32_16x16x32_bf16 v[72:75], a[252:255], v[84:87], v[72:75]\n"
        "s_add_u32 s39, s46, s44\n"
        "v_mul_f32 v92, v88, v92\n"
        "v_mul_f32 v93, v88, v93\n"
        "v_mfma_f32_16x16x32_bf16 v[76:79], v[132:135], v[84:87], 0\n"
        "s_add_u32 s37, s37, 6291456\n"
        "v_mul_f32 v94, v88, v94\n"
        "v_mul_f32 v95, v88, v95\n"
        "v_mfma_f32_16x16x32_bf16 v[76:79], v[128:131], v[80:83], v[76:79]\n"
        "v_rcp_f32 v72, v72\n"
        "v_mfma_f32_16x16x32_bf16 v[104:107], v[136:139], v[80:83], 0\n"
        "s_add_u32 m0, s41, 13312\n"
        "v_cvt_pk_bf16_f32 v92, v92, v93\n"
        "v_cvt_pk_bf16_f32 v93, v94, v95\n"
        "v_mfma_f32_16x16x32_bf16 v[104:107], v[140:143], v[84:87], v[104:107]\n"
        "v_mul_f32 v182, v88, v182\n"
        "v_mul_f32 v183, v88, v183\n"
        "v_mfma_f32_16x16x32_bf16 v[108:111], v[148:151], v[84:87], 0\n"
        "buffer_load_dwordx4 v243, s[8:11], s35 offen lds\n"
        "v_mfma_f32_16x16x32_bf16 v[108:111], v[144:147], v[80:83], v[108:111]\n"
        "v_mul_f32 v184, v88, v184\n"
        "v_mul_f32 v185, v88, v185\n"
        "v_mfma_f32_16x16x32_bf16 v[64:67], a[252:255], v[96:99], 0\n"
        "v_cvt_pk_bf16_f32 v94, v182, v183\n"
        "v_cvt_pk_bf16_f32 v95, v184, v185\n"
        "v_mfma_f32_16x16x32_bf16 v[64:67], a[252:255], v[100:103], v[64:67]\n"
        "ds_write_b128 v245, v[92:95] offset:0\n"
        "v_mfma_f32_16x16x32_bf16 v[68:71], v[132:135], v[100:103], 0\n"
        "v_mul_f32 v202, v88, v202\n"
        "v_mul_f32 v203, v88, v203\n"
        "v_mfma_f32_16x16x32_bf16 v[68:71], v[128:131], v[96:99], v[68:71]\n"
        "v_mul_f32 v204, v88, v204\n"
        "v_mul_f32 v205, v88, v205\n"
        "v_mfma_f32_16x16x32_bf16 v[120:123], v[136:139], v[96:99], 0\n"
        "v_cvt_pk_bf16_f32 v202, v202, v203\n"
        "v_cvt_pk_bf16_f32 v203, v204, v205\n"
        "v_mfma_f32_16x16x32_bf16 v[120:123], v[140:143], v[100:103], v[120:123]\n"
        "v_mul_f32 v76, v72, v76\n"
        "v_mul_f32 v77, v72, v77\n"
        "v_mfma_f32_16x16x32_bf16 v[124:127], v[148:151], v[100:103], 0\n"
        "ds_write_b64 v246, v[202:203] offset:0\n"
        "v_mfma_f32_16x16x32_bf16 v[124:127], v[144:147], v[96:99], v[124:127]\n"
        "v_rcp_f32 v64, v64\n"
        "v_mfma_f32_16x16x32_bf16 v[80:83], a[252:255], v[112:115], 0\n"
        "v_mul_f32 v78, v72, v78\n"
        "v_mul_f32 v79, v72, v79\n"
        "v_mfma_f32_16x16x32_bf16 v[80:83], a[252:255], v[116:119], v[80:83]\n"
        "v_cvt_pk_bf16_f32 v76, v76, v77\n"
        "v_cvt_pk_bf16_f32 v77, v78, v79\n"
        "v_mfma_f32_16x16x32_bf16 v[84:87], v[132:135], v[116:119], 0\n"
        "s_add_u32 m0, s41, 14336\n"
        "v_mul_f32 v104, v72, v104\n"
        "v_mul_f32 v105, v72, v105\n"
        "v_mfma_f32_16x16x32_bf16 v[84:87], v[128:131], v[112:115], v[84:87]\n"
        "v_mul_f32 v106, v72, v106\n"
        "v_mul_f32 v107, v72, v107\n"
        "v_mfma_f32_16x16x32_bf16 v[222:225], v[136:139], v[112:115], 0\n"
        "buffer_load_dwordx4 v244, s[8:11], s35 offen lds\n"
        "v_mfma_f32_16x16x32_bf16 v[222:225], v[140:143], v[116:119], v[222:225]\n"
        "ds_read_b128 a[224:227], v240 offset:0\n"
        "ds_read_b128 a[228:231], v241 offset:0\n"
        "v_mfma_f32_16x16x32_bf16 v[162:165], v[148:151], v[116:119], 0\n"
        "ds_read_b128 a[232:235], v240 offset:128\n"
        "v_cvt_pk_bf16_f32 v78, v104, v105\n"
        "v_mfma_f32_16x16x32_bf16 v[162:165], v[144:147], v[112:115], v[162:165]\n"
        "v_cvt_pk_bf16_f32 v79, v106, v107\n"
        "v_mul_f32 v108, v72, v108\n"
        "v_mfma_f32_16x16x32_bf16 v[96:99], a[236:239], a[144:147], 0\n"
        "ds_write_b128 v245, v[76:79] offset:256\n"
        "v_mfma_f32_16x16x32_bf16 v[96:99], a[240:243], a[148:151], v[96:99]\n"
        "v_mul_f32 v109, v72, v109\n"
        "v_mul_f32 v110, v72, v110\n"
        "v_mfma_f32_16x16x32_bf16 v[96:99], a[244:247], a[152:155], v[96:99]\n"
        "v_mul_f32 v111, v72, v111\n"
        "v_cvt_pk_bf16_f32 v108, v108, v109\n"
        "v_mfma_f32_16x16x32_bf16 v[96:99], a[248:251], a[156:159], v[96:99]\n"
        "v_cvt_pk_bf16_f32 v109, v110, v111\n"
        "v_mul_f32 v68, v64, v68\n"
        "v_mfma_f32_16x16x32_bf16 v[96:99], a[216:219], a[160:163], v[96:99]\n"
        "v_rcp_f32 v80, v80\n"
        "v_mfma_f32_16x16x32_bf16 v[96:99], a[220:223], a[164:167], v[96:99]\n"
        "ds_write_b64 v246, v[108:109] offset:256\n"
        "v_mfma_f32_16x16x32_bf16 v[100:103], a[236:239], a[168:171], 0\n"
        "v_mul_f32 v69, v64, v69\n"
        "v_mul_f32 v70, v64, v70\n"
        "v_mfma_f32_16x16x32_bf16 v[100:103], a[240:243], a[172:175], v[100:103]\n"
        "v_mul_f32 v71, v64, v71\n"
        "v_cvt_pk_bf16_f32 v68, v68, v69\n"
        "v_mfma_f32_16x16x32_bf16 v[100:103], a[244:247], a[176:179], v[100:103]\n"
        "v_cvt_pk_bf16_f32 v134, v96, v97\n"
        "v_cvt_pk_bf16_f32 v135, v98, v99\n"
        "v_mfma_f32_16x16x32_bf16 v[100:103], a[248:251], a[180:183], v[100:103]\n"
        "v_cvt_pk_bf16_f32 v69, v70, v71\n"
        "v_mul_f32 v120, v64, v120\n"
        "v_mfma_f32_16x16x32_bf16 v[100:103], a[216:219], a[184:187], v[100:103]\n"
        "v_mul_f32 v121, v64, v121\n"
        "v_mul_f32 v122, v64, v122\n"
        "v_mfma_f32_16x16x32_bf16 v[100:103], a[220:223], a[188:191], v[100:103]\n"
        "v_mul_f32 v123, v64, v123\n"
        "v_cvt_pk_bf16_f32 v70, v120, v121\n"
        "v_mfma_f32_16x16x32_bf16 v[112:115], a[236:239], a[192:195], 0\n"
        "ds_read_b128 a[236:239], v241 offset:128\n"
        "v_cvt_pk_bf16_f32 v71, v122, v123\n"
        "v_mfma_f32_16x16x32_bf16 v[112:115], a[240:243], a[196:199], v[112:115]\n"
        "ds_read_b128 a[240:243], v240 offset:256\n"
        "v_mul_f32 v124, v64, v124\n"
        "v_mfma_f32_16x16x32_bf16 v[112:115], a[244:247], a[200:203], v[112:115]\n"
        "ds_read_b128 a[244:247], v241 offset:256\n"
        "v_cvt_pk_bf16_f32 v142, v100, v101\n"
        "v_mfma_f32_16x16x32_bf16 v[112:115], a[248:251], a[204:207], v[112:115]\n"
        "ds_read_b128 a[248:251], v240 offset:1536\n"
        "v_cvt_pk_bf16_f32 v143, v102, v103\n"
        "v_mfma_f32_16x16x32_bf16 v[112:115], a[216:219], a[208:211], v[112:115]\n"
        "ds_read_b128 a[216:219], v241 offset:1536\n"
        "v_mul_f32 v125, v64, v125\n"
        "v_mfma_f32_16x16x32_bf16 v[112:115], a[220:223], a[212:215], v[112:115]\n"
        "s_waitcnt lgkmcnt(9)\n"
        "ds_read_b128 a[220:223], v240 offset:1664\n"
        "v_mul_f32 v126, v64, v126\n"
        "v_mfma_f32_16x16x32_bf16 v[96:99], a[224:227], a[144:147], 0\n"
        "s_waitcnt lgkmcnt(9)\n"
        "ds_write_b128 v245, v[68:71] offset:512\n"
        "v_mfma_f32_16x16x32_bf16 v[96:99], a[228:231], a[148:151], v[96:99]\n"
        "s_waitcnt lgkmcnt(9)\n"
        "v_mul_f32 v127, v64, v127\n"
        "v_cvt_pk_bf16_f32 v124, v124, v125\n"
        "v_mfma_f32_16x16x32_bf16 v[96:99], a[232:235], a[152:155], v[96:99]\n"
        "s_waitcnt lgkmcnt(6)\n"
        "v_cvt_pk_bf16_f32 v150, v112, v113\n"
        "v_cvt_pk_bf16_f32 v151, v114, v115\n"
        "v_mfma_f32_16x16x32_bf16 v[96:99], a[236:239], a[156:159], v[96:99]\n"
        "s_waitcnt lgkmcnt(5)\n"
        "v_cvt_pk_bf16_f32 v125, v126, v127\n"
        "v_mul_f32 v84, v80, v84\n"
        "v_mfma_f32_16x16x32_bf16 v[96:99], a[240:243], a[160:163], v[96:99]\n"
        "s_waitcnt lgkmcnt(4)\n"
        "v_mul_f32 v85, v80, v85\n"
        "v_mul_f32 v86, v80, v86\n"
        "v_mfma_f32_16x16x32_bf16 v[96:99], a[244:247], a[164:167], v[96:99]\n"
        "ds_write_b64 v246, v[124:125] offset:512\n"
        "v_mfma_f32_16x16x32_bf16 v[100:103], a[224:227], a[168:171], 0\n"
        "v_mul_f32 v87, v80, v87\n"
        "v_cvt_pk_bf16_f32 v84, v84, v85\n"
        "v_mfma_f32_16x16x32_bf16 v[100:103], a[228:231], a[172:175], v[100:103]\n"
        "v_cvt_pk_bf16_f32 v85, v86, v87\n"
        "v_mul_f32 v222, v80, v222\n"
        "v_mfma_f32_16x16x32_bf16 v[100:103], a[232:235], a[176:179], v[100:103]\n"
        "v_cvt_pk_bf16_f32 v128, v96, v97\n"
        "v_cvt_pk_bf16_f32 v129, v98, v99\n"
        "v_mfma_f32_16x16x32_bf16 v[100:103], a[236:239], a[180:183], v[100:103]\n"
        "v_mul_f32 v223, v80, v223\n"
        "v_mul_f32 v224, v80, v224\n"
        "v_mfma_f32_16x16x32_bf16 v[100:103], a[240:243], a[184:187], v[100:103]\n"
        "v_mul_f32 v225, v80, v225\n"
        "v_cvt_pk_bf16_f32 v86, v222, v223\n"
        "v_mfma_f32_16x16x32_bf16 v[100:103], a[244:247], a[188:191], v[100:103]\n"
        "v_cvt_pk_bf16_f32 v87, v224, v225\n"
        "v_mul_f32 v162, v80, v162\n"
        "v_mfma_f32_16x16x32_bf16 v[112:115], a[224:227], a[192:195], 0\n"
        "ds_read_b128 a[224:227], v241 offset:1664\n"
        "v_mul_f32 v163, v80, v163\n"
        "v_mfma_f32_16x16x32_bf16 v[112:115], a[228:231], a[196:199], v[112:115]\n"
        "ds_read_b128 a[228:231], v240 offset:1792\n"
        "v_mul_f32 v164, v80, v164\n"
        "v_mfma_f32_16x16x32_bf16 v[112:115], a[232:235], a[200:203], v[112:115]\n"
        "ds_read_b128 a[232:235], v241 offset:1792\n"
        "v_cvt_pk_bf16_f32 v136, v100, v101\n"
        "v_mfma_f32_16x16x32_bf16 v[112:115], a[236:239], a[204:207], v[112:115]\n"
        "ds_read_b128 a[236:239], v240 offset:12288\n"
        "v_cvt_pk_bf16_f32 v137, v102, v103\n"
        "v_mfma_f32_16x16x32_bf16 v[112:115], a[240:243], a[208:211], v[112:115]\n"
        "ds_read_b128 a[240:243], v241 offset:12288\n"
        "v_mul_f32 v165, v80, v165\n"
        "v_mfma_f32_16x16x32_bf16 v[112:115], a[244:247], a[212:215], v[112:115]\n"
        "s_waitcnt lgkmcnt(9)\n"
        "ds_read_b128 a[244:247], v240 offset:12416\n"
        "v_cvt_pk_bf16_f32 v162, v162, v163\n"
        "v_mfma_f32_16x16x32_bf16 v[96:99], a[248:251], a[144:147], 0\n"
        "s_waitcnt lgkmcnt(9)\n"
        "ds_write_b128 v245, v[84:87] offset:768\n"
        "v_mfma_f32_16x16x32_bf16 v[96:99], a[216:219], a[148:151], v[96:99]\n"
        "s_waitcnt lgkmcnt(9)\n"
        "v_cvt_pk_bf16_f32 v163, v164, v165\n"
        "v_mfma_f32_16x16x32_bf16 v[96:99], a[220:223], a[152:155], v[96:99]\n"
        "s_waitcnt lgkmcnt(6)\n"
        "v_mfma_f32_16x16x32_bf16 v[96:99], a[224:227], a[156:159], v[96:99]\n"
        "s_waitcnt lgkmcnt(5)\n"
        "v_cvt_pk_bf16_f32 v144, v112, v113\n"
        "v_cvt_pk_bf16_f32 v145, v114, v115\n"
        "v_mfma_f32_16x16x32_bf16 v[96:99], a[228:231], a[160:163], v[96:99]\n"
        "s_waitcnt lgkmcnt(4)\n"
        "v_mfma_f32_16x16x32_bf16 v[96:99], a[232:235], a[164:167], v[96:99]\n"
        "ds_write_b64 v246, v[162:163] offset:768\n"
        "v_mfma_f32_16x16x32_bf16 v[100:103], a[248:251], a[168:171], 0\n"
        "v_mfma_f32_16x16x32_bf16 v[100:103], a[216:219], a[172:175], v[100:103]\n"
        "v_mfma_f32_16x16x32_bf16 v[100:103], a[220:223], a[176:179], v[100:103]\n"
        "v_mfma_f32_16x16x32_bf16 v[100:103], a[224:227], a[180:183], v[100:103]\n"
        "v_mfma_f32_16x16x32_bf16 v[100:103], a[228:231], a[184:187], v[100:103]\n"
        "v_mfma_f32_16x16x32_bf16 v[100:103], a[232:235], a[188:191], v[100:103]\n"
        "v_mfma_f32_16x16x32_bf16 v[112:115], a[248:251], a[192:195], 0\n"
        "ds_read_b128 a[248:251], v241 offset:12416\n"
        "ds_read_b128 v[162:165], v251 offset:0\n"
        "v_mfma_f32_16x16x32_bf16 v[112:115], a[216:219], a[196:199], v[112:115]\n"
        "ds_read_b128 a[216:219], v240 offset:12544\n"
        "v_cvt_pk_bf16_f32 v130, v96, v97\n"
        "v_mfma_f32_16x16x32_bf16 v[112:115], a[220:223], a[200:203], v[112:115]\n"
        "ds_read_b128 a[220:223], v241 offset:12544\n"
        "v_cvt_pk_bf16_f32 v131, v98, v99\n"
        "v_mfma_f32_16x16x32_bf16 v[112:115], a[224:227], a[204:207], v[112:115]\n"
        "v_cvt_pk_bf16_f32 v138, v100, v101\n"
        "v_cvt_pk_bf16_f32 v139, v102, v103\n"
        "v_mfma_f32_16x16x32_bf16 v[112:115], a[228:231], a[208:211], v[112:115]\n"
        "ds_read_b128 v[182:185], v251 offset:512\n"
        "v_mfma_f32_16x16x32_bf16 v[112:115], a[232:235], a[212:215], v[112:115]\n"
        "s_waitcnt lgkmcnt(9)\n"
        "v_mfma_f32_16x16x32_bf16 v[96:99], a[236:239], a[144:147], 0\n"
        "s_waitcnt lgkmcnt(8)\n"
        "v_mfma_f32_16x16x32_bf16 v[96:99], a[240:243], a[148:151], v[96:99]\n"
        "s_waitcnt lgkmcnt(7)\n"
        "v_mfma_f32_16x16x32_bf16 v[96:99], a[244:247], a[152:155], v[96:99]\n"
        "s_waitcnt lgkmcnt(4)\n"
        "ds_read_b128 v[202:205], v251 offset:4096\n"
        "v_mfma_f32_16x16x32_bf16 v[96:99], a[248:251], a[156:159], v[96:99]\n"
        "s_waitcnt lgkmcnt(3)\n"
        "v_mfma_f32_16x16x32_bf16 v[96:99], a[216:219], a[160:163], v[96:99]\n"
        "s_waitcnt lgkmcnt(2)\n"
        "v_mfma_f32_16x16x32_bf16 v[96:99], a[220:223], a[164:167], v[96:99]\n"
        "s_add_u32 s47, s47, 6291456\n"
        "v_cvt_pk_bf16_f32 v146, v112, v113\n"
        "v_cvt_pk_bf16_f32 v147, v114, v115\n"
        "v_mfma_f32_16x16x32_bf16 v[100:103], a[236:239], a[168:171], 0\n"
        "ds_read_b128 v[222:225], v251 offset:4608\n"
        "s_add_u32 s36, s36, 2097152\n"
        "v_mfma_f32_16x16x32_bf16 v[100:103], a[240:243], a[172:175], v[100:103]\n"
        "s_xor_b32 s48, s48, 8192\n"
        "v_cvt_pk_bf16_f32 v132, v96, v97\n"
        "v_cvt_pk_bf16_f32 v133, v98, v99\n"
        "v_mfma_f32_16x16x32_bf16 v[100:103], a[244:247], a[176:179], v[100:103]\n"
        "v_pk_min_u16 v162, v162, s26\n"
        "v_pk_min_u16 v163, v163, s26\n"
        "v_mfma_f32_16x16x32_bf16 v[100:103], a[248:251], a[180:183], v[100:103]\n"
        "v_pk_min_u16 v164, v164, s26\n"
        "v_pk_min_u16 v165, v165, s26\n"
        "v_mfma_f32_16x16x32_bf16 v[100:103], a[216:219], a[184:187], v[100:103]\n"
        "v_add_u32 v240, s31, v240\n"
        "v_add_u32 v241, s31, v241\n"
        "v_mfma_f32_16x16x32_bf16 v[100:103], a[220:223], a[188:191], v[100:103]\n"
        "v_mfma_f32_16x16x32_bf16 v[112:115], a[236:239], a[192:195], 0\n"
        "s_waitcnt lgkmcnt(2)\n"
        "v_pk_min_u16 v182, v182, s26\n"
        "v_pk_min_u16 v183, v183, s26\n"
        "v_mfma_f32_16x16x32_bf16 v[112:115], a[240:243], a[196:199], v[112:115]\n"
        "v_pk_min_u16 v184, v184, s26\n"
        "v_pk_min_u16 v185, v185, s26\n"
        "v_mfma_f32_16x16x32_bf16 v[112:115], a[244:247], a[200:203], v[112:115]\n"
        "v_mfma_f32_16x16x32_bf16 v[112:115], a[248:251], a[204:207], v[112:115]\n"
        "v_cvt_pk_bf16_f32 v140, v100, v101\n"
        "v_cvt_pk_bf16_f32 v141, v102, v103\n"
        "v_mfma_f32_16x16x32_bf16 v[112:115], a[216:219], a[208:211], v[112:115]\n"
        "s_waitcnt lgkmcnt(1)\n"
        "v_pk_min_u16 v202, v202, s26\n"
        "v_pk_min_u16 v203, v203, s26\n"
        "v_mfma_f32_16x16x32_bf16 v[112:115], a[220:223], a[212:215], v[112:115]\n"
        "v_pk_min_u16 v204, v204, s26\n"
        "v_pk_min_u16 v205, v205, s26\n"
        "v_mfma_f32_16x16x32_bf16 v[64:67], v[152:155], v[156:159], v[0:3]\n"
        "v_mfma_f32_16x16x32_bf16 v[64:67], v[168:171], v[160:163], v[64:67]\n"
        "v_mfma_f32_16x16x32_bf16 v[68:71], v[172:175], v[156:159], v[4:7]\n"
        "s_waitcnt lgkmcnt(0)\n"
        "v_pk_min_u16 v222, v222, s26\n"
        "v_pk_min_u16 v223, v223, s26\n"
        "v_mfma_f32_16x16x32_bf16 v[68:71], v[188:191], v[164:167], v[68:71]\n"
        "ds_read_b128 v[162:165], v252 offset:0\n"
        "v_cvt_pk_bf16_f32 v148, v112, v113\n"
        "v_mfma_f32_16x16x32_bf16 v[80:83], v[152:155], v[176:179], v[16:19]\n"
        "v_cvt_pk_bf16_f32 v149, v114, v115\n"
        "v_pk_min_u16 v224, v224, s26\n"
        "v_mfma_f32_16x16x32_bf16 v[80:83], v[168:171], v[180:183], v[80:83]\n"
        "v_pk_min_u16 v225, v225, s26\n"
        "v_max3_f32 v232, v64, v65, v66\n"
        "v_mfma_f32_16x16x32_bf16 v[84:87], v[172:175], v[176:179], v[20:23]\n"
        "v_max3_f32 v232, v232, v67, v68\n"
        "v_max3_f32 v232, v232, v69, v70\n"
        "v_mfma_f32_16x16x32_bf16 v[84:87], v[188:191], v[184:187], v[84:87]\n"
        "ds_read_b128 v[182:185], v252 offset:512\n"
        "v_mfma_f32_16x16x32_bf16 v[96:99], v[152:155], v[196:199], v[32:35]\n"
        "v_max3_f32 v233, v80, v81, v82\n"
        "v_mfma_f32_16x16x32_bf16 v[96:99], v[168:171], v[200:203], v[96:99]\n"
        "v_mfma_f32_16x16x32_bf16 v[100:103], v[172:175], v[196:199], v[36:39]\n"
        "v_mfma_f32_16x16x32_bf16 v[100:103], v[188:191], v[204:207], v[100:103]\n"
        "ds_read_b128 v[202:205], v252 offset:4096\n"
        "v_mfma_f32_16x16x32_bf16 v[112:115], v[152:155], v[216:219], v[48:51]\n"
        "s_waitcnt lgkmcnt(2)\n"
        "v_pk_min_u16 v162, v162, s26\n"
        "v_pk_min_u16 v163, v163, s26\n"
        "v_mfma_f32_16x16x32_bf16 v[112:115], v[168:171], v[220:223], v[112:115]\n"
        "v_pk_min_u16 v164, v164, s26\n"
        "v_pk_min_u16 v165, v165, s26\n"
        "v_mfma_f32_16x16x32_bf16 v[116:119], v[172:175], v[216:219], v[52:55]\n"
        "v_max3_f32 v233, v233, v83, v84\n"
        "v_max3_f32 v233, v233, v85, v86\n"
        "v_mfma_f32_16x16x32_bf16 v[116:119], v[188:191], v[224:227], v[116:119]\n"
        "ds_read_b128 v[222:225], v252 offset:4608\n"
        "v_max3_f32 v234, v96, v97, v98\n"
        "v_mfma_f32_16x16x32_bf16 v[72:75], v[192:195], v[156:159], v[8:11]\n"
        "s_waitcnt lgkmcnt(2)\n"
        "v_pk_min_u16 v182, v182, s26\n"
        "v_pk_min_u16 v183, v183, s26\n"
        "v_mfma_f32_16x16x32_bf16 v[72:75], v[208:211], v[160:163], v[72:75]\n"
        "v_pk_min_u16 v184, v184, s26\n"
        "v_pk_min_u16 v185, v185, s26\n"
        "v_mfma_f32_16x16x32_bf16 v[76:79], v[212:215], v[156:159], v[12:15]\n"
        "v_max3_f32 v234, v234, v99, v100\n"
        "v_max3_f32 v234, v234, v101, v102\n"
        "v_mfma_f32_16x16x32_bf16 v[76:79], v[228:231], v[164:167], v[76:79]\n"
        "v_max3_f32 v235, v112, v113, v114\n"
        "v_max3_f32 v235, v235, v115, v116\n"
        "v_mfma_f32_16x16x32_bf16 v[88:91], v[192:195], v[176:179], v[24:27]\n"
        "s_barrier\n"
        "ds_read_b128 v[152:155], v247 offset:0\n"
        "ds_read_b128 v[160:163], v247 offset:4096\n"
        "v_mfma_f32_16x16x32_bf16 v[88:91], v[208:211], v[180:183], v[88:91]\n"
        "ds_read_b128 v[156:159], v247 offset:256\n"
        "ds_read_b128 v[164:167], v247 offset:4352\n"
        "v_mfma_f32_16x16x32_bf16 v[92:95], v[212:215], v[176:179], v[28:31]\n"
        "ds_read_b128 a[216:219], v248 offset:0\n"
        "ds_read_b128 a[220:223], v248 offset:6144\n"
        "v_mfma_f32_16x16x32_bf16 v[92:95], v[228:231], v[184:187], v[92:95]\n"
        "s_waitcnt lgkmcnt(7)\n"
        "v_pk_min_u16 v202, v202, s26\n"
        "v_pk_min_u16 v203, v203, s26\n"
        "v_mfma_f32_16x16x32_bf16 v[104:107], v[192:195], v[196:199], v[40:43]\n"
        "ds_read_b128 a[224:227], v254 offset:16\n"
        "ds_read_b128 a[228:231], v254 offset:6160\n"
        "v_mfma_f32_16x16x32_bf16 v[104:107], v[208:211], v[200:203], v[104:107]\n"
        "v_pk_min_u16 v204, v204, s26\n"
        "v_pk_min_u16 v205, v205, s26\n"
        "v_mfma_f32_16x16x32_bf16 v[108:111], v[212:215], v[196:199], v[44:47]\n"
        "s_waitcnt lgkmcnt(8)\n"
        "v_pk_min_u16 v222, v222, s26\n"
        "v_pk_min_u16 v223, v223, s26\n"
        "v_mfma_f32_16x16x32_bf16 v[108:111], v[228:231], v[204:207], v[108:111]\n"
        "v_pk_min_u16 v224, v224, s26\n"
        "v_pk_min_u16 v225, v225, s26\n"
        "v_mfma_f32_16x16x32_bf16 v[120:123], v[192:195], v[216:219], v[56:59]\n"
        "ds_read_b128 a[232:235], v248 offset:32\n"
        "v_max3_f32 v232, v232, v71, v72\n"
        "v_mfma_f32_16x16x32_bf16 v[120:123], v[208:211], v[220:223], v[120:123]\n"
        "v_max3_f32 v232, v232, v73, v74\n"
        "v_max3_f32 v232, v232, v75, v76\n"
        "v_mfma_f32_16x16x32_bf16 v[124:127], v[212:215], v[216:219], v[60:63]\n"
        "v_max3_f32 v232, v232, v77, v78\n"
        "v_max_f32 v232, v232, v79\n"
        "v_mfma_f32_16x16x32_bf16 v[124:127], v[228:231], v[224:227], v[124:127]\n"
        "s_waitcnt lgkmcnt(4)\n"
        "v_max3_f32 v233, v233, v87, v88\n"
        "v_max3_f32 v233, v233, v89, v90\n"
        "v_mfma_f32_16x16x32_bf16 v[172:175], v[152:155], a[216:219], 0\n"
        "s_waitcnt lgkmcnt(3)\n"
        "v_max3_f32 v233, v233, v91, v92\n"
        "v_max3_f32 v233, v233, v93, v94\n"
        "v_mfma_f32_16x16x32_bf16 v[172:175], v[160:163], a[220:223], v[172:175]\n"
        "v_max_f32 v233, v233, v95\n"
        "v_max3_f32 v234, v234, v103, v104\n"
        "v_mfma_f32_16x16x32_bf16 v[180:183], v[164:167], a[220:223], 0\n"
        "ds_read_b128 a[220:223], v248 offset:6176\n"
        "ds_read_b128 a[236:239], v247 offset:8192\n"
        "v_mfma_f32_16x16x32_bf16 v[180:183], v[156:159], a[216:219], v[180:183]\n"
        "s_waitcnt lgkmcnt(4)\n"
        "ds_read_b128 a[216:219], v254 offset:48\n"
        "ds_read_b128 a[244:247], v247 offset:12288\n"
        "v_mfma_f32_16x16x32_bf16 v[184:187], v[156:159], a[224:227], 0\n"
        "s_waitcnt lgkmcnt(5)\n"
        "ds_read_b128 a[240:243], v247 offset:8448\n"
        "ds_read_b128 a[248:251], v247 offset:12544\n"
        "v_mfma_f32_16x16x32_bf16 v[184:187], v[164:167], a[228:231], v[184:187]\n"
        "v_permlane32_swap_b32 v232, v233\n"
        "v_mfma_f32_16x16x32_bf16 v[176:179], v[160:163], a[228:231], 0\n"
        "ds_read_b128 a[228:231], v254 offset:6192\n"
        "v_max_f32 v232, v232, v233\n"
        "v_mfma_f32_16x16x32_bf16 v[176:179], v[152:155], a[224:227], v[176:179]\n"
        "s_waitcnt lgkmcnt(7)\n"
        "ds_read_b128 a[224:227], v248 offset:64\n"
        "v_mov_b32 v233, v232\n"
        "v_mfma_f32_16x16x32_bf16 v[212:215], v[152:155], a[232:235], 0\n"
        "s_waitcnt lgkmcnt(7)\n"
        "v_max3_f32 v234, v234, v105, v106\n"
        "v_max3_f32 v234, v234, v107, v108\n"
        "v_mfma_f32_16x16x32_bf16 v[212:215], v[160:163], a[220:223], v[212:215]\n"
        "v_permlane16_swap_b32 v232, v233\n"
        "v_mfma_f32_16x16x32_bf16 v[192:195], v[164:167], a[220:223], 0\n"
        "ds_read_b128 a[220:223], v248 offset:6208\n"
        "v_max_f32 v232, v232, v233\n"
        "v_mfma_f32_16x16x32_bf16 v[192:195], v[156:159], a[232:235], v[192:195]\n"
        "s_waitcnt lgkmcnt(6)\n"
        "ds_read_b128 a[232:235], v254 offset:80\n"
        "v_mul_f32 v236, s25, v232\n"
        "v_mfma_f32_16x16x32_bf16 v[196:199], v[156:159], a[216:219], 0\n"
        "s_waitcnt lgkmcnt(3)\n"
        "v_mul_f32 v237, s25, v232\n"
        "v_max3_f32 v234, v234, v109, v110\n"
        "v_mfma_f32_16x16x32_bf16 v[196:199], v[164:167], a[228:231], v[196:199]\n"
        "v_permlane32_swap_b32 v236, v237\n"
        "v_mfma_f32_16x16x32_bf16 v[216:219], v[160:163], a[228:231], 0\n"
        "ds_read_b128 a[228:231], v254 offset:6224\n"
        "v_max_f32 v234, v234, v111\n"
        "v_mfma_f32_16x16x32_bf16 v[216:219], v[152:155], a[216:219], v[216:219]\n"
        "s_waitcnt lgkmcnt(3)\n"
        "ds_read_b128 a[216:219], v248 offset:36864\n"
        "v_max3_f32 v235, v235, v117, v118\n"
        "v_mfma_f32_16x16x32_bf16 v[220:223], v[152:155], a[224:227], 0\n"
        "s_waitcnt lgkmcnt(3)\n"
        "v_max3_f32 v235, v235, v119, v120\n"
        "v_max3_f32 v235, v235, v121, v122\n"
        "v_mfma_f32_16x16x32_bf16 v[220:223], v[160:163], a[220:223], v[220:223]\n"
        "v_max3_f32 v235, v235, v123, v124\n"
        "v_max3_f32 v235, v235, v125, v126\n"
        "v_mfma_f32_16x16x32_bf16 v[200:203], v[164:167], a[220:223], 0\n"
        "ds_read_b128 a[220:223], v248 offset:43008\n"
        "v_max_f32 v235, v235, v127\n"
        "v_mfma_f32_16x16x32_bf16 v[200:203], v[156:159], a[224:227], v[200:203]\n"
        "s_waitcnt lgkmcnt(3)\n"
        "ds_read_b128 a[224:227], v254 offset:36880\n"
        "v_fma_f32 v64, v64, s24, v236\n"
        "v_mfma_f32_16x16x32_bf16 v[204:207], v[156:159], a[232:235], 0\n"
        "s_waitcnt lgkmcnt(3)\n"
        "v_permlane32_swap_b32 v234, v235\n"
        "v_mfma_f32_16x16x32_bf16 v[204:207], v[164:167], a[228:231], v[204:207]\n"
        "v_max_f32 v234, v234, v235\n"
        "v_mov_b32 v235, v234\n"
        "v_mfma_f32_16x16x32_bf16 v[224:227], v[160:163], a[228:231], 0\n"
        "ds_read_b128 a[228:231], v254 offset:43024\n"
        "v_fma_f32 v65, v65, s24, v236\n"
        "v_mfma_f32_16x16x32_bf16 v[224:227], v[152:155], a[232:235], v[224:227]\n"
        "s_waitcnt lgkmcnt(3)\n"
        "ds_read_b128 a[232:235], v248 offset:36896\n"
        "v_fma_f32 v66, v66, s24, v236\n"
        "v_mfma_f32_16x16x32_bf16 v[172:175], a[236:239], a[216:219], v[172:175]\n"
        "s_waitcnt lgkmcnt(3)\n"
        "v_permlane16_swap_b32 v234, v235\n"
        "v_mfma_f32_16x16x32_bf16 v[172:175], a[244:247], a[220:223], v[172:175]\n"
        "v_max_f32 v234, v234, v235\n"
        "v_mul_f32 v238, s25, v234\n"
        "v_mfma_f32_16x16x32_bf16 v[180:183], a[248:251], a[220:223], v[180:183]\n"
        "ds_read_b128 a[220:223], v248 offset:43040\n"
        "ds_read_b128 v[152:155], v247 offset:16384\n"
        "v_mfma_f32_16x16x32_bf16 v[180:183], a[240:243], a[216:219], v[180:183]\n"
        "s_waitcnt lgkmcnt(4)\n"
        "ds_read_b128 a[216:219], v254 offset:36912\n"
        "ds_read_b128 v[160:163], v247 offset:20480\n"
        "v_mfma_f32_16x16x32_bf16 v[184:187], a[240:243], a[224:227], v[184:187]\n"
        "s_waitcnt lgkmcnt(5)\n"
        "ds_read_b128 v[156:159], v247 offset:16640\n"
        "ds_read_b128 v[164:167], v247 offset:20736\n"
        "v_mfma_f32_16x16x32_bf16 v[184:187], a[248:251], a[228:231], v[184:187]\n"
        "v_mul_f32 v239, s25, v234\n"
        "v_fma_f32 v67, v67, s24, v236\n"
        "v_mfma_f32_16x16x32_bf16 v[176:179], a[244:247], a[228:231], v[176:179]\n"
        "ds_read_b128 a[228:231], v254 offset:43056\n"
        "v_fma_f32 v68, v68, s24, v236\n"
        "v_mfma_f32_16x16x32_bf16 v[176:179], a[236:239], a[224:227], v[176:179]\n"
        "s_waitcnt lgkmcnt(7)\n"
        "ds_read_b128 a[224:227], v248 offset:36928\n"
        "v_fma_f32 v69, v69, s24, v236\n"
        "v_mfma_f32_16x16x32_bf16 v[212:215], a[236:239], a[232:235], v[212:215]\n"
        "s_waitcnt lgkmcnt(7)\n"
        "v_permlane32_swap_b32 v238, v239\n"
        "v_mfma_f32_16x16x32_bf16 v[212:215], a[244:247], a[220:223], v[212:215]\n"
        "v_exp_f32 v64, v64\n"
        "v_mfma_f32_16x16x32_bf16 v[192:195], a[248:251], a[220:223], v[192:195]\n"
        "ds_read_b128 a[220:223], v248 offset:43072\n"
        "v_fma_f32 v70, v70, s24, v236\n"
        "v_mfma_f32_16x16x32_bf16 v[192:195], a[240:243], a[232:235], v[192:195]\n"
        "s_waitcnt lgkmcnt(6)\n"
        "ds_read_b128 a[232:235], v254 offset:36944\n"
        "v_fma_f32 v71, v71, s24, v236\n"
        "v_mfma_f32_16x16x32_bf16 v[196:199], a[240:243], a[216:219], v[196:199]\n"
        "s_waitcnt lgkmcnt(3)\n"
        "v_exp_f32 v65, v65\n"
        "v_mfma_f32_16x16x32_bf16 v[196:199], a[248:251], a[228:231], v[196:199]\n"
        "v_exp_f32 v66, v66\n"
        "v_mfma_f32_16x16x32_bf16 v[216:219], a[244:247], a[228:231], v[216:219]\n"
        "ds_read_b128 a[228:231], v254 offset:43088\n"
        "v_cvt_pk_bf16_f32 v64, v64, v65\n"
        "v_mfma_f32_16x16x32_bf16 v[216:219], a[236:239], a[216:219], v[216:219]\n"
        "s_waitcnt lgkmcnt(3)\n"
        "ds_read_b128 a[216:219], v248 offset:12288\n"
        "v_fma_f32 v72, v72, s24, v236\n"
        "v_mfma_f32_16x16x32_bf16 v[220:223], a[236:239], a[224:227], v[220:223]\n"
        "s_waitcnt lgkmcnt(3)\n"
        "v_exp_f32 v67, v67\n"
        "v_mfma_f32_16x16x32_bf16 v[220:223], a[244:247], a[220:223], v[220:223]\n"
        "v_exp_f32 v68, v68\n"
        "v_mfma_f32_16x16x32_bf16 v[200:203], a[248:251], a[220:223], v[200:203]\n"
        "ds_read_b128 a[220:223], v248 offset:49152\n"
        "v_cvt_pk_bf16_f32 v65, v66, v67\n"
        "v_mfma_f32_16x16x32_bf16 v[200:203], a[240:243], a[224:227], v[200:203]\n"
        "s_waitcnt lgkmcnt(3)\n"
        "ds_read_b128 a[224:227], v254 offset:12304\n"
        "v_fma_f32 v73, v73, s24, v236\n"
        "v_mfma_f32_16x16x32_bf16 v[204:207], a[240:243], a[232:235], v[204:207]\n"
        "s_waitcnt lgkmcnt(3)\n"
        "v_exp_f32 v69, v69\n"
        "v_mfma_f32_16x16x32_bf16 v[204:207], a[248:251], a[228:231], v[204:207]\n"
        "v_exp_f32 v70, v70\n"
        "v_mfma_f32_16x16x32_bf16 v[224:227], a[244:247], a[228:231], v[224:227]\n"
        "ds_read_b128 a[228:231], v254 offset:49168\n"
        "v_cvt_pk_bf16_f32 v66, v68, v69\n"
        "v_mfma_f32_16x16x32_bf16 v[224:227], a[236:239], a[232:235], v[224:227]\n"
        "s_waitcnt lgkmcnt(3)\n"
        "ds_read_b128 a[232:235], v248 offset:12320\n"
        "v_fma_f32 v74, v74, s24, v236\n"
        "v_mfma_f32_16x16x32_bf16 v[172:175], v[152:155], a[216:219], v[172:175]\n"
        "s_waitcnt lgkmcnt(3)\n"
        "v_exp_f32 v71, v71\n"
        "v_mfma_f32_16x16x32_bf16 v[172:175], v[160:163], a[220:223], v[172:175]\n"
        "v_exp_f32 v72, v72\n"
        "v_mfma_f32_16x16x32_bf16 v[180:183], v[164:167], a[220:223], v[180:183]\n"
        "ds_read_b128 a[220:223], v248 offset:49184\n"
        "v_cvt_pk_bf16_f32 v67, v70, v71\n"
        "v_mfma_f32_16x16x32_bf16 v[180:183], v[156:159], a[216:219], v[180:183]\n"
        "s_waitcnt lgkmcnt(3)\n"
        "ds_read_b128 a[216:219], v254 offset:12336\n"
        "v_fma_f32 v75, v75, s24, v236\n"
        "v_mfma_f32_16x16x32_bf16 v[184:187], v[156:159], a[224:227], v[184:187]\n"
        "s_waitcnt lgkmcnt(3)\n"
        "v_exp_f32 v73, v73\n"
        "v_mfma_f32_16x16x32_bf16 v[184:187], v[164:167], a[228:231], v[184:187]\n"
        "v_cvt_pk_bf16_f32 v68, v72, v73\n"
        "v_xor_b32 v251, 0x2000, v251\n"
        "v_mfma_f32_16x16x32_bf16 v[176:179], v[160:163], a[228:231], v[176:179]\n"
        "ds_read_b128 a[228:231], v254 offset:49200\n"
        "v_xor_b32 v252, 0x2000, v252\n"
        "v_mfma_f32_16x16x32_bf16 v[176:179], v[152:155], a[224:227], v[176:179]\n"
        "s_waitcnt lgkmcnt(3)\n"
        "ds_read_b128 a[224:227], v248 offset:12352\n"
        "v_add_u32 v245, s45, v245\n"
        "v_mfma_f32_16x16x32_bf16 v[212:215], v[152:155], a[232:235], v[212:215]\n"
        "s_waitcnt lgkmcnt(3)\n"
        "s_waitcnt vmcnt(0)\n"
        "s_barrier\n"
        "v_exp_f32 v74, v74\n"
        "v_mfma_f32_16x16x32_bf16 v[212:215], v[160:163], a[220:223], v[212:215]\n"
        "v_exp_f32 v75, v75\n"
        "v_mfma_f32_16x16x32_bf16 v[192:195], v[164:167], a[220:223], v[192:195]\n"
        "ds_read_b128 a[220:223], v248 offset:49216\n"
        "ds_read_b128 a[236:239], v240 offset:0\n"
        "v_mfma_f32_16x16x32_bf16 v[192:195], v[156:159], a[232:235], v[192:195]\n"
        "s_waitcnt lgkmcnt(4)\n"
        "ds_read_b128 a[232:235], v254 offset:12368\n"
        "ds_read_b128 a[240:243], v241 offset:0\n"
        "v_mfma_f32_16x16x32_bf16 v[196:199], v[156:159], a[216:219], v[196:199]\n"
        "s_waitcnt lgkmcnt(5)\n"
        "ds_read_b128 a[244:247], v240 offset:128\n"
        "v_cvt_pk_bf16_f32 v69, v74, v75\n"
        "v_mfma_f32_16x16x32_bf16 v[196:199], v[164:167], a[228:231], v[196:199]\n"
        "ds_read_b128 a[248:251], v241 offset:128\n"
        "v_add_u32 v246, s45, v246\n"
        "v_mfma_f32_16x16x32_bf16 v[216:219], v[160:163], a[228:231], v[216:219]\n"
        "ds_read_b128 a[228:231], v254 offset:49232\n"
        "v_add_u32 v247, s45, v247\n"
        "v_mfma_f32_16x16x32_bf16 v[216:219], v[152:155], a[216:219], v[216:219]\n"
        "s_waitcnt lgkmcnt(7)\n"
        "ds_read_b128 a[216:219], v240 offset:256\n"
        "v_mfma_f32_16x16x32_bf16 v[220:223], v[152:155], a[224:227], v[220:223]\n"
        "s_waitcnt lgkmcnt(7)\n"
        "v_mfma_f32_16x16x32_bf16 v[220:223], v[160:163], a[220:223], v[220:223]\n"
        "v_mfma_f32_16x16x32_bf16 v[200:203], v[164:167], a[220:223], v[200:203]\n"
        "ds_read_b128 a[220:223], v241 offset:256\n"
        "v_mfma_f32_16x16x32_bf16 v[200:203], v[156:159], a[224:227], v[200:203]\n"
        "s_waitcnt lgkmcnt(6)\n"
        "v_mfma_f32_16x16x32_bf16 v[204:207], v[156:159], a[232:235], v[204:207]\n"
        "s_waitcnt lgkmcnt(2)\n"
        "v_mfma_f32_16x16x32_bf16 v[204:207], v[164:167], a[228:231], v[204:207]\n"
        "v_mfma_f32_16x16x32_bf16 v[224:227], v[160:163], a[228:231], v[224:227]\n"
        "v_mfma_f32_16x16x32_bf16 v[224:227], v[152:155], a[232:235], v[224:227]\n"
        "s_waitcnt lgkmcnt(0)\n"
        ".p2align 6\n"
        "10:\n"
        "v_mfma_f32_16x16x32_bf16 v[162:165], a[72:75], a[236:239], 0\n"
        "v_fma_f32 v76, v76, s24, v236\n"
        "v_fma_f32 v77, v77, s24, v236\n"
        "v_mfma_f32_16x16x32_bf16 v[162:165], a[76:79], a[240:243], v[162:165]\n"
        "s_waitcnt lgkmcnt(3)\n"
        "s_add_u32 s41, s30, s40\n"
        "v_cvt_pk_bf16_f32 v232, v172, v176\n"
        "v_cvt_pk_bf16_f32 v233, v212, v216\n"
        "v_mfma_f32_16x16x32_bf16 v[162:165], a[80:83], a[244:247], v[162:165]\n"
        "s_waitcnt lgkmcnt(2)\n"
        "v_cvt_pk_bf16_f32 v234, v220, v224\n"
        "v_fma_f32 v78, v78, s24, v236\n"
        "v_mfma_f32_16x16x32_bf16 v[162:165], a[84:87], a[248:251], v[162:165]\n"
        "s_waitcnt lgkmcnt(1)\n"
        "buffer_store_dwordx3 v[232:234], v250, s[16:19], s38 offen offset:0 sc1\n"
        "v_mfma_f32_16x16x32_bf16 v[162:165], a[88:91], a[216:219], v[162:165]\n"
        "s_waitcnt lgkmcnt(0)\n"
        "v_exp_f32 v76, v76\n"
        "v_mfma_f32_16x16x32_bf16 v[162:165], a[92:95], a[220:223], v[162:165]\n"
        "v_exp_f32 v77, v77\n"
        "v_mfma_f32_16x16x32_bf16 v[72:75], a[116:119], a[220:223], 0\n"
        "v_fma_f32 v79, v79, s24, v236\n"
        "v_cvt_pk_bf16_f32 v70, v76, v77\n"
        "v_mfma_f32_16x16x32_bf16 v[72:75], a[112:115], a[216:219], v[72:75]\n"
        "v_exp_f32 v78, v78\n"
        "v_mfma_f32_16x16x32_bf16 v[72:75], a[108:111], a[248:251], v[72:75]\n"
        "v_exp_f32 v79, v79\n"
        "v_mfma_f32_16x16x32_bf16 v[72:75], a[104:107], a[244:247], v[72:75]\n"
        "v_cvt_pk_bf16_f32 v71, v78, v79\n"
        "v_cvt_pk_bf16_f32 v232, v173, v177\n"
        "v_mfma_f32_16x16x32_bf16 v[72:75], a[100:103], a[240:243], v[72:75]\n"
        "v_cvt_pk_bf16_f32 v233, v213, v217\n"
        "v_cvt_pk_bf16_f32 v234, v221, v225\n"
        "v_mfma_f32_16x16x32_bf16 v[72:75], a[96:99], a[236:239], v[72:75]\n"
        "buffer_store_dwordx3 v[232:234], v250, s[16:19], s38 offen offset:384 sc1\n"
        "v_mfma_f32_16x16x32_bf16 v[76:79], a[120:123], a[236:239], 0\n"
        "s_add_u32 s35, s47, 786432\n"
        "v_cvt_pk_bf16_f32 v152, v162, v163\n"
        "v_cvt_pk_bf16_f32 v153, v164, v165\n"
        "v_mfma_f32_16x16x32_bf16 v[76:79], a[124:127], a[240:243], v[76:79]\n"
        "s_sub_u32 s46, s35, 100663296\n"
        "v_fma_f32 v80, v80, s24, v237\n"
        "v_cvt_pk_bf16_f32 v154, v72, v73\n"
        "v_mfma_f32_16x16x32_bf16 v[76:79], a[128:131], a[244:247], v[76:79]\n"
        "s_cmp_lt_u32 s46, 786432\n"
        "v_exp_f32 v80, v80\n"
        "v_mfma_f32_16x16x32_bf16 v[76:79], a[132:135], a[248:251], v[76:79]\n"
        "s_cselect_b32 s35, s46, s35\n"
        "v_cvt_pk_bf16_f32 v155, v74, v75\n"
        "v_cvt_pk_bf16_f32 v232, v174, v178\n"
        "v_mfma_f32_16x16x32_bf16 v[76:79], a[136:139], a[216:219], v[76:79]\n"
        "v_cvt_pk_bf16_f32 v233, v214, v218\n"
        "v_cvt_pk_bf16_f32 v234, v222, v226\n"
        "v_mfma_f32_16x16x32_bf16 v[76:79], a[140:143], a[220:223], v[76:79]\n"
        "v_fma_f32 v81, v81, s24, v237\n"
        "v_fma_f32 v82, v82, s24, v237\n"
        "v_mfma_f32_16x16x32_bf16 v[162:165], a[20:23], a[220:223], 0\n"
        "v_exp_f32 v81, v81\n"
        "v_mfma_f32_16x16x32_bf16 v[162:165], a[16:19], a[216:219], v[162:165]\n"
        "v_exp_f32 v82, v82\n"
        "v_mfma_f32_16x16x32_bf16 v[162:165], a[12:15], a[248:251], v[162:165]\n"
        "buffer_store_dwordx3 v[232:234], v250, s[16:19], s38 offen offset:768 sc1\n"
        "v_mfma_f32_16x16x32_bf16 v[162:165], a[8:11], a[244:247], v[162:165]\n"
        "v_cvt_pk_bf16_f32 v168, v76, v77\n"
        "v_cvt_pk_bf16_f32 v169, v78, v79\n"
        "v_mfma_f32_16x16x32_bf16 v[162:165], a[4:7], a[240:243], v[162:165]\n"
        "v_cvt_pk_bf16_f32 v80, v80, v81\n"
        "v_fma_f32 v83, v83, s24, v237\n"
        "v_mfma_f32_16x16x32_bf16 v[162:165], a[0:3], a[236:239], v[162:165]\n"
        "v_exp_f32 v83, v83\n"
        "v_mfma_f32_16x16x32_bf16 v[72:75], a[24:27], a[236:239], 0\n"
        "v_cvt_pk_bf16_f32 v232, v175, v179\n"
        "v_cvt_pk_bf16_f32 v233, v215, v219\n"
        "v_mfma_f32_16x16x32_bf16 v[72:75], a[28:31], a[240:243], v[72:75]\n"
        "v_cvt_pk_bf16_f32 v234, v223, v227\n"
        "v_cvt_pk_bf16_f32 v81, v82, v83\n"
        "v_mfma_f32_16x16x32_bf16 v[72:75], a[32:35], a[244:247], v[72:75]\n"
        "v_cvt_pk_bf16_f32 v156, v162, v163\n"
        "v_cvt_pk_bf16_f32 v157, v164, v165\n"
        "v_mfma_f32_16x16x32_bf16 v[72:75], a[36:39], a[248:251], v[72:75]\n"
        "v_fma_f32 v84, v84, s24, v237\n"
        "v_fma_f32 v85, v85, s24, v237\n"
        "v_mfma_f32_16x16x32_bf16 v[72:75], a[40:43], a[216:219], v[72:75]\n"
        "v_exp_f32 v84, v84\n"
        "v_mfma_f32_16x16x32_bf16 v[72:75], a[44:47], a[220:223], v[72:75]\n"
        "buffer_store_dwordx3 v[232:234], v250, s[16:19], s38 offen offset:1152 sc1\n"
        "v_mfma_f32_16x16x32_bf16 v[76:79], a[68:71], a[220:223], 0\n"
        "v_exp_f32 v85, v85\n"
        "v_mfma_f32_16x16x32_bf16 v[76:79], a[64:67], a[216:219], v[76:79]\n"
        "v_cvt_pk_bf16_f32 v82, v84, v85\n"
        "v_fma_f32 v86, v86, s24, v237\n"
        "v_mfma_f32_16x16x32_bf16 v[76:79], a[60:63], a[248:251], v[76:79]\n"
        "ds_read_b128 a[224:227], v240 offset:1536\n"
        "v_cvt_pk_bf16_f32 v158, v72, v73\n"
        "v_mfma_f32_16x16x32_bf16 v[76:79], a[56:59], a[244:247], v[76:79]\n"
        "ds_read_b128 a[228:231], v241 offset:1536\n"
        "v_cvt_pk_bf16_f32 v159, v74, v75\n"
        "v_mfma_f32_16x16x32_bf16 v[76:79], a[52:55], a[240:243], v[76:79]\n"
        "ds_read_b128 a[232:235], v240 offset:1664\n"
        "v_cvt_pk_bf16_f32 v232, v180, v184\n"
        "v_mfma_f32_16x16x32_bf16 v[76:79], a[48:51], a[236:239], v[76:79]\n"
        "v_cvt_pk_bf16_f32 v233, v192, v196\n"
        "v_cvt_pk_bf16_f32 v234, v200, v204\n"
        "v_mfma_f32_16x16x32_bf16 v[162:165], a[236:239], a[144:147], 0\n"
        "v_exp_f32 v86, v86\n"
        "v_mfma_f32_16x16x32_bf16 v[162:165], a[240:243], a[148:151], v[162:165]\n"
        "v_fma_f32 v87, v87, s24, v237\n"
        "v_fma_f32 v88, v88, s24, v237\n"
        "v_mfma_f32_16x16x32_bf16 v[162:165], a[244:247], a[152:155], v[162:165]\n"
        "buffer_store_dwordx3 v[232:234], v250, s[16:19], s39 offen offset:0 sc1\n"
        "v_mfma_f32_16x16x32_bf16 v[162:165], a[248:251], a[156:159], v[162:165]\n"
        "v_exp_f32 v87, v87\n"
        "v_mfma_f32_16x16x32_bf16 v[162:165], a[216:219], a[160:163], v[162:165]\n"
        "v_cvt_pk_bf16_f32 v160, v76, v77\n"
        "v_cvt_pk_bf16_f32 v161, v78, v79\n"
        "v_mfma_f32_16x16x32_bf16 v[162:165], a[220:223], a[164:167], v[162:165]\n"
        "v_cvt_pk_bf16_f32 v166, v76, v77\n"
        "v_cvt_pk_bf16_f32 v167, v78, v79\n"
        "v_mfma_f32_16x16x32_bf16 v[72:75], a[220:223], a[188:191], 0\n"
        "v_exp_f32 v88, v88\n"
        "v_mfma_f32_16x16x32_bf16 v[72:75], a[216:219], a[184:187], v[72:75]\n"
        "v_cvt_pk_bf16_f32 v232, v181, v185\n"
        "v_cvt_pk_bf16_f32 v233, v193, v197\n"
        "v_mfma_f32_16x16x32_bf16 v[72:75], a[248:251], a[180:183], v[72:75]\n"
        "v_cvt_pk_bf16_f32 v234, v201, v205\n"
        "v_cvt_pk_bf16_f32 v212, v162, v163\n"
        "v_mfma_f32_16x16x32_bf16 v[72:75], a[244:247], a[176:179], v[72:75]\n"
        "v_cvt_pk_bf16_f32 v213, v164, v165\n"
        "v_cvt_pk_bf16_f32 v83, v86, v87\n"
        "v_mfma_f32_16x16x32_bf16 v[72:75], a[240:243], a[172:175], v[72:75]\n"
        "v_fma_f32 v89, v89, s24, v237\n"
        "v_fma_f32 v90, v90, s24, v237\n"
        "v_mfma_f32_16x16x32_bf16 v[72:75], a[236:239], a[168:171], v[72:75]\n"
        "buffer_store_dwordx3 v[232:234], v250, s[16:19], s39 offen offset:384 sc1\n"
        "v_mfma_f32_16x16x32_bf16 v[76:79], a[236:239], a[192:195], 0\n"
        "ds_read_b128 a[236:239], v241 offset:1664\n"
        "v_fma_f32 v91, v91, s24, v237\n"
        "v_mfma_f32_16x16x32_bf16 v[76:79], a[240:243], a[196:199], v[76:79]\n"
        "ds_read_b128 a[240:243], v240 offset:1792\n"
        "v_fma_f32 v92, v92, s24, v237\n"
        "v_mfma_f32_16x16x32_bf16 v[76:79], a[244:247], a[200:203], v[76:79]\n"
        "ds_read_b128 a[244:247], v241 offset:1792\n"
        "v_cvt_pk_bf16_f32 v214, v72, v73\n"
        "v_mfma_f32_16x16x32_bf16 v[76:79], a[248:251], a[204:207], v[76:79]\n"
        "v_exp_f32 v89, v89\n"
        "v_mfma_f32_16x16x32_bf16 v[76:79], a[216:219], a[208:211], v[76:79]\n"
        "v_cvt_pk_bf16_f32 v215, v74, v75\n"
        "v_cvt_pk_bf16_f32 v232, v182, v186\n"
        "v_mfma_f32_16x16x32_bf16 v[76:79], a[220:223], a[212:215], v[76:79]\n"
        "s_waitcnt lgkmcnt(5)\n"
        "v_cvt_pk_bf16_f32 v233, v194, v198\n"
        "v_cvt_pk_bf16_f32 v234, v202, v206\n"
        "v_mfma_f32_16x16x32_bf16 v[162:165], a[72:75], a[224:227], 0\n"
        "s_waitcnt lgkmcnt(4)\n"
        "v_exp_f32 v90, v90\n"
        "v_mfma_f32_16x16x32_bf16 v[162:165], a[76:79], a[228:231], v[162:165]\n"
        "s_waitcnt lgkmcnt(3)\n"
        "v_exp_f32 v91, v91\n"
        "v_mfma_f32_16x16x32_bf16 v[162:165], a[80:83], a[232:235], v[162:165]\n"
        "s_waitcnt lgkmcnt(2)\n"
        "buffer_store_dwordx3 v[232:234], v250, s[16:19], s39 offen offset:768 sc1\n"
        "v_mfma_f32_16x16x32_bf16 v[162:165], a[84:87], a[236:239], v[162:165]\n"
        "s_waitcnt lgkmcnt(1)\n"
        "v_exp_f32 v92, v92\n"
        "v_mfma_f32_16x16x32_bf16 v[162:165], a[88:91], a[240:243], v[162:165]\n"
        "s_waitcnt lgkmcnt(0)\n"
        "v_cvt_pk_bf16_f32 v216, v76, v77\n"
        "v_cvt_pk_bf16_f32 v217, v78, v79\n"
        "v_mfma_f32_16x16x32_bf16 v[162:165], a[92:95], a[244:247], v[162:165]\n"
        "v_cvt_pk_bf16_f32 v232, v183, v187\n"
        "v_cvt_pk_bf16_f32 v233, v195, v199\n"
        "v_mfma_f32_16x16x32_bf16 v[72:75], a[116:119], a[244:247], 0\n"
        "v_cvt_pk_bf16_f32 v234, v203, v207\n"
        "v_cvt_pk_bf16_f32 v84, v88, v89\n"
        "v_mfma_f32_16x16x32_bf16 v[72:75], a[112:115], a[240:243], v[72:75]\n"
        "v_cvt_pk_bf16_f32 v85, v90, v91\n"
        "v_fma_f32 v93, v93, s24, v237\n"
        "v_mfma_f32_16x16x32_bf16 v[72:75], a[108:111], a[236:239], v[72:75]\n"
        "v_exp_f32 v93, v93\n"
        "v_mfma_f32_16x16x32_bf16 v[72:75], a[104:107], a[232:235], v[72:75]\n"
        "v_cvt_pk_bf16_f32 v172, v162, v163\n"
        "v_cvt_pk_bf16_f32 v173, v164, v165\n"
        "v_mfma_f32_16x16x32_bf16 v[72:75], a[100:103], a[228:231], v[72:75]\n"
        "v_cvt_pk_bf16_f32 v86, v92, v93\n"
        "v_fma_f32 v94, v94, s24, v237\n"
        "v_mfma_f32_16x16x32_bf16 v[72:75], a[96:99], a[224:227], v[72:75]\n"
        "buffer_store_dwordx3 v[232:234], v250, s[16:19], s39 offen offset:1152 sc1\n"
        "v_mfma_f32_16x16x32_bf16 v[76:79], a[120:123], a[224:227], 0\n"
        "v_exp_f32 v94, v94\n"
        "v_mfma_f32_16x16x32_bf16 v[76:79], a[124:127], a[228:231], v[76:79]\n"
        "v_fma_f32 v95, v95, s24, v237\n"
        "v_fma_f32 v96, v96, s24, v238\n"
        "v_mfma_f32_16x16x32_bf16 v[76:79], a[128:131], a[232:235], v[76:79]\n"
        "v_exp_f32 v95, v95\n"
        "v_mfma_f32_16x16x32_bf16 v[76:79], a[132:135], a[236:239], v[76:79]\n"
        "v_exp_f32 v96, v96\n"
        "v_mfma_f32_16x16x32_bf16 v[76:79], a[136:139], a[240:243], v[76:79]\n"
        "v_cvt_pk_bf16_f32 v174, v72, v73\n"
        "v_cvt_pk_bf16_f32 v175, v74, v75\n"
        "v_mfma_f32_16x16x32_bf16 v[76:79], a[140:143], a[244:247], v[76:79]\n"
        "v_cvt_pk_bf16_f32 v87, v94, v95\n"
        "v_fma_f32 v97, v97, s24, v238\n"
        "v_mfma_f32_16x16x32_bf16 v[162:165], a[20:23], a[244:247], 0\n"
        "s_mov_b32 m0, s48\n"
        "v_exp_f32 v97, v97\n"
        "v_mfma_f32_16x16x32_bf16 v[162:165], a[16:19], a[240:243], v[162:165]\n"
        "v_cvt_pk_bf16_f32 v96, v96, v97\n"
        "v_fma_f32 v98, v98, s24, v238\n"
        "v_mfma_f32_16x16x32_bf16 v[162:165], a[12:15], a[236:239], v[162:165]\n"
        "buffer_load_dwordx4 v249, s[12:15], s36 offen offset:0 lds\n"
        "v_mfma_f32_16x16x32_bf16 v[162:165], a[8:11], a[232:235], v[162:165]\n"
        "v_exp_f32 v98, v98\n"
        "v_mfma_f32_16x16x32_bf16 v[162:165], a[4:7], a[228:231], v[162:165]\n"
        "v_cvt_pk_bf16_f32 v190, v76, v77\n"
        "v_cvt_pk_bf16_f32 v191, v78, v79\n"
        "v_mfma_f32_16x16x32_bf16 v[162:165], a[0:3], a[224:227], v[162:165]\n"
        "v_fma_f32 v99, v99, s24, v238\n"
        "v_fma_f32 v100, v100, s24, v238\n"
        "v_mfma_f32_16x16x32_bf16 v[72:75], a[24:27], a[224:227], 0\n"
        "v_exp_f32 v99, v99\n"
        "v_mfma_f32_16x16x32_bf16 v[72:75], a[28:31], a[228:231], v[72:75]\n"
        "v_exp_f32 v100, v100\n"
        "v_mfma_f32_16x16x32_bf16 v[72:75], a[32:35], a[232:235], v[72:75]\n"
        "v_cvt_pk_bf16_f32 v97, v98, v99\n"
        "v_cvt_pk_bf16_f32 v176, v162, v163\n"
        "v_mfma_f32_16x16x32_bf16 v[72:75], a[36:39], a[236:239], v[72:75]\n"
        "v_cvt_pk_bf16_f32 v177, v164, v165\n"
        "v_fma_f32 v101, v101, s24, v238\n"
        "v_mfma_f32_16x16x32_bf16 v[72:75], a[40:43], a[240:243], v[72:75]\n"
        "v_exp_f32 v101, v101\n"
        "v_mfma_f32_16x16x32_bf16 v[72:75], a[44:47], a[244:247], v[72:75]\n"
        "v_cvt_pk_bf16_f32 v98, v100, v101\n"
        "v_fma_f32 v102, v102, s24, v238\n"
        "v_mfma_f32_16x16x32_bf16 v[76:79], a[68:71], a[244:247], 0\n"
        "v_xor_b32 v253, 32, v249\n"
        "v_fma_f32 v103, v103, s24, v238\n"
        "v_mfma_f32_16x16x32_bf16 v[76:79], a[64:67], a[240:243], v[76:79]\n"
        "v_exp_f32 v102, v102\n"
        "v_mfma_f32_16x16x32_bf16 v[76:79], a[60:63], a[236:239], v[76:79]\n"
        "ds_read_b128 a[248:251], v240 offset:12288\n"
        "v_cvt_pk_bf16_f32 v178, v72, v73\n"
        "v_mfma_f32_16x16x32_bf16 v[76:79], a[56:59], a[232:235], v[76:79]\n"
        "ds_read_b128 a[216:219], v241 offset:12288\n"
        "v_cvt_pk_bf16_f32 v179, v74, v75\n"
        "v_mfma_f32_16x16x32_bf16 v[76:79], a[52:55], a[228:231], v[76:79]\n"
        "buffer_load_dwordx4 v253, s[12:15], s36 offen offset:1024 lds\n"
        "v_mfma_f32_16x16x32_bf16 v[76:79], a[48:51], a[224:227], v[76:79]\n"
        "ds_read_b128 a[220:223], v240 offset:12416\n"
        "v_fma_f32 v104, v104, s24, v238\n"
        "v_mfma_f32_16x16x32_bf16 v[162:165], a[224:227], a[144:147], 0\n"
        "v_exp_f32 v103, v103\n"
        "v_mfma_f32_16x16x32_bf16 v[162:165], a[228:231], a[148:151], v[162:165]\n"
        "v_exp_f32 v104, v104\n"
        "v_mfma_f32_16x16x32_bf16 v[162:165], a[232:235], a[152:155], v[162:165]\n"
        "v_cvt_pk_bf16_f32 v99, v102, v103\n"
        "v_cvt_pk_bf16_f32 v180, v76, v77\n"
        "v_mfma_f32_16x16x32_bf16 v[162:165], a[236:239], a[156:159], v[162:165]\n"
        "v_cvt_pk_bf16_f32 v181, v78, v79\n"
        "v_cvt_pk_bf16_f32 v186, v76, v77\n"
        "v_mfma_f32_16x16x32_bf16 v[162:165], a[240:243], a[160:163], v[162:165]\n"
        "v_cvt_pk_bf16_f32 v187, v78, v79\n"
        "v_fma_f32 v105, v105, s24, v238\n"
        "v_mfma_f32_16x16x32_bf16 v[162:165], a[244:247], a[164:167], v[162:165]\n"
        "v_exp_f32 v105, v105\n"
        "v_mfma_f32_16x16x32_bf16 v[72:75], a[244:247], a[188:191], 0\n"
        "v_cvt_pk_bf16_f32 v100, v104, v105\n"
        "v_fma_f32 v106, v106, s24, v238\n"
        "v_mfma_f32_16x16x32_bf16 v[72:75], a[240:243], a[184:187], v[72:75]\n"
        "v_exp_f32 v106, v106\n"
        "v_mfma_f32_16x16x32_bf16 v[72:75], a[236:239], a[180:183], v[72:75]\n"
        "v_fma_f32 v107, v107, s24, v238\n"
        "v_cvt_pk_bf16_f32 v218, v162, v163\n"
        "v_mfma_f32_16x16x32_bf16 v[72:75], a[232:235], a[176:179], v[72:75]\n"
        "v_exp_f32 v107, v107\n"
        "v_mfma_f32_16x16x32_bf16 v[72:75], a[228:231], a[172:175], v[72:75]\n"
        "s_add_u32 m0, s41, 0\n"
        "v_cvt_pk_bf16_f32 v219, v164, v165\n"
        "v_cvt_pk_bf16_f32 v101, v106, v107\n"
        "v_mfma_f32_16x16x32_bf16 v[72:75], a[224:227], a[168:171], v[72:75]\n"
        "v_fma_f32 v108, v108, s24, v238\n"
        "v_fma_f32 v109, v109, s24, v238\n"
        "v_mfma_f32_16x16x32_bf16 v[76:79], a[224:227], a[192:195], 0\n"
        "ds_read_b128 a[224:227], v241 offset:12416\n"
        "v_fma_f32 v110, v110, s24, v238\n"
        "v_mfma_f32_16x16x32_bf16 v[76:79], a[228:231], a[196:199], v[76:79]\n"
        "ds_read_b128 a[228:231], v240 offset:12544\n"
        "v_fma_f32 v111, v111, s24, v238\n"
        "v_mfma_f32_16x16x32_bf16 v[76:79], a[232:235], a[200:203], v[76:79]\n"
        "buffer_load_dwordx4 v242, s[8:11], s47 offen lds\n"
        "s_sub_u32 s31, s30, s28\n"
        "v_mfma_f32_16x16x32_bf16 v[76:79], a[236:239], a[204:207], v[76:79]\n"
        "ds_read_b128 a[232:235], v241 offset:12544\n"
        "s_sub_u32 s45, s28, s29\n"
        "v_cvt_pk_bf16_f32 v220, v72, v73\n"
        "v_mfma_f32_16x16x32_bf16 v[76:79], a[240:243], a[208:211], v[76:79]\n"
        "s_add_u32 s28, s28, s31\n"
        "v_cvt_pk_bf16_f32 v221, v74, v75\n"
        "v_fma_f32 v112, v112, s24, v239\n"
        "v_mfma_f32_16x16x32_bf16 v[76:79], a[244:247], a[212:215], v[76:79]\n"
        "s_waitcnt lgkmcnt(5)\n"
        "v_exp_f32 v108, v108\n"
        "v_mfma_f32_16x16x32_bf16 v[162:165], a[72:75], a[248:251], 0\n"
        "s_waitcnt lgkmcnt(4)\n"
        "v_exp_f32 v109, v109\n"
        "v_mfma_f32_16x16x32_bf16 v[162:165], a[76:79], a[216:219], v[162:165]\n"
        "s_waitcnt lgkmcnt(3)\n"
        "v_exp_f32 v110, v110\n"
        "v_mfma_f32_16x16x32_bf16 v[162:165], a[80:83], a[220:223], v[162:165]\n"
        "s_waitcnt lgkmcnt(2)\n"
        "v_exp_f32 v111, v111\n"
        "v_mfma_f32_16x16x32_bf16 v[162:165], a[84:87], a[224:227], v[162:165]\n"
        "s_waitcnt lgkmcnt(1)\n"
        "v_exp_f32 v112, v112\n"
        "v_mfma_f32_16x16x32_bf16 v[162:165], a[88:91], a[228:231], v[162:165]\n"
        "s_waitcnt lgkmcnt(0)\n"
        "v_cvt_pk_bf16_f32 v222, v76, v77\n"
        "v_cvt_pk_bf16_f32 v223, v78, v79\n"
        "v_mfma_f32_16x16x32_bf16 v[162:165], a[92:95], a[232:235], v[162:165]\n"
        "s_mov_b32 s30, s29\n"
        "v_cvt_pk_bf16_f32 v102, v108, v109\n"
        "v_cvt_pk_bf16_f32 v103, v110, v111\n"
        "v_mfma_f32_16x16x32_bf16 v[72:75], a[116:119], a[232:235], 0\n"
        "s_add_u32 m0, s41, 1024\n"
        "v_fma_f32 v113, v113, s24, v239\n"
        "v_fma_f32 v114, v114, s24, v239\n"
        "v_mfma_f32_16x16x32_bf16 v[72:75], a[112:115], a[228:231], v[72:75]\n"
        "s_add_u32 s29, s29, s45\n"
        "v_exp_f32 v113, v113\n"
        "v_mfma_f32_16x16x32_bf16 v[72:75], a[108:111], a[224:227], v[72:75]\n"
        "buffer_load_dwordx4 v243, s[8:11], s47 offen lds\n"
        "v_mfma_f32_16x16x32_bf16 v[72:75], a[104:107], a[220:223], v[72:75]\n"
        "v_exp_f32 v114, v114\n"
        "v_mfma_f32_16x16x32_bf16 v[72:75], a[100:103], a[216:219], v[72:75]\n"
        "v_cvt_pk_bf16_f32 v192, v162, v163\n"
        "v_cvt_pk_bf16_f32 v193, v164, v165\n"
        "v_mfma_f32_16x16x32_bf16 v[72:75], a[96:99], a[248:251], v[72:75]\n"
        "v_cvt_pk_bf16_f32 v112, v112, v113\n"
        "v_fma_f32 v115, v115, s24, v239\n"
        "v_mfma_f32_16x16x32_bf16 v[76:79], a[120:123], a[248:251], 0\n"
        "v_exp_f32 v115, v115\n"
        "v_mfma_f32_16x16x32_bf16 v[76:79], a[124:127], a[216:219], v[76:79]\n"
        "v_cvt_pk_bf16_f32 v113, v114, v115\n"
        "v_fma_f32 v116, v116, s24, v239\n"
        "v_mfma_f32_16x16x32_bf16 v[76:79], a[128:131], a[220:223], v[76:79]\n"
        "v_exp_f32 v116, v116\n"
        "v_mfma_f32_16x16x32_bf16 v[76:79], a[132:135], a[224:227], v[76:79]\n"
        "v_cvt_pk_bf16_f32 v194, v72, v73\n"
        "v_cvt_pk_bf16_f32 v195, v74, v75\n"
        "v_mfma_f32_16x16x32_bf16 v[76:79], a[136:139], a[228:231], v[76:79]\n"
        "v_fma_f32 v117, v117, s24, v239\n"
        "v_fma_f32 v118, v118, s24, v239\n"
        "v_mfma_f32_16x16x32_bf16 v[76:79], a[140:143], a[232:235], v[76:79]\n"
        "v_exp_f32 v117, v117\n"
        "v_mfma_f32_16x16x32_bf16 v[162:165], a[20:23], a[232:235], 0\n"
        "v_exp_f32 v118, v118\n"
        "v_mfma_f32_16x16x32_bf16 v[162:165], a[16:19], a[228:231], v[162:165]\n"
        "v_cvt_pk_bf16_f32 v114, v116, v117\n"
        "v_fma_f32 v119, v119, s24, v239\n"
        "v_mfma_f32_16x16x32_bf16 v[162:165], a[12:15], a[224:227], v[162:165]\n"
        "s_add_u32 m0, s41, 2048\n"
        "v_exp_f32 v119, v119\n"
        "v_mfma_f32_16x16x32_bf16 v[162:165], a[8:11], a[220:223], v[162:165]\n"
        "v_cvt_pk_bf16_f32 v208, v76, v77\n"
        "v_cvt_pk_bf16_f32 v209, v78, v79\n"
        "v_mfma_f32_16x16x32_bf16 v[162:165], a[4:7], a[216:219], v[162:165]\n"
        "buffer_load_dwordx4 v244, s[8:11], s47 offen lds\n"
        "v_mfma_f32_16x16x32_bf16 v[162:165], a[0:3], a[248:251], v[162:165]\n"
        "v_cvt_pk_bf16_f32 v115, v118, v119\n"
        "v_fma_f32 v120, v120, s24, v239\n"
        "v_mfma_f32_16x16x32_bf16 v[72:75], a[24:27], a[248:251], 0\n"
        "v_exp_f32 v120, v120\n"
        "v_mfma_f32_16x16x32_bf16 v[72:75], a[28:31], a[216:219], v[72:75]\n"
        "v_fma_f32 v121, v121, s24, v239\n"
        "v_fma_f32 v122, v122, s24, v239\n"
        "v_mfma_f32_16x16x32_bf16 v[72:75], a[32:35], a[220:223], v[72:75]\n"
        "v_exp_f32 v121, v121\n"
        "v_mfma_f32_16x16x32_bf16 v[72:75], a[36:39], a[224:227], v[72:75]\n"
        "v_exp_f32 v122, v122\n"
        "v_mfma_f32_16x16x32_bf16 v[72:75], a[40:43], a[228:231], v[72:75]\n"
        "v_cvt_pk_bf16_f32 v196, v162, v163\n"
        "v_cvt_pk_bf16_f32 v197, v164, v165\n"
        "v_mfma_f32_16x16x32_bf16 v[72:75], a[44:47], a[232:235], v[72:75]\n"
        "v_cvt_pk_bf16_f32 v116, v120, v121\n"
        "v_fma_f32 v123, v123, s24, v239\n"
        "v_mfma_f32_16x16x32_bf16 v[76:79], a[68:71], a[232:235], 0\n"
        "v_exp_f32 v123, v123\n"
        "v_mfma_f32_16x16x32_bf16 v[76:79], a[64:67], a[228:231], v[76:79]\n"
        "v_cvt_pk_bf16_f32 v117, v122, v123\n"
        "v_fma_f32 v124, v124, s24, v239\n"
        "v_mfma_f32_16x16x32_bf16 v[76:79], a[60:63], a[224:227], v[76:79]\n"
        "v_exp_f32 v124, v124\n"
        "v_mfma_f32_16x16x32_bf16 v[76:79], a[56:59], a[220:223], v[76:79]\n"
        "v_cvt_pk_bf16_f32 v198, v72, v73\n"
        "v_cvt_pk_bf16_f32 v199, v74, v75\n"
        "v_mfma_f32_16x16x32_bf16 v[76:79], a[52:55], a[216:219], v[76:79]\n"
        "s_add_u32 m0, s41, 12288\n"
        "v_fma_f32 v125, v125, s24, v239\n"
        "v_fma_f32 v126, v126, s24, v239\n"
        "v_mfma_f32_16x16x32_bf16 v[76:79], a[48:51], a[248:251], v[76:79]\n"
        "v_exp_f32 v125, v125\n"
        "v_mfma_f32_16x16x32_bf16 v[182:185], a[252:255], v[64:67], 0\n"
        "buffer_load_dwordx4 v242, s[8:11], s35 offen lds\n"
        "v_mfma_f32_16x16x32_bf16 v[182:185], a[252:255], v[68:71], v[182:185]\n"
        "v_exp_f32 v126, v126\n"
        "v_mfma_f32_16x16x32_bf16 v[202:205], v[132:135], v[68:71], 0\n"
        "v_cvt_pk_bf16_f32 v118, v124, v125\n"
        "v_fma_f32 v127, v127, s24, v239\n"
        "v_mfma_f32_16x16x32_bf16 v[202:205], v[128:131], v[64:67], v[202:205]\n"
        "v_exp_f32 v127, v127\n"
        "v_mfma_f32_16x16x32_bf16 v[88:91], v[136:139], v[64:67], 0\n"
        "v_cvt_pk_bf16_f32 v200, v76, v77\n"
        "v_cvt_pk_bf16_f32 v201, v78, v79\n"
        "v_mfma_f32_16x16x32_bf16 v[88:91], v[140:143], v[68:71], v[88:91]\n"
        "v_cvt_pk_bf16_f32 v206, v76, v77\n"
        "v_cvt_pk_bf16_f32 v207, v78, v79\n"
        "v_mfma_f32_16x16x32_bf16 v[92:95], v[148:151], v[68:71], 0\n"
        "s_cmp_eq_u32 s37, 100663296\n"
        "v_rcp_f32 v182, v182\n"
        "v_mfma_f32_16x16x32_bf16 v[92:95], v[144:147], v[64:67], v[92:95]\n"
        "s_cselect_b32 s46, 0, s37\n"
        "v_cvt_pk_bf16_f32 v119, v126, v127\n"
        "v_mul_f32 v202, v182, v202\n"
        "v_mfma_f32_16x16x32_bf16 v[64:67], a[252:255], v[80:83], 0\n"
        "s_add_u32 s38, s46, s43\n"
        "v_mul_f32 v203, v182, v203\n"
        "v_mul_f32 v204, v182, v204\n"
        "v_mfma_f32_16x16x32_bf16 v[64:67], a[252:255], v[84:87], v[64:67]\n"
        "s_add_u32 s39, s46, s44\n"
        "v_mul_f32 v205, v182, v205\n"
        "v_cvt_pk_bf16_f32 v202, v202, v203\n"
        "v_mfma_f32_16x16x32_bf16 v[68:71], v[132:135], v[84:87], 0\n"
        "s_add_u32 s37, s37, 6291456\n"
        "v_cvt_pk_bf16_f32 v203, v204, v205\n"
        "v_mul_f32 v88, v182, v88\n"
        "v_mfma_f32_16x16x32_bf16 v[68:71], v[128:131], v[80:83], v[68:71]\n"
        "v_rcp_f32 v64, v64\n"
        "v_mfma_f32_16x16x32_bf16 v[72:75], v[136:139], v[80:83], 0\n"
        "s_add_u32 m0, s41, 13312\n"
        "v_mul_f32 v89, v182, v89\n"
        "v_mul_f32 v90, v182, v90\n"
        "v_mfma_f32_16x16x32_bf16 v[72:75], v[140:143], v[84:87], v[72:75]\n"
        "v_mul_f32 v91, v182, v91\n"
        "v_cvt_pk_bf16_f32 v204, v88, v89\n"
        "v_mfma_f32_16x16x32_bf16 v[162:165], v[148:151], v[84:87], 0\n"
        "buffer_load_dwordx4 v243, s[8:11], s35 offen lds\n"
        "v_mfma_f32_16x16x32_bf16 v[162:165], v[144:147], v[80:83], v[162:165]\n"
        "v_cvt_pk_bf16_f32 v205, v90, v91\n"
        "v_mul_f32 v92, v182, v92\n"
        "v_mfma_f32_16x16x32_bf16 v[80:83], a[252:255], v[96:99], 0\n"
        "ds_write_b128 v245, v[202:205] offset:0\n"
        "v_mfma_f32_16x16x32_bf16 v[80:83], a[252:255], v[100:103], v[80:83]\n"
        "v_mul_f32 v93, v182, v93\n"
        "v_mul_f32 v94, v182, v94\n"
        "v_mfma_f32_16x16x32_bf16 v[84:87], v[132:135], v[100:103], 0\n"
        "v_mul_f32 v95, v182, v95\n"
        "v_cvt_pk_bf16_f32 v92, v92, v93\n"
        "v_mfma_f32_16x16x32_bf16 v[84:87], v[128:131], v[96:99], v[84:87]\n"
        "v_cvt_pk_bf16_f32 v93, v94, v95\n"
        "v_mul_f32 v68, v64, v68\n"
        "v_mfma_f32_16x16x32_bf16 v[76:79], v[136:139], v[96:99], 0\n"
        "v_rcp_f32 v80, v80\n"
        "v_mfma_f32_16x16x32_bf16 v[76:79], v[140:143], v[100:103], v[76:79]\n"
        "ds_write_b64 v246, v[92:93] offset:0\n"
        "v_mfma_f32_16x16x32_bf16 v[104:107], v[148:151], v[100:103], 0\n"
        "v_mul_f32 v69, v64, v69\n"
        "v_mul_f32 v70, v64, v70\n"
        "v_mfma_f32_16x16x32_bf16 v[104:107], v[144:147], v[96:99], v[104:107]\n"
        "v_mul_f32 v71, v64, v71\n"
        "v_cvt_pk_bf16_f32 v68, v68, v69\n"
        "v_mfma_f32_16x16x32_bf16 v[96:99], a[252:255], v[112:115], 0\n"
        "v_cvt_pk_bf16_f32 v69, v70, v71\n"
        "v_mul_f32 v72, v64, v72\n"
        "v_mfma_f32_16x16x32_bf16 v[96:99], a[252:255], v[116:119], v[96:99]\n"
        "v_mul_f32 v73, v64, v73\n"
        "v_mul_f32 v74, v64, v74\n"
        "v_mfma_f32_16x16x32_bf16 v[108:111], v[132:135], v[116:119], 0\n"
        "s_add_u32 m0, s41, 14336\n"
        "v_mul_f32 v75, v64, v75\n"
        "v_cvt_pk_bf16_f32 v70, v72, v73\n"
        "v_mfma_f32_16x16x32_bf16 v[108:111], v[128:131], v[112:115], v[108:111]\n"
        "v_mov_b32 v128, v212\n"
        "v_mov_b32 v129, v213\n"
        "v_mfma_f32_16x16x32_bf16 v[120:123], v[136:139], v[112:115], 0\n"
        "buffer_load_dwordx4 v244, s[8:11], s35 offen lds\n"
        "v_mfma_f32_16x16x32_bf16 v[120:123], v[140:143], v[116:119], v[120:123]\n"
        "ds_read_b128 a[236:239], v240 offset:13824\n"
        "v_mov_b32 v136, v214\n"
        "v_mfma_f32_16x16x32_bf16 v[124:127], v[148:151], v[116:119], 0\n"
        "ds_read_b128 a[240:243], v241 offset:13824\n"
        "v_mov_b32 v137, v215\n"
        "v_mfma_f32_16x16x32_bf16 v[124:127], v[144:147], v[112:115], v[124:127]\n"
        "ds_read_b128 a[244:247], v240 offset:13952\n"
        "v_mov_b32 v144, v216\n"
        "v_mfma_f32_16x16x32_bf16 v[100:103], a[248:251], a[144:147], 0\n"
        "v_mov_b32 v145, v217\n"
        "v_cvt_pk_bf16_f32 v71, v74, v75\n"
        "v_mfma_f32_16x16x32_bf16 v[100:103], a[216:219], a[148:151], v[100:103]\n"
        "ds_write_b128 v245, v[68:71] offset:256\n"
        "v_mfma_f32_16x16x32_bf16 v[100:103], a[220:223], a[152:155], v[100:103]\n"
        "v_mov_b32 v130, v218\n"
        "v_mov_b32 v131, v219\n"
        "v_mfma_f32_16x16x32_bf16 v[100:103], a[224:227], a[156:159], v[100:103]\n"
        "v_mov_b32 v138, v220\n"
        "v_mov_b32 v139, v221\n"
        "v_mfma_f32_16x16x32_bf16 v[100:103], a[228:231], a[160:163], v[100:103]\n"
        "v_mul_f32 v162, v64, v162\n"
        "v_mul_f32 v163, v64, v163\n"
        "v_mfma_f32_16x16x32_bf16 v[100:103], a[232:235], a[164:167], v[100:103]\n"
        "v_mul_f32 v164, v64, v164\n"
        "v_mul_f32 v165, v64, v165\n"
        "v_mfma_f32_16x16x32_bf16 v[112:115], a[232:235], a[188:191], 0\n"
        "v_mov_b32 v146, v222\n"
        "v_mov_b32 v147, v223\n"
        "v_mfma_f32_16x16x32_bf16 v[112:115], a[228:231], a[184:187], v[112:115]\n"
        "v_cvt_pk_bf16_f32 v162, v162, v163\n"
        "v_cvt_pk_bf16_f32 v163, v164, v165\n"
        "v_mfma_f32_16x16x32_bf16 v[112:115], a[224:227], a[180:183], v[112:115]\n"
        "ds_write_b64 v246, v[162:163] offset:256\n"
        "v_mfma_f32_16x16x32_bf16 v[112:115], a[220:223], a[176:179], v[112:115]\n"
        "v_cvt_pk_bf16_f32 v132, v100, v101\n"
        "v_cvt_pk_bf16_f32 v133, v102, v103\n"
        "v_mfma_f32_16x16x32_bf16 v[112:115], a[216:219], a[172:175], v[112:115]\n"
        "v_mul_f32 v84, v80, v84\n"
        "v_mul_f32 v85, v80, v85\n"
        "v_mfma_f32_16x16x32_bf16 v[112:115], a[248:251], a[168:171], v[112:115]\n"
        "v_rcp_f32 v96, v96\n"
        "v_mfma_f32_16x16x32_bf16 v[116:119], a[248:251], a[192:195], 0\n"
        "ds_read_b128 a[248:251], v241 offset:13952\n"
        "v_mul_f32 v86, v80, v86\n"
        "v_mfma_f32_16x16x32_bf16 v[116:119], a[216:219], a[196:199], v[116:119]\n"
        "ds_read_b128 a[216:219], v240 offset:14080\n"
        "v_mul_f32 v87, v80, v87\n"
        "v_mfma_f32_16x16x32_bf16 v[116:119], a[220:223], a[200:203], v[116:119]\n"
        "ds_read_b128 a[220:223], v241 offset:14080\n"
        "v_cvt_pk_bf16_f32 v140, v112, v113\n"
        "v_mfma_f32_16x16x32_bf16 v[116:119], a[224:227], a[204:207], v[116:119]\n"
        "v_cvt_pk_bf16_f32 v141, v114, v115\n"
        "v_cvt_pk_bf16_f32 v84, v84, v85\n"
        "v_mfma_f32_16x16x32_bf16 v[116:119], a[228:231], a[208:211], v[116:119]\n"
        "v_cvt_pk_bf16_f32 v85, v86, v87\n"
        "v_mul_f32 v76, v80, v76\n"
        "v_mfma_f32_16x16x32_bf16 v[116:119], a[232:235], a[212:215], v[116:119]\n"
        "s_waitcnt lgkmcnt(7)\n"
        "v_mul_f32 v77, v80, v77\n"
        "v_mul_f32 v78, v80, v78\n"
        "v_mfma_f32_16x16x32_bf16 v[100:103], a[236:239], a[144:147], 0\n"
        "s_waitcnt lgkmcnt(6)\n"
        "v_mul_f32 v79, v80, v79\n"
        "v_cvt_pk_bf16_f32 v86, v76, v77\n"
        "v_mfma_f32_16x16x32_bf16 v[100:103], a[240:243], a[148:151], v[100:103]\n"
        "s_waitcnt lgkmcnt(5)\n"
        "v_cvt_pk_bf16_f32 v87, v78, v79\n"
        "v_mul_f32 v104, v80, v104\n"
        "v_mfma_f32_16x16x32_bf16 v[100:103], a[244:247], a[152:155], v[100:103]\n"
        "s_waitcnt lgkmcnt(2)\n"
        "ds_write_b128 v245, v[84:87] offset:512\n"
        "v_mfma_f32_16x16x32_bf16 v[100:103], a[248:251], a[156:159], v[100:103]\n"
        "s_waitcnt lgkmcnt(2)\n"
        "v_cvt_pk_bf16_f32 v148, v116, v117\n"
        "v_cvt_pk_bf16_f32 v149, v118, v119\n"
        "v_mfma_f32_16x16x32_bf16 v[100:103], a[216:219], a[160:163], v[100:103]\n"
        "s_waitcnt lgkmcnt(1)\n"
        "v_mul_f32 v105, v80, v105\n"
        "v_mul_f32 v106, v80, v106\n"
        "v_mfma_f32_16x16x32_bf16 v[100:103], a[220:223], a[164:167], v[100:103]\n"
        "v_mul_f32 v107, v80, v107\n"
        "v_cvt_pk_bf16_f32 v104, v104, v105\n"
        "v_mfma_f32_16x16x32_bf16 v[112:115], a[220:223], a[188:191], 0\n"
        "v_cvt_pk_bf16_f32 v105, v106, v107\n"
        "v_mul_f32 v108, v96, v108\n"
        "v_mfma_f32_16x16x32_bf16 v[112:115], a[216:219], a[184:187], v[112:115]\n"
        "ds_write_b64 v246, v[104:105] offset:512\n"
        "v_mfma_f32_16x16x32_bf16 v[112:115], a[248:251], a[180:183], v[112:115]\n"
        "v_cvt_pk_bf16_f32 v134, v100, v101\n"
        "v_cvt_pk_bf16_f32 v135, v102, v103\n"
        "v_mfma_f32_16x16x32_bf16 v[112:115], a[244:247], a[176:179], v[112:115]\n"
        "v_mul_f32 v109, v96, v109\n"
        "v_mul_f32 v110, v96, v110\n"
        "v_mfma_f32_16x16x32_bf16 v[112:115], a[240:243], a[172:175], v[112:115]\n"
        "v_mul_f32 v111, v96, v111\n"
        "v_cvt_pk_bf16_f32 v108, v108, v109\n"
        "v_mfma_f32_16x16x32_bf16 v[112:115], a[236:239], a[168:171], v[112:115]\n"
        "v_cvt_pk_bf16_f32 v109, v110, v111\n"
        "v_mul_f32 v120, v96, v120\n"
        "v_mfma_f32_16x16x32_bf16 v[116:119], a[236:239], a[192:195], 0\n"
        "v_mul_f32 v121, v96, v121\n"
        "v_mul_f32 v122, v96, v122\n"
        "v_mfma_f32_16x16x32_bf16 v[116:119], a[240:243], a[196:199], v[116:119]\n"
        "v_mul_f32 v123, v96, v123\n"
        "v_cvt_pk_bf16_f32 v110, v120, v121\n"
        "v_mfma_f32_16x16x32_bf16 v[116:119], a[244:247], a[200:203], v[116:119]\n"
        "v_cvt_pk_bf16_f32 v142, v112, v113\n"
        "v_cvt_pk_bf16_f32 v143, v114, v115\n"
        "v_mfma_f32_16x16x32_bf16 v[116:119], a[248:251], a[204:207], v[116:119]\n"
        "v_cvt_pk_bf16_f32 v111, v122, v123\n"
        "v_mul_f32 v124, v96, v124\n"
        "v_mfma_f32_16x16x32_bf16 v[116:119], a[216:219], a[208:211], v[116:119]\n"
        "ds_write_b128 v245, v[108:111] offset:768\n"
        "v_mfma_f32_16x16x32_bf16 v[116:119], a[220:223], a[212:215], v[116:119]\n"
        "v_mul_f32 v125, v96, v125\n"
        "v_mul_f32 v126, v96, v126\n"
        "v_mfma_f32_16x16x32_bf16 v[100:103], a[20:23], a[220:223], 0\n"
        "v_mul_f32 v127, v96, v127\n"
        "v_cvt_pk_bf16_f32 v124, v124, v125\n"
        "v_mfma_f32_16x16x32_bf16 v[100:103], a[16:19], a[216:219], v[100:103]\n"
        "v_cvt_pk_bf16_f32 v125, v126, v127\n"
        "v_mfma_f32_16x16x32_bf16 v[100:103], a[12:15], a[248:251], v[100:103]\n"
        "v_cvt_pk_bf16_f32 v150, v116, v117\n"
        "v_cvt_pk_bf16_f32 v151, v118, v119\n"
        "v_mfma_f32_16x16x32_bf16 v[100:103], a[8:11], a[244:247], v[100:103]\n"
        "ds_write_b64 v246, v[124:125] offset:768\n"
        "v_mfma_f32_16x16x32_bf16 v[100:103], a[4:7], a[240:243], v[100:103]\n"
        "v_mfma_f32_16x16x32_bf16 v[100:103], a[0:3], a[236:239], v[100:103]\n"
        "v_mfma_f32_16x16x32_bf16 v[112:115], a[24:27], a[236:239], 0\n"
        "v_mfma_f32_16x16x32_bf16 v[112:115], a[28:31], a[240:243], v[112:115]\n"
        "v_mfma_f32_16x16x32_bf16 v[112:115], a[32:35], a[244:247], v[112:115]\n"
        "v_mfma_f32_16x16x32_bf16 v[112:115], a[36:39], a[248:251], v[112:115]\n"
        "v_mfma_f32_16x16x32_bf16 v[112:115], a[40:43], a[216:219], v[112:115]\n"
        "v_mfma_f32_16x16x32_bf16 v[112:115], a[44:47], a[220:223], v[112:115]\n"
        "v_mfma_f32_16x16x32_bf16 v[116:119], a[68:71], a[220:223], 0\n"
        "ds_read_b128 v[162:165], v251 offset:0\n"
        "v_cvt_pk_bf16_f32 v216, v100, v101\n"
        "v_mfma_f32_16x16x32_bf16 v[116:119], a[64:67], a[216:219], v[116:119]\n"
        "v_cvt_pk_bf16_f32 v217, v102, v103\n"
        "v_mfma_f32_16x16x32_bf16 v[116:119], a[60:63], a[248:251], v[116:119]\n"
        "v_mfma_f32_16x16x32_bf16 v[116:119], a[56:59], a[244:247], v[116:119]\n"
        "v_mfma_f32_16x16x32_bf16 v[116:119], a[52:55], a[240:243], v[116:119]\n"
        "ds_read_b128 v[182:185], v251 offset:512\n"
        "v_cvt_pk_bf16_f32 v218, v112, v113\n"
        "v_mfma_f32_16x16x32_bf16 v[116:119], a[48:51], a[236:239], v[116:119]\n"
        "v_cvt_pk_bf16_f32 v219, v114, v115\n"
        "v_mfma_f32_16x16x32_bf16 v[100:103], a[72:75], a[236:239], 0\n"
        "s_add_u32 s47, s47, 6291456\n"
        "v_mfma_f32_16x16x32_bf16 v[100:103], a[76:79], a[240:243], v[100:103]\n"
        "s_add_u32 s36, s36, 2097152\n"
        "v_mfma_f32_16x16x32_bf16 v[100:103], a[80:83], a[244:247], v[100:103]\n"
        "ds_read_b128 v[202:205], v251 offset:4096\n"
        "s_xor_b32 s48, s48, 8192\n"
        "v_cvt_pk_bf16_f32 v220, v116, v117\n"
        "v_mfma_f32_16x16x32_bf16 v[100:103], a[84:87], a[248:251], v[100:103]\n"
        "v_cvt_pk_bf16_f32 v221, v118, v119\n"
        "v_cvt_pk_bf16_f32 v226, v116, v117\n"
        "v_mfma_f32_16x16x32_bf16 v[100:103], a[88:91], a[216:219], v[100:103]\n"
        "v_cvt_pk_bf16_f32 v227, v118, v119\n"
        "v_mfma_f32_16x16x32_bf16 v[100:103], a[92:95], a[220:223], v[100:103]\n"
        "v_mfma_f32_16x16x32_bf16 v[112:115], a[116:119], a[220:223], 0\n"
        "ds_read_b128 v[222:225], v251 offset:4608\n"
        "v_mfma_f32_16x16x32_bf16 v[112:115], a[112:115], a[216:219], v[112:115]\n"
        "v_mfma_f32_16x16x32_bf16 v[112:115], a[108:111], a[248:251], v[112:115]\n"
        "s_waitcnt lgkmcnt(3)\n"
        "v_pk_min_u16 v162, v162, s26\n"
        "v_pk_min_u16 v163, v163, s26\n"
        "v_mfma_f32_16x16x32_bf16 v[112:115], a[104:107], a[244:247], v[112:115]\n"
        "v_pk_min_u16 v164, v164, s26\n"
        "v_cvt_pk_bf16_f32 v212, v100, v101\n"
        "v_mfma_f32_16x16x32_bf16 v[112:115], a[100:103], a[240:243], v[112:115]\n"
        "v_cvt_pk_bf16_f32 v213, v102, v103\n"
        "v_pk_min_u16 v165, v165, s26\n"
        "v_mfma_f32_16x16x32_bf16 v[112:115], a[96:99], a[236:239], v[112:115]\n"
        "v_add_u32 v240, s31, v240\n"
        "v_add_u32 v241, s31, v241\n"
        "v_mfma_f32_16x16x32_bf16 v[116:119], a[120:123], a[236:239], 0\n"
        "s_waitcnt lgkmcnt(2)\n"
        "v_pk_min_u16 v182, v182, s26\n"
        "v_pk_min_u16 v183, v183, s26\n"
        "v_mfma_f32_16x16x32_bf16 v[116:119], a[124:127], a[240:243], v[116:119]\n"
        "v_pk_min_u16 v184, v184, s26\n"
        "v_pk_min_u16 v185, v185, s26\n"
        "v_mfma_f32_16x16x32_bf16 v[116:119], a[128:131], a[244:247], v[116:119]\n"
        "v_cvt_pk_bf16_f32 v214, v112, v113\n"
        "v_cvt_pk_bf16_f32 v215, v114, v115\n"
        "v_mfma_f32_16x16x32_bf16 v[116:119], a[132:135], a[248:251], v[116:119]\n"
        "v_mfma_f32_16x16x32_bf16 v[116:119], a[136:139], a[216:219], v[116:119]\n"
        "s_waitcnt lgkmcnt(1)\n"
        "v_pk_min_u16 v202, v202, s26\n"
        "v_pk_min_u16 v203, v203, s26\n"
        "v_mfma_f32_16x16x32_bf16 v[116:119], a[140:143], a[220:223], v[116:119]\n"
        "v_pk_min_u16 v204, v204, s26\n"
        "v_pk_min_u16 v205, v205, s26\n"
        "v_mfma_f32_16x16x32_bf16 v[64:67], v[152:155], v[156:159], v[0:3]\n"
        "v_mfma_f32_16x16x32_bf16 v[64:67], v[168:171], v[160:163], v[64:67]\n"
        "v_mfma_f32_16x16x32_bf16 v[68:71], v[172:175], v[156:159], v[4:7]\n"
        "s_waitcnt lgkmcnt(0)\n"
        "v_pk_min_u16 v222, v222, s26\n"
        "v_pk_min_u16 v223, v223, s26\n"
        "v_mfma_f32_16x16x32_bf16 v[68:71], v[188:191], v[164:167], v[68:71]\n"
        "ds_read_b128 v[162:165], v252 offset:0\n"
        "v_cvt_pk_bf16_f32 v230, v116, v117\n"
        "v_mfma_f32_16x16x32_bf16 v[80:83], v[152:155], v[176:179], v[16:19]\n"
        "v_cvt_pk_bf16_f32 v231, v118, v119\n"
        "v_pk_min_u16 v224, v224, s26\n"
        "v_mfma_f32_16x16x32_bf16 v[80:83], v[168:171], v[180:183], v[80:83]\n"
        "v_pk_min_u16 v225, v225, s26\n"
        "v_max3_f32 v232, v64, v65, v66\n"
        "v_mfma_f32_16x16x32_bf16 v[84:87], v[172:175], v[176:179], v[20:23]\n"
        "v_max3_f32 v232, v232, v67, v68\n"
        "v_max3_f32 v232, v232, v69, v70\n"
        "v_mfma_f32_16x16x32_bf16 v[84:87], v[188:191], v[184:187], v[84:87]\n"
        "ds_read_b128 v[182:185], v252 offset:512\n"
        "v_mfma_f32_16x16x32_bf16 v[96:99], v[152:155], v[196:199], v[32:35]\n"
        "v_max3_f32 v233, v80, v81, v82\n"
        "v_mfma_f32_16x16x32_bf16 v[96:99], v[168:171], v[200:203], v[96:99]\n"
        "v_mfma_f32_16x16x32_bf16 v[100:103], v[172:175], v[196:199], v[36:39]\n"
        "v_mfma_f32_16x16x32_bf16 v[100:103], v[188:191], v[204:207], v[100:103]\n"
        "ds_read_b128 v[202:205], v252 offset:4096\n"
        "v_mfma_f32_16x16x32_bf16 v[112:115], v[152:155], v[216:219], v[48:51]\n"
        "s_waitcnt lgkmcnt(2)\n"
        "v_pk_min_u16 v162, v162, s26\n"
        "v_pk_min_u16 v163, v163, s26\n"
        "v_mfma_f32_16x16x32_bf16 v[112:115], v[168:171], v[220:223], v[112:115]\n"
        "v_pk_min_u16 v164, v164, s26\n"
        "v_pk_min_u16 v165, v165, s26\n"
        "v_mfma_f32_16x16x32_bf16 v[116:119], v[172:175], v[216:219], v[52:55]\n"
        "v_max3_f32 v233, v233, v83, v84\n"
        "v_max3_f32 v233, v233, v85, v86\n"
        "v_mfma_f32_16x16x32_bf16 v[116:119], v[188:191], v[224:227], v[116:119]\n"
        "ds_read_b128 v[222:225], v252 offset:4608\n"
        "v_max3_f32 v234, v96, v97, v98\n"
        "v_mfma_f32_16x16x32_bf16 v[72:75], v[192:195], v[156:159], v[8:11]\n"
        "s_waitcnt lgkmcnt(2)\n"
        "v_pk_min_u16 v182, v182, s26\n"
        "v_pk_min_u16 v183, v183, s26\n"
        "v_mfma_f32_16x16x32_bf16 v[72:75], v[208:211], v[160:163], v[72:75]\n"
        "v_pk_min_u16 v184, v184, s26\n"
        "v_pk_min_u16 v185, v185, s26\n"
        "v_mfma_f32_16x16x32_bf16 v[76:79], v[212:215], v[156:159], v[12:15]\n"
        "v_max3_f32 v234, v234, v99, v100\n"
        "v_max3_f32 v234, v234, v101, v102\n"
        "v_mfma_f32_16x16x32_bf16 v[76:79], v[228:231], v[164:167], v[76:79]\n"
        "v_max3_f32 v235, v112, v113, v114\n"
        "v_max3_f32 v235, v235, v115, v116\n"
        "v_mfma_f32_16x16x32_bf16 v[88:91], v[192:195], v[176:179], v[24:27]\n"
        "s_barrier\n"
        "ds_read_b128 v[152:155], v247 offset:0\n"
        "s_waitcnt lgkmcnt(2)\n"
        "v_pk_min_u16 v202, v202, s26\n"
        "v_mfma_f32_16x16x32_bf16 v[88:91], v[208:211], v[180:183], v[88:91]\n"
        "ds_read_b128 v[160:163], v247 offset:4096\n"
        "v_pk_min_u16 v203, v203, s26\n"
        "v_mfma_f32_16x16x32_bf16 v[92:95], v[212:215], v[176:179], v[28:31]\n"
        "ds_read_b128 v[156:159], v247 offset:256\n"
        "v_pk_min_u16 v204, v204, s26\n"
        "v_mfma_f32_16x16x32_bf16 v[92:95], v[228:231], v[184:187], v[92:95]\n"
        "ds_read_b128 v[164:167], v247 offset:4352\n"
        "v_pk_min_u16 v205, v205, s26\n"
        "v_mfma_f32_16x16x32_bf16 v[104:107], v[192:195], v[196:199], v[40:43]\n"
        "ds_read_b128 a[216:219], v248 offset:0\n"
        "s_waitcnt lgkmcnt(5)\n"
        "v_pk_min_u16 v222, v222, s26\n"
        "v_mfma_f32_16x16x32_bf16 v[104:107], v[208:211], v[200:203], v[104:107]\n"
        "ds_read_b128 a[220:223], v248 offset:6144\n"
        "v_pk_min_u16 v223, v223, s26\n"
        "v_mfma_f32_16x16x32_bf16 v[108:111], v[212:215], v[196:199], v[44:47]\n"
        "ds_read_b128 a[224:227], v254 offset:16\n"
        "v_pk_min_u16 v224, v224, s26\n"
        "v_mfma_f32_16x16x32_bf16 v[108:111], v[228:231], v[204:207], v[108:111]\n"
        "ds_read_b128 a[228:231], v254 offset:6160\n"
        "v_pk_min_u16 v225, v225, s26\n"
        "v_mfma_f32_16x16x32_bf16 v[120:123], v[192:195], v[216:219], v[56:59]\n"
        "ds_read_b128 a[232:235], v248 offset:32\n"
        "v_max3_f32 v232, v232, v71, v72\n"
        "v_mfma_f32_16x16x32_bf16 v[120:123], v[208:211], v[220:223], v[120:123]\n"
        "v_max3_f32 v232, v232, v73, v74\n"
        "v_max3_f32 v232, v232, v75, v76\n"
        "v_mfma_f32_16x16x32_bf16 v[124:127], v[212:215], v[216:219], v[60:63]\n"
        "v_max3_f32 v232, v232, v77, v78\n"
        "v_max_f32 v232, v232, v79\n"
        "v_mfma_f32_16x16x32_bf16 v[124:127], v[228:231], v[224:227], v[124:127]\n"
        "s_waitcnt lgkmcnt(4)\n"
        "v_max3_f32 v233, v233, v87, v88\n"
        "v_max3_f32 v233, v233, v89, v90\n"
        "v_mfma_f32_16x16x32_bf16 v[172:175], v[152:155], a[216:219], 0\n"
        "s_waitcnt lgkmcnt(3)\n"
        "v_max3_f32 v233, v233, v91, v92\n"
        "v_max3_f32 v233, v233, v93, v94\n"
        "v_mfma_f32_16x16x32_bf16 v[172:175], v[160:163], a[220:223], v[172:175]\n"
        "v_max_f32 v233, v233, v95\n"
        "v_max3_f32 v234, v234, v103, v104\n"
        "v_mfma_f32_16x16x32_bf16 v[180:183], v[164:167], a[220:223], 0\n"
        "ds_read_b128 a[220:223], v248 offset:6176\n"
        "v_max3_f32 v234, v234, v105, v106\n"
        "v_mfma_f32_16x16x32_bf16 v[180:183], v[156:159], a[216:219], v[180:183]\n"
        "s_waitcnt lgkmcnt(3)\n"
        "ds_read_b128 a[216:219], v254 offset:48\n"
        "v_max3_f32 v234, v234, v107, v108\n"
        "v_mfma_f32_16x16x32_bf16 v[184:187], v[156:159], a[224:227], 0\n"
        "s_waitcnt lgkmcnt(3)\n"
        "ds_read_b128 a[236:239], v247 offset:8192\n"
        "v_max3_f32 v234, v234, v109, v110\n"
        "v_mfma_f32_16x16x32_bf16 v[184:187], v[164:167], a[228:231], v[184:187]\n"
        "ds_read_b128 a[244:247], v247 offset:12288\n"
        "v_max_f32 v234, v234, v111\n"
        "v_mfma_f32_16x16x32_bf16 v[176:179], v[160:163], a[228:231], 0\n"
        "ds_read_b128 a[228:231], v254 offset:6192\n"
        "v_max3_f32 v235, v235, v117, v118\n"
        "v_mfma_f32_16x16x32_bf16 v[176:179], v[152:155], a[224:227], v[176:179]\n"
        "s_waitcnt lgkmcnt(5)\n"
        "ds_read_b128 a[224:227], v248 offset:64\n"
        "v_max3_f32 v235, v235, v119, v120\n"
        "v_mfma_f32_16x16x32_bf16 v[212:215], v[152:155], a[232:235], 0\n"
        "s_waitcnt lgkmcnt(5)\n"
        "ds_read_b128 a[240:243], v247 offset:8448\n"
        "v_max3_f32 v235, v235, v121, v122\n"
        "v_mfma_f32_16x16x32_bf16 v[212:215], v[160:163], a[220:223], v[212:215]\n"
        "ds_read_b128 a[248:251], v247 offset:12544\n"
        "v_max3_f32 v235, v235, v123, v124\n"
        "v_mfma_f32_16x16x32_bf16 v[192:195], v[164:167], a[220:223], 0\n"
        "ds_read_b128 a[220:223], v248 offset:6208\n"
        "v_max3_f32 v235, v235, v125, v126\n"
        "v_mfma_f32_16x16x32_bf16 v[192:195], v[156:159], a[232:235], v[192:195]\n"
        "s_waitcnt lgkmcnt(7)\n"
        "ds_read_b128 a[232:235], v254 offset:80\n"
        "v_max_f32 v235, v235, v127\n"
        "v_mfma_f32_16x16x32_bf16 v[196:199], v[156:159], a[216:219], 0\n"
        "s_waitcnt lgkmcnt(5)\n"
        "v_permlane32_swap_b32 v232, v233\n"
        "v_mfma_f32_16x16x32_bf16 v[196:199], v[164:167], a[228:231], v[196:199]\n"
        "v_max_f32 v232, v232, v233\n"
        "v_mov_b32 v233, v232\n"
        "v_mfma_f32_16x16x32_bf16 v[216:219], v[160:163], a[228:231], 0\n"
        "ds_read_b128 a[228:231], v254 offset:6224\n"
        "v_xor_b32 v251, 0x2000, v251\n"
        "v_mfma_f32_16x16x32_bf16 v[216:219], v[152:155], a[216:219], v[216:219]\n"
        "s_waitcnt lgkmcnt(5)\n"
        "ds_read_b128 a[216:219], v248 offset:36864\n"
        "v_xor_b32 v252, 0x2000, v252\n"
        "v_mfma_f32_16x16x32_bf16 v[220:223], v[152:155], a[224:227], 0\n"
        "s_waitcnt lgkmcnt(3)\n"
        "v_permlane16_swap_b32 v232, v233\n"
        "v_mfma_f32_16x16x32_bf16 v[220:223], v[160:163], a[220:223], v[220:223]\n"
        "v_max_f32 v232, v232, v233\n"
        "v_mul_f32 v236, s25, v232\n"
        "v_mfma_f32_16x16x32_bf16 v[200:203], v[164:167], a[220:223], 0\n"
        "ds_read_b128 a[220:223], v248 offset:43008\n"
        "v_mul_f32 v237, s25, v232\n"
        "v_mfma_f32_16x16x32_bf16 v[200:203], v[156:159], a[224:227], v[200:203]\n"
        "s_waitcnt lgkmcnt(3)\n"
        "ds_read_b128 a[224:227], v254 offset:36880\n"
        "v_add_u32 v245, s45, v245\n"
        "v_mfma_f32_16x16x32_bf16 v[204:207], v[156:159], a[232:235], 0\n"
        "s_waitcnt lgkmcnt(3)\n"
        "v_permlane32_swap_b32 v236, v237\n"
        "v_mfma_f32_16x16x32_bf16 v[204:207], v[164:167], a[228:231], v[204:207]\n"
        "v_permlane32_swap_b32 v234, v235\n"
        "v_mfma_f32_16x16x32_bf16 v[224:227], v[160:163], a[228:231], 0\n"
        "ds_read_b128 a[228:231], v254 offset:43024\n"
        "v_max_f32 v234, v234, v235\n"
        "v_mfma_f32_16x16x32_bf16 v[224:227], v[152:155], a[232:235], v[224:227]\n"
        "s_waitcnt lgkmcnt(3)\n"
        "ds_read_b128 a[232:235], v248 offset:36896\n"
        "v_mov_b32 v235, v234\n"
        "v_mfma_f32_16x16x32_bf16 v[172:175], a[236:239], a[216:219], v[172:175]\n"
        "s_waitcnt lgkmcnt(3)\n"
        "v_fma_f32 v64, v64, s24, v236\n"
        "v_fma_f32 v65, v65, s24, v236\n"
        "v_mfma_f32_16x16x32_bf16 v[172:175], a[244:247], a[220:223], v[172:175]\n"
        "v_permlane16_swap_b32 v234, v235\n"
        "v_mfma_f32_16x16x32_bf16 v[180:183], a[248:251], a[220:223], v[180:183]\n"
        "ds_read_b128 a[220:223], v248 offset:43040\n"
        "v_max_f32 v234, v234, v235\n"
        "v_mfma_f32_16x16x32_bf16 v[180:183], a[240:243], a[216:219], v[180:183]\n"
        "s_waitcnt lgkmcnt(3)\n"
        "ds_read_b128 a[216:219], v254 offset:36912\n"
        "v_mul_f32 v238, s25, v234\n"
        "v_mfma_f32_16x16x32_bf16 v[184:187], a[240:243], a[224:227], v[184:187]\n"
        "s_waitcnt lgkmcnt(3)\n"
        "ds_read_b128 v[152:155], v247 offset:16384\n"
        "v_mul_f32 v239, s25, v234\n"
        "v_mfma_f32_16x16x32_bf16 v[184:187], a[248:251], a[228:231], v[184:187]\n"
        "ds_read_b128 v[160:163], v247 offset:20480\n"
        "v_fma_f32 v66, v66, s24, v236\n"
        "v_mfma_f32_16x16x32_bf16 v[176:179], a[244:247], a[228:231], v[176:179]\n"
        "ds_read_b128 a[228:231], v254 offset:43056\n"
        "v_fma_f32 v67, v67, s24, v236\n"
        "v_mfma_f32_16x16x32_bf16 v[176:179], a[236:239], a[224:227], v[176:179]\n"
        "s_waitcnt lgkmcnt(5)\n"
        "ds_read_b128 a[224:227], v248 offset:36928\n"
        "v_fma_f32 v68, v68, s24, v236\n"
        "v_mfma_f32_16x16x32_bf16 v[212:215], a[236:239], a[232:235], v[212:215]\n"
        "s_waitcnt lgkmcnt(5)\n"
        "ds_read_b128 v[156:159], v247 offset:16640\n"
        "v_fma_f32 v69, v69, s24, v236\n"
        "v_mfma_f32_16x16x32_bf16 v[212:215], a[244:247], a[220:223], v[212:215]\n"
        "ds_read_b128 v[164:167], v247 offset:20736\n"
        "v_fma_f32 v70, v70, s24, v236\n"
        "v_mfma_f32_16x16x32_bf16 v[192:195], a[248:251], a[220:223], v[192:195]\n"
        "ds_read_b128 a[220:223], v248 offset:43072\n"
        "v_fma_f32 v71, v71, s24, v236\n"
        "v_mfma_f32_16x16x32_bf16 v[192:195], a[240:243], a[232:235], v[192:195]\n"
        "s_waitcnt lgkmcnt(7)\n"
        "ds_read_b128 a[232:235], v254 offset:36944\n"
        "v_fma_f32 v72, v72, s24, v236\n"
        "v_mfma_f32_16x16x32_bf16 v[196:199], a[240:243], a[216:219], v[196:199]\n"
        "s_waitcnt lgkmcnt(5)\n"
        "v_permlane32_swap_b32 v238, v239\n"
        "v_mfma_f32_16x16x32_bf16 v[196:199], a[248:251], a[228:231], v[196:199]\n"
        "v_exp_f32 v64, v64\n"
        "v_mfma_f32_16x16x32_bf16 v[216:219], a[244:247], a[228:231], v[216:219]\n"
        "ds_read_b128 a[228:231], v254 offset:43088\n"
        "v_fma_f32 v73, v73, s24, v236\n"
        "v_mfma_f32_16x16x32_bf16 v[216:219], a[236:239], a[216:219], v[216:219]\n"
        "s_waitcnt lgkmcnt(5)\n"
        "ds_read_b128 a[216:219], v248 offset:12288\n"
        "v_fma_f32 v74, v74, s24, v236\n"
        "v_mfma_f32_16x16x32_bf16 v[220:223], a[236:239], a[224:227], v[220:223]\n"
        "s_waitcnt lgkmcnt(3)\n"
        "v_exp_f32 v65, v65\n"
        "v_mfma_f32_16x16x32_bf16 v[220:223], a[244:247], a[220:223], v[220:223]\n"
        "v_exp_f32 v66, v66\n"
        "v_mfma_f32_16x16x32_bf16 v[200:203], a[248:251], a[220:223], v[200:203]\n"
        "ds_read_b128 a[220:223], v248 offset:49152\n"
        "v_cvt_pk_bf16_f32 v64, v64, v65\n"
        "v_mfma_f32_16x16x32_bf16 v[200:203], a[240:243], a[224:227], v[200:203]\n"
        "s_waitcnt lgkmcnt(3)\n"
        "ds_read_b128 a[224:227], v254 offset:12304\n"
        "v_fma_f32 v75, v75, s24, v236\n"
        "v_mfma_f32_16x16x32_bf16 v[204:207], a[240:243], a[232:235], v[204:207]\n"
        "s_waitcnt lgkmcnt(3)\n"
        "v_exp_f32 v67, v67\n"
        "v_mfma_f32_16x16x32_bf16 v[204:207], a[248:251], a[228:231], v[204:207]\n"
        "v_exp_f32 v68, v68\n"
        "v_mfma_f32_16x16x32_bf16 v[224:227], a[244:247], a[228:231], v[224:227]\n"
        "ds_read_b128 a[228:231], v254 offset:49168\n"
        "v_cvt_pk_bf16_f32 v65, v66, v67\n"
        "v_mfma_f32_16x16x32_bf16 v[224:227], a[236:239], a[232:235], v[224:227]\n"
        "s_waitcnt lgkmcnt(3)\n"
        "ds_read_b128 a[232:235], v248 offset:12320\n"
        "v_add_u32 v246, s45, v246\n"
        "v_mfma_f32_16x16x32_bf16 v[172:175], v[152:155], a[216:219], v[172:175]\n"
        "s_waitcnt lgkmcnt(3)\n"
        "v_exp_f32 v69, v69\n"
        "v_mfma_f32_16x16x32_bf16 v[172:175], v[160:163], a[220:223], v[172:175]\n"
        "v_cvt_pk_bf16_f32 v66, v68, v69\n"
        "v_add_u32 v247, s45, v247\n"
        "v_mfma_f32_16x16x32_bf16 v[180:183], v[164:167], a[220:223], v[180:183]\n"
        "ds_read_b128 a[220:223], v248 offset:49184\n"
        "v_mfma_f32_16x16x32_bf16 v[180:183], v[156:159], a[216:219], v[180:183]\n"
        "s_waitcnt lgkmcnt(3)\n"
        "ds_read_b128 a[216:219], v254 offset:12336\n"
        "v_mfma_f32_16x16x32_bf16 v[184:187], v[156:159], a[224:227], v[184:187]\n"
        "s_waitcnt lgkmcnt(3)\n"
        "v_exp_f32 v70, v70\n"
        "v_mfma_f32_16x16x32_bf16 v[184:187], v[164:167], a[228:231], v[184:187]\n"
        "v_exp_f32 v71, v71\n"
        "v_mfma_f32_16x16x32_bf16 v[176:179], v[160:163], a[228:231], v[176:179]\n"
        "ds_read_b128 a[228:231], v254 offset:49200\n"
        "v_cvt_pk_bf16_f32 v67, v70, v71\n"
        "v_mfma_f32_16x16x32_bf16 v[176:179], v[152:155], a[224:227], v[176:179]\n"
        "s_waitcnt lgkmcnt(3)\n"
        "ds_read_b128 a[224:227], v248 offset:12352\n"
        "v_mfma_f32_16x16x32_bf16 v[212:215], v[152:155], a[232:235], v[212:215]\n"
        "s_waitcnt lgkmcnt(3)\n"
        "s_waitcnt vmcnt(0)\n"
        "s_barrier\n"
        "v_exp_f32 v72, v72\n"
        "v_mfma_f32_16x16x32_bf16 v[212:215], v[160:163], a[220:223], v[212:215]\n"
        "v_exp_f32 v73, v73\n"
        "v_mfma_f32_16x16x32_bf16 v[192:195], v[164:167], a[220:223], v[192:195]\n"
        "ds_read_b128 a[220:223], v248 offset:49216\n"
        "v_cvt_pk_bf16_f32 v68, v72, v73\n"
        "v_mfma_f32_16x16x32_bf16 v[192:195], v[156:159], a[232:235], v[192:195]\n"
        "s_waitcnt lgkmcnt(3)\n"
        "ds_read_b128 a[232:235], v254 offset:12368\n"
        "v_mfma_f32_16x16x32_bf16 v[196:199], v[156:159], a[216:219], v[196:199]\n"
        "s_waitcnt lgkmcnt(3)\n"
        "ds_read_b128 a[236:239], v240 offset:0\n"
        "v_mfma_f32_16x16x32_bf16 v[196:199], v[164:167], a[228:231], v[196:199]\n"
        "ds_read_b128 a[240:243], v241 offset:0\n"
        "v_mfma_f32_16x16x32_bf16 v[216:219], v[160:163], a[228:231], v[216:219]\n"
        "ds_read_b128 a[228:231], v254 offset:49232\n"
        "v_mfma_f32_16x16x32_bf16 v[216:219], v[152:155], a[216:219], v[216:219]\n"
        "s_waitcnt lgkmcnt(5)\n"
        "ds_read_b128 a[244:247], v240 offset:128\n"
        "v_mfma_f32_16x16x32_bf16 v[220:223], v[152:155], a[224:227], v[220:223]\n"
        "s_waitcnt lgkmcnt(5)\n"
        "ds_read_b128 a[248:251], v241 offset:128\n"
        "v_mfma_f32_16x16x32_bf16 v[220:223], v[160:163], a[220:223], v[220:223]\n"
        "ds_read_b128 a[216:219], v240 offset:256\n"
        "v_mfma_f32_16x16x32_bf16 v[200:203], v[164:167], a[220:223], v[200:203]\n"
        "ds_read_b128 a[220:223], v241 offset:256\n"
        "v_mfma_f32_16x16x32_bf16 v[200:203], v[156:159], a[224:227], v[200:203]\n"
        "s_waitcnt lgkmcnt(7)\n"
        "v_exp_f32 v74, v74\n"
        "v_mfma_f32_16x16x32_bf16 v[204:207], v[156:159], a[232:235], v[204:207]\n"
        "s_waitcnt lgkmcnt(4)\n"
        "v_exp_f32 v75, v75\n"
        "v_mfma_f32_16x16x32_bf16 v[204:207], v[164:167], a[228:231], v[204:207]\n"
        "s_sub_u32 s27, s27, 1\n"
        "v_cvt_pk_bf16_f32 v69, v74, v75\n"
        "v_mfma_f32_16x16x32_bf16 v[224:227], v[160:163], a[228:231], v[224:227]\n"
        "s_cmp_lg_u32 s27, 0\n"
        "v_mfma_f32_16x16x32_bf16 v[224:227], v[152:155], a[232:235], v[224:227]\n"
        "s_cbranch_scc1 10b\n"
        "20:\n"
        "s_waitcnt lgkmcnt(0)\n"
        "s_nop 7\n"
        "v_mfma_f32_16x16x32_bf16 v[48:51], a[252:255], v[64:67], 0\n"
        "s_and_b32 s46, s42, 1\n"
        "s_lshl_b32 s31, s46, 8\n"
        "s_mul_i32 s45, s46, 786432\n"
        "s_sub_u32 s37, s37, s45\n"
        "s_cmp_eq_u32 s46, 0\n"
        "s_cselect_b32 s43, s43, s44\n"
        "s_add_u32 s28, s37, 786432\n"
        "s_cmp_eq_u32 s28, 100663296\n"
        "s_cselect_b32 s28, 0, s28\n"
        "v_fma_f32 v76, v76, s24, v236\n"
        "v_exp_f32 v76, v76\n"
        "v_fma_f32 v77, v77, s24, v236\n"
        "v_exp_f32 v77, v77\n"
        "v_fma_f32 v78, v78, s24, v236\n"
        "v_cvt_pk_bf16_f32 v70, v76, v77\n"
        "v_exp_f32 v78, v78\n"
        "v_fma_f32 v79, v79, s24, v236\n"
        "v_exp_f32 v79, v79\n"
        "v_subrev_u32 v247, s31, v247\n"
        "v_cvt_pk_bf16_f32 v71, v78, v79\n"
        "s_nop 1\n"
        "v_mfma_f32_16x16x32_bf16 v[48:51], a[252:255], v[68:71], v[48:51]\n"
        "v_cvt_pk_bf16_f32 v232, v172, v176\n"
        "v_cvt_pk_bf16_f32 v233, v212, v216\n"
        "v_cvt_pk_bf16_f32 v234, v220, v224\n"
        "v_fma_f32 v80, v80, s24, v237\n"
        "v_exp_f32 v80, v80\n"
        "v_mfma_f32_16x16x32_bf16 v[52:55], v[132:135], v[68:71], 0\n"
        "buffer_store_dwordx3 v[232:234], v250, s[16:19], s38 offen offset:0 sc1\n"
        "ds_read_b128 a[216:219], v248 offset:0\n"
        "v_mfma_f32_16x16x32_bf16 v[52:55], v[128:131], v[64:67], v[52:55]\n"
        "ds_read_b128 a[220:223], v248 offset:6144\n"
        "v_cvt_pk_bf16_f32 v232, v173, v177\n"
        "v_cvt_pk_bf16_f32 v233, v213, v217\n"
        "v_cvt_pk_bf16_f32 v234, v221, v225\n"
        "v_fma_f32 v81, v81, s24, v237\n"
        "v_fma_f32 v82, v82, s24, v237\n"
        "v_mfma_f32_16x16x32_bf16 v[56:59], v[136:139], v[64:67], 0\n"
        "buffer_store_dwordx3 v[232:234], v250, s[16:19], s38 offen offset:384 sc1\n"
        "ds_read_b128 a[224:227], v248 offset:36864\n"
        "v_mfma_f32_16x16x32_bf16 v[56:59], v[140:143], v[68:71], v[56:59]\n"
        "ds_read_b128 a[228:231], v248 offset:43008\n"
        "v_cvt_pk_bf16_f32 v232, v174, v178\n"
        "v_cvt_pk_bf16_f32 v233, v214, v218\n"
        "v_cvt_pk_bf16_f32 v234, v222, v226\n"
        "v_exp_f32 v81, v81\n"
        "v_mfma_f32_16x16x32_bf16 v[60:63], v[148:151], v[68:71], 0\n"
        "buffer_store_dwordx3 v[232:234], v250, s[16:19], s38 offen offset:768 sc1\n"
        "ds_read_b128 a[232:235], v248 offset:12288\n"
        "v_mfma_f32_16x16x32_bf16 v[60:63], v[144:147], v[64:67], v[60:63]\n"
        "ds_read_b128 a[236:239], v248 offset:49152\n"
        "v_cvt_pk_bf16_f32 v232, v175, v179\n"
        "v_cvt_pk_bf16_f32 v233, v215, v219\n"
        "v_cvt_pk_bf16_f32 v234, v223, v227\n"
        "v_cvt_pk_bf16_f32 v80, v80, v81\n"
        "v_fma_f32 v83, v83, s24, v237\n"
        "v_exp_f32 v82, v82\n"
        "v_exp_f32 v83, v83\n"
        "v_fma_f32 v84, v84, s24, v237\n"
        "v_cvt_pk_bf16_f32 v81, v82, v83\n"
        "v_exp_f32 v84, v84\n"
        "v_fma_f32 v85, v85, s24, v237\n"
        "v_exp_f32 v85, v85\n"
        "v_fma_f32 v86, v86, s24, v237\n"
        "v_cvt_pk_bf16_f32 v82, v84, v85\n"
        "v_exp_f32 v86, v86\n"
        "v_fma_f32 v87, v87, s24, v237\n"
        "v_exp_f32 v87, v87\n"
        "v_fma_f32 v88, v88, s24, v237\n"
        "v_cvt_pk_bf16_f32 v83, v86, v87\n"
        "s_nop 1\n"
        "v_mfma_f32_16x16x32_bf16 v[168:171], a[252:255], v[80:83], 0\n"
        "buffer_store_dwordx3 v[232:234], v250, s[16:19], s38 offen offset:1152 sc1\n"
        "ds_read_b128 a[240:243], v254 offset:16\n"
        "v_exp_f32 v88, v88\n"
        "v_fma_f32 v89, v89, s24, v237\n"
        "v_exp_f32 v89, v89\n"
        "v_fma_f32 v90, v90, s24, v237\n"
        "v_cvt_pk_bf16_f32 v84, v88, v89\n"
        "v_exp_f32 v90, v90\n"
        "v_fma_f32 v91, v91, s24, v237\n"
        "v_exp_f32 v91, v91\n"
        "v_fma_f32 v92, v92, s24, v237\n"
        "v_cvt_pk_bf16_f32 v85, v90, v91\n"
        "v_exp_f32 v92, v92\n"
        "v_fma_f32 v93, v93, s24, v237\n"
        "v_exp_f32 v93, v93\n"
        "v_fma_f32 v94, v94, s24, v237\n"
        "v_cvt_pk_bf16_f32 v86, v92, v93\n"
        "v_exp_f32 v94, v94\n"
        "v_fma_f32 v95, v95, s24, v237\n"
        "v_exp_f32 v95, v95\n"
        "v_cvt_pk_bf16_f32 v232, v180, v184\n"
        "v_cvt_pk_bf16_f32 v87, v94, v95\n"
        "s_nop 1\n"
        "v_mfma_f32_16x16x32_bf16 v[168:171], a[252:255], v[84:87], v[168:171]\n"
        "ds_read_b128 a[244:247], v254 offset:6160\n"
        "v_cvt_pk_bf16_f32 v233, v192, v196\n"
        "v_cvt_pk_bf16_f32 v234, v200, v204\n"
        "v_rcp_f32 v48, v48\n"
        "v_fma_f32 v96, v96, s24, v238\n"
        "v_mfma_f32_16x16x32_bf16 v[188:191], v[132:135], v[84:87], 0\n"
        "buffer_store_dwordx3 v[232:234], v250, s[16:19], s39 offen offset:0 sc1\n"
        "ds_read_b128 a[248:251], v254 offset:36880\n"
        "v_mfma_f32_16x16x32_bf16 v[188:191], v[128:131], v[80:83], v[188:191]\n"
        "v_cvt_pk_bf16_f32 v232, v181, v185\n"
        "v_cvt_pk_bf16_f32 v233, v193, v197\n"
        "v_cvt_pk_bf16_f32 v234, v201, v205\n"
        "v_mul_f32 v52, v48, v52\n"
        "v_mul_f32 v53, v48, v53\n"
        "v_mul_f32 v54, v48, v54\n"
        "v_mfma_f32_16x16x32_bf16 v[208:211], v[136:139], v[80:83], 0\n"
        "buffer_store_dwordx3 v[232:234], v250, s[16:19], s39 offen offset:384 sc1\n"
        "v_mul_f32 v55, v48, v55\n"
        "v_mfma_f32_16x16x32_bf16 v[208:211], v[140:143], v[84:87], v[208:211]\n"
        "v_cvt_pk_bf16_f32 v232, v182, v186\n"
        "v_cvt_pk_bf16_f32 v233, v194, v198\n"
        "v_cvt_pk_bf16_f32 v234, v202, v206\n"
        "v_cvt_pk_bf16_f32 v52, v52, v53\n"
        "v_cvt_pk_bf16_f32 v53, v54, v55\n"
        "v_mul_f32 v56, v48, v56\n"
        "v_mfma_f32_16x16x32_bf16 v[228:231], v[148:151], v[84:87], 0\n"
        "buffer_store_dwordx3 v[232:234], v250, s[16:19], s39 offen offset:768 sc1\n"
        "v_mul_f32 v57, v48, v57\n"
        "v_mfma_f32_16x16x32_bf16 v[228:231], v[144:147], v[80:83], v[228:231]\n"
        "v_cvt_pk_bf16_f32 v232, v183, v187\n"
        "v_cvt_pk_bf16_f32 v233, v195, v199\n"
        "v_cvt_pk_bf16_f32 v234, v203, v207\n"
        "v_mul_f32 v58, v48, v58\n"
        "v_mul_f32 v59, v48, v59\n"
        "v_cvt_pk_bf16_f32 v54, v56, v57\n"
        "v_cvt_pk_bf16_f32 v55, v58, v59\n"
        "v_mul_f32 v60, v48, v60\n"
        "v_mul_f32 v61, v48, v61\n"
        "v_mul_f32 v62, v48, v62\n"
        "v_mul_f32 v63, v48, v63\n"
        "v_cvt_pk_bf16_f32 v60, v60, v61\n"
        "v_cvt_pk_bf16_f32 v61, v62, v63\n"
        "ds_write_b128 v245, v[52:55] offset:0\n"
        "ds_write_b64 v246, v[60:61] offset:0\n"
        "v_rcp_f32 v168, v168\n"
        "v_exp_f32 v96, v96\n"
        "v_mul_f32 v188, v168, v188\n"
        "v_mul_f32 v189, v168, v189\n"
        "v_mul_f32 v190, v168, v190\n"
        "v_mul_f32 v191, v168, v191\n"
        "v_cvt_pk_bf16_f32 v188, v188, v189\n"
        "v_cvt_pk_bf16_f32 v189, v190, v191\n"
        "v_mul_f32 v208, v168, v208\n"
        "v_mul_f32 v209, v168, v209\n"
        "v_mul_f32 v210, v168, v210\n"
        "v_mul_f32 v211, v168, v211\n"
        "v_cvt_pk_bf16_f32 v190, v208, v209\n"
        "v_cvt_pk_bf16_f32 v191, v210, v211\n"
        "v_mul_f32 v228, v168, v228\n"
        "v_mul_f32 v229, v168, v229\n"
        "v_mul_f32 v230, v168, v230\n"
        "v_mul_f32 v231, v168, v231\n"
        "v_cvt_pk_bf16_f32 v228, v228, v229\n"
        "v_cvt_pk_bf16_f32 v229, v230, v231\n"
        "ds_write_b128 v245, v[188:191] offset:256\n"
        "ds_write_b64 v246, v[228:229] offset:256\n"
        "s_waitcnt lgkmcnt(0)\n"
        "s_barrier\n"
        "ds_read_b128 v[152:155], v247 offset:0\n"
        "s_waitcnt lgkmcnt(0)\n"
        "v_mfma_f32_16x16x32_bf16 v[0:3], v[152:155], a[216:219], 0\n"
        "buffer_store_dwordx3 v[232:234], v250, s[16:19], s39 offen offset:1152 sc1\n"
        "ds_read_b128 v[156:159], v247 offset:4096\n"
        "s_waitcnt lgkmcnt(0)\n"
        "v_mfma_f32_16x16x32_bf16 v[0:3], v[156:159], a[220:223], v[0:3]\n"
        "ds_read_b128 v[172:175], v247 offset:8192\n"
        "ds_read_b128 v[176:179], v247 offset:12288\n"
        "ds_read_b128 v[212:215], v247 offset:16384\n"
        "ds_read_b128 v[216:219], v247 offset:20480\n"
        "s_waitcnt lgkmcnt(3)\n"
        "v_mfma_f32_16x16x32_bf16 v[0:3], v[172:175], a[224:227], v[0:3]\n"
        "s_waitcnt lgkmcnt(2)\n"
        "ds_read_b128 a[216:219], v254 offset:43024\n"
        "ds_read_b128 a[220:223], v254 offset:12304\n"
        "v_fma_f32 v97, v97, s24, v238\n"
        "v_fma_f32 v98, v98, s24, v238\n"
        "v_mfma_f32_16x16x32_bf16 v[0:3], v[176:179], a[228:231], v[0:3]\n"
        "s_waitcnt lgkmcnt(3)\n"
        "ds_read_b128 a[224:227], v254 offset:49168\n"
        "ds_read_b128 a[228:231], v248 offset:32\n"
        "v_exp_f32 v97, v97\n"
        "v_mfma_f32_16x16x32_bf16 v[0:3], v[212:215], a[232:235], v[0:3]\n"
        "s_waitcnt lgkmcnt(4)\n"
        "ds_read_b128 a[232:235], v248 offset:6176\n"
        "v_exp_f32 v98, v98\n"
        "v_cvt_pk_bf16_f32 v96, v96, v97\n"
        "v_mfma_f32_16x16x32_bf16 v[0:3], v[216:219], a[236:239], v[0:3]\n"
        "ds_read_b128 a[236:239], v248 offset:36896\n"
        "v_fma_f32 v99, v99, s24, v238\n"
        "v_exp_f32 v99, v99\n"
        "v_mfma_f32_16x16x32_bf16 v[4:7], v[152:155], a[240:243], 0\n"
        "ds_read_b128 a[240:243], v248 offset:43040\n"
        "v_cvt_pk_bf16_f32 v97, v98, v99\n"
        "v_fma_f32 v100, v100, s24, v238\n"
        "v_fma_f32 v101, v101, s24, v238\n"
        "v_mfma_f32_16x16x32_bf16 v[4:7], v[156:159], a[244:247], v[4:7]\n"
        "ds_read_b128 a[244:247], v248 offset:12320\n"
        "v_exp_f32 v100, v100\n"
        "v_fma_f32 v102, v102, s24, v238\n"
        "v_mfma_f32_16x16x32_bf16 v[4:7], v[172:175], a[248:251], v[4:7]\n"
        "s_waitcnt lgkmcnt(7)\n"
        "ds_read_b128 a[248:251], v248 offset:49184\n"
        "v_exp_f32 v101, v101\n"
        "v_fma_f32 v103, v103, s24, v238\n"
        "v_mfma_f32_16x16x32_bf16 v[4:7], v[176:179], a[216:219], v[4:7]\n"
        "s_waitcnt lgkmcnt(7)\n"
        "v_cvt_pk_bf16_f32 v98, v100, v101\n"
        "v_exp_f32 v102, v102\n"
        "v_fma_f32 v104, v104, s24, v238\n"
        "v_mfma_f32_16x16x32_bf16 v[4:7], v[212:215], a[220:223], v[4:7]\n"
        "s_waitcnt lgkmcnt(6)\n"
        "v_exp_f32 v103, v103\n"
        "v_exp_f32 v104, v104\n"
        "v_mfma_f32_16x16x32_bf16 v[4:7], v[216:219], a[224:227], v[4:7]\n"
        "s_waitcnt lgkmcnt(5)\n"
        "v_cvt_pk_bf16_f32 v99, v102, v103\n"
        "v_fma_f32 v105, v105, s24, v238\n"
        "v_exp_f32 v105, v105\n"
        "v_mfma_f32_16x16x32_bf16 v[8:11], v[152:155], a[228:231], 0\n"
        "s_waitcnt lgkmcnt(4)\n"
        "ds_read_b128 a[216:219], v254 offset:48\n"
        "v_cvt_pk_bf16_f32 v100, v104, v105\n"
        "v_fma_f32 v106, v106, s24, v238\n"
        "v_fma_f32 v107, v107, s24, v238\n"
        "v_mfma_f32_16x16x32_bf16 v[8:11], v[156:159], a[232:235], v[8:11]\n"
        "s_waitcnt lgkmcnt(4)\n"
        "ds_read_b128 a[220:223], v254 offset:6192\n"
        "v_exp_f32 v106, v106\n"
        "v_fma_f32 v108, v108, s24, v238\n"
        "v_mfma_f32_16x16x32_bf16 v[8:11], v[172:175], a[236:239], v[8:11]\n"
        "s_waitcnt lgkmcnt(4)\n"
        "ds_read_b128 a[224:227], v254 offset:36912\n"
        "v_exp_f32 v107, v107\n"
        "v_fma_f32 v109, v109, s24, v238\n"
        "v_mfma_f32_16x16x32_bf16 v[8:11], v[176:179], a[240:243], v[8:11]\n"
        "s_waitcnt lgkmcnt(4)\n"
        "ds_read_b128 a[228:231], v254 offset:43056\n"
        "v_cvt_pk_bf16_f32 v101, v106, v107\n"
        "v_exp_f32 v108, v108\n"
        "v_mfma_f32_16x16x32_bf16 v[8:11], v[212:215], a[244:247], v[8:11]\n"
        "s_waitcnt lgkmcnt(4)\n"
        "ds_read_b128 a[232:235], v254 offset:12336\n"
        "v_exp_f32 v109, v109\n"
        "v_fma_f32 v110, v110, s24, v238\n"
        "v_mfma_f32_16x16x32_bf16 v[8:11], v[216:219], a[248:251], v[8:11]\n"
        "ds_read_b128 a[236:239], v254 offset:49200\n"
        "s_add_u32 s38, s37, s43\n"
        "v_cvt_pk_bf16_f32 v102, v108, v109\n"
        "v_exp_f32 v110, v110\n"
        "v_mfma_f32_16x16x32_bf16 v[64:67], a[252:255], v[96:99], 0\n"
        "ds_read_b128 a[240:243], v248 offset:64\n"
        "s_add_u32 s39, s28, s43\n"
        "v_fma_f32 v111, v111, s24, v238\n"
        "v_exp_f32 v111, v111\n"
        "v_fma_f32 v112, v112, s24, v239\n"
        "v_cvt_pk_bf16_f32 v103, v110, v111\n"
        "s_nop 1\n"
        "v_mfma_f32_16x16x32_bf16 v[64:67], a[252:255], v[100:103], v[64:67]\n"
        "ds_read_b128 a[244:247], v248 offset:6208\n"
        "v_exp_f32 v112, v112\n"
        "v_fma_f32 v113, v113, s24, v239\n"
        "v_mfma_f32_16x16x32_bf16 v[68:71], v[132:135], v[100:103], 0\n"
        "ds_read_b128 a[248:251], v248 offset:36928\n"
        "v_exp_f32 v113, v113\n"
        "v_fma_f32 v114, v114, s24, v239\n"
        "v_mfma_f32_16x16x32_bf16 v[68:71], v[128:131], v[96:99], v[68:71]\n"
        "v_rcp_f32 v64, v64\n"
        "v_exp_f32 v114, v114\n"
        "v_mfma_f32_16x16x32_bf16 v[72:75], v[136:139], v[96:99], 0\n"
        "v_cvt_pk_bf16_f32 v112, v112, v113\n"
        "v_fma_f32 v115, v115, s24, v239\n"
        "v_exp_f32 v115, v115\n"
        "v_mfma_f32_16x16x32_bf16 v[72:75], v[140:143], v[100:103], v[72:75]\n"
        "v_cvt_pk_bf16_f32 v113, v114, v115\n"
        "v_mul_f32 v68, v64, v68\n"
        "v_mul_f32 v69, v64, v69\n"
        "v_mul_f32 v70, v64, v70\n"
        "v_mfma_f32_16x16x32_bf16 v[76:79], v[148:151], v[100:103], 0\n"
        "v_mul_f32 v71, v64, v71\n"
        "v_cvt_pk_bf16_f32 v68, v68, v69\n"
        "v_cvt_pk_bf16_f32 v69, v70, v71\n"
        "v_mul_f32 v72, v64, v72\n"
        "v_mfma_f32_16x16x32_bf16 v[76:79], v[144:147], v[96:99], v[76:79]\n"
        "s_waitcnt lgkmcnt(8)\n"
        "v_mul_f32 v73, v64, v73\n"
        "v_mul_f32 v74, v64, v74\n"
        "v_mul_f32 v75, v64, v75\n"
        "v_cvt_pk_bf16_f32 v70, v72, v73\n"
        "v_mfma_f32_16x16x32_bf16 v[12:15], v[152:155], a[216:219], 0\n"
        "s_waitcnt lgkmcnt(7)\n"
        "ds_read_b128 a[216:219], v248 offset:43072\n"
        "v_cvt_pk_bf16_f32 v71, v74, v75\n"
        "v_fma_f32 v116, v116, s24, v239\n"
        "v_mul_f32 v76, v64, v76\n"
        "v_mfma_f32_16x16x32_bf16 v[12:15], v[156:159], a[220:223], v[12:15]\n"
        "s_waitcnt lgkmcnt(7)\n"
        "ds_read_b128 a[220:223], v248 offset:12352\n"
        "v_mul_f32 v77, v64, v77\n"
        "v_mul_f32 v78, v64, v78\n"
        "v_mul_f32 v79, v64, v79\n"
        "v_mfma_f32_16x16x32_bf16 v[12:15], v[172:175], a[224:227], v[12:15]\n"
        "s_waitcnt lgkmcnt(7)\n"
        "ds_write_b128 v245, v[68:71] offset:512\n"
        "v_mfma_f32_16x16x32_bf16 v[12:15], v[176:179], a[228:231], v[12:15]\n"
        "s_waitcnt lgkmcnt(7)\n"
        "ds_read_b128 a[224:227], v248 offset:49216\n"
        "ds_read_b128 a[228:231], v254 offset:80\n"
        "v_cvt_pk_bf16_f32 v76, v76, v77\n"
        "v_cvt_pk_bf16_f32 v77, v78, v79\n"
        "v_mfma_f32_16x16x32_bf16 v[12:15], v[212:215], a[232:235], v[12:15]\n"
        "s_waitcnt lgkmcnt(8)\n"
        "ds_read_b128 a[232:235], v254 offset:6224\n"
        "v_exp_f32 v116, v116\n"
        "v_fma_f32 v117, v117, s24, v239\n"
        "v_mfma_f32_16x16x32_bf16 v[12:15], v[216:219], a[236:239], v[12:15]\n"
        "s_waitcnt lgkmcnt(8)\n"
        "ds_write_b64 v246, v[76:77] offset:512\n"
        "ds_read_b128 a[236:239], v254 offset:36944\n"
        "v_mfma_f32_16x16x32_bf16 v[16:19], v[152:155], a[240:243], 0\n"
        "s_waitcnt lgkmcnt(9)\n"
        "ds_read_b128 a[240:243], v254 offset:43088\n"
        "v_exp_f32 v117, v117\n"
        "v_fma_f32 v118, v118, s24, v239\n"
        "v_mfma_f32_16x16x32_bf16 v[16:19], v[156:159], a[244:247], v[16:19]\n"
        "s_waitcnt lgkmcnt(9)\n"
        "ds_read_b128 a[244:247], v254 offset:12368\n"
        "v_exp_f32 v118, v118\n"
        "v_cvt_pk_bf16_f32 v114, v116, v117\n"
        "v_mfma_f32_16x16x32_bf16 v[16:19], v[172:175], a[248:251], v[16:19]\n"
        "s_waitcnt lgkmcnt(9)\n"
        "ds_read_b128 a[248:251], v254 offset:49232\n"
        "v_fma_f32 v119, v119, s24, v239\n"
        "v_exp_f32 v119, v119\n"
        "v_mfma_f32_16x16x32_bf16 v[16:19], v[176:179], a[216:219], v[16:19]\n"
        "s_waitcnt lgkmcnt(9)\n"
        "v_cvt_pk_bf16_f32 v115, v118, v119\n"
        "v_fma_f32 v120, v120, s24, v239\n"
        "v_exp_f32 v120, v120\n"
        "v_mfma_f32_16x16x32_bf16 v[16:19], v[212:215], a[220:223], v[16:19]\n"
        "s_waitcnt lgkmcnt(7)\n"
        "v_fma_f32 v121, v121, s24, v239\n"
        "v_exp_f32 v121, v121\n"
        "v_fma_f32 v122, v122, s24, v239\n"
        "v_mfma_f32_16x16x32_bf16 v[16:19], v[216:219], a[224:227], v[16:19]\n"
        "s_waitcnt lgkmcnt(6)\n"
        "v_cvt_pk_bf16_f32 v116, v120, v121\n"
        "v_exp_f32 v122, v122\n"
        "v_fma_f32 v123, v123, s24, v239\n"
        "v_mfma_f32_16x16x32_bf16 v[20:23], v[152:155], a[228:231], 0\n"
        "s_waitcnt lgkmcnt(5)\n"
        "ds_read_b128 a[216:219], v248 offset:0\n"
        "v_exp_f32 v123, v123\n"
        "v_fma_f32 v124, v124, s24, v239\n"
        "v_mfma_f32_16x16x32_bf16 v[20:23], v[156:159], a[232:235], v[20:23]\n"
        "s_waitcnt lgkmcnt(4)\n"
        "ds_read_b128 a[220:223], v248 offset:6144\n"
        "v_cvt_pk_bf16_f32 v117, v122, v123\n"
        "v_exp_f32 v124, v124\n"
        "v_mfma_f32_16x16x32_bf16 v[20:23], v[172:175], a[236:239], v[20:23]\n"
        "s_waitcnt lgkmcnt(4)\n"
        "ds_read_b128 a[224:227], v248 offset:36864\n"
        "v_fma_f32 v125, v125, s24, v239\n"
        "v_exp_f32 v125, v125\n"
        "v_mfma_f32_16x16x32_bf16 v[20:23], v[176:179], a[240:243], v[20:23]\n"
        "s_waitcnt lgkmcnt(4)\n"
        "ds_read_b128 a[228:231], v248 offset:43008\n"
        "v_cvt_pk_bf16_f32 v118, v124, v125\n"
        "v_fma_f32 v126, v126, s24, v239\n"
        "v_fma_f32 v127, v127, s24, v239\n"
        "v_mfma_f32_16x16x32_bf16 v[20:23], v[212:215], a[244:247], v[20:23]\n"
        "s_waitcnt lgkmcnt(4)\n"
        "ds_read_b128 a[232:235], v248 offset:12288\n"
        "v_exp_f32 v126, v126\n"
        "v_cvt_pk_bf16_f32 v192, v0, v4\n"
        "v_mfma_f32_16x16x32_bf16 v[20:23], v[216:219], a[248:251], v[20:23]\n"
        "ds_read_b128 a[236:239], v248 offset:49152\n"
        "v_exp_f32 v127, v127\n"
        "v_cvt_pk_bf16_f32 v193, v8, v12\n"
        "v_mfma_f32_16x16x32_bf16 v[80:83], a[252:255], v[112:115], 0\n"
        "ds_read_b128 a[240:243], v254 offset:16\n"
        "v_cvt_pk_bf16_f32 v119, v126, v127\n"
        "v_cvt_pk_bf16_f32 v196, v1, v5\n"
        "v_cvt_pk_bf16_f32 v197, v9, v13\n"
        "v_mfma_f32_16x16x32_bf16 v[80:83], a[252:255], v[116:119], v[80:83]\n"
        "ds_read_b128 a[244:247], v254 offset:6160\n"
        "v_cvt_pk_bf16_f32 v194, v16, v20\n"
        "v_cvt_pk_bf16_f32 v198, v17, v21\n"
        "v_cvt_pk_bf16_f32 v200, v2, v6\n"
        "v_mfma_f32_16x16x32_bf16 v[84:87], v[132:135], v[116:119], 0\n"
        "buffer_store_dwordx3 v[192:194], v250, s[16:19], s38 offen offset:0 sc1\n"
        "v_mfma_f32_16x16x32_bf16 v[84:87], v[128:131], v[112:115], v[84:87]\n"
        "ds_read_b128 a[248:251], v254 offset:36880\n"
        "v_rcp_f32 v80, v80\n"
        "v_cvt_pk_bf16_f32 v201, v10, v14\n"
        "v_mfma_f32_16x16x32_bf16 v[88:91], v[136:139], v[112:115], 0\n"
        "v_cvt_pk_bf16_f32 v202, v18, v22\n"
        "v_cvt_pk_bf16_f32 v204, v3, v7\n"
        "v_cvt_pk_bf16_f32 v205, v11, v15\n"
        "v_cvt_pk_bf16_f32 v206, v19, v23\n"
        "v_mfma_f32_16x16x32_bf16 v[88:91], v[140:143], v[116:119], v[88:91]\n"
        "buffer_store_dwordx3 v[196:198], v250, s[16:19], s38 offen offset:384 sc1\n"
        "v_mfma_f32_16x16x32_bf16 v[92:95], v[148:151], v[116:119], 0\n"
        "v_mul_f32 v84, v80, v84\n"
        "v_mul_f32 v85, v80, v85\n"
        "v_mul_f32 v86, v80, v86\n"
        "v_mul_f32 v87, v80, v87\n"
        "v_mfma_f32_16x16x32_bf16 v[92:95], v[144:147], v[112:115], v[92:95]\n"
        "v_cvt_pk_bf16_f32 v84, v84, v85\n"
        "v_cvt_pk_bf16_f32 v85, v86, v87\n"
        "v_mul_f32 v88, v80, v88\n"
        "v_mul_f32 v89, v80, v89\n"
        "v_mul_f32 v90, v80, v90\n"
        "v_mul_f32 v91, v80, v91\n"
        "v_cvt_pk_bf16_f32 v86, v88, v89\n"
        "v_cvt_pk_bf16_f32 v87, v90, v91\n"
        "v_mul_f32 v92, v80, v92\n"
        "v_mul_f32 v93, v80, v93\n"
        "v_mul_f32 v94, v80, v94\n"
        "v_mul_f32 v95, v80, v95\n"
        "v_cvt_pk_bf16_f32 v92, v92, v93\n"
        "v_cvt_pk_bf16_f32 v93, v94, v95\n"
        "ds_write_b128 v245, v[84:87] offset:768\n"
        "ds_write_b64 v246, v[92:93] offset:768\n"
        "s_waitcnt lgkmcnt(0)\n"
        "s_barrier\n"
        "ds_read_b128 v[128:131], v247 offset:512\n"
        "s_waitcnt lgkmcnt(0)\n"
        "v_mfma_f32_16x16x32_bf16 v[24:27], v[128:131], a[216:219], 0\n"
        "ds_read_b128 v[132:135], v247 offset:4608\n"
        "ds_read_b128 v[136:139], v247 offset:8704\n"
        "s_waitcnt lgkmcnt(1)\n"
        "v_mfma_f32_16x16x32_bf16 v[24:27], v[132:135], a[220:223], v[24:27]\n"
        "s_waitcnt lgkmcnt(0)\n"
        "ds_read_b128 v[140:143], v247 offset:12800\n"
        "ds_read_b128 v[144:147], v247 offset:16896\n"
        "v_mfma_f32_16x16x32_bf16 v[24:27], v[136:139], a[224:227], v[24:27]\n"
        "s_waitcnt lgkmcnt(1)\n"
        "ds_read_b128 v[148:151], v247 offset:20992\n"
        "ds_read_b128 a[216:219], v254 offset:43024\n"
        "v_mfma_f32_16x16x32_bf16 v[24:27], v[140:143], a[228:231], v[24:27]\n"
        "s_waitcnt lgkmcnt(2)\n"
        "buffer_store_dwordx3 v[200:202], v250, s[16:19], s38 offen offset:768 sc1\n"
        "v_mfma_f32_16x16x32_bf16 v[24:27], v[144:147], a[232:235], v[24:27]\n"
        "s_waitcnt lgkmcnt(1)\n"
        "ds_read_b128 a[220:223], v254 offset:12304\n"
        "ds_read_b128 a[224:227], v254 offset:49168\n"
        "v_mfma_f32_16x16x32_bf16 v[24:27], v[148:151], a[236:239], v[24:27]\n"
        "ds_read_b128 a[228:231], v248 offset:32\n"
        "ds_read_b128 a[232:235], v248 offset:6176\n"
        "v_mfma_f32_16x16x32_bf16 v[28:31], v[128:131], a[240:243], 0\n"
        "buffer_store_dwordx3 v[204:206], v250, s[16:19], s38 offen offset:1152 sc1\n"
        "v_mfma_f32_16x16x32_bf16 v[28:31], v[132:135], a[244:247], v[28:31]\n"
        "ds_read_b128 a[236:239], v248 offset:36896\n"
        "ds_read_b128 a[240:243], v248 offset:43040\n"
        "v_mfma_f32_16x16x32_bf16 v[28:31], v[136:139], a[248:251], v[28:31]\n"
        "s_waitcnt lgkmcnt(6)\n"
        "ds_read_b128 a[244:247], v248 offset:12320\n"
        "ds_read_b128 a[248:251], v248 offset:49184\n"
        "v_mfma_f32_16x16x32_bf16 v[28:31], v[140:143], a[216:219], v[28:31]\n"
        "s_waitcnt lgkmcnt(7)\n"
        "ds_read_b128 a[216:219], v254 offset:48\n"
        "v_mfma_f32_16x16x32_bf16 v[28:31], v[144:147], a[220:223], v[28:31]\n"
        "s_waitcnt lgkmcnt(7)\n"
        "ds_read_b128 a[220:223], v254 offset:6192\n"
        "v_mfma_f32_16x16x32_bf16 v[28:31], v[148:151], a[224:227], v[28:31]\n"
        "s_waitcnt lgkmcnt(7)\n"
        "ds_read_b128 a[224:227], v254 offset:36912\n"
        "v_mfma_f32_16x16x32_bf16 v[32:35], v[128:131], a[228:231], 0\n"
        "s_waitcnt lgkmcnt(7)\n"
        "ds_read_b128 a[228:231], v254 offset:43056\n"
        "v_mfma_f32_16x16x32_bf16 v[32:35], v[132:135], a[232:235], v[32:35]\n"
        "s_waitcnt lgkmcnt(7)\n"
        "ds_read_b128 a[232:235], v254 offset:12336\n"
        "v_mfma_f32_16x16x32_bf16 v[32:35], v[136:139], a[236:239], v[32:35]\n"
        "s_waitcnt lgkmcnt(7)\n"
        "ds_read_b128 a[236:239], v254 offset:49200\n"
        "v_mfma_f32_16x16x32_bf16 v[32:35], v[140:143], a[240:243], v[32:35]\n"
        "s_waitcnt lgkmcnt(7)\n"
        "ds_read_b128 a[240:243], v248 offset:64\n"
        "v_cvt_pk_bf16_f32 v180, v24, v28\n"
        "v_mfma_f32_16x16x32_bf16 v[32:35], v[144:147], a[244:247], v[32:35]\n"
        "s_waitcnt lgkmcnt(7)\n"
        "ds_read_b128 a[244:247], v248 offset:6208\n"
        "v_cvt_pk_bf16_f32 v184, v25, v29\n"
        "v_mfma_f32_16x16x32_bf16 v[32:35], v[148:151], a[248:251], v[32:35]\n"
        "s_waitcnt lgkmcnt(7)\n"
        "ds_read_b128 a[248:251], v248 offset:36928\n"
        "v_cvt_pk_bf16_f32 v220, v26, v30\n"
        "v_mfma_f32_16x16x32_bf16 v[36:39], v[128:131], a[216:219], 0\n"
        "s_waitcnt lgkmcnt(7)\n"
        "ds_read_b128 a[216:219], v248 offset:43072\n"
        "v_cvt_pk_bf16_f32 v224, v27, v31\n"
        "v_mfma_f32_16x16x32_bf16 v[36:39], v[132:135], a[220:223], v[36:39]\n"
        "s_waitcnt lgkmcnt(7)\n"
        "ds_read_b128 a[220:223], v248 offset:12352\n"
        "v_mfma_f32_16x16x32_bf16 v[36:39], v[136:139], a[224:227], v[36:39]\n"
        "s_waitcnt lgkmcnt(7)\n"
        "ds_read_b128 a[224:227], v248 offset:49216\n"
        "v_mfma_f32_16x16x32_bf16 v[36:39], v[140:143], a[228:231], v[36:39]\n"
        "s_waitcnt lgkmcnt(7)\n"
        "ds_read_b128 a[228:231], v254 offset:80\n"
        "v_mfma_f32_16x16x32_bf16 v[36:39], v[144:147], a[232:235], v[36:39]\n"
        "s_waitcnt lgkmcnt(7)\n"
        "ds_read_b128 a[232:235], v254 offset:6224\n"
        "v_mfma_f32_16x16x32_bf16 v[36:39], v[148:151], a[236:239], v[36:39]\n"
        "s_waitcnt lgkmcnt(7)\n"
        "ds_read_b128 a[236:239], v254 offset:36944\n"
        "v_mfma_f32_16x16x32_bf16 v[40:43], v[128:131], a[240:243], 0\n"
        "s_waitcnt lgkmcnt(7)\n"
        "ds_read_b128 a[240:243], v254 offset:43088\n"
        "v_mfma_f32_16x16x32_bf16 v[40:43], v[132:135], a[244:247], v[40:43]\n"
        "s_waitcnt lgkmcnt(7)\n"
        "ds_read_b128 a[244:247], v254 offset:12368\n"
        "v_mfma_f32_16x16x32_bf16 v[40:43], v[136:139], a[248:251], v[40:43]\n"
        "s_waitcnt lgkmcnt(7)\n"
        "ds_read_b128 a[248:251], v254 offset:49232\n"
        "v_mfma_f32_16x16x32_bf16 v[40:43], v[140:143], a[216:219], v[40:43]\n"
        "s_waitcnt lgkmcnt(7)\n"
        "v_cvt_pk_bf16_f32 v181, v32, v36\n"
        "v_cvt_pk_bf16_f32 v185, v33, v37\n"
        "v_mfma_f32_16x16x32_bf16 v[40:43], v[144:147], a[220:223], v[40:43]\n"
        "s_waitcnt lgkmcnt(6)\n"
        "v_cvt_pk_bf16_f32 v221, v34, v38\n"
        "v_cvt_pk_bf16_f32 v225, v35, v39\n"
        "v_mfma_f32_16x16x32_bf16 v[40:43], v[148:151], a[224:227], v[40:43]\n"
        "s_waitcnt lgkmcnt(5)\n"
        "v_mfma_f32_16x16x32_bf16 v[44:47], v[128:131], a[228:231], 0\n"
        "s_waitcnt lgkmcnt(4)\n"
        "v_mfma_f32_16x16x32_bf16 v[44:47], v[132:135], a[232:235], v[44:47]\n"
        "s_waitcnt lgkmcnt(3)\n"
        "v_mfma_f32_16x16x32_bf16 v[44:47], v[136:139], a[236:239], v[44:47]\n"
        "s_waitcnt lgkmcnt(2)\n"
        "v_mfma_f32_16x16x32_bf16 v[44:47], v[140:143], a[240:243], v[44:47]\n"
        "s_waitcnt lgkmcnt(1)\n"
        "v_mfma_f32_16x16x32_bf16 v[44:47], v[144:147], a[244:247], v[44:47]\n"
        "s_waitcnt lgkmcnt(0)\n"
        "v_mfma_f32_16x16x32_bf16 v[44:47], v[148:151], a[248:251], v[44:47]\n"
        "s_nop 7\n"
        "v_cvt_pk_bf16_f32 v182, v40, v44\n"
        "buffer_store_dwordx3 v[180:182], v250, s[16:19], s39 offen offset:0 sc1\n"
        "v_cvt_pk_bf16_f32 v186, v41, v45\n"
        "buffer_store_dwordx3 v[184:186], v250, s[16:19], s39 offen offset:384 sc1\n"
        "v_cvt_pk_bf16_f32 v222, v42, v46\n"
        "buffer_store_dwordx3 v[220:222], v250, s[16:19], s39 offen offset:768 sc1\n"
        "v_cvt_pk_bf16_f32 v226, v43, v47\n"
        "buffer_store_dwordx3 v[224:226], v250, s[16:19], s39 offen offset:1152 sc1\n"
        : "+{v240}"(xr[0]), "+{v241}"(xr[1]), "+{v251}"(mr[0]), "+{v252}"(mr[1]), "+{v242}"(dm[0]), "+{v243}"(dm[1]), "+{v244}"(dm[2]), "+{v245}"(ow), "+{v246}"(ow2), "+{v247}"(ord), "+{v248}"(wor), "+{v249}"(ms), "+{v250}"(st), "+{v142}"(l16), "+{v170}"(id0), "+{v171}"(id1), "+{v139}"(rs), "+{v144}"(rr), "+{v140}"(oa), "+{v141}"(ob), "+{v147}"(od), "+{v145}"(rbs), "+{v146}"(rbw), "+{v254}"(wor2), "+{v255}"(ml), "+{v148}"(pf0), "+{v149}"(pf1), "+{v128}"(wsrc[0]), "+{v129}"(wsrc[1]), "+{v130}"(wsrc[2]), "+{v131}"(wrd[0]), "+{v132}"(wrd[1]), "+{v133}"(wrd[2]), "+{s47}"(ya), "+{s36}"(mk), "+{s37}"(raw), "+{s43}"(x0), "+{s44}"(x1)
        : "{s[8:11]}"(srdx), "{s[12:15]}"(srdm), "{s[16:19]}"(srdo), "{s[20:23]}"(srdw), "{s[52:55]}"(srdwo), "{s[56:59]}"(srdrp), "{s[60:63]}"(srdrb), "{s42}"(wv), "s"(ldsb)
        : "v0", "v1", "v2", "v3", "v4", "v5", "v6", "v7", "v8", "v9", "v10", "v11", "v12", "v13", "v14", "v15", "v16", "v17", "v18", "v19", "v20", "v21", "v22", "v23", "v24", "v25", "v26", "v27", "v28", "v29", "v30", "v31", "v32", "v33", "v34", "v35", "v36", "v37", "v38", "v39", "v40", "v41", "v42", "v43", "v44", "v45", "v46", "v47", "v48", "v49", "v50", "v51", "v52", "v53", "v54", "v55", "v56", "v57", "v58", "v59", "v60", "v61", "v62", "v63", "v64", "v65", "v66", "v67", "v68", "v69", "v70", "v71", "v72", "v73", "v74", "v75", "v76", "v77", "v78", "v79", "v80", "v81", "v82", "v83", "v84", "v85", "v86", "v87", "v88", "v89", "v90", "v91", "v92", "v93", "v94", "v95", "v96", "v97", "v98", "v99", "v100", "v101", "v102", "v103", "v104", "v105", "v106", "v107", "v108", "v109", "v110", "v111", "v112", "v113", "v114", "v115", "v116", "v117", "v118", "v119", "v120", "v121", "v122", "v123", "v124", "v125", "v126", "v127", "v134", "v135", "v136", "v137", "v138", "v143", "v150", "v151", "v152", "v153", "v154", "v155", "v156", "v157", "v158", "v159", "v160", "v161", "v162", "v163", "v164", "v165", "v166", "v167", "v168", "v169", "v172", "v173", "v174", "v175", "v176", "v177", "v178", "v179", "v180", "v181", "v182", "v183", "v184", "v185", "v186", "v187", "v188", "v189", "v190", "v191", "v192", "v193", "v194", "v195", "v196", "v197", "v198", "v199", "v200", "v201", "v202", "v203", "v204", "v205", "v206", "v207", "v208", "v209", "v210", "v211", "v212", "v213", "v214", "v215", "v216", "v217", "v218", "v219", "v220", "v221", "v222", "v223", "v224", "v225", "v226", "v227", "v228", "v229", "v230", "v231", "v232", "v233", "v234", "v235", "v236", "v237", "v238", "v239", "v253", "a0", "a1", "a2", "a3", "a4", "a5", "a6", "a7", "a8", "a9", "a10", "a11", "a12", "a13", "a14", "a15", "a16", "a17", "a18", "a19", "a20", "a21", "a22", "a23", "a24", "a25", "a26", "a27", "a28", "a29", "a30", "a31", "a32", "a33", "a34", "a35", "a36", "a37", "a38", "a39", "a40", "a41", "a42", "a43", "a44", "a45", "a46", "a47", "a48", "a49", "a50", "a51", "a52", "a53", "a54", "a55", "a56", "a57", "a58", "a59", "a60", "a61", "a62", "a63", "a64", "a65", "a66", "a67", "a68", "a69", "a70", "a71", "a72", "a73", "a74", "a75", "a76", "a77", "a78", "a79", "a80", "a81", "a82", "a83", "a84", "a85", "a86", "a87", "a88", "a89", "a90", "a91", "a92", "a93", "a94", "a95", "a96", "a97", "a98", "a99", "a100", "a101", "a102", "a103", "a104", "a105", "a106", "a107", "a108", "a109", "a110", "a111", "a112", "a113", "a114", "a115", "a116", "a117", "a118", "a119", "a120", "a121", "a122", "a123", "a124", "a125", "a126", "a127", "a128", "a129", "a130", "a131", "a132", "a133", "a134", "a135", "a136", "a137", "a138", "a139", "a140", "a141", "a142", "a143", "a144", "a145", "a146", "a147", "a148", "a149", "a150", "a151", "a152", "a153", "a154", "a155", "a156", "a157", "a158", "a159", "a160", "a161", "a162", "a163", "a164", "a165", "a166", "a167", "a168", "a169", "a170", "a171", "a172", "a173", "a174", "a175", "a176", "a177", "a178", "a179", "a180", "a181", "a182", "a183", "a184", "a185", "a186", "a187", "a188", "a189", "a190", "a191", "a192", "a193", "a194", "a195", "a196", "a197", "a198", "a199", "a200", "a201", "a202", "a203", "a204", "a205", "a206", "a207", "a208", "a209", "a210", "a211", "a212", "a213", "a214", "a215", "a216", "a217", "a218", "a219", "a220", "a221", "a222", "a223", "a224", "a225", "a226", "a227", "a228", "a229", "a230", "a231", "a232", "a233", "a234", "a235", "a236", "a237", "a238", "a239", "a240", "a241", "a242", "a243", "a244", "a245", "a246", "a247", "a248", "a249", "a250", "a251", "a252", "a253", "a254", "a255", "s24", "s25", "s26", "s27", "s28", "s29", "s30", "s31", "s34", "s35", "s38", "s39", "s40", "s41", "s45", "s46", "s48", "s49", "s50", "s51", "s64", "s65", "s66", "s67", "vcc", "scc", "memory");
}


__global__ __launch_bounds__(256, 1) __attribute__((aligned(4096))) void win_attn_kernel(const char* __restrict__ x, const char* __restrict__ wqkv,
                                                          const char* __restrict__ wo, const char* __restrict__ rpb,
                                                          const char* __restrict__ rpi, const char* __restrict__ mask,
                                                          char* __restrict__ out, char* ws) {
    __shared__ __attribute__((aligned(16))) char smem[163840];
    unsigned tid = threadIdx.x, w, nit;
    unsigned long long hist;
    const unsigned ldsb = (unsigned)(size_t)smem;
    asm volatile(
        "s_load_dwordx2 s[8:9], s[0:1], 0\n"
        "s_load_dwordx2 s[20:21], s[0:1], 8\n"
        "s_load_dwordx2 s[52:53], s[0:1], 16\n"
        "s_load_dwordx2 s[60:61], s[0:1], 24\n"
        "s_load_dwordx2 s[56:57], s[0:1], 32\n"
        "s_load_dwordx2 s[12:13], s[0:1], 40\n"
        "s_load_dwordx2 s[16:17], s[0:1], 48\n"
        "v_readfirstlane_b32 s42, v0\n"
        "v_and_b32 v1, 63, v0\n"
        "v_lshlrev_b32 v145, 3, v0\n"
        "s_lshr_b32 s42, s42, 6\n"
        "s_bfe_u32 s41, s2, 0x50003\n"
        "s_lshl2_add_u32 s41, s41, s42\n"
        "s_lshl_b32 s49, s41, 8\n"
        "v_and_b32 v10, 1, v0\n"
        "v_lshl_add_u32 v10, v10, 7, s49\n"
        "v_min_u32 v10, 32256, v10\n"
        "s_getpc_b64 s[38:39]\n"
        "global_load_dword v66, v10, s[38:39]\n"
        "v_bfe_u32 v2, v0, 4, 2\n"
        "s_and_b32 s51, s42, 1\n"
        "s_lshl_b32 s66, s51, 3\n"
        "v_xor_b32 v11, v1, v2\n"
        "v_xor_b32 v11, s66, v11\n"
        "v_lshlrev_b32 v139, 4, v11\n"
        "v_xor_b32 v144, 64, v139\n"
        "s_lshl_b32 s34, s42, 12\n"
        "s_mul_i32 s40, s42, 3072\n"
        "s_and_b32 s31, s2, 63\n"
        "s_lshl_b32 s31, s31, 3\n"
        "s_add_u32 s31, s31, 4\n"
        "s_lshr_b32 s45, s2, 6\n"
        "s_lshl_b32 s45, s45, 3\n"
        "s_add_u32 s47, s45, s42\n"
        "s_add_u32 s47, s47, 68\n"
        "s_mul_i32 s47, s47, 196608\n"
        "s_lshl_b32 s46, s42, 1\n"
        "s_lshl_b32 s49, s41, 11\n"
        "v_lshl_add_u32 v148, v1, 7, s49\n"
        "v_lshl_or_b32 v148, v2, 28, v148\n"
        "s_mov_b32 s62, 1800\n"
        "s_mov_b32 s63, 0x20000\n"
        "s_mov_b32 s58, 16384\n"
        "s_mov_b32 s59, 0x20000\n"
        "s_mov_b32 s22, 221184\n"
        "s_mov_b32 s23, 0x20000\n"
        "s_mov_b32 s10, 100663296\n"
        "s_mov_b32 s11, 0x20000\n"
        "v_mul_u32_u24 v15, 171, v1\n"
        "v_lshrrev_b32 v15, 12, v15\n"
        "v_mul_u32_u24 v13, 24, v15\n"
        "v_sub_u32 v18, v1, v13\n"
        "v_bfe_u32 v21, v15, 1, 1\n"
        "v_add_u32 v13, s31, v15\n"
        "v_and_b32 v13, 0x1ff, v13\n"
        "v_mul_u32_u24 v13, 384, v13\n"
        "v_bitop3_b32 v14, v21, v18, s46 bitop3:0x36\n"
        "v_lshl_add_u32 v242, v14, 4, v13\n"
        "v_add_u32 v12, 64, v1\n"
        "v_mul_u32_u24 v16, 171, v12\n"
        "v_lshrrev_b32 v16, 12, v16\n"
        "v_mul_u32_u24 v13, 24, v16\n"
        "v_sub_u32 v19, v12, v13\n"
        "v_bfe_u32 v22, v16, 1, 1\n"
        "v_add_u32 v13, s31, v16\n"
        "v_and_b32 v13, 0x1ff, v13\n"
        "v_mul_u32_u24 v13, 384, v13\n"
        "v_bitop3_b32 v14, v22, v19, s46 bitop3:0x36\n"
        "v_lshl_add_u32 v243, v14, 4, v13\n"
        "v_add_u32 v12, 128, v1\n"
        "v_mul_u32_u24 v17, 171, v12\n"
        "v_lshrrev_b32 v17, 12, v17\n"
        "v_mul_u32_u24 v13, 24, v17\n"
        "v_sub_u32 v20, v12, v13\n"
        "v_bfe_u32 v23, v17, 1, 1\n"
        "v_add_u32 v13, s31, v17\n"
        "v_and_b32 v13, 0x1ff, v13\n"
        "v_mul_u32_u24 v13, 384, v13\n"
        "v_bitop3_b32 v14, v23, v20, s46 bitop3:0x36\n"
        "v_lshl_add_u32 v244, v14, 4, v13\n"
        "s_mov_b32 s14, 33554432\n"
        "s_mov_b32 s15, 0x20000\n"
        "v_bfe_u32 v4, v0, 4, 1\n"
        "s_lshl_b32 s66, s51, 2\n"
        "v_bitop3_b32 v11, v4, v1, s66 bitop3:0x36\n"
        "v_lshlrev_b32 v249, 4, v11\n"
        "s_waitcnt lgkmcnt(0)\n"
        "buffer_load_dword v64, v148, s[20:23], 0 offen\n"
        "buffer_load_dwordx2 v[28:29], v145, s[60:63], 0 offen\n"
        "s_add_u32 m0, s34, 122880\n"
        "s_mov_b32 s64, s34\n"
        "buffer_load_dwordx4 v139, s[56:59], s64 offen lds\n"
        "v_xor_b32 v149, 32, v249\n"
        "s_lshl_b32 s48, s42, 11\n"
        "s_lshl_b32 s36, s2, 13\n"
        "s_add_u32 s36, s36, s48\n"
        "s_add_u32 s36, s36, 4194304\n"
        "s_add_u32 s48, s48, 147456\n"
        "s_mov_b32 s54, 73728\n"
        "s_mov_b32 s55, 0x20000\n"
        "s_mov_b32 s18, 100663296\n"
        "s_mov_b32 s19, 0x20000\n"
        "s_mul_i32 s65, s42, 9216\n"
        "v_lshl_or_b32 v13, v21, 1, s66\n"
        "buffer_load_dwordx4 v139, s[56:59], s64 offen offset:1024 lds\n"
        "v_xor_b32 v13, v13, v18\n"
        "v_mul_u32_u24 v14, 1152, v15\n"
        "v_lshl_add_u32 v128, v13, 4, v14\n"
        "v_lshl_or_b32 v13, v22, 1, s66\n"
        "v_xor_b32 v13, v13, v19\n"
        "v_mul_u32_u24 v14, 1152, v16\n"
        "v_lshl_add_u32 v129, v13, 4, v14\n"
        "v_subrev_u32 v129, 1024, v129\n"
        "v_lshl_or_b32 v13, v23, 1, s66\n"
        "v_xor_b32 v13, v13, v20\n"
        "v_mul_u32_u24 v14, 1152, v17\n"
        "v_lshl_add_u32 v130, v13, 4, v14\n"
        "buffer_load_dwordx4 v144, s[56:59], s64 offen offset:2048 lds\n"
        "v_subrev_u32 v130, 2048, v130\n"
        "v_bfe_u32 v6, v0, 2, 2\n"
        "v_and_b32 v7, 3, v0\n"
        "v_lshlrev_b32 v150, 4, v1\n"
        "v_lshl_add_u32 v8, v6, 3, v7\n"
        "v_and_b32 v9, 0xf0, v150\n"
        "v_lshlrev_b32 v146, 2, v0\n"
        "v_add_u32 v146, 139264, v146\n"
        "v_lshlrev_b32 v10, 5, v2\n"
        "v_xor_b32 v10, v10, v9\n"
        "v_lshl_add_u32 v10, v8, 8, v10\n"
        "v_add_u32 v140, 122880, v10\n"
        "buffer_load_dwordx4 v144, s[56:59], s64 offen offset:3072 lds\n"
        "v_xor_b32 v141, 16, v140\n"
        "v_xor_b32 v142, 128, v140\n"
        "v_xor_b32 v143, 144, v140\n"
        "s_lshl_b32 s67, s42, 10\n"
        "s_add_u32 s67, s67, 139264\n"
        "v_bfe_u32 v10, v0, 1, 3\n"
        "v_xor_b32 v10, v10, v2\n"
        "v_lshlrev_b32 v10, 4, v10\n"
        "v_mul_u32_u24 v11, 384, v8\n"
        "v_add_u32 v11, 73728, v11\n"
        "v_add_u32 v240, v10, v11\n"
        "v_xor_b32 v241, 64, v240\n"
        "s_add_u32 m0, s40, 73728\n"
        "s_sub_u32 s64, s47, 12582912\n"
        "buffer_load_dwordx4 v242, s[8:11], s64 offen nt lds\n"
        "v_lshl_add_u32 v10, v8, 7, v10\n"
        "v_add_u32 v251, 147456, v10\n"
        "v_xor_b32 v252, 64, v251\n"
        "v_lshl_add_u32 v10, v2, 3, v6\n"
        "v_mul_u32_u24 v10, 384, v10\n"
        "v_and_b32 v11, 1, v0\n"
        "v_lshl_add_u32 v10, v11, 3, v10\n"
        "v_and_b32 v12, 2, v6\n"
        "v_lshl_or_b32 v12, v4, 2, v12\n"
        "s_mul_i32 s35, s42, 6\n"
        "v_bfe_u32 v13, v0, 1, 1\n"
        "v_add_u32 v13, s35, v13\n"
        "s_add_u32 m0, s40, 74752\n"
        "s_sub_u32 s64, s47, 12582912\n"
        "buffer_load_dwordx4 v243, s[8:11], s64 offen nt lds\n"
        "v_xor_b32 v14, v13, v12\n"
        "v_lshl_add_u32 v131, v14, 4, v10\n"
        "v_add_u32 v136, 98304, v131\n"
        "v_add_u32 v13, 2, v13\n"
        "v_xor_b32 v14, v13, v12\n"
        "v_lshl_add_u32 v132, v14, 4, v10\n"
        "v_add_u32 v137, 98304, v132\n"
        "v_add_u32 v13, 2, v13\n"
        "v_xor_b32 v14, v13, v12\n"
        "v_lshl_add_u32 v133, v14, 4, v10\n"
        "v_add_u32 v138, 98304, v133\n"
        "v_lshlrev_b32 v10, 4, v11\n"
        "s_add_u32 m0, s40, 75776\n"
        "s_sub_u32 s64, s47, 12582912\n"
        "buffer_load_dwordx4 v244, s[8:11], s64 offen nt lds\n"
        "v_mov_b32 v12, 0x3fb9\n"
        "v_cmp_eq_u32 vcc, v6, v2\n"
        "v_lshlrev_b32 v10, v10, v12\n"
        "v_bfe_i32 v13, v0, 0, 1\n"
        "v_cndmask_b32 v10, 0, v10, vcc\n"
        "v_cmp_gt_u32 vcc, 2, v7\n"
        "v_and_b32 v13, 0xaaaaaaaa, v13\n"
        "v_or_b32 v255, 0x44444444, v13\n"
        "v_cndmask_b32 v170, 0, v10, vcc\n"
        "v_xor_b32 v171, v170, v10\n"
        "v_mov_b64 v[188:189], v[170:171]\n"
        "v_mov_b64 v[210:211], v[170:171]\n"
        "s_add_u32 m0, s40, 86016\n"
        "s_sub_u32 s64, s47, 11796480\n"
        "buffer_load_dwordx4 v242, s[8:11], s64 offen nt lds\n"
        "v_mov_b64 v[228:229], v[170:171]\n"
        "v_bfe_u32 v5, v0, 5, 1\n"
        "v_and_b32 v3, 15, v0\n"
        "v_lshl_add_u32 v10, v2, 10, v9\n"
        "s_add_u32 s49, s34, 73728\n"
        "v_add_u32 v245, s49, v10\n"
        "s_lshl_b32 s49, s51, 9\n"
        "s_add_u32 s49, s49, 73728\n"
        "v_add_u32 v247, s49, v10\n"
        "v_lshl_add_u32 v10, v5, 10, v9\n"
        "v_lshl_add_u32 v10, v4, 3, v10\n"
        "s_lshl_b32 s49, s42, 11\n"
        "s_add_u32 m0, s40, 87040\n"
        "s_sub_u32 s64, s47, 11796480\n"
        "buffer_load_dwordx4 v243, s[8:11], s64 offen nt lds\n"
        "s_add_u32 s49, s49, 90112\n"
        "v_add_u32 v246, s49, v10\n"
        "s_lshr_b32 s35, s42, 1\n"
        "s_mul_i32 s49, s35, 18432\n"
        "v_mul_u32_u24 v12, 1536, v2\n"
        "v_add_u32 v12, s49, v12\n"
        "v_mad_u32_u24 v13, v9, 6, v12\n"
        "v_lshl_add_u32 v248, v4, 4, v13\n"
        "v_mad_i32_i24 v254, v4, -16, v13\n"
        "v_mul_u32_u24 v10, 196608, v2\n"
        "v_lshl_add_u32 v10, v3, 2, v10\n"
        "s_lshl_b32 s49, s35, 6\n"
        "s_add_u32 m0, s40, 88064\n"
        "s_sub_u32 s64, s47, 11796480\n"
        "buffer_load_dwordx4 v244, s[8:11], s64 offen nt lds\n"
        "s_add_u32 s49, s49, 128\n"
        "v_add_u32 v250, s49, v10\n"
        "s_mul_i32 s26, s35, 192\n"
        "s_sub_u32 s26, s26, 128\n"
        "s_mul_i32 s43, s31, 384\n"
        "s_add_u32 s44, s31, 4\n"
        "s_and_b32 s44, s44, 511\n"
        "s_mul_i32 s44, s44, 384\n"
        "s_add_u32 s37, s45, s66\n"
        "s_add_u32 s37, s37, 4\n"
        "s_mul_i32 s37, s37, 196608\n"
        "s_mul_i32 s51, s51, 36864\n"
        "s_mov_b32 m0, s48\n"
        "s_sub_u32 s64, s36, 4194304\n"
        "buffer_load_dwordx4 v249, s[12:15], s64 offen nt lds\n"
        "s_mul_i32 s66, s35, 18432\n"
        "s_add_u32 s66, s66, s51\n"
        "v_add_u32 v12, s51, v12\n"
        "v_bfrev_b32 v14, v0\n"
        "v_bfe_u32 v10, v14, 28, 2\n"
        "v_mad_u32_u24 v10, v10, 6, v7\n"
        "v_bfe_u32 v13, v0, 1, 3\n"
        "v_mad_u32_u24 v13, v13, 6, v11\n"
        "v_add_u32 v13, 4, v13\n"
        "v_xor_b32 v10, v10, v4\n"
        "v_xor_b32 v13, v13, v4\n"
        "v_lshl_add_u32 v236, v10, 4, v12\n"
        "buffer_load_dwordx4 v149, s[12:15], s64 offen offset:1024 nt lds\n"
        "v_lshl_add_u32 v237, v13, 4, v12\n"
        "v_lshrrev_b32 v10, 2, v1\n"
        "v_mul_u32_u24 v10, 192, v10\n"
        "v_add_u32 v10, s66, v10\n"
        "v_lshlrev_b32 v13, 5, v4\n"
        "v_add_u32 v12, v10, v13\n"
        "v_sub_u32 v13, v10, v13\n"
        "v_lshrrev_b32 v14, 30, v14\n"
        "v_lshl_add_u32 v234, v14, 3, v12\n"
        "v_lshl_add_u32 v235, v14, 3, v13\n"
        "v_lshl_add_u32 v238, v7, 3, v12\n"
        "v_lshl_add_u32 v239, v7, 3, v13\n"
        "s_load_dwordx2 s[4:5], s[0:1], 56\n"
        "s_lshl_b32 s46, s35, 8\n"
        "s_mul_i32 s35, s35, -192\n"
        "s_movk_i32 s49, 1536\n"
        "v_mul_u32_u24 v10, 171, v1\n"
        "v_lshrrev_b32 v10, 11, v10\n"
        "v_mad_i32_i24 v13, v10, -12, v1\n"
        "v_lshrrev_b32 v13, 3, v13\n"
        "v_mul_u32_u24 v14, 192, v10\n"
        "v_add_u32 v14, s46, v14\n"
        "v_mad_i32_i24 v14, v13, s35, v14\n"
        "v_and_b32 v13, 4, v10\n"
        "v_lshlrev_b32 v13, 3, v13\n"
        "v_xad_u32 v124, v150, v13, v14\n"
        "v_lshrrev_b32 v13, 2, v10\n"
        "v_and_or_b32 v13, v10, 8, v13\n"
        "v_mad_u32_u24 v127, v13, s49, v124\n"
        "v_add_u32 v12, 64, v1\n"
        "v_mul_u32_u24 v10, 171, v12\n"
        "v_lshrrev_b32 v10, 11, v10\n"
        "v_mad_i32_i24 v13, v10, -12, v12\n"
        "v_lshrrev_b32 v13, 3, v13\n"
        "v_mul_u32_u24 v14, 192, v10\n"
        "v_add_u32 v14, s46, v14\n"
        "v_mad_i32_i24 v14, v13, s35, v14\n"
        "v_and_b32 v13, 4, v10\n"
        "v_lshlrev_b32 v13, 3, v13\n"
        "v_xad_u32 v125, v150, v13, v14\n"
        "v_lshrrev_b32 v13, 2, v10\n"
        "v_and_or_b32 v13, v10, 8, v13\n"
        "v_mad_u32_u24 v232, v13, s49, v125\n"
        "v_add_u32 v12, 128, v1\n"
        "v_mul_u32_u24 v10, 171, v12\n"
        "v_lshrrev_b32 v10, 11, v10\n"
        "v_mad_i32_i24 v13, v10, -12, v12\n"
        "v_lshrrev_b32 v13, 3, v13\n"
        "v_mul_u32_u24 v14, 192, v10\n"
        "v_add_u32 v14, s46, v14\n"
        "v_mad_i32_i24 v14, v13, s35, v14\n"
        "v_and_b32 v13, 4, v10\n"
        "v_lshlrev_b32 v13, 3, v13\n"
        "v_xad_u32 v126, v150, v13, v14\n"
        "v_lshrrev_b32 v13, 2, v10\n"
        "v_and_or_b32 v13, v10, 8, v13\n"
        "v_mad_u32_u24 v233, v13, s49, v126\n"
        "s_mov_b32 s24, 0x3fb8aa3b\n"
        "s_mov_b32 s25, 0x4f800000\n"
        "s_mov_b32 s50, 0x3e553b95\n"
        "s_mov_b32 s27, 0\n"
        "s_mov_b32 s28, 98304\n"
        "s_mov_b32 s30, 73728\n"
        "s_mov_b32 s29, 122880\n"
        "v_mov_b32 v253, 0x3f803f80\n"
        "s_nop 0\n"
        "v_accvgpr_write_b32 a252, v253\n"
        "v_accvgpr_write_b32 a253, v253\n"
        "v_accvgpr_write_b32 a254, v253\n"
        "v_accvgpr_write_b32 a255, v253\n"
        "s_waitcnt vmcnt(12)\n"
        "v_lshlrev_b32 v32, 16, v28\n"
        "v_and_b32 v33, 0xffff0000, v28\n"
        "v_lshlrev_b32 v34, 16, v29\n"
        "v_and_b32 v35, 0xffff0000, v29\n"
        "v_mul_f32 v32, s24, v32\n"
        "v_mul_f32 v33, s24, v33\n"
        "v_mul_f32 v34, s24, v34\n"
        "v_mul_f32 v35, s24, v35\n"
        "ds_write_b32 v146, v32 offset:0\n"
        "ds_write_b32 v146, v33 offset:1024\n"
        "ds_write_b32 v146, v34 offset:2048\n"
        "ds_write_b32 v146, v35 offset:3072\n"
        "s_cmp_eq_u32 s42, 0\n"
        "s_cselect_b32 s3, 4, 0\n"
        "s_cmp_eq_u32 s2, 0\n"
        "s_cselect_b32 s6, s3, 0\n"
        "s_mov_b32 s7, 0x20000\n"
        "v_mov_b32 v67, 0\n"
        "s_add_u32 s24, s42, 4\n"
        "s_mul_i32 s24, s24, 196608\n"
        "s_lshl_b32 s34, s42, 11\n"
        "s_and_b32 s37, s42, 1\n"
        "s_add_u32 s37, s37, 1\n"
        "s_mul_i32 s37, s37, 786432\n"
        "s_waitcnt vmcnt(8) lgkmcnt(0)\n"
        "s_barrier\n"
        "s_mov_b32 s56, s2\n"
        "s_add_u32 s57, s2, 256\n"
        "s_add_u32 s58, s2, 512\n"
        "s_mov_b32 s59, -1\n"
        "s_mul_i32 s60, s2, 0x01010101\n"
        "s_mov_b32 s61, s60\n"
        "ds_read_b128 v[0:3], v140 offset:0\n"
        "ds_read_b128 v[4:7], v141 offset:0\n"
        "ds_read_b128 v[8:11], v142 offset:0\n"
        "ds_read_b128 v[12:15], v143 offset:0\n"
        "ds_read_b128 v[16:19], v140 offset:1024\n"
        "ds_read_b128 v[20:23], v141 offset:1024\n"
        "ds_read_b128 v[24:27], v142 offset:1024\n"
        "ds_read_b128 v[28:31], v143 offset:1024\n"
        "ds_read_b128 v[32:35], v140 offset:8192\n"
        "ds_read_b128 v[36:39], v141 offset:8192\n"
        "ds_read_b128 v[40:43], v142 offset:8192\n"
        "ds_read_b128 v[44:47], v143 offset:8192\n"
        "ds_read_b128 v[48:51], v140 offset:9216\n"
        "ds_read_b128 v[52:55], v141 offset:9216\n"
        "ds_read_b128 v[56:59], v142 offset:9216\n"
        "ds_read_b128 v[60:63], v143 offset:9216\n"
        "s_add_u32 m0, s40, 0\n"
        "s_add_u32 s64, s65, 0\n"
        "buffer_load_dwordx4 v128, s[20:23], s64 offen lds\n"
        "s_waitcnt lgkmcnt(15)\n"
        "v_lshl_add_u32 v0, v0, 2, s67\n"
        "v_lshl_add_u32 v1, v1, 2, s67\n"
        "v_lshl_add_u32 v2, v2, 2, s67\n"
        "v_lshl_add_u32 v3, v3, 2, s67\n"
        "s_waitcnt lgkmcnt(14)\n"
        "v_lshl_add_u32 v4, v4, 2, s67\n"
        "v_lshl_add_u32 v5, v5, 2, s67\n"
        "v_lshl_add_u32 v6, v6, 2, s67\n"
        "v_lshl_add_u32 v7, v7, 2, s67\n"
        "ds_read_b32 v0, v0\n"
        "ds_read_b32 v1, v1\n"
        "ds_read_b32 v2, v2\n"
        "ds_read_b32 v3, v3\n"
        "ds_read_b32 v4, v4\n"
        "ds_read_b32 v5, v5\n"
        "ds_read_b32 v6, v6\n"
        "ds_read_b32 v7, v7\n"
        "buffer_load_dwordx4 v129, s[20:23], s64 offen offset:1024 lds\n"
        "s_waitcnt lgkmcnt(15)\n"
        "v_lshl_add_u32 v8, v8, 2, s67\n"
        "v_lshl_add_u32 v9, v9, 2, s67\n"
        "v_lshl_add_u32 v10, v10, 2, s67\n"
        "v_lshl_add_u32 v11, v11, 2, s67\n"
        "v_lshl_add_u32 v12, v12, 2, s67\n"
        "v_lshl_add_u32 v13, v13, 2, s67\n"
        "v_lshl_add_u32 v14, v14, 2, s67\n"
        "v_lshl_add_u32 v15, v15, 2, s67\n"
        "ds_read_b32 v8, v8\n"
        "ds_read_b32 v9, v9\n"
        "ds_read_b32 v10, v10\n"
        "ds_read_b32 v11, v11\n"
        "ds_read_b32 v12, v12\n"
        "ds_read_b32 v13, v13\n"
        "ds_read_b32 v14, v14\n"
        "ds_read_b32 v15, v15\n"
        "buffer_load_dwordx4 v130, s[20:23], s64 offen offset:2048 lds\n"
        "v_lshl_add_u32 v16, v16, 2, s67\n"
        "v_lshl_add_u32 v17, v17, 2, s67\n"
        "v_lshl_add_u32 v18, v18, 2, s67\n"
        "v_lshl_add_u32 v19, v19, 2, s67\n"
        "v_lshl_add_u32 v20, v20, 2, s67\n"
        "v_lshl_add_u32 v21, v21, 2, s67\n"
        "v_lshl_add_u32 v22, v22, 2, s67\n"
        "v_lshl_add_u32 v23, v23, 2, s67\n"
        "ds_read_b32 v16, v16\n"
        "ds_read_b32 v17, v17\n"
        "ds_read_b32 v18, v18\n"
        "ds_read_b32 v19, v19\n"
        "ds_read_b32 v20, v20\n"
        "ds_read_b32 v21, v21\n"
        "ds_read_b32 v22, v22\n"
        "ds_read_b32 v23, v23\n"
        "s_add_u32 m0, s40, 12288\n"
        "s_add_u32 s64, s65, 36864\n"
        "buffer_load_dwordx4 v128, s[20:23], s64 offen lds\n"
        "v_lshl_add_u32 v24, v24, 2, s67\n"
        "v_lshl_add_u32 v25, v25, 2, s67\n"
        "v_lshl_add_u32 v26, v26, 2, s67\n"
        "v_lshl_add_u32 v27, v27, 2, s67\n"
        "v_lshl_add_u32 v28, v28, 2, s67\n"
        "v_lshl_add_u32 v29, v29, 2, s67\n"
        "v_lshl_add_u32 v30, v30, 2, s67\n"
        "v_lshl_add_u32 v31, v31, 2, s67\n"
        "ds_read_b32 v24, v24\n"
        "ds_read_b32 v25, v25\n"
        "ds_read_b32 v26, v26\n"
        "ds_read_b32 v27, v27\n"
        "ds_read_b32 v28, v28\n"
        "ds_read_b32 v29, v29\n"
        "ds_read_b32 v30, v30\n"
        "ds_read_b32 v31, v31\n"
        "buffer_load_dwordx4 v129, s[20:23], s64 offen offset:1024 lds\n"
        "buffer_load_dwordx4 v130, s[20:23], s64 offen offset:2048 lds\n"
        "s_mov_b64 exec, 0\n"
        "s_cmp_eq_u32 s42, 0\n"
        "s_cbranch_scc1 41f\n"
        "s_cmp_eq_u32 s42, 1\n"
        "s_cbranch_scc1 42f\n"
        "s_cmp_eq_u32 s42, 2\n"
        "s_cbranch_scc1 43f\n"
        "s_branch 40f\n"
        ".fill 281, 4, 0xbf800000\n"
        "40:\n"
        "s_mov_b64 exec, -1\n"
        "s_add_u32 m0, s40, 24576\n"
        "s_add_u32 s64, s65, 73728\n"
        "buffer_load_dwordx4 v128, s[20:23], s64 offen lds\n"
        "v_lshl_add_u32 v32, v32, 2, s67\n"
        "v_lshl_add_u32 v33, v33, 2, s67\n"
        "v_lshl_add_u32 v34, v34, 2, s67\n"
        "v_lshl_add_u32 v35, v35, 2, s67\n"
        "s_waitcnt lgkmcnt(15)\n"
        "v_lshl_add_u32 v36, v36, 2, s67\n"
        "v_lshl_add_u32 v37, v37, 2, s67\n"
        "v_lshl_add_u32 v38, v38, 2, s67\n"
        "v_lshl_add_u32 v39, v39, 2, s67\n"
        "ds_read_b32 v32, v32\n"
        "ds_read_b32 v33, v33\n"
        "ds_read_b32 v34, v34\n"
        "ds_read_b32 v35, v35\n"
        "ds_read_b32 v36, v36\n"
        "ds_read_b32 v37, v37\n"
        "ds_read_b32 v38, v38\n"
        "ds_read_b32 v39, v39\n"
        "buffer_load_dwordx4 v129, s[20:23], s64 offen offset:1024 lds\n"
        "v_lshl_add_u32 v40, v40, 2, s67\n"
        "v_lshl_add_u32 v41, v41, 2, s67\n"
        "v_lshl_add_u32 v42, v42, 2, s67\n"
        "v_lshl_add_u32 v43, v43, 2, s67\n"
        "v_lshl_add_u32 v44, v44, 2, s67\n"
        "v_lshl_add_u32 v45, v45, 2, s67\n"
        "v_lshl_add_u32 v46, v46, 2, s67\n"
        "v_lshl_add_u32 v47, v47, 2, s67\n"
        "ds_read_b32 v40, v40\n"
        "ds_read_b32 v41, v41\n"
        "ds_read_b32 v42, v42\n"
        "ds_read_b32 v43, v43\n"
        "ds_read_b32 v44, v44\n"
        "ds_read_b32 v45, v45\n"
        "ds_read_b32 v46, v46\n"
        "ds_read_b32 v47, v47\n"
        "buffer_load_dwordx4 v130, s[20:23], s64 offen offset:2048 lds\n"
        "v_lshl_add_u32 v48, v48, 2, s67\n"
        "v_lshl_add_u32 v49, v49, 2, s67\n"
        "v_lshl_add_u32 v50, v50, 2, s67\n"
        "v_lshl_add_u32 v51, v51, 2, s67\n"
        "v_lshl_add_u32 v52, v52, 2, s67\n"
        "v_lshl_add_u32 v53, v53, 2, s67\n"
        "v_lshl_add_u32 v54, v54, 2, s67\n"
        "v_lshl_add_u32 v55, v55, 2, s67\n"
        "ds_read_b32 v48, v48\n"
        "ds_read_b32 v49, v49\n"
        "ds_read_b32 v50, v50\n"
        "ds_read_b32 v51, v51\n"
        "ds_read_b32 v52, v52\n"
        "ds_read_b32 v53, v53\n"
        "ds_read_b32 v54, v54\n"
        "ds_read_b32 v55, v55\n"
        "s_add_u32 m0, s40, 36864\n"
        "s_add_u32 s64, s65, 110592\n"
        "buffer_load_dwordx4 v128, s[20:23], s64 offen lds\n"
        "v_lshl_add_u32 v56, v56, 2, s67\n"
        "v_lshl_add_u32 v57, v57, 2, s67\n"
        "v_lshl_add_u32 v58, v58, 2, s67\n"
        "v_lshl_add_u32 v59, v59, 2, s67\n"
        "v_lshl_add_u32 v60, v60, 2, s67\n"
        "v_lshl_add_u32 v61, v61, 2, s67\n"
        "v_lshl_add_u32 v62, v62, 2, s67\n"
        "v_lshl_add_u32 v63, v63, 2, s67\n"
        "ds_read_b32 v56, v56\n"
        "ds_read_b32 v57, v57\n"
        "ds_read_b32 v58, v58\n"
        "ds_read_b32 v59, v59\n"
        "ds_read_b32 v60, v60\n"
        "ds_read_b32 v61, v61\n"
        "ds_read_b32 v62, v62\n"
        "ds_read_b32 v63, v63\n"
        "buffer_load_dwordx4 v129, s[20:23], s64 offen offset:1024 lds\n"
        "buffer_load_dwordx4 v130, s[20:23], s64 offen offset:2048 lds\n"
        "s_waitcnt vmcnt(14)\n"
        "s_barrier\n"
        "s_add_u32 m0, s40, 49152\n"
        "s_add_u32 s64, s65, 147456\n"
        "buffer_load_dwordx4 v128, s[20:23], s64 offen lds\n"
        "ds_read_b128 a[144:147], v240 offset:0\n"
        "ds_read_b128 a[148:151], v241 offset:0\n"
        "ds_read_b128 a[160:163], v240 offset:1536\n"
        "ds_read_b128 a[164:167], v241 offset:1536\n"
        "buffer_load_dwordx4 v129, s[20:23], s64 offen offset:1024 lds\n"
        "ds_read_b128 a[176:179], v240 offset:12288\n"
        "ds_read_b128 a[180:183], v241 offset:12288\n"
        "ds_read_b128 a[192:195], v240 offset:13824\n"
        "ds_read_b128 a[196:199], v241 offset:13824\n"
        "buffer_load_dwordx4 v130, s[20:23], s64 offen offset:2048 lds\n"
        "ds_read_b128 a[152:155], v240 offset:128\n"
        "ds_read_b128 a[156:159], v241 offset:128\n"
        "ds_read_b128 a[168:171], v240 offset:1664\n"
        "ds_read_b128 a[172:175], v241 offset:1664\n"
        "s_add_u32 m0, s40, 61440\n"
        "s_add_u32 s64, s65, 184320\n"
        "buffer_load_dwordx4 v128, s[20:23], s64 offen lds\n"
        "ds_read_b128 a[184:187], v240 offset:12416\n"
        "ds_read_b128 a[188:191], v241 offset:12416\n"
        "ds_read_b128 a[200:203], v240 offset:13952\n"
        "ds_read_b128 a[204:207], v241 offset:13952\n"
        "buffer_load_dwordx4 v129, s[20:23], s64 offen offset:1024 lds\n"
        "ds_read_b128 a[216:219], v240 offset:256\n"
        "ds_read_b128 a[220:223], v241 offset:256\n"
        "ds_read_b128 a[224:227], v240 offset:1792\n"
        "ds_read_b128 a[228:231], v241 offset:1792\n"
        "buffer_load_dwordx4 v130, s[20:23], s64 offen offset:2048 lds\n"
        "ds_read_b128 a[232:235], v240 offset:12544\n"
        "ds_read_b128 a[236:239], v241 offset:12544\n"
        "ds_read_b128 a[240:243], v240 offset:14080\n"
        "ds_read_b128 a[244:247], v241 offset:14080\n"
        "s_lshr_b32 s67, s42, 1\n"
        "s_add_u32 s67, s67, s40\n"
        "s_sub_u32 s67, -2, s67\n"
        "s_waitcnt vmcnt(12)\n"
        "s_barrier\n"
        "ds_read_b64_tr_b16 a[0:1], v131 offset:0\n"
        "ds_read_b64_tr_b16 a[2:3], v131 offset:1536\n"
        "ds_read_b64_tr_b16 a[4:5], v131 offset:12288\n"
        "ds_read_b64_tr_b16 a[6:7], v131 offset:13824\n"
        "ds_read_b64_tr_b16 a[24:25], v132 offset:0\n"
        "ds_read_b64_tr_b16 a[26:27], v132 offset:1536\n"
        "ds_read_b64_tr_b16 a[28:29], v132 offset:12288\n"
        "ds_read_b64_tr_b16 a[30:31], v132 offset:13824\n"
        "ds_read_b64_tr_b16 a[48:49], v133 offset:0\n"
        "ds_read_b64_tr_b16 a[50:51], v133 offset:1536\n"
        "ds_read_b64_tr_b16 a[52:53], v133 offset:12288\n"
        "ds_read_b64_tr_b16 a[54:55], v133 offset:13824\n"
        "s_waitcnt lgkmcnt(10)\n"
        "v_mfma_f32_16x16x32_bf16 v[96:99], a[0:3], a[144:147], 0\n"
        "s_waitcnt lgkmcnt(8)\n"
        "s_add_u32 m0, s40, 98304\n"
        "s_add_u32 s64, s65, 384\n"
        "v_mfma_f32_16x16x32_bf16 v[96:99], a[4:7], a[148:151], v[96:99]\n"
        "s_waitcnt lgkmcnt(4)\n"
        "buffer_load_dwordx4 v128, s[20:23], s64 offen lds\n"
        "v_mfma_f32_16x16x32_bf16 v[100:103], a[28:31], a[148:151], 0\n"
        "v_mfma_f32_16x16x32_bf16 v[100:103], a[24:27], a[144:147], v[100:103]\n"
        "s_waitcnt lgkmcnt(2)\n"
        "v_mfma_f32_16x16x32_bf16 v[104:107], a[48:51], a[144:147], 0\n"
        "s_waitcnt lgkmcnt(0)\n"
        "v_mfma_f32_16x16x32_bf16 v[104:107], a[52:55], a[148:151], v[104:107]\n"
        "buffer_load_dwordx4 v129, s[20:23], s64 offen offset:1024 lds\n"
        "v_mfma_f32_16x16x32_bf16 v[108:111], a[4:7], a[164:167], 0\n"
        "v_mfma_f32_16x16x32_bf16 v[108:111], a[0:3], a[160:163], v[108:111]\n"
        "v_mfma_f32_16x16x32_bf16 v[112:115], a[24:27], a[160:163], 0\n"
        "v_mfma_f32_16x16x32_bf16 v[112:115], a[28:31], a[164:167], v[112:115]\n"
        "buffer_load_dwordx4 v130, s[20:23], s64 offen offset:2048 lds\n"
        "v_mfma_f32_16x16x32_bf16 v[116:119], a[52:55], a[164:167], 0\n"
        "s_add_u32 m0, s40, 110592\n"
        "v_mfma_f32_16x16x32_bf16 v[116:119], a[48:51], a[160:163], v[116:119]\n"
        "s_add_u32 s64, s65, 37248\n"
        "v_mfma_f32_16x16x32_bf16 v[120:123], a[0:3], a[176:179], 0\n"
        "v_mfma_f32_16x16x32_bf16 v[120:123], a[4:7], a[180:183], v[120:123]\n"
        "s_waitcnt vmcnt(9)\n"
        "buffer_load_dwordx4 v128, s[20:23], s64 offen lds\n"
        "v_mfma_f32_16x16x32_bf16 v[92:95], a[28:31], a[180:183], 0\n"
        "s_barrier\n"
        "ds_read_b64_tr_b16 a[8:9], v131 offset:24576\n"
        "v_mfma_f32_16x16x32_bf16 v[92:95], a[24:27], a[176:179], v[92:95]\n"
        "ds_read_b64_tr_b16 a[10:11], v131 offset:26112\n"
        "v_mfma_f32_16x16x32_bf16 v[152:155], a[48:51], a[176:179], 0\n"
        "ds_read_b64_tr_b16 a[12:13], v131 offset:36864\n"
        "v_mfma_f32_16x16x32_bf16 v[152:155], a[52:55], a[180:183], v[152:155]\n"
        "buffer_load_dwordx4 v129, s[20:23], s64 offen offset:1024 lds\n"
        "v_mfma_f32_16x16x32_bf16 v[172:175], a[4:7], a[196:199], 0\n"
        "ds_read_b64_tr_b16 a[14:15], v131 offset:38400\n"
        "ds_read_b64_tr_b16 a[32:33], v132 offset:24576\n"
        "v_mfma_f32_16x16x32_bf16 v[172:175], a[0:3], a[192:195], v[172:175]\n"
        "ds_read_b64_tr_b16 a[34:35], v132 offset:26112\n"
        "v_mfma_f32_16x16x32_bf16 v[192:195], a[24:27], a[192:195], 0\n"
        "ds_read_b64_tr_b16 a[36:37], v132 offset:36864\n"
        "v_mfma_f32_16x16x32_bf16 v[192:195], a[28:31], a[196:199], v[192:195]\n"
        "buffer_load_dwordx4 v130, s[20:23], s64 offen offset:2048 lds\n"
        "v_mfma_f32_16x16x32_bf16 v[212:215], a[52:55], a[196:199], 0\n"
        "s_add_u32 m0, s40, 0\n"
        "ds_read_b64_tr_b16 a[38:39], v132 offset:38400\n"
        "ds_read_b64_tr_b16 a[56:57], v133 offset:24576\n"
        "v_mfma_f32_16x16x32_bf16 v[212:215], a[48:51], a[192:195], v[212:215]\n"
        "s_waitcnt lgkmcnt(7)\n"
        "s_add_u32 s64, s65, 74112\n"
        "ds_read_b64_tr_b16 a[58:59], v133 offset:26112\n"
        "v_mfma_f32_16x16x32_bf16 v[96:99], a[8:11], a[152:155], v[96:99]\n"
        "s_waitcnt lgkmcnt(6)\n"
        "ds_read_b64_tr_b16 a[60:61], v133 offset:36864\n"
        "v_mfma_f32_16x16x32_bf16 v[96:99], a[12:15], a[156:159], v[96:99]\n"
        "s_waitcnt lgkmcnt(3)\n"
        "ds_read_b64_tr_b16 a[62:63], v133 offset:38400\n"
        "v_mfma_f32_16x16x32_bf16 v[100:103], a[36:39], a[156:159], v[100:103]\n"
        "buffer_load_dwordx4 v128, s[20:23], s64 offen lds\n"
        "v_mfma_f32_16x16x32_bf16 v[100:103], a[32:35], a[152:155], v[100:103]\n"
        "s_waitcnt lgkmcnt(2)\n"
        "v_mfma_f32_16x16x32_bf16 v[104:107], a[56:59], a[152:155], v[104:107]\n"
        "s_waitcnt lgkmcnt(0)\n"
        "v_mfma_f32_16x16x32_bf16 v[104:107], a[60:63], a[156:159], v[104:107]\n"
        "buffer_load_dwordx4 v129, s[20:23], s64 offen offset:1024 lds\n"
        "v_mfma_f32_16x16x32_bf16 v[108:111], a[12:15], a[172:175], v[108:111]\n"
        "v_mfma_f32_16x16x32_bf16 v[108:111], a[8:11], a[168:171], v[108:111]\n"
        "buffer_atomic_swap v67, v249, s[4:7], 0 offen sc1\n"
        "v_mfma_f32_16x16x32_bf16 v[112:115], a[32:35], a[168:171], v[112:115]\n"
        "v_mfma_f32_16x16x32_bf16 v[112:115], a[36:39], a[172:175], v[112:115]\n"
        "buffer_load_dwordx4 v130, s[20:23], s64 offen offset:2048 lds\n"
        "v_mfma_f32_16x16x32_bf16 v[116:119], a[60:63], a[172:175], v[116:119]\n"
        "s_add_u32 m0, s40, 12288\n"
        "v_mfma_f32_16x16x32_bf16 v[116:119], a[56:59], a[168:171], v[116:119]\n"
        "s_add_u32 s64, s65, 110976\n"
        "v_mfma_f32_16x16x32_bf16 v[120:123], a[8:11], a[184:187], v[120:123]\n"
        "s_waitcnt vmcnt(10)\n"
        "v_mfma_f32_16x16x32_bf16 v[120:123], a[12:15], a[188:191], v[120:123]\n"
        "s_barrier\n"
        "buffer_load_dwordx4 v128, s[20:23], s64 offen lds\n"
        "v_mfma_f32_16x16x32_bf16 v[92:95], a[36:39], a[188:191], v[92:95]\n"
        "ds_read_b64_tr_b16 a[16:17], v131 offset:49152\n"
        "ds_read_b64_tr_b16 a[18:19], v131 offset:50688\n"
        "v_mfma_f32_16x16x32_bf16 v[92:95], a[32:35], a[184:187], v[92:95]\n"
        "ds_read_b64_tr_b16 a[20:21], v131 offset:61440\n"
        "v_mfma_f32_16x16x32_bf16 v[152:155], a[56:59], a[184:187], v[152:155]\n"
        "ds_read_b64_tr_b16 a[22:23], v131 offset:62976\n"
        "v_mfma_f32_16x16x32_bf16 v[152:155], a[60:63], a[188:191], v[152:155]\n"
        "buffer_load_dwordx4 v129, s[20:23], s64 offen offset:1024 lds\n"
        "v_mfma_f32_16x16x32_bf16 v[172:175], a[12:15], a[204:207], v[172:175]\n"
        "ds_read_b64_tr_b16 a[40:41], v132 offset:49152\n"
        "ds_read_b64_tr_b16 a[42:43], v132 offset:50688\n"
        "v_mfma_f32_16x16x32_bf16 v[172:175], a[8:11], a[200:203], v[172:175]\n"
        "ds_read_b64_tr_b16 a[44:45], v132 offset:61440\n"
        "v_mfma_f32_16x16x32_bf16 v[192:195], a[32:35], a[200:203], v[192:195]\n"
        "ds_read_b64_tr_b16 a[46:47], v132 offset:62976\n"
        "v_mfma_f32_16x16x32_bf16 v[192:195], a[36:39], a[204:207], v[192:195]\n"
        "buffer_load_dwordx4 v130, s[20:23], s64 offen offset:2048 lds\n"
        "v_mfma_f32_16x16x32_bf16 v[212:215], a[60:63], a[204:207], v[212:215]\n"
        "s_add_u32 m0, s40, 24576\n"
        "ds_read_b64_tr_b16 a[64:65], v133 offset:49152\n"
        "ds_read_b64_tr_b16 a[66:67], v133 offset:50688\n"
        "v_mfma_f32_16x16x32_bf16 v[212:215], a[56:59], a[200:203], v[212:215]\n"
        "s_waitcnt lgkmcnt(8)\n"
        "s_add_u32 s64, s65, 147840\n"
        "ds_read_b64_tr_b16 a[68:69], v133 offset:61440\n"
        "v_mfma_f32_16x16x32_bf16 v[96:99], a[16:19], a[216:219], v[96:99]\n"
        "s_waitcnt lgkmcnt(7)\n"
        "ds_read_b64_tr_b16 a[70:71], v133 offset:62976\n"
        "v_mfma_f32_16x16x32_bf16 v[96:99], a[20:23], a[220:223], v[96:99]\n"
        "s_waitcnt lgkmcnt(4)\n"
        "buffer_load_dwordx4 v128, s[20:23], s64 offen lds\n"
        "v_mfma_f32_16x16x32_bf16 v[100:103], a[44:47], a[220:223], v[100:103]\n"
        "v_mfma_f32_16x16x32_bf16 v[100:103], a[40:43], a[216:219], v[100:103]\n"
        "s_waitcnt lgkmcnt(2)\n"
        "v_mfma_f32_16x16x32_bf16 v[104:107], a[64:67], a[216:219], v[104:107]\n"
        "s_waitcnt lgkmcnt(0)\n"
        "v_mfma_f32_16x16x32_bf16 v[104:107], a[68:71], a[220:223], v[104:107]\n"
        "buffer_load_dwordx4 v129, s[20:23], s64 offen offset:1024 lds\n"
        "v_mfma_f32_16x16x32_bf16 v[108:111], a[20:23], a[228:231], v[108:111]\n"
        "v_mfma_f32_16x16x32_bf16 v[108:111], a[16:19], a[224:227], v[108:111]\n"
        "v_mul_f32 v96, s50, v96\n"
        "v_mul_f32 v97, s50, v97\n"
        "v_mfma_f32_16x16x32_bf16 v[112:115], a[40:43], a[224:227], v[112:115]\n"
        "v_mul_f32 v98, s50, v98\n"
        "v_mul_f32 v99, s50, v99\n"
        "v_mfma_f32_16x16x32_bf16 v[112:115], a[44:47], a[228:231], v[112:115]\n"
        "buffer_load_dwordx4 v130, s[20:23], s64 offen offset:2048 lds\n"
        "v_mfma_f32_16x16x32_bf16 v[116:119], a[68:71], a[228:231], v[116:119]\n"
        "s_add_u32 m0, s40, 36864\n"
        "v_cvt_pk_bf16_f32 v156, v96, v97\n"
        "v_cvt_pk_bf16_f32 v157, v98, v99\n"
        "v_mfma_f32_16x16x32_bf16 v[116:119], a[64:67], a[224:227], v[116:119]\n"
        "s_add_u32 s64, s65, 184704\n"
        "v_mul_f32 v100, s50, v100\n"
        "v_mul_f32 v101, s50, v101\n"
        "v_mfma_f32_16x16x32_bf16 v[120:123], a[16:19], a[232:235], v[120:123]\n"
        "s_waitcnt vmcnt(10)\n"
        "s_barrier\n"
        "ds_read_b64_tr_b16 a[72:73], v136 offset:0\n"
        "v_mul_f32 v102, s50, v102\n"
        "v_mfma_f32_16x16x32_bf16 v[120:123], a[20:23], a[236:239], v[120:123]\n"
        "buffer_load_dwordx4 v128, s[20:23], s64 offen lds\n"
        "v_mfma_f32_16x16x32_bf16 v[92:95], a[44:47], a[236:239], v[92:95]\n"
        "ds_read_b64_tr_b16 a[74:75], v136 offset:1536\n"
        "ds_read_b64_tr_b16 a[76:77], v136 offset:12288\n"
        "v_mfma_f32_16x16x32_bf16 v[92:95], a[40:43], a[232:235], v[92:95]\n"
        "ds_read_b64_tr_b16 a[78:79], v136 offset:13824\n"
        "v_mul_f32 v103, s50, v103\n"
        "v_mfma_f32_16x16x32_bf16 v[152:155], a[64:67], a[232:235], v[152:155]\n"
        "ds_read_b64_tr_b16 a[96:97], v137 offset:0\n"
        "v_cvt_pk_bf16_f32 v158, v100, v101\n"
        "v_mfma_f32_16x16x32_bf16 v[152:155], a[68:71], a[236:239], v[152:155]\n"
        "buffer_load_dwordx4 v129, s[20:23], s64 offen offset:1024 lds\n"
        "v_mfma_f32_16x16x32_bf16 v[172:175], a[20:23], a[244:247], v[172:175]\n"
        "ds_read_b64_tr_b16 a[98:99], v137 offset:1536\n"
        "ds_read_b64_tr_b16 a[100:101], v137 offset:12288\n"
        "v_mfma_f32_16x16x32_bf16 v[172:175], a[16:19], a[240:243], v[172:175]\n"
        "ds_read_b64_tr_b16 a[102:103], v137 offset:13824\n"
        "v_cvt_pk_bf16_f32 v159, v102, v103\n"
        "v_mfma_f32_16x16x32_bf16 v[192:195], a[40:43], a[240:243], v[192:195]\n"
        "ds_read_b64_tr_b16 a[120:121], v138 offset:0\n"
        "v_mul_f32 v104, s50, v104\n"
        "v_mfma_f32_16x16x32_bf16 v[192:195], a[44:47], a[244:247], v[192:195]\n"
        "buffer_load_dwordx4 v130, s[20:23], s64 offen offset:2048 lds\n"
        "v_mfma_f32_16x16x32_bf16 v[212:215], a[68:71], a[244:247], v[212:215]\n"
        "s_add_u32 m0, s40, 49152\n"
        "ds_read_b64_tr_b16 a[122:123], v138 offset:1536\n"
        "ds_read_b64_tr_b16 a[124:125], v138 offset:12288\n"
        "v_mfma_f32_16x16x32_bf16 v[212:215], a[64:67], a[240:243], v[212:215]\n"
        "s_waitcnt lgkmcnt(9)\n"
        "s_add_u32 s64, s65, 768\n"
        "ds_read_b64_tr_b16 a[126:127], v138 offset:13824\n"
        "v_mul_f32 v105, s50, v105\n"
        "v_mfma_f32_16x16x32_bf16 v[64:67], a[72:75], a[144:147], 0\n"
        "s_waitcnt lgkmcnt(8)\n"
        "v_mul_f32 v106, s50, v106\n"
        "v_mul_f32 v107, s50, v107\n"
        "v_mfma_f32_16x16x32_bf16 v[64:67], a[76:79], a[148:151], v[64:67]\n"
        "s_waitcnt lgkmcnt(4)\n"
        "buffer_load_dwordx4 v128, s[20:23], s64 offen lds\n"
        "v_mfma_f32_16x16x32_bf16 v[68:71], a[100:103], a[148:151], 0\n"
        "v_cvt_pk_bf16_f32 v160, v104, v105\n"
        "v_cvt_pk_bf16_f32 v161, v106, v107\n"
        "v_mfma_f32_16x16x32_bf16 v[68:71], a[96:99], a[144:147], v[68:71]\n"
        "s_waitcnt lgkmcnt(2)\n"
        "v_mul_f32 v108, s50, v108\n"
        "v_mul_f32 v109, s50, v109\n"
        "v_mfma_f32_16x16x32_bf16 v[72:75], a[120:123], a[144:147], 0\n"
        "s_waitcnt lgkmcnt(0)\n"
        "v_mov_b64 v[166:167], v[160:161]\n"
        "v_mul_f32 v110, s50, v110\n"
        "v_mfma_f32_16x16x32_bf16 v[72:75], a[124:127], a[148:151], v[72:75]\n"
        "buffer_load_dwordx4 v129, s[20:23], s64 offen offset:1024 lds\n"
        "v_mfma_f32_16x16x32_bf16 v[76:79], a[76:79], a[164:167], 0\n"
        "v_mul_f32 v111, s50, v111\n"
        "v_cvt_pk_bf16_f32 v176, v108, v109\n"
        "v_mfma_f32_16x16x32_bf16 v[76:79], a[72:75], a[160:163], v[76:79]\n"
        "v_cvt_pk_bf16_f32 v177, v110, v111\n"
        "v_mul_f32 v112, s50, v112\n"
        "v_mfma_f32_16x16x32_bf16 v[80:83], a[96:99], a[160:163], 0\n"
        "v_mul_f32 v113, s50, v113\n"
        "v_mul_f32 v114, s50, v114\n"
        "v_mfma_f32_16x16x32_bf16 v[80:83], a[100:103], a[164:167], v[80:83]\n"
        "s_waitcnt vmcnt(8)\n"
        "buffer_load_dwordx4 v130, s[20:23], s64 offen offset:2048 lds\n"
        "v_mfma_f32_16x16x32_bf16 v[84:87], a[124:127], a[164:167], 0\n"
        "s_barrier\n"
        "ds_read_b64_tr_b16 a[80:81], v131 offset:0\n"
        "v_mul_f32 v115, s50, v115\n"
        "v_mfma_f32_16x16x32_bf16 v[84:87], a[120:123], a[160:163], v[84:87]\n"
        "s_add_u32 m0, s40, 61440\n"
        "ds_read_b64_tr_b16 a[82:83], v131 offset:1536\n"
        "v_cvt_pk_bf16_f32 v178, v112, v113\n"
        "v_mfma_f32_16x16x32_bf16 v[88:91], a[72:75], a[176:179], 0\n"
        "s_add_u32 s64, s65, 37632\n"
        "ds_read_b64_tr_b16 a[84:85], v131 offset:12288\n"
        "v_cvt_pk_bf16_f32 v179, v114, v115\n"
        "v_mfma_f32_16x16x32_bf16 v[88:91], a[76:79], a[180:183], v[88:91]\n"
        "buffer_load_dwordx4 v128, s[20:23], s64 offen lds\n"
        "v_mfma_f32_16x16x32_bf16 v[108:111], a[100:103], a[180:183], 0\n"
        "ds_read_b64_tr_b16 a[86:87], v131 offset:13824\n"
        "ds_read_b64_tr_b16 a[104:105], v132 offset:0\n"
        "v_mfma_f32_16x16x32_bf16 v[108:111], a[96:99], a[176:179], v[108:111]\n"
        "ds_read_b64_tr_b16 a[106:107], v132 offset:1536\n"
        "v_mul_f32 v116, s50, v116\n"
        "v_mfma_f32_16x16x32_bf16 v[112:115], a[120:123], a[176:179], 0\n"
        "ds_read_b64_tr_b16 a[108:109], v132 offset:12288\n"
        "v_mul_f32 v117, s50, v117\n"
        "v_mfma_f32_16x16x32_bf16 v[112:115], a[124:127], a[180:183], v[112:115]\n"
        "buffer_load_dwordx4 v129, s[20:23], s64 offen offset:1024 lds\n"
        "v_mfma_f32_16x16x32_bf16 v[96:99], a[76:79], a[196:199], 0\n"
        "ds_read_b64_tr_b16 a[110:111], v132 offset:13824\n"
        "ds_read_b64_tr_b16 a[128:129], v133 offset:0\n"
        "v_mfma_f32_16x16x32_bf16 v[96:99], a[72:75], a[192:195], v[96:99]\n"
        "ds_read_b64_tr_b16 a[130:131], v133 offset:1536\n"
        "v_mul_f32 v118, s50, v118\n"
        "v_mfma_f32_16x16x32_bf16 v[100:103], a[96:99], a[192:195], 0\n"
        "ds_read_b64_tr_b16 a[132:133], v133 offset:12288\n"
        "v_mul_f32 v119, s50, v119\n"
        "v_mfma_f32_16x16x32_bf16 v[100:103], a[100:103], a[196:199], v[100:103]\n"
        "buffer_load_dwordx4 v130, s[20:23], s64 offen offset:2048 lds\n"
        "v_mfma_f32_16x16x32_bf16 v[104:107], a[124:127], a[196:199], 0\n"
        "ds_read_b64_tr_b16 a[134:135], v133 offset:13824\n"
        "v_cvt_pk_bf16_f32 v180, v116, v117\n"
        "v_mfma_f32_16x16x32_bf16 v[104:107], a[120:123], a[192:195], v[104:107]\n"
        "s_waitcnt lgkmcnt(10)\n"
        "v_cvt_pk_bf16_f32 v181, v118, v119\n"
        "v_mul_f32 v120, s50, v120\n"
        "v_mfma_f32_16x16x32_bf16 v[64:67], a[80:83], a[152:155], v[64:67]\n"
        "s_waitcnt lgkmcnt(8)\n"
        "v_mul_f32 v121, s50, v121\n"
        "v_mov_b64 v[186:187], v[180:181]\n"
        "v_mfma_f32_16x16x32_bf16 v[64:67], a[84:87], a[156:159], v[64:67]\n"
        "s_waitcnt lgkmcnt(4)\n"
        "v_mul_f32 v122, s50, v122\n"
        "v_mul_f32 v123, s50, v123\n"
        "v_mfma_f32_16x16x32_bf16 v[68:71], a[108:111], a[156:159], v[68:71]\n"
        "v_cvt_pk_bf16_f32 v196, v120, v121\n"
        "v_cvt_pk_bf16_f32 v197, v122, v123\n"
        "v_mfma_f32_16x16x32_bf16 v[68:71], a[104:107], a[152:155], v[68:71]\n"
        "s_waitcnt lgkmcnt(2)\n"
        "v_mul_f32 v92, s50, v92\n"
        "v_mul_f32 v93, s50, v93\n"
        "v_mfma_f32_16x16x32_bf16 v[72:75], a[128:131], a[152:155], v[72:75]\n"
        "s_waitcnt lgkmcnt(0)\n"
        "v_mul_f32 v94, s50, v94\n"
        "v_mul_f32 v95, s50, v95\n"
        "v_mfma_f32_16x16x32_bf16 v[72:75], a[132:135], a[156:159], v[72:75]\n"
        "v_cvt_pk_bf16_f32 v198, v92, v93\n"
        "v_cvt_pk_bf16_f32 v199, v94, v95\n"
        "v_mfma_f32_16x16x32_bf16 v[76:79], a[84:87], a[172:175], v[76:79]\n"
        "v_mul_f32 v152, s50, v152\n"
        "v_mul_f32 v153, s50, v153\n"
        "v_mfma_f32_16x16x32_bf16 v[76:79], a[80:83], a[168:171], v[76:79]\n"
        "v_mul_f32 v154, s50, v154\n"
        "v_mul_f32 v155, s50, v155\n"
        "v_mfma_f32_16x16x32_bf16 v[80:83], a[104:107], a[168:171], v[80:83]\n"
        "s_waitcnt vmcnt(6)\n"
        "v_cvt_pk_bf16_f32 v200, v152, v153\n"
        "v_cvt_pk_bf16_f32 v201, v154, v155\n"
        "v_mfma_f32_16x16x32_bf16 v[80:83], a[108:111], a[172:175], v[80:83]\n"
        "s_barrier\n"
        "s_add_u32 m0, s40, 0\n"
        "ds_read_b64_tr_b16 a[88:89], v131 offset:24576\n"
        "v_mfma_f32_16x16x32_bf16 v[84:87], a[132:135], a[172:175], v[84:87]\n"
        "s_add_u32 s64, s65, 74496\n"
        "ds_read_b64_tr_b16 a[90:91], v131 offset:26112\n"
        "v_mul_f32 v172, s50, v172\n"
        "v_mfma_f32_16x16x32_bf16 v[84:87], a[128:131], a[168:171], v[84:87]\n"
        "ds_read_b64_tr_b16 a[92:93], v131 offset:36864\n"
        "v_mul_f32 v173, s50, v173\n"
        "v_mfma_f32_16x16x32_bf16 v[88:91], a[80:83], a[184:187], v[88:91]\n"
        "buffer_load_dwordx4 v128, s[20:23], s64 offen lds\n"
        "v_mfma_f32_16x16x32_bf16 v[88:91], a[84:87], a[188:191], v[88:91]\n"
        "ds_read_b64_tr_b16 a[94:95], v131 offset:38400\n"
        "ds_read_b64_tr_b16 a[112:113], v132 offset:24576\n"
        "v_mfma_f32_16x16x32_bf16 v[108:111], a[108:111], a[188:191], v[108:111]\n"
        "ds_read_b64_tr_b16 a[114:115], v132 offset:26112\n"
        "v_mov_b64 v[206:207], v[200:201]\n"
        "v_mfma_f32_16x16x32_bf16 v[108:111], a[104:107], a[184:187], v[108:111]\n"
        "ds_read_b64_tr_b16 a[116:117], v132 offset:36864\n"
        "v_mul_f32 v174, s50, v174\n"
        "v_mfma_f32_16x16x32_bf16 v[112:115], a[128:131], a[184:187], v[112:115]\n"
        "buffer_load_dwordx4 v129, s[20:23], s64 offen offset:1024 lds\n"
        "v_mfma_f32_16x16x32_bf16 v[112:115], a[132:135], a[188:191], v[112:115]\n"
        "ds_read_b64_tr_b16 a[118:119], v132 offset:38400\n"
        "ds_read_b64_tr_b16 a[136:137], v133 offset:24576\n"
        "v_mfma_f32_16x16x32_bf16 v[96:99], a[84:87], a[204:207], v[96:99]\n"
        "ds_read_b64_tr_b16 a[138:139], v133 offset:26112\n"
        "v_mul_f32 v175, s50, v175\n"
        "v_mfma_f32_16x16x32_bf16 v[96:99], a[80:83], a[200:203], v[96:99]\n"
        "ds_read_b64_tr_b16 a[140:141], v133 offset:36864\n"
        "v_cvt_pk_bf16_f32 v216, v172, v173\n"
        "v_mfma_f32_16x16x32_bf16 v[100:103], a[104:107], a[200:203], v[100:103]\n"
        "buffer_load_dwordx4 v130, s[20:23], s64 offen offset:2048 lds\n"
        "v_mfma_f32_16x16x32_bf16 v[100:103], a[108:111], a[204:207], v[100:103]\n"
        "s_add_u32 m0, s40, 12288\n"
        "ds_read_b64_tr_b16 a[142:143], v133 offset:38400\n"
        "v_cvt_pk_bf16_f32 v217, v174, v175\n"
        "v_mfma_f32_16x16x32_bf16 v[104:107], a[132:135], a[204:207], v[104:107]\n"
        "s_add_u32 s64, s65, 111360\n"
        "v_mul_f32 v192, s50, v192\n"
        "v_mul_f32 v193, s50, v193\n"
        "v_mfma_f32_16x16x32_bf16 v[104:107], a[128:131], a[200:203], v[104:107]\n"
        "s_waitcnt lgkmcnt(10)\n"
        "buffer_load_dwordx4 v128, s[20:23], s64 offen lds\n"
        "v_mfma_f32_16x16x32_bf16 v[64:67], a[88:91], a[216:219], v[64:67]\n"
        "s_waitcnt lgkmcnt(8)\n"
        "v_mul_f32 v194, s50, v194\n"
        "v_mul_f32 v195, s50, v195\n"
        "v_mfma_f32_16x16x32_bf16 v[64:67], a[92:95], a[220:223], v[64:67]\n"
        "s_waitcnt lgkmcnt(4)\n"
        "v_cvt_pk_bf16_f32 v218, v192, v193\n"
        "v_cvt_pk_bf16_f32 v219, v194, v195\n"
        "v_mfma_f32_16x16x32_bf16 v[68:71], a[116:119], a[220:223], v[68:71]\n"
        "v_mul_f32 v212, s50, v212\n"
        "v_mul_f32 v213, s50, v213\n"
        "v_mfma_f32_16x16x32_bf16 v[68:71], a[112:115], a[216:219], v[68:71]\n"
        "s_waitcnt lgkmcnt(2)\n"
        "buffer_load_dwordx4 v129, s[20:23], s64 offen offset:1024 lds\n"
        "v_mfma_f32_16x16x32_bf16 v[72:75], a[136:139], a[216:219], v[72:75]\n"
        "s_waitcnt lgkmcnt(0)\n"
        "v_mul_f32 v214, s50, v214\n"
        "v_mul_f32 v215, s50, v215\n"
        "v_mfma_f32_16x16x32_bf16 v[72:75], a[140:143], a[220:223], v[72:75]\n"
        "v_cvt_pk_bf16_f32 v220, v212, v213\n"
        "v_cvt_pk_bf16_f32 v221, v214, v215\n"
        "v_mfma_f32_16x16x32_bf16 v[76:79], a[92:95], a[228:231], v[76:79]\n"
        "v_mov_b64 v[226:227], v[220:221]\n"
        "v_cvt_pk_bf16_f32 v152, v64, v65\n"
        "v_mfma_f32_16x16x32_bf16 v[76:79], a[88:91], a[224:227], v[76:79]\n"
        "buffer_load_dwordx4 v130, s[20:23], s64 offen offset:2048 lds\n"
        "v_mfma_f32_16x16x32_bf16 v[80:83], a[112:115], a[224:227], v[80:83]\n"
        "v_cvt_pk_bf16_f32 v153, v66, v67\n"
        "v_cvt_pk_bf16_f32 v154, v68, v69\n"
        "v_mfma_f32_16x16x32_bf16 v[80:83], a[116:119], a[228:231], v[80:83]\n"
        "v_cvt_pk_bf16_f32 v155, v70, v71\n"
        "v_cvt_pk_bf16_f32 v168, v72, v73\n"
        "v_mfma_f32_16x16x32_bf16 v[84:87], a[140:143], a[228:231], v[84:87]\n"
        "v_cvt_pk_bf16_f32 v169, v74, v75\n"
        "v_cvt_pk_bf16_f32 v172, v76, v77\n"
        "v_mfma_f32_16x16x32_bf16 v[84:87], a[136:139], a[224:227], v[84:87]\n"
        "s_waitcnt vmcnt(6)\n"
        "ds_read_b128 v[162:165], v251 offset:0\n"
        "v_cvt_pk_bf16_f32 v173, v78, v79\n"
        "v_mfma_f32_16x16x32_bf16 v[88:91], a[88:91], a[232:235], v[88:91]\n"
        "s_barrier\n"
        "s_add_u32 m0, s40, 24576\n"
        "ds_read_b64_tr_b16 a[144:145], v131 offset:49152\n"
        "v_mfma_f32_16x16x32_bf16 v[88:91], a[92:95], a[236:239], v[88:91]\n"
        "s_add_u32 s64, s65, 148224\n"
        "ds_read_b64_tr_b16 a[146:147], v131 offset:50688\n"
        "v_cvt_pk_bf16_f32 v174, v80, v81\n"
        "v_mfma_f32_16x16x32_bf16 v[108:111], a[116:119], a[236:239], v[108:111]\n"
        "ds_read_b64_tr_b16 a[148:149], v131 offset:61440\n"
        "v_cvt_pk_bf16_f32 v175, v82, v83\n"
        "v_mfma_f32_16x16x32_bf16 v[108:111], a[112:115], a[232:235], v[108:111]\n"
        "buffer_load_dwordx4 v128, s[20:23], s64 offen lds\n"
        "v_mfma_f32_16x16x32_bf16 v[112:115], a[136:139], a[232:235], v[112:115]\n"
        "ds_read_b64_tr_b16 a[150:151], v131 offset:62976\n"
        "ds_read_b64_tr_b16 a[168:169], v132 offset:49152\n"
        "v_mfma_f32_16x16x32_bf16 v[112:115], a[140:143], a[236:239], v[112:115]\n"
        "ds_read_b64_tr_b16 a[170:171], v132 offset:50688\n"
        "v_cvt_pk_bf16_f32 v190, v84, v85\n"
        "v_mfma_f32_16x16x32_bf16 v[96:99], a[92:95], a[244:247], v[96:99]\n"
        "ds_read_b64_tr_b16 a[172:173], v132 offset:61440\n"
        "ds_read_b128 v[182:185], v251 offset:512\n"
        "v_mfma_f32_16x16x32_bf16 v[96:99], a[88:91], a[240:243], v[96:99]\n"
        "buffer_load_dwordx4 v129, s[20:23], s64 offen offset:1024 lds\n"
        "v_mfma_f32_16x16x32_bf16 v[100:103], a[112:115], a[240:243], v[100:103]\n"
        "ds_read_b64_tr_b16 a[174:175], v132 offset:62976\n"
        "ds_read_b64_tr_b16 a[192:193], v133 offset:49152\n"
        "v_mfma_f32_16x16x32_bf16 v[100:103], a[116:119], a[244:247], v[100:103]\n"
        "ds_read_b64_tr_b16 a[194:195], v133 offset:50688\n"
        "v_cvt_pk_bf16_f32 v191, v86, v87\n"
        "v_mfma_f32_16x16x32_bf16 v[104:107], a[140:143], a[244:247], v[104:107]\n"
        "ds_read_b64_tr_b16 a[196:197], v133 offset:61440\n"
        "ds_read_b128 v[202:205], v251 offset:4096\n"
        "v_mfma_f32_16x16x32_bf16 v[104:107], a[136:139], a[240:243], v[104:107]\n"
        "buffer_load_dwordx4 v130, s[20:23], s64 offen offset:2048 lds\n"
        "v_mfma_f32_16x16x32_bf16 v[64:67], v[152:155], v[156:159], v[0:3]\n"
        "s_waitcnt lgkmcnt(13)\n"
        "s_add_u32 m0, s40, 36864\n"
        "ds_read_b64_tr_b16 a[198:199], v133 offset:62976\n"
        "v_cvt_pk_bf16_f32 v192, v88, v89\n"
        "v_mfma_f32_16x16x32_bf16 v[64:67], v[168:171], v[160:163], v[64:67]\n"
        "s_add_u32 s64, s65, 185088\n"
        "v_cvt_pk_bf16_f32 v193, v90, v91\n"
        "v_cvt_pk_bf16_f32 v194, v108, v109\n"
        "v_mfma_f32_16x16x32_bf16 v[68:71], v[172:175], v[156:159], v[4:7]\n"
        "buffer_load_dwordx4 v128, s[20:23], s64 offen lds\n"
        "v_mfma_f32_16x16x32_bf16 v[68:71], v[188:191], v[164:167], v[68:71]\n"
        "ds_read_b128 v[222:225], v251 offset:4608\n"
        "v_cvt_pk_bf16_f32 v195, v110, v111\n"
        "v_mfma_f32_16x16x32_bf16 v[72:75], v[152:155], v[176:179], v[16:19]\n"
        "s_waitcnt lgkmcnt(7)\n"
        "v_cvt_pk_bf16_f32 v208, v112, v113\n"
        "v_cvt_pk_bf16_f32 v209, v114, v115\n"
        "v_mfma_f32_16x16x32_bf16 v[72:75], v[168:171], v[180:183], v[72:75]\n"
        "v_cvt_pk_bf16_f32 v212, v96, v97\n"
        "v_cvt_pk_bf16_f32 v213, v98, v99\n"
        "v_mfma_f32_16x16x32_bf16 v[76:79], v[172:175], v[176:179], v[20:23]\n"
        "buffer_load_dwordx4 v129, s[20:23], s64 offen offset:1024 lds\n"
        "v_mfma_f32_16x16x32_bf16 v[76:79], v[188:191], v[184:187], v[76:79]\n"
        "ds_read_b128 v[162:165], v252 offset:0\n"
        "v_cvt_pk_bf16_f32 v214, v100, v101\n"
        "v_mfma_f32_16x16x32_bf16 v[80:83], v[152:155], v[196:199], v[32:35]\n"
        "s_waitcnt lgkmcnt(3)\n"
        "ds_read_b128 v[182:185], v252 offset:512\n"
        "v_cvt_pk_bf16_f32 v215, v102, v103\n"
        "v_mfma_f32_16x16x32_bf16 v[80:83], v[168:171], v[200:203], v[80:83]\n"
        "41:\n"
        "s_cbranch_execz 44f\n"
        "v_cvt_pk_bf16_f32 v230, v104, v105\n"
        "v_cvt_pk_bf16_f32 v231, v106, v107\n"
        "v_mfma_f32_16x16x32_bf16 v[84:87], v[172:175], v[196:199], v[36:39]\n"
        "buffer_load_dwordx4 v130, s[20:23], s64 offen offset:2048 lds\n"
        "v_mfma_f32_16x16x32_bf16 v[84:87], v[188:191], v[204:207], v[84:87]\n"
        "s_add_u32 m0, s40, 98304\n"
        "ds_read_b128 v[202:205], v252 offset:4096\n"
        "v_mfma_f32_16x16x32_bf16 v[88:91], v[152:155], v[216:219], v[48:51]\n"
        "s_waitcnt vmcnt(6) lgkmcnt(3)\n"
        "s_barrier\n"
        "ds_read_b64_tr_b16 a[152:153], v131 offset:0\n"
        "ds_read_b128 a[248:251], v240 offset:0\n"
        "v_mfma_f32_16x16x32_bf16 v[88:91], v[168:171], v[220:223], v[88:91]\n"
        "ds_read_b64_tr_b16 a[154:155], v131 offset:1536\n"
        "ds_read_b128 a[216:219], v241 offset:0\n"
        "v_mfma_f32_16x16x32_bf16 v[92:95], v[172:175], v[216:219], v[52:55]\n"
        "ds_read_b64_tr_b16 a[156:157], v131 offset:12288\n"
        "ds_read_b128 a[220:223], v240 offset:128\n"
        "v_mfma_f32_16x16x32_bf16 v[92:95], v[188:191], v[224:227], v[92:95]\n"
        "buffer_load_dword v148, s[52:55], 0 offen lds\n"
        "v_mfma_f32_16x16x32_bf16 v[108:111], v[212:215], v[156:159], v[12:15]\n"
        "s_waitcnt lgkmcnt(8)\n"
        "s_add_u32 m0, s40, 98304\n"
        "s_sub_u32 s64, s47, 6291456\n"
        "ds_read_b64_tr_b16 a[158:159], v131 offset:13824\n"
        "v_mfma_f32_16x16x32_bf16 v[108:111], v[228:231], v[164:167], v[108:111]\n"
        "ds_read_b128 a[224:227], v241 offset:128\n"
        "ds_read_b64_tr_b16 a[176:177], v132 offset:0\n"
        "v_mfma_f32_16x16x32_bf16 v[112:115], v[212:215], v[176:179], v[28:31]\n"
        "s_waitcnt lgkmcnt(10)\n"
        "buffer_load_dwordx4 v242, s[8:11], s64 offen lds\n"
        "v_mfma_f32_16x16x32_bf16 v[112:115], v[228:231], v[184:187], v[112:115]\n"
        "s_add_u32 m0, s40, 99328\n"
        "s_sub_u32 s64, s47, 6291456\n"
        "ds_read_b64_tr_b16 a[178:179], v132 offset:1536\n"
        "v_mfma_f32_16x16x32_bf16 v[116:119], v[212:215], v[196:199], v[44:47]\n"
        "s_waitcnt lgkmcnt(10)\n"
        "ds_read_b64_tr_b16 a[180:181], v132 offset:12288\n"
        "ds_read_b64_tr_b16 a[182:183], v132 offset:13824\n"
        "v_mfma_f32_16x16x32_bf16 v[116:119], v[228:231], v[204:207], v[116:119]\n"
        "s_waitcnt lgkmcnt(10)\n"
        "ds_read_b64_tr_b16 a[200:201], v133 offset:0\n"
        "ds_read_b64_tr_b16 a[202:203], v133 offset:1536\n"
        "v_mfma_f32_16x16x32_bf16 v[120:123], a[248:251], a[144:147], 0\n"
        "s_waitcnt lgkmcnt(10)\n"
        "buffer_load_dwordx4 v243, s[8:11], s64 offen lds\n"
        "v_mfma_f32_16x16x32_bf16 v[120:123], a[216:219], a[148:151], v[120:123]\n"
        "s_waitcnt lgkmcnt(8)\n"
        "s_add_u32 m0, s40, 100352\n"
        "s_sub_u32 s64, s47, 6291456\n"
        "ds_read_b64_tr_b16 a[204:205], v133 offset:12288\n"
        "v_mfma_f32_16x16x32_bf16 v[120:123], a[220:223], a[152:155], v[120:123]\n"
        "s_waitcnt lgkmcnt(7)\n"
        "ds_read_b64_tr_b16 a[206:207], v133 offset:13824\n"
        "v_mfma_f32_16x16x32_bf16 v[120:123], a[224:227], a[156:159], v[120:123]\n"
        "s_waitcnt lgkmcnt(4)\n"
        "buffer_load_dwordx4 v244, s[8:11], s64 offen lds\n"
        "v_mfma_f32_16x16x32_bf16 v[96:99], a[224:227], a[180:183], 0\n"
        "s_add_u32 m0, s40, 110592\n"
        "ds_read_b128 a[228:231], v240 offset:1536\n"
        "v_mfma_f32_16x16x32_bf16 v[96:99], a[220:223], a[176:179], v[96:99]\n"
        "s_sub_u32 s64, s47, 5505024\n"
        "ds_read_b128 a[232:235], v241 offset:1536\n"
        "v_mfma_f32_16x16x32_bf16 v[96:99], a[216:219], a[172:175], v[96:99]\n"
        "buffer_load_dwordx4 v242, s[8:11], s64 offen lds\n"
        "v_mfma_f32_16x16x32_bf16 v[96:99], a[248:251], a[168:171], v[96:99]\n"
        "s_add_u32 m0, s40, 111616\n"
        "ds_read_b128 a[236:239], v240 offset:1664\n"
        "ds_read_b128 a[240:243], v241 offset:1664\n"
        "v_mfma_f32_16x16x32_bf16 v[100:103], a[248:251], a[192:195], 0\n"
        "s_sub_u32 s64, s47, 5505024\n"
        "v_exp_f32 v64, v64\n"
        "v_mfma_f32_16x16x32_bf16 v[100:103], a[216:219], a[196:199], v[100:103]\n"
        "s_waitcnt lgkmcnt(6)\n"
        "v_exp_f32 v65, v65\n"
        "v_mfma_f32_16x16x32_bf16 v[100:103], a[220:223], a[200:203], v[100:103]\n"
        "s_waitcnt lgkmcnt(4)\n"
        "buffer_load_dwordx4 v243, s[8:11], s64 offen lds\n"
        "v_mfma_f32_16x16x32_bf16 v[100:103], a[224:227], a[204:207], v[100:103]\n"
        "s_add_u32 m0, s40, 112640\n"
        "v_exp_f32 v66, v66\n"
        "v_exp_f32 v68, v68\n"
        "v_cvt_pk_bf16_f32 v64, v64, v65\n"
        "v_exp_f32 v67, v67\n"
        "v_exp_f32 v69, v69\n"
        "v_cvt_pk_bf16_f32 v65, v66, v67\n"
        "v_exp_f32 v70, v70\n"
        "v_cvt_pk_bf16_f32 v66, v68, v69\n"
        "v_exp_f32 v71, v71\n"
        "v_exp_f32 v72, v72\n"
        "v_cvt_pk_bf16_f32 v67, v70, v71\n"
        "v_mfma_f32_16x16x32_bf16 v[68:71], v[192:195], v[156:159], v[8:11]\n"
        "s_sub_u32 s64, s47, 5505024\n"
        "v_exp_f32 v73, v73\n"
        "v_mfma_f32_16x16x32_bf16 v[68:71], v[208:211], v[160:163], v[68:71]\n"
        "s_waitcnt lgkmcnt(0)\n"
        "v_exp_f32 v74, v74\n"
        "v_mfma_f32_16x16x32_bf16 v[104:107], a[240:243], a[156:159], 0\n"
        "buffer_load_dwordx4 v244, s[8:11], s64 offen lds\n"
        "v_mfma_f32_16x16x32_bf16 v[104:107], a[236:239], a[152:155], v[104:107]\n"
        "s_xor_b32 m0, s48, 8192\n"
        "ds_read_b128 a[244:247], v240 offset:12288\n"
        "v_cvt_pk_bf16_f32 v72, v72, v73\n"
        "v_mfma_f32_16x16x32_bf16 v[104:107], a[232:235], a[148:151], v[104:107]\n"
        "s_sub_u32 s64, s36, 2097152\n"
        "v_exp_f32 v75, v75\n"
        "v_mfma_f32_16x16x32_bf16 v[104:107], a[228:231], a[144:147], v[104:107]\n"
        "s_waitcnt vmcnt(7)\n"
        "ds_read_b128 a[248:251], v241 offset:12288\n"
        "v_cvt_pk_bf16_f32 v73, v74, v75\n"
        "v_mfma_f32_16x16x32_bf16 v[156:159], a[228:231], a[168:171], 0\n"
        "s_barrier\n"
        "ds_read_b64_tr_b16 a[160:161], v131 offset:24576\n"
        "v_mfma_f32_16x16x32_bf16 v[156:159], a[232:235], a[172:175], v[156:159]\n"
        "buffer_load_dwordx4 v249, s[12:15], s64 offen lds\n"
        "v_mfma_f32_16x16x32_bf16 v[156:159], a[236:239], a[176:179], v[156:159]\n"
        "ds_read_b64_tr_b16 a[162:163], v131 offset:26112\n"
        "ds_read_b64_tr_b16 a[164:165], v131 offset:36864\n"
        "v_mfma_f32_16x16x32_bf16 v[156:159], a[240:243], a[180:183], v[156:159]\n"
        "ds_read_b128 a[216:219], v240 offset:12416\n"
        "ds_read_b64_tr_b16 a[166:167], v131 offset:38400\n"
        "v_mfma_f32_16x16x32_bf16 v[160:163], a[240:243], a[204:207], 0\n"
        "ds_read_b128 a[220:223], v241 offset:12416\n"
        "ds_read_b64_tr_b16 a[184:185], v132 offset:24576\n"
        "v_mfma_f32_16x16x32_bf16 v[160:163], a[236:239], a[200:203], v[160:163]\n"
        "buffer_load_dwordx4 v149, s[12:15], s64 offen offset:1024 lds\n"
        "v_mfma_f32_16x16x32_bf16 v[160:163], a[232:235], a[196:199], v[160:163]\n"
        "ds_read_b64_tr_b16 a[186:187], v132 offset:26112\n"
        "ds_read_b64_tr_b16 a[188:189], v132 offset:36864\n"
        "v_mfma_f32_16x16x32_bf16 v[160:163], a[228:231], a[192:195], v[160:163]\n"
        "ds_read_b64_tr_b16 a[190:191], v132 offset:38400\n"
        "v_exp_f32 v76, v76\n"
        "v_exp_f32 v77, v77\n"
        "v_exp_f32 v78, v78\n"
        "v_cvt_pk_bf16_f32 v74, v76, v77\n"
        "v_exp_f32 v79, v79\n"
        "v_exp_f32 v80, v80\n"
        "v_cvt_pk_bf16_f32 v75, v78, v79\n"
        "v_mfma_f32_16x16x32_bf16 v[76:79], v[192:195], v[176:179], v[24:27]\n"
        "ds_read_b64_tr_b16 a[208:209], v133 offset:24576\n"
        "v_mfma_f32_16x16x32_bf16 v[76:79], v[208:211], v[180:183], v[76:79]\n"
        "s_waitcnt lgkmcnt(12)\n"
        "ds_read_b64_tr_b16 a[210:211], v133 offset:26112\n"
        "v_mfma_f32_16x16x32_bf16 v[164:167], a[244:247], a[144:147], 0\n"
        "s_waitcnt lgkmcnt(12)\n"
        "ds_read_b64_tr_b16 a[212:213], v133 offset:36864\n"
        "v_mfma_f32_16x16x32_bf16 v[164:167], a[248:251], a[148:151], v[164:167]\n"
        "s_waitcnt lgkmcnt(9)\n"
        "ds_read_b64_tr_b16 a[214:215], v133 offset:38400\n"
        "v_mfma_f32_16x16x32_bf16 v[164:167], a[216:219], a[152:155], v[164:167]\n"
        "s_waitcnt lgkmcnt(8)\n"
        "v_exp_f32 v81, v81\n"
        "v_mfma_f32_16x16x32_bf16 v[164:167], a[220:223], a[156:159], v[164:167]\n"
        "v_exp_f32 v82, v82\n"
        "v_mfma_f32_16x16x32_bf16 v[176:179], a[220:223], a[180:183], 0\n"
        "ds_read_b128 a[224:227], v240 offset:13824\n"
        "v_cvt_pk_bf16_f32 v80, v80, v81\n"
        "v_mfma_f32_16x16x32_bf16 v[176:179], a[216:219], a[176:179], v[176:179]\n"
        "ds_read_b128 a[228:231], v241 offset:13824\n"
        "v_mfma_f32_16x16x32_bf16 v[176:179], a[248:251], a[172:175], v[176:179]\n"
        "ds_read_b128 a[232:235], v240 offset:13952\n"
        "v_mfma_f32_16x16x32_bf16 v[176:179], a[244:247], a[168:171], v[176:179]\n"
        "ds_read_b128 a[236:239], v241 offset:13952\n"
        "v_mfma_f32_16x16x32_bf16 v[180:183], a[244:247], a[192:195], 0\n"
        "v_exp_f32 v83, v83\n"
        "v_mfma_f32_16x16x32_bf16 v[180:183], a[248:251], a[196:199], v[180:183]\n"
        "ds_read_b128 v[222:225], v252 offset:4608\n"
        "v_cvt_pk_bf16_f32 v81, v82, v83\n"
        "v_mfma_f32_16x16x32_bf16 v[180:183], a[216:219], a[200:203], v[180:183]\n"
        "v_exp_f32 v84, v84\n"
        "v_mfma_f32_16x16x32_bf16 v[180:183], a[220:223], a[204:207], v[180:183]\n"
        "v_exp_f32 v85, v85\n"
        "v_exp_f32 v86, v86\n"
        "v_cvt_pk_bf16_f32 v82, v84, v85\n"
        "v_exp_f32 v87, v87\n"
        "v_exp_f32 v88, v88\n"
        "v_cvt_pk_bf16_f32 v83, v86, v87\n"
        "v_mfma_f32_16x16x32_bf16 v[84:87], v[192:195], v[196:199], v[40:43]\n"
        "v_exp_f32 v89, v89\n"
        "v_mfma_f32_16x16x32_bf16 v[84:87], v[208:211], v[200:203], v[84:87]\n"
        "s_waitcnt lgkmcnt(1)\n"
        "v_exp_f32 v90, v90\n"
        "v_mfma_f32_16x16x32_bf16 v[184:187], a[236:239], a[156:159], 0\n"
        "ds_read_b128 a[240:243], v240 offset:256\n"
        "v_cvt_pk_bf16_f32 v88, v88, v89\n"
        "v_mfma_f32_16x16x32_bf16 v[184:187], a[232:235], a[152:155], v[184:187]\n"
        "v_exp_f32 v91, v91\n"
        "v_mfma_f32_16x16x32_bf16 v[184:187], a[228:231], a[148:151], v[184:187]\n"
        "ds_read_b128 a[244:247], v241 offset:256\n"
        "v_cvt_pk_bf16_f32 v89, v90, v91\n"
        "v_mfma_f32_16x16x32_bf16 v[184:187], a[224:227], a[144:147], v[184:187]\n"
        "v_exp_f32 v92, v92\n"
        "v_mfma_f32_16x16x32_bf16 v[196:199], a[224:227], a[168:171], 0\n"
        "v_exp_f32 v93, v93\n"
        "v_mfma_f32_16x16x32_bf16 v[196:199], a[228:231], a[172:175], v[196:199]\n"
        "v_exp_f32 v94, v94\n"
        "v_mfma_f32_16x16x32_bf16 v[196:199], a[232:235], a[176:179], v[196:199]\n"
        "ds_read_b128 a[248:251], v240 offset:1792\n"
        "v_cvt_pk_bf16_f32 v90, v92, v93\n"
        "v_mfma_f32_16x16x32_bf16 v[196:199], a[236:239], a[180:183], v[196:199]\n"
        "v_exp_f32 v95, v95\n"
        "v_mfma_f32_16x16x32_bf16 v[200:203], a[236:239], a[204:207], 0\n"
        "ds_read_b128 a[216:219], v241 offset:1792\n"
        "v_cvt_pk_bf16_f32 v91, v94, v95\n"
        "v_mfma_f32_16x16x32_bf16 v[200:203], a[232:235], a[200:203], v[200:203]\n"
        "v_exp_f32 v108, v108\n"
        "v_mfma_f32_16x16x32_bf16 v[200:203], a[228:231], a[196:199], v[200:203]\n"
        "v_exp_f32 v109, v109\n"
        "v_mfma_f32_16x16x32_bf16 v[200:203], a[224:227], a[192:195], v[200:203]\n"
        "v_exp_f32 v110, v110\n"
        "v_exp_f32 v111, v111\n"
        "v_exp_f32 v70, v70\n"
        "v_exp_f32 v69, v69\n"
        "v_exp_f32 v68, v68\n"
        "v_exp_f32 v71, v71\n"
        "v_cvt_pk_bf16_f32 v68, v68, v69\n"
        "v_cvt_pk_bf16_f32 v69, v70, v71\n"
        "v_cvt_pk_bf16_f32 v70, v108, v109\n"
        "v_cvt_pk_bf16_f32 v71, v110, v111\n"
        "v_mfma_f32_16x16x32_bf16 v[108:111], v[192:195], v[216:219], v[56:59]\n"
        "s_waitcnt lgkmcnt(4)\n"
        "v_exp_f32 v112, v112\n"
        "v_mfma_f32_16x16x32_bf16 v[108:111], v[208:211], v[220:223], v[108:111]\n"
        "v_exp_f32 v113, v113\n"
        "v_exp_f32 v114, v114\n"
        "v_exp_f32 v115, v115\n"
        "v_exp_f32 v78, v78\n"
        "v_exp_f32 v77, v77\n"
        "v_exp_f32 v76, v76\n"
        "v_exp_f32 v79, v79\n"
        "v_cvt_pk_bf16_f32 v76, v76, v77\n"
        "v_cvt_pk_bf16_f32 v77, v78, v79\n"
        "v_cvt_pk_bf16_f32 v78, v112, v113\n"
        "v_cvt_pk_bf16_f32 v79, v114, v115\n"
        "v_mfma_f32_16x16x32_bf16 v[112:115], v[212:215], v[216:219], v[60:63]\n"
        "v_exp_f32 v116, v116\n"
        "v_mfma_f32_16x16x32_bf16 v[112:115], v[228:231], v[224:227], v[112:115]\n"
        "s_waitcnt lgkmcnt(3)\n"
        "v_exp_f32 v117, v117\n"
        "v_mfma_f32_16x16x32_bf16 v[120:123], a[240:243], a[160:163], v[120:123]\n"
        "s_waitcnt lgkmcnt(2)\n"
        "v_exp_f32 v118, v118\n"
        "v_mfma_f32_16x16x32_bf16 v[120:123], a[244:247], a[164:167], v[120:123]\n"
        "v_exp_f32 v119, v119\n"
        "v_mfma_f32_16x16x32_bf16 v[96:99], a[244:247], a[188:191], v[96:99]\n"
        "ds_read_b128 a[220:223], v240 offset:12544\n"
        "v_mfma_f32_16x16x32_bf16 v[96:99], a[240:243], a[184:187], v[96:99]\n"
        "ds_read_b128 a[224:227], v241 offset:12544\n"
        "v_mfma_f32_16x16x32_bf16 v[100:103], a[240:243], a[208:211], v[100:103]\n"
        "v_exp_f32 v84, v84\n"
        "v_mfma_f32_16x16x32_bf16 v[100:103], a[244:247], a[212:215], v[100:103]\n"
        "s_waitcnt lgkmcnt(2)\n"
        "v_exp_f32 v85, v85\n"
        "v_mfma_f32_16x16x32_bf16 v[104:107], a[216:219], a[164:167], v[104:107]\n"
        "v_exp_f32 v86, v86\n"
        "v_mfma_f32_16x16x32_bf16 v[104:107], a[248:251], a[160:163], v[104:107]\n"
        "v_cvt_pk_bf16_f32 v84, v84, v85\n"
        "v_cvt_pk_bf16_f32 v128, v120, v121\n"
        "v_mfma_f32_16x16x32_bf16 v[156:159], a[248:251], a[184:187], v[156:159]\n"
        "ds_read_b128 a[228:231], v240 offset:14080\n"
        "v_cvt_pk_bf16_f32 v129, v122, v123\n"
        "v_mfma_f32_16x16x32_bf16 v[156:159], a[216:219], a[188:191], v[156:159]\n"
        "ds_read_b128 a[232:235], v241 offset:14080\n"
        "v_cvt_pk_bf16_f32 v136, v96, v97\n"
        "v_mfma_f32_16x16x32_bf16 v[160:163], a[216:219], a[212:215], v[160:163]\n"
        "v_exp_f32 v87, v87\n"
        "v_mfma_f32_16x16x32_bf16 v[160:163], a[248:251], a[208:211], v[160:163]\n"
        "s_waitcnt lgkmcnt(3)\n"
        "v_cvt_pk_bf16_f32 v85, v86, v87\n"
        "v_cvt_pk_bf16_f32 v86, v116, v117\n"
        "v_mfma_f32_16x16x32_bf16 v[164:167], a[220:223], a[160:163], v[164:167]\n"
        "s_waitcnt lgkmcnt(2)\n"
        "v_cvt_pk_bf16_f32 v87, v118, v119\n"
        "v_cvt_pk_bf16_f32 v137, v98, v99\n"
        "v_mfma_f32_16x16x32_bf16 v[164:167], a[224:227], a[164:167], v[164:167]\n"
        "v_exp_f32 v108, v108\n"
        "v_mfma_f32_16x16x32_bf16 v[176:179], a[224:227], a[188:191], v[176:179]\n"
        "v_exp_f32 v109, v109\n"
        "v_mfma_f32_16x16x32_bf16 v[176:179], a[220:223], a[184:187], v[176:179]\n"
        "v_exp_f32 v110, v110\n"
        "v_mfma_f32_16x16x32_bf16 v[180:183], a[220:223], a[208:211], v[180:183]\n"
        "v_cvt_pk_bf16_f32 v92, v108, v109\n"
        "v_cvt_pk_bf16_f32 v144, v100, v101\n"
        "v_mfma_f32_16x16x32_bf16 v[180:183], a[224:227], a[212:215], v[180:183]\n"
        "s_waitcnt lgkmcnt(0)\n"
        "v_exp_f32 v111, v111\n"
        "v_mfma_f32_16x16x32_bf16 v[184:187], a[232:235], a[164:167], v[184:187]\n"
        "v_cvt_pk_bf16_f32 v93, v110, v111\n"
        "v_cvt_pk_bf16_f32 v145, v102, v103\n"
        "v_mfma_f32_16x16x32_bf16 v[184:187], a[228:231], a[160:163], v[184:187]\n"
        "v_exp_f32 v112, v112\n"
        "v_mfma_f32_16x16x32_bf16 v[196:199], a[228:231], a[184:187], v[196:199]\n"
        "v_exp_f32 v113, v113\n"
        "v_mfma_f32_16x16x32_bf16 v[196:199], a[232:235], a[188:191], v[196:199]\n"
        "v_exp_f32 v114, v114\n"
        "v_mfma_f32_16x16x32_bf16 v[200:203], a[232:235], a[212:215], v[200:203]\n"
        "v_cvt_pk_bf16_f32 v94, v112, v113\n"
        "v_cvt_pk_bf16_f32 v130, v104, v105\n"
        "v_mfma_f32_16x16x32_bf16 v[200:203], a[228:231], a[208:211], v[200:203]\n"
        "v_exp_f32 v115, v115\n"
        "v_mov_b64 v[120:121], 0\n"
        "s_nop 1\n"
        "v_smfmac_f32_16x16x64_bf16 v[120:123], a[252:255], v[64:71], v255\n"
        "v_cvt_pk_bf16_f32 v95, v114, v115\n"
        "v_cvt_pk_bf16_f32 v131, v106, v107\n"
        "s_nop 1\n"
        "v_mfma_f32_16x16x32_bf16 v[116:119], v[128:131], v[64:67], 0\n"
        "v_cvt_pk_bf16_f32 v138, v156, v157\n"
        "v_cvt_pk_bf16_f32 v139, v158, v159\n"
        "v_cvt_pk_bf16_f32 v132, v164, v165\n"
        "v_cvt_pk_bf16_f32 v133, v166, v167\n"
        "v_cvt_pk_bf16_f32 v134, v184, v185\n"
        "v_cvt_pk_bf16_f32 v135, v186, v187\n"
        "s_nop 1\n"
        "v_mfma_f32_16x16x32_bf16 v[116:119], v[132:135], v[68:71], v[116:119]\n"
        "v_cvt_pk_bf16_f32 v146, v160, v161\n"
        "v_cvt_pk_bf16_f32 v147, v162, v163\n"
        "v_cvt_pk_bf16_f32 v140, v176, v177\n"
        "v_cvt_pk_bf16_f32 v141, v178, v179\n"
        "v_cvt_pk_bf16_f32 v142, v196, v197\n"
        "v_cvt_pk_bf16_f32 v143, v198, v199\n"
        "s_nop 1\n"
        "v_mfma_f32_16x16x32_bf16 v[152:155], v[140:143], v[68:71], 0\n"
        "v_mov_b64 v[172:173], 0\n"
        "v_cvt_pk_bf16_f32 v148, v180, v181\n"
        "v_mfma_f32_16x16x32_bf16 v[152:155], v[136:139], v[64:67], v[152:155]\n"
        "v_cvt_pk_bf16_f32 v149, v182, v183\n"
        "v_cvt_pk_bf16_f32 v150, v200, v201\n"
        "v_mfma_f32_16x16x32_bf16 v[160:163], v[144:147], v[64:67], 0\n"
        "v_cvt_pk_bf16_f32 v151, v202, v203\n"
        "v_mov_b64 v[192:193], 0\n"
        "s_nop 0\n"
        "v_mfma_f32_16x16x32_bf16 v[160:163], v[148:151], v[68:71], v[160:163]\n"
        "v_add_f32 v120, v120, v121\n"
        "v_add_u32 v240, 24576, v240\n"
        "v_smfmac_f32_16x16x64_bf16 v[172:175], a[252:255], v[72:79], v255\n"
        "v_add_u32 v241, 24576, v241\n"
        "v_mfma_f32_16x16x32_bf16 v[176:179], v[128:131], v[72:75], 0\n"
        "v_rcp_f32 v121, v120\n"
        "v_mfma_f32_16x16x32_bf16 v[176:179], v[132:135], v[76:79], v[176:179]\n"
        "v_mov_b64 v[212:213], 0\n"
        "v_mul_f32 v116, v121, v116\n"
        "v_mfma_f32_16x16x32_bf16 v[180:183], v[140:143], v[76:79], 0\n"
        "v_mul_f32 v117, v121, v117\n"
        "v_mul_f32 v118, v121, v118\n"
        "v_mfma_f32_16x16x32_bf16 v[180:183], v[136:139], v[72:75], v[180:183]\n"
        "v_mul_f32 v119, v121, v119\n"
        "v_cvt_pk_bf16_f32 v116, v116, v117\n"
        "v_mfma_f32_16x16x32_bf16 v[184:187], v[144:147], v[72:75], 0\n"
        "v_cvt_pk_bf16_f32 v117, v118, v119\n"
        "v_mul_f32 v152, v121, v152\n"
        "v_mfma_f32_16x16x32_bf16 v[184:187], v[148:151], v[76:79], v[184:187]\n"
        "s_waitcnt vmcnt(5)\n"
        "v_mul_f32 v153, v121, v153\n"
        "v_maximum3_f32 v253, v253, v120, v121\n"
        "v_smfmac_f32_16x16x64_bf16 v[192:195], a[252:255], v[80:87], v255\n"
        "s_barrier\n"
        "ds_read_b128 a[236:239], v240 offset:0\n"
        "v_mul_f32 v154, v121, v154\n"
        "v_mfma_f32_16x16x32_bf16 v[196:199], v[128:131], v[80:83], 0\n"
        "s_add_u32 m0, s66, 0\n"
        "ds_read_b128 a[240:243], v241 offset:0\n"
        "v_mul_f32 v155, v121, v155\n"
        "v_mfma_f32_16x16x32_bf16 v[196:199], v[132:135], v[84:87], v[196:199]\n"
        "s_add_u32 s64, s51, 0\n"
        "v_cvt_pk_bf16_f32 v118, v152, v153\n"
        "v_cvt_pk_bf16_f32 v119, v154, v155\n"
        "v_mfma_f32_16x16x32_bf16 v[200:203], v[140:143], v[84:87], 0\n"
        "ds_read_b128 a[244:247], v240 offset:128\n"
        "v_mul_f32 v160, v121, v160\n"
        "v_mfma_f32_16x16x32_bf16 v[200:203], v[136:139], v[80:83], v[200:203]\n"
        "buffer_load_dwordx4 v124, s[52:55], s64 offen lds\n"
        "v_mfma_f32_16x16x32_bf16 v[204:207], v[144:147], v[80:83], 0\n"
        "ds_read_b128 a[248:251], v241 offset:128\n"
        "v_mul_f32 v161, v121, v161\n"
        "v_mfma_f32_16x16x32_bf16 v[204:207], v[148:151], v[84:87], v[204:207]\n"
        "ds_read_b128 a[216:219], v240 offset:256\n"
        "v_mul_f32 v162, v121, v162\n"
        "v_smfmac_f32_16x16x64_bf16 v[212:215], a[252:255], v[88:95], v255\n"
        "ds_read_b128 a[220:223], v241 offset:256\n"
        "v_mul_f32 v163, v121, v163\n"
        "v_mfma_f32_16x16x32_bf16 v[216:219], v[128:131], v[88:91], 0\n"
        "ds_write_b128 v245, v[116:119] offset:0\n"
        "v_mfma_f32_16x16x32_bf16 v[216:219], v[132:135], v[92:95], v[216:219]\n"
        "v_cvt_pk_bf16_f32 v160, v160, v161\n"
        "v_cvt_pk_bf16_f32 v161, v162, v163\n"
        "v_mfma_f32_16x16x32_bf16 v[220:223], v[140:143], v[92:95], 0\n"
        "v_add_f32 v172, v172, v173\n"
        "v_add_f32 v192, v192, v193\n"
        "v_mfma_f32_16x16x32_bf16 v[220:223], v[136:139], v[88:91], v[220:223]\n"
        "v_rcp_f32 v173, v172\n"
        "v_mfma_f32_16x16x32_bf16 v[224:227], v[144:147], v[88:91], 0\n"
        "ds_write_b64 v246, v[160:161] offset:0\n"
        "v_mfma_f32_16x16x32_bf16 v[224:227], v[148:151], v[92:95], v[224:227]\n"
        "s_waitcnt lgkmcnt(7)\n"
        "v_mul_f32 v176, v173, v176\n"
        "v_mul_f32 v177, v173, v177\n"
        "v_mfma_f32_16x16x32_bf16 v[96:99], a[72:75], a[236:239], 0\n"
        "s_waitcnt lgkmcnt(6)\n"
        "v_mul_f32 v178, v173, v178\n"
        "v_mul_f32 v179, v173, v179\n"
        "v_mfma_f32_16x16x32_bf16 v[96:99], a[76:79], a[240:243], v[96:99]\n"
        "s_waitcnt lgkmcnt(5)\n"
        "buffer_load_dwordx4 v125, s[52:55], s64 offen offset:1024 lds\n"
        "v_mfma_f32_16x16x32_bf16 v[96:99], a[80:83], a[244:247], v[96:99]\n"
        "s_waitcnt lgkmcnt(4)\n"
        "v_cvt_pk_bf16_f32 v176, v176, v177\n"
        "v_cvt_pk_bf16_f32 v177, v178, v179\n"
        "v_mfma_f32_16x16x32_bf16 v[96:99], a[84:87], a[248:251], v[96:99]\n"
        "s_waitcnt lgkmcnt(3)\n"
        "v_rcp_f32 v193, v192\n"
        "v_mfma_f32_16x16x32_bf16 v[96:99], a[88:91], a[216:219], v[96:99]\n"
        "s_waitcnt lgkmcnt(2)\n"
        "v_mul_f32 v180, v173, v180\n"
        "v_mul_f32 v181, v173, v181\n"
        "v_mfma_f32_16x16x32_bf16 v[96:99], a[92:95], a[220:223], v[96:99]\n"
        "s_add_u32 s41, s29, s40\n"
        "v_maximum3_f32 v253, v253, v172, v173\n"
        "v_mul_f32 v182, v173, v182\n"
        "v_mfma_f32_16x16x32_bf16 v[100:103], a[116:119], a[220:223], 0\n"
        "s_lshr_b32 s47, s58, 6\n"
        "v_mul_f32 v183, v173, v183\n"
        "v_cvt_pk_bf16_f32 v178, v180, v181\n"
        "v_mfma_f32_16x16x32_bf16 v[100:103], a[112:115], a[216:219], v[100:103]\n"
        "s_mul_i32 s47, s47, 1572864\n"
        "v_cvt_pk_bf16_f32 v179, v182, v183\n"
        "v_cvt_pk_bf16_f32 v152, v96, v97\n"
        "v_mfma_f32_16x16x32_bf16 v[100:103], a[108:111], a[248:251], v[100:103]\n"
        "buffer_load_dwordx4 v126, s[52:55], s64 offen offset:2048 lds\n"
        "v_mfma_f32_16x16x32_bf16 v[100:103], a[104:107], a[244:247], v[100:103]\n"
        "s_sub_u32 s31, s28, s30\n"
        "v_cvt_pk_bf16_f32 v153, v98, v99\n"
        "v_mul_f32 v184, v173, v184\n"
        "v_mfma_f32_16x16x32_bf16 v[100:103], a[100:103], a[240:243], v[100:103]\n"
        "s_sub_u32 s45, s29, s28\n"
        "ds_write_b128 v245, v[176:179] offset:256\n"
        "v_mfma_f32_16x16x32_bf16 v[100:103], a[96:99], a[236:239], v[100:103]\n"
        "s_mov_b32 s29, s30\n"
        "v_mul_f32 v185, v173, v185\n"
        "v_mul_f32 v186, v173, v186\n"
        "v_mfma_f32_16x16x32_bf16 v[104:107], a[120:123], a[236:239], 0\n"
        "s_mov_b32 s30, s28\n"
        "v_mul_f32 v187, v173, v187\n"
        "v_cvt_pk_bf16_f32 v184, v184, v185\n"
        "v_mfma_f32_16x16x32_bf16 v[104:107], a[124:127], a[240:243], v[104:107]\n"
        "s_add_u32 s28, s28, s45\n"
        "v_cvt_pk_bf16_f32 v185, v186, v187\n"
        "v_cvt_pk_bf16_f32 v154, v100, v101\n"
        "v_mfma_f32_16x16x32_bf16 v[104:107], a[128:131], a[244:247], v[104:107]\n"
        "s_add_u32 m0, s66, 3072\n"
        "v_cvt_pk_bf16_f32 v155, v102, v103\n"
        "v_mul_f32 v196, v193, v196\n"
        "v_mfma_f32_16x16x32_bf16 v[104:107], a[132:135], a[248:251], v[104:107]\n"
        "s_add_u32 s47, s47, s24\n"
        "ds_write_b64 v246, v[184:185] offset:256\n"
        "v_mfma_f32_16x16x32_bf16 v[104:107], a[136:139], a[216:219], v[104:107]\n"
        "s_add_u32 s64, s51, 6144\n"
        "v_mul_f32 v197, v193, v197\n"
        "v_mul_f32 v198, v193, v198\n"
        "v_mfma_f32_16x16x32_bf16 v[104:107], a[140:143], a[220:223], v[104:107]\n"
        "s_lshl_b32 s36, s58, 13\n"
        "v_mul_f32 v199, v193, v199\n"
        "v_cvt_pk_bf16_f32 v196, v196, v197\n"
        "v_mfma_f32_16x16x32_bf16 v[96:99], a[20:23], a[220:223], 0\n"
        "s_add_u32 s36, s36, s34\n"
        "v_cvt_pk_bf16_f32 v197, v198, v199\n"
        "v_mul_f32 v200, v193, v200\n"
        "v_mfma_f32_16x16x32_bf16 v[96:99], a[16:19], a[216:219], v[96:99]\n"
        "buffer_load_dwordx4 v124, s[52:55], s64 offen lds\n"
        "v_mfma_f32_16x16x32_bf16 v[96:99], a[12:15], a[248:251], v[96:99]\n"
        "s_sub_u32 s62, s58, s57\n"
        "v_cvt_pk_bf16_f32 v168, v104, v105\n"
        "v_cvt_pk_bf16_f32 v169, v106, v107\n"
        "v_mfma_f32_16x16x32_bf16 v[96:99], a[8:11], a[244:247], v[96:99]\n"
        "s_and_b32 s62, s62, 63\n"
        "v_mul_f32 v201, v193, v201\n"
        "v_maximum3_f32 v253, v253, v192, v193\n"
        "v_mfma_f32_16x16x32_bf16 v[96:99], a[4:7], a[240:243], v[96:99]\n"
        "s_mul_i32 s62, s62, 3072\n"
        "v_mul_f32 v202, v193, v202\n"
        "v_mul_f32 v203, v193, v203\n"
        "v_mfma_f32_16x16x32_bf16 v[96:99], a[0:3], a[236:239], v[96:99]\n"
        "s_add_u32 s35, s47, 786432\n"
        "v_cvt_pk_bf16_f32 v198, v200, v201\n"
        "v_cvt_pk_bf16_f32 v199, v202, v203\n"
        "v_mfma_f32_16x16x32_bf16 v[100:103], a[24:27], a[236:239], 0\n"
        "s_sub_u32 s46, s35, 100663296\n"
        "ds_write_b128 v245, v[196:199] offset:512\n"
        "v_mfma_f32_16x16x32_bf16 v[100:103], a[28:31], a[240:243], v[100:103]\n"
        "s_cmp_lt_u32 s46, 786432\n"
        "v_mul_f32 v204, v193, v204\n"
        "v_mul_f32 v96, s50, v96\n"
        "v_mfma_f32_16x16x32_bf16 v[100:103], a[32:35], a[244:247], v[100:103]\n"
        "s_cselect_b32 s35, s46, s35\n"
        "v_mul_f32 v97, s50, v97\n"
        "v_mul_f32 v98, s50, v98\n"
        "v_mfma_f32_16x16x32_bf16 v[100:103], a[36:39], a[248:251], v[100:103]\n"
        "v_mul_f32 v99, s50, v99\n"
        "v_cvt_pk_bf16_f32 v156, v96, v97\n"
        "v_mfma_f32_16x16x32_bf16 v[100:103], a[40:43], a[216:219], v[100:103]\n"
        "buffer_load_dwordx4 v125, s[52:55], s64 offen offset:1024 lds\n"
        "v_mfma_f32_16x16x32_bf16 v[100:103], a[44:47], a[220:223], v[100:103]\n"
        "v_cvt_pk_bf16_f32 v157, v98, v99\n"
        "v_mul_f32 v205, v193, v205\n"
        "v_mfma_f32_16x16x32_bf16 v[104:107], a[68:71], a[220:223], 0\n"
        "v_mul_f32 v206, v193, v206\n"
        "v_mul_f32 v207, v193, v207\n"
        "v_mfma_f32_16x16x32_bf16 v[104:107], a[64:67], a[216:219], v[104:107]\n"
        "v_cvt_pk_bf16_f32 v204, v204, v205\n"
        "v_cvt_pk_bf16_f32 v205, v206, v207\n"
        "v_mfma_f32_16x16x32_bf16 v[104:107], a[60:63], a[248:251], v[104:107]\n"
        "v_mul_f32 v100, s50, v100\n"
        "v_mul_f32 v101, s50, v101\n"
        "v_mfma_f32_16x16x32_bf16 v[104:107], a[56:59], a[244:247], v[104:107]\n"
        "v_mul_f32 v102, s50, v102\n"
        "v_mul_f32 v103, s50, v103\n"
        "v_mfma_f32_16x16x32_bf16 v[104:107], a[52:55], a[240:243], v[104:107]\n"
        "v_cvt_pk_bf16_f32 v158, v100, v101\n"
        "v_cvt_pk_bf16_f32 v159, v102, v103\n"
        "v_mfma_f32_16x16x32_bf16 v[104:107], a[48:51], a[236:239], v[104:107]\n"
        "buffer_load_dwordx4 v126, s[52:55], s64 offen offset:2048 lds\n"
        "v_mfma_f32_16x16x32_bf16 v[96:99], a[236:239], a[144:147], 0\n"
        "ds_write_b64 v246, v[204:205] offset:512\n"
        "v_mfma_f32_16x16x32_bf16 v[96:99], a[240:243], a[148:151], v[96:99]\n"
        "v_add_f32 v212, v212, v213\n"
        "v_xor_b32 v251, 0x2000, v251\n"
        "v_mfma_f32_16x16x32_bf16 v[96:99], a[244:247], a[152:155], v[96:99]\n"
        "v_rcp_f32 v213, v212\n"
        "v_mfma_f32_16x16x32_bf16 v[96:99], a[248:251], a[156:159], v[96:99]\n"
        "v_mul_f32 v104, s50, v104\n"
        "v_mul_f32 v105, s50, v105\n"
        "v_mfma_f32_16x16x32_bf16 v[96:99], a[216:219], a[160:163], v[96:99]\n"
        "s_lshl_b64 s[60:61], s[60:61], 8\n"
        "v_mul_f32 v106, s50, v106\n"
        "v_mul_f32 v107, s50, v107\n"
        "v_mfma_f32_16x16x32_bf16 v[96:99], a[220:223], a[164:167], v[96:99]\n"
        "s_add_u32 m0, s66, 6144\n"
        "v_cvt_pk_bf16_f32 v160, v104, v105\n"
        "v_cvt_pk_bf16_f32 v161, v106, v107\n"
        "v_mfma_f32_16x16x32_bf16 v[100:103], a[220:223], a[188:191], 0\n"
        "s_add_u32 s64, s51, 18432\n"
        "v_mov_b64 v[166:167], v[160:161]\n"
        "v_mul_f32 v216, v213, v216\n"
        "v_mfma_f32_16x16x32_bf16 v[100:103], a[216:219], a[184:187], v[100:103]\n"
        "s_and_b32 s63, s58, 255\n"
        "v_mul_f32 v217, v213, v217\n"
        "v_mul_f32 v218, v213, v218\n"
        "v_mfma_f32_16x16x32_bf16 v[100:103], a[248:251], a[180:183], v[100:103]\n"
        "buffer_load_dwordx4 v124, s[52:55], s64 offen lds\n"
        "v_mfma_f32_16x16x32_bf16 v[100:103], a[244:247], a[176:179], v[100:103]\n"
        "s_or_b32 s60, s60, s63\n"
        "ds_read_b128 a[224:227], v240 offset:1536\n"
        "ds_read_b128 a[228:231], v241 offset:1536\n"
        "v_mfma_f32_16x16x32_bf16 v[100:103], a[240:243], a[172:175], v[100:103]\n"
        "s_add_u32 s27, s27, 1\n"
        "ds_read_b128 a[232:235], v240 offset:1664\n"
        "v_mul_f32 v219, v213, v219\n"
        "v_mfma_f32_16x16x32_bf16 v[100:103], a[236:239], a[168:171], v[100:103]\n"
        "s_add_u32 s63, s58, 256\n"
        "v_cvt_pk_bf16_f32 v216, v216, v217\n"
        "v_cvt_pk_bf16_f32 v217, v218, v219\n"
        "v_mfma_f32_16x16x32_bf16 v[104:107], a[236:239], a[192:195], 0\n"
        "s_cmp_lt_u32 s63, 3328\n"
        "ds_read_b128 a[236:239], v241 offset:1664\n"
        "v_cvt_pk_bf16_f32 v128, v96, v97\n"
        "v_mfma_f32_16x16x32_bf16 v[104:107], a[240:243], a[196:199], v[104:107]\n"
        "s_cselect_b32 s63, s63, -1\n"
        "ds_read_b128 a[240:243], v240 offset:1792\n"
        "v_cvt_pk_bf16_f32 v129, v98, v99\n"
        "v_mfma_f32_16x16x32_bf16 v[104:107], a[244:247], a[200:203], v[104:107]\n"
        "s_cmp_lt_u32 s27, 3072\n"
        "ds_read_b128 a[244:247], v241 offset:1792\n"
        "v_mul_f32 v220, v213, v220\n"
        "v_mfma_f32_16x16x32_bf16 v[104:107], a[248:251], a[204:207], v[104:107]\n"
        "s_cselect_b32 s49, 4096, 0\n"
        "v_mul_f32 v221, v213, v221\n"
        "v_maximum3_f32 v253, v253, v212, v213\n"
        "v_mfma_f32_16x16x32_bf16 v[104:107], a[216:219], a[208:211], v[104:107]\n"
        "v_mul_f32 v222, v213, v222\n"
        "v_mul_f32 v223, v213, v223\n"
        "v_mfma_f32_16x16x32_bf16 v[104:107], a[220:223], a[212:215], v[104:107]\n"
        "s_waitcnt lgkmcnt(5)\n"
        "buffer_load_dwordx4 v125, s[52:55], s64 offen offset:1024 lds\n"
        "v_mfma_f32_16x16x32_bf16 v[96:99], a[72:75], a[224:227], 0\n"
        "s_waitcnt lgkmcnt(4)\n"
        "v_cvt_pk_bf16_f32 v218, v220, v221\n"
        "v_cvt_pk_bf16_f32 v219, v222, v223\n"
        "v_mfma_f32_16x16x32_bf16 v[96:99], a[76:79], a[228:231], v[96:99]\n"
        "s_waitcnt lgkmcnt(3)\n"
        "v_xor_b32 v252, 0x2000, v252\n"
        "v_mul_f32 v224, v213, v224\n"
        "v_mfma_f32_16x16x32_bf16 v[96:99], a[80:83], a[232:235], v[96:99]\n"
        "s_waitcnt lgkmcnt(2)\n"
        "ds_write_b128 v245, v[216:219] offset:768\n"
        "v_mfma_f32_16x16x32_bf16 v[96:99], a[84:87], a[236:239], v[96:99]\n"
        "s_waitcnt lgkmcnt(2)\n"
        "v_mul_f32 v225, v213, v225\n"
        "v_cvt_pk_bf16_f32 v136, v100, v101\n"
        "v_mfma_f32_16x16x32_bf16 v[96:99], a[88:91], a[240:243], v[96:99]\n"
        "s_waitcnt lgkmcnt(1)\n"
        "v_cvt_pk_bf16_f32 v137, v102, v103\n"
        "v_mul_f32 v226, v213, v226\n"
        "v_mfma_f32_16x16x32_bf16 v[96:99], a[92:95], a[244:247], v[96:99]\n"
        "s_waitcnt vmcnt(8)\n"
        "v_mul_f32 v227, v213, v227\n"
        "v_cvt_pk_bf16_f32 v224, v224, v225\n"
        "v_mfma_f32_16x16x32_bf16 v[100:103], a[116:119], a[244:247], 0\n"
        "s_barrier\n"
        "v_cvt_pk_bf16_f32 v225, v226, v227\n"
        "v_cvt_pk_bf16_f32 v144, v104, v105\n"
        "v_mfma_f32_16x16x32_bf16 v[100:103], a[112:115], a[240:243], v[100:103]\n"
        "42:\n"
        "s_cbranch_execz 45f\n"
        "ds_write_b64 v246, v[224:225] offset:768\n"
        "v_mfma_f32_16x16x32_bf16 v[100:103], a[108:111], a[236:239], v[100:103]\n"
        "v_cvt_pk_bf16_f32 v145, v106, v107\n"
        "v_cvt_pk_bf16_f32 v172, v96, v97\n"
        "v_mfma_f32_16x16x32_bf16 v[100:103], a[104:107], a[232:235], v[100:103]\n"
        "v_cvt_pk_bf16_f32 v173, v98, v99\n"
        "v_add_u32 v245, s31, v245\n"
        "v_mfma_f32_16x16x32_bf16 v[100:103], a[100:103], a[228:231], v[100:103]\n"
        "buffer_load_dwordx4 v126, s[52:55], s64 offen offset:2048 lds\n"
        "v_mfma_f32_16x16x32_bf16 v[100:103], a[96:99], a[224:227], v[100:103]\n"
        "s_waitcnt vmcnt(3)\n"
        "ds_read_b64_tr_b16 v[88:89], v234 offset:0\n"
        "ds_read_b64_tr_b16 v[90:91], v234 offset:3072\n"
        "v_mfma_f32_16x16x32_bf16 v[104:107], a[120:123], a[224:227], 0\n"
        "ds_read_b64_tr_b16 v[92:93], v235 offset:32\n"
        "v_add_u32 v246, s31, v246\n"
        "v_mfma_f32_16x16x32_bf16 v[104:107], a[124:127], a[228:231], v[104:107]\n"
        "ds_read_b64_tr_b16 v[94:95], v235 offset:3104\n"
        "v_mfma_f32_16x16x32_bf16 v[104:107], a[128:131], a[232:235], v[104:107]\n"
        "s_add_u32 m0, s66, 9216\n"
        "ds_read_b64_tr_b16 v[120:121], v234 offset:64\n"
        "ds_read_b128 v[162:165], v251 offset:0\n"
        "v_mfma_f32_16x16x32_bf16 v[104:107], a[132:135], a[236:239], v[104:107]\n"
        "s_add_u32 s64, s51, 24576\n"
        "ds_read_b64_tr_b16 v[122:123], v234 offset:3136\n"
        "v_cvt_pk_bf16_f32 v174, v100, v101\n"
        "v_mfma_f32_16x16x32_bf16 v[104:107], a[136:139], a[240:243], v[104:107]\n"
        "ds_read_b64_tr_b16 v[216:217], v235 offset:96\n"
        "v_cvt_pk_bf16_f32 v175, v102, v103\n"
        "v_mfma_f32_16x16x32_bf16 v[104:107], a[140:143], a[244:247], v[104:107]\n"
        "ds_read_b64_tr_b16 v[218:219], v235 offset:3168\n"
        "v_mfma_f32_16x16x32_bf16 v[96:99], a[20:23], a[244:247], 0\n"
        "buffer_load_dwordx4 v124, s[52:55], s64 offen lds\n"
        "v_mfma_f32_16x16x32_bf16 v[96:99], a[16:19], a[240:243], v[96:99]\n"
        "ds_read_b64_tr_b16 v[220:221], v238 offset:128\n"
        "ds_read_b64_tr_b16 v[222:223], v238 offset:3200\n"
        "v_mfma_f32_16x16x32_bf16 v[96:99], a[12:15], a[236:239], v[96:99]\n"
        "ds_read_b64_tr_b16 v[224:225], v239 offset:160\n"
        "v_cvt_pk_bf16_f32 v190, v104, v105\n"
        "v_mfma_f32_16x16x32_bf16 v[96:99], a[8:11], a[232:235], v[96:99]\n"
        "ds_read_b64_tr_b16 v[226:227], v239 offset:3232\n"
        "v_cvt_pk_bf16_f32 v191, v106, v107\n"
        "v_mfma_f32_16x16x32_bf16 v[96:99], a[4:7], a[228:231], v[96:99]\n"
        "s_waitcnt lgkmcnt(11)\n"
        "ds_write_b128 v236, v[88:91] offset:0\n"
        "v_mfma_f32_16x16x32_bf16 v[96:99], a[0:3], a[224:227], v[96:99]\n"
        "v_mfma_f32_16x16x32_bf16 v[64:67], v[152:155], v[156:159], v[0:3]\n"
        "s_waitcnt lgkmcnt(8)\n"
        "v_mfma_f32_16x16x32_bf16 v[64:67], v[168:171], v[160:163], v[64:67]\n"
        "v_mfma_f32_16x16x32_bf16 v[68:71], v[172:175], v[156:159], v[4:7]\n"
        "buffer_load_dwordx4 v125, s[52:55], s64 offen offset:1024 lds\n"
        "v_mfma_f32_16x16x32_bf16 v[68:71], v[188:191], v[164:167], v[68:71]\n"
        "ds_write_b128 v236, v[92:95] offset:384\n"
        "v_mfma_f32_16x16x32_bf16 v[100:103], a[24:27], a[224:227], 0\n"
        "v_mfma_f32_16x16x32_bf16 v[100:103], a[28:31], a[228:231], v[100:103]\n"
        "v_mul_f32 v96, s50, v96\n"
        "v_mul_f32 v97, s50, v97\n"
        "v_mfma_f32_16x16x32_bf16 v[100:103], a[32:35], a[232:235], v[100:103]\n"
        "v_mul_f32 v98, s50, v98\n"
        "v_mul_f32 v99, s50, v99\n"
        "v_mfma_f32_16x16x32_bf16 v[100:103], a[36:39], a[236:239], v[100:103]\n"
        "v_cvt_pk_bf16_f32 v176, v96, v97\n"
        "v_cvt_pk_bf16_f32 v177, v98, v99\n"
        "v_mfma_f32_16x16x32_bf16 v[100:103], a[40:43], a[240:243], v[100:103]\n"
        "s_waitcnt lgkmcnt(8)\n"
        "ds_write_b128 v236, v[120:123] offset:768\n"
        "v_mfma_f32_16x16x32_bf16 v[100:103], a[44:47], a[244:247], v[100:103]\n"
        "v_exp_f32 v64, v64\n"
        "v_mfma_f32_16x16x32_bf16 v[104:107], a[68:71], a[244:247], 0\n"
        "v_exp_f32 v65, v65\n"
        "v_mfma_f32_16x16x32_bf16 v[104:107], a[64:67], a[240:243], v[104:107]\n"
        "v_exp_f32 v66, v66\n"
        "v_mfma_f32_16x16x32_bf16 v[104:107], a[60:63], a[236:239], v[104:107]\n"
        "buffer_load_dwordx4 v126, s[52:55], s64 offen offset:2048 lds\n"
        "v_mfma_f32_16x16x32_bf16 v[104:107], a[56:59], a[232:235], v[104:107]\n"
        "ds_read_b128 v[182:185], v251 offset:512\n"
        "v_mul_f32 v100, s50, v100\n"
        "v_mfma_f32_16x16x32_bf16 v[104:107], a[52:55], a[228:231], v[104:107]\n"
        "v_mul_f32 v101, s50, v101\n"
        "v_mul_f32 v102, s50, v102\n"
        "v_mfma_f32_16x16x32_bf16 v[104:107], a[48:51], a[224:227], v[104:107]\n"
        "v_mul_f32 v103, s50, v103\n"
        "v_cvt_pk_bf16_f32 v178, v100, v101\n"
        "v_mfma_f32_16x16x32_bf16 v[96:99], a[224:227], a[144:147], 0\n"
        "ds_read_b128 a[248:251], v240 offset:12288\n"
        "v_cvt_pk_bf16_f32 v179, v102, v103\n"
        "v_mfma_f32_16x16x32_bf16 v[96:99], a[228:231], a[148:151], v[96:99]\n"
        "s_add_u32 m0, s66, 12288\n"
        "s_waitcnt lgkmcnt(9)\n"
        "ds_write_b128 v236, v[216:219] offset:1152\n"
        "v_mfma_f32_16x16x32_bf16 v[96:99], a[232:235], a[152:155], v[96:99]\n"
        "s_add_u32 s64, s51, 12288\n"
        "v_mul_f32 v104, s50, v104\n"
        "v_mul_f32 v105, s50, v105\n"
        "v_mfma_f32_16x16x32_bf16 v[96:99], a[236:239], a[156:159], v[96:99]\n"
        "v_mul_f32 v106, s50, v106\n"
        "v_mul_f32 v107, s50, v107\n"
        "v_mfma_f32_16x16x32_bf16 v[96:99], a[240:243], a[160:163], v[96:99]\n"
        "v_cvt_pk_bf16_f32 v180, v104, v105\n"
        "v_cvt_pk_bf16_f32 v181, v106, v107\n"
        "v_mfma_f32_16x16x32_bf16 v[96:99], a[244:247], a[164:167], v[96:99]\n"
        "buffer_load_dwordx4 v127, s[52:55], s64 offen lds\n"
        "v_mfma_f32_16x16x32_bf16 v[72:75], v[152:155], v[176:179], v[16:19]\n"
        "s_waitcnt lgkmcnt(2)\n"
        "ds_write_b128 v237, v[220:223] offset:0\n"
        "v_mfma_f32_16x16x32_bf16 v[72:75], v[168:171], v[180:183], v[72:75]\n"
        "v_mov_b64 v[186:187], v[180:181]\n"
        "v_cvt_pk_bf16_f32 v64, v64, v65\n"
        "v_mfma_f32_16x16x32_bf16 v[76:79], v[172:175], v[176:179], v[20:23]\n"
        "v_exp_f32 v67, v67\n"
        "v_mfma_f32_16x16x32_bf16 v[76:79], v[188:191], v[184:187], v[76:79]\n"
        "v_cvt_pk_bf16_f32 v130, v96, v97\n"
        "v_cvt_pk_bf16_f32 v131, v98, v99\n"
        "v_mfma_f32_16x16x32_bf16 v[100:103], a[244:247], a[188:191], 0\n"
        "ds_write_b128 v237, v[224:227] offset:768\n"
        "v_mfma_f32_16x16x32_bf16 v[100:103], a[240:243], a[184:187], v[100:103]\n"
        "v_exp_f32 v68, v68\n"
        "v_mfma_f32_16x16x32_bf16 v[100:103], a[236:239], a[180:183], v[100:103]\n"
        "v_exp_f32 v69, v69\n"
        "v_mfma_f32_16x16x32_bf16 v[100:103], a[232:235], a[176:179], v[100:103]\n"
        "v_exp_f32 v70, v70\n"
        "v_mfma_f32_16x16x32_bf16 v[100:103], a[228:231], a[172:175], v[100:103]\n"
        "buffer_load_dwordx4 v232, s[52:55], s64 offen offset:1024 lds\n"
        "v_mfma_f32_16x16x32_bf16 v[100:103], a[224:227], a[168:171], v[100:103]\n"
        "ds_read_b128 a[216:219], v241 offset:12288\n"
        "ds_read_b128 a[220:223], v240 offset:12416\n"
        "v_mfma_f32_16x16x32_bf16 v[104:107], a[224:227], a[192:195], 0\n"
        "ds_read_b128 a[224:227], v241 offset:12416\n"
        "v_cvt_pk_bf16_f32 v65, v66, v67\n"
        "v_mfma_f32_16x16x32_bf16 v[104:107], a[228:231], a[196:199], v[104:107]\n"
        "ds_read_b128 a[228:231], v240 offset:12544\n"
        "v_cvt_pk_bf16_f32 v66, v68, v69\n"
        "v_mfma_f32_16x16x32_bf16 v[104:107], a[232:235], a[200:203], v[104:107]\n"
        "ds_read_b128 a[232:235], v241 offset:12544\n"
        "v_cvt_pk_bf16_f32 v138, v100, v101\n"
        "v_mfma_f32_16x16x32_bf16 v[104:107], a[236:239], a[204:207], v[104:107]\n"
        "v_cvt_pk_bf16_f32 v139, v102, v103\n"
        "v_mfma_f32_16x16x32_bf16 v[104:107], a[240:243], a[208:211], v[104:107]\n"
        "v_exp_f32 v71, v71\n"
        "v_mfma_f32_16x16x32_bf16 v[104:107], a[244:247], a[212:215], v[104:107]\n"
        "s_waitcnt lgkmcnt(8)\n"
        "v_exp_f32 v72, v72\n"
        "v_mfma_f32_16x16x32_bf16 v[96:99], a[72:75], a[248:251], 0\n"
        "s_waitcnt lgkmcnt(4)\n"
        "buffer_load_dwordx4 v233, s[52:55], s64 offen offset:2048 lds\n"
        "v_mfma_f32_16x16x32_bf16 v[96:99], a[76:79], a[216:219], v[96:99]\n"
        "s_waitcnt lgkmcnt(3)\n"
        "v_exp_f32 v73, v73\n"
        "v_mfma_f32_16x16x32_bf16 v[96:99], a[80:83], a[220:223], v[96:99]\n"
        "s_waitcnt lgkmcnt(2)\n"
        "v_exp_f32 v74, v74\n"
        "v_mfma_f32_16x16x32_bf16 v[96:99], a[84:87], a[224:227], v[96:99]\n"
        "s_waitcnt lgkmcnt(1)\n"
        "v_cvt_pk_bf16_f32 v146, v104, v105\n"
        "v_cvt_pk_bf16_f32 v147, v106, v107\n"
        "v_mfma_f32_16x16x32_bf16 v[96:99], a[88:91], a[228:231], v[96:99]\n"
        "s_waitcnt vmcnt(3) lgkmcnt(0)\n"
        "ds_read_b64_tr_b16 v[88:89], v234 offset:6144\n"
        "v_cvt_pk_bf16_f32 v67, v70, v71\n"
        "v_mfma_f32_16x16x32_bf16 v[96:99], a[92:95], a[232:235], v[96:99]\n"
        "ds_read_b64_tr_b16 v[90:91], v234 offset:9216\n"
        "v_cvt_pk_bf16_f32 v72, v72, v73\n"
        "v_mfma_f32_16x16x32_bf16 v[100:103], a[116:119], a[232:235], 0\n"
        "s_add_u32 m0, s66, 15360\n"
        "ds_read_b64_tr_b16 v[92:93], v235 offset:6176\n"
        "v_mfma_f32_16x16x32_bf16 v[100:103], a[112:115], a[228:231], v[100:103]\n"
        "s_add_u32 s64, s51, 13824\n"
        "ds_read_b64_tr_b16 v[94:95], v235 offset:9248\n"
        "v_cvt_pk_bf16_f32 v192, v96, v97\n"
        "v_mfma_f32_16x16x32_bf16 v[100:103], a[108:111], a[224:227], v[100:103]\n"
        "ds_read_b64_tr_b16 v[120:121], v234 offset:6208\n"
        "v_cvt_pk_bf16_f32 v193, v98, v99\n"
        "v_mfma_f32_16x16x32_bf16 v[100:103], a[104:107], a[220:223], v[100:103]\n"
        "ds_read_b64_tr_b16 v[122:123], v234 offset:9280\n"
        "v_mfma_f32_16x16x32_bf16 v[100:103], a[100:103], a[216:219], v[100:103]\n"
        "buffer_load_dwordx4 v127, s[52:55], s64 offen lds\n"
        "v_mfma_f32_16x16x32_bf16 v[100:103], a[96:99], a[248:251], v[100:103]\n"
        "ds_read_b64_tr_b16 v[216:217], v235 offset:6240\n"
        "ds_read_b64_tr_b16 v[218:219], v235 offset:9312\n"
        "v_mfma_f32_16x16x32_bf16 v[104:107], a[120:123], a[248:251], 0\n"
        "ds_read_b64_tr_b16 v[220:221], v238 offset:6272\n"
        "v_mfma_f32_16x16x32_bf16 v[104:107], a[124:127], a[216:219], v[104:107]\n"
        "ds_read_b64_tr_b16 v[222:223], v238 offset:9344\n"
        "v_mfma_f32_16x16x32_bf16 v[104:107], a[128:131], a[220:223], v[104:107]\n"
        "ds_read_b64_tr_b16 v[224:225], v239 offset:6304\n"
        "v_cvt_pk_bf16_f32 v194, v100, v101\n"
        "v_mfma_f32_16x16x32_bf16 v[104:107], a[132:135], a[224:227], v[104:107]\n"
        "ds_read_b64_tr_b16 v[226:227], v239 offset:9376\n"
        "v_cvt_pk_bf16_f32 v195, v102, v103\n"
        "v_mfma_f32_16x16x32_bf16 v[104:107], a[136:139], a[228:231], v[104:107]\n"
        "s_waitcnt lgkmcnt(10)\n"
        "ds_write_b128 v236, v[88:91] offset:6144\n"
        "v_mfma_f32_16x16x32_bf16 v[104:107], a[140:143], a[232:235], v[104:107]\n"
        "v_exp_f32 v75, v75\n"
        "v_mfma_f32_16x16x32_bf16 v[96:99], a[20:23], a[232:235], 0\n"
        "v_exp_f32 v76, v76\n"
        "v_mfma_f32_16x16x32_bf16 v[96:99], a[16:19], a[228:231], v[96:99]\n"
        "v_exp_f32 v77, v77\n"
        "v_mfma_f32_16x16x32_bf16 v[96:99], a[12:15], a[224:227], v[96:99]\n"
        "buffer_load_dwordx4 v232, s[52:55], s64 offen offset:1024 lds\n"
        "v_mfma_f32_16x16x32_bf16 v[96:99], a[8:11], a[220:223], v[96:99]\n"
        "s_waitcnt lgkmcnt(9)\n"
        "ds_write_b128 v236, v[92:95] offset:6528\n"
        "v_mfma_f32_16x16x32_bf16 v[96:99], a[4:7], a[216:219], v[96:99]\n"
        "v_cvt_pk_bf16_f32 v208, v104, v105\n"
        "v_cvt_pk_bf16_f32 v209, v106, v107\n"
        "v_mfma_f32_16x16x32_bf16 v[96:99], a[0:3], a[248:251], v[96:99]\n"
        "v_exp_f32 v78, v78\n"
        "v_mfma_f32_16x16x32_bf16 v[100:103], a[24:27], a[248:251], 0\n"
        "v_exp_f32 v79, v79\n"
        "v_mfma_f32_16x16x32_bf16 v[100:103], a[28:31], a[216:219], v[100:103]\n"
        "ds_read_b128 v[162:165], v252 offset:0\n"
        "v_cvt_pk_bf16_f32 v73, v74, v75\n"
        "v_mfma_f32_16x16x32_bf16 v[100:103], a[32:35], a[220:223], v[100:103]\n"
        "ds_read_b128 v[182:185], v252 offset:512\n"
        "v_mul_f32 v96, s50, v96\n"
        "v_mfma_f32_16x16x32_bf16 v[100:103], a[36:39], a[224:227], v[100:103]\n"
        "buffer_load_dwordx4 v233, s[52:55], s64 offen offset:2048 lds\n"
        "v_mfma_f32_16x16x32_bf16 v[100:103], a[40:43], a[228:231], v[100:103]\n"
        "s_waitcnt lgkmcnt(10)\n"
        "ds_write_b128 v236, v[120:123] offset:6912\n"
        "v_mfma_f32_16x16x32_bf16 v[100:103], a[44:47], a[232:235], v[100:103]\n"
        "v_mul_f32 v97, s50, v97\n"
        "v_mul_f32 v98, s50, v98\n"
        "v_mfma_f32_16x16x32_bf16 v[104:107], a[68:71], a[232:235], 0\n"
        "v_mul_f32 v99, s50, v99\n"
        "v_cvt_pk_bf16_f32 v196, v96, v97\n"
        "v_mfma_f32_16x16x32_bf16 v[104:107], a[64:67], a[228:231], v[104:107]\n"
        "v_cvt_pk_bf16_f32 v197, v98, v99\n"
        "v_cvt_pk_bf16_f32 v74, v76, v77\n"
        "v_mfma_f32_16x16x32_bf16 v[104:107], a[60:63], a[224:227], v[104:107]\n"
        "s_waitcnt lgkmcnt(9)\n"
        "ds_write_b128 v236, v[216:219] offset:7296\n"
        "v_mfma_f32_16x16x32_bf16 v[104:107], a[56:59], a[220:223], v[104:107]\n"
        "v_mul_f32 v100, s50, v100\n"
        "v_mul_f32 v101, s50, v101\n"
        "v_mfma_f32_16x16x32_bf16 v[104:107], a[52:55], a[216:219], v[104:107]\n"
        "s_add_u32 m0, s41, 0\n"
        "v_mul_f32 v102, s50, v102\n"
        "v_mul_f32 v103, s50, v103\n"
        "v_mfma_f32_16x16x32_bf16 v[104:107], a[48:51], a[248:251], v[104:107]\n"
        "v_cvt_pk_bf16_f32 v198, v100, v101\n"
        "v_cvt_pk_bf16_f32 v199, v102, v103\n"
        "v_mfma_f32_16x16x32_bf16 v[68:71], v[192:195], v[156:159], v[8:11]\n"
        "s_waitcnt lgkmcnt(3)\n"
        "buffer_load_dwordx4 v242, s[8:11], s47 offen lds\n"
        "v_mfma_f32_16x16x32_bf16 v[68:71], v[208:211], v[160:163], v[68:71]\n"
        "ds_write_b128 v237, v[220:223] offset:6144\n"
        "v_mfma_f32_16x16x32_bf16 v[96:99], a[248:251], a[144:147], 0\n"
        "v_cvt_pk_bf16_f32 v75, v78, v79\n"
        "v_mul_f32 v104, s50, v104\n"
        "v_mfma_f32_16x16x32_bf16 v[96:99], a[216:219], a[148:151], v[96:99]\n"
        "s_add_u32 m0, s41, 1024\n"
        "v_mul_f32 v105, s50, v105\n"
        "v_mul_f32 v106, s50, v106\n"
        "v_mfma_f32_16x16x32_bf16 v[96:99], a[220:223], a[152:155], v[96:99]\n"
        "v_mul_f32 v107, s50, v107\n"
        "v_cvt_pk_bf16_f32 v200, v104, v105\n"
        "v_mfma_f32_16x16x32_bf16 v[96:99], a[224:227], a[156:159], v[96:99]\n"
        "buffer_load_dwordx4 v243, s[8:11], s47 offen lds\n"
        "v_mfma_f32_16x16x32_bf16 v[96:99], a[228:231], a[160:163], v[96:99]\n"
        "ds_read_b128 v[202:205], v251 offset:4096\n"
        "v_cvt_pk_bf16_f32 v201, v106, v107\n"
        "v_mfma_f32_16x16x32_bf16 v[96:99], a[232:235], a[164:167], v[96:99]\n"
        "ds_write_b128 v237, v[224:227] offset:6912\n"
        "v_mfma_f32_16x16x32_bf16 v[76:79], v[192:195], v[176:179], v[24:27]\n"
        "s_waitcnt lgkmcnt(5)\n"
        "s_add_u32 m0, s41, 2048\n"
        "v_mov_b64 v[206:207], v[200:201]\n"
        "v_mfma_f32_16x16x32_bf16 v[76:79], v[208:211], v[180:183], v[76:79]\n"
        "v_exp_f32 v68, v68\n"
        "v_mfma_f32_16x16x32_bf16 v[100:103], a[232:235], a[188:191], 0\n"
        "v_exp_f32 v69, v69\n"
        "v_mfma_f32_16x16x32_bf16 v[100:103], a[228:231], a[184:187], v[100:103]\n"
        "buffer_load_dwordx4 v244, s[8:11], s47 offen lds\n"
        "v_mfma_f32_16x16x32_bf16 v[100:103], a[224:227], a[180:183], v[100:103]\n"
        "v_cvt_pk_bf16_f32 v132, v96, v97\n"
        "v_cvt_pk_bf16_f32 v133, v98, v99\n"
        "v_mfma_f32_16x16x32_bf16 v[100:103], a[220:223], a[176:179], v[100:103]\n"
        "s_add_u32 m0, s41, 12288\n"
        "ds_read_b128 a[236:239], v240 offset:13824\n"
        "v_cvt_pk_bf16_f32 v68, v68, v69\n"
        "v_mfma_f32_16x16x32_bf16 v[100:103], a[216:219], a[172:175], v[100:103]\n"
        "s_waitcnt vmcnt(3)\n"
        "ds_read_b64_tr_b16 v[220:221], v234 offset:12288\n"
        "v_mfma_f32_16x16x32_bf16 v[100:103], a[248:251], a[168:171], v[100:103]\n"
        "ds_read_b64_tr_b16 v[222:223], v234 offset:15360\n"
        "v_mfma_f32_16x16x32_bf16 v[80:83], v[152:155], v[196:199], v[32:35]\n"
        "s_waitcnt lgkmcnt(4)\n"
        "buffer_load_dwordx4 v242, s[8:11], s35 offen lds\n"
        "v_mfma_f32_16x16x32_bf16 v[80:83], v[168:171], v[200:203], v[80:83]\n"
        "ds_read_b64_tr_b16 v[224:225], v235 offset:12320\n"
        "ds_read_b64_tr_b16 v[226:227], v235 offset:15392\n"
        "v_mfma_f32_16x16x32_bf16 v[104:107], a[248:251], a[192:195], 0\n"
        "s_add_u32 m0, s41, 13312\n"
        "ds_read_b128 a[240:243], v241 offset:13824\n"
        "ds_read_b64_tr_b16 v[88:89], v234 offset:12352\n"
        "v_mfma_f32_16x16x32_bf16 v[104:107], a[216:219], a[196:199], v[104:107]\n"
        "ds_read_b128 a[244:247], v240 offset:13952\n"
        "ds_read_b64_tr_b16 v[90:91], v234 offset:15424\n"
        "v_mfma_f32_16x16x32_bf16 v[104:107], a[220:223], a[200:203], v[104:107]\n"
        "ds_read_b128 a[248:251], v241 offset:13952\n"
        "ds_read_b64_tr_b16 v[92:93], v235 offset:12384\n"
        "v_mfma_f32_16x16x32_bf16 v[104:107], a[224:227], a[204:207], v[104:107]\n"
        "ds_read_b128 a[216:219], v240 offset:14080\n"
        "ds_read_b64_tr_b16 v[94:95], v235 offset:15456\n"
        "v_mfma_f32_16x16x32_bf16 v[104:107], a[228:231], a[208:211], v[104:107]\n"
        "buffer_load_dwordx4 v243, s[8:11], s35 offen lds\n"
        "v_mfma_f32_16x16x32_bf16 v[104:107], a[232:235], a[212:215], v[104:107]\n"
        "s_add_u32 m0, s41, 14336\n"
        "ds_read_b128 a[220:223], v241 offset:14080\n"
        "ds_read_b64_tr_b16 v[120:121], v238 offset:12416\n"
        "v_mfma_f32_16x16x32_bf16 v[84:87], v[172:175], v[196:199], v[36:39]\n"
        "ds_read_b64_tr_b16 v[122:123], v238 offset:15488\n"
        "ds_read_b64_tr_b16 v[124:125], v239 offset:12448\n"
        "v_mfma_f32_16x16x32_bf16 v[84:87], v[188:191], v[204:207], v[84:87]\n"
        "s_waitcnt lgkmcnt(15)\n"
        "ds_read_b64_tr_b16 v[126:127], v239 offset:15520\n"
        "v_cvt_pk_bf16_f32 v140, v100, v101\n"
        "v_mfma_f32_16x16x32_bf16 v[96:99], a[72:75], a[236:239], 0\n"
        "s_waitcnt lgkmcnt(12)\n"
        "buffer_load_dwordx4 v244, s[8:11], s35 offen lds\n"
        "v_mfma_f32_16x16x32_bf16 v[96:99], a[76:79], a[240:243], v[96:99]\n"
        "s_waitcnt lgkmcnt(10)\n"
        "v_cvt_pk_bf16_f32 v141, v102, v103\n"
        "v_cvt_pk_bf16_f32 v148, v104, v105\n"
        "v_mfma_f32_16x16x32_bf16 v[96:99], a[80:83], a[244:247], v[96:99]\n"
        "s_waitcnt lgkmcnt(8)\n"
        "s_mov_b32 m0, s48\n"
        "v_cvt_pk_bf16_f32 v149, v106, v107\n"
        "v_mfma_f32_16x16x32_bf16 v[96:99], a[84:87], a[248:251], v[96:99]\n"
        "s_waitcnt lgkmcnt(6)\n"
        "v_exp_f32 v70, v70\n"
        "v_mfma_f32_16x16x32_bf16 v[96:99], a[88:91], a[216:219], v[96:99]\n"
        "s_waitcnt lgkmcnt(4)\n"
        "buffer_load_dwordx4 v249, s[12:15], s36 offen offset:0 lds\n"
        "v_mfma_f32_16x16x32_bf16 v[96:99], a[92:95], a[220:223], v[96:99]\n"
        "ds_read_b128 v[202:205], v252 offset:4096\n"
        "v_xor_b32 v116, 32, v249\n"
        "v_mfma_f32_16x16x32_bf16 v[100:103], a[116:119], a[220:223], 0\n"
        "ds_write_b128 v236, v[220:223] offset:12288\n"
        "v_mfma_f32_16x16x32_bf16 v[100:103], a[112:115], a[216:219], v[100:103]\n"
        "v_exp_f32 v71, v71\n"
        "v_mfma_f32_16x16x32_bf16 v[100:103], a[108:111], a[248:251], v[100:103]\n"
        "v_exp_f32 v76, v76\n"
        "v_mfma_f32_16x16x32_bf16 v[100:103], a[104:107], a[244:247], v[100:103]\n"
        "v_cvt_pk_bf16_f32 v212, v96, v97\n"
        "v_cvt_pk_bf16_f32 v213, v98, v99\n"
        "v_mfma_f32_16x16x32_bf16 v[100:103], a[100:103], a[240:243], v[100:103]\n"
        "buffer_load_dwordx4 v116, s[12:15], s36 offen offset:1024 lds\n"
        "v_mfma_f32_16x16x32_bf16 v[100:103], a[96:99], a[236:239], v[100:103]\n"
        "ds_write_b128 v236, v[224:227] offset:12672\n"
        "v_mfma_f32_16x16x32_bf16 v[104:107], a[120:123], a[236:239], 0\n"
        "v_exp_f32 v77, v77\n"
        "v_mfma_f32_16x16x32_bf16 v[104:107], a[124:127], a[240:243], v[104:107]\n"
        "v_exp_f32 v78, v78\n"
        "v_mfma_f32_16x16x32_bf16 v[104:107], a[128:131], a[244:247], v[104:107]\n"
        "v_exp_f32 v79, v79\n"
        "v_mfma_f32_16x16x32_bf16 v[104:107], a[132:135], a[248:251], v[104:107]\n"
        "v_cvt_pk_bf16_f32 v214, v100, v101\n"
        "v_cvt_pk_bf16_f32 v215, v102, v103\n"
        "v_mfma_f32_16x16x32_bf16 v[104:107], a[136:139], a[216:219], v[104:107]\n"
        "ds_write_b128 v236, v[88:91] offset:13056\n"
        "v_mfma_f32_16x16x32_bf16 v[104:107], a[140:143], a[220:223], v[104:107]\n"
        "v_cvt_pk_bf16_f32 v69, v70, v71\n"
        "v_cvt_pk_bf16_f32 v76, v76, v77\n"
        "v_mfma_f32_16x16x32_bf16 v[96:99], a[20:23], a[220:223], 0\n"
        "v_exp_f32 v80, v80\n"
        "v_mfma_f32_16x16x32_bf16 v[96:99], a[16:19], a[216:219], v[96:99]\n"
        "v_exp_f32 v81, v81\n"
        "v_mfma_f32_16x16x32_bf16 v[96:99], a[12:15], a[248:251], v[96:99]\n"
        "s_lshr_b32 s46, s56, 6\n"
        "v_cvt_pk_bf16_f32 v230, v104, v105\n"
        "v_cvt_pk_bf16_f32 v231, v106, v107\n"
        "v_mfma_f32_16x16x32_bf16 v[96:99], a[8:11], a[244:247], v[96:99]\n"
        "s_mul_i32 s46, s46, 1572864\n"
        "ds_write_b128 v236, v[92:95] offset:13440\n"
        "v_mfma_f32_16x16x32_bf16 v[96:99], a[4:7], a[240:243], v[96:99]\n"
        "s_add_u32 s46, s46, s37\n"
        "v_cvt_pk_bf16_f32 v77, v78, v79\n"
        "v_cvt_pk_bf16_f32 v80, v80, v81\n"
        "v_mfma_f32_16x16x32_bf16 v[96:99], a[0:3], a[236:239], v[96:99]\n"
        "s_cmp_eq_u32 s46, 100663296\n"
        "v_exp_f32 v82, v82\n"
        "v_mfma_f32_16x16x32_bf16 v[108:111], v[212:215], v[156:159], v[12:15]\n"
        "s_cselect_b32 s46, 0, s46\n"
        "v_exp_f32 v83, v83\n"
        "v_mfma_f32_16x16x32_bf16 v[108:111], v[228:231], v[164:167], v[108:111]\n"
        "s_and_b32 s43, s56, 63\n"
        "v_exp_f32 v84, v84\n"
        "v_mfma_f32_16x16x32_bf16 v[112:115], v[212:215], v[176:179], v[28:31]\n"
        "s_mul_i32 s43, s43, 3072\n"
        "v_mul_f32 v96, s50, v96\n"
        "v_mul_f32 v97, s50, v97\n"
        "v_mfma_f32_16x16x32_bf16 v[112:115], v[228:231], v[184:187], v[112:115]\n"
        "s_add_u32 s44, s43, 3072\n"
        "v_mul_f32 v98, s50, v98\n"
        "v_mul_f32 v99, s50, v99\n"
        "v_mfma_f32_16x16x32_bf16 v[116:119], v[212:215], v[196:199], v[44:47]\n"
        "s_waitcnt lgkmcnt(4)\n"
        "ds_write_b128 v237, v[120:123] offset:12288\n"
        "v_mfma_f32_16x16x32_bf16 v[116:119], v[228:231], v[204:207], v[116:119]\n"
        "s_cmp_eq_u32 s44, 196608\n"
        "v_cvt_pk_bf16_f32 v216, v96, v97\n"
        "v_cvt_pk_bf16_f32 v217, v98, v99\n"
        "v_mfma_f32_16x16x32_bf16 v[100:103], a[24:27], a[236:239], 0\n"
        "s_cselect_b32 s44, 0, s44\n"
        "v_cvt_pk_bf16_f32 v81, v82, v83\n"
        "v_add_u32 v240, s45, v240\n"
        "v_mfma_f32_16x16x32_bf16 v[100:103], a[28:31], a[240:243], v[100:103]\n"
        "s_add_u32 s39, s46, s44\n"
        "v_exp_f32 v85, v85\n"
        "v_mfma_f32_16x16x32_bf16 v[100:103], a[32:35], a[244:247], v[100:103]\n"
        "s_add_u32 s43, s43, s46\n"
        "v_exp_f32 v86, v86\n"
        "v_mfma_f32_16x16x32_bf16 v[100:103], a[36:39], a[248:251], v[100:103]\n"
        "s_add_u32 s38, s43, 1536\n"
        "ds_read_b128 v[222:225], v251 offset:4608\n"
        "v_cvt_pk_bf16_f32 v82, v84, v85\n"
        "v_mfma_f32_16x16x32_bf16 v[100:103], a[40:43], a[216:219], v[100:103]\n"
        "ds_write_b128 v237, v[124:127] offset:13056\n"
        "v_mfma_f32_16x16x32_bf16 v[100:103], a[44:47], a[220:223], v[100:103]\n"
        "v_exp_f32 v87, v87\n"
        "v_add_u32 v241, s45, v241\n"
        "v_cvt_pk_bf16_f32 v83, v86, v87\n"
        "v_mfma_f32_16x16x32_bf16 v[84:87], v[192:195], v[196:199], v[40:43]\n"
        "v_exp_f32 v108, v108\n"
        "v_mfma_f32_16x16x32_bf16 v[84:87], v[208:211], v[200:203], v[84:87]\n"
        "v_exp_f32 v109, v109\n"
        "v_mfma_f32_16x16x32_bf16 v[104:107], a[68:71], a[220:223], 0\n"
        "v_mul_f32 v100, s50, v100\n"
        "v_mul_f32 v101, s50, v101\n"
        "v_mfma_f32_16x16x32_bf16 v[104:107], a[64:67], a[216:219], v[104:107]\n"
        "v_mul_f32 v102, s50, v102\n"
        "v_mul_f32 v103, s50, v103\n"
        "v_mfma_f32_16x16x32_bf16 v[104:107], a[60:63], a[248:251], v[104:107]\n"
        "v_cvt_pk_bf16_f32 v218, v100, v101\n"
        "v_cvt_pk_bf16_f32 v219, v102, v103\n"
        "v_mfma_f32_16x16x32_bf16 v[104:107], a[56:59], a[244:247], v[104:107]\n"
        "s_waitcnt lgkmcnt(0)\n"
        "v_exp_f32 v110, v110\n"
        "v_mfma_f32_16x16x32_bf16 v[104:107], a[52:55], a[240:243], v[104:107]\n"
        "s_barrier\n"
        "v_exp_f32 v111, v111\n"
        "v_mfma_f32_16x16x32_bf16 v[104:107], a[48:51], a[236:239], v[104:107]\n"
        "v_exp_f32 v112, v112\n"
        "v_mfma_f32_16x16x32_bf16 v[96:99], a[236:239], a[144:147], 0\n"
        "v_exp_f32 v113, v113\n"
        "v_mfma_f32_16x16x32_bf16 v[96:99], a[240:243], a[148:151], v[96:99]\n"
        "v_exp_f32 v114, v114\n"
        "v_mfma_f32_16x16x32_bf16 v[96:99], a[244:247], a[152:155], v[96:99]\n"
        "v_exp_f32 v115, v115\n"
        "v_mfma_f32_16x16x32_bf16 v[96:99], a[248:251], a[156:159], v[96:99]\n"
        "v_mul_f32 v104, s50, v104\n"
        "v_mul_f32 v105, s50, v105\n"
        "v_mfma_f32_16x16x32_bf16 v[96:99], a[216:219], a[160:163], v[96:99]\n"
        "v_mul_f32 v106, s50, v106\n"
        "v_mul_f32 v107, s50, v107\n"
        "v_mfma_f32_16x16x32_bf16 v[96:99], a[220:223], a[164:167], v[96:99]\n"
        "v_cvt_pk_bf16_f32 v220, v104, v105\n"
        "v_cvt_pk_bf16_f32 v221, v106, v107\n"
        "v_mfma_f32_16x16x32_bf16 v[88:91], v[152:155], v[216:219], v[48:51]\n"
        "ds_read_b128 v[156:159], v247 offset:0\n"
        "v_mov_b64 v[226:227], v[220:221]\n"
        "v_mfma_f32_16x16x32_bf16 v[88:91], v[168:171], v[220:223], v[88:91]\n"
        "ds_read_b128 v[164:167], v247 offset:4096\n"
        "v_cvt_pk_bf16_f32 v70, v108, v109\n"
        "v_mfma_f32_16x16x32_bf16 v[92:95], v[172:175], v[216:219], v[52:55]\n"
        "s_mov_b32 s56, s57\n"
        "ds_read_b128 v[160:163], v247 offset:256\n"
        "v_cvt_pk_bf16_f32 v134, v96, v97\n"
        "v_mfma_f32_16x16x32_bf16 v[92:95], v[188:191], v[224:227], v[92:95]\n"
        "s_mov_b32 s57, s58\n"
        "ds_read_b128 v[222:225], v252 offset:4608\n"
        "v_cvt_pk_bf16_f32 v135, v98, v99\n"
        "v_mfma_f32_16x16x32_bf16 v[100:103], a[220:223], a[188:191], 0\n"
        "s_xor_b32 s48, s48, 8192\n"
        "ds_read_b128 v[152:155], v247 offset:4352\n"
        "v_cvt_pk_bf16_f32 v71, v110, v111\n"
        "v_mfma_f32_16x16x32_bf16 v[100:103], a[216:219], a[184:187], v[100:103]\n"
        "v_exp_f32 v116, v116\n"
        "v_mfma_f32_16x16x32_bf16 v[100:103], a[248:251], a[180:183], v[100:103]\n"
        "v_exp_f32 v117, v117\n"
        "v_mfma_f32_16x16x32_bf16 v[100:103], a[244:247], a[176:179], v[100:103]\n"
        "v_exp_f32 v118, v118\n"
        "v_mfma_f32_16x16x32_bf16 v[100:103], a[240:243], a[172:175], v[100:103]\n"
        "v_exp_f32 v119, v119\n"
        "v_mfma_f32_16x16x32_bf16 v[100:103], a[236:239], a[168:171], v[100:103]\n"
        "v_exp_f32 v84, v84\n"
        "v_mfma_f32_16x16x32_bf16 v[104:107], a[236:239], a[192:195], 0\n"
        "ds_read_b128 a[236:239], v248 offset:0\n"
        "v_cvt_pk_bf16_f32 v78, v112, v113\n"
        "v_mfma_f32_16x16x32_bf16 v[104:107], a[240:243], a[196:199], v[104:107]\n"
        "ds_read_b128 a[240:243], v248 offset:6144\n"
        "v_cvt_pk_bf16_f32 v79, v114, v115\n"
        "v_mfma_f32_16x16x32_bf16 v[104:107], a[244:247], a[200:203], v[104:107]\n"
        "ds_read_b128 a[244:247], v254 offset:16\n"
        "v_cvt_pk_bf16_f32 v142, v100, v101\n"
        "v_mfma_f32_16x16x32_bf16 v[104:107], a[248:251], a[204:207], v[104:107]\n"
        "ds_read_b128 a[248:251], v254 offset:6160\n"
        "v_cvt_pk_bf16_f32 v143, v102, v103\n"
        "v_mfma_f32_16x16x32_bf16 v[104:107], a[216:219], a[208:211], v[104:107]\n"
        "v_exp_f32 v85, v85\n"
        "v_mfma_f32_16x16x32_bf16 v[104:107], a[220:223], a[212:215], v[104:107]\n"
        "v_exp_f32 v86, v86\n"
        "v_mfma_f32_16x16x32_bf16 v[108:111], v[192:195], v[216:219], v[56:59]\n"
        "s_waitcnt lgkmcnt(5)\n"
        "ds_read_b128 a[216:219], v248 offset:32\n"
        "v_cvt_pk_bf16_f32 v84, v84, v85\n"
        "v_mfma_f32_16x16x32_bf16 v[108:111], v[208:211], v[220:223], v[108:111]\n"
        "v_exp_f32 v87, v87\n"
        "v_mfma_f32_16x16x32_bf16 v[112:115], v[212:215], v[216:219], v[60:63]\n"
        "ds_read_b128 a[220:223], v248 offset:6176\n"
        "v_cvt_pk_bf16_f32 v150, v104, v105\n"
        "v_mfma_f32_16x16x32_bf16 v[112:115], v[228:231], v[224:227], v[112:115]\n"
        "s_waitcnt lgkmcnt(5)\n"
        "ds_read_b128 a[224:227], v254 offset:48\n"
        "v_cvt_pk_bf16_f32 v151, v106, v107\n"
        "v_mfma_f32_16x16x32_bf16 v[172:175], v[156:159], a[236:239], 0\n"
        "s_waitcnt lgkmcnt(5)\n"
        "ds_read_b128 a[228:231], v254 offset:6192\n"
        "v_cvt_pk_bf16_f32 v85, v86, v87\n"
        "v_mfma_f32_16x16x32_bf16 v[172:175], v[164:167], a[240:243], v[172:175]\n"
        "v_cvt_pk_bf16_f32 v86, v116, v117\n"
        "v_mov_b64 v[116:117], 0\n"
        "v_mfma_f32_16x16x32_bf16 v[200:203], v[152:155], a[240:243], 0\n"
        "ds_read_b128 a[232:235], v248 offset:64\n"
        "v_cvt_pk_bf16_f32 v87, v118, v119\n"
        "v_mfma_f32_16x16x32_bf16 v[200:203], v[160:163], a[236:239], v[200:203]\n"
        "v_exp_f32 v88, v88\n"
        "v_smfmac_f32_16x16x64_bf16 v[116:119], a[252:255], v[64:71], v255\n"
        "v_exp_f32 v89, v89\n"
        "v_mfma_f32_16x16x32_bf16 v[120:123], v[128:131], v[64:67], 0\n"
        "v_exp_f32 v90, v90\n"
        "v_mfma_f32_16x16x32_bf16 v[120:123], v[132:135], v[68:71], v[120:123]\n"
        "ds_read_b128 a[236:239], v248 offset:6208\n"
        "v_cvt_pk_bf16_f32 v88, v88, v89\n"
        "v_mfma_f32_16x16x32_bf16 v[124:127], v[140:143], v[68:71], 0\n"
        "v_exp_f32 v91, v91\n"
        "v_mfma_f32_16x16x32_bf16 v[124:127], v[136:139], v[64:67], v[124:127]\n"
        "v_cvt_pk_bf16_f32 v89, v90, v91\n"
        "v_add_f32 v116, v116, v117\n"
        "v_mfma_f32_16x16x32_bf16 v[96:99], v[144:147], v[64:67], 0\n"
        "v_exp_f32 v92, v92\n"
        "v_mfma_f32_16x16x32_bf16 v[96:99], v[148:151], v[68:71], v[96:99]\n"
        "s_waitcnt lgkmcnt(7)\n"
        "v_exp_f32 v93, v93\n"
        "v_mfma_f32_16x16x32_bf16 v[204:207], v[160:163], a[244:247], 0\n"
        "s_waitcnt lgkmcnt(6)\n"
        "v_exp_f32 v94, v94\n"
        "v_mfma_f32_16x16x32_bf16 v[204:207], v[152:155], a[248:251], v[204:207]\n"
        "ds_read_b128 a[240:243], v254 offset:80\n"
        "v_cvt_pk_bf16_f32 v90, v92, v93\n"
        "v_mfma_f32_16x16x32_bf16 v[176:179], v[164:167], a[248:251], 0\n"
        "v_exp_f32 v95, v95\n"
        "v_mfma_f32_16x16x32_bf16 v[176:179], v[156:159], a[244:247], v[176:179]\n"
        "s_waitcnt lgkmcnt(6)\n"
        "ds_read_b128 a[244:247], v254 offset:6224\n"
        "v_cvt_pk_bf16_f32 v91, v94, v95\n"
        "v_mfma_f32_16x16x32_bf16 v[180:183], v[156:159], a[216:219], 0\n"
        "s_waitcnt lgkmcnt(6)\n"
        "ds_read_b128 a[248:251], v248 offset:36864\n"
        "v_mov_b64 v[100:101], 0\n"
        "v_mfma_f32_16x16x32_bf16 v[180:183], v[164:167], a[220:223], v[180:183]\n"
        "v_exp_f32 v108, v108\n"
        "v_mfma_f32_16x16x32_bf16 v[212:215], v[152:155], a[220:223], 0\n"
        "v_exp_f32 v109, v109\n"
        "v_mfma_f32_16x16x32_bf16 v[212:215], v[160:163], a[216:219], v[212:215]\n"
        "s_waitcnt lgkmcnt(6)\n"
        "v_exp_f32 v110, v110\n"
        "v_mfma_f32_16x16x32_bf16 v[216:219], v[160:163], a[224:227], 0\n"
        "s_waitcnt lgkmcnt(5)\n"
        "v_rcp_f32 v117, v116\n"
        "v_mfma_f32_16x16x32_bf16 v[216:219], v[152:155], a[228:231], v[216:219]\n"
        "v_cvt_pk_bf16_f32 v92, v108, v109\n"
        "v_mul_f32 v120, v117, v120\n"
        "v_mfma_f32_16x16x32_bf16 v[184:187], v[164:167], a[228:231], 0\n"
        "v_exp_f32 v111, v111\n"
        "v_mfma_f32_16x16x32_bf16 v[184:187], v[156:159], a[224:227], v[184:187]\n"
        "s_waitcnt lgkmcnt(4)\n"
        "v_mul_f32 v121, v117, v121\n"
        "v_mul_f32 v122, v117, v122\n"
        "v_mfma_f32_16x16x32_bf16 v[192:195], v[156:159], a[232:235], 0\n"
        "s_waitcnt lgkmcnt(3)\n"
        "v_mul_f32 v123, v117, v123\n"
        "v_cvt_pk_bf16_f32 v93, v110, v111\n"
        "v_mfma_f32_16x16x32_bf16 v[192:195], v[164:167], a[236:239], v[192:195]\n"
        "v_exp_f32 v112, v112\n"
        "v_mfma_f32_16x16x32_bf16 v[220:223], v[152:155], a[236:239], 0\n"
        "v_cvt_pk_bf16_f32 v120, v120, v121\n"
        "v_cvt_pk_bf16_f32 v121, v122, v123\n"
        "v_mfma_f32_16x16x32_bf16 v[220:223], v[160:163], a[232:235], v[220:223]\n"
        "ds_read_b128 v[232:235], v247 offset:8192\n"
        "v_mul_f32 v124, v117, v124\n"
        "v_smfmac_f32_16x16x64_bf16 v[100:103], a[252:255], v[72:79], v255\n"
        "43:\n"
        "s_cbranch_execz 46f\n"
        "ds_read_b128 v[64:67], v247 offset:12288\n"
        "v_mul_f32 v125, v117, v125\n"
        "v_mfma_f32_16x16x32_bf16 v[104:107], v[128:131], v[72:75], 0\n"
        "ds_read_b128 v[236:239], v247 offset:8448\n"
        "v_maximum3_f32 v253, v253, v116, v117\n"
        "v_mfma_f32_16x16x32_bf16 v[104:107], v[132:135], v[76:79], v[104:107]\n"
        "ds_read_b128 v[68:71], v247 offset:12544\n"
        "ds_read_b128 a[216:219], v248 offset:43008\n"
        "v_mfma_f32_16x16x32_bf16 v[108:111], v[140:143], v[76:79], 0\n"
        "v_exp_f32 v113, v113\n"
        "v_mfma_f32_16x16x32_bf16 v[108:111], v[136:139], v[72:75], v[108:111]\n"
        "ds_write_b32 v245, v120 offset:0\n"
        "v_exp_f32 v114, v114\n"
        "v_cvt_pk_bf16_f32 v94, v112, v113\n"
        "v_exp_f32 v115, v115\n"
        "v_mul_f32 v126, v117, v126\n"
        "v_cvt_pk_bf16_f32 v95, v114, v115\n"
        "v_mfma_f32_16x16x32_bf16 v[112:115], v[144:147], v[72:75], 0\n"
        "ds_read_b128 a[220:223], v254 offset:36880\n"
        "v_mul_f32 v127, v117, v127\n"
        "v_mfma_f32_16x16x32_bf16 v[112:115], v[148:151], v[76:79], v[112:115]\n"
        "s_waitcnt lgkmcnt(9)\n"
        "ds_read_b128 a[224:227], v254 offset:43024\n"
        "v_cvt_pk_bf16_f32 v124, v124, v125\n"
        "v_mfma_f32_16x16x32_bf16 v[224:227], v[160:163], a[240:243], 0\n"
        "s_waitcnt lgkmcnt(9)\n"
        "ds_write_b32 v245, v121 offset:4\n"
        "v_mfma_f32_16x16x32_bf16 v[224:227], v[152:155], a[244:247], v[224:227]\n"
        "v_cvt_pk_bf16_f32 v125, v126, v127\n"
        "v_mul_f32 v96, v117, v96\n"
        "v_mfma_f32_16x16x32_bf16 v[196:199], v[164:167], a[244:247], 0\n"
        "ds_read_b128 a[228:231], v248 offset:36896\n"
        "v_mul_f32 v97, v117, v97\n"
        "v_mfma_f32_16x16x32_bf16 v[196:199], v[156:159], a[240:243], v[196:199]\n"
        "s_waitcnt lgkmcnt(9)\n"
        "ds_read_b128 a[232:235], v248 offset:43040\n"
        "v_mul_f32 v98, v117, v98\n"
        "v_mfma_f32_16x16x32_bf16 v[172:175], v[232:235], a[248:251], v[172:175]\n"
        "s_waitcnt lgkmcnt(6)\n"
        "ds_write_b32 v245, v124 offset:8\n"
        "v_mfma_f32_16x16x32_bf16 v[172:175], v[64:67], a[216:219], v[172:175]\n"
        "ds_write_b32 v245, v125 offset:12\n"
        "v_mfma_f32_16x16x32_bf16 v[200:203], v[68:71], a[216:219], v[200:203]\n"
        "ds_read_b128 a[236:239], v254 offset:36912\n"
        "v_mul_f32 v99, v117, v99\n"
        "v_mfma_f32_16x16x32_bf16 v[200:203], v[236:239], a[248:251], v[200:203]\n"
        "s_waitcnt lgkmcnt(7)\n"
        "ds_read_b128 a[240:243], v254 offset:43056\n"
        "v_cvt_pk_bf16_f32 v96, v96, v97\n"
        "v_mfma_f32_16x16x32_bf16 v[204:207], v[236:239], a[220:223], v[204:207]\n"
        "s_waitcnt lgkmcnt(7)\n"
        "v_cvt_pk_bf16_f32 v97, v98, v99\n"
        "v_add_f32 v100, v100, v101\n"
        "v_mfma_f32_16x16x32_bf16 v[204:207], v[68:71], a[224:227], v[204:207]\n"
        "ds_write_b32 v246, v96 offset:0\n"
        "v_mfma_f32_16x16x32_bf16 v[176:179], v[64:67], a[224:227], v[176:179]\n"
        "ds_write_b32 v246, v97 offset:4\n"
        "v_mfma_f32_16x16x32_bf16 v[176:179], v[232:235], a[220:223], v[176:179]\n"
        "s_waitcnt lgkmcnt(7)\n"
        "v_rcp_f32 v101, v100\n"
        "v_mfma_f32_16x16x32_bf16 v[180:183], v[232:235], a[228:231], v[180:183]\n"
        "s_waitcnt lgkmcnt(6)\n"
        "v_mul_f32 v104, v101, v104\n"
        "v_mul_f32 v105, v101, v105\n"
        "v_mfma_f32_16x16x32_bf16 v[180:183], v[64:67], a[232:235], v[180:183]\n"
        "v_mov_b64 v[116:117], 0\n"
        "v_mul_f32 v106, v101, v106\n"
        "v_mfma_f32_16x16x32_bf16 v[212:215], v[68:71], a[232:235], v[212:215]\n"
        "v_mul_f32 v107, v101, v107\n"
        "v_cvt_pk_bf16_f32 v104, v104, v105\n"
        "v_mfma_f32_16x16x32_bf16 v[212:215], v[236:239], a[228:231], v[212:215]\n"
        "s_waitcnt lgkmcnt(3)\n"
        "v_cvt_pk_bf16_f32 v105, v106, v107\n"
        "v_mul_f32 v108, v101, v108\n"
        "v_mfma_f32_16x16x32_bf16 v[216:219], v[236:239], a[236:239], v[216:219]\n"
        "s_waitcnt lgkmcnt(2)\n"
        "ds_write_b32 v245, v104 offset:256\n"
        "v_mfma_f32_16x16x32_bf16 v[216:219], v[68:71], a[240:243], v[216:219]\n"
        "ds_read_b128 a[244:247], v248 offset:36928\n"
        "v_mul_f32 v109, v101, v109\n"
        "v_mfma_f32_16x16x32_bf16 v[184:187], v[64:67], a[240:243], v[184:187]\n"
        "ds_read_b128 a[248:251], v248 offset:43072\n"
        "v_maximum3_f32 v253, v253, v100, v101\n"
        "v_mfma_f32_16x16x32_bf16 v[184:187], v[232:235], a[236:239], v[184:187]\n"
        "ds_write_b32 v245, v105 offset:260\n"
        "v_smfmac_f32_16x16x64_bf16 v[116:119], a[252:255], v[80:87], v255\n"
        "v_mul_f32 v110, v101, v110\n"
        "v_mul_f32 v111, v101, v111\n"
        "v_mfma_f32_16x16x32_bf16 v[120:123], v[128:131], v[80:83], 0\n"
        "ds_read_b128 a[216:219], v254 offset:36944\n"
        "v_cvt_pk_bf16_f32 v108, v108, v109\n"
        "v_mfma_f32_16x16x32_bf16 v[120:123], v[132:135], v[84:87], v[120:123]\n"
        "ds_read_b128 a[220:223], v254 offset:43088\n"
        "v_cvt_pk_bf16_f32 v109, v110, v111\n"
        "v_mfma_f32_16x16x32_bf16 v[124:127], v[140:143], v[84:87], 0\n"
        "ds_read_b128 v[156:159], v247 offset:16384\n"
        "v_mul_f32 v112, v101, v112\n"
        "v_mfma_f32_16x16x32_bf16 v[124:127], v[136:139], v[80:83], v[124:127]\n"
        "ds_read_b128 v[164:167], v247 offset:20480\n"
        "v_mul_f32 v113, v101, v113\n"
        "v_mfma_f32_16x16x32_bf16 v[96:99], v[144:147], v[80:83], 0\n"
        "ds_read_b128 v[160:163], v247 offset:16640\n"
        "ds_read_b128 a[224:227], v248 offset:12288\n"
        "v_mfma_f32_16x16x32_bf16 v[96:99], v[148:151], v[84:87], v[96:99]\n"
        "s_waitcnt lgkmcnt(8)\n"
        "ds_read_b128 v[152:155], v247 offset:20736\n"
        "ds_read_b128 a[228:231], v248 offset:49152\n"
        "v_mfma_f32_16x16x32_bf16 v[192:195], v[232:235], a[244:247], v[192:195]\n"
        "s_waitcnt lgkmcnt(9)\n"
        "ds_write_b32 v245, v108 offset:264\n"
        "v_mfma_f32_16x16x32_bf16 v[192:195], v[64:67], a[248:251], v[192:195]\n"
        "ds_write_b32 v245, v109 offset:268\n"
        "v_mfma_f32_16x16x32_bf16 v[220:223], v[68:71], a[248:251], v[220:223]\n"
        "ds_read_b128 a[232:235], v254 offset:12304\n"
        "v_mul_f32 v114, v101, v114\n"
        "v_mfma_f32_16x16x32_bf16 v[220:223], v[236:239], a[244:247], v[220:223]\n"
        "s_waitcnt lgkmcnt(10)\n"
        "ds_read_b128 a[236:239], v254 offset:49168\n"
        "v_mul_f32 v115, v101, v115\n"
        "v_mfma_f32_16x16x32_bf16 v[224:227], v[236:239], a[216:219], v[224:227]\n"
        "s_waitcnt lgkmcnt(10)\n"
        "ds_read_b128 a[240:243], v248 offset:12320\n"
        "v_cvt_pk_bf16_f32 v112, v112, v113\n"
        "v_mfma_f32_16x16x32_bf16 v[224:227], v[68:71], a[220:223], v[224:227]\n"
        "v_cvt_pk_bf16_f32 v113, v114, v115\n"
        "v_add_f32 v116, v116, v117\n"
        "v_mfma_f32_16x16x32_bf16 v[196:199], v[64:67], a[220:223], v[196:199]\n"
        "ds_write_b32 v246, v112 offset:256\n"
        "v_mfma_f32_16x16x32_bf16 v[196:199], v[232:235], a[216:219], v[196:199]\n"
        "s_waitcnt lgkmcnt(8)\n"
        "ds_read_b128 a[244:247], v248 offset:49184\n"
        "v_add_u32 v247, s31, v247\n"
        "v_mfma_f32_16x16x32_bf16 v[172:175], v[156:159], a[224:227], v[172:175]\n"
        "s_waitcnt lgkmcnt(7)\n"
        "ds_write_b32 v246, v113 offset:260\n"
        "v_mfma_f32_16x16x32_bf16 v[172:175], v[164:167], a[228:231], v[172:175]\n"
        "v_rcp_f32 v117, v116\n"
        "v_mfma_f32_16x16x32_bf16 v[200:203], v[152:155], a[228:231], v[200:203]\n"
        "ds_read_b128 a[248:251], v254 offset:12336\n"
        "v_mul_f32 v120, v117, v120\n"
        "v_mfma_f32_16x16x32_bf16 v[200:203], v[160:163], a[224:227], v[200:203]\n"
        "s_waitcnt lgkmcnt(6)\n"
        "ds_read_b128 a[216:219], v254 offset:49200\n"
        "v_mul_f32 v121, v117, v121\n"
        "v_mfma_f32_16x16x32_bf16 v[204:207], v[160:163], a[232:235], v[204:207]\n"
        "s_waitcnt lgkmcnt(6)\n"
        "v_mul_f32 v122, v117, v122\n"
        "v_mul_f32 v123, v117, v123\n"
        "v_mfma_f32_16x16x32_bf16 v[204:207], v[152:155], a[236:239], v[204:207]\n"
        "v_cvt_pk_bf16_f32 v120, v120, v121\n"
        "v_cvt_pk_bf16_f32 v121, v122, v123\n"
        "v_mfma_f32_16x16x32_bf16 v[176:179], v[164:167], a[236:239], v[176:179]\n"
        "ds_read_b128 a[220:223], v248 offset:12352\n"
        "v_mul_f32 v124, v117, v124\n"
        "v_mfma_f32_16x16x32_bf16 v[176:179], v[156:159], a[232:235], v[176:179]\n"
        "s_waitcnt lgkmcnt(6)\n"
        "ds_read_b128 a[224:227], v248 offset:49216\n"
        "v_mul_f32 v125, v117, v125\n"
        "v_mfma_f32_16x16x32_bf16 v[180:183], v[156:159], a[240:243], v[180:183]\n"
        "s_waitcnt lgkmcnt(5)\n"
        "ds_write_b32 v245, v120 offset:512\n"
        "v_mfma_f32_16x16x32_bf16 v[180:183], v[164:167], a[244:247], v[180:183]\n"
        "ds_write_b32 v245, v121 offset:516\n"
        "v_mfma_f32_16x16x32_bf16 v[212:215], v[152:155], a[244:247], v[212:215]\n"
        "ds_read_b128 a[228:231], v254 offset:12368\n"
        "v_maximum3_f32 v253, v253, v116, v117\n"
        "v_mfma_f32_16x16x32_bf16 v[212:215], v[160:163], a[240:243], v[212:215]\n"
        "s_waitcnt lgkmcnt(6)\n"
        "ds_read_b128 a[232:235], v254 offset:49232\n"
        "v_mul_f32 v126, v117, v126\n"
        "v_mfma_f32_16x16x32_bf16 v[216:219], v[160:163], a[248:251], v[216:219]\n"
        "s_waitcnt lgkmcnt(6)\n"
        "v_mul_f32 v127, v117, v127\n"
        "v_cvt_pk_bf16_f32 v124, v124, v125\n"
        "v_mfma_f32_16x16x32_bf16 v[216:219], v[152:155], a[216:219], v[216:219]\n"
        "v_cvt_pk_bf16_f32 v125, v126, v127\n"
        "v_mul_f32 v96, v117, v96\n"
        "v_mfma_f32_16x16x32_bf16 v[184:187], v[164:167], a[216:219], v[184:187]\n"
        "s_waitcnt vmcnt(0) lgkmcnt(5)\n"
        "ds_write_b32 v245, v124 offset:520\n"
        "v_mfma_f32_16x16x32_bf16 v[184:187], v[156:159], a[248:251], v[184:187]\n"
        "s_barrier\n"
        "ds_write_b32 v245, v125 offset:524\n"
        "v_mfma_f32_16x16x32_bf16 v[192:195], v[156:159], a[220:223], v[192:195]\n"
        "s_waitcnt lgkmcnt(6)\n"
        "v_mul_f32 v97, v117, v97\n"
        "v_mul_f32 v98, v117, v98\n"
        "v_mfma_f32_16x16x32_bf16 v[192:195], v[164:167], a[224:227], v[192:195]\n"
        "v_mul_f32 v99, v117, v99\n"
        "v_cvt_pk_bf16_f32 v96, v96, v97\n"
        "v_mfma_f32_16x16x32_bf16 v[220:223], v[152:155], a[224:227], v[220:223]\n"
        "v_cvt_pk_bf16_f32 v97, v98, v99\n"
        "v_mov_b64 v[80:81], 0\n"
        "v_mfma_f32_16x16x32_bf16 v[220:223], v[160:163], a[220:223], v[220:223]\n"
        "s_waitcnt lgkmcnt(3)\n"
        "ds_write_b32 v246, v96 offset:512\n"
        "v_mfma_f32_16x16x32_bf16 v[224:227], v[160:163], a[228:231], v[224:227]\n"
        "s_waitcnt lgkmcnt(3)\n"
        "ds_write_b32 v246, v97 offset:516\n"
        "v_mfma_f32_16x16x32_bf16 v[224:227], v[152:155], a[232:235], v[224:227]\n"
        "ds_read_b128 a[236:239], v240 offset:0\n"
        "v_mfma_f32_16x16x32_bf16 v[196:199], v[164:167], a[232:235], v[196:199]\n"
        "ds_read_b128 a[240:243], v241 offset:0\n"
        "v_mfma_f32_16x16x32_bf16 v[196:199], v[156:159], a[228:231], v[196:199]\n"
        "ds_read_b128 a[244:247], v240 offset:128\n"
        "v_smfmac_f32_16x16x64_bf16 v[80:83], a[252:255], v[88:95], v255\n"
        "ds_read_b128 a[248:251], v241 offset:128\n"
        "v_mfma_f32_16x16x32_bf16 v[84:87], v[128:131], v[88:91], 0\n"
        "s_min_u32 s58, s63, s59\n"
        "ds_read_b128 a[216:219], v240 offset:256\n"
        "v_mfma_f32_16x16x32_bf16 v[84:87], v[132:135], v[92:95], v[84:87]\n"
        "ds_read_b128 a[220:223], v241 offset:256\n"
        "v_mfma_f32_16x16x32_bf16 v[232:235], v[140:143], v[92:95], 0\n"
        "v_mfma_f32_16x16x32_bf16 v[232:235], v[136:139], v[88:91], v[232:235]\n"
        "v_mfma_f32_16x16x32_bf16 v[236:239], v[144:147], v[88:91], 0\n"
        "v_mfma_f32_16x16x32_bf16 v[236:239], v[148:151], v[92:95], v[236:239]\n"
        "s_waitcnt lgkmcnt(0)\n"
        "s_branch 11f\n"
        ".p2align 6\n"
        "10:\n"
        "v_mfma_f32_16x16x32_bf16 v[232:235], v[136:139], v[88:91], v[232:235]\n"
        "s_cmp_ge_u32 s58, 3072\n"
        "v_mov_b32 v120, 1\n"
        "v_mfma_f32_16x16x32_bf16 v[236:239], v[144:147], v[88:91], 0\n"
        "s_cselect_b32 s6, s3, 0\n"
        "v_mfma_f32_16x16x32_bf16 v[236:239], v[148:151], v[92:95], v[236:239]\n"
        "s_waitcnt lgkmcnt(5)\n"
        "buffer_atomic_add v120, v249, s[4:7], 0 offen sc0 sc1\n"
        "11:\n"
        "v_mfma_f32_16x16x32_bf16 v[96:99], a[72:75], a[236:239], 0\n"
        "s_waitcnt lgkmcnt(4)\n"
        "v_and_b32 v117, 60, v250\n"
        "v_add3_u32 v117, v250, v117, s26\n"
        "v_mfma_f32_16x16x32_bf16 v[96:99], a[76:79], a[240:243], v[96:99]\n"
        "s_waitcnt lgkmcnt(3)\n"
        "v_cvt_pk_bf16_f32 v108, v172, v176\n"
        "v_cvt_pk_bf16_f32 v109, v180, v184\n"
        "v_mfma_f32_16x16x32_bf16 v[96:99], a[80:83], a[244:247], v[96:99]\n"
        "s_waitcnt lgkmcnt(2)\n"
        "v_cvt_pk_bf16_f32 v110, v192, v196\n"
        "v_add_f32 v80, v80, v81\n"
        "v_mfma_f32_16x16x32_bf16 v[96:99], a[84:87], a[248:251], v[96:99]\n"
        "s_waitcnt lgkmcnt(1)\n"
        "buffer_store_dwordx2 v[108:109], v117, s[16:19], s38 offen offset:0 sc1\n"
        "v_mfma_f32_16x16x32_bf16 v[96:99], a[88:91], a[216:219], v[96:99]\n"
        "s_waitcnt lgkmcnt(0)\n"
        "v_rcp_f32 v81, v80\n"
        "v_mfma_f32_16x16x32_bf16 v[96:99], a[92:95], a[220:223], v[96:99]\n"
        "s_add_u32 s41, s29, s40\n"
        "v_cvt_pk_bf16_f32 v112, v173, v177\n"
        "v_cvt_pk_bf16_f32 v113, v181, v185\n"
        "v_mfma_f32_16x16x32_bf16 v[100:103], a[116:119], a[220:223], 0\n"
        "s_lshr_b32 s47, s58, 6\n"
        "v_mul_f32 v84, v81, v84\n"
        "v_mul_f32 v85, v81, v85\n"
        "v_mfma_f32_16x16x32_bf16 v[100:103], a[112:115], a[216:219], v[100:103]\n"
        "s_mul_i32 s47, s47, 1572864\n"
        "v_cvt_pk_bf16_f32 v152, v96, v97\n"
        "v_cvt_pk_bf16_f32 v153, v98, v99\n"
        "v_mfma_f32_16x16x32_bf16 v[100:103], a[108:111], a[248:251], v[100:103]\n"
        "buffer_store_dword v110, v250, s[16:19], s38 offen offset:0 sc1\n"
        "v_mfma_f32_16x16x32_bf16 v[100:103], a[104:107], a[244:247], v[100:103]\n"
        "s_add_u32 s47, s47, s24\n"
        "v_cvt_pk_bf16_f32 v114, v193, v197\n"
        "v_mul_f32 v86, v81, v86\n"
        "v_mfma_f32_16x16x32_bf16 v[100:103], a[100:103], a[240:243], v[100:103]\n"
        "s_sub_u32 s31, s28, s30\n"
        "v_mul_f32 v87, v81, v87\n"
        "v_cvt_pk_bf16_f32 v84, v84, v85\n"
        "v_mfma_f32_16x16x32_bf16 v[100:103], a[96:99], a[236:239], v[100:103]\n"
        "s_sub_u32 s45, s29, s28\n"
        "ds_write_b32 v245, v84 offset:768\n"
        "v_mfma_f32_16x16x32_bf16 v[104:107], a[120:123], a[236:239], 0\n"
        "s_mov_b32 s29, s30\n"
        "v_cvt_pk_bf16_f32 v85, v86, v87\n"
        "v_mul_f32 v232, v81, v232\n"
        "v_mfma_f32_16x16x32_bf16 v[104:107], a[124:127], a[240:243], v[104:107]\n"
        "buffer_store_dwordx2 v[112:113], v117, s[16:19], s38 offen offset:384 sc1\n"
        "v_mfma_f32_16x16x32_bf16 v[104:107], a[128:131], a[244:247], v[104:107]\n"
        "s_mov_b32 s30, s28\n"
        "v_cvt_pk_bf16_f32 v154, v100, v101\n"
        "v_cvt_pk_bf16_f32 v155, v102, v103\n"
        "v_mfma_f32_16x16x32_bf16 v[104:107], a[132:135], a[248:251], v[104:107]\n"
        "s_add_u32 s28, s28, s45\n"
        "ds_write_b32 v245, v85 offset:772\n"
        "v_mfma_f32_16x16x32_bf16 v[104:107], a[136:139], a[216:219], v[104:107]\n"
        "s_lshl_b32 s36, s58, 13\n"
        "v_cvt_pk_bf16_f32 v108, v174, v178\n"
        "v_cvt_pk_bf16_f32 v109, v182, v186\n"
        "v_mfma_f32_16x16x32_bf16 v[104:107], a[140:143], a[220:223], v[104:107]\n"
        "s_add_u32 s36, s36, s34\n"
        "v_mul_f32 v233, v81, v233\n"
        "v_maximum3_f32 v253, v253, v80, v81\n"
        "v_mfma_f32_16x16x32_bf16 v[96:99], a[20:23], a[220:223], 0\n"
        "buffer_store_dword v114, v250, s[16:19], s38 offen offset:384 sc1\n"
        "v_mfma_f32_16x16x32_bf16 v[96:99], a[16:19], a[216:219], v[96:99]\n"
        "s_sub_u32 s62, s58, s57\n"
        "v_cvt_pk_bf16_f32 v110, v194, v198\n"
        "v_mul_f32 v234, v81, v234\n"
        "v_mfma_f32_16x16x32_bf16 v[96:99], a[12:15], a[248:251], v[96:99]\n"
        "s_and_b32 s62, s62, 63\n"
        "v_cvt_pk_bf16_f32 v168, v104, v105\n"
        "v_cvt_pk_bf16_f32 v169, v106, v107\n"
        "v_mfma_f32_16x16x32_bf16 v[96:99], a[8:11], a[244:247], v[96:99]\n"
        "s_mul_i32 s62, s62, 3072\n"
        "v_mul_f32 v235, v81, v235\n"
        "v_cvt_pk_bf16_f32 v232, v232, v233\n"
        "v_mfma_f32_16x16x32_bf16 v[96:99], a[4:7], a[240:243], v[96:99]\n"
        "s_add_u32 s35, s47, 786432\n"
        "ds_write_b32 v245, v232 offset:776\n"
        "v_mfma_f32_16x16x32_bf16 v[96:99], a[0:3], a[236:239], v[96:99]\n"
        "buffer_store_dwordx2 v[108:109], v117, s[16:19], s38 offen offset:768 sc1\n"
        "v_mfma_f32_16x16x32_bf16 v[100:103], a[24:27], a[236:239], 0\n"
        "s_sub_u32 s46, s35, 100663296\n"
        "v_cvt_pk_bf16_f32 v112, v175, v179\n"
        "v_cvt_pk_bf16_f32 v113, v183, v187\n"
        "v_mfma_f32_16x16x32_bf16 v[100:103], a[28:31], a[240:243], v[100:103]\n"
        "s_cmp_lt_u32 s46, 786432\n"
        "v_cvt_pk_bf16_f32 v233, v234, v235\n"
        "v_xor_b32 v251, 0x2000, v251\n"
        "v_mfma_f32_16x16x32_bf16 v[100:103], a[32:35], a[244:247], v[100:103]\n"
        "s_cselect_b32 s35, s46, s35\n"
        "v_mul_f32 v96, s50, v96\n"
        "v_mul_f32 v97, s50, v97\n"
        "v_mfma_f32_16x16x32_bf16 v[100:103], a[36:39], a[248:251], v[100:103]\n"
        "v_mul_f32 v98, s50, v98\n"
        "v_mul_f32 v99, s50, v99\n"
        "v_mfma_f32_16x16x32_bf16 v[100:103], a[40:43], a[216:219], v[100:103]\n"
        "v_cvt_pk_bf16_f32 v156, v96, v97\n"
        "v_cvt_pk_bf16_f32 v157, v98, v99\n"
        "v_mfma_f32_16x16x32_bf16 v[100:103], a[44:47], a[220:223], v[100:103]\n"
        "buffer_store_dword v110, v250, s[16:19], s38 offen offset:768 sc1\n"
        "v_mfma_f32_16x16x32_bf16 v[104:107], a[68:71], a[220:223], 0\n"
        "ds_write_b32 v245, v233 offset:780\n"
        "v_mfma_f32_16x16x32_bf16 v[104:107], a[64:67], a[216:219], v[104:107]\n"
        "v_cvt_pk_bf16_f32 v114, v195, v199\n"
        "v_xor_b32 v252, 0x2000, v252\n"
        "v_mfma_f32_16x16x32_bf16 v[104:107], a[60:63], a[248:251], v[104:107]\n"
        "ds_read_b128 a[224:227], v240 offset:1536\n"
        "v_mul_f32 v100, s50, v100\n"
        "v_mfma_f32_16x16x32_bf16 v[104:107], a[56:59], a[244:247], v[104:107]\n"
        "v_mul_f32 v101, s50, v101\n"
        "v_mul_f32 v102, s50, v102\n"
        "v_mfma_f32_16x16x32_bf16 v[104:107], a[52:55], a[240:243], v[104:107]\n"
        "v_mul_f32 v103, s50, v103\n"
        "v_cvt_pk_bf16_f32 v158, v100, v101\n"
        "v_mfma_f32_16x16x32_bf16 v[104:107], a[48:51], a[236:239], v[104:107]\n"
        "buffer_store_dwordx2 v[112:113], v117, s[16:19], s38 offen offset:1152 sc1\n"
        "v_mfma_f32_16x16x32_bf16 v[96:99], a[236:239], a[144:147], 0\n"
        "ds_read_b128 a[228:231], v241 offset:1536\n"
        "v_cvt_pk_bf16_f32 v159, v102, v103\n"
        "v_mfma_f32_16x16x32_bf16 v[96:99], a[240:243], a[148:151], v[96:99]\n"
        "ds_read_b128 a[232:235], v240 offset:1664\n"
        "v_cvt_pk_bf16_f32 v108, v200, v204\n"
        "v_mfma_f32_16x16x32_bf16 v[96:99], a[244:247], a[152:155], v[96:99]\n"
        "v_mul_f32 v104, s50, v104\n"
        "v_mul_f32 v105, s50, v105\n"
        "v_mfma_f32_16x16x32_bf16 v[96:99], a[248:251], a[156:159], v[96:99]\n"
        "buffer_store_dword v114, v250, s[16:19], s38 offen offset:1152 sc1\n"
        "v_mfma_f32_16x16x32_bf16 v[96:99], a[216:219], a[160:163], v[96:99]\n"
        "s_lshl_b64 s[60:61], s[60:61], 8\n"
        "v_mul_f32 v106, s50, v106\n"
        "v_mul_f32 v107, s50, v107\n"
        "v_mfma_f32_16x16x32_bf16 v[96:99], a[220:223], a[164:167], v[96:99]\n"
        "s_and_b32 s63, s58, 255\n"
        "v_cvt_pk_bf16_f32 v160, v104, v105\n"
        "v_cvt_pk_bf16_f32 v161, v106, v107\n"
        "v_mfma_f32_16x16x32_bf16 v[100:103], a[220:223], a[188:191], 0\n"
        "s_or_b32 s60, s60, s63\n"
        "v_mov_b64 v[166:167], v[160:161]\n"
        "v_cvt_pk_bf16_f32 v109, v212, v216\n"
        "v_mfma_f32_16x16x32_bf16 v[100:103], a[216:219], a[184:187], v[100:103]\n"
        "buffer_store_dwordx2 v[108:109], v117, s[16:19], s39 offen offset:0 sc1\n"
        "v_mfma_f32_16x16x32_bf16 v[100:103], a[248:251], a[180:183], v[100:103]\n"
        "s_add_u32 s27, s27, 1\n"
        "v_cvt_pk_bf16_f32 v128, v96, v97\n"
        "v_cvt_pk_bf16_f32 v129, v98, v99\n"
        "v_mfma_f32_16x16x32_bf16 v[100:103], a[244:247], a[176:179], v[100:103]\n"
        "s_add_u32 s63, s58, 256\n"
        "v_cvt_pk_bf16_f32 v110, v220, v224\n"
        "v_cvt_pk_bf16_f32 v112, v201, v205\n"
        "v_mfma_f32_16x16x32_bf16 v[100:103], a[240:243], a[172:175], v[100:103]\n"
        "s_cmp_lt_u32 s27, 3072\n"
        "v_cvt_pk_bf16_f32 v113, v213, v217\n"
        "v_mul_f32 v236, v81, v236\n"
        "v_mfma_f32_16x16x32_bf16 v[100:103], a[236:239], a[168:171], v[100:103]\n"
        "s_cselect_b32 s49, 4096, 0\n"
        "v_mul_f32 v237, v81, v237\n"
        "v_mul_f32 v238, v81, v238\n"
        "v_mfma_f32_16x16x32_bf16 v[104:107], a[236:239], a[192:195], 0\n"
        "s_cmp_lt_u32 s63, 3328\n"
        "ds_read_b128 a[236:239], v241 offset:1664\n"
        "v_mul_f32 v239, v81, v239\n"
        "v_mfma_f32_16x16x32_bf16 v[104:107], a[240:243], a[196:199], v[104:107]\n"
        "s_cselect_b32 s63, s63, -1\n"
        "ds_read_b128 a[240:243], v240 offset:1792\n"
        "v_cvt_pk_bf16_f32 v236, v236, v237\n"
        "v_mfma_f32_16x16x32_bf16 v[104:107], a[244:247], a[200:203], v[104:107]\n"
        "ds_read_b128 a[244:247], v241 offset:1792\n"
        "v_cvt_pk_bf16_f32 v136, v100, v101\n"
        "v_mfma_f32_16x16x32_bf16 v[104:107], a[248:251], a[204:207], v[104:107]\n"
        "buffer_store_dword v110, v250, s[16:19], s39 offen offset:0 sc1\n"
        "v_mfma_f32_16x16x32_bf16 v[104:107], a[216:219], a[208:211], v[104:107]\n"
        "v_cvt_pk_bf16_f32 v137, v102, v103\n"
        "v_cvt_pk_bf16_f32 v237, v238, v239\n"
        "v_mfma_f32_16x16x32_bf16 v[104:107], a[220:223], a[212:215], v[104:107]\n"
        "s_waitcnt lgkmcnt(5)\n"
        "buffer_store_dwordx2 v[112:113], v117, s[16:19], s39 offen offset:384 sc1\n"
        "v_mfma_f32_16x16x32_bf16 v[96:99], a[72:75], a[224:227], 0\n"
        "s_waitcnt lgkmcnt(4)\n"
        "ds_write_b32 v246, v236 offset:768\n"
        "v_mfma_f32_16x16x32_bf16 v[96:99], a[76:79], a[228:231], v[96:99]\n"
        "s_waitcnt lgkmcnt(4)\n"
        "ds_write_b32 v246, v237 offset:772\n"
        "v_mfma_f32_16x16x32_bf16 v[96:99], a[80:83], a[232:235], v[96:99]\n"
        "s_waitcnt lgkmcnt(4)\n"
        "ds_read_b128 v[162:165], v251 offset:0\n"
        "v_cvt_pk_bf16_f32 v114, v221, v225\n"
        "v_mfma_f32_16x16x32_bf16 v[96:99], a[84:87], a[236:239], v[96:99]\n"
        "s_waitcnt lgkmcnt(4)\n"
        "v_cvt_pk_bf16_f32 v144, v104, v105\n"
        "v_cvt_pk_bf16_f32 v145, v106, v107\n"
        "v_mfma_f32_16x16x32_bf16 v[96:99], a[88:91], a[240:243], v[96:99]\n"
        "s_waitcnt lgkmcnt(3)\n"
        "buffer_store_dword v114, v250, s[16:19], s39 offen offset:384 sc1\n"
        "v_mfma_f32_16x16x32_bf16 v[96:99], a[92:95], a[244:247], v[96:99]\n"
        "v_cvt_pk_bf16_f32 v108, v202, v206\n"
        "v_cvt_pk_bf16_f32 v109, v214, v218\n"
        "v_mfma_f32_16x16x32_bf16 v[100:103], a[116:119], a[244:247], 0\n"
        "v_cvt_pk_bf16_f32 v110, v222, v226\n"
        "v_add_u32 v245, s31, v245\n"
        "v_mfma_f32_16x16x32_bf16 v[100:103], a[112:115], a[240:243], v[100:103]\n"
        "v_add_u32 v246, s31, v246\n"
        "v_add_u32 v242, s62, v242\n"
        "v_mfma_f32_16x16x32_bf16 v[100:103], a[108:111], a[236:239], v[100:103]\n"
        "v_cvt_pk_bf16_f32 v172, v96, v97\n"
        "v_cvt_pk_bf16_f32 v173, v98, v99\n"
        "v_mfma_f32_16x16x32_bf16 v[100:103], a[104:107], a[232:235], v[100:103]\n"
        "buffer_store_dwordx2 v[108:109], v117, s[16:19], s39 offen offset:768 sc1\n"
        "v_mfma_f32_16x16x32_bf16 v[100:103], a[100:103], a[228:231], v[100:103]\n"
        "v_cvt_pk_bf16_f32 v112, v203, v207\n"
        "v_cvt_pk_bf16_f32 v113, v215, v219\n"
        "v_mfma_f32_16x16x32_bf16 v[100:103], a[96:99], a[224:227], v[100:103]\n"
        "s_nop 0\n"
        "v_subrev_u32 v118, 196608, v242\n"
        "v_add_u32 v243, s62, v243\n"
        "v_mfma_f32_16x16x32_bf16 v[104:107], a[120:123], a[224:227], 0\n"
        "s_nop 0\n"
        "v_min_u32 v242, v242, v118\n"
        "v_subrev_u32 v119, 196608, v243\n"
        "v_mfma_f32_16x16x32_bf16 v[104:107], a[124:127], a[228:231], v[104:107]\n"
        "v_cvt_pk_bf16_f32 v174, v100, v101\n"
        "v_cvt_pk_bf16_f32 v175, v102, v103\n"
        "v_mfma_f32_16x16x32_bf16 v[104:107], a[128:131], a[232:235], v[104:107]\n"
        "buffer_store_dword v110, v250, s[16:19], s39 offen offset:768 sc1\n"
        "v_mfma_f32_16x16x32_bf16 v[104:107], a[132:135], a[236:239], v[104:107]\n"
        "v_cvt_pk_bf16_f32 v114, v223, v227\n"
        "v_min_u32 v243, v243, v119\n"
        "v_mfma_f32_16x16x32_bf16 v[104:107], a[136:139], a[240:243], v[104:107]\n"
        "v_add_u32 v244, s62, v244\n"
        "v_mfma_f32_16x16x32_bf16 v[104:107], a[140:143], a[244:247], v[104:107]\n"
        "s_nop 1\n"
        "v_subrev_u32 v118, 196608, v244\n"
        "v_mfma_f32_16x16x32_bf16 v[96:99], a[20:23], a[244:247], 0\n"
        "s_nop 1\n"
        "v_min_u32 v244, v244, v118\n"
        "v_mfma_f32_16x16x32_bf16 v[96:99], a[16:19], a[240:243], v[96:99]\n"
        "buffer_store_dwordx2 v[112:113], v117, s[16:19], s39 offen offset:1152 sc1\n"
        "v_mfma_f32_16x16x32_bf16 v[96:99], a[12:15], a[236:239], v[96:99]\n"
        "v_cvt_pk_bf16_f32 v190, v104, v105\n"
        "v_cvt_pk_bf16_f32 v191, v106, v107\n"
        "v_mfma_f32_16x16x32_bf16 v[96:99], a[8:11], a[232:235], v[96:99]\n"
        "v_mfma_f32_16x16x32_bf16 v[96:99], a[4:7], a[228:231], v[96:99]\n"
        "ds_read_b128 v[182:185], v251 offset:512\n"
        "v_mfma_f32_16x16x32_bf16 v[96:99], a[0:3], a[224:227], v[96:99]\n"
        "s_nop 2\n"
        "v_mfma_f32_16x16x32_bf16 v[64:67], v[152:155], v[156:159], v[0:3]\n"
        "s_waitcnt lgkmcnt(1)\n"
        "buffer_store_dword v114, v250, s[16:19], s39 offen offset:1152 sc1\n"
        "v_mfma_f32_16x16x32_bf16 v[64:67], v[168:171], v[160:163], v[64:67]\n"
        "s_nop 0\n"
        "v_mfma_f32_16x16x32_bf16 v[68:71], v[172:175], v[156:159], v[4:7]\n"
        "v_mul_f32 v96, s50, v96\n"
        "v_mul_f32 v97, s50, v97\n"
        "v_mfma_f32_16x16x32_bf16 v[68:71], v[188:191], v[164:167], v[68:71]\n"
        "v_mul_f32 v98, s50, v98\n"
        "v_mul_f32 v99, s50, v99\n"
        "v_mfma_f32_16x16x32_bf16 v[100:103], a[24:27], a[224:227], 0\n"
        "v_cvt_pk_bf16_f32 v176, v96, v97\n"
        "v_cvt_pk_bf16_f32 v177, v98, v99\n"
        "v_mfma_f32_16x16x32_bf16 v[100:103], a[28:31], a[228:231], v[100:103]\n"
        "v_exp_f32 v64, v64\n"
        "v_mfma_f32_16x16x32_bf16 v[100:103], a[32:35], a[232:235], v[100:103]\n"
        "s_add_u32 m0, s41, 0\n"
        "v_exp_f32 v65, v65\n"
        "v_mfma_f32_16x16x32_bf16 v[100:103], a[36:39], a[236:239], v[100:103]\n"
        "v_exp_f32 v66, v66\n"
        "v_mfma_f32_16x16x32_bf16 v[100:103], a[40:43], a[240:243], v[100:103]\n"
        "buffer_load_dwordx4 v242, s[8:11], s47 offen lds\n"
        "v_mfma_f32_16x16x32_bf16 v[100:103], a[44:47], a[244:247], v[100:103]\n"
        "v_exp_f32 v67, v67\n"
        "v_mfma_f32_16x16x32_bf16 v[104:107], a[68:71], a[244:247], 0\n"
        "v_exp_f32 v68, v68\n"
        "v_mfma_f32_16x16x32_bf16 v[104:107], a[64:67], a[240:243], v[104:107]\n"
        "s_add_u32 m0, s41, 1024\n"
        "v_exp_f32 v69, v69\n"
        "v_mfma_f32_16x16x32_bf16 v[104:107], a[60:63], a[236:239], v[104:107]\n"
        "v_exp_f32 v70, v70\n"
        "v_mfma_f32_16x16x32_bf16 v[104:107], a[56:59], a[232:235], v[104:107]\n"
        "buffer_load_dwordx4 v243, s[8:11], s47 offen lds\n"
        "v_mfma_f32_16x16x32_bf16 v[104:107], a[52:55], a[228:231], v[104:107]\n"
        "v_mul_f32 v100, s50, v100\n"
        "v_mul_f32 v101, s50, v101\n"
        "v_mfma_f32_16x16x32_bf16 v[104:107], a[48:51], a[224:227], v[104:107]\n"
        "v_mul_f32 v102, s50, v102\n"
        "v_mul_f32 v103, s50, v103\n"
        "v_mfma_f32_16x16x32_bf16 v[96:99], a[224:227], a[144:147], 0\n"
        "s_add_u32 m0, s41, 2048\n"
        "v_cvt_pk_bf16_f32 v178, v100, v101\n"
        "v_cvt_pk_bf16_f32 v179, v102, v103\n"
        "v_mfma_f32_16x16x32_bf16 v[96:99], a[228:231], a[148:151], v[96:99]\n"
        "ds_read_b128 a[248:251], v240 offset:12288\n"
        "v_mul_f32 v104, s50, v104\n"
        "v_mfma_f32_16x16x32_bf16 v[96:99], a[232:235], a[152:155], v[96:99]\n"
        "buffer_load_dwordx4 v244, s[8:11], s47 offen lds\n"
        "v_mfma_f32_16x16x32_bf16 v[96:99], a[236:239], a[156:159], v[96:99]\n"
        "v_mul_f32 v105, s50, v105\n"
        "v_mul_f32 v106, s50, v106\n"
        "v_mfma_f32_16x16x32_bf16 v[96:99], a[240:243], a[160:163], v[96:99]\n"
        "v_mul_f32 v107, s50, v107\n"
        "v_cvt_pk_bf16_f32 v180, v104, v105\n"
        "v_mfma_f32_16x16x32_bf16 v[96:99], a[244:247], a[164:167], v[96:99]\n"
        "s_add_u32 m0, s41, 12288\n"
        "ds_read_b128 a[216:219], v241 offset:12288\n"
        "v_cvt_pk_bf16_f32 v181, v106, v107\n"
        "v_mfma_f32_16x16x32_bf16 v[72:75], v[152:155], v[176:179], v[16:19]\n"
        "s_waitcnt lgkmcnt(2)\n"
        "ds_read_b128 a[220:223], v240 offset:12416\n"
        "v_mov_b64 v[186:187], v[180:181]\n"
        "v_mfma_f32_16x16x32_bf16 v[72:75], v[168:171], v[180:183], v[72:75]\n"
        "buffer_load_dwordx4 v242, s[8:11], s35 offen lds\n"
        "v_mfma_f32_16x16x32_bf16 v[76:79], v[172:175], v[176:179], v[20:23]\n"
        "v_cvt_pk_bf16_f32 v130, v96, v97\n"
        "v_cvt_pk_bf16_f32 v131, v98, v99\n"
        "v_mfma_f32_16x16x32_bf16 v[76:79], v[188:191], v[184:187], v[76:79]\n"
        "v_exp_f32 v71, v71\n"
        "v_mfma_f32_16x16x32_bf16 v[100:103], a[244:247], a[188:191], 0\n"
        "s_add_u32 m0, s41, 13312\n"
        "v_exp_f32 v72, v72\n"
        "v_mfma_f32_16x16x32_bf16 v[100:103], a[240:243], a[184:187], v[100:103]\n"
        "v_exp_f32 v73, v73\n"
        "v_mfma_f32_16x16x32_bf16 v[100:103], a[236:239], a[180:183], v[100:103]\n"
        "buffer_load_dwordx4 v243, s[8:11], s35 offen lds\n"
        "v_mfma_f32_16x16x32_bf16 v[100:103], a[232:235], a[176:179], v[100:103]\n"
        "v_exp_f32 v74, v74\n"
        "v_mfma_f32_16x16x32_bf16 v[100:103], a[228:231], a[172:175], v[100:103]\n"
        "v_exp_f32 v75, v75\n"
        "v_mfma_f32_16x16x32_bf16 v[100:103], a[224:227], a[168:171], v[100:103]\n"
        "s_add_u32 m0, s41, 14336\n"
        "v_exp_f32 v76, v76\n"
        "v_mfma_f32_16x16x32_bf16 v[104:107], a[224:227], a[192:195], 0\n"
        "ds_read_b128 a[224:227], v241 offset:12416\n"
        "v_cvt_pk_bf16_f32 v64, v64, v65\n"
        "v_mfma_f32_16x16x32_bf16 v[104:107], a[228:231], a[196:199], v[104:107]\n"
        "ds_read_b128 a[228:231], v240 offset:12544\n"
        "v_cvt_pk_bf16_f32 v65, v66, v67\n"
        "v_mfma_f32_16x16x32_bf16 v[104:107], a[232:235], a[200:203], v[104:107]\n"
        "ds_read_b128 a[232:235], v241 offset:12544\n"
        "v_cvt_pk_bf16_f32 v138, v100, v101\n"
        "v_mfma_f32_16x16x32_bf16 v[104:107], a[236:239], a[204:207], v[104:107]\n"
        "buffer_load_dwordx4 v244, s[8:11], s35 offen lds\n"
        "v_mfma_f32_16x16x32_bf16 v[104:107], a[240:243], a[208:211], v[104:107]\n"
        "s_mov_b32 m0, s48\n"
        "v_cvt_pk_bf16_f32 v139, v102, v103\n"
        "v_cvt_pk_bf16_f32 v66, v68, v69\n"
        "v_mfma_f32_16x16x32_bf16 v[104:107], a[244:247], a[212:215], v[104:107]\n"
        "s_waitcnt lgkmcnt(5)\n"
        "v_exp_f32 v77, v77\n"
        "v_mfma_f32_16x16x32_bf16 v[96:99], a[72:75], a[248:251], 0\n"
        "s_waitcnt lgkmcnt(4)\n"
        "buffer_load_dwordx4 v249, s[12:15], s36 offen offset:0 lds\n"
        "v_mfma_f32_16x16x32_bf16 v[96:99], a[76:79], a[216:219], v[96:99]\n"
        "s_waitcnt lgkmcnt(3)\n"
        "ds_read_b128 v[162:165], v252 offset:0\n"
        "v_xor_b32 v116, 32, v249\n"
        "v_mfma_f32_16x16x32_bf16 v[96:99], a[80:83], a[220:223], v[96:99]\n"
        "s_waitcnt lgkmcnt(3)\n"
        "ds_read_b128 v[182:185], v252 offset:512\n"
        "v_cvt_pk_bf16_f32 v146, v104, v105\n"
        "v_mfma_f32_16x16x32_bf16 v[96:99], a[84:87], a[224:227], v[96:99]\n"
        "s_waitcnt lgkmcnt(3)\n"
        "v_cvt_pk_bf16_f32 v147, v106, v107\n"
        "v_cvt_pk_bf16_f32 v67, v70, v71\n"
        "v_mfma_f32_16x16x32_bf16 v[96:99], a[88:91], a[228:231], v[96:99]\n"
        "s_waitcnt lgkmcnt(2)\n"
        "v_exp_f32 v78, v78\n"
        "v_mfma_f32_16x16x32_bf16 v[96:99], a[92:95], a[232:235], v[96:99]\n"
        "buffer_load_dwordx4 v116, s[12:15], s36 offen offset:1024 lds\n"
        "v_mfma_f32_16x16x32_bf16 v[100:103], a[116:119], a[232:235], 0\n"
        "v_exp_f32 v79, v79\n"
        "v_mfma_f32_16x16x32_bf16 v[100:103], a[112:115], a[228:231], v[100:103]\n"
        "v_cvt_pk_bf16_f32 v72, v72, v73\n"
        "v_cvt_pk_bf16_f32 v73, v74, v75\n"
        "v_mfma_f32_16x16x32_bf16 v[100:103], a[108:111], a[224:227], v[100:103]\n"
        "v_cvt_pk_bf16_f32 v74, v76, v77\n"
        "v_cvt_pk_bf16_f32 v192, v96, v97\n"
        "v_mfma_f32_16x16x32_bf16 v[100:103], a[104:107], a[220:223], v[100:103]\n"
        "v_cvt_pk_bf16_f32 v193, v98, v99\n"
        "v_cvt_pk_bf16_f32 v75, v78, v79\n"
        "v_mfma_f32_16x16x32_bf16 v[100:103], a[100:103], a[216:219], v[100:103]\n"
        "44:\n"
        "s_cbranch_execz 40b\n"
        "v_mfma_f32_16x16x32_bf16 v[100:103], a[96:99], a[248:251], v[100:103]\n"
        "s_nop 2\n"
        "v_mfma_f32_16x16x32_bf16 v[104:107], a[120:123], a[248:251], 0\n"
        "s_nop 2\n"
        "v_mfma_f32_16x16x32_bf16 v[104:107], a[124:127], a[216:219], v[104:107]\n"
        "v_cvt_pk_bf16_f32 v194, v100, v101\n"
        "v_cvt_pk_bf16_f32 v195, v102, v103\n"
        "v_mfma_f32_16x16x32_bf16 v[104:107], a[128:131], a[220:223], v[104:107]\n"
        "v_mfma_f32_16x16x32_bf16 v[104:107], a[132:135], a[224:227], v[104:107]\n"
        "v_mfma_f32_16x16x32_bf16 v[104:107], a[136:139], a[228:231], v[104:107]\n"
        "v_mfma_f32_16x16x32_bf16 v[104:107], a[140:143], a[232:235], v[104:107]\n"
        "s_nop 2\n"
        "v_mfma_f32_16x16x32_bf16 v[96:99], a[20:23], a[232:235], 0\n"
        "s_nop 2\n"
        "v_mfma_f32_16x16x32_bf16 v[96:99], a[16:19], a[228:231], v[96:99]\n"
        "v_cvt_pk_bf16_f32 v208, v104, v105\n"
        "v_cvt_pk_bf16_f32 v209, v106, v107\n"
        "v_mfma_f32_16x16x32_bf16 v[96:99], a[12:15], a[224:227], v[96:99]\n"
        "v_mfma_f32_16x16x32_bf16 v[96:99], a[8:11], a[220:223], v[96:99]\n"
        "v_mfma_f32_16x16x32_bf16 v[96:99], a[4:7], a[216:219], v[96:99]\n"
        "ds_read_b128 v[202:205], v251 offset:4096\n"
        "v_mfma_f32_16x16x32_bf16 v[96:99], a[0:3], a[248:251], v[96:99]\n"
        "s_nop 2\n"
        "v_mfma_f32_16x16x32_bf16 v[68:71], v[192:195], v[156:159], v[8:11]\n"
        "s_waitcnt lgkmcnt(2)\n"
        "s_nop 1\n"
        "v_mfma_f32_16x16x32_bf16 v[68:71], v[208:211], v[160:163], v[68:71]\n"
        "v_mfma_f32_16x16x32_bf16 v[76:79], v[192:195], v[176:179], v[24:27]\n"
        "s_waitcnt lgkmcnt(1)\n"
        "v_mul_f32 v96, s50, v96\n"
        "v_mul_f32 v97, s50, v97\n"
        "v_mfma_f32_16x16x32_bf16 v[76:79], v[208:211], v[180:183], v[76:79]\n"
        "v_mul_f32 v98, s50, v98\n"
        "v_mul_f32 v99, s50, v99\n"
        "v_mfma_f32_16x16x32_bf16 v[100:103], a[24:27], a[248:251], 0\n"
        "v_cvt_pk_bf16_f32 v196, v96, v97\n"
        "v_cvt_pk_bf16_f32 v197, v98, v99\n"
        "v_mfma_f32_16x16x32_bf16 v[100:103], a[28:31], a[216:219], v[100:103]\n"
        "v_exp_f32 v68, v68\n"
        "v_mfma_f32_16x16x32_bf16 v[100:103], a[32:35], a[220:223], v[100:103]\n"
        "v_exp_f32 v69, v69\n"
        "v_mfma_f32_16x16x32_bf16 v[100:103], a[36:39], a[224:227], v[100:103]\n"
        "v_exp_f32 v70, v70\n"
        "v_mfma_f32_16x16x32_bf16 v[100:103], a[40:43], a[228:231], v[100:103]\n"
        "v_exp_f32 v71, v71\n"
        "v_mfma_f32_16x16x32_bf16 v[100:103], a[44:47], a[232:235], v[100:103]\n"
        "v_exp_f32 v76, v76\n"
        "v_mfma_f32_16x16x32_bf16 v[104:107], a[68:71], a[232:235], 0\n"
        "v_exp_f32 v77, v77\n"
        "v_mfma_f32_16x16x32_bf16 v[104:107], a[64:67], a[228:231], v[104:107]\n"
        "v_exp_f32 v78, v78\n"
        "v_mfma_f32_16x16x32_bf16 v[104:107], a[60:63], a[224:227], v[104:107]\n"
        "v_exp_f32 v79, v79\n"
        "v_mfma_f32_16x16x32_bf16 v[104:107], a[56:59], a[220:223], v[104:107]\n"
        "v_mul_f32 v100, s50, v100\n"
        "v_mul_f32 v101, s50, v101\n"
        "v_mfma_f32_16x16x32_bf16 v[104:107], a[52:55], a[216:219], v[104:107]\n"
        "v_mul_f32 v102, s50, v102\n"
        "v_mul_f32 v103, s50, v103\n"
        "v_mfma_f32_16x16x32_bf16 v[104:107], a[48:51], a[248:251], v[104:107]\n"
        "v_cvt_pk_bf16_f32 v198, v100, v101\n"
        "v_cvt_pk_bf16_f32 v199, v102, v103\n"
        "v_mfma_f32_16x16x32_bf16 v[96:99], a[248:251], a[144:147], 0\n"
        "ds_read_b128 a[236:239], v240 offset:13824\n"
        "v_cvt_pk_bf16_f32 v68, v68, v69\n"
        "v_mfma_f32_16x16x32_bf16 v[96:99], a[216:219], a[148:151], v[96:99]\n"
        "ds_read_b128 a[240:243], v241 offset:13824\n"
        "v_cvt_pk_bf16_f32 v69, v70, v71\n"
        "v_mfma_f32_16x16x32_bf16 v[96:99], a[220:223], a[152:155], v[96:99]\n"
        "v_mul_f32 v104, s50, v104\n"
        "v_mul_f32 v105, s50, v105\n"
        "v_mfma_f32_16x16x32_bf16 v[96:99], a[224:227], a[156:159], v[96:99]\n"
        "v_mul_f32 v106, s50, v106\n"
        "v_mul_f32 v107, s50, v107\n"
        "v_mfma_f32_16x16x32_bf16 v[96:99], a[228:231], a[160:163], v[96:99]\n"
        "v_cvt_pk_bf16_f32 v200, v104, v105\n"
        "v_cvt_pk_bf16_f32 v201, v106, v107\n"
        "v_mfma_f32_16x16x32_bf16 v[96:99], a[232:235], a[164:167], v[96:99]\n"
        "ds_read_b128 a[244:247], v240 offset:13952\n"
        "v_mov_b64 v[206:207], v[200:201]\n"
        "v_mfma_f32_16x16x32_bf16 v[80:83], v[152:155], v[196:199], v[32:35]\n"
        "s_waitcnt lgkmcnt(3)\n"
        "v_cvt_pk_bf16_f32 v76, v76, v77\n"
        "v_cvt_pk_bf16_f32 v77, v78, v79\n"
        "v_mfma_f32_16x16x32_bf16 v[80:83], v[168:171], v[200:203], v[80:83]\n"
        "s_nop 0\n"
        "v_mfma_f32_16x16x32_bf16 v[84:87], v[172:175], v[196:199], v[36:39]\n"
        "v_cvt_pk_bf16_f32 v132, v96, v97\n"
        "v_cvt_pk_bf16_f32 v133, v98, v99\n"
        "v_mfma_f32_16x16x32_bf16 v[84:87], v[188:191], v[204:207], v[84:87]\n"
        "ds_read_b128 v[202:205], v252 offset:4096\n"
        "v_lshrrev_b32 v121, 6, v250\n"
        "v_mfma_f32_16x16x32_bf16 v[100:103], a[232:235], a[188:191], 0\n"
        "v_add3_u32 v121, v121, v245, s67\n"
        "v_mfma_f32_16x16x32_bf16 v[100:103], a[228:231], a[184:187], v[100:103]\n"
        "v_exp_f32 v80, v80\n"
        "v_mfma_f32_16x16x32_bf16 v[100:103], a[224:227], a[180:183], v[100:103]\n"
        "v_exp_f32 v81, v81\n"
        "v_mfma_f32_16x16x32_bf16 v[100:103], a[220:223], a[176:179], v[100:103]\n"
        "v_exp_f32 v82, v82\n"
        "v_mfma_f32_16x16x32_bf16 v[100:103], a[216:219], a[172:175], v[100:103]\n"
        "v_exp_f32 v83, v83\n"
        "v_mfma_f32_16x16x32_bf16 v[100:103], a[248:251], a[168:171], v[100:103]\n"
        "v_exp_f32 v84, v84\n"
        "v_mfma_f32_16x16x32_bf16 v[104:107], a[248:251], a[192:195], 0\n"
        "ds_read_b128 a[248:251], v241 offset:13952\n"
        "v_cvt_pk_bf16_f32 v80, v80, v81\n"
        "v_mfma_f32_16x16x32_bf16 v[104:107], a[216:219], a[196:199], v[104:107]\n"
        "ds_read_b128 a[216:219], v240 offset:14080\n"
        "v_cvt_pk_bf16_f32 v81, v82, v83\n"
        "v_mfma_f32_16x16x32_bf16 v[104:107], a[220:223], a[200:203], v[104:107]\n"
        "ds_read_b128 a[220:223], v241 offset:14080\n"
        "v_cvt_pk_bf16_f32 v140, v100, v101\n"
        "v_mfma_f32_16x16x32_bf16 v[104:107], a[224:227], a[204:207], v[104:107]\n"
        "v_cvt_pk_bf16_f32 v141, v102, v103\n"
        "v_mfma_f32_16x16x32_bf16 v[104:107], a[228:231], a[208:211], v[104:107]\n"
        "v_exp_f32 v85, v85\n"
        "v_mfma_f32_16x16x32_bf16 v[104:107], a[232:235], a[212:215], v[104:107]\n"
        "s_waitcnt lgkmcnt(6)\n"
        "v_exp_f32 v86, v86\n"
        "v_mfma_f32_16x16x32_bf16 v[96:99], a[72:75], a[236:239], 0\n"
        "s_waitcnt lgkmcnt(5)\n"
        "v_cvt_pk_bf16_f32 v82, v84, v85\n"
        "v_mfma_f32_16x16x32_bf16 v[96:99], a[76:79], a[240:243], v[96:99]\n"
        "s_waitcnt lgkmcnt(4)\n"
        "v_exp_f32 v87, v87\n"
        "v_mfma_f32_16x16x32_bf16 v[96:99], a[80:83], a[244:247], v[96:99]\n"
        "s_waitcnt lgkmcnt(2)\n"
        "v_cvt_pk_bf16_f32 v83, v86, v87\n"
        "v_mfma_f32_16x16x32_bf16 v[96:99], a[84:87], a[248:251], v[96:99]\n"
        "s_waitcnt lgkmcnt(1)\n"
        "v_cvt_pk_bf16_f32 v148, v104, v105\n"
        "v_cvt_pk_bf16_f32 v149, v106, v107\n"
        "v_mfma_f32_16x16x32_bf16 v[96:99], a[88:91], a[216:219], v[96:99]\n"
        "s_waitcnt lgkmcnt(0)\n"
        "v_mfma_f32_16x16x32_bf16 v[96:99], a[92:95], a[220:223], v[96:99]\n"
        "s_nop 2\n"
        "v_mfma_f32_16x16x32_bf16 v[84:87], v[192:195], v[196:199], v[40:43]\n"
        "s_nop 1\n"
        "v_mfma_f32_16x16x32_bf16 v[84:87], v[208:211], v[200:203], v[84:87]\n"
        "v_mfma_f32_16x16x32_bf16 v[100:103], a[116:119], a[220:223], 0\n"
        "v_cvt_pk_bf16_f32 v212, v96, v97\n"
        "v_cvt_pk_bf16_f32 v213, v98, v99\n"
        "v_mfma_f32_16x16x32_bf16 v[100:103], a[112:115], a[216:219], v[100:103]\n"
        "v_add_u32 v240, s45, v240\n"
        "v_add_u32 v241, s45, v241\n"
        "v_mfma_f32_16x16x32_bf16 v[100:103], a[108:111], a[248:251], v[100:103]\n"
        "v_mfma_f32_16x16x32_bf16 v[100:103], a[104:107], a[244:247], v[100:103]\n"
        "v_exp_f32 v84, v84\n"
        "v_mfma_f32_16x16x32_bf16 v[100:103], a[100:103], a[240:243], v[100:103]\n"
        "v_exp_f32 v85, v85\n"
        "v_mfma_f32_16x16x32_bf16 v[100:103], a[96:99], a[236:239], v[100:103]\n"
        "v_exp_f32 v86, v86\n"
        "v_mfma_f32_16x16x32_bf16 v[104:107], a[120:123], a[236:239], 0\n"
        "v_exp_f32 v87, v87\n"
        "v_mfma_f32_16x16x32_bf16 v[104:107], a[124:127], a[240:243], v[104:107]\n"
        "v_cvt_pk_bf16_f32 v84, v84, v85\n"
        "v_cvt_pk_bf16_f32 v85, v86, v87\n"
        "v_mfma_f32_16x16x32_bf16 v[104:107], a[128:131], a[244:247], v[104:107]\n"
        "s_waitcnt vmcnt(24)\n"
        "v_readfirstlane_b32 s59, v120\n"
        "v_mfma_f32_16x16x32_bf16 v[104:107], a[132:135], a[248:251], v[104:107]\n"
        "v_cvt_pk_bf16_f32 v214, v100, v101\n"
        "v_cvt_pk_bf16_f32 v215, v102, v103\n"
        "v_mfma_f32_16x16x32_bf16 v[104:107], a[136:139], a[216:219], v[104:107]\n"
        "s_min_u32 s59, s59, 768\n"
        "v_mfma_f32_16x16x32_bf16 v[104:107], a[140:143], a[220:223], v[104:107]\n"
        "s_add_u32 s59, s59, 3328\n"
        "s_nop 0\n"
        "v_mov_b32 v120, s59\n"
        "v_mfma_f32_16x16x32_bf16 v[96:99], a[20:23], a[220:223], 0\n"
        "s_lshr_b32 s46, s56, 6\n"
        "s_nop 1\n"
        "v_mfma_f32_16x16x32_bf16 v[96:99], a[16:19], a[216:219], v[96:99]\n"
        "s_mul_i32 s46, s46, 1572864\n"
        "v_cvt_pk_bf16_f32 v230, v104, v105\n"
        "v_cvt_pk_bf16_f32 v231, v106, v107\n"
        "v_mfma_f32_16x16x32_bf16 v[96:99], a[12:15], a[248:251], v[96:99]\n"
        "s_barrier\n"
        "v_mfma_f32_16x16x32_bf16 v[96:99], a[8:11], a[244:247], v[96:99]\n"
        "s_add_u32 s46, s46, s37\n"
        "ds_write_b32 v121, v120 offset:768\n"
        "v_mfma_f32_16x16x32_bf16 v[96:99], a[4:7], a[240:243], v[96:99]\n"
        "s_cmp_eq_u32 s46, 100663296\n"
        "v_mfma_f32_16x16x32_bf16 v[96:99], a[0:3], a[236:239], v[96:99]\n"
        "s_cselect_b32 s46, 0, s46\n"
        "s_nop 1\n"
        "v_mfma_f32_16x16x32_bf16 v[108:111], v[212:215], v[156:159], v[12:15]\n"
        "s_and_b32 s43, s56, 63\n"
        "ds_read_b128 v[222:225], v251 offset:4608\n"
        "v_mfma_f32_16x16x32_bf16 v[108:111], v[228:231], v[164:167], v[108:111]\n"
        "s_mul_i32 s43, s43, 3072\n"
        "v_mul_f32 v96, s50, v96\n"
        "v_mul_f32 v97, s50, v97\n"
        "v_mfma_f32_16x16x32_bf16 v[112:115], v[212:215], v[176:179], v[28:31]\n"
        "s_add_u32 s44, s43, 3072\n"
        "v_mul_f32 v98, s50, v98\n"
        "v_mul_f32 v99, s50, v99\n"
        "v_mfma_f32_16x16x32_bf16 v[112:115], v[228:231], v[184:187], v[112:115]\n"
        "s_cmp_eq_u32 s44, 196608\n"
        "v_cvt_pk_bf16_f32 v216, v96, v97\n"
        "v_cvt_pk_bf16_f32 v217, v98, v99\n"
        "v_mfma_f32_16x16x32_bf16 v[116:119], v[212:215], v[196:199], v[44:47]\n"
        "s_cselect_b32 s44, 0, s44\n"
        "v_exp_f32 v108, v108\n"
        "v_mfma_f32_16x16x32_bf16 v[116:119], v[228:231], v[204:207], v[116:119]\n"
        "s_add_u32 s39, s46, s44\n"
        "v_exp_f32 v109, v109\n"
        "v_mfma_f32_16x16x32_bf16 v[100:103], a[24:27], a[236:239], 0\n"
        "s_add_u32 s43, s43, s46\n"
        "v_exp_f32 v110, v110\n"
        "v_mfma_f32_16x16x32_bf16 v[100:103], a[28:31], a[240:243], v[100:103]\n"
        "s_add_u32 s38, s43, 1536\n"
        "v_exp_f32 v111, v111\n"
        "v_mfma_f32_16x16x32_bf16 v[100:103], a[32:35], a[244:247], v[100:103]\n"
        "v_exp_f32 v112, v112\n"
        "v_mfma_f32_16x16x32_bf16 v[100:103], a[36:39], a[248:251], v[100:103]\n"
        "v_exp_f32 v113, v113\n"
        "v_mfma_f32_16x16x32_bf16 v[100:103], a[40:43], a[216:219], v[100:103]\n"
        "v_exp_f32 v114, v114\n"
        "v_mfma_f32_16x16x32_bf16 v[100:103], a[44:47], a[220:223], v[100:103]\n"
        "v_exp_f32 v115, v115\n"
        "v_mfma_f32_16x16x32_bf16 v[104:107], a[68:71], a[220:223], 0\n"
        "v_exp_f32 v116, v116\n"
        "v_mfma_f32_16x16x32_bf16 v[104:107], a[64:67], a[216:219], v[104:107]\n"
        "v_exp_f32 v117, v117\n"
        "v_mfma_f32_16x16x32_bf16 v[104:107], a[60:63], a[248:251], v[104:107]\n"
        "v_exp_f32 v118, v118\n"
        "v_mfma_f32_16x16x32_bf16 v[104:107], a[56:59], a[244:247], v[104:107]\n"
        "v_mul_f32 v100, s50, v100\n"
        "v_mul_f32 v101, s50, v101\n"
        "v_mfma_f32_16x16x32_bf16 v[104:107], a[52:55], a[240:243], v[104:107]\n"
        "v_mul_f32 v102, s50, v102\n"
        "v_mul_f32 v103, s50, v103\n"
        "v_mfma_f32_16x16x32_bf16 v[104:107], a[48:51], a[236:239], v[104:107]\n"
        "v_cvt_pk_bf16_f32 v218, v100, v101\n"
        "v_cvt_pk_bf16_f32 v219, v102, v103\n"
        "v_mfma_f32_16x16x32_bf16 v[96:99], a[236:239], a[144:147], 0\n"
        "v_exp_f32 v119, v119\n"
        "v_mfma_f32_16x16x32_bf16 v[96:99], a[240:243], a[148:151], v[96:99]\n"
        "v_cvt_pk_bf16_f32 v70, v108, v109\n"
        "v_cvt_pk_bf16_f32 v71, v110, v111\n"
        "v_mfma_f32_16x16x32_bf16 v[96:99], a[244:247], a[152:155], v[96:99]\n"
        "v_mul_f32 v104, s50, v104\n"
        "v_mul_f32 v105, s50, v105\n"
        "v_mfma_f32_16x16x32_bf16 v[96:99], a[248:251], a[156:159], v[96:99]\n"
        "v_mul_f32 v106, s50, v106\n"
        "v_mul_f32 v107, s50, v107\n"
        "v_mfma_f32_16x16x32_bf16 v[96:99], a[216:219], a[160:163], v[96:99]\n"
        "v_cvt_pk_bf16_f32 v220, v104, v105\n"
        "v_cvt_pk_bf16_f32 v221, v106, v107\n"
        "v_mfma_f32_16x16x32_bf16 v[96:99], a[220:223], a[164:167], v[96:99]\n"
        "ds_read_b128 v[156:159], v247 offset:0\n"
        "v_mov_b64 v[226:227], v[220:221]\n"
        "v_mfma_f32_16x16x32_bf16 v[88:91], v[152:155], v[216:219], v[48:51]\n"
        "s_waitcnt lgkmcnt(1)\n"
        "ds_read_b128 v[164:167], v247 offset:4096\n"
        "v_cvt_pk_bf16_f32 v78, v112, v113\n"
        "v_mfma_f32_16x16x32_bf16 v[88:91], v[168:171], v[220:223], v[88:91]\n"
        "ds_read_b128 v[160:163], v247 offset:256\n"
        "v_cvt_pk_bf16_f32 v79, v114, v115\n"
        "v_mfma_f32_16x16x32_bf16 v[92:95], v[172:175], v[216:219], v[52:55]\n"
        "s_mov_b32 s56, s57\n"
        "ds_read_b128 v[152:155], v247 offset:4352\n"
        "v_cvt_pk_bf16_f32 v134, v96, v97\n"
        "v_mfma_f32_16x16x32_bf16 v[92:95], v[188:191], v[224:227], v[92:95]\n"
        "s_mov_b32 s57, s58\n"
        "ds_read_b128 v[222:225], v252 offset:4608\n"
        "v_cvt_pk_bf16_f32 v135, v98, v99\n"
        "v_mfma_f32_16x16x32_bf16 v[100:103], a[220:223], a[188:191], 0\n"
        "s_xor_b32 s48, s48, 8192\n"
        "v_exp_f32 v88, v88\n"
        "v_mfma_f32_16x16x32_bf16 v[100:103], a[216:219], a[184:187], v[100:103]\n"
        "v_exp_f32 v89, v89\n"
        "v_mfma_f32_16x16x32_bf16 v[100:103], a[248:251], a[180:183], v[100:103]\n"
        "v_exp_f32 v90, v90\n"
        "v_mfma_f32_16x16x32_bf16 v[100:103], a[244:247], a[176:179], v[100:103]\n"
        "v_exp_f32 v91, v91\n"
        "v_mfma_f32_16x16x32_bf16 v[100:103], a[240:243], a[172:175], v[100:103]\n"
        "v_exp_f32 v92, v92\n"
        "v_mfma_f32_16x16x32_bf16 v[100:103], a[236:239], a[168:171], v[100:103]\n"
        "v_exp_f32 v93, v93\n"
        "v_mfma_f32_16x16x32_bf16 v[104:107], a[236:239], a[192:195], 0\n"
        "ds_read_b128 a[236:239], v248 offset:0\n"
        "v_cvt_pk_bf16_f32 v86, v116, v117\n"
        "v_mfma_f32_16x16x32_bf16 v[104:107], a[240:243], a[196:199], v[104:107]\n"
        "ds_read_b128 a[240:243], v248 offset:6144\n"
        "v_cvt_pk_bf16_f32 v87, v118, v119\n"
        "v_mfma_f32_16x16x32_bf16 v[104:107], a[244:247], a[200:203], v[104:107]\n"
        "ds_read_b128 a[244:247], v254 offset:16\n"
        "v_mov_b64 v[116:117], 0\n"
        "v_mfma_f32_16x16x32_bf16 v[104:107], a[248:251], a[204:207], v[104:107]\n"
        "v_cvt_pk_bf16_f32 v142, v100, v101\n"
        "v_cvt_pk_bf16_f32 v143, v102, v103\n"
        "v_mfma_f32_16x16x32_bf16 v[104:107], a[216:219], a[208:211], v[104:107]\n"
        "ds_read_b128 a[248:251], v254 offset:6160\n"
        "v_cvt_pk_bf16_f32 v88, v88, v89\n"
        "v_mfma_f32_16x16x32_bf16 v[104:107], a[220:223], a[212:215], v[104:107]\n"
        "ds_read_b128 a[216:219], v248 offset:32\n"
        "v_cvt_pk_bf16_f32 v89, v90, v91\n"
        "v_mfma_f32_16x16x32_bf16 v[108:111], v[192:195], v[216:219], v[56:59]\n"
        "s_waitcnt lgkmcnt(5)\n"
        "ds_read_b128 a[220:223], v248 offset:6176\n"
        "v_cvt_pk_bf16_f32 v90, v92, v93\n"
        "v_mfma_f32_16x16x32_bf16 v[108:111], v[208:211], v[220:223], v[108:111]\n"
        "v_exp_f32 v94, v94\n"
        "v_mfma_f32_16x16x32_bf16 v[112:115], v[212:215], v[216:219], v[60:63]\n"
        "ds_read_b128 a[224:227], v254 offset:48\n"
        "v_cvt_pk_bf16_f32 v150, v104, v105\n"
        "v_mfma_f32_16x16x32_bf16 v[112:115], v[228:231], v[224:227], v[112:115]\n"
        "s_waitcnt lgkmcnt(6)\n"
        "ds_read_b128 a[228:231], v254 offset:6192\n"
        "v_cvt_pk_bf16_f32 v151, v106, v107\n"
        "v_mfma_f32_16x16x32_bf16 v[172:175], v[156:159], a[236:239], 0\n"
        "s_waitcnt lgkmcnt(6)\n"
        "v_exp_f32 v95, v95\n"
        "v_mfma_f32_16x16x32_bf16 v[172:175], v[164:167], a[240:243], v[172:175]\n"
        "ds_read_b128 a[232:235], v248 offset:64\n"
        "v_cvt_pk_bf16_f32 v91, v94, v95\n"
        "v_mfma_f32_16x16x32_bf16 v[200:203], v[152:155], a[240:243], 0\n"
        "v_exp_f32 v108, v108\n"
        "v_mfma_f32_16x16x32_bf16 v[200:203], v[160:163], a[236:239], v[200:203]\n"
        "v_exp_f32 v109, v109\n"
        "v_smfmac_f32_16x16x64_bf16 v[116:119], a[252:255], v[64:71], v255\n"
        "ds_read_b128 a[236:239], v248 offset:6208\n"
        "v_cvt_pk_bf16_f32 v92, v108, v109\n"
        "v_mfma_f32_16x16x32_bf16 v[120:123], v[128:131], v[64:67], 0\n"
        "v_exp_f32 v110, v110\n"
        "v_mfma_f32_16x16x32_bf16 v[120:123], v[132:135], v[68:71], v[120:123]\n"
        "v_exp_f32 v111, v111\n"
        "v_mfma_f32_16x16x32_bf16 v[124:127], v[140:143], v[68:71], 0\n"
        "v_exp_f32 v112, v112\n"
        "v_mfma_f32_16x16x32_bf16 v[124:127], v[136:139], v[64:67], v[124:127]\n"
        "v_exp_f32 v113, v113\n"
        "v_mfma_f32_16x16x32_bf16 v[96:99], v[144:147], v[64:67], 0\n"
        "v_exp_f32 v114, v114\n"
        "v_mfma_f32_16x16x32_bf16 v[96:99], v[148:151], v[68:71], v[96:99]\n"
        "s_waitcnt lgkmcnt(7)\n"
        "ds_read_b128 a[240:243], v254 offset:80\n"
        "v_add_f32 v116, v116, v117\n"
        "v_mfma_f32_16x16x32_bf16 v[204:207], v[160:163], a[244:247], 0\n"
        "s_waitcnt lgkmcnt(7)\n"
        "v_rcp_f32 v117, v116\n"
        "v_mfma_f32_16x16x32_bf16 v[204:207], v[152:155], a[248:251], v[204:207]\n"
        "v_exp_f32 v115, v115\n"
        "v_mfma_f32_16x16x32_bf16 v[176:179], v[164:167], a[248:251], 0\n"
        "v_mul_f32 v120, v117, v120\n"
        "v_mul_f32 v121, v117, v121\n"
        "v_mfma_f32_16x16x32_bf16 v[176:179], v[156:159], a[244:247], v[176:179]\n"
        "s_waitcnt lgkmcnt(6)\n"
        "ds_read_b128 a[244:247], v254 offset:6224\n"
        "v_mul_f32 v122, v117, v122\n"
        "v_mfma_f32_16x16x32_bf16 v[180:183], v[156:159], a[216:219], 0\n"
        "s_waitcnt lgkmcnt(6)\n"
        "ds_read_b128 a[248:251], v248 offset:36864\n"
        "v_mov_b64 v[100:101], 0\n"
        "v_mfma_f32_16x16x32_bf16 v[180:183], v[164:167], a[220:223], v[180:183]\n"
        "ds_read_b128 v[232:235], v247 offset:8192\n"
        "v_mul_f32 v123, v117, v123\n"
        "v_mfma_f32_16x16x32_bf16 v[212:215], v[152:155], a[220:223], 0\n"
        "ds_read_b128 v[64:67], v247 offset:12288\n"
        "v_cvt_pk_bf16_f32 v93, v110, v111\n"
        "v_mfma_f32_16x16x32_bf16 v[212:215], v[160:163], a[216:219], v[212:215]\n"
        "s_waitcnt lgkmcnt(8)\n"
        "ds_read_b128 v[236:239], v247 offset:8448\n"
        "v_cvt_pk_bf16_f32 v120, v120, v121\n"
        "v_mfma_f32_16x16x32_bf16 v[216:219], v[160:163], a[224:227], 0\n"
        "s_waitcnt lgkmcnt(8)\n"
        "ds_read_b128 v[68:71], v247 offset:12544\n"
        "v_cvt_pk_bf16_f32 v121, v122, v123\n"
        "v_mfma_f32_16x16x32_bf16 v[216:219], v[152:155], a[228:231], v[216:219]\n"
        "ds_write_b32 v245, v120 offset:0\n"
        "v_mfma_f32_16x16x32_bf16 v[184:187], v[164:167], a[228:231], 0\n"
        "ds_write_b32 v245, v121 offset:4\n"
        "v_mfma_f32_16x16x32_bf16 v[184:187], v[156:159], a[224:227], v[184:187]\n"
        "s_waitcnt lgkmcnt(10)\n"
        "ds_read_b128 a[216:219], v248 offset:43008\n"
        "v_cvt_pk_bf16_f32 v94, v112, v113\n"
        "v_mfma_f32_16x16x32_bf16 v[192:195], v[156:159], a[232:235], 0\n"
        "s_waitcnt lgkmcnt(10)\n"
        "v_mul_f32 v124, v117, v124\n"
        "v_mul_f32 v125, v117, v125\n"
        "v_mfma_f32_16x16x32_bf16 v[192:195], v[164:167], a[236:239], v[192:195]\n"
        "v_maximum3_f32 v253, v253, v116, v117\n"
        "v_mul_f32 v126, v117, v126\n"
        "v_mfma_f32_16x16x32_bf16 v[220:223], v[152:155], a[236:239], 0\n"
        "v_mul_f32 v127, v117, v127\n"
        "v_cvt_pk_bf16_f32 v95, v114, v115\n"
        "v_mfma_f32_16x16x32_bf16 v[220:223], v[160:163], a[232:235], v[220:223]\n"
        "ds_read_b128 a[220:223], v254 offset:36880\n"
        "v_cvt_pk_bf16_f32 v124, v124, v125\n"
        "v_smfmac_f32_16x16x64_bf16 v[100:103], a[252:255], v[72:79], v255\n"
        "ds_read_b128 a[224:227], v254 offset:43024\n"
        "v_cvt_pk_bf16_f32 v125, v126, v127\n"
        "v_mfma_f32_16x16x32_bf16 v[104:107], v[128:131], v[72:75], 0\n"
        "ds_write_b32 v245, v124 offset:8\n"
        "v_mfma_f32_16x16x32_bf16 v[104:107], v[132:135], v[76:79], v[104:107]\n"
        "ds_write_b32 v245, v125 offset:12\n"
        "v_mfma_f32_16x16x32_bf16 v[108:111], v[140:143], v[76:79], 0\n"
        "v_mul_f32 v96, v117, v96\n"
        "v_mul_f32 v97, v117, v97\n"
        "v_mfma_f32_16x16x32_bf16 v[108:111], v[136:139], v[72:75], v[108:111]\n"
        "v_mul_f32 v98, v117, v98\n"
        "v_mul_f32 v99, v117, v99\n"
        "v_mfma_f32_16x16x32_bf16 v[112:115], v[144:147], v[72:75], 0\n"
        "ds_read_b128 a[228:231], v248 offset:36896\n"
        "v_cvt_pk_bf16_f32 v96, v96, v97\n"
        "v_mfma_f32_16x16x32_bf16 v[112:115], v[148:151], v[76:79], v[112:115]\n"
        "s_waitcnt lgkmcnt(14)\n"
        "ds_read_b128 a[232:235], v248 offset:43040\n"
        "v_cvt_pk_bf16_f32 v97, v98, v99\n"
        "v_mfma_f32_16x16x32_bf16 v[224:227], v[160:163], a[240:243], 0\n"
        "s_waitcnt lgkmcnt(14)\n"
        "ds_write_b32 v246, v96 offset:0\n"
        "v_mfma_f32_16x16x32_bf16 v[224:227], v[152:155], a[244:247], v[224:227]\n"
        "ds_write_b32 v246, v97 offset:4\n"
        "v_mfma_f32_16x16x32_bf16 v[196:199], v[164:167], a[244:247], 0\n"
        "ds_read_b128 a[236:239], v254 offset:36912\n"
        "v_add_f32 v100, v100, v101\n"
        "v_mfma_f32_16x16x32_bf16 v[196:199], v[156:159], a[240:243], v[196:199]\n"
        "s_waitcnt lgkmcnt(15)\n"
        "v_rcp_f32 v101, v100\n"
        "v_mfma_f32_16x16x32_bf16 v[172:175], v[232:235], a[248:251], v[172:175]\n"
        "s_waitcnt lgkmcnt(9)\n"
        "ds_read_b128 a[240:243], v254 offset:43056\n"
        "v_mul_f32 v104, v101, v104\n"
        "v_mfma_f32_16x16x32_bf16 v[172:175], v[64:67], a[216:219], v[172:175]\n"
        "ds_read_b128 a[244:247], v248 offset:36928\n"
        "v_mul_f32 v105, v101, v105\n"
        "v_mfma_f32_16x16x32_bf16 v[200:203], v[68:71], a[216:219], v[200:203]\n"
        "v_mul_f32 v106, v101, v106\n"
        "v_mul_f32 v107, v101, v107\n"
        "v_mfma_f32_16x16x32_bf16 v[200:203], v[236:239], a[248:251], v[200:203]\n"
        "s_waitcnt lgkmcnt(10)\n"
        "ds_read_b128 a[248:251], v248 offset:43072\n"
        "v_cvt_pk_bf16_f32 v104, v104, v105\n"
        "v_mfma_f32_16x16x32_bf16 v[204:207], v[236:239], a[220:223], v[204:207]\n"
        "s_waitcnt lgkmcnt(10)\n"
        "ds_read_b128 a[216:219], v254 offset:36944\n"
        "v_mov_b64 v[116:117], 0\n"
        "v_mfma_f32_16x16x32_bf16 v[204:207], v[68:71], a[224:227], v[204:207]\n"
        "v_cvt_pk_bf16_f32 v105, v106, v107\n"
        "v_mul_f32 v108, v101, v108\n"
        "v_mfma_f32_16x16x32_bf16 v[176:179], v[64:67], a[224:227], v[176:179]\n"
        "ds_write_b32 v245, v104 offset:256\n"
        "v_mfma_f32_16x16x32_bf16 v[176:179], v[232:235], a[220:223], v[176:179]\n"
        "s_waitcnt lgkmcnt(9)\n"
        "ds_write_b32 v245, v105 offset:260\n"
        "v_mfma_f32_16x16x32_bf16 v[180:183], v[232:235], a[228:231], v[180:183]\n"
        "s_waitcnt lgkmcnt(9)\n"
        "ds_read_b128 a[220:223], v254 offset:43088\n"
        "v_mul_f32 v109, v101, v109\n"
        "v_mfma_f32_16x16x32_bf16 v[180:183], v[64:67], a[232:235], v[180:183]\n"
        "v_maximum3_f32 v253, v253, v100, v101\n"
        "v_mul_f32 v110, v101, v110\n"
        "v_mfma_f32_16x16x32_bf16 v[212:215], v[68:71], a[232:235], v[212:215]\n"
        "v_mul_f32 v111, v101, v111\n"
        "v_cvt_pk_bf16_f32 v108, v108, v109\n"
        "v_mfma_f32_16x16x32_bf16 v[212:215], v[236:239], a[228:231], v[212:215]\n"
        "s_waitcnt lgkmcnt(7)\n"
        "ds_read_b128 v[156:159], v247 offset:16384\n"
        "v_cvt_pk_bf16_f32 v109, v110, v111\n"
        "v_mfma_f32_16x16x32_bf16 v[216:219], v[236:239], a[236:239], v[216:219]\n"
        "s_waitcnt lgkmcnt(7)\n"
        "ds_write_b32 v245, v108 offset:264\n"
        "v_mfma_f32_16x16x32_bf16 v[216:219], v[68:71], a[240:243], v[216:219]\n"
        "ds_write_b32 v245, v109 offset:268\n"
        "v_mfma_f32_16x16x32_bf16 v[184:187], v[64:67], a[240:243], v[184:187]\n"
        "v_mul_f32 v112, v101, v112\n"
        "v_mul_f32 v113, v101, v113\n"
        "v_mfma_f32_16x16x32_bf16 v[184:187], v[232:235], a[236:239], v[184:187]\n"
        "v_mul_f32 v114, v101, v114\n"
        "v_mul_f32 v115, v101, v115\n"
        "v_smfmac_f32_16x16x64_bf16 v[116:119], a[252:255], v[80:87], v255\n"
        "ds_read_b128 v[164:167], v247 offset:20480\n"
        "v_cvt_pk_bf16_f32 v112, v112, v113\n"
        "v_mfma_f32_16x16x32_bf16 v[120:123], v[128:131], v[80:83], 0\n"
        "ds_read_b128 v[160:163], v247 offset:16640\n"
        "v_cvt_pk_bf16_f32 v113, v114, v115\n"
        "v_mfma_f32_16x16x32_bf16 v[120:123], v[132:135], v[84:87], v[120:123]\n"
        "ds_write_b32 v246, v112 offset:256\n"
        "v_mfma_f32_16x16x32_bf16 v[124:127], v[140:143], v[84:87], 0\n"
        "ds_write_b32 v246, v113 offset:260\n"
        "v_mfma_f32_16x16x32_bf16 v[124:127], v[136:139], v[80:83], v[124:127]\n"
        "ds_read_b128 a[224:227], v248 offset:12288\n"
        "v_add_f32 v116, v116, v117\n"
        "v_mfma_f32_16x16x32_bf16 v[96:99], v[144:147], v[80:83], 0\n"
        "ds_read_b128 v[152:155], v247 offset:20736\n"
        "v_mfma_f32_16x16x32_bf16 v[96:99], v[148:151], v[84:87], v[96:99]\n"
        "s_waitcnt lgkmcnt(14)\n"
        "ds_read_b128 a[228:231], v248 offset:49152\n"
        "v_mfma_f32_16x16x32_bf16 v[192:195], v[232:235], a[244:247], v[192:195]\n"
        "s_waitcnt lgkmcnt(14)\n"
        "v_rcp_f32 v117, v116\n"
        "v_mfma_f32_16x16x32_bf16 v[192:195], v[64:67], a[248:251], v[192:195]\n"
        "ds_read_b128 a[232:235], v254 offset:12304\n"
        "v_mul_f32 v120, v117, v120\n"
        "v_mfma_f32_16x16x32_bf16 v[220:223], v[68:71], a[248:251], v[220:223]\n"
        "ds_read_b128 a[236:239], v254 offset:49168\n"
        "v_mul_f32 v121, v117, v121\n"
        "v_mfma_f32_16x16x32_bf16 v[220:223], v[236:239], a[244:247], v[220:223]\n"
        "s_waitcnt lgkmcnt(15)\n"
        "ds_read_b128 a[240:243], v248 offset:12320\n"
        "v_mul_f32 v122, v117, v122\n"
        "v_mfma_f32_16x16x32_bf16 v[224:227], v[236:239], a[216:219], v[224:227]\n"
        "s_waitcnt lgkmcnt(13)\n"
        "ds_read_b128 a[244:247], v248 offset:49184\n"
        "v_mul_f32 v123, v117, v123\n"
        "v_mfma_f32_16x16x32_bf16 v[224:227], v[68:71], a[220:223], v[224:227]\n"
        "ds_read_b128 a[248:251], v254 offset:12336\n"
        "v_cvt_pk_bf16_f32 v120, v120, v121\n"
        "v_mfma_f32_16x16x32_bf16 v[196:199], v[64:67], a[220:223], v[196:199]\n"
        "ds_read_b128 a[220:223], v248 offset:12352\n"
        "v_cvt_pk_bf16_f32 v121, v122, v123\n"
        "v_mfma_f32_16x16x32_bf16 v[196:199], v[232:235], a[216:219], v[196:199]\n"
        "s_waitcnt lgkmcnt(8)\n"
        "ds_write_b32 v245, v120 offset:512\n"
        "v_mfma_f32_16x16x32_bf16 v[172:175], v[156:159], a[224:227], v[172:175]\n"
        "s_waitcnt lgkmcnt(7)\n"
        "ds_read_b128 a[216:219], v254 offset:49200\n"
        "v_mul_f32 v124, v117, v124\n"
        "v_mfma_f32_16x16x32_bf16 v[172:175], v[164:167], a[228:231], v[172:175]\n"
        "ds_write_b32 v245, v121 offset:516\n"
        "v_mfma_f32_16x16x32_bf16 v[200:203], v[152:155], a[228:231], v[200:203]\n"
        "ds_read_b128 a[228:231], v254 offset:12368\n"
        "v_mul_f32 v125, v117, v125\n"
        "v_mfma_f32_16x16x32_bf16 v[200:203], v[160:163], a[224:227], v[200:203]\n"
        "s_waitcnt lgkmcnt(9)\n"
        "ds_read_b128 a[224:227], v248 offset:49216\n"
        "v_maximum3_f32 v253, v253, v116, v117\n"
        "v_mfma_f32_16x16x32_bf16 v[204:207], v[160:163], a[232:235], v[204:207]\n"
        "s_waitcnt lgkmcnt(9)\n"
        "v_mul_f32 v126, v117, v126\n"
        "v_mul_f32 v127, v117, v127\n"
        "v_mfma_f32_16x16x32_bf16 v[204:207], v[152:155], a[236:239], v[204:207]\n"
        "v_cvt_pk_bf16_f32 v124, v124, v125\n"
        "v_cvt_pk_bf16_f32 v125, v126, v127\n"
        "v_mfma_f32_16x16x32_bf16 v[176:179], v[164:167], a[236:239], v[176:179]\n"
        "ds_write_b32 v245, v124 offset:520\n"
        "v_mfma_f32_16x16x32_bf16 v[176:179], v[156:159], a[232:235], v[176:179]\n"
        "s_waitcnt lgkmcnt(9)\n"
        "ds_write_b32 v245, v125 offset:524\n"
        "v_mfma_f32_16x16x32_bf16 v[180:183], v[156:159], a[240:243], v[180:183]\n"
        "s_waitcnt lgkmcnt(9)\n"
        "ds_read_b128 a[232:235], v254 offset:49232\n"
        "v_mul_f32 v96, v117, v96\n"
        "v_mfma_f32_16x16x32_bf16 v[180:183], v[164:167], a[244:247], v[180:183]\n"
        "v_mul_f32 v97, v117, v97\n"
        "v_mul_f32 v98, v117, v98\n"
        "v_mfma_f32_16x16x32_bf16 v[212:215], v[152:155], a[244:247], v[212:215]\n"
        "v_mul_f32 v99, v117, v99\n"
        "v_cvt_pk_bf16_f32 v96, v96, v97\n"
        "v_mfma_f32_16x16x32_bf16 v[212:215], v[160:163], a[240:243], v[212:215]\n"
        "s_waitcnt lgkmcnt(9)\n"
        "v_cvt_pk_bf16_f32 v97, v98, v99\n"
        "v_add_u32 v247, s31, v247\n"
        "v_mfma_f32_16x16x32_bf16 v[216:219], v[160:163], a[248:251], v[216:219]\n"
        "s_waitcnt lgkmcnt(6)\n"
        "v_mov_b64 v[80:81], 0\n"
        "v_mfma_f32_16x16x32_bf16 v[216:219], v[152:155], a[216:219], v[216:219]\n"
        "ds_write_b32 v246, v96 offset:512\n"
        "v_mfma_f32_16x16x32_bf16 v[184:187], v[164:167], a[216:219], v[184:187]\n"
        "s_waitcnt vmcnt(0)\n"
        "ds_write_b32 v246, v97 offset:516\n"
        "v_mfma_f32_16x16x32_bf16 v[184:187], v[156:159], a[248:251], v[184:187]\n"
        "s_barrier\n"
        "v_mfma_f32_16x16x32_bf16 v[192:195], v[156:159], a[220:223], v[192:195]\n"
        "s_waitcnt lgkmcnt(5)\n"
        "ds_read_b32 v108, v245 offset:768\n"
        "v_mfma_f32_16x16x32_bf16 v[192:195], v[164:167], a[224:227], v[192:195]\n"
        "ds_read_b128 a[236:239], v240 offset:0\n"
        "v_mfma_f32_16x16x32_bf16 v[220:223], v[152:155], a[224:227], v[220:223]\n"
        "ds_read_b128 a[240:243], v241 offset:0\n"
        "v_mfma_f32_16x16x32_bf16 v[220:223], v[160:163], a[220:223], v[220:223]\n"
        "ds_read_b128 a[244:247], v240 offset:128\n"
        "v_mfma_f32_16x16x32_bf16 v[224:227], v[160:163], a[228:231], v[224:227]\n"
        "s_waitcnt lgkmcnt(6)\n"
        "ds_read_b128 a[248:251], v241 offset:128\n"
        "v_mfma_f32_16x16x32_bf16 v[224:227], v[152:155], a[232:235], v[224:227]\n"
        "ds_read_b128 a[216:219], v240 offset:256\n"
        "v_mfma_f32_16x16x32_bf16 v[196:199], v[164:167], a[232:235], v[196:199]\n"
        "ds_read_b128 a[220:223], v241 offset:256\n"
        "v_mfma_f32_16x16x32_bf16 v[196:199], v[156:159], a[228:231], v[196:199]\n"
        "v_smfmac_f32_16x16x64_bf16 v[80:83], a[252:255], v[88:95], v255\n"
        "s_waitcnt lgkmcnt(6)\n"
        "v_readfirstlane_b32 s59, v108\n"
        "v_mfma_f32_16x16x32_bf16 v[84:87], v[128:131], v[88:91], 0\n"
        "v_mfma_f32_16x16x32_bf16 v[84:87], v[132:135], v[92:95], v[84:87]\n"
        "s_min_u32 s58, s63, s59\n"
        "s_cmp_lt_u32 s58, s49\n"
        "v_mfma_f32_16x16x32_bf16 v[232:235], v[140:143], v[92:95], 0\n"
        "s_cbranch_scc1 10b\n"
        "v_mfma_f32_16x16x32_bf16 v[232:235], v[136:139], v[88:91], v[232:235]\n"
        "v_mfma_f32_16x16x32_bf16 v[236:239], v[144:147], v[88:91], 0\n"
        "v_mfma_f32_16x16x32_bf16 v[236:239], v[148:151], v[92:95], v[236:239]\n"
        "20:\n"
        "s_waitcnt lgkmcnt(0)\n"
        "v_mfma_f32_16x16x32_bf16 v[96:99], a[72:75], a[236:239], 0\n"
        "v_and_b32 v242, 60, v250\n"
        "v_add3_u32 v242, v250, v242, s26\n"
        "v_mfma_f32_16x16x32_bf16 v[96:99], a[76:79], a[240:243], v[96:99]\n"
        "v_cvt_pk_bf16_f32 v108, v172, v176\n"
        "v_cvt_pk_bf16_f32 v109, v180, v184\n"
        "v_mfma_f32_16x16x32_bf16 v[96:99], a[80:83], a[244:247], v[96:99]\n"
        "v_cvt_pk_bf16_f32 v110, v192, v196\n"
        "v_add_f32 v80, v80, v81\n"
        "v_mfma_f32_16x16x32_bf16 v[96:99], a[84:87], a[248:251], v[96:99]\n"
        "buffer_store_dwordx2 v[108:109], v242, s[16:19], s38 offen offset:0 sc1\n"
        "v_mfma_f32_16x16x32_bf16 v[96:99], a[88:91], a[216:219], v[96:99]\n"
        "v_rcp_f32 v81, v80\n"
        "v_mfma_f32_16x16x32_bf16 v[96:99], a[92:95], a[220:223], v[96:99]\n"
        "45:\n"
        "s_cbranch_execz 40b\n"
        "v_cvt_pk_bf16_f32 v112, v173, v177\n"
        "v_cvt_pk_bf16_f32 v113, v181, v185\n"
        "v_mfma_f32_16x16x32_bf16 v[100:103], a[116:119], a[220:223], 0\n"
        "v_mul_f32 v84, v81, v84\n"
        "v_mul_f32 v85, v81, v85\n"
        "v_mfma_f32_16x16x32_bf16 v[100:103], a[112:115], a[216:219], v[100:103]\n"
        "v_mul_f32 v86, v81, v86\n"
        "v_mul_f32 v87, v81, v87\n"
        "v_mfma_f32_16x16x32_bf16 v[100:103], a[108:111], a[248:251], v[100:103]\n"
        "buffer_store_dword v110, v250, s[16:19], s38 offen offset:0 sc1\n"
        "v_mfma_f32_16x16x32_bf16 v[100:103], a[104:107], a[244:247], v[100:103]\n"
        "s_sub_u32 s31, s29, s30\n"
        "v_cvt_pk_bf16_f32 v152, v96, v97\n"
        "v_cvt_pk_bf16_f32 v153, v98, v99\n"
        "v_mfma_f32_16x16x32_bf16 v[100:103], a[100:103], a[240:243], v[100:103]\n"
        "v_cvt_pk_bf16_f32 v114, v193, v197\n"
        "v_cvt_pk_bf16_f32 v84, v84, v85\n"
        "v_mfma_f32_16x16x32_bf16 v[100:103], a[96:99], a[236:239], v[100:103]\n"
        "ds_write_b32 v245, v84 offset:768\n"
        "v_mfma_f32_16x16x32_bf16 v[104:107], a[120:123], a[236:239], 0\n"
        "v_cvt_pk_bf16_f32 v85, v86, v87\n"
        "v_mul_f32 v232, v81, v232\n"
        "v_mfma_f32_16x16x32_bf16 v[104:107], a[124:127], a[240:243], v[104:107]\n"
        "buffer_store_dwordx2 v[112:113], v242, s[16:19], s38 offen offset:384 sc1\n"
        "v_mfma_f32_16x16x32_bf16 v[104:107], a[128:131], a[244:247], v[104:107]\n"
        "ds_write_b32 v245, v85 offset:772\n"
        "v_mfma_f32_16x16x32_bf16 v[104:107], a[132:135], a[248:251], v[104:107]\n"
        "v_cvt_pk_bf16_f32 v154, v100, v101\n"
        "v_cvt_pk_bf16_f32 v155, v102, v103\n"
        "v_mfma_f32_16x16x32_bf16 v[104:107], a[136:139], a[216:219], v[104:107]\n"
        "v_cvt_pk_bf16_f32 v108, v174, v178\n"
        "v_cvt_pk_bf16_f32 v109, v182, v186\n"
        "v_mfma_f32_16x16x32_bf16 v[104:107], a[140:143], a[220:223], v[104:107]\n"
        "v_mul_f32 v233, v81, v233\n"
        "v_maximum3_f32 v253, v253, v80, v81\n"
        "v_mfma_f32_16x16x32_bf16 v[96:99], a[20:23], a[220:223], 0\n"
        "buffer_store_dword v114, v250, s[16:19], s38 offen offset:384 sc1\n"
        "v_mfma_f32_16x16x32_bf16 v[96:99], a[16:19], a[216:219], v[96:99]\n"
        "v_cvt_pk_bf16_f32 v110, v194, v198\n"
        "v_mul_f32 v234, v81, v234\n"
        "v_mfma_f32_16x16x32_bf16 v[96:99], a[12:15], a[248:251], v[96:99]\n"
        "v_cvt_pk_bf16_f32 v168, v104, v105\n"
        "v_cvt_pk_bf16_f32 v169, v106, v107\n"
        "v_mfma_f32_16x16x32_bf16 v[96:99], a[8:11], a[244:247], v[96:99]\n"
        "v_mul_f32 v235, v81, v235\n"
        "v_cvt_pk_bf16_f32 v232, v232, v233\n"
        "v_mfma_f32_16x16x32_bf16 v[96:99], a[4:7], a[240:243], v[96:99]\n"
        "ds_write_b32 v245, v232 offset:776\n"
        "v_mfma_f32_16x16x32_bf16 v[96:99], a[0:3], a[236:239], v[96:99]\n"
        "buffer_store_dwordx2 v[108:109], v242, s[16:19], s38 offen offset:768 sc1\n"
        "v_mfma_f32_16x16x32_bf16 v[100:103], a[24:27], a[236:239], 0\n"
        "v_cvt_pk_bf16_f32 v112, v175, v179\n"
        "v_cvt_pk_bf16_f32 v113, v183, v187\n"
        "v_mfma_f32_16x16x32_bf16 v[100:103], a[28:31], a[240:243], v[100:103]\n"
        "v_cvt_pk_bf16_f32 v233, v234, v235\n"
        "v_xor_b32 v251, 0x2000, v251\n"
        "v_mfma_f32_16x16x32_bf16 v[100:103], a[32:35], a[244:247], v[100:103]\n"
        "v_mul_f32 v96, s50, v96\n"
        "v_mul_f32 v97, s50, v97\n"
        "v_mfma_f32_16x16x32_bf16 v[100:103], a[36:39], a[248:251], v[100:103]\n"
        "v_mul_f32 v98, s50, v98\n"
        "v_mul_f32 v99, s50, v99\n"
        "v_mfma_f32_16x16x32_bf16 v[100:103], a[40:43], a[216:219], v[100:103]\n"
        "buffer_store_dword v110, v250, s[16:19], s38 offen offset:768 sc1\n"
        "v_mfma_f32_16x16x32_bf16 v[100:103], a[44:47], a[220:223], v[100:103]\n"
        "v_cvt_pk_bf16_f32 v156, v96, v97\n"
        "v_cvt_pk_bf16_f32 v157, v98, v99\n"
        "v_mfma_f32_16x16x32_bf16 v[104:107], a[68:71], a[220:223], 0\n"
        "ds_write_b32 v245, v233 offset:780\n"
        "v_mfma_f32_16x16x32_bf16 v[104:107], a[64:67], a[216:219], v[104:107]\n"
        "v_cvt_pk_bf16_f32 v114, v195, v199\n"
        "v_xor_b32 v252, 0x2000, v252\n"
        "v_mfma_f32_16x16x32_bf16 v[104:107], a[60:63], a[248:251], v[104:107]\n"
        "v_mul_f32 v100, s50, v100\n"
        "v_mul_f32 v101, s50, v101\n"
        "v_mfma_f32_16x16x32_bf16 v[104:107], a[56:59], a[244:247], v[104:107]\n"
        "buffer_store_dwordx2 v[112:113], v242, s[16:19], s38 offen offset:1152 sc1\n"
        "v_mfma_f32_16x16x32_bf16 v[104:107], a[52:55], a[240:243], v[104:107]\n"
        "v_mul_f32 v102, s50, v102\n"
        "v_mul_f32 v103, s50, v103\n"
        "v_mfma_f32_16x16x32_bf16 v[104:107], a[48:51], a[236:239], v[104:107]\n"
        "v_cvt_pk_bf16_f32 v158, v100, v101\n"
        "v_cvt_pk_bf16_f32 v159, v102, v103\n"
        "v_mfma_f32_16x16x32_bf16 v[96:99], a[236:239], a[144:147], 0\n"
        "ds_read_b128 a[224:227], v240 offset:1536\n"
        "v_cvt_pk_bf16_f32 v108, v200, v204\n"
        "v_mfma_f32_16x16x32_bf16 v[96:99], a[240:243], a[148:151], v[96:99]\n"
        "ds_read_b128 a[228:231], v241 offset:1536\n"
        "v_cvt_pk_bf16_f32 v109, v212, v216\n"
        "v_mfma_f32_16x16x32_bf16 v[96:99], a[244:247], a[152:155], v[96:99]\n"
        "buffer_store_dword v114, v250, s[16:19], s38 offen offset:1152 sc1\n"
        "v_mfma_f32_16x16x32_bf16 v[96:99], a[248:251], a[156:159], v[96:99]\n"
        "v_mul_f32 v104, s50, v104\n"
        "v_mul_f32 v105, s50, v105\n"
        "v_mfma_f32_16x16x32_bf16 v[96:99], a[216:219], a[160:163], v[96:99]\n"
        "v_mul_f32 v106, s50, v106\n"
        "v_mul_f32 v107, s50, v107\n"
        "v_mfma_f32_16x16x32_bf16 v[96:99], a[220:223], a[164:167], v[96:99]\n"
        "v_cvt_pk_bf16_f32 v160, v104, v105\n"
        "v_cvt_pk_bf16_f32 v161, v106, v107\n"
        "v_mfma_f32_16x16x32_bf16 v[100:103], a[220:223], a[188:191], 0\n"
        "ds_read_b128 a[232:235], v240 offset:1664\n"
        "v_mov_b64 v[166:167], v[160:161]\n"
        "v_mfma_f32_16x16x32_bf16 v[100:103], a[216:219], a[184:187], v[100:103]\n"
        "buffer_store_dwordx2 v[108:109], v242, s[16:19], s39 offen offset:0 sc1\n"
        "v_mfma_f32_16x16x32_bf16 v[100:103], a[248:251], a[180:183], v[100:103]\n"
        "v_cvt_pk_bf16_f32 v128, v96, v97\n"
        "v_cvt_pk_bf16_f32 v129, v98, v99\n"
        "v_mfma_f32_16x16x32_bf16 v[100:103], a[244:247], a[176:179], v[100:103]\n"
        "v_cvt_pk_bf16_f32 v110, v220, v224\n"
        "v_cvt_pk_bf16_f32 v112, v201, v205\n"
        "v_mfma_f32_16x16x32_bf16 v[100:103], a[240:243], a[172:175], v[100:103]\n"
        "v_cvt_pk_bf16_f32 v113, v213, v217\n"
        "v_mul_f32 v236, v81, v236\n"
        "v_mfma_f32_16x16x32_bf16 v[100:103], a[236:239], a[168:171], v[100:103]\n"
        "v_mul_f32 v237, v81, v237\n"
        "v_mul_f32 v238, v81, v238\n"
        "v_mfma_f32_16x16x32_bf16 v[104:107], a[236:239], a[192:195], 0\n"
        "buffer_store_dword v110, v250, s[16:19], s39 offen offset:0 sc1\n"
        "v_mfma_f32_16x16x32_bf16 v[104:107], a[240:243], a[196:199], v[104:107]\n"
        "ds_read_b128 a[236:239], v241 offset:1664\n"
        "ds_read_b128 a[240:243], v240 offset:1792\n"
        "v_mfma_f32_16x16x32_bf16 v[104:107], a[244:247], a[200:203], v[104:107]\n"
        "ds_read_b128 a[244:247], v241 offset:1792\n"
        "v_cvt_pk_bf16_f32 v136, v100, v101\n"
        "v_mfma_f32_16x16x32_bf16 v[104:107], a[248:251], a[204:207], v[104:107]\n"
        "v_cvt_pk_bf16_f32 v137, v102, v103\n"
        "v_mul_f32 v239, v81, v239\n"
        "v_mfma_f32_16x16x32_bf16 v[104:107], a[216:219], a[208:211], v[104:107]\n"
        "v_cvt_pk_bf16_f32 v236, v236, v237\n"
        "v_cvt_pk_bf16_f32 v237, v238, v239\n"
        "v_mfma_f32_16x16x32_bf16 v[104:107], a[220:223], a[212:215], v[104:107]\n"
        "s_waitcnt lgkmcnt(5)\n"
        "buffer_store_dwordx2 v[112:113], v242, s[16:19], s39 offen offset:384 sc1\n"
        "v_mfma_f32_16x16x32_bf16 v[96:99], a[72:75], a[224:227], 0\n"
        "s_waitcnt lgkmcnt(4)\n"
        "ds_write_b32 v246, v236 offset:768\n"
        "v_mfma_f32_16x16x32_bf16 v[96:99], a[76:79], a[228:231], v[96:99]\n"
        "s_waitcnt lgkmcnt(4)\n"
        "ds_write_b32 v246, v237 offset:772\n"
        "v_mfma_f32_16x16x32_bf16 v[96:99], a[80:83], a[232:235], v[96:99]\n"
        "s_waitcnt lgkmcnt(4)\n"
        "ds_read_b128 v[162:165], v251 offset:0\n"
        "v_cvt_pk_bf16_f32 v114, v221, v225\n"
        "v_mfma_f32_16x16x32_bf16 v[96:99], a[84:87], a[236:239], v[96:99]\n"
        "s_waitcnt lgkmcnt(4)\n"
        "v_cvt_pk_bf16_f32 v144, v104, v105\n"
        "v_cvt_pk_bf16_f32 v145, v106, v107\n"
        "v_mfma_f32_16x16x32_bf16 v[96:99], a[88:91], a[240:243], v[96:99]\n"
        "s_waitcnt lgkmcnt(3)\n"
        "buffer_store_dword v114, v250, s[16:19], s39 offen offset:384 sc1\n"
        "v_mfma_f32_16x16x32_bf16 v[96:99], a[92:95], a[244:247], v[96:99]\n"
        "v_cvt_pk_bf16_f32 v108, v202, v206\n"
        "v_cvt_pk_bf16_f32 v109, v214, v218\n"
        "v_mfma_f32_16x16x32_bf16 v[100:103], a[116:119], a[244:247], 0\n"
        "v_cvt_pk_bf16_f32 v110, v222, v226\n"
        "v_add_u32 v245, s31, v245\n"
        "v_mfma_f32_16x16x32_bf16 v[100:103], a[112:115], a[240:243], v[100:103]\n"
        "v_add_u32 v246, s31, v246\n"
        "v_mfma_f32_16x16x32_bf16 v[100:103], a[108:111], a[236:239], v[100:103]\n"
        "v_cvt_pk_bf16_f32 v172, v96, v97\n"
        "v_cvt_pk_bf16_f32 v173, v98, v99\n"
        "v_mfma_f32_16x16x32_bf16 v[100:103], a[104:107], a[232:235], v[100:103]\n"
        "buffer_store_dwordx2 v[108:109], v242, s[16:19], s39 offen offset:768 sc1\n"
        "v_mfma_f32_16x16x32_bf16 v[100:103], a[100:103], a[228:231], v[100:103]\n"
        "v_cvt_pk_bf16_f32 v112, v203, v207\n"
        "v_cvt_pk_bf16_f32 v113, v215, v219\n"
        "v_mfma_f32_16x16x32_bf16 v[100:103], a[96:99], a[224:227], v[100:103]\n"
        "s_nop 2\n"
        "v_mfma_f32_16x16x32_bf16 v[104:107], a[120:123], a[224:227], 0\n"
        "s_nop 2\n"
        "v_mfma_f32_16x16x32_bf16 v[104:107], a[124:127], a[228:231], v[104:107]\n"
        "v_cvt_pk_bf16_f32 v174, v100, v101\n"
        "v_cvt_pk_bf16_f32 v175, v102, v103\n"
        "v_mfma_f32_16x16x32_bf16 v[104:107], a[128:131], a[232:235], v[104:107]\n"
        "buffer_store_dword v110, v250, s[16:19], s39 offen offset:768 sc1\n"
        "v_mfma_f32_16x16x32_bf16 v[104:107], a[132:135], a[236:239], v[104:107]\n"
        "v_cvt_pk_bf16_f32 v114, v223, v227\n"
        "v_mfma_f32_16x16x32_bf16 v[104:107], a[136:139], a[240:243], v[104:107]\n"
        "v_mfma_f32_16x16x32_bf16 v[104:107], a[140:143], a[244:247], v[104:107]\n"
        "s_nop 2\n"
        "v_mfma_f32_16x16x32_bf16 v[96:99], a[20:23], a[244:247], 0\n"
        "ds_read_b128 a[248:251], v240 offset:12288\n"
        "s_nop 0\n"
        "v_mfma_f32_16x16x32_bf16 v[96:99], a[16:19], a[240:243], v[96:99]\n"
        "buffer_store_dwordx2 v[112:113], v242, s[16:19], s39 offen offset:1152 sc1\n"
        "v_mfma_f32_16x16x32_bf16 v[96:99], a[12:15], a[236:239], v[96:99]\n"
        "ds_read_b128 a[216:219], v241 offset:12288\n"
        "v_cvt_pk_bf16_f32 v190, v104, v105\n"
        "v_mfma_f32_16x16x32_bf16 v[96:99], a[8:11], a[232:235], v[96:99]\n"
        "ds_read_b128 a[220:223], v240 offset:12416\n"
        "v_cvt_pk_bf16_f32 v191, v106, v107\n"
        "v_mfma_f32_16x16x32_bf16 v[96:99], a[4:7], a[228:231], v[96:99]\n"
        "ds_read_b128 v[182:185], v251 offset:512\n"
        "v_mfma_f32_16x16x32_bf16 v[96:99], a[0:3], a[224:227], v[96:99]\n"
        "s_nop 2\n"
        "v_mfma_f32_16x16x32_bf16 v[0:3], v[152:155], v[156:159], v[0:3]\n"
        "s_waitcnt lgkmcnt(4)\n"
        "buffer_store_dword v114, v250, s[16:19], s39 offen offset:1152 sc1\n"
        "v_mfma_f32_16x16x32_bf16 v[0:3], v[168:171], v[160:163], v[0:3]\n"
        "s_nop 0\n"
        "v_mfma_f32_16x16x32_bf16 v[4:7], v[172:175], v[156:159], v[4:7]\n"
        "v_mul_f32 v96, s50, v96\n"
        "v_mul_f32 v97, s50, v97\n"
        "v_mfma_f32_16x16x32_bf16 v[4:7], v[188:191], v[164:167], v[4:7]\n"
        "v_mul_f32 v98, s50, v98\n"
        "v_mul_f32 v99, s50, v99\n"
        "v_mfma_f32_16x16x32_bf16 v[100:103], a[24:27], a[224:227], 0\n"
        "v_cvt_pk_bf16_f32 v176, v96, v97\n"
        "v_cvt_pk_bf16_f32 v177, v98, v99\n"
        "v_mfma_f32_16x16x32_bf16 v[100:103], a[28:31], a[228:231], v[100:103]\n"
        "v_exp_f32 v0, v0\n"
        "v_mfma_f32_16x16x32_bf16 v[100:103], a[32:35], a[232:235], v[100:103]\n"
        "v_exp_f32 v1, v1\n"
        "v_mfma_f32_16x16x32_bf16 v[100:103], a[36:39], a[236:239], v[100:103]\n"
        "v_exp_f32 v2, v2\n"
        "v_mfma_f32_16x16x32_bf16 v[100:103], a[40:43], a[240:243], v[100:103]\n"
        "v_exp_f32 v3, v3\n"
        "v_mfma_f32_16x16x32_bf16 v[100:103], a[44:47], a[244:247], v[100:103]\n"
        "v_exp_f32 v4, v4\n"
        "v_mfma_f32_16x16x32_bf16 v[104:107], a[68:71], a[244:247], 0\n"
        "ds_read_b128 a[244:247], v241 offset:12416\n"
        "v_cvt_pk_bf16_f32 v64, v0, v1\n"
        "v_mfma_f32_16x16x32_bf16 v[104:107], a[64:67], a[240:243], v[104:107]\n"
        "ds_read_b128 a[240:243], v240 offset:12544\n"
        "v_cvt_pk_bf16_f32 v65, v2, v3\n"
        "v_mfma_f32_16x16x32_bf16 v[104:107], a[60:63], a[236:239], v[104:107]\n"
        "s_barrier\n"
        "ds_read_b128 a[236:239], v241 offset:12544\n"
        "v_mul_f32 v100, s50, v100\n"
        "v_mfma_f32_16x16x32_bf16 v[104:107], a[56:59], a[232:235], v[104:107]\n"
        "v_mul_f32 v101, s50, v101\n"
        "v_mul_f32 v102, s50, v102\n"
        "v_mfma_f32_16x16x32_bf16 v[104:107], a[52:55], a[228:231], v[104:107]\n"
        "v_mul_f32 v103, s50, v103\n"
        "v_cvt_pk_bf16_f32 v178, v100, v101\n"
        "v_mfma_f32_16x16x32_bf16 v[104:107], a[48:51], a[224:227], v[104:107]\n"
        "s_waitcnt lgkmcnt(6)\n"
        "v_cvt_pk_bf16_f32 v179, v102, v103\n"
        "v_mfma_f32_16x16x32_bf16 v[96:99], a[72:75], a[248:251], 0\n"
        "s_waitcnt lgkmcnt(5)\n"
        "v_exp_f32 v5, v5\n"
        "v_mfma_f32_16x16x32_bf16 v[96:99], a[76:79], a[216:219], v[96:99]\n"
        "s_waitcnt lgkmcnt(4)\n"
        "v_exp_f32 v6, v6\n"
        "v_mfma_f32_16x16x32_bf16 v[96:99], a[80:83], a[220:223], v[96:99]\n"
        "s_waitcnt lgkmcnt(2)\n"
        "v_exp_f32 v7, v7\n"
        "v_mfma_f32_16x16x32_bf16 v[96:99], a[84:87], a[244:247], v[96:99]\n"
        "s_waitcnt lgkmcnt(1)\n"
        "v_mul_f32 v104, s50, v104\n"
        "v_mul_f32 v105, s50, v105\n"
        "v_mfma_f32_16x16x32_bf16 v[96:99], a[88:91], a[240:243], v[96:99]\n"
        "s_waitcnt lgkmcnt(0)\n"
        "v_mul_f32 v106, s50, v106\n"
        "v_mul_f32 v107, s50, v107\n"
        "v_mfma_f32_16x16x32_bf16 v[96:99], a[92:95], a[236:239], v[96:99]\n"
        "v_cvt_pk_bf16_f32 v180, v104, v105\n"
        "v_cvt_pk_bf16_f32 v181, v106, v107\n"
        "v_mfma_f32_16x16x32_bf16 v[16:19], v[152:155], v[176:179], v[16:19]\n"
        "ds_read_b128 v[162:165], v252 offset:0\n"
        "v_mov_b64 v[186:187], v[180:181]\n"
        "v_mfma_f32_16x16x32_bf16 v[16:19], v[168:171], v[180:183], v[16:19]\n"
        "s_lshr_b32 s46, s56, 6\n"
        "v_cvt_pk_bf16_f32 v66, v4, v5\n"
        "v_cvt_pk_bf16_f32 v192, v96, v97\n"
        "v_mfma_f32_16x16x32_bf16 v[20:23], v[172:175], v[176:179], v[20:23]\n"
        "s_mul_i32 s46, s46, 1572864\n"
        "v_cvt_pk_bf16_f32 v193, v98, v99\n"
        "v_cvt_pk_bf16_f32 v67, v6, v7\n"
        "v_mfma_f32_16x16x32_bf16 v[20:23], v[188:191], v[184:187], v[20:23]\n"
        "s_add_u32 s46, s46, s37\n"
        "v_exp_f32 v16, v16\n"
        "v_mfma_f32_16x16x32_bf16 v[100:103], a[116:119], a[236:239], 0\n"
        "s_cmp_eq_u32 s46, 100663296\n"
        "ds_read_b128 v[182:185], v252 offset:512\n"
        "v_add_u32 v243, s31, v247\n"
        "v_mfma_f32_16x16x32_bf16 v[100:103], a[112:115], a[240:243], v[100:103]\n"
        "s_cselect_b32 s46, 0, s46\n"
        "v_exp_f32 v17, v17\n"
        "v_mfma_f32_16x16x32_bf16 v[100:103], a[108:111], a[244:247], v[100:103]\n"
        "s_and_b32 s43, s56, 63\n"
        "v_exp_f32 v18, v18\n"
        "v_mfma_f32_16x16x32_bf16 v[100:103], a[104:107], a[220:223], v[100:103]\n"
        "s_mul_i32 s43, s43, 3072\n"
        "v_exp_f32 v19, v19\n"
        "v_mfma_f32_16x16x32_bf16 v[100:103], a[100:103], a[216:219], v[100:103]\n"
        "s_add_u32 s44, s43, 3072\n"
        "v_exp_f32 v20, v20\n"
        "v_mfma_f32_16x16x32_bf16 v[100:103], a[96:99], a[248:251], v[100:103]\n"
        "s_cmp_eq_u32 s44, 196608\n"
        "v_exp_f32 v21, v21\n"
        "v_mfma_f32_16x16x32_bf16 v[104:107], a[120:123], a[248:251], 0\n"
        "s_cselect_b32 s44, 0, s44\n"
        "v_exp_f32 v22, v22\n"
        "v_mfma_f32_16x16x32_bf16 v[104:107], a[124:127], a[216:219], v[104:107]\n"
        "s_add_u32 s39, s46, s44\n"
        "v_exp_f32 v23, v23\n"
        "v_mfma_f32_16x16x32_bf16 v[104:107], a[128:131], a[220:223], v[104:107]\n"
        "s_add_u32 s43, s43, s46\n"
        "v_cvt_pk_bf16_f32 v194, v100, v101\n"
        "v_cvt_pk_bf16_f32 v195, v102, v103\n"
        "v_mfma_f32_16x16x32_bf16 v[104:107], a[132:135], a[244:247], v[104:107]\n"
        "s_add_u32 s38, s43, 1536\n"
        "v_cvt_pk_bf16_f32 v72, v16, v17\n"
        "v_cvt_pk_bf16_f32 v73, v18, v19\n"
        "v_mfma_f32_16x16x32_bf16 v[104:107], a[136:139], a[240:243], v[104:107]\n"
        "s_lshl_b32 s29, s42, 8\n"
        "v_cvt_pk_bf16_f32 v74, v20, v21\n"
        "v_cvt_pk_bf16_f32 v75, v22, v23\n"
        "v_mfma_f32_16x16x32_bf16 v[104:107], a[140:143], a[236:239], v[104:107]\n"
        "s_add_u32 s29, s29, 147456\n"
        "v_mbcnt_lo_u32_b32 v244, -1, 0\n"
        "v_mbcnt_hi_u32_b32 v244, -1, v244\n"
        "v_mfma_f32_16x16x32_bf16 v[96:99], a[20:23], a[236:239], 0\n"
        "s_mov_b32 s47, 147456\n"
        "ds_read_b128 a[232:235], v240 offset:13824\n"
        "v_lshl_add_u32 v249, v244, 4, s47\n"
        "v_mfma_f32_16x16x32_bf16 v[96:99], a[16:19], a[240:243], v[96:99]\n"
        "ds_read_b128 a[228:231], v241 offset:13824\n"
        "v_cvt_pk_bf16_f32 v208, v104, v105\n"
        "v_mfma_f32_16x16x32_bf16 v[96:99], a[12:15], a[244:247], v[96:99]\n"
        "ds_read_b128 a[224:227], v240 offset:13952\n"
        "v_cvt_pk_bf16_f32 v209, v106, v107\n"
        "v_mfma_f32_16x16x32_bf16 v[96:99], a[8:11], a[220:223], v[96:99]\n"
        "v_lshl_add_u32 v244, v244, 2, s29\n"
        "v_mfma_f32_16x16x32_bf16 v[96:99], a[4:7], a[216:219], v[96:99]\n"
        "ds_read_b128 v[202:205], v251 offset:4096\n"
        "v_mfma_f32_16x16x32_bf16 v[96:99], a[0:3], a[248:251], v[96:99]\n"
        "s_nop 2\n"
        "v_mfma_f32_16x16x32_bf16 v[8:11], v[192:195], v[156:159], v[8:11]\n"
        "s_waitcnt lgkmcnt(5)\n"
        "s_nop 1\n"
        "v_mfma_f32_16x16x32_bf16 v[8:11], v[208:211], v[160:163], v[8:11]\n"
        "v_mfma_f32_16x16x32_bf16 v[24:27], v[192:195], v[176:179], v[24:27]\n"
        "s_waitcnt lgkmcnt(4)\n"
        "v_mul_f32 v96, s50, v96\n"
        "v_mul_f32 v97, s50, v97\n"
        "v_mfma_f32_16x16x32_bf16 v[24:27], v[208:211], v[180:183], v[24:27]\n"
        "v_mul_f32 v98, s50, v98\n"
        "v_mul_f32 v99, s50, v99\n"
        "v_mfma_f32_16x16x32_bf16 v[100:103], a[24:27], a[248:251], 0\n"
        "v_cvt_pk_bf16_f32 v196, v96, v97\n"
        "v_cvt_pk_bf16_f32 v197, v98, v99\n"
        "v_mfma_f32_16x16x32_bf16 v[100:103], a[28:31], a[216:219], v[100:103]\n"
        "v_exp_f32 v8, v8\n"
        "v_mfma_f32_16x16x32_bf16 v[100:103], a[32:35], a[220:223], v[100:103]\n"
        "v_exp_f32 v9, v9\n"
        "v_mfma_f32_16x16x32_bf16 v[100:103], a[36:39], a[244:247], v[100:103]\n"
        "v_exp_f32 v10, v10\n"
        "v_mfma_f32_16x16x32_bf16 v[100:103], a[40:43], a[240:243], v[100:103]\n"
        "s_lshr_b32 s46, s57, 6\n"
        "v_exp_f32 v11, v11\n"
        "v_mfma_f32_16x16x32_bf16 v[100:103], a[44:47], a[236:239], v[100:103]\n"
        "s_mul_i32 s46, s46, 1572864\n"
        "v_exp_f32 v24, v24\n"
        "v_mfma_f32_16x16x32_bf16 v[104:107], a[68:71], a[236:239], 0\n"
        "s_add_u32 s46, s46, s37\n"
        "ds_read_b128 a[236:239], v241 offset:13952\n"
        "v_cvt_pk_bf16_f32 v68, v8, v9\n"
        "v_mfma_f32_16x16x32_bf16 v[104:107], a[64:67], a[240:243], v[104:107]\n"
        "s_cmp_eq_u32 s46, 100663296\n"
        "ds_read_b128 a[240:243], v240 offset:14080\n"
        "v_mul_f32 v100, s50, v100\n"
        "v_mfma_f32_16x16x32_bf16 v[104:107], a[60:63], a[244:247], v[104:107]\n"
        "s_cselect_b32 s46, 0, s46\n"
        "ds_read_b128 a[244:247], v241 offset:14080\n"
        "v_mul_f32 v101, s50, v101\n"
        "v_mfma_f32_16x16x32_bf16 v[104:107], a[56:59], a[220:223], v[104:107]\n"
        "s_and_b32 s43, s57, 63\n"
        "v_mul_f32 v102, s50, v102\n"
        "v_mul_f32 v103, s50, v103\n"
        "v_mfma_f32_16x16x32_bf16 v[104:107], a[52:55], a[216:219], v[104:107]\n"
        "s_mul_i32 s43, s43, 3072\n"
        "v_cvt_pk_bf16_f32 v198, v100, v101\n"
        "v_cvt_pk_bf16_f32 v199, v102, v103\n"
        "v_mfma_f32_16x16x32_bf16 v[104:107], a[48:51], a[248:251], v[104:107]\n"
        "s_waitcnt lgkmcnt(6)\n"
        "v_exp_f32 v25, v25\n"
        "v_mfma_f32_16x16x32_bf16 v[96:99], a[72:75], a[232:235], 0\n"
        "s_waitcnt lgkmcnt(5)\n"
        "v_cvt_pk_bf16_f32 v69, v10, v11\n"
        "v_cvt_pk_bf16_f32 v76, v24, v25\n"
        "v_mfma_f32_16x16x32_bf16 v[96:99], a[76:79], a[228:231], v[96:99]\n"
        "s_waitcnt lgkmcnt(4)\n"
        "v_exp_f32 v26, v26\n"
        "v_mfma_f32_16x16x32_bf16 v[96:99], a[80:83], a[224:227], v[96:99]\n"
        "s_waitcnt lgkmcnt(2)\n"
        "v_exp_f32 v27, v27\n"
        "v_mfma_f32_16x16x32_bf16 v[96:99], a[84:87], a[236:239], v[96:99]\n"
        "s_waitcnt lgkmcnt(1)\n"
        "v_mul_f32 v104, s50, v104\n"
        "v_mul_f32 v105, s50, v105\n"
        "v_mfma_f32_16x16x32_bf16 v[96:99], a[88:91], a[240:243], v[96:99]\n"
        "s_waitcnt lgkmcnt(0)\n"
        "v_mul_f32 v106, s50, v106\n"
        "v_mul_f32 v107, s50, v107\n"
        "v_mfma_f32_16x16x32_bf16 v[96:99], a[92:95], a[244:247], v[96:99]\n"
        "s_add_u32 s44, s43, 3072\n"
        "v_cvt_pk_bf16_f32 v200, v104, v105\n"
        "v_cvt_pk_bf16_f32 v201, v106, v107\n"
        "v_mfma_f32_16x16x32_bf16 v[32:35], v[152:155], v[196:199], v[32:35]\n"
        "v_mov_b64 v[206:207], v[200:201]\n"
        "v_cvt_pk_bf16_f32 v77, v26, v27\n"
        "v_mfma_f32_16x16x32_bf16 v[32:35], v[168:171], v[200:203], v[32:35]\n"
        "s_cmp_eq_u32 s44, 196608\n"
        "v_cvt_pk_bf16_f32 v212, v96, v97\n"
        "v_cvt_pk_bf16_f32 v213, v98, v99\n"
        "v_mfma_f32_16x16x32_bf16 v[36:39], v[172:175], v[196:199], v[36:39]\n"
        "s_cselect_b32 s44, 0, s44\n"
        "v_mfma_f32_16x16x32_bf16 v[36:39], v[188:191], v[204:207], v[36:39]\n"
        "s_add_u32 s36, s46, s44\n"
        "ds_read_b128 v[202:205], v252 offset:4096\n"
        "v_mfma_f32_16x16x32_bf16 v[100:103], a[116:119], a[244:247], 0\n"
        "s_add_u32 s43, s43, s46\n"
        "v_exp_f32 v32, v32\n"
        "v_mfma_f32_16x16x32_bf16 v[100:103], a[112:115], a[240:243], v[100:103]\n"
        "s_add_u32 s35, s43, 1536\n"
        "v_exp_f32 v33, v33\n"
        "v_mfma_f32_16x16x32_bf16 v[100:103], a[108:111], a[236:239], v[100:103]\n"
        "v_exp_f32 v34, v34\n"
        "v_mfma_f32_16x16x32_bf16 v[100:103], a[104:107], a[224:227], v[100:103]\n"
        "ds_read_b128 a[72:75], v248 offset:0\n"
        "v_cvt_pk_bf16_f32 v80, v32, v33\n"
        "v_mfma_f32_16x16x32_bf16 v[100:103], a[100:103], a[228:231], v[100:103]\n"
        "v_exp_f32 v35, v35\n"
        "v_mfma_f32_16x16x32_bf16 v[100:103], a[96:99], a[232:235], v[100:103]\n"
        "ds_read_b128 a[76:79], v248 offset:6144\n"
        "v_cvt_pk_bf16_f32 v81, v34, v35\n"
        "v_mfma_f32_16x16x32_bf16 v[40:43], v[192:195], v[196:199], v[40:43]\n"
        "s_waitcnt lgkmcnt(2)\n"
        "v_exp_f32 v36, v36\n"
        "v_mfma_f32_16x16x32_bf16 v[40:43], v[208:211], v[200:203], v[40:43]\n"
        "v_exp_f32 v37, v37\n"
        "v_mfma_f32_16x16x32_bf16 v[104:107], a[120:123], a[232:235], 0\n"
        "v_exp_f32 v38, v38\n"
        "v_mfma_f32_16x16x32_bf16 v[104:107], a[124:127], a[228:231], v[104:107]\n"
        "v_cvt_pk_bf16_f32 v214, v100, v101\n"
        "v_cvt_pk_bf16_f32 v215, v102, v103\n"
        "v_mfma_f32_16x16x32_bf16 v[104:107], a[128:131], a[224:227], v[104:107]\n"
        "ds_read_b128 a[80:83], v248 offset:36864\n"
        "v_cvt_pk_bf16_f32 v82, v36, v37\n"
        "v_mfma_f32_16x16x32_bf16 v[104:107], a[132:135], a[236:239], v[104:107]\n"
        "v_exp_f32 v39, v39\n"
        "v_mfma_f32_16x16x32_bf16 v[104:107], a[136:139], a[240:243], v[104:107]\n"
        "ds_read_b128 a[84:87], v248 offset:43008\n"
        "v_cvt_pk_bf16_f32 v83, v38, v39\n"
        "v_mfma_f32_16x16x32_bf16 v[104:107], a[140:143], a[244:247], v[104:107]\n"
        "v_exp_f32 v40, v40\n"
        "v_mfma_f32_16x16x32_bf16 v[96:99], a[20:23], a[244:247], 0\n"
        "v_exp_f32 v41, v41\n"
        "v_mfma_f32_16x16x32_bf16 v[96:99], a[16:19], a[240:243], v[96:99]\n"
        "v_exp_f32 v42, v42\n"
        "v_mfma_f32_16x16x32_bf16 v[96:99], a[12:15], a[236:239], v[96:99]\n"
        "ds_read_b128 a[88:91], v248 offset:12288\n"
        "v_cvt_pk_bf16_f32 v84, v40, v41\n"
        "v_mfma_f32_16x16x32_bf16 v[96:99], a[8:11], a[224:227], v[96:99]\n"
        "v_cvt_pk_bf16_f32 v230, v104, v105\n"
        "v_cvt_pk_bf16_f32 v231, v106, v107\n"
        "v_mfma_f32_16x16x32_bf16 v[96:99], a[4:7], a[228:231], v[96:99]\n"
        "v_exp_f32 v43, v43\n"
        "v_mfma_f32_16x16x32_bf16 v[96:99], a[0:3], a[232:235], v[96:99]\n"
        "ds_read_b128 a[92:95], v248 offset:49152\n"
        "v_cvt_pk_bf16_f32 v85, v42, v43\n"
        "v_mfma_f32_16x16x32_bf16 v[12:15], v[212:215], v[156:159], v[12:15]\n"
        "ds_read_b128 v[222:225], v251 offset:4608\n"
        "s_nop 0\n"
        "v_mfma_f32_16x16x32_bf16 v[12:15], v[228:231], v[164:167], v[12:15]\n"
        "ds_read_b128 a[96:99], v254 offset:6160\n"
        "v_mfma_f32_16x16x32_bf16 v[28:31], v[212:215], v[176:179], v[28:31]\n"
        "v_mul_f32 v96, s50, v96\n"
        "v_mul_f32 v97, s50, v97\n"
        "v_mfma_f32_16x16x32_bf16 v[28:31], v[228:231], v[184:187], v[28:31]\n"
        "v_mul_f32 v98, s50, v98\n"
        "v_mul_f32 v99, s50, v99\n"
        "v_mfma_f32_16x16x32_bf16 v[44:47], v[212:215], v[196:199], v[44:47]\n"
        "v_cvt_pk_bf16_f32 v216, v96, v97\n"
        "v_cvt_pk_bf16_f32 v217, v98, v99\n"
        "v_mfma_f32_16x16x32_bf16 v[44:47], v[228:231], v[204:207], v[44:47]\n"
        "v_exp_f32 v12, v12\n"
        "v_mfma_f32_16x16x32_bf16 v[100:103], a[24:27], a[232:235], 0\n"
        "v_exp_f32 v13, v13\n"
        "v_mfma_f32_16x16x32_bf16 v[100:103], a[28:31], a[228:231], v[100:103]\n"
        "v_exp_f32 v14, v14\n"
        "v_mfma_f32_16x16x32_bf16 v[100:103], a[32:35], a[224:227], v[100:103]\n"
        "ds_read_b128 a[100:103], v254 offset:16\n"
        "v_cvt_pk_bf16_f32 v70, v12, v13\n"
        "v_mfma_f32_16x16x32_bf16 v[100:103], a[36:39], a[236:239], v[100:103]\n"
        "v_exp_f32 v15, v15\n"
        "v_mfma_f32_16x16x32_bf16 v[100:103], a[40:43], a[240:243], v[100:103]\n"
        "ds_read_b128 a[104:107], v254 offset:43024\n"
        "v_cvt_pk_bf16_f32 v71, v14, v15\n"
        "v_mfma_f32_16x16x32_bf16 v[100:103], a[44:47], a[244:247], v[100:103]\n"
        "v_exp_f32 v28, v28\n"
        "v_mfma_f32_16x16x32_bf16 v[104:107], a[68:71], a[244:247], 0\n"
        "v_exp_f32 v29, v29\n"
        "v_mfma_f32_16x16x32_bf16 v[104:107], a[64:67], a[240:243], v[104:107]\n"
        "v_exp_f32 v30, v30\n"
        "v_mfma_f32_16x16x32_bf16 v[104:107], a[60:63], a[236:239], v[104:107]\n"
        "ds_read_b128 a[108:111], v254 offset:36880\n"
        "v_cvt_pk_bf16_f32 v78, v28, v29\n"
        "v_mfma_f32_16x16x32_bf16 v[104:107], a[56:59], a[224:227], v[104:107]\n"
        "v_mul_f32 v100, s50, v100\n"
        "v_mul_f32 v101, s50, v101\n"
        "v_mfma_f32_16x16x32_bf16 v[104:107], a[52:55], a[228:231], v[104:107]\n"
        "v_mul_f32 v102, s50, v102\n"
        "v_mul_f32 v103, s50, v103\n"
        "v_mfma_f32_16x16x32_bf16 v[104:107], a[48:51], a[232:235], v[104:107]\n"
        "v_cvt_pk_bf16_f32 v218, v100, v101\n"
        "v_cvt_pk_bf16_f32 v219, v102, v103\n"
        "v_mfma_f32_16x16x32_bf16 v[96:99], a[232:235], a[144:147], 0\n"
        "v_exp_f32 v31, v31\n"
        "v_mfma_f32_16x16x32_bf16 v[96:99], a[228:231], a[148:151], v[96:99]\n"
        "ds_read_b128 a[112:115], v254 offset:49168\n"
        "v_cvt_pk_bf16_f32 v79, v30, v31\n"
        "v_mfma_f32_16x16x32_bf16 v[96:99], a[224:227], a[152:155], v[96:99]\n"
        "v_mul_f32 v104, s50, v104\n"
        "v_mul_f32 v105, s50, v105\n"
        "v_mfma_f32_16x16x32_bf16 v[96:99], a[236:239], a[156:159], v[96:99]\n"
        "v_mul_f32 v106, s50, v106\n"
        "v_mul_f32 v107, s50, v107\n"
        "v_mfma_f32_16x16x32_bf16 v[96:99], a[240:243], a[160:163], v[96:99]\n"
        "v_cvt_pk_bf16_f32 v220, v104, v105\n"
        "v_cvt_pk_bf16_f32 v221, v106, v107\n"
        "v_mfma_f32_16x16x32_bf16 v[96:99], a[244:247], a[164:167], v[96:99]\n"
        "ds_read_b128 a[220:223], v240 offset:12288\n"
        "v_mov_b64 v[226:227], v[220:221]\n"
        "v_mfma_f32_16x16x32_bf16 v[48:51], v[152:155], v[216:219], v[48:51]\n"
        "s_waitcnt lgkmcnt(6)\n"
        "v_exp_f32 v44, v44\n"
        "v_mfma_f32_16x16x32_bf16 v[48:51], v[168:171], v[220:223], v[48:51]\n"
        "v_exp_f32 v45, v45\n"
        "v_mfma_f32_16x16x32_bf16 v[52:55], v[172:175], v[216:219], v[52:55]\n"
        "v_exp_f32 v46, v46\n"
        "v_mfma_f32_16x16x32_bf16 v[52:55], v[188:191], v[224:227], v[52:55]\n"
        "ds_read_b128 v[222:225], v252 offset:4608\n"
        "v_cvt_pk_bf16_f32 v134, v96, v97\n"
        "v_mfma_f32_16x16x32_bf16 v[100:103], a[244:247], a[188:191], 0\n"
        "ds_read_b128 a[216:219], v241 offset:12288\n"
        "v_cvt_pk_bf16_f32 v135, v98, v99\n"
        "v_mfma_f32_16x16x32_bf16 v[100:103], a[240:243], a[184:187], v[100:103]\n"
        "ds_read_b128 a[248:251], v240 offset:12416\n"
        "v_cvt_pk_bf16_f32 v86, v44, v45\n"
        "v_mfma_f32_16x16x32_bf16 v[100:103], a[236:239], a[180:183], v[100:103]\n"
        "v_exp_f32 v47, v47\n"
        "v_mfma_f32_16x16x32_bf16 v[100:103], a[224:227], a[176:179], v[100:103]\n"
        "ds_read_b128 a[116:119], v254 offset:12304\n"
        "v_cvt_pk_bf16_f32 v87, v46, v47\n"
        "v_mfma_f32_16x16x32_bf16 v[100:103], a[228:231], a[172:175], v[100:103]\n"
        "v_exp_f32 v48, v48\n"
        "v_mfma_f32_16x16x32_bf16 v[100:103], a[232:235], a[168:171], v[100:103]\n"
        "v_exp_f32 v49, v49\n"
        "v_mfma_f32_16x16x32_bf16 v[56:59], v[192:195], v[216:219], v[56:59]\n"
        "s_waitcnt lgkmcnt(3)\n"
        "v_exp_f32 v50, v50\n"
        "v_mfma_f32_16x16x32_bf16 v[56:59], v[208:211], v[220:223], v[56:59]\n"
        "ds_read_b128 a[120:123], v248 offset:32\n"
        "v_cvt_pk_bf16_f32 v88, v48, v49\n"
        "v_mfma_f32_16x16x32_bf16 v[60:63], v[212:215], v[216:219], v[60:63]\n"
        "v_exp_f32 v51, v51\n"
        "v_mfma_f32_16x16x32_bf16 v[60:63], v[228:231], v[224:227], v[60:63]\n"
        "v_cvt_pk_bf16_f32 v142, v100, v101\n"
        "v_cvt_pk_bf16_f32 v143, v102, v103\n"
        "v_mfma_f32_16x16x32_bf16 v[104:107], a[232:235], a[192:195], 0\n"
        "ds_read_b128 a[232:235], v241 offset:12416\n"
        "v_cvt_pk_bf16_f32 v89, v50, v51\n"
        "v_mfma_f32_16x16x32_bf16 v[104:107], a[228:231], a[196:199], v[104:107]\n"
        "ds_read_b128 a[228:231], v240 offset:12544\n"
        "v_mfma_f32_16x16x32_bf16 v[104:107], a[224:227], a[200:203], v[104:107]\n"
        "ds_read_b128 a[224:227], v241 offset:12544\n"
        "v_mfma_f32_16x16x32_bf16 v[104:107], a[236:239], a[204:207], v[104:107]\n"
        "v_exp_f32 v52, v52\n"
        "v_mfma_f32_16x16x32_bf16 v[104:107], a[240:243], a[208:211], v[104:107]\n"
        "v_exp_f32 v53, v53\n"
        "v_mfma_f32_16x16x32_bf16 v[104:107], a[244:247], a[212:215], v[104:107]\n"
        "v_exp_f32 v54, v54\n"
        "v_mfma_f32_16x16x32_bf16 v[96:99], a[220:223], a[144:147], 0\n"
        "s_waitcnt lgkmcnt(6)\n"
        "ds_read_b128 a[236:239], v240 offset:1536\n"
        "v_cvt_pk_bf16_f32 v90, v52, v53\n"
        "v_mfma_f32_16x16x32_bf16 v[96:99], a[216:219], a[148:151], v[96:99]\n"
        "s_waitcnt lgkmcnt(6)\n"
        "v_exp_f32 v55, v55\n"
        "v_mfma_f32_16x16x32_bf16 v[96:99], a[248:251], a[152:155], v[96:99]\n"
        "s_waitcnt lgkmcnt(3)\n"
        "ds_read_b128 a[240:243], v241 offset:1536\n"
        "v_cvt_pk_bf16_f32 v150, v104, v105\n"
        "v_mfma_f32_16x16x32_bf16 v[96:99], a[232:235], a[156:159], v[96:99]\n"
        "s_waitcnt lgkmcnt(3)\n"
        "ds_read_b128 a[244:247], v240 offset:1664\n"
        "v_cvt_pk_bf16_f32 v151, v106, v107\n"
        "v_mfma_f32_16x16x32_bf16 v[96:99], a[228:231], a[160:163], v[96:99]\n"
        "s_waitcnt lgkmcnt(3)\n"
        "ds_read_b128 a[124:127], v248 offset:6176\n"
        "v_cvt_pk_bf16_f32 v91, v54, v55\n"
        "v_mfma_f32_16x16x32_bf16 v[96:99], a[224:227], a[164:167], v[96:99]\n"
        "v_exp_f32 v56, v56\n"
        "v_mfma_f32_16x16x32_bf16 v[100:103], a[224:227], a[188:191], 0\n"
        "v_exp_f32 v57, v57\n"
        "v_mfma_f32_16x16x32_bf16 v[100:103], a[228:231], a[184:187], v[100:103]\n"
        "v_exp_f32 v58, v58\n"
        "v_mfma_f32_16x16x32_bf16 v[100:103], a[232:235], a[180:183], v[100:103]\n"
        "46:\n"
        "s_cbranch_execz 40b\n"
        "ds_read_b128 v[32:35], v247 offset:0\n"
        "v_cvt_pk_bf16_f32 v92, v56, v57\n"
        "v_mfma_f32_16x16x32_bf16 v[100:103], a[248:251], a[176:179], v[100:103]\n"
        "v_cvt_pk_bf16_f32 v132, v96, v97\n"
        "v_cvt_pk_bf16_f32 v133, v98, v99\n"
        "v_mfma_f32_16x16x32_bf16 v[100:103], a[216:219], a[172:175], v[100:103]\n"
        "v_exp_f32 v59, v59\n"
        "v_mfma_f32_16x16x32_bf16 v[100:103], a[220:223], a[168:171], v[100:103]\n"
        "ds_read_b128 v[156:159], v247 offset:4096\n"
        "v_cvt_pk_bf16_f32 v93, v58, v59\n"
        "v_mfma_f32_16x16x32_bf16 v[104:107], a[220:223], a[192:195], 0\n"
        "ds_read_b128 a[220:223], v241 offset:1664\n"
        "v_mfma_f32_16x16x32_bf16 v[104:107], a[216:219], a[196:199], v[104:107]\n"
        "ds_read_b128 a[216:219], v240 offset:1792\n"
        "v_mfma_f32_16x16x32_bf16 v[104:107], a[248:251], a[200:203], v[104:107]\n"
        "ds_read_b128 a[248:251], v241 offset:1792\n"
        "v_cvt_pk_bf16_f32 v140, v100, v101\n"
        "v_mfma_f32_16x16x32_bf16 v[104:107], a[232:235], a[204:207], v[104:107]\n"
        "ds_read_b128 v[160:163], v247 offset:8192\n"
        "v_cvt_pk_bf16_f32 v141, v102, v103\n"
        "v_mfma_f32_16x16x32_bf16 v[104:107], a[228:231], a[208:211], v[104:107]\n"
        "v_exp_f32 v60, v60\n"
        "v_mfma_f32_16x16x32_bf16 v[104:107], a[224:227], a[212:215], v[104:107]\n"
        "s_waitcnt lgkmcnt(9)\n"
        "v_exp_f32 v61, v61\n"
        "v_mfma_f32_16x16x32_bf16 v[96:99], a[236:239], a[144:147], 0\n"
        "s_waitcnt lgkmcnt(8)\n"
        "v_exp_f32 v62, v62\n"
        "v_mfma_f32_16x16x32_bf16 v[96:99], a[240:243], a[148:151], v[96:99]\n"
        "s_waitcnt lgkmcnt(7)\n"
        "ds_read_b128 v[164:167], v247 offset:12288\n"
        "v_cvt_pk_bf16_f32 v94, v60, v61\n"
        "v_mfma_f32_16x16x32_bf16 v[96:99], a[244:247], a[152:155], v[96:99]\n"
        "s_waitcnt lgkmcnt(4)\n"
        "v_exp_f32 v63, v63\n"
        "v_mfma_f32_16x16x32_bf16 v[96:99], a[220:223], a[156:159], v[96:99]\n"
        "s_waitcnt lgkmcnt(3)\n"
        "v_cvt_pk_bf16_f32 v148, v104, v105\n"
        "v_cvt_pk_bf16_f32 v149, v106, v107\n"
        "v_mfma_f32_16x16x32_bf16 v[96:99], a[216:219], a[160:163], v[96:99]\n"
        "s_waitcnt lgkmcnt(2)\n"
        "ds_read_b128 v[152:155], v247 offset:16384\n"
        "v_cvt_pk_bf16_f32 v95, v62, v63\n"
        "v_mfma_f32_16x16x32_bf16 v[96:99], a[248:251], a[164:167], v[96:99]\n"
        "ds_read_b128 v[168:171], v247 offset:20480\n"
        "s_nop 0\n"
        "v_mfma_f32_16x16x32_bf16 v[100:103], a[248:251], a[188:191], 0\n"
        "ds_read_b128 a[128:131], v248 offset:36896\n"
        "s_nop 0\n"
        "v_mfma_f32_16x16x32_bf16 v[100:103], a[216:219], a[184:187], v[100:103]\n"
        "ds_read_b128 a[132:135], v248 offset:43040\n"
        "v_mfma_f32_16x16x32_bf16 v[100:103], a[220:223], a[180:183], v[100:103]\n"
        "ds_read_b128 a[136:139], v248 offset:12320\n"
        "v_cvt_pk_bf16_f32 v130, v96, v97\n"
        "v_mfma_f32_16x16x32_bf16 v[100:103], a[244:247], a[176:179], v[100:103]\n"
        "ds_read_b128 a[140:143], v248 offset:49184\n"
        "v_cvt_pk_bf16_f32 v131, v98, v99\n"
        "v_mfma_f32_16x16x32_bf16 v[100:103], a[240:243], a[172:175], v[100:103]\n"
        "ds_read_b128 a[0:3], v254 offset:6192\n"
        "v_mfma_f32_16x16x32_bf16 v[100:103], a[236:239], a[168:171], v[100:103]\n"
        "ds_read_b128 a[4:7], v254 offset:48\n"
        "v_mfma_f32_16x16x32_bf16 v[104:107], a[236:239], a[192:195], 0\n"
        "ds_read_b128 a[8:11], v254 offset:43056\n"
        "v_mov_b64 v[116:117], 0\n"
        "v_mfma_f32_16x16x32_bf16 v[104:107], a[240:243], a[196:199], v[104:107]\n"
        "ds_read_b128 a[12:15], v254 offset:36912\n"
        "s_nop 0\n"
        "v_mfma_f32_16x16x32_bf16 v[104:107], a[244:247], a[200:203], v[104:107]\n"
        "ds_read_b128 a[16:19], v254 offset:49200\n"
        "v_cvt_pk_bf16_f32 v138, v100, v101\n"
        "v_mfma_f32_16x16x32_bf16 v[104:107], a[220:223], a[204:207], v[104:107]\n"
        "ds_read_b128 a[20:23], v254 offset:12336\n"
        "v_cvt_pk_bf16_f32 v139, v102, v103\n"
        "v_mfma_f32_16x16x32_bf16 v[104:107], a[216:219], a[208:211], v[104:107]\n"
        "ds_read_b128 a[24:27], v248 offset:64\n"
        "v_mfma_f32_16x16x32_bf16 v[104:107], a[248:251], a[212:215], v[104:107]\n"
        "ds_read_b128 a[28:31], v248 offset:6208\n"
        "v_mfma_f32_16x16x32_bf16 v[172:175], v[32:35], a[72:75], 0\n"
        "ds_read_b128 a[32:35], v248 offset:36928\n"
        "v_mfma_f32_16x16x32_bf16 v[172:175], v[156:159], a[76:79], v[172:175]\n"
        "s_waitcnt lgkmcnt(15)\n"
        "ds_read_b128 a[36:39], v248 offset:43072\n"
        "v_mfma_f32_16x16x32_bf16 v[172:175], v[160:163], a[80:83], v[172:175]\n"
        "s_waitcnt lgkmcnt(15)\n"
        "ds_read_b128 a[40:43], v248 offset:12352\n"
        "v_mfma_f32_16x16x32_bf16 v[172:175], v[164:167], a[84:87], v[172:175]\n"
        "s_waitcnt lgkmcnt(15)\n"
        "v_cvt_pk_bf16_f32 v146, v104, v105\n"
        "v_cvt_pk_bf16_f32 v147, v106, v107\n"
        "v_mfma_f32_16x16x32_bf16 v[172:175], v[152:155], a[88:91], v[172:175]\n"
        "s_waitcnt lgkmcnt(15)\n"
        "ds_read_b128 a[44:47], v248 offset:49216\n"
        "v_mfma_f32_16x16x32_bf16 v[172:175], v[168:171], a[92:95], v[172:175]\n"
        "ds_read_b128 a[48:51], v254 offset:6224\n"
        "v_smfmac_f32_16x16x64_bf16 v[116:119], a[252:255], v[64:71], v255\n"
        "ds_read_b128 a[52:55], v254 offset:80\n"
        "v_mfma_f32_16x16x32_bf16 v[120:123], v[128:131], v[64:67], 0\n"
        "ds_read_b128 a[56:59], v254 offset:43088\n"
        "v_mfma_f32_16x16x32_bf16 v[120:123], v[132:135], v[68:71], v[120:123]\n"
        "ds_read_b128 a[60:63], v254 offset:36944\n"
        "v_mfma_f32_16x16x32_bf16 v[124:127], v[140:143], v[68:71], 0\n"
        "ds_read_b128 a[64:67], v254 offset:49232\n"
        "v_mfma_f32_16x16x32_bf16 v[124:127], v[136:139], v[64:67], v[124:127]\n"
        "ds_read_b128 a[68:71], v254 offset:12368\n"
        "v_add_f32 v116, v116, v117\n"
        "v_mfma_f32_16x16x32_bf16 v[232:235], v[144:147], v[64:67], 0\n"
        "v_rcp_f32 v117, v116\n"
        "v_mfma_f32_16x16x32_bf16 v[232:235], v[148:151], v[68:71], v[232:235]\n"
        "v_mul_f32 v120, v117, v120\n"
        "v_mul_f32 v121, v117, v121\n"
        "v_mfma_f32_16x16x32_bf16 v[176:179], v[156:159], a[96:99], 0\n"
        "v_mul_f32 v122, v117, v122\n"
        "v_mul_f32 v123, v117, v123\n"
        "v_mfma_f32_16x16x32_bf16 v[176:179], v[32:35], a[100:103], v[176:179]\n"
        "v_cvt_pk_bf16_f32 v120, v120, v121\n"
        "v_cvt_pk_bf16_f32 v121, v122, v123\n"
        "v_mfma_f32_16x16x32_bf16 v[176:179], v[164:167], a[104:107], v[176:179]\n"
        "ds_write_b32 v245, v120 offset:0\n"
        "v_mfma_f32_16x16x32_bf16 v[176:179], v[160:163], a[108:111], v[176:179]\n"
        "ds_write_b32 v245, v121 offset:4\n"
        "v_mfma_f32_16x16x32_bf16 v[176:179], v[168:171], a[112:115], v[176:179]\n"
        "v_mul_f32 v124, v117, v124\n"
        "v_mul_f32 v125, v117, v125\n"
        "v_mfma_f32_16x16x32_bf16 v[176:179], v[152:155], a[116:119], v[176:179]\n"
        "v_maximum3_f32 v253, v253, v116, v117\n"
        "v_mul_f32 v126, v117, v126\n"
        "v_mfma_f32_16x16x32_bf16 v[180:183], v[32:35], a[120:123], 0\n"
        "v_mov_b64 v[100:101], 0\n"
        "v_mul_f32 v127, v117, v127\n"
        "v_mfma_f32_16x16x32_bf16 v[180:183], v[156:159], a[124:127], v[180:183]\n"
        "s_waitcnt lgkmcnt(15)\n"
        "ds_read_b128 v[0:3], v247 offset:256\n"
        "v_cvt_pk_bf16_f32 v124, v124, v125\n"
        "v_mfma_f32_16x16x32_bf16 v[180:183], v[160:163], a[128:131], v[180:183]\n"
        "s_waitcnt lgkmcnt(15)\n"
        "ds_read_b128 v[4:7], v247 offset:4352\n"
        "v_cvt_pk_bf16_f32 v125, v126, v127\n"
        "v_mfma_f32_16x16x32_bf16 v[180:183], v[164:167], a[132:135], v[180:183]\n"
        "s_waitcnt lgkmcnt(15)\n"
        "ds_write_b32 v245, v124 offset:8\n"
        "v_mfma_f32_16x16x32_bf16 v[180:183], v[152:155], a[136:139], v[180:183]\n"
        "s_waitcnt lgkmcnt(15)\n"
        "ds_write_b32 v245, v125 offset:12\n"
        "v_mfma_f32_16x16x32_bf16 v[180:183], v[168:171], a[140:143], v[180:183]\n"
        "s_waitcnt lgkmcnt(15)\n"
        "ds_read_b128 v[8:11], v247 offset:8448\n"
        "v_mul_f32 v232, v117, v232\n"
        "v_mfma_f32_16x16x32_bf16 v[184:187], v[156:159], a[0:3], 0\n"
        "s_waitcnt lgkmcnt(15)\n"
        "ds_read_b128 v[16:19], v247 offset:12544\n"
        "v_mul_f32 v233, v117, v233\n"
        "v_mfma_f32_16x16x32_bf16 v[184:187], v[32:35], a[4:7], v[184:187]\n"
        "s_waitcnt lgkmcnt(15)\n"
        "ds_read_b128 v[20:23], v247 offset:16640\n"
        "v_mul_f32 v234, v117, v234\n"
        "v_mfma_f32_16x16x32_bf16 v[184:187], v[164:167], a[8:11], v[184:187]\n"
        "s_waitcnt lgkmcnt(15)\n"
        "ds_read_b128 v[12:15], v247 offset:20736\n"
        "v_mul_f32 v235, v117, v235\n"
        "v_mfma_f32_16x16x32_bf16 v[184:187], v[160:163], a[12:15], v[184:187]\n"
        "s_waitcnt lgkmcnt(15)\n"
        "v_cvt_pk_bf16_f32 v232, v232, v233\n"
        "v_cvt_pk_bf16_f32 v233, v234, v235\n"
        "v_mfma_f32_16x16x32_bf16 v[184:187], v[168:171], a[16:19], v[184:187]\n"
        "s_waitcnt lgkmcnt(15)\n"
        "ds_write_b32 v246, v232 offset:0\n"
        "v_mfma_f32_16x16x32_bf16 v[184:187], v[152:155], a[20:23], v[184:187]\n"
        "v_cvt_pk_bf16_f32 v188, v172, v176\n"
        "v_cvt_pk_bf16_f32 v208, v173, v177\n"
        "v_smfmac_f32_16x16x64_bf16 v[100:103], a[252:255], v[80:87], v255\n"
        "ds_write_b32 v246, v233 offset:4\n"
        "v_mfma_f32_16x16x32_bf16 v[104:107], v[128:131], v[80:83], 0\n"
        "v_cvt_pk_bf16_f32 v228, v174, v178\n"
        "s_nop 0\n"
        "v_mfma_f32_16x16x32_bf16 v[104:107], v[132:135], v[84:87], v[104:107]\n"
        "v_cvt_pk_bf16_f32 v189, v180, v184\n"
        "v_cvt_pk_bf16_f32 v209, v181, v185\n"
        "v_mfma_f32_16x16x32_bf16 v[108:111], v[140:143], v[84:87], 0\n"
        "buffer_store_dwordx2 v[188:189], v242, s[16:19], s38 offen offset:0 sc1\n"
        "v_mfma_f32_16x16x32_bf16 v[108:111], v[136:139], v[80:83], v[108:111]\n"
        "v_cvt_pk_bf16_f32 v229, v182, v186\n"
        "v_cvt_pk_bf16_f32 v236, v175, v179\n"
        "v_mfma_f32_16x16x32_bf16 v[112:115], v[144:147], v[80:83], 0\n"
        "v_cvt_pk_bf16_f32 v237, v183, v187\n"
        "v_add_f32 v100, v100, v101\n"
        "v_mfma_f32_16x16x32_bf16 v[112:115], v[148:151], v[84:87], v[112:115]\n"
        "s_waitcnt lgkmcnt(15)\n"
        "buffer_store_dwordx2 v[208:209], v242, s[16:19], s38 offen offset:384 sc1\n"
        "v_mfma_f32_16x16x32_bf16 v[192:195], v[32:35], a[24:27], 0\n"
        "s_waitcnt lgkmcnt(15)\n"
        "v_rcp_f32 v101, v100\n"
        "v_mfma_f32_16x16x32_bf16 v[192:195], v[156:159], a[28:31], v[192:195]\n"
        "s_waitcnt lgkmcnt(15)\n"
        "v_mul_f32 v104, v101, v104\n"
        "v_mul_f32 v105, v101, v105\n"
        "v_mfma_f32_16x16x32_bf16 v[192:195], v[160:163], a[32:35], v[192:195]\n"
        "s_waitcnt lgkmcnt(15)\n"
        "buffer_store_dwordx2 v[228:229], v242, s[16:19], s38 offen offset:768 sc1\n"
        "v_mfma_f32_16x16x32_bf16 v[192:195], v[164:167], a[36:39], v[192:195]\n"
        "s_waitcnt lgkmcnt(15)\n"
        "v_mul_f32 v106, v101, v106\n"
        "v_mul_f32 v107, v101, v107\n"
        "v_mfma_f32_16x16x32_bf16 v[192:195], v[152:155], a[40:43], v[192:195]\n"
        "s_waitcnt lgkmcnt(15)\n"
        "v_cvt_pk_bf16_f32 v104, v104, v105\n"
        "v_cvt_pk_bf16_f32 v105, v106, v107\n"
        "v_mfma_f32_16x16x32_bf16 v[192:195], v[168:171], a[44:47], v[192:195]\n"
        "s_waitcnt lgkmcnt(15)\n"
        "buffer_store_dwordx2 v[236:237], v242, s[16:19], s38 offen offset:1152 sc1\n"
        "v_mfma_f32_16x16x32_bf16 v[196:199], v[156:159], a[48:51], 0\n"
        "s_waitcnt lgkmcnt(15)\n"
        "v_mov_b64 v[48:49], 0\n"
        "v_mul_f32 v108, v101, v108\n"
        "v_mfma_f32_16x16x32_bf16 v[196:199], v[32:35], a[52:55], v[196:199]\n"
        "s_waitcnt lgkmcnt(15)\n"
        "ds_write_b32 v245, v104 offset:512\n"
        "v_mfma_f32_16x16x32_bf16 v[196:199], v[164:167], a[56:59], v[196:199]\n"
        "s_waitcnt lgkmcnt(15)\n"
        "ds_write_b32 v245, v105 offset:516\n"
        "v_mfma_f32_16x16x32_bf16 v[196:199], v[160:163], a[60:63], v[196:199]\n"
        "s_waitcnt lgkmcnt(15)\n"
        "v_mul_f32 v109, v101, v109\n"
        "v_maximum3_f32 v253, v253, v100, v101\n"
        "v_mfma_f32_16x16x32_bf16 v[196:199], v[168:171], a[64:67], v[196:199]\n"
        "s_waitcnt lgkmcnt(14)\n"
        "v_mul_f32 v110, v101, v110\n"
        "v_mul_f32 v111, v101, v111\n"
        "v_mfma_f32_16x16x32_bf16 v[196:199], v[152:155], a[68:71], v[196:199]\n"
        "s_waitcnt lgkmcnt(10)\n"
        "v_cvt_pk_bf16_f32 v108, v108, v109\n"
        "v_cvt_pk_bf16_f32 v109, v110, v111\n"
        "v_mfma_f32_16x16x32_bf16 v[200:203], v[4:7], a[76:79], 0\n"
        "ds_write_b32 v245, v108 offset:520\n"
        "v_mfma_f32_16x16x32_bf16 v[200:203], v[0:3], a[72:75], v[200:203]\n"
        "s_waitcnt lgkmcnt(7)\n"
        "ds_write_b32 v245, v109 offset:524\n"
        "v_mfma_f32_16x16x32_bf16 v[200:203], v[16:19], a[84:87], v[200:203]\n"
        "v_mul_f32 v112, v101, v112\n"
        "v_cvt_pk_bf16_f32 v190, v192, v196\n"
        "v_mfma_f32_16x16x32_bf16 v[200:203], v[8:11], a[80:83], v[200:203]\n"
        "s_waitcnt lgkmcnt(6)\n"
        "buffer_store_dword v190, v250, s[16:19], s38 offen offset:0 sc1\n"
        "v_mfma_f32_16x16x32_bf16 v[200:203], v[12:15], a[92:95], v[200:203]\n"
        "v_mul_f32 v113, v101, v113\n"
        "v_cvt_pk_bf16_f32 v210, v193, v197\n"
        "v_mfma_f32_16x16x32_bf16 v[200:203], v[20:23], a[88:91], v[200:203]\n"
        "v_mul_f32 v114, v101, v114\n"
        "v_mul_f32 v115, v101, v115\n"
        "v_smfmac_f32_16x16x64_bf16 v[48:51], a[252:255], v[72:79], v255\n"
        "buffer_store_dword v210, v250, s[16:19], s38 offen offset:384 sc1\n"
        "v_mfma_f32_16x16x32_bf16 v[52:55], v[128:131], v[72:75], 0\n"
        "v_cvt_pk_bf16_f32 v112, v112, v113\n"
        "v_cvt_pk_bf16_f32 v113, v114, v115\n"
        "v_mfma_f32_16x16x32_bf16 v[52:55], v[132:135], v[76:79], v[52:55]\n"
        "ds_write_b32 v246, v112 offset:512\n"
        "v_mfma_f32_16x16x32_bf16 v[56:59], v[140:143], v[76:79], 0\n"
        "v_cvt_pk_bf16_f32 v230, v194, v198\n"
        "v_cvt_pk_bf16_f32 v238, v195, v199\n"
        "v_mfma_f32_16x16x32_bf16 v[56:59], v[136:139], v[72:75], v[56:59]\n"
        "buffer_store_dword v230, v250, s[16:19], s38 offen offset:768 sc1\n"
        "v_mfma_f32_16x16x32_bf16 v[60:63], v[144:147], v[72:75], 0\n"
        "ds_write_b32 v246, v113 offset:516\n"
        "v_mfma_f32_16x16x32_bf16 v[60:63], v[148:151], v[76:79], v[60:63]\n"
        "buffer_store_dword v238, v250, s[16:19], s38 offen offset:1152 sc1\n"
        "v_mfma_f32_16x16x32_bf16 v[204:207], v[0:3], a[100:103], 0\n"
        "v_add_f32 v48, v48, v49\n"
        "v_mfma_f32_16x16x32_bf16 v[204:207], v[4:7], a[96:99], v[204:207]\n"
        "v_rcp_f32 v49, v48\n"
        "v_mfma_f32_16x16x32_bf16 v[204:207], v[8:11], a[108:111], v[204:207]\n"
        "v_mul_f32 v52, v49, v52\n"
        "v_mul_f32 v53, v49, v53\n"
        "v_mfma_f32_16x16x32_bf16 v[204:207], v[16:19], a[104:107], v[204:207]\n"
        "v_mul_f32 v54, v49, v54\n"
        "v_mul_f32 v55, v49, v55\n"
        "v_mfma_f32_16x16x32_bf16 v[204:207], v[20:23], a[116:119], v[204:207]\n"
        "v_cvt_pk_bf16_f32 v52, v52, v53\n"
        "v_cvt_pk_bf16_f32 v53, v54, v55\n"
        "v_mfma_f32_16x16x32_bf16 v[204:207], v[12:15], a[112:115], v[204:207]\n"
        "ds_write_b32 v245, v52 offset:256\n"
        "v_mfma_f32_16x16x32_bf16 v[212:215], v[4:7], a[124:127], 0\n"
        "v_mov_b64 v[64:65], 0\n"
        "v_mul_f32 v56, v49, v56\n"
        "v_mfma_f32_16x16x32_bf16 v[212:215], v[0:3], a[120:123], v[212:215]\n"
        "ds_write_b32 v245, v53 offset:260\n"
        "v_mfma_f32_16x16x32_bf16 v[212:215], v[16:19], a[132:135], v[212:215]\n"
        "v_mul_f32 v57, v49, v57\n"
        "v_maximum3_f32 v253, v253, v48, v49\n"
        "v_mfma_f32_16x16x32_bf16 v[212:215], v[8:11], a[128:131], v[212:215]\n"
        "v_mul_f32 v58, v49, v58\n"
        "v_mul_f32 v59, v49, v59\n"
        "v_mfma_f32_16x16x32_bf16 v[212:215], v[12:15], a[140:143], v[212:215]\n"
        "v_cvt_pk_bf16_f32 v56, v56, v57\n"
        "v_cvt_pk_bf16_f32 v57, v58, v59\n"
        "v_mfma_f32_16x16x32_bf16 v[212:215], v[20:23], a[136:139], v[212:215]\n"
        "ds_write_b32 v245, v56 offset:264\n"
        "v_mfma_f32_16x16x32_bf16 v[216:219], v[0:3], a[4:7], 0\n"
        "ds_write_b32 v245, v57 offset:268\n"
        "v_mfma_f32_16x16x32_bf16 v[216:219], v[4:7], a[0:3], v[216:219]\n"
        "v_mul_f32 v60, v49, v60\n"
        "v_mul_f32 v61, v49, v61\n"
        "v_mfma_f32_16x16x32_bf16 v[216:219], v[8:11], a[12:15], v[216:219]\n"
        "v_mul_f32 v62, v49, v62\n"
        "v_mul_f32 v63, v49, v63\n"
        "v_mfma_f32_16x16x32_bf16 v[216:219], v[16:19], a[8:11], v[216:219]\n"
        "v_cvt_pk_bf16_f32 v60, v60, v61\n"
        "v_cvt_pk_bf16_f32 v61, v62, v63\n"
        "v_mfma_f32_16x16x32_bf16 v[216:219], v[20:23], a[20:23], v[216:219]\n"
        "ds_write_b32 v246, v60 offset:256\n"
        "v_mfma_f32_16x16x32_bf16 v[216:219], v[12:15], a[16:19], v[216:219]\n"
        "v_cvt_pk_bf16_f32 v188, v200, v204\n"
        "v_cvt_pk_bf16_f32 v208, v201, v205\n"
        "v_smfmac_f32_16x16x64_bf16 v[64:67], a[252:255], v[88:95], v255\n"
        "ds_write_b32 v246, v61 offset:260\n"
        "v_mfma_f32_16x16x32_bf16 v[68:71], v[128:131], v[88:91], 0\n"
        "v_cvt_pk_bf16_f32 v228, v202, v206\n"
        "s_nop 0\n"
        "v_mfma_f32_16x16x32_bf16 v[68:71], v[132:135], v[92:95], v[68:71]\n"
        "s_waitcnt lgkmcnt(6)\n"
        "v_cvt_pk_bf16_f32 v189, v212, v216\n"
        "v_cvt_pk_bf16_f32 v209, v213, v217\n"
        "v_mfma_f32_16x16x32_bf16 v[80:83], v[140:143], v[92:95], 0\n"
        "s_barrier\n"
        "buffer_store_dwordx2 v[188:189], v242, s[16:19], s39 offen offset:0 sc1\n"
        "v_mfma_f32_16x16x32_bf16 v[80:83], v[136:139], v[88:91], v[80:83]\n"
        "ds_read_b128 a[168:171], v243 offset:0\n"
        "v_cvt_pk_bf16_f32 v229, v214, v218\n"
        "v_mfma_f32_16x16x32_bf16 v[84:87], v[144:147], v[88:91], 0\n"
        "ds_read_b128 a[172:175], v243 offset:4096\n"
        "v_cvt_pk_bf16_f32 v236, v203, v207\n"
        "v_mfma_f32_16x16x32_bf16 v[84:87], v[148:151], v[92:95], v[84:87]\n"
        "buffer_store_dwordx2 v[208:209], v242, s[16:19], s39 offen offset:384 sc1\n"
        "v_mfma_f32_16x16x32_bf16 v[220:223], v[4:7], a[28:31], 0\n"
        "ds_read_b128 a[176:179], v243 offset:8192\n"
        "v_cvt_pk_bf16_f32 v237, v215, v219\n"
        "v_mfma_f32_16x16x32_bf16 v[220:223], v[0:3], a[24:27], v[220:223]\n"
        "ds_read_b128 a[180:183], v243 offset:12288\n"
        "v_add_f32 v64, v64, v65\n"
        "v_mfma_f32_16x16x32_bf16 v[220:223], v[16:19], a[36:39], v[220:223]\n"
        "buffer_store_dwordx2 v[228:229], v242, s[16:19], s39 offen offset:768 sc1\n"
        "v_mfma_f32_16x16x32_bf16 v[220:223], v[8:11], a[32:35], v[220:223]\n"
        "ds_read_b128 a[184:187], v243 offset:16384\n"
        "v_mfma_f32_16x16x32_bf16 v[220:223], v[12:15], a[44:47], v[220:223]\n"
        "v_rcp_f32 v65, v64\n"
        "v_mfma_f32_16x16x32_bf16 v[220:223], v[20:23], a[40:43], v[220:223]\n"
        "buffer_store_dwordx2 v[236:237], v242, s[16:19], s39 offen offset:1152 sc1\n"
        "v_mfma_f32_16x16x32_bf16 v[224:227], v[0:3], a[52:55], 0\n"
        "ds_read_b128 a[188:191], v243 offset:20480\n"
        "v_mul_f32 v68, v65, v68\n"
        "v_mfma_f32_16x16x32_bf16 v[224:227], v[4:7], a[48:51], v[224:227]\n"
        "v_mul_f32 v69, v65, v69\n"
        "v_mul_f32 v70, v65, v70\n"
        "v_mfma_f32_16x16x32_bf16 v[224:227], v[8:11], a[60:63], v[224:227]\n"
        "v_mul_f32 v71, v65, v71\n"
        "v_cvt_pk_bf16_f32 v68, v68, v69\n"
        "v_mfma_f32_16x16x32_bf16 v[224:227], v[16:19], a[56:59], v[224:227]\n"
        "v_cvt_pk_bf16_f32 v69, v70, v71\n"
        "v_mul_f32 v80, v65, v80\n"
        "v_mfma_f32_16x16x32_bf16 v[224:227], v[20:23], a[68:71], v[224:227]\n"
        "ds_write_b32 v245, v68 offset:768\n"
        "v_mfma_f32_16x16x32_bf16 v[224:227], v[12:15], a[64:67], v[224:227]\n"
        "s_waitcnt lgkmcnt(6)\n"
        "ds_write_b32 v245, v69 offset:772\n"
        "v_mfma_f32_16x16x32_bf16 v[24:27], a[168:171], a[72:75], 0\n"
        "s_waitcnt lgkmcnt(6)\n"
        "v_mul_f32 v81, v65, v81\n"
        "v_maximum3_f32 v253, v253, v64, v65\n"
        "v_mfma_f32_16x16x32_bf16 v[24:27], a[172:175], a[76:79], v[24:27]\n"
        "s_waitcnt lgkmcnt(5)\n"
        "v_mul_f32 v82, v65, v82\n"
        "v_mul_f32 v83, v65, v83\n"
        "v_mfma_f32_16x16x32_bf16 v[24:27], a[176:179], a[80:83], v[24:27]\n"
        "s_waitcnt lgkmcnt(4)\n"
        "v_cvt_pk_bf16_f32 v80, v80, v81\n"
        "v_cvt_pk_bf16_f32 v81, v82, v83\n"
        "v_mfma_f32_16x16x32_bf16 v[24:27], a[180:183], a[84:87], v[24:27]\n"
        "s_waitcnt lgkmcnt(3)\n"
        "v_cvt_pk_bf16_f32 v190, v220, v224\n"
        "v_mul_f32 v84, v65, v84\n"
        "v_mfma_f32_16x16x32_bf16 v[24:27], a[184:187], a[88:91], v[24:27]\n"
        "s_waitcnt lgkmcnt(2)\n"
        "buffer_store_dword v190, v250, s[16:19], s39 offen offset:0 sc1\n"
        "v_mfma_f32_16x16x32_bf16 v[24:27], a[188:191], a[92:95], v[24:27]\n"
        "ds_write_b32 v245, v80 offset:776\n"
        "v_mfma_f32_16x16x32_bf16 v[28:31], a[172:175], a[96:99], 0\n"
        "ds_write_b32 v245, v81 offset:780\n"
        "v_mfma_f32_16x16x32_bf16 v[28:31], a[168:171], a[100:103], v[28:31]\n"
        "v_mul_f32 v85, v65, v85\n"
        "v_cvt_pk_bf16_f32 v210, v221, v225\n"
        "v_mfma_f32_16x16x32_bf16 v[28:31], a[180:183], a[104:107], v[28:31]\n"
        "buffer_store_dword v210, v250, s[16:19], s39 offen offset:384 sc1\n"
        "v_mfma_f32_16x16x32_bf16 v[28:31], a[176:179], a[108:111], v[28:31]\n"
        "v_mul_f32 v86, v65, v86\n"
        "v_mul_f32 v87, v65, v87\n"
        "v_mfma_f32_16x16x32_bf16 v[28:31], a[188:191], a[112:115], v[28:31]\n"
        "v_cvt_pk_bf16_f32 v84, v84, v85\n"
        "v_cvt_pk_bf16_f32 v85, v86, v87\n"
        "v_mfma_f32_16x16x32_bf16 v[28:31], a[184:187], a[116:119], v[28:31]\n"
        "ds_write_b32 v246, v84 offset:768\n"
        "v_mfma_f32_16x16x32_bf16 v[32:35], a[168:171], a[120:123], 0\n"
        "v_cvt_pk_bf16_f32 v230, v222, v226\n"
        "v_cvt_pk_bf16_f32 v238, v223, v227\n"
        "v_mfma_f32_16x16x32_bf16 v[32:35], a[172:175], a[124:127], v[32:35]\n"
        "buffer_store_dword v230, v250, s[16:19], s39 offen offset:768 sc1\n"
        "v_mfma_f32_16x16x32_bf16 v[32:35], a[176:179], a[128:131], v[32:35]\n"
        "buffer_store_dword v238, v250, s[16:19], s39 offen offset:1152 sc1\n"
        "v_mfma_f32_16x16x32_bf16 v[32:35], a[180:183], a[132:135], v[32:35]\n"
        "ds_write_b32 v246, v85 offset:772\n"
        "v_mfma_f32_16x16x32_bf16 v[32:35], a[184:187], a[136:139], v[32:35]\n"
        "ds_write_b32 v244, v253\n"
        "v_mfma_f32_16x16x32_bf16 v[32:35], a[188:191], a[140:143], v[32:35]\n"
        "v_cvt_pk_bf16_f32 v172, v24, v28\n"
        "v_mfma_f32_16x16x32_bf16 v[36:39], a[172:175], a[0:3], 0\n"
        "v_mfma_f32_16x16x32_bf16 v[36:39], a[168:171], a[4:7], v[36:39]\n"
        "v_mfma_f32_16x16x32_bf16 v[36:39], a[180:183], a[8:11], v[36:39]\n"
        "v_cvt_pk_bf16_f32 v176, v25, v29\n"
        "v_mfma_f32_16x16x32_bf16 v[36:39], a[176:179], a[12:15], v[36:39]\n"
        "v_mfma_f32_16x16x32_bf16 v[36:39], a[188:191], a[16:19], v[36:39]\n"
        "s_waitcnt lgkmcnt(0)\n"
        "v_mfma_f32_16x16x32_bf16 v[36:39], a[184:187], a[20:23], v[36:39]\n"
        "s_barrier\n"
        "ds_read_b128 a[192:195], v243 offset:256\n"
        "v_cvt_pk_bf16_f32 v180, v26, v30\n"
        "v_mfma_f32_16x16x32_bf16 v[40:43], a[168:171], a[24:27], 0\n"
        "ds_read_b128 a[196:199], v243 offset:4352\n"
        "s_nop 0\n"
        "v_mfma_f32_16x16x32_bf16 v[40:43], a[172:175], a[28:31], v[40:43]\n"
        "ds_read_b128 a[200:203], v243 offset:8448\n"
        "v_cvt_pk_bf16_f32 v173, v32, v36\n"
        "v_mfma_f32_16x16x32_bf16 v[40:43], a[176:179], a[32:35], v[40:43]\n"
        "ds_read_b128 a[204:207], v243 offset:12544\n"
        "v_cvt_pk_bf16_f32 v177, v33, v37\n"
        "v_mfma_f32_16x16x32_bf16 v[40:43], a[180:183], a[36:39], v[40:43]\n"
        "buffer_store_dwordx2 v[172:173], v242, s[16:19], s35 offen offset:0 sc1\n"
        "v_mfma_f32_16x16x32_bf16 v[40:43], a[184:187], a[40:43], v[40:43]\n"
        "ds_read_b128 a[208:211], v243 offset:16640\n"
        "v_cvt_pk_bf16_f32 v181, v34, v38\n"
        "v_mfma_f32_16x16x32_bf16 v[40:43], a[188:191], a[44:47], v[40:43]\n"
        "ds_read_b128 a[212:215], v243 offset:20736\n"
        "v_cvt_pk_bf16_f32 v184, v27, v31\n"
        "v_mfma_f32_16x16x32_bf16 v[44:47], a[172:175], a[48:51], 0\n"
        "buffer_store_dwordx2 v[176:177], v242, s[16:19], s35 offen offset:384 sc1\n"
        "v_mfma_f32_16x16x32_bf16 v[44:47], a[168:171], a[52:55], v[44:47]\n"
        "ds_read_b128 v[100:103], v249\n"
        "v_cvt_pk_bf16_f32 v185, v35, v39\n"
        "v_mfma_f32_16x16x32_bf16 v[44:47], a[180:183], a[56:59], v[44:47]\n"
        "v_mfma_f32_16x16x32_bf16 v[44:47], a[176:179], a[60:63], v[44:47]\n"
        "buffer_store_dwordx2 v[180:181], v242, s[16:19], s35 offen offset:768 sc1\n"
        "v_mfma_f32_16x16x32_bf16 v[44:47], a[188:191], a[64:67], v[44:47]\n"
        "v_mfma_f32_16x16x32_bf16 v[44:47], a[184:187], a[68:71], v[44:47]\n"
        "s_waitcnt lgkmcnt(5)\n"
        "s_nop 1\n"
        "v_mfma_f32_16x16x32_bf16 v[0:3], a[196:199], a[76:79], 0\n"
        "buffer_store_dwordx2 v[184:185], v242, s[16:19], s35 offen offset:1152 sc1\n"
        "v_mfma_f32_16x16x32_bf16 v[0:3], a[192:195], a[72:75], v[0:3]\n"
        "s_waitcnt lgkmcnt(3)\n"
        "s_nop 1\n"
        "v_mfma_f32_16x16x32_bf16 v[0:3], a[204:207], a[84:87], v[0:3]\n"
        "v_cvt_pk_bf16_f32 v174, v40, v44\n"
        "v_cvt_pk_bf16_f32 v178, v41, v45\n"
        "v_mfma_f32_16x16x32_bf16 v[0:3], a[200:203], a[80:83], v[0:3]\n"
        "s_waitcnt lgkmcnt(1)\n"
        "buffer_store_dword v174, v250, s[16:19], s35 offen offset:0 sc1\n"
        "v_mfma_f32_16x16x32_bf16 v[0:3], a[212:215], a[92:95], v[0:3]\n"
        "v_cvt_pk_bf16_f32 v182, v42, v46\n"
        "v_cvt_pk_bf16_f32 v186, v43, v47\n"
        "v_mfma_f32_16x16x32_bf16 v[0:3], a[208:211], a[88:91], v[0:3]\n"
        "v_mfma_f32_16x16x32_bf16 v[4:7], a[192:195], a[100:103], 0\n"
        "buffer_store_dword v178, v250, s[16:19], s35 offen offset:384 sc1\n"
        "v_mfma_f32_16x16x32_bf16 v[4:7], a[196:199], a[96:99], v[4:7]\n"
        "s_waitcnt lgkmcnt(0)\n"
        "v_maximum3_f32 v100, v100, v101, v102\n"
        "v_maximum3_f32 v100, v100, v103, v103\n"
        "v_mfma_f32_16x16x32_bf16 v[4:7], a[200:203], a[108:111], v[4:7]\n"
        "v_cmp_nge_f32 vcc, s25, v100\n"
        "v_mfma_f32_16x16x32_bf16 v[4:7], a[204:207], a[104:107], v[4:7]\n"
        "buffer_store_dword v182, v250, s[16:19], s35 offen offset:768 sc1\n"
        "v_mfma_f32_16x16x32_bf16 v[4:7], a[208:211], a[116:119], v[4:7]\n"
        "v_mfma_f32_16x16x32_bf16 v[4:7], a[212:215], a[112:115], v[4:7]\n"
        "v_mfma_f32_16x16x32_bf16 v[8:11], a[196:199], a[124:127], 0\n"
        "buffer_store_dword v186, v250, s[16:19], s35 offen offset:1152 sc1\n"
        "v_mfma_f32_16x16x32_bf16 v[8:11], a[192:195], a[120:123], v[8:11]\n"
        "v_mfma_f32_16x16x32_bf16 v[8:11], a[204:207], a[132:135], v[8:11]\n"
        "v_mfma_f32_16x16x32_bf16 v[8:11], a[200:203], a[128:131], v[8:11]\n"
        "v_mfma_f32_16x16x32_bf16 v[8:11], a[212:215], a[140:143], v[8:11]\n"
        "v_mfma_f32_16x16x32_bf16 v[8:11], a[208:211], a[136:139], v[8:11]\n"
        "v_mfma_f32_16x16x32_bf16 v[12:15], a[192:195], a[4:7], 0\n"
        "v_cvt_pk_bf16_f32 v192, v0, v4\n"
        "v_mfma_f32_16x16x32_bf16 v[12:15], a[196:199], a[0:3], v[12:15]\n"
        "v_cvt_pk_bf16_f32 v196, v1, v5\n"
        "v_mfma_f32_16x16x32_bf16 v[12:15], a[200:203], a[12:15], v[12:15]\n"
        "v_mfma_f32_16x16x32_bf16 v[12:15], a[204:207], a[8:11], v[12:15]\n"
        "v_cvt_pk_bf16_f32 v200, v2, v6\n"
        "v_mfma_f32_16x16x32_bf16 v[12:15], a[208:211], a[20:23], v[12:15]\n"
        "v_mfma_f32_16x16x32_bf16 v[12:15], a[212:215], a[16:19], v[12:15]\n"
        "v_cvt_pk_bf16_f32 v204, v3, v7\n"
        "s_nop 0\n"
        "v_mfma_f32_16x16x32_bf16 v[16:19], a[196:199], a[28:31], 0\n"
        "s_nop 2\n"
        "v_mfma_f32_16x16x32_bf16 v[16:19], a[192:195], a[24:27], v[16:19]\n"
        "v_mfma_f32_16x16x32_bf16 v[16:19], a[204:207], a[36:39], v[16:19]\n"
        "v_cvt_pk_bf16_f32 v193, v8, v12\n"
        "v_cvt_pk_bf16_f32 v197, v9, v13\n"
        "v_mfma_f32_16x16x32_bf16 v[16:19], a[200:203], a[32:35], v[16:19]\n"
        "buffer_store_dwordx2 v[192:193], v242, s[16:19], s36 offen offset:0 sc1\n"
        "v_mfma_f32_16x16x32_bf16 v[16:19], a[212:215], a[44:47], v[16:19]\n"
        "v_cvt_pk_bf16_f32 v201, v10, v14\n"
        "v_cvt_pk_bf16_f32 v205, v11, v15\n"
        "v_mfma_f32_16x16x32_bf16 v[16:19], a[208:211], a[40:43], v[16:19]\n"
        "buffer_store_dwordx2 v[196:197], v242, s[16:19], s36 offen offset:384 sc1\n"
        "v_mfma_f32_16x16x32_bf16 v[20:23], a[192:195], a[52:55], 0\n"
        "v_mfma_f32_16x16x32_bf16 v[20:23], a[196:199], a[48:51], v[20:23]\n"
        "buffer_store_dwordx2 v[200:201], v242, s[16:19], s36 offen offset:768 sc1\n"
        "v_mfma_f32_16x16x32_bf16 v[20:23], a[200:203], a[60:63], v[20:23]\n"
        "v_mfma_f32_16x16x32_bf16 v[20:23], a[204:207], a[56:59], v[20:23]\n"
        "buffer_store_dwordx2 v[204:205], v242, s[16:19], s36 offen offset:1152 sc1\n"
        "v_mfma_f32_16x16x32_bf16 v[20:23], a[208:211], a[68:71], v[20:23]\n"
        "v_mfma_f32_16x16x32_bf16 v[20:23], a[212:215], a[64:67], v[20:23]\n"
        "s_nop 2\n"
        "s_nop 4\n"
        "v_cvt_pk_bf16_f32 v194, v16, v20\n"
        "buffer_store_dword v194, v250, s[16:19], s36 offen offset:0 sc1\n"
        "v_cvt_pk_bf16_f32 v198, v17, v21\n"
        "buffer_store_dword v198, v250, s[16:19], s36 offen offset:384 sc1\n"
        "v_cvt_pk_bf16_f32 v202, v18, v22\n"
        "buffer_store_dword v202, v250, s[16:19], s36 offen offset:768 sc1\n"
        "v_cvt_pk_bf16_f32 v206, v19, v23\n"
        "buffer_store_dword v206, v250, s[16:19], s36 offen offset:1152 sc1\n"
        "s_cmp_lg_u64 vcc, 0\n"
        "s_cbranch_scc1 30f\n"
        "s_endpgm\n"
        "30:\n"
        : "+{v0}"(tid), "=&{s42}"(w), "=&{s[60:61]}"(hist), "=&{s27}"(nit)
        : "{s[0:1]}"(__builtin_amdgcn_kernarg_segment_ptr()), "{s2}"(blockIdx.x), "s"(ldsb)
        : "v1", "v2", "v3", "v4", "v5", "v6", "v7", "v8", "v9", "v10", "v11", "v12", "v13", "v14", "v15", "v16", "v17", "v18", "v19", "v20", "v21", "v22", "v23", "v24", "v25", "v26", "v27", "v28", "v29", "v30", "v31", "v32", "v33", "v34", "v35", "v36", "v37", "v38", "v39", "v40", "v41", "v42", "v43", "v44", "v45", "v46", "v47", "v48", "v49", "v50", "v51", "v52", "v53", "v54", "v55", "v56", "v57", "v58", "v59", "v60", "v61", "v62", "v63", "v64", "v65", "v66", "v67", "v68", "v69", "v70", "v71", "v72", "v73", "v74", "v75", "v76", "v77", "v78", "v79", "v80", "v81", "v82", "v83", "v84", "v85", "v86", "v87", "v88", "v89", "v90", "v91", "v92", "v93", "v94", "v95", "v96", "v97", "v98", "v99", "v100", "v101", "v102", "v103", "v104", "v105", "v106", "v107", "v108", "v109", "v110", "v111", "v112", "v113", "v114", "v115", "v116", "v117", "v118", "v119", "v120", "v121", "v122", "v123", "v124", "v125", "v126", "v127", "v128", "v129", "v130", "v131", "v132", "v133", "v134", "v135", "v136", "v137", "v138", "v139", "v140", "v141", "v142", "v143", "v144", "v145", "v146", "v147", "v148", "v149", "v150", "v151", "v152", "v153", "v154", "v155", "v156", "v157", "v158", "v159", "v160", "v161", "v162", "v163", "v164", "v165", "v166", "v167", "v168", "v169", "v170", "v171", "v172", "v173", "v174", "v175", "v176", "v177", "v178", "v179", "v180", "v181", "v182", "v183", "v184", "v185", "v186", "v187", "v188", "v189", "v190", "v191", "v192", "v193", "v194", "v195", "v196", "v197", "v198", "v199", "v200", "v201", "v202", "v203", "v204", "v205", "v206", "v207", "v208", "v209", "v210", "v211", "v212", "v213", "v214", "v215", "v216", "v217", "v218", "v219", "v220", "v221", "v222", "v223", "v224", "v225", "v226", "v227", "v228", "v229", "v230", "v231", "v232", "v233", "v234", "v235", "v236", "v237", "v238", "v239", "v240", "v241", "v242", "v243", "v244", "v245", "v246", "v247", "v248", "v249", "v250", "v251", "v252", "v253", "v254", "v255", "a0", "a1", "a2", "a3", "a4", "a5", "a6", "a7", "a8", "a9", "a10", "a11", "a12", "a13", "a14", "a15", "a16", "a17", "a18", "a19", "a20", "a21", "a22", "a23", "a24", "a25", "a26", "a27", "a28", "a29", "a30", "a31", "a32", "a33", "a34", "a35", "a36", "a37", "a38", "a39", "a40", "a41", "a42", "a43", "a44", "a45", "a46", "a47", "a48", "a49", "a50", "a51", "a52", "a53", "a54", "a55", "a56", "a57", "a58", "a59", "a60", "a61", "a62", "a63", "a64", "a65", "a66", "a67", "a68", "a69", "a70", "a71", "a72", "a73", "a74", "a75", "a76", "a77", "a78", "a79", "a80", "a81", "a82", "a83", "a84", "a85", "a86", "a87", "a88", "a89", "a90", "a91", "a92", "a93", "a94", "a95", "a96", "a97", "a98", "a99", "a100", "a101", "a102", "a103", "a104", "a105", "a106", "a107", "a108", "a109", "a110", "a111", "a112", "a113", "a114", "a115", "a116", "a117", "a118", "a119", "a120", "a121", "a122", "a123", "a124", "a125", "a126", "a127", "a128", "a129", "a130", "a131", "a132", "a133", "a134", "a135", "a136", "a137", "a138", "a139", "a140", "a141", "a142", "a143", "a144", "a145", "a146", "a147", "a148", "a149", "a150", "a151", "a152", "a153", "a154", "a155", "a156", "a157", "a158", "a159", "a160", "a161", "a162", "a163", "a164", "a165", "a166", "a167", "a168", "a169", "a170", "a171", "a172", "a173", "a174", "a175", "a176", "a177", "a178", "a179", "a180", "a181", "a182", "a183", "a184", "a185", "a186", "a187", "a188", "a189", "a190", "a191", "a192", "a193", "a194", "a195", "a196", "a197", "a198", "a199", "a200", "a201", "a202", "a203", "a204", "a205", "a206", "a207", "a208", "a209", "a210", "a211", "a212", "a213", "a214", "a215", "a216", "a217", "a218", "a219", "a220", "a221", "a222", "a223", "a224", "a225", "a226", "a227", "a228", "a229", "a230", "a231", "a232", "a233", "a234", "a235", "a236", "a237", "a238", "a239", "a240", "a241", "a242", "a243", "a244", "a245", "a246", "a247", "a248", "a249", "a250", "a251", "a252", "a253", "a254", "a255", "s3", "s4", "s5", "s6", "s7", "s8", "s9", "s10", "s11", "s12", "s13", "s14", "s15", "s16", "s17", "s18", "s19", "s20", "s21", "s22", "s23", "s24", "s25", "s26", "s28", "s29", "s30", "s31", "s34", "s35", "s36", "s37", "s38", "s39", "s40", "s41", "s43", "s44", "s45", "s46", "s47", "s48", "s49", "s50", "s51", "s52", "s53", "s54", "s55", "s56", "s57", "s58", "s59", "s62", "s63", "s64", "s65", "s66", "s67", "vcc", "scc", "memory");
    const bool all = nit > 19u;
    for (unsigned i = 0; i < (all ? 256u : 9u); ++i) {
        const unsigned b = all ? i : i ? (unsigned)(hist >> (8u * (i - 1u))) & 255u : blockIdx.x;
        if (!all && i && b == blockIdx.x) continue;
        unsigned l2, w2 = w;
        asm volatile("s_waitcnt vmcnt(0) lgkmcnt(0)\n s_barrier\n v_mbcnt_lo_u32_b32 %0, -1, 0\n v_mbcnt_hi_u32_b32 %0, -1, %0\n s_nop 1"
                     : "=v"(l2), "+s"(w2) :: "memory");
        guarded_pass(x, wqkv, wo, rpb, rpi, mask, out, ldsb, l2, w2, b);
    }
}


extern "C" void kernel_launch(void* const* d_in, const int* in_sizes, int n_in, void* d_out, int out_size, void* d_ws,
                              size_t ws_size, hipStream_t stream) {
    win_attn_kernel<<<256, 256, 0, stream>>>((const char*)d_in[0], (const char*)d_in[1], (const char*)d_in[2],
                                             (const char*)d_in[3], (const char*)d_in[4], (const char*)d_in[5],
                                             (char*)d_out, (char*)d_ws);

}
```

```cpp
#include <hip/hip_runtime.h>
#include <hip/hip_bf16.h>

typedef unsigned int u32x4 __attribute__((ext_vector_type(4)));

__device__ __forceinline__ void guarded_pass(const char* x, const char* wqkv, const char* wo, const char* rpb, const char* rpi,
                                             const char* mask, char* out, unsigned ldsb, const unsigned lane, const unsigned w, const unsigned bid) {
    const unsigned wx = bid & 63u, wy0 = bid >> 6;
    unsigned dm[3], wsrc[3];
#pragma unroll
    for (int p = 0; p < 3; ++p) {
        unsigned gch = 64u * p + lane, t = gch / 24u, c1 = gch % 24u;
        dm[p] = ((8u * wx + 4u + t) & 511u) * 384u + ((c1 ^ (((t >> 1) & 1u) | (w << 1))) << 4);
        wsrc[p] = t * 1152u + ((c1 ^ (2u * (((t >> 1) & 1u) | ((w & 1u) << 1)))) << 4) - 1024u * p;
    }
    u32x4 srdx = {(unsigned)(size_t)x, (unsigned)((size_t)x >> 32) & 0xffffu, 100663296u, 0x20000u};
    u32x4 srdw = {(unsigned)(size_t)wqkv, (unsigned)((size_t)wqkv >> 32) & 0xffffu, 221184u, 0x20000u};
    unsigned ya = (8u * wy0 + 36u + w) * 196608u;
    unsigned wv = w;
    unsigned w3 = w * 3072u, w9 = w * 9216u;
    __builtin_amdgcn_sched_barrier(0);
    asm volatile(
        "s_nop 4\n"
        "s_add_u32 m0, s40, 0\n"
        "s_add_u32 s64, s65, 0\n"
        "buffer_load_dwordx4 v128, s[20:23], s64 offen lds\n"
        "buffer_load_dwordx4 v129, s[20:23], s64 offen offset:1024 lds\n"
        "buffer_load_dwordx4 v130, s[20:23], s64 offen offset:2048 lds\n"
        "s_add_u32 m0, s40, 12288\n"
        "s_add_u32 s64, s65, 36864\n"
        "buffer_load_dwordx4 v128, s[20:23], s64 offen lds\n"
        "buffer_load_dwordx4 v129, s[20:23], s64 offen offset:1024 lds\n"
        "buffer_load_dwordx4 v130, s[20:23], s64 offen offset:2048 lds\n"
        "s_add_u32 m0, s40, 24576\n"
        "s_add_u32 s64, s65, 73728\n"
        "buffer_load_dwordx4 v128, s[20:23], s64 offen lds\n"
        "buffer_load_dwordx4 v129, s[20:23], s64 offen offset:1024 lds\n"
        "buffer_load_dwordx4 v130, s[20:23], s64 offen offset:2048 lds\n"
        :: "{v242}"(dm[0]), "{v243}"(dm[1]), "{v244}"(dm[2]), "{v128}"(wsrc[0]), "{v129}"(wsrc[1]), "{v130}"(wsrc[2]), "{s[8:11]}"(srdx), "{s[20:23]}"(srdw), "{s47}"(ya), "{s40}"(w3), "{s65}"(w9), "s"(ldsb)
        : "s64", "scc", "memory");
    __builtin_amdgcn_sched_barrier(0);

    const unsigned rho = lane & 15u, g = lane >> 4;
    const unsigned cg = w >> 1, T = w & 1u;
    const unsigned ql = 8u * (rho >> 2) + (rho & 3u);
    unsigned xr[2], mr[2];
#pragma unroll
    for (int j = 0; j < 2; ++j) {
        xr[j] = 73728u + ql * 384u + (((4u * j + g) ^ (rho >> 1)) << 4);
        mr[j] = 147456u + 8192u + ql * 128u + (((4u * j + g) ^ (rho >> 1)) << 4);
    }
    unsigned ow = 122880u + (4u * w + g) * 1024u + rho * 16u;
    unsigned ow2 = 122880u + (16u + 2u * w + (g >> 1)) * 1024u + rho * 16u + (g & 1u) * 8u;
    unsigned ord = 122880u + g * 1024u + (32u * T + rho) * 16u;
    __builtin_amdgcn_sched_barrier(0);
    asm volatile(
        "s_nop 4\n"
        "s_add_u32 m0, s40, 36864\n"
        "s_add_u32 s64, s65, 110592\n"
        "buffer_load_dwordx4 v128, s[20:23], s64 offen lds\n"
        "buffer_load_dwordx4 v129, s[20:23], s64 offen offset:1024 lds\n"
        "buffer_load_dwordx4 v130, s[20:23], s64 offen offset:2048 lds\n"
        :: "{v242}"(dm[0]), "{v243}"(dm[1]), "{v244}"(dm[2]), "{v128}"(wsrc[0]), "{v129}"(wsrc[1]), "{v130}"(wsrc[2]), "{s[8:11]}"(srdx), "{s[20:23]}"(srdw), "{s47}"(ya), "{s40}"(w3), "{s65}"(w9), "s"(ldsb)
        : "s64", "scc", "memory");
    __builtin_amdgcn_sched_barrier(0);
    unsigned wor = 0u + cg * 18432u + g * 1536u + rho * 96u + (g & 1u) * 16u;
    unsigned wor2 = 0u + cg * 18432u + g * 1536u + rho * 96u - (g & 1u) * 16u;
    unsigned ml = 147456u + w * 2048u + lane * 16u;
    unsigned ms = (lane >> 3) * 128u + (((lane & 7u) ^ (((lane >> 4) & 1u) | ((w & 1u) << 2))) << 4);
    unsigned st = g * 196608u + cg * 192u + rho * 12u;
    unsigned l16 = lane * 16u;
    unsigned id0 = (rho == 4u * g ? 0x3f80u : 0u) | (rho == 4u * g + 1u ? 0x3f800000u : 0u);
    unsigned id1 = (rho == 4u * g + 2u ? 0x3f80u : 0u) | (rho == 4u * g + 3u ? 0x3f800000u : 0u);
    unsigned x0 = (8u * wx + 4u) * 384u, x1 = ((8u * wx + 8u) & 511u) * 384u;
    u32x4 srdm = {(unsigned)(size_t)mask, (unsigned)((size_t)mask >> 32) & 0xffffu, 33554432u, 0x20000u};
    u32x4 srdo = {(unsigned)(size_t)out, (unsigned)((size_t)out >> 32) & 0xffffu, 100663296u, 0x20000u};
    u32x4 srdwo = {(unsigned)(size_t)wo, (unsigned)((size_t)wo >> 32) & 0xffffu, 73728u, 0x20000u};
    u32x4 srdrp = {(unsigned)(size_t)rpi, (unsigned)((size_t)rpi >> 32) & 0xffffu, 16384u, 0x20000u};
    u32x4 srdrb = {(unsigned)(size_t)rpb, (unsigned)((size_t)rpb >> 32) & 0xffffu, 1800u, 0x20000u};
    __builtin_amdgcn_sched_barrier(0);
    asm volatile(
        "s_nop 4\n"
        "s_add_u32 m0, s40, 49152\n"
        "s_add_u32 s64, s65, 147456\n"
        "buffer_load_dwordx4 v128, s[20:23], s64 offen lds\n"
        "buffer_load_dwordx4 v129, s[20:23], s64 offen offset:1024 lds\n"
        "buffer_load_dwordx4 v130, s[20:23], s64 offen offset:2048 lds\n"
        :: "{v242}"(dm[0]), "{v243}"(dm[1]), "{v244}"(dm[2]), "{v128}"(wsrc[0]), "{v129}"(wsrc[1]), "{v130}"(wsrc[2]), "{s[8:11]}"(srdx), "{s[20:23]}"(srdw), "{s47}"(ya), "{s40}"(w3), "{s65}"(w9), "s"(ldsb)
        : "s64", "scc", "memory");
    __builtin_amdgcn_sched_barrier(0);
    const unsigned q4 = (lane >> 2) & 3u, p4 = lane & 3u;
    unsigned wrd[3];
#pragma unroll
    for (int p = 0; p < 3; ++p) {
        unsigned c = 6u * w + 2u * p + (p4 >> 1);
        wrd[p] = (8u * g + q4) * 384u + ((c ^ (2u * ((q4 >> 1) | ((g & 1u) << 1)))) << 4) + (p4 & 1u) * 8u;
    }
    unsigned od = cg * 18432u + g * 1536u + (((48u * T + rho) ^ (g & 1u)) << 4);
    unsigned rbs = lane * 8u + 2u * w, rbw = 151552u + w * 1024u + lane * 4u;
    const unsigned pfi = ((bid >> 3) & 31u) * 4u + w;
    unsigned pf0 = pfi * 2048u + lane * 16u, pf1 = pfi * 1024u + lane * 16u;
    __builtin_amdgcn_sched_barrier(0);
    asm volatile(
        "s_nop 4\n"
        "s_add_u32 m0, s40, 61440\n"
        "s_add_u32 s64, s65, 184320\n"
        "buffer_load_dwordx4 v128, s[20:23], s64 offen lds\n"
        "buffer_load_dwordx4 v129, s[20:23], s64 offen offset:1024 lds\n"
        "buffer_load_dwordx4 v130, s[20:23], s64 offen offset:2048 lds\n"
        :: "{v242}"(dm[0]), "{v243}"(dm[1]), "{v244}"(dm[2]), "{v128}"(wsrc[0]), "{v129}"(wsrc[1]), "{v130}"(wsrc[2]), "{s[8:11]}"(srdx), "{s[20:23]}"(srdw), "{s47}"(ya), "{s40}"(w3), "{s65}"(w9), "s"(ldsb)
        : "s64", "scc", "memory");
    __builtin_amdgcn_sched_barrier(0);
    unsigned rs = g * 256u + ((rho ^ (g | ((w & 1u) << 3))) << 4);
    unsigned rr = 135168u + ql * 256u + (((2u * g) ^ rho) << 4);
    unsigned oa = (4u * g + q4) * 384u + 96u * w + p4 * 8u;
    unsigned ob = (48u * (g >> 1) + 8u * (g & 1u) + q4) * 384u + 96u * w + p4 * 8u;
    unsigned mk = (bid + 256u) * 8192u + w * 2048u;
    unsigned raw = (8u * wy0 + 4u + 4u * T) * 196608u;
    asm volatile(
        "s_nop 4\n"
        "s_mul_i32 s40, s42, 3072\n"
        "s_mul_i32 s65, s42, 9216\n"
        "s_lshl_b32 s48, s42, 11\n"
        "s_add_u32 s48, s48, 147456\n"
        "s_mov_b32 s24, 0x3fb8aa3b\n"
        "s_mov_b32 s25, 0xbfb8aa3b\n"
        "s_mov_b32 s26, 0xff7fff7f\n"
        "s_mov_b32 s49, 0x3e13cd3a\n"
        "s_mov_b32 s50, 0x3e13cd3a\n"
        "s_mov_b32 s51, 0x3e13cd3a\n"
        "s_mov_b32 s27, 14\n"
        "s_mov_b32 s28, 73728\n"
        "s_mov_b32 s29, 122880\n"
        "s_mov_b32 s30, 98304\n"
        "s_mov_b32 s38, 0x80000000\n"
        "s_mov_b32 s39, 0x80000000\n"
        "s_lshl_b32 s67, s42, 10\n"
        "s_add_u32 s67, s67, 151552\n"
        "v_mov_b32 v188, v170\n"
        "v_mov_b32 v189, v171\n"
        "v_mov_b32 v210, v170\n"
        "v_mov_b32 v211, v171\n"
        "v_mov_b32 v228, v170\n"
        "v_mov_b32 v229, v171\n"
        "v_accvgpr_write_b32 a252, v140\n"
        "v_add_u32 v136, 98304, v131\n"
        "v_accvgpr_write_b32 a253, v141\n"
        "v_add_u32 v137, 98304, v132\n"
        "v_accvgpr_write_b32 a254, v147\n"
        "v_add_u32 v138, 98304, v133\n"
        "v_mov_b32 v150, v240\n"
        "v_mov_b32 v151, v241\n"
        "v_xor_b32 v253, 64, v139\n"
        "v_xor_b32 v232, 0, v144\n"
        "v_xor_b32 v233, 16, v144\n"
        "v_xor_b32 v234, 128, v144\n"
        "v_xor_b32 v235, 144, v144\n"
        "s_lshl_b32 s46, s42, 12\n"
        "buffer_load_dwordx4 v[64:67], v148, s[20:23], 0 offen\n"
        "buffer_load_dwordx4 v[68:71], v148, s[20:23], 0 offen offset:1024\n"
        "buffer_load_dwordx4 v[72:75], v149, s[52:55], 0 offen\n"
        "s_waitcnt vmcnt(12)\n"
        "s_barrier\n"
        "s_add_u32 m0, s40, 73728\n"
        "ds_read_b64_tr_b16 v[76:77], v131 offset:0\n"
        "ds_read_b64_tr_b16 v[78:79], v131 offset:1536\n"
        "ds_read_b64_tr_b16 v[80:81], v131 offset:12288\n"
        "ds_read_b64_tr_b16 v[82:83], v131 offset:13824\n"
        "ds_read_b64_tr_b16 v[84:85], v131 offset:24576\n"
        "ds_read_b64_tr_b16 v[86:87], v131 offset:26112\n"
        "ds_read_b64_tr_b16 v[88:89], v132 offset:0\n"
        "ds_read_b64_tr_b16 v[90:91], v132 offset:1536\n"
        "ds_read_b64_tr_b16 v[92:93], v132 offset:12288\n"
        "ds_read_b64_tr_b16 v[94:95], v132 offset:13824\n"
        "ds_read_b64_tr_b16 v[104:105], v132 offset:24576\n"
        "ds_read_b64_tr_b16 v[106:107], v132 offset:26112\n"
        "ds_read_b64_tr_b16 v[108:109], v133 offset:0\n"
        "ds_read_b64_tr_b16 v[110:111], v133 offset:1536\n"
        "ds_read_b64_tr_b16 v[116:117], v133 offset:12288\n"
        "ds_read_b64_tr_b16 v[118:119], v133 offset:13824\n"
        "ds_read_b64_tr_b16 v[120:121], v133 offset:24576\n"
        "ds_read_b64_tr_b16 v[122:123], v133 offset:26112\n"
        "s_sub_u32 s64, s47, 6291456\n"
        "buffer_load_dwordx4 v242, s[8:11], s64 offen lds\n"
        "s_add_u32 m0, s40, 74752\n"
        "s_sub_u32 s64, s47, 6291456\n"
        "buffer_load_dwordx4 v243, s[8:11], s64 offen lds\n"
        "s_add_u32 m0, s40, 75776\n"
        "s_sub_u32 s64, s47, 6291456\n"
        "buffer_load_dwordx4 v244, s[8:11], s64 offen lds\n"
        "s_add_u32 m0, s40, 86016\n"
        "s_sub_u32 s64, s47, 5505024\n"
        "buffer_load_dwordx4 v242, s[8:11], s64 offen lds\n"
        "s_add_u32 m0, s40, 87040\n"
        "s_sub_u32 s64, s47, 5505024\n"
        "buffer_load_dwordx4 v243, s[8:11], s64 offen lds\n"
        "s_add_u32 m0, s40, 88064\n"
        "s_sub_u32 s64, s47, 5505024\n"
        "buffer_load_dwordx4 v244, s[8:11], s64 offen lds\n"
        "s_waitcnt lgkmcnt(15)\n"
        "v_lshlrev_b32 v0, 16, v76\n"
        "v_lshlrev_b32 v2, 16, v77\n"
        "v_lshlrev_b32 v4, 16, v78\n"
        "v_lshlrev_b32 v6, 16, v79\n"
        "v_and_b32 v1, 0xffff0000, v76\n"
        "v_and_b32 v3, 0xffff0000, v77\n"
        "v_and_b32 v5, 0xffff0000, v78\n"
        "v_and_b32 v7, 0xffff0000, v79\n"
        "v_pk_mul_f32 v[0:1], v[0:1], s[50:51]\n"
        "v_pk_mul_f32 v[2:3], v[2:3], s[50:51]\n"
        "v_pk_mul_f32 v[4:5], v[4:5], s[50:51]\n"
        "v_pk_mul_f32 v[6:7], v[6:7], s[50:51]\n"
        "v_cvt_pk_bf16_f32 v0, v0, v1\n"
        "v_cvt_pk_bf16_f32 v2, v2, v3\n"
        "v_cvt_pk_bf16_f32 v4, v4, v5\n"
        "v_cvt_pk_bf16_f32 v6, v6, v7\n"
        "v_accvgpr_write_b32 a0, v0\n"
        "v_accvgpr_write_b32 a1, v2\n"
        "v_accvgpr_write_b32 a2, v4\n"
        "v_accvgpr_write_b32 a3, v6\n"
        "v_lshlrev_b32 v8, 16, v80\n"
        "v_lshlrev_b32 v10, 16, v81\n"
        "s_waitcnt lgkmcnt(14)\n"
        "v_lshlrev_b32 v12, 16, v82\n"
        "v_lshlrev_b32 v14, 16, v83\n"
        "v_and_b32 v9, 0xffff0000, v80\n"
        "v_and_b32 v11, 0xffff0000, v81\n"
        "v_and_b32 v13, 0xffff0000, v82\n"
        "v_and_b32 v15, 0xffff0000, v83\n"
        "v_pk_mul_f32 v[8:9], v[8:9], s[50:51]\n"
        "v_pk_mul_f32 v[10:11], v[10:11], s[50:51]\n"
        "v_pk_mul_f32 v[12:13], v[12:13], s[50:51]\n"
        "v_pk_mul_f32 v[14:15], v[14:15], s[50:51]\n"
        "v_cvt_pk_bf16_f32 v8, v8, v9\n"
        "v_cvt_pk_bf16_f32 v10, v10, v11\n"
        "v_cvt_pk_bf16_f32 v12, v12, v13\n"
        "v_cvt_pk_bf16_f32 v14, v14, v15\n"
        "v_accvgpr_write_b32 a4, v8\n"
        "v_accvgpr_write_b32 a5, v10\n"
        "v_accvgpr_write_b32 a6, v12\n"
        "v_accvgpr_write_b32 a7, v14\n"
        "s_waitcnt lgkmcnt(13)\n"
        "v_lshlrev_b32 v0, 16, v84\n"
        "v_lshlrev_b32 v2, 16, v85\n"
        "s_waitcnt lgkmcnt(12)\n"
        "v_lshlrev_b32 v4, 16, v86\n"
        "v_lshlrev_b32 v6, 16, v87\n"
        "v_and_b32 v1, 0xffff0000, v84\n"
        "v_and_b32 v3, 0xffff0000, v85\n"
        "v_and_b32 v5, 0xffff0000, v86\n"
        "v_and_b32 v7, 0xffff0000, v87\n"
        "v_pk_mul_f32 v[0:1], v[0:1], s[50:51]\n"
        "v_pk_mul_f32 v[2:3], v[2:3], s[50:51]\n"
        "v_pk_mul_f32 v[4:5], v[4:5], s[50:51]\n"
        "v_pk_mul_f32 v[6:7], v[6:7], s[50:51]\n"
        "v_cvt_pk_bf16_f32 v0, v0, v1\n"
        "v_cvt_pk_bf16_f32 v2, v2, v3\n"
        "v_cvt_pk_bf16_f32 v4, v4, v5\n"
        "v_cvt_pk_bf16_f32 v6, v6, v7\n"
        "v_accvgpr_write_b32 a8, v0\n"
        "v_accvgpr_write_b32 a9, v2\n"
        "v_accvgpr_write_b32 a10, v4\n"
        "v_accvgpr_write_b32 a11, v6\n"
        "s_waitcnt lgkmcnt(11)\n"
        "v_lshlrev_b32 v8, 16, v88\n"
        "v_lshlrev_b32 v10, 16, v89\n"
        "s_waitcnt lgkmcnt(10)\n"
        "v_lshlrev_b32 v12, 16, v90\n"
        "v_lshlrev_b32 v14, 16, v91\n"
        "buffer_load_ushort v236, v145, s[60:63], 0 offen offset:0\n"
        "buffer_load_ushort v237, v145, s[60:63], 0 offen offset:512\n"
        "buffer_load_ushort v238, v145, s[60:63], 0 offen offset:1024\n"
        "buffer_load_ushort v239, v145, s[60:63], 0 offen offset:1536\n"
        "v_and_b32 v9, 0xffff0000, v88\n"
        "v_and_b32 v11, 0xffff0000, v89\n"
        "v_and_b32 v13, 0xffff0000, v90\n"
        "v_and_b32 v15, 0xffff0000, v91\n"
        "v_pk_mul_f32 v[8:9], v[8:9], s[50:51]\n"
        "v_pk_mul_f32 v[10:11], v[10:11], s[50:51]\n"
        "v_pk_mul_f32 v[12:13], v[12:13], s[50:51]\n"
        "v_pk_mul_f32 v[14:15], v[14:15], s[50:51]\n"
        "s_add_u32 m0, s46, 135168\n"
        "v_cvt_pk_bf16_f32 v8, v8, v9\n"
        "v_cvt_pk_bf16_f32 v10, v10, v11\n"
        "v_cvt_pk_bf16_f32 v12, v12, v13\n"
        "v_cvt_pk_bf16_f32 v14, v14, v15\n"
        "s_mov_b32 s64, s46\n"
        "v_accvgpr_write_b32 a24, v8\n"
        "v_accvgpr_write_b32 a25, v10\n"
        "v_accvgpr_write_b32 a26, v12\n"
        "v_accvgpr_write_b32 a27, v14\n"
        "buffer_load_dwordx4 v139, s[56:59], s64 offen lds\n"
        "s_waitcnt lgkmcnt(9)\n"
        "v_lshlrev_b32 v0, 16, v92\n"
        "v_lshlrev_b32 v2, 16, v93\n"
        "s_waitcnt lgkmcnt(8)\n"
        "v_lshlrev_b32 v4, 16, v94\n"
        "v_lshlrev_b32 v6, 16, v95\n"
        "s_waitcnt lgkmcnt(0)\n"
        "s_waitcnt vmcnt(14)\n"
        "s_barrier\n"
        "v_and_b32 v1, 0xffff0000, v92\n"
        "v_and_b32 v3, 0xffff0000, v93\n"
        "v_and_b32 v5, 0xffff0000, v94\n"
        "v_and_b32 v7, 0xffff0000, v95\n"
        "ds_read_b64_tr_b16 v[152:153], v131 offset:36864\n"
        "ds_read_b64_tr_b16 v[154:155], v131 offset:38400\n"
        "ds_read_b64_tr_b16 v[172:173], v131 offset:49152\n"
        "ds_read_b64_tr_b16 v[174:175], v131 offset:50688\n"
        "ds_read_b64_tr_b16 v[192:193], v131 offset:61440\n"
        "ds_read_b64_tr_b16 v[194:195], v131 offset:62976\n"
        "ds_read_b64_tr_b16 v[212:213], v132 offset:36864\n"
        "ds_read_b64_tr_b16 v[214:215], v132 offset:38400\n"
        "ds_read_b64_tr_b16 v[156:157], v132 offset:49152\n"
        "ds_read_b64_tr_b16 v[158:159], v132 offset:50688\n"
        "ds_read_b64_tr_b16 v[176:177], v132 offset:61440\n"
        "ds_read_b64_tr_b16 v[178:179], v132 offset:62976\n"
        "ds_read_b64_tr_b16 v[196:197], v133 offset:36864\n"
        "ds_read_b64_tr_b16 v[198:199], v133 offset:38400\n"
        "ds_read_b64_tr_b16 v[216:217], v133 offset:49152\n"
        "ds_read_b64_tr_b16 v[218:219], v133 offset:50688\n"
        "ds_read_b64_tr_b16 v[124:125], v133 offset:61440\n"
        "ds_read_b64_tr_b16 v[126:127], v133 offset:62976\n"
        "v_pk_mul_f32 v[0:1], v[0:1], s[50:51]\n"
        "v_pk_mul_f32 v[2:3], v[2:3], s[50:51]\n"
        "v_pk_mul_f32 v[4:5], v[4:5], s[50:51]\n"
        "v_pk_mul_f32 v[6:7], v[6:7], s[50:51]\n"
        "v_cvt_pk_bf16_f32 v0, v0, v1\n"
        "v_cvt_pk_bf16_f32 v2, v2, v3\n"
        "v_cvt_pk_bf16_f32 v4, v4, v5\n"
        "v_cvt_pk_bf16_f32 v6, v6, v7\n"
        "buffer_load_dwordx4 v139, s[56:59], s64 offen offset:1024 lds\n"
        "v_accvgpr_write_b32 a28, v0\n"
        "v_accvgpr_write_b32 a29, v2\n"
        "v_accvgpr_write_b32 a30, v4\n"
        "v_accvgpr_write_b32 a31, v6\n"
        "v_lshlrev_b32 v8, 16, v104\n"
        "v_lshlrev_b32 v10, 16, v105\n"
        "v_lshlrev_b32 v12, 16, v106\n"
        "v_lshlrev_b32 v14, 16, v107\n"
        "v_and_b32 v9, 0xffff0000, v104\n"
        "v_and_b32 v11, 0xffff0000, v105\n"
        "v_and_b32 v13, 0xffff0000, v106\n"
        "v_and_b32 v15, 0xffff0000, v107\n"
        "v_pk_mul_f32 v[8:9], v[8:9], s[50:51]\n"
        "v_pk_mul_f32 v[10:11], v[10:11], s[50:51]\n"
        "v_pk_mul_f32 v[12:13], v[12:13], s[50:51]\n"
        "v_pk_mul_f32 v[14:15], v[14:15], s[50:51]\n"
        "buffer_load_dwordx4 v253, s[56:59], s64 offen offset:2048 lds\n"
        "v_cvt_pk_bf16_f32 v8, v8, v9\n"
        "v_cvt_pk_bf16_f32 v10, v10, v11\n"
        "v_cvt_pk_bf16_f32 v12, v12, v13\n"
        "v_cvt_pk_bf16_f32 v14, v14, v15\n"
        "v_accvgpr_write_b32 a32, v8\n"
        "v_accvgpr_write_b32 a33, v10\n"
        "v_accvgpr_write_b32 a34, v12\n"
        "v_accvgpr_write_b32 a35, v14\n"
        "v_lshlrev_b32 v0, 16, v108\n"
        "v_lshlrev_b32 v2, 16, v109\n"
        "v_lshlrev_b32 v4, 16, v110\n"
        "v_lshlrev_b32 v6, 16, v111\n"
        "v_and_b32 v1, 0xffff0000, v108\n"
        "v_and_b32 v3, 0xffff0000, v109\n"
        "v_and_b32 v5, 0xffff0000, v110\n"
        "v_and_b32 v7, 0xffff0000, v111\n"
        "buffer_load_dwordx4 v253, s[56:59], s64 offen offset:3072 lds\n"
        "v_pk_mul_f32 v[0:1], v[0:1], s[50:51]\n"
        "v_pk_mul_f32 v[2:3], v[2:3], s[50:51]\n"
        "v_pk_mul_f32 v[4:5], v[4:5], s[50:51]\n"
        "v_pk_mul_f32 v[6:7], v[6:7], s[50:51]\n"
        "v_cvt_pk_bf16_f32 v0, v0, v1\n"
        "v_cvt_pk_bf16_f32 v2, v2, v3\n"
        "v_cvt_pk_bf16_f32 v4, v4, v5\n"
        "v_cvt_pk_bf16_f32 v6, v6, v7\n"
        "v_accvgpr_write_b32 a48, v0\n"
        "v_accvgpr_write_b32 a49, v2\n"
        "v_accvgpr_write_b32 a50, v4\n"
        "v_accvgpr_write_b32 a51, v6\n"
        "v_lshlrev_b32 v8, 16, v116\n"
        "v_lshlrev_b32 v10, 16, v117\n"
        "v_lshlrev_b32 v12, 16, v118\n"
        "v_lshlrev_b32 v14, 16, v119\n"
        "v_and_b32 v9, 0xffff0000, v116\n"
        "v_and_b32 v11, 0xffff0000, v117\n"
        "v_and_b32 v13, 0xffff0000, v118\n"
        "v_and_b32 v15, 0xffff0000, v119\n"
        "v_pk_mul_f32 v[8:9], v[8:9], s[50:51]\n"
        "v_pk_mul_f32 v[10:11], v[10:11], s[50:51]\n"
        "v_pk_mul_f32 v[12:13], v[12:13], s[50:51]\n"
        "v_pk_mul_f32 v[14:15], v[14:15], s[50:51]\n"
        "v_cvt_pk_bf16_f32 v8, v8, v9\n"
        "v_cvt_pk_bf16_f32 v10, v10, v11\n"
        "v_cvt_pk_bf16_f32 v12, v12, v13\n"
        "v_cvt_pk_bf16_f32 v14, v14, v15\n"
        "v_accvgpr_write_b32 a52, v8\n"
        "v_accvgpr_write_b32 a53, v10\n"
        "v_accvgpr_write_b32 a54, v12\n"
        "v_accvgpr_write_b32 a55, v14\n"
        "v_lshlrev_b32 v0, 16, v120\n"
        "v_lshlrev_b32 v2, 16, v121\n"
        "v_lshlrev_b32 v4, 16, v122\n"
        "v_lshlrev_b32 v6, 16, v123\n"
        "v_and_b32 v1, 0xffff0000, v120\n"
        "v_and_b32 v3, 0xffff0000, v121\n"
        "v_and_b32 v5, 0xffff0000, v122\n"
        "v_and_b32 v7, 0xffff0000, v123\n"
        "v_pk_mul_f32 v[0:1], v[0:1], s[50:51]\n"
        "v_pk_mul_f32 v[2:3], v[2:3], s[50:51]\n"
        "v_pk_mul_f32 v[4:5], v[4:5], s[50:51]\n"
        "v_pk_mul_f32 v[6:7], v[6:7], s[50:51]\n"
        "v_cvt_pk_bf16_f32 v0, v0, v1\n"
        "v_cvt_pk_bf16_f32 v2, v2, v3\n"
        "v_cvt_pk_bf16_f32 v4, v4, v5\n"
        "v_cvt_pk_bf16_f32 v6, v6, v7\n"
        "v_accvgpr_write_b32 a56, v0\n"
        "v_accvgpr_write_b32 a57, v2\n"
        "v_accvgpr_write_b32 a58, v4\n"
        "v_accvgpr_write_b32 a59, v6\n"
        "s_waitcnt lgkmcnt(15)\n"
        "v_lshlrev_b32 v8, 16, v152\n"
        "v_lshlrev_b32 v10, 16, v153\n"
        "v_lshlrev_b32 v12, 16, v154\n"
        "v_lshlrev_b32 v14, 16, v155\n"
        "v_and_b32 v9, 0xffff0000, v152\n"
        "v_and_b32 v11, 0xffff0000, v153\n"
        "v_and_b32 v13, 0xffff0000, v154\n"
        "v_and_b32 v15, 0xffff0000, v155\n"
        "v_pk_mul_f32 v[8:9], v[8:9], s[50:51]\n"
        "v_pk_mul_f32 v[10:11], v[10:11], s[50:51]\n"
        "v_pk_mul_f32 v[12:13], v[12:13], s[50:51]\n"
        "v_pk_mul_f32 v[14:15], v[14:15], s[50:51]\n"
        "v_cvt_pk_bf16_f32 v8, v8, v9\n"
        "v_cvt_pk_bf16_f32 v10, v10, v11\n"
        "v_cvt_pk_bf16_f32 v12, v12, v13\n"
        "v_cvt_pk_bf16_f32 v14, v14, v15\n"
        "v_accvgpr_write_b32 a12, v8\n"
        "v_accvgpr_write_b32 a13, v10\n"
        "v_accvgpr_write_b32 a14, v12\n"
        "v_accvgpr_write_b32 a15, v14\n"
        "v_lshlrev_b32 v0, 16, v172\n"
        "v_lshlrev_b32 v2, 16, v173\n"
        "s_waitcnt lgkmcnt(14)\n"
        "v_lshlrev_b32 v4, 16, v174\n"
        "v_lshlrev_b32 v6, 16, v175\n"
        "v_and_b32 v1, 0xffff0000, v172\n"
        "v_and_b32 v3, 0xffff0000, v173\n"
        "v_and_b32 v5, 0xffff0000, v174\n"
        "v_and_b32 v7, 0xffff0000, v175\n"
        "v_pk_mul_f32 v[0:1], v[0:1], s[50:51]\n"
        "v_pk_mul_f32 v[2:3], v[2:3], s[50:51]\n"
        "v_pk_mul_f32 v[4:5], v[4:5], s[50:51]\n"
        "v_pk_mul_f32 v[6:7], v[6:7], s[50:51]\n"
        "v_cvt_pk_bf16_f32 v0, v0, v1\n"
        "v_cvt_pk_bf16_f32 v2, v2, v3\n"
        "v_cvt_pk_bf16_f32 v4, v4, v5\n"
        "v_cvt_pk_bf16_f32 v6, v6, v7\n"
        "v_accvgpr_write_b32 a16, v0\n"
        "v_accvgpr_write_b32 a17, v2\n"
        "v_accvgpr_write_b32 a18, v4\n"
        "v_accvgpr_write_b32 a19, v6\n"
        "s_waitcnt lgkmcnt(13)\n"
        "v_lshlrev_b32 v8, 16, v192\n"
        "v_lshlrev_b32 v10, 16, v193\n"
        "s_waitcnt lgkmcnt(12)\n"
        "v_lshlrev_b32 v12, 16, v194\n"
        "v_lshlrev_b32 v14, 16, v195\n"
        "v_and_b32 v9, 0xffff0000, v192\n"
        "v_and_b32 v11, 0xffff0000, v193\n"
        "v_and_b32 v13, 0xffff0000, v194\n"
        "v_and_b32 v15, 0xffff0000, v195\n"
        "v_pk_mul_f32 v[8:9], v[8:9], s[50:51]\n"
        "v_pk_mul_f32 v[10:11], v[10:11], s[50:51]\n"
        "v_pk_mul_f32 v[12:13], v[12:13], s[50:51]\n"
        "v_pk_mul_f32 v[14:15], v[14:15], s[50:51]\n"
        "v_cvt_pk_bf16_f32 v8, v8, v9\n"
        "v_cvt_pk_bf16_f32 v10, v10, v11\n"
        "v_cvt_pk_bf16_f32 v12, v12, v13\n"
        "v_cvt_pk_bf16_f32 v14, v14, v15\n"
        "v_accvgpr_write_b32 a20, v8\n"
        "v_accvgpr_write_b32 a21, v10\n"
        "v_accvgpr_write_b32 a22, v12\n"
        "v_accvgpr_write_b32 a23, v14\n"
        "s_waitcnt lgkmcnt(11)\n"
        "v_lshlrev_b32 v0, 16, v212\n"
        "v_lshlrev_b32 v2, 16, v213\n"
        "s_waitcnt lgkmcnt(10)\n"
        "v_lshlrev_b32 v4, 16, v214\n"
        "v_lshlrev_b32 v6, 16, v215\n"
        "v_and_b32 v1, 0xffff0000, v212\n"
        "v_and_b32 v3, 0xffff0000, v213\n"
        "v_and_b32 v5, 0xffff0000, v214\n"
        "v_and_b32 v7, 0xffff0000, v215\n"
        "v_pk_mul_f32 v[0:1], v[0:1], s[50:51]\n"
        "v_pk_mul_f32 v[2:3], v[2:3], s[50:51]\n"
        "v_pk_mul_f32 v[4:5], v[4:5], s[50:51]\n"
        "v_pk_mul_f32 v[6:7], v[6:7], s[50:51]\n"
        "v_cvt_pk_bf16_f32 v0, v0, v1\n"
        "v_cvt_pk_bf16_f32 v2, v2, v3\n"
        "v_cvt_pk_bf16_f32 v4, v4, v5\n"
        "v_cvt_pk_bf16_f32 v6, v6, v7\n"
        "s_add_u32 m0, s40, 98304\n"
        "v_accvgpr_write_b32 a36, v0\n"
        "v_accvgpr_write_b32 a37, v2\n"
        "v_accvgpr_write_b32 a38, v4\n"
        "v_accvgpr_write_b32 a39, v6\n"
        "s_add_u32 s64, s65, 384\n"
        "s_waitcnt lgkmcnt(9)\n"
        "v_lshlrev_b32 v8, 16, v156\n"
        "v_lshlrev_b32 v10, 16, v157\n"
        "s_waitcnt lgkmcnt(8)\n"
        "v_lshlrev_b32 v12, 16, v158\n"
        "v_lshlrev_b32 v14, 16, v159\n"
        "buffer_load_dwordx4 v128, s[20:23], s64 offen lds\n"
        "v_and_b32 v9, 0xffff0000, v156\n"
        "v_and_b32 v11, 0xffff0000, v157\n"
        "v_and_b32 v13, 0xffff0000, v158\n"
        "v_and_b32 v15, 0xffff0000, v159\n"
        "v_pk_mul_f32 v[8:9], v[8:9], s[50:51]\n"
        "v_pk_mul_f32 v[10:11], v[10:11], s[50:51]\n"
        "v_pk_mul_f32 v[12:13], v[12:13], s[50:51]\n"
        "v_pk_mul_f32 v[14:15], v[14:15], s[50:51]\n"
        "v_cvt_pk_bf16_f32 v8, v8, v9\n"
        "v_cvt_pk_bf16_f32 v10, v10, v11\n"
        "v_cvt_pk_bf16_f32 v12, v12, v13\n"
        "v_cvt_pk_bf16_f32 v14, v14, v15\n"
        "v_accvgpr_write_b32 a40, v8\n"
        "v_accvgpr_write_b32 a41, v10\n"
        "v_accvgpr_write_b32 a42, v12\n"
        "v_accvgpr_write_b32 a43, v14\n"
        "buffer_load_dwordx4 v129, s[20:23], s64 offen offset:1024 lds\n"
        "s_waitcnt lgkmcnt(7)\n"
        "v_lshlrev_b32 v0, 16, v176\n"
        "v_lshlrev_b32 v2, 16, v177\n"
        "s_waitcnt lgkmcnt(6)\n"
        "v_lshlrev_b32 v4, 16, v178\n"
        "v_lshlrev_b32 v6, 16, v179\n"
        "v_and_b32 v1, 0xffff0000, v176\n"
        "v_and_b32 v3, 0xffff0000, v177\n"
        "v_and_b32 v5, 0xffff0000, v178\n"
        "v_and_b32 v7, 0xffff0000, v179\n"
        "v_pk_mul_f32 v[0:1], v[0:1], s[50:51]\n"
        "v_pk_mul_f32 v[2:3], v[2:3], s[50:51]\n"
        "v_pk_mul_f32 v[4:5], v[4:5], s[50:51]\n"
        "v_pk_mul_f32 v[6:7], v[6:7], s[50:51]\n"
        "v_cvt_pk_bf16_f32 v0, v0, v1\n"
        "v_cvt_pk_bf16_f32 v2, v2, v3\n"
        "v_cvt_pk_bf16_f32 v4, v4, v5\n"
        "v_cvt_pk_bf16_f32 v6, v6, v7\n"
        "buffer_load_dwordx4 v130, s[20:23], s64 offen offset:2048 lds\n"
        "v_accvgpr_write_b32 a44, v0\n"
        "v_accvgpr_write_b32 a45, v2\n"
        "v_accvgpr_write_b32 a46, v4\n"
        "v_accvgpr_write_b32 a47, v6\n"
        "s_waitcnt lgkmcnt(5)\n"
        "v_lshlrev_b32 v8, 16, v196\n"
        "v_lshlrev_b32 v10, 16, v197\n"
        "s_waitcnt lgkmcnt(4)\n"
        "v_lshlrev_b32 v12, 16, v198\n"
        "v_lshlrev_b32 v14, 16, v199\n"
        "s_add_u32 m0, s40, 110592\n"
        "v_and_b32 v9, 0xffff0000, v196\n"
        "v_and_b32 v11, 0xffff0000, v197\n"
        "v_and_b32 v13, 0xffff0000, v198\n"
        "v_and_b32 v15, 0xffff0000, v199\n"
        "s_add_u32 s64, s65, 37248\n"
        "v_pk_mul_f32 v[8:9], v[8:9], s[50:51]\n"
        "v_pk_mul_f32 v[10:11], v[10:11], s[50:51]\n"
        "v_pk_mul_f32 v[12:13], v[12:13], s[50:51]\n"
        "v_pk_mul_f32 v[14:15], v[14:15], s[50:51]\n"
        "buffer_load_dwordx4 v128, s[20:23], s64 offen lds\n"
        "v_cvt_pk_bf16_f32 v8, v8, v9\n"
        "v_cvt_pk_bf16_f32 v10, v10, v11\n"
        "v_cvt_pk_bf16_f32 v12, v12, v13\n"
        "v_cvt_pk_bf16_f32 v14, v14, v15\n"
        "v_accvgpr_write_b32 a60, v8\n"
        "v_accvgpr_write_b32 a61, v10\n"
        "v_accvgpr_write_b32 a62, v12\n"
        "v_accvgpr_write_b32 a63, v14\n"
        "s_waitcnt lgkmcnt(3)\n"
        "v_lshlrev_b32 v0, 16, v216\n"
        "v_lshlrev_b32 v2, 16, v217\n"
        "s_waitcnt lgkmcnt(2)\n"
        "v_lshlrev_b32 v4, 16, v218\n"
        "v_lshlrev_b32 v6, 16, v219\n"
        "v_and_b32 v1, 0xffff0000, v216\n"
        "v_and_b32 v3, 0xffff0000, v217\n"
        "v_and_b32 v5, 0xffff0000, v218\n"
        "v_and_b32 v7, 0xffff0000, v219\n"
        "buffer_load_dwordx4 v129, s[20:23], s64 offen offset:1024 lds\n"
        "v_pk_mul_f32 v[0:1], v[0:1], s[50:51]\n"
        "v_pk_mul_f32 v[2:3], v[2:3], s[50:51]\n"
        "v_pk_mul_f32 v[4:5], v[4:5], s[50:51]\n"
        "v_pk_mul_f32 v[6:7], v[6:7], s[50:51]\n"
        "v_cvt_pk_bf16_f32 v0, v0, v1\n"
        "v_cvt_pk_bf16_f32 v2, v2, v3\n"
        "v_cvt_pk_bf16_f32 v4, v4, v5\n"
        "v_cvt_pk_bf16_f32 v6, v6, v7\n"
        "v_accvgpr_write_b32 a64, v0\n"
        "v_accvgpr_write_b32 a65, v2\n"
        "v_accvgpr_write_b32 a66, v4\n"
        "v_accvgpr_write_b32 a67, v6\n"
        "s_waitcnt lgkmcnt(1)\n"
        "v_lshlrev_b32 v8, 16, v124\n"
        "v_lshlrev_b32 v10, 16, v125\n"
        "s_waitcnt lgkmcnt(0)\n"
        "v_lshlrev_b32 v12, 16, v126\n"
        "v_lshlrev_b32 v14, 16, v127\n"
        "buffer_load_dwordx4 v130, s[20:23], s64 offen offset:2048 lds\n"
        "v_and_b32 v9, 0xffff0000, v124\n"
        "v_and_b32 v11, 0xffff0000, v125\n"
        "v_and_b32 v13, 0xffff0000, v126\n"
        "v_and_b32 v15, 0xffff0000, v127\n"
        "v_pk_mul_f32 v[8:9], v[8:9], s[50:51]\n"
        "v_pk_mul_f32 v[10:11], v[10:11], s[50:51]\n"
        "v_pk_mul_f32 v[12:13], v[12:13], s[50:51]\n"
        "v_pk_mul_f32 v[14:15], v[14:15], s[50:51]\n"
        "s_add_u32 m0, s40, 122880\n"
        "v_cvt_pk_bf16_f32 v8, v8, v9\n"
        "v_cvt_pk_bf16_f32 v10, v10, v11\n"
        "v_cvt_pk_bf16_f32 v12, v12, v13\n"
        "v_cvt_pk_bf16_f32 v14, v14, v15\n"
        "s_add_u32 s64, s65, 74112\n"
        "v_accvgpr_write_b32 a68, v8\n"
        "v_accvgpr_write_b32 a69, v10\n"
        "v_accvgpr_write_b32 a70, v12\n"
        "v_accvgpr_write_b32 a71, v14\n"
        "buffer_load_dwordx4 v128, s[20:23], s64 offen lds\n"
        "buffer_load_dwordx4 v129, s[20:23], s64 offen offset:1024 lds\n"
        "buffer_load_dwordx4 v130, s[20:23], s64 offen offset:2048 lds\n"
        "s_waitcnt vmcnt(20)\n"
        "s_barrier\n"
        "ds_read_b128 a[224:227], v150 offset:0\n"
        "s_waitcnt lgkmcnt(0)\n"
        "v_mfma_f32_16x16x32_bf16 v[96:99], a[0:3], a[224:227], 0\n"
        "ds_read_b128 a[228:231], v151 offset:0\n"
        "ds_read_b128 a[232:235], v150 offset:128\n"
        "s_add_u32 m0, s40, 0\n"
        "s_add_u32 s64, s65, 110976\n"
        "s_waitcnt lgkmcnt(1)\n"
        "v_mfma_f32_16x16x32_bf16 v[96:99], a[4:7], a[228:231], v[96:99]\n"
        "s_waitcnt lgkmcnt(0)\n"
        "ds_read_b128 a[236:239], v151 offset:128\n"
        "ds_read_b128 a[240:243], v150 offset:256\n"
        "v_mfma_f32_16x16x32_bf16 v[96:99], a[8:11], a[232:235], v[96:99]\n"
        "s_waitcnt lgkmcnt(1)\n"
        "ds_read_b128 a[244:247], v151 offset:256\n"
        "buffer_load_dwordx4 v128, s[20:23], s64 offen lds\n"
        "v_mfma_f32_16x16x32_bf16 v[96:99], a[12:15], a[236:239], v[96:99]\n"
        "s_waitcnt lgkmcnt(1)\n"
        "ds_read_b128 a[248:251], v150 offset:1536\n"
        "ds_read_b128 a[216:219], v151 offset:1536\n"
        "v_mfma_f32_16x16x32_bf16 v[96:99], a[16:19], a[240:243], v[96:99]\n"
        "s_waitcnt lgkmcnt(2)\n"
        "ds_read_b128 a[220:223], v150 offset:1664\n"
        "v_mfma_f32_16x16x32_bf16 v[96:99], a[20:23], a[244:247], v[96:99]\n"
        "buffer_load_dwordx4 v129, s[20:23], s64 offen offset:1024 lds\n"
        "v_mfma_f32_16x16x32_bf16 v[100:103], a[24:27], a[224:227], 0\n"
        "v_mfma_f32_16x16x32_bf16 v[100:103], a[28:31], a[228:231], v[100:103]\n"
        "v_mfma_f32_16x16x32_bf16 v[100:103], a[32:35], a[232:235], v[100:103]\n"
        "v_mfma_f32_16x16x32_bf16 v[100:103], a[36:39], a[236:239], v[100:103]\n"
        "buffer_load_dwordx4 v130, s[20:23], s64 offen offset:2048 lds\n"
        "v_mfma_f32_16x16x32_bf16 v[100:103], a[40:43], a[240:243], v[100:103]\n"
        "s_waitcnt vmcnt(19)\n"
        "v_lshlrev_b32 v236, 16, v236\n"
        "v_cvt_pk_bf16_f32 v156, v96, v97\n"
        "v_mfma_f32_16x16x32_bf16 v[100:103], a[44:47], a[244:247], v[100:103]\n"
        "s_add_u32 m0, s40, 12288\n"
        "ds_write_b32 v146, v236 offset:0\n"
        "v_mfma_f32_16x16x32_bf16 v[112:115], a[48:51], a[224:227], 0\n"
        "ds_read_b128 a[224:227], v151 offset:1664\n"
        "s_add_u32 s64, s65, 147840\n"
        "v_cvt_pk_bf16_f32 v157, v98, v99\n"
        "s_waitcnt vmcnt(18)\n"
        "v_lshlrev_b32 v237, 16, v237\n"
        "v_mfma_f32_16x16x32_bf16 v[112:115], a[52:55], a[228:231], v[112:115]\n"
        "ds_read_b128 a[228:231], v150 offset:1792\n"
        "buffer_load_dwordx4 v128, s[20:23], s64 offen lds\n"
        "v_cvt_pk_bf16_f32 v158, v100, v101\n"
        "v_mfma_f32_16x16x32_bf16 v[112:115], a[56:59], a[232:235], v[112:115]\n"
        "ds_read_b128 a[232:235], v151 offset:1792\n"
        "ds_write_b32 v146, v237 offset:256\n"
        "v_mfma_f32_16x16x32_bf16 v[112:115], a[60:63], a[236:239], v[112:115]\n"
        "v_cvt_pk_bf16_f32 v159, v102, v103\n"
        "s_waitcnt vmcnt(18)\n"
        "v_lshlrev_b32 v238, 16, v238\n"
        "s_waitcnt vmcnt(17)\n"
        "v_lshlrev_b32 v239, 16, v239\n"
        "v_mfma_f32_16x16x32_bf16 v[112:115], a[64:67], a[240:243], v[112:115]\n"
        "v_mfma_f32_16x16x32_bf16 v[112:115], a[68:71], a[244:247], v[112:115]\n"
        "s_waitcnt lgkmcnt(7)\n"
        "ds_write_b32 v146, v238 offset:512\n"
        "buffer_load_dwordx4 v129, s[20:23], s64 offen offset:1024 lds\n"
        "v_mfma_f32_16x16x32_bf16 v[96:99], a[0:3], a[248:251], 0\n"
        "s_waitcnt lgkmcnt(7)\n"
        "ds_write_b32 v146, v239 offset:768\n"
        "s_add_u32 s35, s47, 786432\n"
        "s_sub_u32 s46, s35, 100663296\n"
        "s_cmp_lt_u32 s46, 786432\n"
        "s_waitcnt vmcnt(5)\n"
        "s_barrier\n"
        "v_mfma_f32_16x16x32_bf16 v[96:99], a[4:7], a[216:219], v[96:99]\n"
        "s_waitcnt lgkmcnt(7)\n"
        "s_cselect_b32 s35, s46, s35\n"
        "s_add_u32 s41, s30, s40\n"
        "ds_read_b64_tr_b16 a[72:73], v136 offset:0\n"
        "ds_read_b64_tr_b16 a[74:75], v136 offset:1536\n"
        "v_mfma_f32_16x16x32_bf16 v[96:99], a[8:11], a[220:223], v[96:99]\n"
        "s_waitcnt lgkmcnt(7)\n"
        "ds_read_b64_tr_b16 a[76:77], v136 offset:12288\n"
        "ds_read_b64_tr_b16 a[78:79], v136 offset:13824\n"
        "v_cvt_pk_bf16_f32 v160, v112, v113\n"
        "v_mfma_f32_16x16x32_bf16 v[96:99], a[12:15], a[224:227], v[96:99]\n"
        "s_waitcnt lgkmcnt(8)\n"
        "buffer_load_dwordx4 v130, s[20:23], s64 offen offset:2048 lds\n"
        "ds_read_b64_tr_b16 a[80:81], v136 offset:24576\n"
        "v_cvt_pk_bf16_f32 v161, v114, v115\n"
        "v_mfma_f32_16x16x32_bf16 v[96:99], a[16:19], a[228:231], v[96:99]\n"
        "s_waitcnt lgkmcnt(8)\n"
        "ds_read_b64_tr_b16 a[82:83], v136 offset:26112\n"
        "ds_read_b64_tr_b16 a[96:97], v137 offset:0\n"
        "v_cvt_pk_bf16_f32 v166, v112, v113\n"
        "v_mfma_f32_16x16x32_bf16 v[96:99], a[20:23], a[232:235], v[96:99]\n"
        "s_add_u32 m0, s40, 24576\n"
        "ds_read_b64_tr_b16 a[98:99], v137 offset:1536\n"
        "ds_read_b128 a[236:239], v150 offset:12288\n"
        "v_cvt_pk_bf16_f32 v167, v114, v115\n"
        "v_mfma_f32_16x16x32_bf16 v[100:103], a[24:27], a[248:251], 0\n"
        "s_add_u32 s64, s65, 184704\n"
        "ds_read_b64_tr_b16 a[100:101], v137 offset:12288\n"
        "ds_read_b128 a[240:243], v151 offset:12288\n"
        "v_cvt_pk_bf16_f32 v176, v96, v97\n"
        "v_mfma_f32_16x16x32_bf16 v[100:103], a[28:31], a[216:219], v[100:103]\n"
        "buffer_load_dwordx4 v128, s[20:23], s64 offen lds\n"
        "ds_read_b64_tr_b16 a[102:103], v137 offset:13824\n"
        "v_cvt_pk_bf16_f32 v177, v98, v99\n"
        "v_mfma_f32_16x16x32_bf16 v[100:103], a[32:35], a[220:223], v[100:103]\n"
        "ds_read_b128 v[0:3], v232 offset:0\n"
        "ds_read_b128 a[244:247], v150 offset:12416\n"
        "v_mfma_f32_16x16x32_bf16 v[100:103], a[36:39], a[224:227], v[100:103]\n"
        "ds_read_b64_tr_b16 a[104:105], v137 offset:24576\n"
        "ds_read_b64_tr_b16 a[106:107], v137 offset:26112\n"
        "v_mfma_f32_16x16x32_bf16 v[100:103], a[40:43], a[228:231], v[100:103]\n"
        "ds_read_b64_tr_b16 a[120:121], v138 offset:0\n"
        "ds_read_b64_tr_b16 a[122:123], v138 offset:1536\n"
        "s_waitcnt lgkmcnt(5)\n"
        "v_lshl_add_u32 v0, v0, 2, s67\n"
        "v_mfma_f32_16x16x32_bf16 v[100:103], a[44:47], a[232:235], v[100:103]\n"
        "buffer_load_dwordx4 v129, s[20:23], s64 offen offset:1024 lds\n"
        "ds_read_b64_tr_b16 a[124:125], v138 offset:12288\n"
        "v_lshl_add_u32 v1, v1, 2, s67\n"
        "v_mfma_f32_16x16x32_bf16 v[112:115], a[48:51], a[248:251], 0\n"
        "ds_read_b128 a[248:251], v151 offset:12416\n"
        "ds_read_b128 v[16:19], v232 offset:1024\n"
        "v_lshl_add_u32 v2, v2, 2, s67\n"
        "v_mfma_f32_16x16x32_bf16 v[112:115], a[52:55], a[216:219], v[112:115]\n"
        "ds_read_b128 a[216:219], v150 offset:12544\n"
        "ds_read_b64_tr_b16 a[126:127], v138 offset:13824\n"
        "v_cvt_pk_bf16_f32 v178, v100, v101\n"
        "v_mfma_f32_16x16x32_bf16 v[112:115], a[56:59], a[220:223], v[112:115]\n"
        "ds_read_b128 a[220:223], v151 offset:12544\n"
        "ds_read_b64_tr_b16 a[128:129], v138 offset:24576\n"
        "v_cvt_pk_bf16_f32 v179, v102, v103\n"
        "v_mfma_f32_16x16x32_bf16 v[112:115], a[60:63], a[224:227], v[112:115]\n"
        "buffer_load_dwordx4 v130, s[20:23], s64 offen offset:2048 lds\n"
        "ds_read_b64_tr_b16 a[130:131], v138 offset:26112\n"
        "v_lshl_add_u32 v3, v3, 2, s67\n"
        "v_mfma_f32_16x16x32_bf16 v[112:115], a[64:67], a[228:231], v[112:115]\n"
        "ds_read_b128 v[32:35], v232 offset:8192\n"
        "ds_read_b128 v[48:51], v232 offset:9216\n"
        "s_waitcnt lgkmcnt(7)\n"
        "v_lshl_add_u32 v16, v16, 2, s67\n"
        "v_mfma_f32_16x16x32_bf16 v[112:115], a[68:71], a[232:235], v[112:115]\n"
        "ds_read_b128 a[224:227], v150 offset:13824\n"
        "ds_read_b128 a[228:231], v151 offset:13824\n"
        "v_xor_b32 v253, 32, v249\n"
        "v_mfma_f32_16x16x32_bf16 v[96:99], a[0:3], a[236:239], 0\n"
        "ds_read_b128 a[232:235], v150 offset:13952\n"
        "ds_read_b32 v0, v0\n"
        "v_lshl_add_u32 v17, v17, 2, s67\n"
        "v_mfma_f32_16x16x32_bf16 v[96:99], a[4:7], a[240:243], v[96:99]\n"
        "s_xor_b32 m0, s48, 8192\n"
        "ds_read_b32 v1, v1\n"
        "ds_read_b32 v2, v2\n"
        "v_cvt_pk_bf16_f32 v180, v112, v113\n"
        "v_mfma_f32_16x16x32_bf16 v[96:99], a[8:11], a[244:247], v[96:99]\n"
        "s_sub_u32 s64, s36, 2097152\n"
        "ds_read_b128 v[4:7], v233 offset:0\n"
        "ds_read_b32 v3, v3\n"
        "v_cvt_pk_bf16_f32 v181, v114, v115\n"
        "v_mfma_f32_16x16x32_bf16 v[96:99], a[12:15], a[248:251], v[96:99]\n"
        "s_waitcnt lgkmcnt(14)\n"
        "buffer_load_dwordx4 v249, s[12:15], s64 offen lds\n"
        "ds_read_b32 v16, v16\n"
        "v_cvt_pk_bf16_f32 v186, v112, v113\n"
        "v_mfma_f32_16x16x32_bf16 v[96:99], a[16:19], a[216:219], v[96:99]\n"
        "s_waitcnt lgkmcnt(13)\n"
        "ds_read_b32 v17, v17\n"
        "v_cvt_pk_bf16_f32 v187, v114, v115\n"
        "v_lshl_add_u32 v18, v18, 2, s67\n"
        "v_mfma_f32_16x16x32_bf16 v[96:99], a[20:23], a[220:223], v[96:99]\n"
        "ds_read_b32 v18, v18\n"
        "v_lshl_add_u32 v19, v19, 2, s67\n"
        "s_waitcnt lgkmcnt(12)\n"
        "v_lshl_add_u32 v32, v32, 2, s67\n"
        "v_mfma_f32_16x16x32_bf16 v[100:103], a[24:27], a[236:239], 0\n"
        "s_barrier\n"
        "ds_read_b32 v19, v19\n"
        "ds_read_b32 v32, v32\n"
        "v_lshl_add_u32 v33, v33, 2, s67\n"
        "v_mfma_f32_16x16x32_bf16 v[100:103], a[28:31], a[240:243], v[100:103]\n"
        "buffer_load_dwordx4 v253, s[12:15], s64 offen offset:1024 lds\n"
        "ds_read_b128 v[20:23], v233 offset:1024\n"
        "v_cvt_pk_bf16_f32 v196, v96, v97\n"
        "v_mfma_f32_16x16x32_bf16 v[100:103], a[32:35], a[244:247], v[100:103]\n"
        "ds_read_b32 v33, v33\n"
        "v_cvt_pk_bf16_f32 v197, v98, v99\n"
        "v_lshl_add_u32 v34, v34, 2, s67\n"
        "v_mfma_f32_16x16x32_bf16 v[100:103], a[36:39], a[248:251], v[100:103]\n"
        "ds_read_b32 v34, v34\n"
        "v_lshl_add_u32 v35, v35, 2, s67\n"
        "s_waitcnt lgkmcnt(15)\n"
        "v_lshl_add_u32 v48, v48, 2, s67\n"
        "v_mfma_f32_16x16x32_bf16 v[100:103], a[40:43], a[216:219], v[100:103]\n"
        "s_add_u32 m0, s41, 0\n"
        "ds_read_b32 v35, v35\n"
        "ds_read_b32 v48, v48\n"
        "v_lshl_add_u32 v49, v49, 2, s67\n"
        "v_mfma_f32_16x16x32_bf16 v[100:103], a[44:47], a[220:223], v[100:103]\n"
        "buffer_load_dwordx4 v242, s[8:11], s47 offen lds\n"
        "ds_read_b32 v49, v49\n"
        "v_mfma_f32_16x16x32_bf16 v[112:115], a[48:51], a[236:239], 0\n"
        "ds_read_b128 a[236:239], v151 offset:13952\n"
        "ds_read_b128 v[36:39], v233 offset:8192\n"
        "v_lshl_add_u32 v50, v50, 2, s67\n"
        "v_mfma_f32_16x16x32_bf16 v[112:115], a[52:55], a[240:243], v[112:115]\n"
        "ds_read_b128 a[240:243], v150 offset:14080\n"
        "ds_read_b32 v50, v50\n"
        "v_cvt_pk_bf16_f32 v198, v100, v101\n"
        "v_mfma_f32_16x16x32_bf16 v[112:115], a[56:59], a[244:247], v[112:115]\n"
        "ds_read_b128 a[244:247], v151 offset:14080\n"
        "v_cvt_pk_bf16_f32 v199, v102, v103\n"
        "v_lshl_add_u32 v51, v51, 2, s67\n"
        "v_mfma_f32_16x16x32_bf16 v[112:115], a[60:63], a[248:251], v[112:115]\n"
        "s_add_u32 m0, s41, 1024\n"
        "ds_read_b128 a[248:251], v150 offset:0\n"
        "ds_read_b32 v51, v51\n"
        "s_waitcnt lgkmcnt(15)\n"
        "v_lshl_add_u32 v4, v4, 2, s67\n"
        "v_mfma_f32_16x16x32_bf16 v[112:115], a[64:67], a[216:219], v[112:115]\n"
        "buffer_load_dwordx4 v243, s[8:11], s47 offen lds\n"
        "ds_read_b128 a[216:219], v151 offset:0\n"
        "v_mfma_f32_16x16x32_bf16 v[112:115], a[68:71], a[220:223], v[112:115]\n"
        "ds_read_b128 v[52:55], v233 offset:9216\n"
        "ds_read_b128 a[220:223], v150 offset:128\n"
        "v_lshl_add_u32 v5, v5, 2, s67\n"
        "v_mfma_f32_16x16x32_bf16 v[96:99], a[0:3], a[224:227], 0\n"
        "ds_read_b32 v4, v4\n"
        "ds_read_b32 v5, v5\n"
        "v_lshl_add_u32 v6, v6, 2, s67\n"
        "v_mfma_f32_16x16x32_bf16 v[96:99], a[4:7], a[228:231], v[96:99]\n"
        "ds_read_b32 v6, v6\n"
        "v_cvt_pk_bf16_f32 v200, v112, v113\n"
        "v_cvt_pk_bf16_f32 v201, v114, v115\n"
        "v_mfma_f32_16x16x32_bf16 v[96:99], a[8:11], a[232:235], v[96:99]\n"
        "s_waitcnt lgkmcnt(12)\n"
        "s_add_u32 m0, s41, 2048\n"
        "v_cvt_pk_bf16_f32 v206, v112, v113\n"
        "v_cvt_pk_bf16_f32 v207, v114, v115\n"
        "v_lshl_add_u32 v7, v7, 2, s67\n"
        "v_mfma_f32_16x16x32_bf16 v[96:99], a[12:15], a[236:239], v[96:99]\n"
        "s_waitcnt lgkmcnt(10)\n"
        "buffer_load_dwordx4 v244, s[8:11], s47 offen lds\n"
        "ds_read_b32 v7, v7\n"
        "v_mfma_f32_16x16x32_bf16 v[96:99], a[16:19], a[240:243], v[96:99]\n"
        "s_waitcnt lgkmcnt(9)\n"
        "ds_read_b128 v[8:11], v234 offset:0\n"
        "v_lshl_add_u32 v20, v20, 2, s67\n"
        "v_lshl_add_u32 v21, v21, 2, s67\n"
        "v_mfma_f32_16x16x32_bf16 v[96:99], a[20:23], a[244:247], v[96:99]\n"
        "ds_read_b32 v20, v20\n"
        "ds_read_b32 v21, v21\n"
        "v_lshl_add_u32 v22, v22, 2, s67\n"
        "v_mfma_f32_16x16x32_bf16 v[100:103], a[24:27], a[224:227], 0\n"
        "s_waitcnt vmcnt(5)\n"
        "s_barrier\n"
        "ds_read_b32 v22, v22\n"
        "v_lshl_add_u32 v23, v23, 2, s67\n"
        "v_lshl_add_u32 v36, v36, 2, s67\n"
        "v_mfma_f32_16x16x32_bf16 v[100:103], a[28:31], a[228:231], v[100:103]\n"
        "s_add_u32 m0, s41, 12288\n"
        "ds_read_b64_tr_b16 a[84:85], v131 offset:0\n"
        "ds_read_b64_tr_b16 a[86:87], v131 offset:1536\n"
        "ds_read_b64_tr_b16 a[88:89], v131 offset:12288\n"
        "v_mfma_f32_16x16x32_bf16 v[100:103], a[32:35], a[232:235], v[100:103]\n"
        "ds_read_b64_tr_b16 a[90:91], v131 offset:13824\n"
        "ds_read_b64_tr_b16 a[92:93], v131 offset:24576\n"
        "ds_read_b64_tr_b16 a[94:95], v131 offset:26112\n"
        "s_add_u32 s47, s47, 6291456\n"
        "v_mfma_f32_16x16x32_bf16 v[100:103], a[36:39], a[236:239], v[100:103]\n"
        "ds_read_b64_tr_b16 a[108:109], v132 offset:0\n"
        "ds_read_b64_tr_b16 a[110:111], v132 offset:1536\n"
        "ds_read_b64_tr_b16 a[112:113], v132 offset:12288\n"
        "s_sub_u32 s31, s30, s28\n"
        "v_mfma_f32_16x16x32_bf16 v[100:103], a[40:43], a[240:243], v[100:103]\n"
        "ds_read_b64_tr_b16 a[114:115], v132 offset:13824\n"
        "ds_read_b64_tr_b16 a[116:117], v132 offset:24576\n"
        "ds_read_b64_tr_b16 a[118:119], v132 offset:26112\n"
        "s_sub_u32 s45, s28, s29\n"
        "v_mfma_f32_16x16x32_bf16 v[100:103], a[44:47], a[244:247], v[100:103]\n"
        "ds_read_b64_tr_b16 a[132:133], v133 offset:0\n"
        "ds_read_b64_tr_b16 a[134:135], v133 offset:1536\n"
        "ds_read_b64_tr_b16 a[136:137], v133 offset:12288\n"
        "s_add_u32 s28, s28, s31\n"
        "v_mfma_f32_16x16x32_bf16 v[112:115], a[48:51], a[224:227], 0\n"
        "ds_read_b64_tr_b16 a[138:139], v133 offset:13824\n"
        "ds_read_b64_tr_b16 a[140:141], v133 offset:24576\n"
        "ds_read_b64_tr_b16 a[142:143], v133 offset:26112\n"
        "s_mov_b32 s30, s29\n"
        "v_mfma_f32_16x16x32_bf16 v[112:115], a[52:55], a[228:231], v[112:115]\n"
        "buffer_load_dwordx4 v242, s[8:11], s35 offen lds\n"
        "ds_read_b128 a[224:227], v151 offset:128\n"
        "s_add_u32 m0, s41, 13312\n"
        "v_mfma_f32_16x16x32_bf16 v[112:115], a[56:59], a[232:235], v[112:115]\n"
        "ds_read_b128 a[228:231], v150 offset:256\n"
        "buffer_load_dwordx4 v243, s[8:11], s35 offen lds\n"
        "s_add_u32 s29, s29, s45\n"
        "v_mfma_f32_16x16x32_bf16 v[112:115], a[60:63], a[236:239], v[112:115]\n"
        "ds_read_b128 a[232:235], v151 offset:256\n"
        "ds_read_b128 v[24:27], v234 offset:1024\n"
        "v_cvt_pk_bf16_f32 v216, v96, v97\n"
        "v_mfma_f32_16x16x32_bf16 v[112:115], a[64:67], a[240:243], v[112:115]\n"
        "ds_read_b32 v23, v23\n"
        "ds_read_b128 v[40:43], v234 offset:8192\n"
        "v_cvt_pk_bf16_f32 v217, v98, v99\n"
        "v_mfma_f32_16x16x32_bf16 v[112:115], a[68:71], a[244:247], v[112:115]\n"
        "s_waitcnt lgkmcnt(15)\n"
        "s_add_u32 m0, s41, 14336\n"
        "ds_read_b32 v36, v36\n"
        "ds_read_b128 a[236:239], v150 offset:1536\n"
        "v_cvt_pk_bf16_f32 v218, v100, v101\n"
        "v_mfma_f32_16x16x32_bf16 v[96:99], a[72:75], a[248:251], 0\n"
        "s_waitcnt lgkmcnt(15)\n"
        "buffer_load_dwordx4 v244, s[8:11], s35 offen lds\n"
        "ds_read_b128 a[240:243], v151 offset:1536\n"
        "v_mfma_f32_16x16x32_bf16 v[96:99], a[76:79], a[216:219], v[96:99]\n"
        "s_waitcnt lgkmcnt(15)\n"
        "ds_read_b128 a[244:247], v150 offset:1664\n"
        "ds_read_b128 v[56:59], v234 offset:9216\n"
        "v_cvt_pk_bf16_f32 v219, v102, v103\n"
        "v_mfma_f32_16x16x32_bf16 v[96:99], a[80:83], a[220:223], v[96:99]\n"
        "s_waitcnt lgkmcnt(10)\n"
        "v_cvt_pk_bf16_f32 v220, v112, v113\n"
        "v_cvt_pk_bf16_f32 v221, v114, v115\n"
        "v_cvt_pk_bf16_f32 v226, v112, v113\n"
        "v_mfma_f32_16x16x32_bf16 v[96:99], a[84:87], a[224:227], v[96:99]\n"
        "s_waitcnt lgkmcnt(9)\n"
        "s_add_u32 m0, s40, 36864\n"
        "v_cvt_pk_bf16_f32 v227, v114, v115\n"
        "v_lshl_add_u32 v37, v37, 2, s67\n"
        "v_lshl_add_u32 v38, v38, 2, s67\n"
        "v_mfma_f32_16x16x32_bf16 v[96:99], a[88:91], a[228:231], v[96:99]\n"
        "s_waitcnt lgkmcnt(8)\n"
        "s_add_u32 s64, s65, 768\n"
        "ds_read_b32 v37, v37\n"
        "ds_read_b32 v38, v38\n"
        "v_lshl_add_u32 v39, v39, 2, s67\n"
        "v_mfma_f32_16x16x32_bf16 v[96:99], a[92:95], a[232:235], v[96:99]\n"
        "buffer_load_dwordx4 v128, s[20:23], s64 offen lds\n"
        "ds_read_b32 v39, v39\n"
        "v_lshl_add_u32 v52, v52, 2, s67\n"
        "v_mfma_f32_16x16x32_bf16 v[100:103], a[96:99], a[248:251], 0\n"
        "ds_read_b32 v52, v52\n"
        "ds_read_b128 v[12:15], v235 offset:0\n"
        "v_lshl_add_u32 v53, v53, 2, s67\n"
        "v_mfma_f32_16x16x32_bf16 v[100:103], a[100:103], a[216:219], v[100:103]\n"
        "ds_read_b32 v53, v53\n"
        "v_cvt_pk_bf16_f32 v152, v96, v97\n"
        "v_cvt_pk_bf16_f32 v153, v98, v99\n"
        "v_mfma_f32_16x16x32_bf16 v[100:103], a[104:107], a[220:223], v[100:103]\n"
        "v_lshl_add_u32 v54, v54, 2, s67\n"
        "v_lshl_add_u32 v55, v55, 2, s67\n"
        "v_lshl_add_u32 v8, v8, 2, s67\n"
        "v_mfma_f32_16x16x32_bf16 v[100:103], a[108:111], a[224:227], v[100:103]\n"
        "ds_read_b32 v54, v54\n"
        "ds_read_b32 v55, v55\n"
        "v_lshl_add_u32 v9, v9, 2, s67\n"
        "v_mfma_f32_16x16x32_bf16 v[100:103], a[112:115], a[228:231], v[100:103]\n"
        "buffer_load_dwordx4 v129, s[20:23], s64 offen offset:1024 lds\n"
        "ds_read_b32 v8, v8\n"
        "v_lshl_add_u32 v10, v10, 2, s67\n"
        "v_mfma_f32_16x16x32_bf16 v[100:103], a[116:119], a[232:235], v[100:103]\n"
        "ds_read_b32 v9, v9\n"
        "ds_read_b32 v10, v10\n"
        "v_lshl_add_u32 v11, v11, 2, s67\n"
        "v_mfma_f32_16x16x32_bf16 v[112:115], a[120:123], a[248:251], 0\n"
        "ds_read_b128 a[248:251], v151 offset:1664\n"
        "ds_read_b32 v11, v11\n"
        "s_waitcnt lgkmcnt(15)\n"
        "v_lshl_add_u32 v24, v24, 2, s67\n"
        "v_mfma_f32_16x16x32_bf16 v[112:115], a[124:127], a[216:219], v[112:115]\n"
        "ds_read_b128 a[216:219], v150 offset:1792\n"
        "ds_read_b128 v[28:31], v235 offset:1024\n"
        "v_cvt_pk_bf16_f32 v154, v100, v101\n"
        "v_mfma_f32_16x16x32_bf16 v[112:115], a[128:131], a[220:223], v[112:115]\n"
        "ds_read_b128 a[220:223], v151 offset:1792\n"
        "ds_read_b32 v24, v24\n"
        "v_cvt_pk_bf16_f32 v155, v102, v103\n"
        "v_mfma_f32_16x16x32_bf16 v[112:115], a[132:135], a[224:227], v[112:115]\n"
        "buffer_load_dwordx4 v130, s[20:23], s64 offen offset:2048 lds\n"
        "ds_read_b128 a[224:227], v150 offset:12288\n"
        "v_lshl_add_u32 v25, v25, 2, s67\n"
        "v_mfma_f32_16x16x32_bf16 v[112:115], a[136:139], a[228:231], v[112:115]\n"
        "ds_read_b32 v25, v25\n"
        "ds_read_b128 v[44:47], v235 offset:8192\n"
        "v_lshl_add_u32 v26, v26, 2, s67\n"
        "v_mfma_f32_16x16x32_bf16 v[112:115], a[140:143], a[232:235], v[112:115]\n"
        "ds_read_b32 v26, v26\n"
        "ds_read_b128 a[228:231], v151 offset:12288\n"
        "v_lshl_add_u32 v27, v27, 2, s67\n"
        "v_mfma_f32_16x16x32_bf16 v[96:99], a[72:75], a[236:239], 0\n"
        "s_add_u32 m0, s40, 49152\n"
        "ds_read_b32 v27, v27\n"
        "ds_read_b128 a[232:235], v150 offset:12416\n"
        "v_lshl_add_u32 v40, v40, 2, s67\n"
        "v_mfma_f32_16x16x32_bf16 v[96:99], a[76:79], a[240:243], v[96:99]\n"
        "s_waitcnt lgkmcnt(15)\n"
        "s_add_u32 s64, s65, 37632\n"
        "ds_read_b32 v40, v40\n"
        "v_cvt_pk_bf16_f32 v168, v112, v113\n"
        "v_cvt_pk_bf16_f32 v169, v114, v115\n"
        "v_mfma_f32_16x16x32_bf16 v[96:99], a[80:83], a[244:247], v[96:99]\n"
        "s_waitcnt lgkmcnt(13)\n"
        "buffer_load_dwordx4 v128, s[20:23], s64 offen lds\n"
        "v_lshl_add_u32 v41, v41, 2, s67\n"
        "v_lshl_add_u32 v42, v42, 2, s67\n"
        "v_mfma_f32_16x16x32_bf16 v[96:99], a[84:87], a[248:251], v[96:99]\n"
        "s_waitcnt lgkmcnt(11)\n"
        "ds_read_b32 v41, v41\n"
        "ds_read_b32 v42, v42\n"
        "v_lshl_add_u32 v43, v43, 2, s67\n"
        "v_mfma_f32_16x16x32_bf16 v[96:99], a[88:91], a[216:219], v[96:99]\n"
        "s_waitcnt lgkmcnt(11)\n"
        "ds_read_b32 v43, v43\n"
        "ds_read_b128 v[60:63], v235 offset:9216\n"
        "v_lshl_add_u32 v56, v56, 2, s67\n"
        "v_mfma_f32_16x16x32_bf16 v[96:99], a[92:95], a[220:223], v[96:99]\n"
        "ds_read_b32 v56, v56\n"
        "v_lshl_add_u32 v57, v57, 2, s67\n"
        "v_lshl_add_u32 v58, v58, 2, s67\n"
        "v_mfma_f32_16x16x32_bf16 v[100:103], a[96:99], a[236:239], 0\n"
        "ds_read_b32 v57, v57\n"
        "ds_read_b32 v58, v58\n"
        "v_lshl_add_u32 v59, v59, 2, s67\n"
        "v_mfma_f32_16x16x32_bf16 v[100:103], a[100:103], a[240:243], v[100:103]\n"
        "buffer_load_dwordx4 v129, s[20:23], s64 offen offset:1024 lds\n"
        "ds_read_b32 v59, v59\n"
        "v_cvt_pk_bf16_f32 v172, v96, v97\n"
        "v_mfma_f32_16x16x32_bf16 v[100:103], a[104:107], a[244:247], v[100:103]\n"
        "v_cvt_pk_bf16_f32 v173, v98, v99\n"
        "v_lshl_add_u32 v12, v12, 2, s67\n"
        "v_lshl_add_u32 v13, v13, 2, s67\n"
        "v_mfma_f32_16x16x32_bf16 v[100:103], a[108:111], a[248:251], v[100:103]\n"
        "ds_read_b32 v12, v12\n"
        "ds_read_b32 v13, v13\n"
        "v_lshl_add_u32 v14, v14, 2, s67\n"
        "v_mfma_f32_16x16x32_bf16 v[100:103], a[112:115], a[216:219], v[100:103]\n"
        "ds_read_b32 v14, v14\n"
        "v_lshl_add_u32 v15, v15, 2, s67\n"
        "v_lshl_add_u32 v28, v28, 2, s67\n"
        "v_mfma_f32_16x16x32_bf16 v[100:103], a[116:119], a[220:223], v[100:103]\n"
        "ds_read_b32 v15, v15\n"
        "ds_read_b32 v28, v28\n"
        "v_lshl_add_u32 v29, v29, 2, s67\n"
        "v_mfma_f32_16x16x32_bf16 v[112:115], a[120:123], a[236:239], 0\n"
        "ds_read_b128 a[236:239], v151 offset:12416\n"
        "buffer_load_dwordx4 v130, s[20:23], s64 offen offset:2048 lds\n"
        "v_lshl_add_u32 v30, v30, 2, s67\n"
        "v_mfma_f32_16x16x32_bf16 v[112:115], a[124:127], a[240:243], v[112:115]\n"
        "ds_read_b128 a[240:243], v150 offset:12544\n"
        "ds_read_b32 v29, v29\n"
        "v_cvt_pk_bf16_f32 v174, v100, v101\n"
        "v_mfma_f32_16x16x32_bf16 v[112:115], a[128:131], a[244:247], v[112:115]\n"
        "ds_read_b128 a[244:247], v151 offset:12544\n"
        "ds_read_b32 v30, v30\n"
        "v_cvt_pk_bf16_f32 v175, v102, v103\n"
        "v_mfma_f32_16x16x32_bf16 v[112:115], a[132:135], a[248:251], v[112:115]\n"
        "s_add_u32 m0, s40, 61440\n"
        "ds_read_b128 a[248:251], v150 offset:13824\n"
        "v_lshl_add_u32 v31, v31, 2, s67\n"
        "s_waitcnt lgkmcnt(15)\n"
        "v_lshl_add_u32 v44, v44, 2, s67\n"
        "v_mfma_f32_16x16x32_bf16 v[112:115], a[136:139], a[216:219], v[112:115]\n"
        "s_add_u32 s64, s65, 74496\n"
        "ds_read_b32 v31, v31\n"
        "ds_read_b128 a[216:219], v151 offset:13824\n"
        "v_lshl_add_u32 v45, v45, 2, s67\n"
        "v_mfma_f32_16x16x32_bf16 v[112:115], a[140:143], a[220:223], v[112:115]\n"
        "buffer_load_dwordx4 v128, s[20:23], s64 offen lds\n"
        "ds_read_b128 a[220:223], v150 offset:13952\n"
        "v_lshl_add_u32 v46, v46, 2, s67\n"
        "v_mfma_f32_16x16x32_bf16 v[96:99], a[72:75], a[224:227], 0\n"
        "ds_read_b32 v44, v44\n"
        "ds_read_b32 v45, v45\n"
        "v_lshl_add_u32 v47, v47, 2, s67\n"
        "v_mfma_f32_16x16x32_bf16 v[96:99], a[76:79], a[228:231], v[96:99]\n"
        "ds_read_b32 v46, v46\n"
        "ds_read_b32 v47, v47\n"
        "v_cvt_pk_bf16_f32 v190, v112, v113\n"
        "v_mfma_f32_16x16x32_bf16 v[96:99], a[80:83], a[232:235], v[96:99]\n"
        "s_waitcnt lgkmcnt(12)\n"
        "v_cvt_pk_bf16_f32 v191, v114, v115\n"
        "v_lshl_add_u32 v60, v60, 2, s67\n"
        "v_lshl_add_u32 v61, v61, 2, s67\n"
        "v_mfma_f32_16x16x32_bf16 v[96:99], a[84:87], a[236:239], v[96:99]\n"
        "s_waitcnt lgkmcnt(11)\n"
        "ds_read_b32 v60, v60\n"
        "ds_read_b32 v61, v61\n"
        "v_lshl_add_u32 v62, v62, 2, s67\n"
        "v_mfma_f32_16x16x32_bf16 v[96:99], a[88:91], a[240:243], v[96:99]\n"
        "s_waitcnt lgkmcnt(11)\n"
        "buffer_load_dwordx4 v129, s[20:23], s64 offen offset:1024 lds\n"
        "ds_read_b32 v62, v62\n"
        "v_lshl_add_u32 v63, v63, 2, s67\n"
        "v_mfma_f32_16x16x32_bf16 v[96:99], a[92:95], a[244:247], v[96:99]\n"
        "ds_read_b32 v63, v63\n"
        "v_add_u32 v240, s31, v240\n"
        "v_add_u32 v241, s31, v241\n"
        "v_mfma_f32_16x16x32_bf16 v[100:103], a[96:99], a[224:227], 0\n"
        "v_add_u32 v245, s45, v245\n"
        "v_add_u32 v246, s45, v246\n"
        "v_add_u32 v247, s45, v247\n"
        "v_mfma_f32_16x16x32_bf16 v[100:103], a[100:103], a[228:231], v[100:103]\n"
        "v_cvt_pk_bf16_f32 v192, v96, v97\n"
        "v_cvt_pk_bf16_f32 v193, v98, v99\n"
        "v_mfma_f32_16x16x32_bf16 v[100:103], a[104:107], a[232:235], v[100:103]\n"
        "v_mfma_f32_16x16x32_bf16 v[100:103], a[108:111], a[236:239], v[100:103]\n"
        "buffer_load_dwordx4 v130, s[20:23], s64 offen offset:2048 lds\n"
        "v_mfma_f32_16x16x32_bf16 v[100:103], a[112:115], a[240:243], v[100:103]\n"
        "v_mfma_f32_16x16x32_bf16 v[100:103], a[116:119], a[244:247], v[100:103]\n"
        "v_mfma_f32_16x16x32_bf16 v[112:115], a[120:123], a[224:227], 0\n"
        "ds_read_b128 a[224:227], v151 offset:13952\n"
        "v_mfma_f32_16x16x32_bf16 v[112:115], a[124:127], a[228:231], v[112:115]\n"
        "ds_read_b128 a[228:231], v150 offset:14080\n"
        "v_mfma_f32_16x16x32_bf16 v[112:115], a[128:131], a[232:235], v[112:115]\n"
        "ds_read_b128 a[232:235], v151 offset:14080\n"
        "v_mfma_f32_16x16x32_bf16 v[112:115], a[132:135], a[236:239], v[112:115]\n"
        "v_mfma_f32_16x16x32_bf16 v[112:115], a[136:139], a[240:243], v[112:115]\n"
        "v_cvt_pk_bf16_f32 v194, v100, v101\n"
        "v_cvt_pk_bf16_f32 v195, v102, v103\n"
        "v_mfma_f32_16x16x32_bf16 v[112:115], a[140:143], a[244:247], v[112:115]\n"
        "s_waitcnt lgkmcnt(14)\n"
        "s_waitcnt vmcnt(15)\n"
        "s_barrier\n"
        "ds_read_b128 v[162:165], v251 offset:0\n"
        "ds_read_b128 v[182:185], v251 offset:512\n"
        "v_mfma_f32_16x16x32_bf16 v[96:99], a[72:75], a[248:251], 0\n"
        "s_waitcnt lgkmcnt(14)\n"
        "s_add_u32 m0, s40, 0\n"
        "v_mfma_f32_16x16x32_bf16 v[96:99], a[76:79], a[216:219], v[96:99]\n"
        "s_waitcnt lgkmcnt(13)\n"
        "s_add_u32 s64, s65, 111360\n"
        "v_mfma_f32_16x16x32_bf16 v[96:99], a[80:83], a[220:223], v[96:99]\n"
        "s_waitcnt lgkmcnt(4)\n"
        "buffer_load_dwordx4 v128, s[20:23], s64 offen lds\n"
        "ds_read_b128 v[202:205], v251 offset:4096\n"
        "v_cvt_pk_bf16_f32 v208, v112, v113\n"
        "v_mfma_f32_16x16x32_bf16 v[96:99], a[84:87], a[224:227], v[96:99]\n"
        "s_waitcnt lgkmcnt(4)\n"
        "v_cvt_pk_bf16_f32 v209, v114, v115\n"
        "v_mfma_f32_16x16x32_bf16 v[96:99], a[88:91], a[228:231], v[96:99]\n"
        "s_waitcnt lgkmcnt(3)\n"
        "v_mfma_f32_16x16x32_bf16 v[96:99], a[92:95], a[232:235], v[96:99]\n"
        "v_mfma_f32_16x16x32_bf16 v[100:103], a[96:99], a[248:251], 0\n"
        "ds_read_b128 v[222:225], v251 offset:4608\n"
        "v_mfma_f32_16x16x32_bf16 v[100:103], a[100:103], a[216:219], v[100:103]\n"
        "buffer_load_dwordx4 v129, s[20:23], s64 offen offset:1024 lds\n"
        "v_mfma_f32_16x16x32_bf16 v[100:103], a[104:107], a[220:223], v[100:103]\n"
        "s_waitcnt lgkmcnt(3)\n"
        "v_pk_min_u16 v162, v162, s26\n"
        "v_pk_min_u16 v163, v163, s26\n"
        "v_pk_min_u16 v164, v164, s26\n"
        "v_mfma_f32_16x16x32_bf16 v[100:103], a[108:111], a[224:227], v[100:103]\n"
        "v_cvt_pk_bf16_f32 v212, v96, v97\n"
        "v_cvt_pk_bf16_f32 v213, v98, v99\n"
        "v_pk_min_u16 v165, v165, s26\n"
        "v_mfma_f32_16x16x32_bf16 v[100:103], a[112:115], a[228:231], v[100:103]\n"
        "v_mfma_f32_16x16x32_bf16 v[100:103], a[116:119], a[232:235], v[100:103]\n"
        "v_mfma_f32_16x16x32_bf16 v[112:115], a[120:123], a[248:251], 0\n"
        "buffer_load_dwordx4 v130, s[20:23], s64 offen offset:2048 lds\n"
        "s_waitcnt lgkmcnt(2)\n"
        "v_pk_min_u16 v182, v182, s26\n"
        "v_pk_min_u16 v183, v183, s26\n"
        "v_mfma_f32_16x16x32_bf16 v[112:115], a[124:127], a[216:219], v[112:115]\n"
        "v_pk_min_u16 v184, v184, s26\n"
        "v_pk_min_u16 v185, v185, s26\n"
        "v_mfma_f32_16x16x32_bf16 v[112:115], a[128:131], a[220:223], v[112:115]\n"
        "v_cvt_pk_bf16_f32 v214, v100, v101\n"
        "v_cvt_pk_bf16_f32 v215, v102, v103\n"
        "v_mfma_f32_16x16x32_bf16 v[112:115], a[132:135], a[224:227], v[112:115]\n"
        "s_add_u32 m0, s40, 12288\n"
        "v_mfma_f32_16x16x32_bf16 v[112:115], a[136:139], a[228:231], v[112:115]\n"
        "s_add_u32 s64, s65, 148224\n"
        "s_waitcnt lgkmcnt(1)\n"
        "v_pk_min_u16 v202, v202, s26\n"
        "v_pk_min_u16 v203, v203, s26\n"
        "v_pk_min_u16 v204, v204, s26\n"
        "v_mfma_f32_16x16x32_bf16 v[112:115], a[140:143], a[232:235], v[112:115]\n"
        "buffer_load_dwordx4 v128, s[20:23], s64 offen lds\n"
        "v_pk_min_u16 v205, v205, s26\n"
        "v_mfma_f32_16x16x32_bf16 v[64:67], v[152:155], v[156:159], v[0:3]\n"
        "v_mfma_f32_16x16x32_bf16 v[64:67], v[168:171], v[160:163], v[64:67]\n"
        "v_mfma_f32_16x16x32_bf16 v[68:71], v[172:175], v[156:159], v[4:7]\n"
        "s_waitcnt lgkmcnt(0)\n"
        "v_pk_min_u16 v222, v222, s26\n"
        "v_pk_min_u16 v223, v223, s26\n"
        "v_pk_min_u16 v224, v224, s26\n"
        "v_mfma_f32_16x16x32_bf16 v[68:71], v[188:191], v[164:167], v[68:71]\n"
        "ds_read_b128 v[162:165], v252 offset:0\n"
        "v_cvt_pk_bf16_f32 v230, v112, v113\n"
        "v_cvt_pk_bf16_f32 v231, v114, v115\n"
        "v_mfma_f32_16x16x32_bf16 v[80:83], v[152:155], v[176:179], v[16:19]\n"
        "buffer_load_dwordx4 v129, s[20:23], s64 offen offset:1024 lds\n"
        "v_pk_min_u16 v225, v225, s26\n"
        "v_max3_f32 v232, v64, v65, v66\n"
        "v_mfma_f32_16x16x32_bf16 v[80:83], v[168:171], v[180:183], v[80:83]\n"
        "v_max3_f32 v232, v232, v67, v68\n"
        "v_max3_f32 v232, v232, v69, v70\n"
        "v_mfma_f32_16x16x32_bf16 v[84:87], v[172:175], v[176:179], v[20:23]\n"
        "v_mfma_f32_16x16x32_bf16 v[84:87], v[188:191], v[184:187], v[84:87]\n"
        "ds_read_b128 v[182:185], v252 offset:512\n"
        "v_mfma_f32_16x16x32_bf16 v[96:99], v[152:155], v[196:199], v[32:35]\n"
        "v_mfma_f32_16x16x32_bf16 v[96:99], v[168:171], v[200:203], v[96:99]\n"
        "buffer_load_dwordx4 v130, s[20:23], s64 offen offset:2048 lds\n"
        "v_max3_f32 v233, v80, v81, v82\n"
        "v_mfma_f32_16x16x32_bf16 v[100:103], v[172:175], v[196:199], v[36:39]\n"
        "v_mfma_f32_16x16x32_bf16 v[100:103], v[188:191], v[204:207], v[100:103]\n"
        "ds_read_b128 v[202:205], v252 offset:4096\n"
        "s_waitcnt vmcnt(15)\n"
        "s_barrier\n"
        "v_max3_f32 v233, v233, v83, v84\n"
        "v_max3_f32 v233, v233, v85, v86\n"
        "v_mfma_f32_16x16x32_bf16 v[112:115], v[152:155], v[216:219], v[48:51]\n"
        "s_add_u32 m0, s40, 24576\n"
        "ds_read_b128 a[236:239], v240 offset:0\n"
        "s_waitcnt lgkmcnt(3)\n"
        "v_pk_min_u16 v162, v162, s26\n"
        "v_pk_min_u16 v163, v163, s26\n"
        "v_mfma_f32_16x16x32_bf16 v[112:115], v[168:171], v[220:223], v[112:115]\n"
        "s_add_u32 s64, s65, 185088\n"
        "ds_read_b128 a[240:243], v241 offset:0\n"
        "v_pk_min_u16 v164, v164, s26\n"
        "v_pk_min_u16 v165, v165, s26\n"
        "v_mfma_f32_16x16x32_bf16 v[116:119], v[172:175], v[216:219], v[52:55]\n"
        "buffer_load_dwordx4 v128, s[20:23], s64 offen lds\n"
        "ds_read_b128 a[244:247], v240 offset:128\n"
        "v_max3_f32 v234, v96, v97, v98\n"
        "v_mfma_f32_16x16x32_bf16 v[116:119], v[188:191], v[224:227], v[116:119]\n"
        "ds_read_b128 v[222:225], v252 offset:4608\n"
        "ds_read_b128 a[248:251], v241 offset:128\n"
        "v_max3_f32 v234, v234, v99, v100\n"
        "v_mfma_f32_16x16x32_bf16 v[72:75], v[192:195], v[156:159], v[8:11]\n"
        "ds_read_b128 a[216:219], v240 offset:256\n"
        "s_waitcnt lgkmcnt(7)\n"
        "v_pk_min_u16 v182, v182, s26\n"
        "v_pk_min_u16 v183, v183, s26\n"
        "v_mfma_f32_16x16x32_bf16 v[72:75], v[208:211], v[160:163], v[72:75]\n"
        "ds_read_b128 a[220:223], v241 offset:256\n"
        "v_pk_min_u16 v184, v184, s26\n"
        "v_pk_min_u16 v185, v185, s26\n"
        "v_mfma_f32_16x16x32_bf16 v[76:79], v[212:215], v[156:159], v[12:15]\n"
        "v_max3_f32 v234, v234, v101, v102\n"
        "v_max3_f32 v235, v112, v113, v114\n"
        "v_max3_f32 v235, v235, v115, v116\n"
        "v_mfma_f32_16x16x32_bf16 v[76:79], v[228:231], v[164:167], v[76:79]\n"
        "buffer_load_dwordx4 v129, s[20:23], s64 offen offset:1024 lds\n"
        "v_max3_f32 v232, v232, v71, v72\n"
        "v_max3_f32 v232, v232, v73, v74\n"
        "v_mfma_f32_16x16x32_bf16 v[88:91], v[192:195], v[176:179], v[24:27]\n"
        "s_waitcnt lgkmcnt(7)\n"
        "v_pk_min_u16 v202, v202, s26\n"
        "v_pk_min_u16 v203, v203, s26\n"
        "v_pk_min_u16 v204, v204, s26\n"
        "v_mfma_f32_16x16x32_bf16 v[88:91], v[208:211], v[180:183], v[88:91]\n"
        "v_pk_min_u16 v205, v205, s26\n"
        "v_max3_f32 v232, v232, v75, v76\n"
        "v_max3_f32 v232, v232, v77, v78\n"
        "v_mfma_f32_16x16x32_bf16 v[92:95], v[212:215], v[176:179], v[28:31]\n"
        "v_max_f32 v232, v232, v79\n"
        "v_max3_f32 v235, v235, v117, v118\n"
        "v_xor_b32 v251, 0x2000, v251\n"
        "v_mfma_f32_16x16x32_bf16 v[92:95], v[228:231], v[184:187], v[92:95]\n"
        "v_max3_f32 v233, v233, v87, v88\n"
        "v_max3_f32 v233, v233, v89, v90\n"
        "v_xor_b32 v252, 0x2000, v252\n"
        "v_mfma_f32_16x16x32_bf16 v[104:107], v[192:195], v[196:199], v[40:43]\n"
        "buffer_load_dwordx4 v130, s[20:23], s64 offen offset:2048 lds\n"
        "s_waitcnt lgkmcnt(3)\n"
        "v_pk_min_u16 v222, v222, s26\n"
        "v_pk_min_u16 v223, v223, s26\n"
        "v_mfma_f32_16x16x32_bf16 v[104:107], v[208:211], v[200:203], v[104:107]\n"
        "v_pk_min_u16 v224, v224, s26\n"
        "v_pk_min_u16 v225, v225, s26\n"
        "v_max3_f32 v233, v233, v91, v92\n"
        "v_mfma_f32_16x16x32_bf16 v[108:111], v[212:215], v[196:199], v[44:47]\n"
        "s_waitcnt vmcnt(9)\n"
        "s_barrier\n"
        "v_max3_f32 v233, v233, v93, v94\n"
        "v_max_f32 v233, v233, v95\n"
        "v_mfma_f32_16x16x32_bf16 v[108:111], v[228:231], v[204:207], v[108:111]\n"
        "ds_read_b64_tr_b16 a[144:145], v131 offset:36864\n"
        "ds_read_b64_tr_b16 a[146:147], v131 offset:38400\n"
        "v_max3_f32 v234, v234, v103, v104\n"
        "v_mfma_f32_16x16x32_bf16 v[120:123], v[192:195], v[216:219], v[56:59]\n"
        "s_mov_b32 m0, s48\n"
        "s_xor_b32 s48, s48, 8192\n"
        "ds_read_b64_tr_b16 a[148:149], v131 offset:49152\n"
        "ds_read_b64_tr_b16 a[150:151], v131 offset:50688\n"
        "v_mfma_f32_16x16x32_bf16 v[120:123], v[208:211], v[220:223], v[120:123]\n"
        "buffer_load_dwordx4 v249, s[12:15], s36 offen offset:0 lds\n"
        "ds_read_b64_tr_b16 a[152:153], v131 offset:61440\n"
        "v_mfma_f32_16x16x32_bf16 v[124:127], v[212:215], v[216:219], v[60:63]\n"
        "ds_read_b64_tr_b16 a[154:155], v131 offset:62976\n"
        "ds_read_b64_tr_b16 a[168:169], v132 offset:36864\n"
        "v_xor_b32 v253, 32, v249\n"
        "v_mfma_f32_16x16x32_bf16 v[124:127], v[228:231], v[224:227], v[124:127]\n"
        "ds_read_b64_tr_b16 a[170:171], v132 offset:38400\n"
        "ds_read_b64_tr_b16 a[172:173], v132 offset:49152\n"
        "v_max3_f32 v234, v234, v105, v106\n"
        "v_mfma_f32_16x16x32_bf16 v[212:215], a[72:75], a[236:239], 0\n"
        "ds_read_b64_tr_b16 a[174:175], v132 offset:50688\n"
        "ds_read_b64_tr_b16 a[176:177], v132 offset:61440\n"
        "v_max3_f32 v234, v234, v107, v108\n"
        "v_mfma_f32_16x16x32_bf16 v[212:215], a[76:79], a[240:243], v[212:215]\n"
        "ds_read_b64_tr_b16 a[178:179], v132 offset:62976\n"
        "ds_read_b64_tr_b16 a[192:193], v133 offset:36864\n"
        "v_max3_f32 v234, v234, v109, v110\n"
        "v_mfma_f32_16x16x32_bf16 v[212:215], a[80:83], a[244:247], v[212:215]\n"
        "s_waitcnt lgkmcnt(15)\n"
        "buffer_load_dwordx4 v253, s[12:15], s36 offen offset:1024 lds\n"
        "s_add_u32 s36, s36, 2097152\n"
        "ds_read_b64_tr_b16 a[194:195], v133 offset:38400\n"
        "v_mfma_f32_16x16x32_bf16 v[212:215], a[84:87], a[248:251], v[212:215]\n"
        "s_waitcnt lgkmcnt(15)\n"
        "ds_read_b64_tr_b16 a[196:197], v133 offset:49152\n"
        "ds_read_b64_tr_b16 a[198:199], v133 offset:50688\n"
        "v_max_f32 v234, v234, v111\n"
        "v_mfma_f32_16x16x32_bf16 v[212:215], a[88:91], a[216:219], v[212:215]\n"
        "s_waitcnt lgkmcnt(15)\n"
        "ds_read_b64_tr_b16 a[200:201], v133 offset:61440\n"
        "ds_read_b64_tr_b16 a[202:203], v133 offset:62976\n"
        "v_max3_f32 v235, v235, v119, v120\n"
        "v_mfma_f32_16x16x32_bf16 v[212:215], a[92:95], a[220:223], v[212:215]\n"
        "v_permlane32_swap_b32 v232, v233\n"
        "v_max_f32 v232, v232, v233\n"
        "v_mfma_f32_16x16x32_bf16 v[216:219], a[96:99], a[236:239], 0\n"
        "v_mov_b32 v233, v232\n"
        "v_max3_f32 v235, v235, v121, v122\n"
        "v_max3_f32 v235, v235, v123, v124\n"
        "v_mfma_f32_16x16x32_bf16 v[216:219], a[100:103], a[240:243], v[216:219]\n"
        "v_permlane16_swap_b32 v232, v233\n"
        "v_max_f32 v232, v232, v233\n"
        "v_mfma_f32_16x16x32_bf16 v[216:219], a[104:107], a[244:247], v[216:219]\n"
        "v_mul_f32 v236, s25, v232\n"
        "v_mul_f32 v237, s25, v232\n"
        "v_max3_f32 v235, v235, v125, v126\n"
        "v_mfma_f32_16x16x32_bf16 v[216:219], a[108:111], a[248:251], v[216:219]\n"
        "v_permlane32_swap_b32 v236, v237\n"
        "v_max_f32 v235, v235, v127\n"
        "v_mfma_f32_16x16x32_bf16 v[216:219], a[112:115], a[216:219], v[216:219]\n"
        "v_cvt_pk_bf16_f32 v152, v212, v213\n"
        "v_permlane32_swap_b32 v234, v235\n"
        "v_mfma_f32_16x16x32_bf16 v[216:219], a[116:119], a[220:223], v[216:219]\n"
        "v_max_f32 v234, v234, v235\n"
        "v_mov_b32 v235, v234\n"
        "v_cvt_pk_bf16_f32 v153, v214, v215\n"
        "v_mfma_f32_16x16x32_bf16 v[162:165], a[120:123], a[236:239], 0\n"
        "v_permlane16_swap_b32 v234, v235\n"
        "v_max_f32 v234, v234, v235\n"
        "v_mfma_f32_16x16x32_bf16 v[162:165], a[124:127], a[240:243], v[162:165]\n"
        "v_mul_f32 v238, s25, v234\n"
        "v_mul_f32 v239, s25, v234\n"
        "v_cvt_pk_bf16_f32 v154, v216, v217\n"
        "v_mfma_f32_16x16x32_bf16 v[162:165], a[128:131], a[244:247], v[162:165]\n"
        "ds_read_b128 a[224:227], v240 offset:1536\n"
        "v_permlane32_swap_b32 v238, v239\n"
        "v_mfma_f32_16x16x32_bf16 v[162:165], a[132:135], a[248:251], v[162:165]\n"
        "ds_read_b128 a[228:231], v241 offset:1536\n"
        "v_cvt_pk_bf16_f32 v155, v218, v219\n"
        "v_fma_f32 v64, v64, s24, v236\n"
        "v_mfma_f32_16x16x32_bf16 v[162:165], a[136:139], a[216:219], v[162:165]\n"
        "ds_read_b128 a[232:235], v240 offset:1664\n"
        "v_exp_f32 v64, v64\n"
        "v_mfma_f32_16x16x32_bf16 v[162:165], a[140:143], a[220:223], v[162:165]\n"
        "v_fma_f32 v65, v65, s24, v236\n"
        "v_exp_f32 v65, v65\n"
        "v_mfma_f32_16x16x32_bf16 v[212:215], a[0:3], a[236:239], 0\n"
        "v_cvt_pk_bf16_f32 v64, v64, v65\n"
        "v_fma_f32 v66, v66, s24, v236\n"
        "v_fma_f32 v67, v67, s24, v236\n"
        "v_mfma_f32_16x16x32_bf16 v[212:215], a[4:7], a[240:243], v[212:215]\n"
        "v_exp_f32 v66, v66\n"
        "v_cvt_pk_bf16_f32 v168, v162, v163\n"
        "v_mfma_f32_16x16x32_bf16 v[212:215], a[8:11], a[244:247], v[212:215]\n"
        "v_exp_f32 v67, v67\n"
        "v_cvt_pk_bf16_f32 v169, v164, v165\n"
        "v_mfma_f32_16x16x32_bf16 v[212:215], a[12:15], a[248:251], v[212:215]\n"
        "v_cvt_pk_bf16_f32 v65, v66, v67\n"
        "v_fma_f32 v68, v68, s24, v236\n"
        "v_fma_f32 v69, v69, s24, v236\n"
        "v_mfma_f32_16x16x32_bf16 v[212:215], a[16:19], a[216:219], v[212:215]\n"
        "v_exp_f32 v68, v68\n"
        "v_fma_f32 v70, v70, s24, v236\n"
        "v_mfma_f32_16x16x32_bf16 v[212:215], a[20:23], a[220:223], v[212:215]\n"
        "v_exp_f32 v69, v69\n"
        "v_fma_f32 v71, v71, s24, v236\n"
        "v_mfma_f32_16x16x32_bf16 v[216:219], a[24:27], a[236:239], 0\n"
        "s_waitcnt lgkmcnt(3)\n"
        "s_waitcnt vmcnt(2)\n"
        "s_barrier\n"
        "v_exp_f32 v70, v70\n"
        "v_cvt_pk_bf16_f32 v66, v68, v69\n"
        "v_mfma_f32_16x16x32_bf16 v[216:219], a[28:31], a[240:243], v[216:219]\n"
        "ds_read_b64_tr_b16 a[156:157], v131 offset:0\n"
        "s_add_u32 m0, s65, 36864\n"
        "ds_read_b64_tr_b16 a[158:159], v131 offset:1536\n"
        "v_cvt_pk_bf16_f32 v156, v212, v213\n"
        "v_mfma_f32_16x16x32_bf16 v[216:219], a[32:35], a[244:247], v[216:219]\n"
        "s_add_u32 s64, s65, 36864\n"
        "ds_read_b64_tr_b16 a[160:161], v131 offset:12288\n"
        "ds_read_b64_tr_b16 a[162:163], v131 offset:13824\n"
        "v_cvt_pk_bf16_f32 v157, v214, v215\n"
        "v_mfma_f32_16x16x32_bf16 v[216:219], a[36:39], a[248:251], v[216:219]\n"
        "buffer_load_dwordx4 v142, s[52:55], s64 offen lds\n"
        "ds_read_b64_tr_b16 a[164:165], v131 offset:24576\n"
        "v_fma_f32 v72, v72, s24, v236\n"
        "v_mfma_f32_16x16x32_bf16 v[216:219], a[40:43], a[216:219], v[216:219]\n"
        "ds_read_b64_tr_b16 a[166:167], v131 offset:26112\n"
        "ds_read_b64_tr_b16 a[180:181], v132 offset:0\n"
        "v_fma_f32 v73, v73, s24, v236\n"
        "v_mfma_f32_16x16x32_bf16 v[216:219], a[44:47], a[220:223], v[216:219]\n"
        "ds_read_b64_tr_b16 a[182:183], v132 offset:1536\n"
        "ds_read_b64_tr_b16 a[184:185], v132 offset:12288\n"
        "v_fma_f32 v74, v74, s24, v236\n"
        "v_mfma_f32_16x16x32_bf16 v[162:165], a[48:51], a[236:239], 0\n"
        "ds_read_b128 a[236:239], v241 offset:1664\n"
        "ds_read_b64_tr_b16 a[186:187], v132 offset:13824\n"
        "v_fma_f32 v75, v75, s24, v236\n"
        "v_mfma_f32_16x16x32_bf16 v[162:165], a[52:55], a[240:243], v[162:165]\n"
        "ds_read_b128 a[240:243], v240 offset:1792\n"
        "ds_read_b64_tr_b16 a[188:189], v132 offset:24576\n"
        "v_cvt_pk_bf16_f32 v158, v216, v217\n"
        "v_mfma_f32_16x16x32_bf16 v[162:165], a[56:59], a[244:247], v[162:165]\n"
        "ds_read_b128 a[244:247], v241 offset:1792\n"
        "buffer_load_dwordx4 v142, s[52:55], s64 offen offset:1024 lds\n"
        "v_cvt_pk_bf16_f32 v159, v218, v219\n"
        "v_mfma_f32_16x16x32_bf16 v[162:165], a[60:63], a[248:251], v[162:165]\n"
        "ds_read_b64_tr_b16 a[190:191], v132 offset:26112\n"
        "ds_read_b64_tr_b16 a[204:205], v133 offset:0\n"
        "v_fma_f32 v76, v76, s24, v236\n"
        "v_mfma_f32_16x16x32_bf16 v[162:165], a[64:67], a[216:219], v[162:165]\n"
        "ds_read_b64_tr_b16 a[206:207], v133 offset:1536\n"
        "ds_read_b64_tr_b16 a[208:209], v133 offset:12288\n"
        "v_fma_f32 v77, v77, s24, v236\n"
        "v_mfma_f32_16x16x32_bf16 v[162:165], a[68:71], a[220:223], v[162:165]\n"
        "s_waitcnt lgkmcnt(15)\n"
        "ds_read_b64_tr_b16 a[210:211], v133 offset:13824\n"
        "ds_read_b64_tr_b16 a[212:213], v133 offset:24576\n"
        "v_fma_f32 v78, v78, s24, v236\n"
        "v_mfma_f32_16x16x32_bf16 v[212:215], a[72:75], a[224:227], 0\n"
        "s_waitcnt lgkmcnt(15)\n"
        "ds_read_b64_tr_b16 a[214:215], v133 offset:26112\n"
        "v_exp_f32 v71, v71\n"
        "v_mfma_f32_16x16x32_bf16 v[212:215], a[76:79], a[228:231], v[212:215]\n"
        "s_waitcnt lgkmcnt(15)\n"
        "buffer_load_dwordx4 v142, s[52:55], s64 offen offset:2048 lds\n"
        "v_exp_f32 v72, v72\n"
        "v_mfma_f32_16x16x32_bf16 v[212:215], a[80:83], a[232:235], v[212:215]\n"
        "s_waitcnt lgkmcnt(11)\n"
        "v_exp_f32 v73, v73\n"
        "v_cvt_pk_bf16_f32 v160, v162, v163\n"
        "v_mfma_f32_16x16x32_bf16 v[212:215], a[84:87], a[236:239], v[212:215]\n"
        "s_waitcnt lgkmcnt(9)\n"
        "v_exp_f32 v74, v74\n"
        "v_cvt_pk_bf16_f32 v161, v164, v165\n"
        "v_mfma_f32_16x16x32_bf16 v[212:215], a[88:91], a[240:243], v[212:215]\n"
        "s_waitcnt lgkmcnt(7)\n"
        "s_add_u32 m0, s65, 39936\n"
        "v_cvt_pk_bf16_f32 v166, v162, v163\n"
        "v_cvt_pk_bf16_f32 v167, v164, v165\n"
        "v_cvt_pk_bf16_f32 v67, v70, v71\n"
        "v_mfma_f32_16x16x32_bf16 v[212:215], a[92:95], a[244:247], v[212:215]\n"
        "s_add_u32 s64, s65, 39936\n"
        "v_exp_f32 v75, v75\n"
        "v_cvt_pk_bf16_f32 v68, v72, v73\n"
        "v_mfma_f32_16x16x32_bf16 v[216:219], a[96:99], a[224:227], 0\n"
        "buffer_load_dwordx4 v142, s[52:55], s64 offen lds\n"
        "v_exp_f32 v76, v76\n"
        "v_mfma_f32_16x16x32_bf16 v[216:219], a[100:103], a[228:231], v[216:219]\n"
        "v_exp_f32 v77, v77\n"
        "v_cvt_pk_bf16_f32 v172, v212, v213\n"
        "v_mfma_f32_16x16x32_bf16 v[216:219], a[104:107], a[232:235], v[216:219]\n"
        "v_exp_f32 v78, v78\n"
        "v_cvt_pk_bf16_f32 v173, v214, v215\n"
        "v_mfma_f32_16x16x32_bf16 v[216:219], a[108:111], a[236:239], v[216:219]\n"
        "v_cvt_pk_bf16_f32 v69, v74, v75\n"
        "v_cvt_pk_bf16_f32 v70, v76, v77\n"
        "v_fma_f32 v79, v79, s24, v236\n"
        "v_mfma_f32_16x16x32_bf16 v[216:219], a[112:115], a[240:243], v[216:219]\n"
        "s_waitcnt lgkmcnt(0)\n"
        "s_barrier\n"
        "v_exp_f32 v79, v79\n"
        "v_fma_f32 v80, v80, s24, v237\n"
        "v_mfma_f32_16x16x32_bf16 v[216:219], a[116:119], a[244:247], v[216:219]\n"
        "buffer_load_dwordx4 v142, s[52:55], s64 offen offset:1024 lds\n"
        "v_exp_f32 v80, v80\n"
        "v_mfma_f32_16x16x32_bf16 v[162:165], a[120:123], a[224:227], 0\n"
        "v_cvt_pk_bf16_f32 v71, v78, v79\n"
        "v_fma_f32 v81, v81, s24, v237\n"
        "v_fma_f32 v82, v82, s24, v237\n"
        "v_mfma_f32_16x16x32_bf16 v[162:165], a[124:127], a[228:231], v[162:165]\n"
        "v_exp_f32 v81, v81\n"
        "v_cvt_pk_bf16_f32 v174, v216, v217\n"
        "v_mfma_f32_16x16x32_bf16 v[162:165], a[128:131], a[232:235], v[162:165]\n"
        "ds_read_b128 a[248:251], v240 offset:12288\n"
        "v_exp_f32 v82, v82\n"
        "v_mfma_f32_16x16x32_bf16 v[162:165], a[132:135], a[236:239], v[162:165]\n"
        "ds_read_b128 a[216:219], v241 offset:12288\n"
        "v_cvt_pk_bf16_f32 v175, v218, v219\n"
        "v_cvt_pk_bf16_f32 v80, v80, v81\n"
        "v_mfma_f32_16x16x32_bf16 v[162:165], a[136:139], a[240:243], v[162:165]\n"
        "buffer_load_dwordx4 v142, s[52:55], s64 offen offset:2048 lds\n"
        "ds_read_b128 a[220:223], v240 offset:12416\n"
        "v_fma_f32 v83, v83, s24, v237\n"
        "v_mfma_f32_16x16x32_bf16 v[162:165], a[140:143], a[244:247], v[162:165]\n"
        "v_exp_f32 v83, v83\n"
        "v_fma_f32 v84, v84, s24, v237\n"
        "v_mfma_f32_16x16x32_bf16 v[212:215], a[0:3], a[224:227], 0\n"
        "v_exp_f32 v84, v84\n"
        "v_cvt_pk_bf16_f32 v81, v82, v83\n"
        "v_mfma_f32_16x16x32_bf16 v[212:215], a[4:7], a[228:231], v[212:215]\n"
        "s_add_u32 m0, s65, 43008\n"
        "v_fma_f32 v85, v85, s24, v237\n"
        "v_exp_f32 v85, v85\n"
        "v_mfma_f32_16x16x32_bf16 v[212:215], a[8:11], a[232:235], v[212:215]\n"
        "s_add_u32 s64, s65, 43008\n"
        "v_cvt_pk_bf16_f32 v190, v162, v163\n"
        "v_cvt_pk_bf16_f32 v191, v164, v165\n"
        "v_cvt_pk_bf16_f32 v82, v84, v85\n"
        "v_mfma_f32_16x16x32_bf16 v[212:215], a[12:15], a[236:239], v[212:215]\n"
        "buffer_load_dwordx4 v142, s[52:55], s64 offen lds\n"
        "v_fma_f32 v86, v86, s24, v237\n"
        "v_fma_f32 v87, v87, s24, v237\n"
        "v_mfma_f32_16x16x32_bf16 v[212:215], a[16:19], a[240:243], v[212:215]\n"
        "v_exp_f32 v86, v86\n"
        "v_fma_f32 v88, v88, s24, v237\n"
        "v_mfma_f32_16x16x32_bf16 v[212:215], a[20:23], a[244:247], v[212:215]\n"
        "v_exp_f32 v87, v87\n"
        "v_fma_f32 v89, v89, s24, v237\n"
        "v_mfma_f32_16x16x32_bf16 v[216:219], a[24:27], a[224:227], 0\n"
        "v_exp_f32 v88, v88\n"
        "v_cvt_pk_bf16_f32 v83, v86, v87\n"
        "v_mfma_f32_16x16x32_bf16 v[216:219], a[28:31], a[228:231], v[216:219]\n"
        "v_exp_f32 v89, v89\n"
        "v_fma_f32 v90, v90, s24, v237\n"
        "v_mfma_f32_16x16x32_bf16 v[216:219], a[32:35], a[232:235], v[216:219]\n"
        "buffer_load_dwordx4 v142, s[52:55], s64 offen offset:1024 lds\n"
        "v_exp_f32 v90, v90\n"
        "v_mfma_f32_16x16x32_bf16 v[216:219], a[36:39], a[236:239], v[216:219]\n"
        "v_cvt_pk_bf16_f32 v176, v212, v213\n"
        "v_cvt_pk_bf16_f32 v177, v214, v215\n"
        "v_cvt_pk_bf16_f32 v84, v88, v89\n"
        "v_mfma_f32_16x16x32_bf16 v[216:219], a[40:43], a[240:243], v[216:219]\n"
        "v_fma_f32 v91, v91, s24, v237\n"
        "v_exp_f32 v91, v91\n"
        "v_mfma_f32_16x16x32_bf16 v[216:219], a[44:47], a[244:247], v[216:219]\n"
        "v_cvt_pk_bf16_f32 v85, v90, v91\n"
        "v_fma_f32 v92, v92, s24, v237\n"
        "v_fma_f32 v93, v93, s24, v237\n"
        "v_mfma_f32_16x16x32_bf16 v[162:165], a[48:51], a[224:227], 0\n"
        "ds_read_b128 a[224:227], v241 offset:12416\n"
        "v_exp_f32 v92, v92\n"
        "v_mfma_f32_16x16x32_bf16 v[162:165], a[52:55], a[228:231], v[162:165]\n"
        "ds_read_b128 a[228:231], v240 offset:12544\n"
        "buffer_load_dwordx4 v142, s[52:55], s64 offen offset:2048 lds\n"
        "v_cvt_pk_bf16_f32 v178, v216, v217\n"
        "v_mfma_f32_16x16x32_bf16 v[162:165], a[56:59], a[232:235], v[162:165]\n"
        "ds_read_b128 a[232:235], v241 offset:12544\n"
        "v_exp_f32 v93, v93\n"
        "v_mfma_f32_16x16x32_bf16 v[162:165], a[60:63], a[236:239], v[162:165]\n"
        "v_cvt_pk_bf16_f32 v179, v218, v219\n"
        "v_cvt_pk_bf16_f32 v86, v92, v93\n"
        "v_fma_f32 v94, v94, s24, v237\n"
        "v_mfma_f32_16x16x32_bf16 v[162:165], a[64:67], a[240:243], v[162:165]\n"
        "s_add_u32 m0, s65, 0\n"
        "v_exp_f32 v94, v94\n"
        "v_fma_f32 v95, v95, s24, v237\n"
        "v_mfma_f32_16x16x32_bf16 v[162:165], a[68:71], a[244:247], v[162:165]\n"
        "s_waitcnt lgkmcnt(5)\n"
        "s_add_u32 s64, s65, 0\n"
        "v_exp_f32 v95, v95\n"
        "v_fma_f32 v96, v96, s24, v238\n"
        "v_mfma_f32_16x16x32_bf16 v[72:75], a[72:75], a[248:251], 0\n"
        "s_waitcnt lgkmcnt(4)\n"
        "buffer_load_dwordx4 v142, s[52:55], s64 offen lds\n"
        "v_exp_f32 v96, v96\n"
        "v_mfma_f32_16x16x32_bf16 v[72:75], a[76:79], a[216:219], v[72:75]\n"
        "s_waitcnt lgkmcnt(3)\n"
        "v_cvt_pk_bf16_f32 v87, v94, v95\n"
        "v_cvt_pk_bf16_f32 v180, v162, v163\n"
        "v_cvt_pk_bf16_f32 v181, v164, v165\n"
        "v_mfma_f32_16x16x32_bf16 v[72:75], a[80:83], a[220:223], v[72:75]\n"
        "s_waitcnt lgkmcnt(2)\n"
        "v_cvt_pk_bf16_f32 v186, v162, v163\n"
        "v_cvt_pk_bf16_f32 v187, v164, v165\n"
        "v_fma_f32 v97, v97, s24, v238\n"
        "v_mfma_f32_16x16x32_bf16 v[72:75], a[84:87], a[224:227], v[72:75]\n"
        "s_waitcnt lgkmcnt(1)\n"
        "v_exp_f32 v97, v97\n"
        "v_fma_f32 v98, v98, s24, v238\n"
        "v_mfma_f32_16x16x32_bf16 v[72:75], a[88:91], a[228:231], v[72:75]\n"
        "s_waitcnt lgkmcnt(0)\n"
        "v_exp_f32 v98, v98\n"
        "v_accvgpr_read_b32 v232, a252\n"
        "v_mfma_f32_16x16x32_bf16 v[72:75], a[92:95], a[232:235], v[72:75]\n"
        "buffer_load_dwordx4 v142, s[52:55], s64 offen offset:1024 lds\n"
        "v_accvgpr_read_b32 v233, a253\n"
        "v_accvgpr_read_b32 v234, a254\n"
        "v_mfma_f32_16x16x32_bf16 v[76:79], a[96:99], a[248:251], 0\n"
        "v_mov_b32 v235, 0x3f803f80\n"
        "v_accvgpr_write_b32 a252, v235\n"
        "v_accvgpr_write_b32 a253, v235\n"
        "v_mfma_f32_16x16x32_bf16 v[76:79], a[100:103], a[216:219], v[76:79]\n"
        "v_cvt_pk_bf16_f32 v192, v72, v73\n"
        "v_cvt_pk_bf16_f32 v193, v74, v75\n"
        "v_accvgpr_write_b32 a254, v235\n"
        "v_mfma_f32_16x16x32_bf16 v[76:79], a[104:107], a[220:223], v[76:79]\n"
        "v_accvgpr_write_b32 a255, v235\n"
        "v_cvt_pk_bf16_f32 v96, v96, v97\n"
        "v_fma_f32 v99, v99, s24, v238\n"
        "v_mfma_f32_16x16x32_bf16 v[76:79], a[108:111], a[224:227], v[76:79]\n"
        "v_exp_f32 v99, v99\n"
        "v_fma_f32 v100, v100, s24, v238\n"
        "v_mfma_f32_16x16x32_bf16 v[76:79], a[112:115], a[228:231], v[76:79]\n"
        "buffer_load_dwordx4 v142, s[52:55], s64 offen offset:2048 lds\n"
        "v_exp_f32 v100, v100\n"
        "v_mfma_f32_16x16x32_bf16 v[76:79], a[116:119], a[232:235], v[76:79]\n"
        "v_cvt_pk_bf16_f32 v97, v98, v99\n"
        "v_fma_f32 v101, v101, s24, v238\n"
        "v_fma_f32 v102, v102, s24, v238\n"
        "v_mfma_f32_16x16x32_bf16 v[162:165], a[120:123], a[248:251], 0\n"
        "ds_read_b128 a[236:239], v150 offset:0\n"
        "v_exp_f32 v101, v101\n"
        "v_mfma_f32_16x16x32_bf16 v[162:165], a[124:127], a[216:219], v[162:165]\n"
        "s_add_u32 m0, s65, 3072\n"
        "ds_read_b128 a[240:243], v151 offset:0\n"
        "v_exp_f32 v102, v102\n"
        "v_mfma_f32_16x16x32_bf16 v[162:165], a[128:131], a[220:223], v[162:165]\n"
        "s_add_u32 s64, s65, 3072\n"
        "s_waitcnt vmcnt(3)\n"
        "s_barrier\n"
        "ds_read_b64_tr_b16 v[212:213], v232 offset:36864\n"
        "ds_read_b128 a[244:247], v150 offset:128\n"
        "v_mfma_f32_16x16x32_bf16 v[162:165], a[132:135], a[224:227], v[162:165]\n"
        "buffer_load_dwordx4 v142, s[52:55], s64 offen lds\n"
        "v_cvt_pk_bf16_f32 v194, v76, v77\n"
        "v_cvt_pk_bf16_f32 v195, v78, v79\n"
        "v_mfma_f32_16x16x32_bf16 v[162:165], a[136:139], a[228:231], v[162:165]\n"
        "ds_read_b64_tr_b16 v[214:215], v232 offset:43008\n"
        "v_cvt_pk_bf16_f32 v98, v100, v101\n"
        "v_fma_f32 v103, v103, s24, v238\n"
        "v_mfma_f32_16x16x32_bf16 v[162:165], a[140:143], a[232:235], v[162:165]\n"
        "v_exp_f32 v103, v103\n"
        "v_fma_f32 v104, v104, s24, v238\n"
        "v_mfma_f32_16x16x32_bf16 v[72:75], a[0:3], a[248:251], 0\n"
        "ds_read_b64_tr_b16 v[216:217], v232 offset:55296\n"
        "v_exp_f32 v104, v104\n"
        "v_mfma_f32_16x16x32_bf16 v[72:75], a[4:7], a[216:219], v[72:75]\n"
        "v_cvt_pk_bf16_f32 v99, v102, v103\n"
        "v_fma_f32 v105, v105, s24, v238\n"
        "v_cvt_pk_bf16_f32 v208, v162, v163\n"
        "v_mfma_f32_16x16x32_bf16 v[72:75], a[8:11], a[220:223], v[72:75]\n"
        "buffer_load_dwordx4 v142, s[52:55], s64 offen offset:1024 lds\n"
        "ds_read_b64_tr_b16 v[218:219], v232 offset:61440\n"
        "v_cvt_pk_bf16_f32 v209, v164, v165\n"
        "v_mfma_f32_16x16x32_bf16 v[72:75], a[12:15], a[224:227], v[72:75]\n"
        "v_exp_f32 v105, v105\n"
        "v_fma_f32 v106, v106, s24, v238\n"
        "v_mfma_f32_16x16x32_bf16 v[72:75], a[16:19], a[228:231], v[72:75]\n"
        "ds_read_b64_tr_b16 v[220:221], v233 offset:49152\n"
        "v_exp_f32 v106, v106\n"
        "v_mfma_f32_16x16x32_bf16 v[72:75], a[20:23], a[232:235], v[72:75]\n"
        "v_cvt_pk_bf16_f32 v100, v104, v105\n"
        "v_fma_f32 v107, v107, s24, v238\n"
        "v_fma_f32 v108, v108, s24, v238\n"
        "v_mfma_f32_16x16x32_bf16 v[76:79], a[24:27], a[248:251], 0\n"
        "ds_read_b64_tr_b16 v[222:223], v233 offset:50688\n"
        "v_exp_f32 v107, v107\n"
        "v_mfma_f32_16x16x32_bf16 v[76:79], a[28:31], a[216:219], v[76:79]\n"
        "buffer_load_dwordx4 v142, s[52:55], s64 offen offset:2048 lds\n"
        "v_exp_f32 v108, v108\n"
        "v_mfma_f32_16x16x32_bf16 v[76:79], a[32:35], a[220:223], v[76:79]\n"
        "ds_read_b64_tr_b16 v[224:225], v232 offset:36896\n"
        "v_cvt_pk_bf16_f32 v196, v72, v73\n"
        "v_cvt_pk_bf16_f32 v197, v74, v75\n"
        "v_mfma_f32_16x16x32_bf16 v[76:79], a[36:39], a[224:227], v[76:79]\n"
        "v_cvt_pk_bf16_f32 v101, v106, v107\n"
        "v_fma_f32 v109, v109, s24, v238\n"
        "v_fma_f32 v110, v110, s24, v238\n"
        "v_mfma_f32_16x16x32_bf16 v[76:79], a[40:43], a[228:231], v[76:79]\n"
        "s_add_u32 m0, s65, 6144\n"
        "ds_read_b64_tr_b16 v[226:227], v232 offset:43040\n"
        "v_exp_f32 v109, v109\n"
        "v_mfma_f32_16x16x32_bf16 v[76:79], a[44:47], a[232:235], v[76:79]\n"
        "s_add_u32 s64, s65, 6144\n"
        "v_exp_f32 v110, v110\n"
        "v_cvt_pk_bf16_f32 v102, v108, v109\n"
        "v_mfma_f32_16x16x32_bf16 v[162:165], a[48:51], a[248:251], 0\n"
        "ds_read_b128 a[248:251], v151 offset:128\n"
        "buffer_load_dwordx4 v142, s[52:55], s64 offen lds\n"
        "v_fma_f32 v111, v111, s24, v238\n"
        "v_mfma_f32_16x16x32_bf16 v[162:165], a[52:55], a[216:219], v[162:165]\n"
        "ds_read_b128 a[216:219], v150 offset:256\n"
        "ds_read_b64_tr_b16 v[182:183], v232 offset:55328\n"
        "v_cvt_pk_bf16_f32 v198, v76, v77\n"
        "v_mfma_f32_16x16x32_bf16 v[162:165], a[56:59], a[220:223], v[162:165]\n"
        "ds_read_b128 a[220:223], v151 offset:256\n"
        "ds_read_b64_tr_b16 v[184:185], v232 offset:61472\n"
        "s_sub_u32 s31, s30, s28\n"
        "v_cvt_pk_bf16_f32 v199, v78, v79\n"
        "v_mfma_f32_16x16x32_bf16 v[162:165], a[60:63], a[224:227], v[162:165]\n"
        "s_sub_u32 s45, s28, s29\n"
        "v_exp_f32 v111, v111\n"
        "v_fma_f32 v112, v112, s24, v239\n"
        "v_mfma_f32_16x16x32_bf16 v[162:165], a[64:67], a[228:231], v[162:165]\n"
        "ds_read_b64_tr_b16 v[202:203], v233 offset:49184\n"
        "ds_read_b128 a[228:231], v150 offset:1536\n"
        "s_add_u32 s28, s28, s31\n"
        "v_cvt_pk_bf16_f32 v103, v110, v111\n"
        "v_mfma_f32_16x16x32_bf16 v[162:165], a[68:71], a[232:235], v[162:165]\n"
        "s_waitcnt lgkmcnt(15)\n"
        "buffer_load_dwordx4 v142, s[52:55], s64 offen offset:1024 lds\n"
        "ds_read_b128 a[232:235], v151 offset:1536\n"
        "v_fma_f32 v113, v113, s24, v239\n"
        "v_mfma_f32_16x16x32_bf16 v[72:75], a[236:239], a[144:147], 0\n"
        "s_waitcnt lgkmcnt(15)\n"
        "ds_read_b64_tr_b16 v[204:205], v233 offset:50720\n"
        "v_exp_f32 v112, v112\n"
        "v_mfma_f32_16x16x32_bf16 v[72:75], a[240:243], a[148:151], v[72:75]\n"
        "s_waitcnt lgkmcnt(15)\n"
        "v_exp_f32 v113, v113\n"
        "v_cvt_pk_bf16_f32 v200, v162, v163\n"
        "v_mfma_f32_16x16x32_bf16 v[72:75], a[244:247], a[152:155], v[72:75]\n"
        "s_waitcnt lgkmcnt(8)\n"
        "ds_read_b64_tr_b16 v[88:89], v232 offset:36928\n"
        "v_cvt_pk_bf16_f32 v201, v164, v165\n"
        "v_cvt_pk_bf16_f32 v206, v162, v163\n"
        "v_mfma_f32_16x16x32_bf16 v[72:75], a[248:251], a[156:159], v[72:75]\n"
        "s_waitcnt lgkmcnt(8)\n"
        "v_cvt_pk_bf16_f32 v207, v164, v165\n"
        "v_cvt_pk_bf16_f32 v112, v112, v113\n"
        "v_fma_f32 v114, v114, s24, v239\n"
        "v_mfma_f32_16x16x32_bf16 v[72:75], a[216:219], a[160:163], v[72:75]\n"
        "s_waitcnt lgkmcnt(6)\n"
        "buffer_load_dwordx4 v142, s[52:55], s64 offen offset:2048 lds\n"
        "s_add_u32 s41, s30, s40\n"
        "s_add_u32 s35, s47, 786432\n"
        "s_sub_u32 s46, s35, 100663296\n"
        "s_cmp_lt_u32 s46, 786432\n"
        "v_mfma_f32_16x16x32_bf16 v[72:75], a[220:223], a[164:167], v[72:75]\n"
        "s_cselect_b32 s35, s46, s35\n"
        "ds_read_b64_tr_b16 v[90:91], v232 offset:43072\n"
        "v_exp_f32 v114, v114\n"
        "v_mfma_f32_16x16x32_bf16 v[76:79], a[236:239], a[168:171], 0\n"
        "ds_read_b64_tr_b16 v[92:93], v232 offset:55360\n"
        "v_fma_f32 v115, v115, s24, v239\n"
        "v_fma_f32 v116, v116, s24, v239\n"
        "v_mfma_f32_16x16x32_bf16 v[76:79], a[240:243], a[172:175], v[76:79]\n"
        "v_exp_f32 v115, v115\n"
        "v_cvt_pk_bf16_f32 v128, v72, v73\n"
        "v_mfma_f32_16x16x32_bf16 v[76:79], a[244:247], a[176:179], v[76:79]\n"
        "ds_read_b64_tr_b16 v[94:95], v232 offset:61504\n"
        "v_exp_f32 v116, v116\n"
        "v_mfma_f32_16x16x32_bf16 v[76:79], a[248:251], a[180:183], v[76:79]\n"
        "s_mov_b32 m0, s48\n"
        "v_cvt_pk_bf16_f32 v129, v74, v75\n"
        "v_cvt_pk_bf16_f32 v113, v114, v115\n"
        "v_fma_f32 v117, v117, s24, v239\n"
        "v_mfma_f32_16x16x32_bf16 v[76:79], a[216:219], a[184:187], v[76:79]\n"
        "buffer_load_dwordx4 v249, s[12:15], s36 offen offset:0 lds\n"
        "ds_read_b64_tr_b16 v[104:105], v233 offset:49216\n"
        "v_mfma_f32_16x16x32_bf16 v[76:79], a[220:223], a[188:191], v[76:79]\n"
        "v_exp_f32 v117, v117\n"
        "v_fma_f32 v118, v118, s24, v239\n"
        "v_mfma_f32_16x16x32_bf16 v[162:165], a[236:239], a[192:195], 0\n"
        "ds_read_b128 a[236:239], v150 offset:1664\n"
        "ds_read_b64_tr_b16 v[106:107], v233 offset:50752\n"
        "v_cvt_pk_bf16_f32 v114, v116, v117\n"
        "v_mfma_f32_16x16x32_bf16 v[162:165], a[240:243], a[196:199], v[162:165]\n"
        "ds_read_b128 a[240:243], v151 offset:1664\n"
        "ds_read_b128 a[224:227], v151 offset:12288\n"
        "v_xor_b32 v253, 32, v249\n"
        "v_mfma_f32_16x16x32_bf16 v[162:165], a[244:247], a[200:203], v[162:165]\n"
        "ds_read_b128 a[244:247], v150 offset:1792\n"
        "v_exp_f32 v118, v118\n"
        "v_mfma_f32_16x16x32_bf16 v[162:165], a[248:251], a[204:207], v[162:165]\n"
        "ds_read_b128 a[248:251], v151 offset:1792\n"
        "buffer_load_dwordx4 v253, s[12:15], s36 offen offset:1024 lds\n"
        "s_mov_b32 s30, s29\n"
        "v_mfma_f32_16x16x32_bf16 v[162:165], a[216:219], a[208:211], v[162:165]\n"
        "s_waitcnt lgkmcnt(4)\n"
        "s_barrier\n"
        "ds_write_b128 v234, v[212:215] offset:36864\n"
        "v_mfma_f32_16x16x32_bf16 v[162:165], a[220:223], a[212:215], v[162:165]\n"
        "s_add_u32 s29, s29, s45\n"
        "ds_read_b128 a[220:223], v150 offset:12288\n"
        "v_cvt_pk_bf16_f32 v136, v76, v77\n"
        "v_mfma_f32_16x16x32_bf16 v[72:75], a[228:231], a[144:147], 0\n"
        "v_cvt_pk_bf16_f32 v137, v78, v79\n"
        "v_fma_f32 v119, v119, s24, v239\n"
        "v_mfma_f32_16x16x32_bf16 v[72:75], a[232:235], a[148:151], v[72:75]\n"
        "s_add_u32 m0, s41, 0\n"
        "v_exp_f32 v119, v119\n"
        "v_mfma_f32_16x16x32_bf16 v[72:75], a[236:239], a[152:155], v[72:75]\n"
        "s_waitcnt lgkmcnt(5)\n"
        "buffer_load_dwordx4 v242, s[8:11], s47 offen lds\n"
        "v_mfma_f32_16x16x32_bf16 v[72:75], a[240:243], a[156:159], v[72:75]\n"
        "s_waitcnt lgkmcnt(3)\n"
        "ds_write_b128 v234, v[216:219] offset:43008\n"
        "v_mfma_f32_16x16x32_bf16 v[72:75], a[244:247], a[160:163], v[72:75]\n"
        "s_waitcnt lgkmcnt(3)\n"
        "v_cvt_pk_bf16_f32 v144, v162, v163\n"
        "v_cvt_pk_bf16_f32 v145, v164, v165\n"
        "v_mfma_f32_16x16x32_bf16 v[72:75], a[248:251], a[164:167], v[72:75]\n"
        "v_cvt_pk_bf16_f32 v115, v118, v119\n"
        "v_fma_f32 v120, v120, s24, v239\n"
        "v_mfma_f32_16x16x32_bf16 v[76:79], a[228:231], a[168:171], 0\n"
        "ds_write_b128 v234, v[220:223] offset:49152\n"
        "v_mfma_f32_16x16x32_bf16 v[76:79], a[232:235], a[172:175], v[76:79]\n"
        "v_exp_f32 v120, v120\n"
        "v_mfma_f32_16x16x32_bf16 v[76:79], a[236:239], a[176:179], v[76:79]\n"
        "v_fma_f32 v121, v121, s24, v239\n"
        "v_cvt_pk_bf16_f32 v130, v72, v73\n"
        "v_mfma_f32_16x16x32_bf16 v[76:79], a[240:243], a[180:183], v[76:79]\n"
        "ds_write_b128 v234, v[224:227] offset:37120\n"
        "v_mfma_f32_16x16x32_bf16 v[76:79], a[244:247], a[184:187], v[76:79]\n"
        "v_cvt_pk_bf16_f32 v131, v74, v75\n"
        "v_fma_f32 v122, v122, s24, v239\n"
        "v_mfma_f32_16x16x32_bf16 v[76:79], a[248:251], a[188:191], v[76:79]\n"
        "v_exp_f32 v121, v121\n"
        "v_mfma_f32_16x16x32_bf16 v[162:165], a[228:231], a[192:195], 0\n"
        "ds_read_b128 a[228:231], v150 offset:12416\n"
        "v_cvt_pk_bf16_f32 v116, v120, v121\n"
        "v_mfma_f32_16x16x32_bf16 v[162:165], a[232:235], a[196:199], v[162:165]\n"
        "ds_read_b128 a[232:235], v151 offset:12416\n"
        "ds_read_b128 a[216:219], v151 offset:13824\n"
        "v_mfma_f32_16x16x32_bf16 v[162:165], a[236:239], a[200:203], v[162:165]\n"
        "ds_write_b128 v234, v[182:185] offset:43264\n"
        "v_mfma_f32_16x16x32_bf16 v[162:165], a[240:243], a[204:207], v[162:165]\n"
        "ds_read_b128 a[236:239], v150 offset:12544\n"
        "ds_read_b128 a[240:243], v151 offset:12544\n"
        "v_mfma_f32_16x16x32_bf16 v[162:165], a[244:247], a[208:211], v[162:165]\n"
        "v_cvt_pk_bf16_f32 v138, v76, v77\n"
        "v_cvt_pk_bf16_f32 v139, v78, v79\n"
        "v_mfma_f32_16x16x32_bf16 v[162:165], a[248:251], a[212:215], v[162:165]\n"
        "s_waitcnt lgkmcnt(9)\n"
        "ds_write_b128 v234, v[202:205] offset:49408\n"
        "v_mfma_f32_16x16x32_bf16 v[72:75], a[220:223], a[144:147], 0\n"
        "ds_read_b128 a[248:251], v150 offset:13824\n"
        "v_fma_f32 v123, v123, s24, v239\n"
        "v_mfma_f32_16x16x32_bf16 v[72:75], a[224:227], a[148:151], v[72:75]\n"
        "s_waitcnt lgkmcnt(7)\n"
        "v_exp_f32 v122, v122\n"
        "v_mfma_f32_16x16x32_bf16 v[72:75], a[228:231], a[152:155], v[72:75]\n"
        "s_waitcnt lgkmcnt(6)\n"
        "ds_write_b128 v234, v[88:91] offset:37376\n"
        "v_mfma_f32_16x16x32_bf16 v[72:75], a[232:235], a[156:159], v[72:75]\n"
        "s_waitcnt lgkmcnt(4)\n"
        "v_exp_f32 v123, v123\n"
        "v_mfma_f32_16x16x32_bf16 v[72:75], a[236:239], a[160:163], v[72:75]\n"
        "s_waitcnt lgkmcnt(3)\n"
        "v_cvt_pk_bf16_f32 v146, v162, v163\n"
        "v_cvt_pk_bf16_f32 v147, v164, v165\n"
        "v_mfma_f32_16x16x32_bf16 v[72:75], a[240:243], a[164:167], v[72:75]\n"
        "ds_write_b128 v234, v[92:95] offset:43520\n"
        "v_mfma_f32_16x16x32_bf16 v[76:79], a[220:223], a[168:171], 0\n"
        "v_cvt_pk_bf16_f32 v117, v122, v123\n"
        "v_fma_f32 v124, v124, s24, v239\n"
        "v_mfma_f32_16x16x32_bf16 v[76:79], a[224:227], a[172:175], v[76:79]\n"
        "v_fma_f32 v125, v125, s24, v239\n"
        "v_fma_f32 v126, v126, s24, v239\n"
        "v_mfma_f32_16x16x32_bf16 v[76:79], a[228:231], a[176:179], v[76:79]\n"
        "ds_write_b128 v234, v[104:107] offset:49664\n"
        "s_waitcnt vmcnt(3)\n"
        "s_barrier\n"
        "v_mfma_f32_16x16x32_bf16 v[76:79], a[232:235], a[180:183], v[76:79]\n"
        "ds_read_b64_tr_b16 v[108:109], v232 offset:0\n"
        "v_fma_f32 v127, v127, s24, v239\n"
        "v_mfma_f32_16x16x32_bf16 v[76:79], a[236:239], a[184:187], v[76:79]\n"
        "ds_read_b64_tr_b16 v[110:111], v232 offset:6144\n"
        "v_cvt_pk_bf16_f32 v132, v72, v73\n"
        "v_mfma_f32_16x16x32_bf16 v[76:79], a[240:243], a[188:191], v[76:79]\n"
        "v_exp_f32 v124, v124\n"
        "v_mfma_f32_16x16x32_bf16 v[162:165], a[220:223], a[192:195], 0\n"
        "ds_read_b128 a[220:223], v150 offset:13952\n"
        "ds_read_b64_tr_b16 v[120:121], v232 offset:18432\n"
        "v_mfma_f32_16x16x32_bf16 v[162:165], a[224:227], a[196:199], v[162:165]\n"
        "ds_read_b128 a[224:227], v151 offset:13952\n"
        "v_cvt_pk_bf16_f32 v133, v74, v75\n"
        "v_mfma_f32_16x16x32_bf16 v[162:165], a[228:231], a[200:203], v[162:165]\n"
        "ds_read_b128 a[228:231], v150 offset:14080\n"
        "ds_read_b64_tr_b16 v[122:123], v232 offset:24576\n"
        "v_mfma_f32_16x16x32_bf16 v[162:165], a[232:235], a[204:207], v[162:165]\n"
        "ds_read_b128 a[232:235], v151 offset:14080\n"
        "v_cvt_pk_bf16_f32 v140, v76, v77\n"
        "v_mfma_f32_16x16x32_bf16 v[162:165], a[236:239], a[208:211], v[162:165]\n"
        "ds_read_b128 a[236:239], v240 offset:13824\n"
        "ds_read_b128 a[244:247], v240 offset:13952\n"
        "v_mfma_f32_16x16x32_bf16 v[162:165], a[240:243], a[212:215], v[162:165]\n"
        "s_waitcnt lgkmcnt(13)\n"
        "ds_read_b128 a[240:243], v241 offset:13824\n"
        "v_cvt_pk_bf16_f32 v141, v78, v79\n"
        "v_mfma_f32_16x16x32_bf16 v[72:75], a[248:251], a[144:147], 0\n"
        "v_exp_f32 v125, v125\n"
        "v_mfma_f32_16x16x32_bf16 v[72:75], a[216:219], a[148:151], v[72:75]\n"
        "s_waitcnt lgkmcnt(8)\n"
        "v_cvt_pk_bf16_f32 v118, v124, v125\n"
        "v_mfma_f32_16x16x32_bf16 v[72:75], a[220:223], a[152:155], v[72:75]\n"
        "s_waitcnt lgkmcnt(6)\n"
        "ds_read_b64_tr_b16 v[124:125], v233 offset:12288\n"
        "ds_read_b64_tr_b16 v[222:223], v232 offset:32\n"
        "v_mfma_f32_16x16x32_bf16 v[72:75], a[224:227], a[156:159], v[72:75]\n"
        "s_waitcnt lgkmcnt(7)\n"
        "v_exp_f32 v126, v126\n"
        "v_mfma_f32_16x16x32_bf16 v[72:75], a[228:231], a[160:163], v[72:75]\n"
        "s_waitcnt lgkmcnt(5)\n"
        "ds_read_b64_tr_b16 v[224:225], v232 offset:6176\n"
        "v_cvt_pk_bf16_f32 v148, v162, v163\n"
        "v_mfma_f32_16x16x32_bf16 v[72:75], a[232:235], a[164:167], v[72:75]\n"
        "v_exp_f32 v127, v127\n"
        "v_mfma_f32_16x16x32_bf16 v[76:79], a[248:251], a[168:171], 0\n"
        "ds_read_b64_tr_b16 v[182:183], v232 offset:18464\n"
        "v_cvt_pk_bf16_f32 v119, v126, v127\n"
        "v_mfma_f32_16x16x32_bf16 v[76:79], a[216:219], a[172:175], v[76:79]\n"
        "ds_read_b64_tr_b16 v[126:127], v233 offset:13824\n"
        "v_cvt_pk_bf16_f32 v149, v164, v165\n"
        "v_mfma_f32_16x16x32_bf16 v[76:79], a[220:223], a[176:179], v[76:79]\n"
        "ds_read_b64_tr_b16 v[184:185], v232 offset:24608\n"
        "v_cvt_pk_bf16_f32 v134, v72, v73\n"
        "v_mfma_f32_16x16x32_bf16 v[76:79], a[224:227], a[180:183], v[76:79]\n"
        "v_cvt_pk_bf16_f32 v135, v74, v75\n"
        "v_mfma_f32_16x16x32_bf16 v[76:79], a[228:231], a[184:187], v[76:79]\n"
        "ds_read_b64_tr_b16 v[202:203], v233 offset:12320\n"
        "v_mfma_f32_16x16x32_bf16 v[76:79], a[232:235], a[188:191], v[76:79]\n"
        "v_mfma_f32_16x16x32_bf16 v[162:165], a[248:251], a[192:195], 0\n"
        "ds_read_b128 a[248:251], v241 offset:13952\n"
        "ds_read_b64_tr_b16 v[204:205], v233 offset:13856\n"
        "v_mfma_f32_16x16x32_bf16 v[162:165], a[216:219], a[196:199], v[162:165]\n"
        "ds_read_b128 a[216:219], v240 offset:14080\n"
        "v_mfma_f32_16x16x32_bf16 v[162:165], a[220:223], a[200:203], v[162:165]\n"
        "ds_read_b128 a[220:223], v241 offset:14080\n"
        "ds_read_b64_tr_b16 v[88:89], v232 offset:64\n"
        "v_mfma_f32_16x16x32_bf16 v[162:165], a[224:227], a[204:207], v[162:165]\n"
        "v_cvt_pk_bf16_f32 v142, v76, v77\n"
        "v_cvt_pk_bf16_f32 v143, v78, v79\n"
        "v_mfma_f32_16x16x32_bf16 v[162:165], a[228:231], a[208:211], v[162:165]\n"
        "ds_read_b64_tr_b16 v[90:91], v232 offset:6208\n"
        "v_mfma_f32_16x16x32_bf16 v[162:165], a[232:235], a[212:215], v[162:165]\n"
        "s_waitcnt lgkmcnt(15)\n"
        "v_mfma_f32_16x16x32_bf16 v[72:75], a[72:75], a[236:239], 0\n"
        "s_waitcnt lgkmcnt(13)\n"
        "ds_read_b64_tr_b16 v[92:93], v232 offset:18496\n"
        "v_mfma_f32_16x16x32_bf16 v[72:75], a[76:79], a[240:243], v[72:75]\n"
        "v_mfma_f32_16x16x32_bf16 v[72:75], a[80:83], a[244:247], v[72:75]\n"
        "s_waitcnt lgkmcnt(6)\n"
        "ds_read_b64_tr_b16 v[94:95], v232 offset:24640\n"
        "v_mfma_f32_16x16x32_bf16 v[72:75], a[84:87], a[248:251], v[72:75]\n"
        "s_waitcnt lgkmcnt(5)\n"
        "v_mfma_f32_16x16x32_bf16 v[72:75], a[88:91], a[216:219], v[72:75]\n"
        "s_waitcnt lgkmcnt(4)\n"
        "ds_read_b64_tr_b16 v[104:105], v233 offset:12352\n"
        "v_cvt_pk_bf16_f32 v150, v162, v163\n"
        "v_mfma_f32_16x16x32_bf16 v[72:75], a[92:95], a[220:223], v[72:75]\n"
        "v_cvt_pk_bf16_f32 v151, v164, v165\n"
        "v_mfma_f32_16x16x32_bf16 v[76:79], a[96:99], a[236:239], 0\n"
        "s_add_u32 m0, s41, 1024\n"
        "ds_read_b64_tr_b16 v[106:107], v233 offset:13888\n"
        "v_mfma_f32_16x16x32_bf16 v[76:79], a[100:103], a[240:243], v[76:79]\n"
        "v_mfma_f32_16x16x32_bf16 v[76:79], a[104:107], a[244:247], v[76:79]\n"
        "buffer_load_dwordx4 v243, s[8:11], s47 offen lds\n"
        "s_waitcnt lgkmcnt(0)\n"
        "s_barrier\n"
        "v_mfma_f32_16x16x32_bf16 v[76:79], a[108:111], a[248:251], v[76:79]\n"
        "ds_write_b128 v234, v[108:111] offset:0\n"
        "v_mfma_f32_16x16x32_bf16 v[76:79], a[112:115], a[216:219], v[76:79]\n"
        "v_cvt_pk_bf16_f32 v212, v72, v73\n"
        "v_cvt_pk_bf16_f32 v213, v74, v75\n"
        "v_mfma_f32_16x16x32_bf16 v[76:79], a[116:119], a[220:223], v[76:79]\n"
        "v_mfma_f32_16x16x32_bf16 v[162:165], a[120:123], a[236:239], 0\n"
        "ds_write_b128 v234, v[120:123] offset:6144\n"
        "v_mfma_f32_16x16x32_bf16 v[162:165], a[124:127], a[240:243], v[162:165]\n"
        "v_mfma_f32_16x16x32_bf16 v[162:165], a[128:131], a[244:247], v[162:165]\n"
        "v_mfma_f32_16x16x32_bf16 v[162:165], a[132:135], a[248:251], v[162:165]\n"
        "ds_write_b128 v234, v[124:127] offset:12288\n"
        "v_mfma_f32_16x16x32_bf16 v[162:165], a[136:139], a[216:219], v[162:165]\n"
        "v_mfma_f32_16x16x32_bf16 v[162:165], a[140:143], a[220:223], v[162:165]\n"
        "v_cvt_pk_bf16_f32 v214, v76, v77\n"
        "v_cvt_pk_bf16_f32 v215, v78, v79\n"
        "v_mfma_f32_16x16x32_bf16 v[72:75], a[0:3], a[236:239], 0\n"
        "ds_write_b128 v234, v[222:225] offset:256\n"
        "v_mfma_f32_16x16x32_bf16 v[72:75], a[4:7], a[240:243], v[72:75]\n"
        "v_mfma_f32_16x16x32_bf16 v[72:75], a[8:11], a[244:247], v[72:75]\n"
        "s_add_u32 m0, s41, 2048\n"
        "v_mfma_f32_16x16x32_bf16 v[72:75], a[12:15], a[248:251], v[72:75]\n"
        "ds_write_b128 v234, v[182:185] offset:6400\n"
        "v_mfma_f32_16x16x32_bf16 v[72:75], a[16:19], a[216:219], v[72:75]\n"
        "buffer_load_dwordx4 v244, s[8:11], s47 offen lds\n"
        "v_mfma_f32_16x16x32_bf16 v[72:75], a[20:23], a[220:223], v[72:75]\n"
        "v_cvt_pk_bf16_f32 v230, v162, v163\n"
        "v_cvt_pk_bf16_f32 v231, v164, v165\n"
        "v_mfma_f32_16x16x32_bf16 v[76:79], a[24:27], a[236:239], 0\n"
        "ds_write_b128 v234, v[202:205] offset:12544\n"
        "v_mfma_f32_16x16x32_bf16 v[76:79], a[28:31], a[240:243], v[76:79]\n"
        "v_mfma_f32_16x16x32_bf16 v[76:79], a[32:35], a[244:247], v[76:79]\n"
        "v_mfma_f32_16x16x32_bf16 v[76:79], a[36:39], a[248:251], v[76:79]\n"
        "ds_write_b128 v234, v[88:91] offset:512\n"
        "v_mfma_f32_16x16x32_bf16 v[76:79], a[40:43], a[216:219], v[76:79]\n"
        "v_cvt_pk_bf16_f32 v216, v72, v73\n"
        "v_cvt_pk_bf16_f32 v217, v74, v75\n"
        "v_mfma_f32_16x16x32_bf16 v[76:79], a[44:47], a[220:223], v[76:79]\n"
        "v_mfma_f32_16x16x32_bf16 v[162:165], a[48:51], a[236:239], 0\n"
        "ds_write_b128 v234, v[92:95] offset:6656\n"
        "v_mfma_f32_16x16x32_bf16 v[162:165], a[52:55], a[240:243], v[162:165]\n"
        "v_mfma_f32_16x16x32_bf16 v[162:165], a[56:59], a[244:247], v[162:165]\n"
        "v_mfma_f32_16x16x32_bf16 v[162:165], a[60:63], a[248:251], v[162:165]\n"
        "ds_write_b128 v234, v[104:107] offset:12800\n"
        "v_mfma_f32_16x16x32_bf16 v[162:165], a[64:67], a[216:219], v[162:165]\n"
        "s_add_u32 m0, s41, 12288\n"
        "v_cvt_pk_bf16_f32 v218, v76, v77\n"
        "v_cvt_pk_bf16_f32 v219, v78, v79\n"
        "v_mfma_f32_16x16x32_bf16 v[162:165], a[68:71], a[220:223], v[162:165]\n"
        "v_mfma_f32_16x16x32_bf16 v[88:91], a[252:255], v[64:67], 0\n"
        "buffer_load_dwordx4 v242, s[8:11], s35 offen lds\n"
        "v_mfma_f32_16x16x32_bf16 v[88:91], a[252:255], v[68:71], v[88:91]\n"
        "v_mfma_f32_16x16x32_bf16 v[92:95], v[132:135], v[68:71], 0\n"
        "v_mfma_f32_16x16x32_bf16 v[92:95], v[128:131], v[64:67], v[92:95]\n"
        "v_mfma_f32_16x16x32_bf16 v[182:185], v[136:139], v[64:67], 0\n"
        "v_mfma_f32_16x16x32_bf16 v[182:185], v[140:143], v[68:71], v[182:185]\n"
        "v_mfma_f32_16x16x32_bf16 v[202:205], v[148:151], v[68:71], 0\n"
        "s_cmp_eq_u32 s37, 100663296\n"
        "v_cvt_pk_bf16_f32 v220, v162, v163\n"
        "v_cvt_pk_bf16_f32 v221, v164, v165\n"
        "v_mfma_f32_16x16x32_bf16 v[202:205], v[144:147], v[64:67], v[202:205]\n"
        "s_cselect_b32 s46, 0, s37\n"
        "v_cvt_pk_bf16_f32 v226, v162, v163\n"
        "v_cvt_pk_bf16_f32 v227, v164, v165\n"
        "v_mfma_f32_16x16x32_bf16 v[72:75], a[252:255], v[80:83], 0\n"
        "s_add_u32 s38, s46, s43\n"
        "v_rcp_f32 v88, v88\n"
        "v_mfma_f32_16x16x32_bf16 v[72:75], a[252:255], v[84:87], v[72:75]\n"
        "s_add_u32 s39, s46, s44\n"
        "v_mul_f32 v92, v88, v92\n"
        "v_mul_f32 v93, v88, v93\n"
        "v_mfma_f32_16x16x32_bf16 v[76:79], v[132:135], v[84:87], 0\n"
        "s_add_u32 s37, s37, 6291456\n"
        "v_mul_f32 v94, v88, v94\n"
        "v_mul_f32 v95, v88, v95\n"
        "v_mfma_f32_16x16x32_bf16 v[76:79], v[128:131], v[80:83], v[76:79]\n"
        "v_rcp_f32 v72, v72\n"
        "v_mfma_f32_16x16x32_bf16 v[104:107], v[136:139], v[80:83], 0\n"
        "s_add_u32 m0, s41, 13312\n"
        "v_cvt_pk_bf16_f32 v92, v92, v93\n"
        "v_cvt_pk_bf16_f32 v93, v94, v95\n"
        "v_mfma_f32_16x16x32_bf16 v[104:107], v[140:143], v[84:87], v[104:107]\n"
        "v_mul_f32 v182, v88, v182\n"
        "v_mul_f32 v183, v88, v183\n"
        "v_mfma_f32_16x16x32_bf16 v[108:111], v[148:151], v[84:87], 0\n"
        "buffer_load_dwordx4 v243, s[8:11], s35 offen lds\n"
        "v_mfma_f32_16x16x32_bf16 v[108:111], v[144:147], v[80:83], v[108:111]\n"
        "v_mul_f32 v184, v88, v184\n"
        "v_mul_f32 v185, v88, v185\n"
        "v_mfma_f32_16x16x32_bf16 v[64:67], a[252:255], v[96:99], 0\n"
        "v_cvt_pk_bf16_f32 v94, v182, v183\n"
        "v_cvt_pk_bf16_f32 v95, v184, v185\n"
        "v_mfma_f32_16x16x32_bf16 v[64:67], a[252:255], v[100:103], v[64:67]\n"
        "ds_write_b128 v245, v[92:95] offset:0\n"
        "v_mfma_f32_16x16x32_bf16 v[68:71], v[132:135], v[100:103], 0\n"
        "v_mul_f32 v202, v88, v202\n"
        "v_mul_f32 v203, v88, v203\n"
        "v_mfma_f32_16x16x32_bf16 v[68:71], v[128:131], v[96:99], v[68:71]\n"
        "v_mul_f32 v204, v88, v204\n"
        "v_mul_f32 v205, v88, v205\n"
        "v_mfma_f32_16x16x32_bf16 v[120:123], v[136:139], v[96:99], 0\n"
        "v_cvt_pk_bf16_f32 v202, v202, v203\n"
        "v_cvt_pk_bf16_f32 v203, v204, v205\n"
        "v_mfma_f32_16x16x32_bf16 v[120:123], v[140:143], v[100:103], v[120:123]\n"
        "v_mul_f32 v76, v72, v76\n"
        "v_mul_f32 v77, v72, v77\n"
        "v_mfma_f32_16x16x32_bf16 v[124:127], v[148:151], v[100:103], 0\n"
        "ds_write_b64 v246, v[202:203] offset:0\n"
        "v_mfma_f32_16x16x32_bf16 v[124:127], v[144:147], v[96:99], v[124:127]\n"
        "v_rcp_f32 v64, v64\n"
        "v_mfma_f32_16x16x32_bf16 v[80:83], a[252:255], v[112:115], 0\n"
        "v_mul_f32 v78, v72, v78\n"
        "v_mul_f32 v79, v72, v79\n"
        "v_mfma_f32_16x16x32_bf16 v[80:83], a[252:255], v[116:119], v[80:83]\n"
        "v_cvt_pk_bf16_f32 v76, v76, v77\n"
        "v_cvt_pk_bf16_f32 v77, v78, v79\n"
        "v_mfma_f32_16x16x32_bf16 v[84:87], v[132:135], v[116:119], 0\n"
        "s_add_u32 m0, s41, 14336\n"
        "v_mul_f32 v104, v72, v104\n"
        "v_mul_f32 v105, v72, v105\n"
        "v_mfma_f32_16x16x32_bf16 v[84:87], v[128:131], v[112:115], v[84:87]\n"
        "v_mul_f32 v106, v72, v106\n"
        "v_mul_f32 v107, v72, v107\n"
        "v_mfma_f32_16x16x32_bf16 v[222:225], v[136:139], v[112:115], 0\n"
        "buffer_load_dwordx4 v244, s[8:11], s35 offen lds\n"
        "v_mfma_f32_16x16x32_bf16 v[222:225], v[140:143], v[116:119], v[222:225]\n"
        "ds_read_b128 a[224:227], v240 offset:0\n"
        "ds_read_b128 a[228:231], v241 offset:0\n"
        "v_mfma_f32_16x16x32_bf16 v[162:165], v[148:151], v[116:119], 0\n"
        "ds_read_b128 a[232:235], v240 offset:128\n"
        "v_cvt_pk_bf16_f32 v78, v104, v105\n"
        "v_mfma_f32_16x16x32_bf16 v[162:165], v[144:147], v[112:115], v[162:165]\n"
        "v_cvt_pk_bf16_f32 v79, v106, v107\n"
        "v_mul_f32 v108, v72, v108\n"
        "v_mfma_f32_16x16x32_bf16 v[96:99], a[236:239], a[144:147], 0\n"
        "ds_write_b128 v245, v[76:79] offset:256\n"
        "v_mfma_f32_16x16x32_bf16 v[96:99], a[240:243], a[148:151], v[96:99]\n"
        "v_mul_f32 v109, v72, v109\n"
        "v_mul_f32 v110, v72, v110\n"
        "v_mfma_f32_16x16x32_bf16 v[96:99], a[244:247], a[152:155], v[96:99]\n"
        "v_mul_f32 v111, v72, v111\n"
        "v_cvt_pk_bf16_f32 v108, v108, v109\n"
        "v_mfma_f32_16x16x32_bf16 v[96:99], a[248:251], a[156:159], v[96:99]\n"
        "v_cvt_pk_bf16_f32 v109, v110, v111\n"
        "v_mul_f32 v68, v64, v68\n"
        "v_mfma_f32_16x16x32_bf16 v[96:99], a[216:219], a[160:163], v[96:99]\n"
        "v_rcp_f32 v80, v80\n"
        "v_mfma_f32_16x16x32_bf16 v[96:99], a[220:223], a[164:167], v[96:99]\n"
        "ds_write_b64 v246, v[108:109] offset:256\n"
        "v_mfma_f32_16x16x32_bf16 v[100:103], a[236:239], a[168:171], 0\n"
        "v_mul_f32 v69, v64, v69\n"
        "v_mul_f32 v70, v64, v70\n"
        "v_mfma_f32_16x16x32_bf16 v[100:103], a[240:243], a[172:175], v[100:103]\n"
        "v_mul_f32 v71, v64, v71\n"
        "v_cvt_pk_bf16_f32 v68, v68, v69\n"
        "v_mfma_f32_16x16x32_bf16 v[100:103], a[244:247], a[176:179], v[100:103]\n"
        "v_cvt_pk_bf16_f32 v134, v96, v97\n"
        "v_cvt_pk_bf16_f32 v135, v98, v99\n"
        "v_mfma_f32_16x16x32_bf16 v[100:103], a[248:251], a[180:183], v[100:103]\n"
        "v_cvt_pk_bf16_f32 v69, v70, v71\n"
        "v_mul_f32 v120, v64, v120\n"
        "v_mfma_f32_16x16x32_bf16 v[100:103], a[216:219], a[184:187], v[100:103]\n"
        "v_mul_f32 v121, v64, v121\n"
        "v_mul_f32 v122, v64, v122\n"
        "v_mfma_f32_16x16x32_bf16 v[100:103], a[220:223], a[188:191], v[100:103]\n"
        "v_mul_f32 v123, v64, v123\n"
        "v_cvt_pk_bf16_f32 v70, v120, v121\n"
        "v_mfma_f32_16x16x32_bf16 v[112:115], a[236:239], a[192:195], 0\n"
        "ds_read_b128 a[236:239], v241 offset:128\n"
        "v_cvt_pk_bf16_f32 v71, v122, v123\n"
        "v_mfma_f32_16x16x32_bf16 v[112:115], a[240:243], a[196:199], v[112:115]\n"
        "ds_read_b128 a[240:243], v240 offset:256\n"
        "v_mul_f32 v124, v64, v124\n"
        "v_mfma_f32_16x16x32_bf16 v[112:115], a[244:247], a[200:203], v[112:115]\n"
        "ds_read_b128 a[244:247], v241 offset:256\n"
        "v_cvt_pk_bf16_f32 v142, v100, v101\n"
        "v_mfma_f32_16x16x32_bf16 v[112:115], a[248:251], a[204:207], v[112:115]\n"
        "ds_read_b128 a[248:251], v240 offset:1536\n"
        "v_cvt_pk_bf16_f32 v143, v102, v103\n"
        "v_mfma_f32_16x16x32_bf16 v[112:115], a[216:219], a[208:211], v[112:115]\n"
        "ds_read_b128 a[216:219], v241 offset:1536\n"
        "v_mul_f32 v125, v64, v125\n"
        "v_mfma_f32_16x16x32_bf16 v[112:115], a[220:223], a[212:215], v[112:115]\n"
        "s_waitcnt lgkmcnt(9)\n"
        "ds_read_b128 a[220:223], v240 offset:1664\n"
        "v_mul_f32 v126, v64, v126\n"
        "v_mfma_f32_16x16x32_bf16 v[96:99], a[224:227], a[144:147], 0\n"
        "s_waitcnt lgkmcnt(9)\n"
        "ds_write_b128 v245, v[68:71] offset:512\n"
        "v_mfma_f32_16x16x32_bf16 v[96:99], a[228:231], a[148:151], v[96:99]\n"
        "s_waitcnt lgkmcnt(9)\n"
        "v_mul_f32 v127, v64, v127\n"
        "v_cvt_pk_bf16_f32 v124, v124, v125\n"
        "v_mfma_f32_16x16x32_bf16 v[96:99], a[232:235], a[152:155], v[96:99]\n"
        "s_waitcnt lgkmcnt(6)\n"
        "v_cvt_pk_bf16_f32 v150, v112, v113\n"
        "v_cvt_pk_bf16_f32 v151, v114, v115\n"
        "v_mfma_f32_16x16x32_bf16 v[96:99], a[236:239], a[156:159], v[96:99]\n"
        "s_waitcnt lgkmcnt(5)\n"
        "v_cvt_pk_bf16_f32 v125, v126, v127\n"
        "v_mul_f32 v84, v80, v84\n"
        "v_mfma_f32_16x16x32_bf16 v[96:99], a[240:243], a[160:163], v[96:99]\n"
        "s_waitcnt lgkmcnt(4)\n"
        "v_mul_f32 v85, v80, v85\n"
        "v_mul_f32 v86, v80, v86\n"
        "v_mfma_f32_16x16x32_bf16 v[96:99], a[244:247], a[164:167], v[96:99]\n"
        "ds_write_b64 v246, v[124:125] offset:512\n"
        "v_mfma_f32_16x16x32_bf16 v[100:103], a[224:227], a[168:171], 0\n"
        "v_mul_f32 v87, v80, v87\n"
        "v_cvt_pk_bf16_f32 v84, v84, v85\n"
        "v_mfma_f32_16x16x32_bf16 v[100:103], a[228:231], a[172:175], v[100:103]\n"
        "v_cvt_pk_bf16_f32 v85, v86, v87\n"
        "v_mul_f32 v222, v80, v222\n"
        "v_mfma_f32_16x16x32_bf16 v[100:103], a[232:235], a[176:179], v[100:103]\n"
        "v_cvt_pk_bf16_f32 v128, v96, v97\n"
        "v_cvt_pk_bf16_f32 v129, v98, v99\n"
        "v_mfma_f32_16x16x32_bf16 v[100:103], a[236:239], a[180:183], v[100:103]\n"
        "v_mul_f32 v223, v80, v223\n"
        "v_mul_f32 v224, v80, v224\n"
        "v_mfma_f32_16x16x32_bf16 v[100:103], a[240:243], a[184:187], v[100:103]\n"
        "v_mul_f32 v225, v80, v225\n"
        "v_cvt_pk_bf16_f32 v86, v222, v223\n"
        "v_mfma_f32_16x16x32_bf16 v[100:103], a[244:247], a[188:191], v[100:103]\n"
        "v_cvt_pk_bf16_f32 v87, v224, v225\n"
        "v_mul_f32 v162, v80, v162\n"
        "v_mfma_f32_16x16x32_bf16 v[112:115], a[224:227], a[192:195], 0\n"
        "ds_read_b128 a[224:227], v241 offset:1664\n"
        "v_mul_f32 v163, v80, v163\n"
        "v_mfma_f32_16x16x32_bf16 v[112:115], a[228:231], a[196:199], v[112:115]\n"
        "ds_read_b128 a[228:231], v240 offset:1792\n"
        "v_mul_f32 v164, v80, v164\n"
        "v_mfma_f32_16x16x32_bf16 v[112:115], a[232:235], a[200:203], v[112:115]\n"
        "ds_read_b128 a[232:235], v241 offset:1792\n"
        "v_cvt_pk_bf16_f32 v136, v100, v101\n"
        "v_mfma_f32_16x16x32_bf16 v[112:115], a[236:239], a[204:207], v[112:115]\n"
        "ds_read_b128 a[236:239], v240 offset:12288\n"
        "v_cvt_pk_bf16_f32 v137, v102, v103\n"
        "v_mfma_f32_16x16x32_bf16 v[112:115], a[240:243], a[208:211], v[112:115]\n"
        "ds_read_b128 a[240:243], v241 offset:12288\n"
        "v_mul_f32 v165, v80, v165\n"
        "v_mfma_f32_16x16x32_bf16 v[112:115], a[244:247], a[212:215], v[112:115]\n"
        "s_waitcnt lgkmcnt(9)\n"
        "ds_read_b128 a[244:247], v240 offset:12416\n"
        "v_cvt_pk_bf16_f32 v162, v162, v163\n"
        "v_mfma_f32_16x16x32_bf16 v[96:99], a[248:251], a[144:147], 0\n"
        "s_waitcnt lgkmcnt(9)\n"
        "ds_write_b128 v245, v[84:87] offset:768\n"
        "v_mfma_f32_16x16x32_bf16 v[96:99], a[216:219], a[148:151], v[96:99]\n"
        "s_waitcnt lgkmcnt(9)\n"
        "v_cvt_pk_bf16_f32 v163, v164, v165\n"
        "v_mfma_f32_16x16x32_bf16 v[96:99], a[220:223], a[152:155], v[96:99]\n"
        "s_waitcnt lgkmcnt(6)\n"
        "v_mfma_f32_16x16x32_bf16 v[96:99], a[224:227], a[156:159], v[96:99]\n"
        "s_waitcnt lgkmcnt(5)\n"
        "v_cvt_pk_bf16_f32 v144, v112, v113\n"
        "v_cvt_pk_bf16_f32 v145, v114, v115\n"
        "v_mfma_f32_16x16x32_bf16 v[96:99], a[228:231], a[160:163], v[96:99]\n"
        "s_waitcnt lgkmcnt(4)\n"
        "v_mfma_f32_16x16x32_bf16 v[96:99], a[232:235], a[164:167], v[96:99]\n"
        "ds_write_b64 v246, v[162:163] offset:768\n"
        "v_mfma_f32_16x16x32_bf16 v[100:103], a[248:251], a[168:171], 0\n"
        "v_mfma_f32_16x16x32_bf16 v[100:103], a[216:219], a[172:175], v[100:103]\n"
        "v_mfma_f32_16x16x32_bf16 v[100:103], a[220:223], a[176:179], v[100:103]\n"
        "v_mfma_f32_16x16x32_bf16 v[100:103], a[224:227], a[180:183], v[100:103]\n"
        "v_mfma_f32_16x16x32_bf16 v[100:103], a[228:231], a[184:187], v[100:103]\n"
        "v_mfma_f32_16x16x32_bf16 v[100:103], a[232:235], a[188:191], v[100:103]\n"
        "v_mfma_f32_16x16x32_bf16 v[112:115], a[248:251], a[192:195], 0\n"
        "ds_read_b128 a[248:251], v241 offset:12416\n"
        "ds_read_b128 v[162:165], v251 offset:0\n"
        "v_mfma_f32_16x16x32_bf16 v[112:115], a[216:219], a[196:199], v[112:115]\n"
        "ds_read_b128 a[216:219], v240 offset:12544\n"
        "v_cvt_pk_bf16_f32 v130, v96, v97\n"
        "v_mfma_f32_16x16x32_bf16 v[112:115], a[220:223], a[200:203], v[112:115]\n"
        "ds_read_b128 a[220:223], v241 offset:12544\n"
        "v_cvt_pk_bf16_f32 v131, v98, v99\n"
        "v_mfma_f32_16x16x32_bf16 v[112:115], a[224:227], a[204:207], v[112:115]\n"
        "v_cvt_pk_bf16_f32 v138, v100, v101\n"
        "v_cvt_pk_bf16_f32 v139, v102, v103\n"
        "v_mfma_f32_16x16x32_bf16 v[112:115], a[228:231], a[208:211], v[112:115]\n"
        "ds_read_b128 v[182:185], v251 offset:512\n"
        "v_mfma_f32_16x16x32_bf16 v[112:115], a[232:235], a[212:215], v[112:115]\n"
        "s_waitcnt lgkmcnt(9)\n"
        "v_mfma_f32_16x16x32_bf16 v[96:99], a[236:239], a[144:147], 0\n"
        "s_waitcnt lgkmcnt(8)\n"
        "v_mfma_f32_16x16x32_bf16 v[96:99], a[240:243], a[148:151], v[96:99]\n"
        "s_waitcnt lgkmcnt(7)\n"
        "v_mfma_f32_16x16x32_bf16 v[96:99], a[244:247], a[152:155], v[96:99]\n"
        "s_waitcnt lgkmcnt(4)\n"
        "ds_read_b128 v[202:205], v251 offset:4096\n"
        "v_mfma_f32_16x16x32_bf16 v[96:99], a[248:251], a[156:159], v[96:99]\n"
        "s_waitcnt lgkmcnt(3)\n"
        "v_mfma_f32_16x16x32_bf16 v[96:99], a[216:219], a[160:163], v[96:99]\n"
        "s_waitcnt lgkmcnt(2)\n"
        "v_mfma_f32_16x16x32_bf16 v[96:99], a[220:223], a[164:167], v[96:99]\n"
        "s_add_u32 s47, s47, 6291456\n"
        "v_cvt_pk_bf16_f32 v146, v112, v113\n"
        "v_cvt_pk_bf16_f32 v147, v114, v115\n"
        "v_mfma_f32_16x16x32_bf16 v[100:103], a[236:239], a[168:171], 0\n"
        "ds_read_b128 v[222:225], v251 offset:4608\n"
        "s_add_u32 s36, s36, 2097152\n"
        "v_mfma_f32_16x16x32_bf16 v[100:103], a[240:243], a[172:175], v[100:103]\n"
        "s_xor_b32 s48, s48, 8192\n"
        "v_cvt_pk_bf16_f32 v132, v96, v97\n"
        "v_cvt_pk_bf16_f32 v133, v98, v99\n"
        "v_mfma_f32_16x16x32_bf16 v[100:103], a[244:247], a[176:179], v[100:103]\n"
        "v_pk_min_u16 v162, v162, s26\n"
        "v_pk_min_u16 v163, v163, s26\n"
        "v_mfma_f32_16x16x32_bf16 v[100:103], a[248:251], a[180:183], v[100:103]\n"
        "v_pk_min_u16 v164, v164, s26\n"
        "v_pk_min_u16 v165, v165, s26\n"
        "v_mfma_f32_16x16x32_bf16 v[100:103], a[216:219], a[184:187], v[100:103]\n"
        "v_add_u32 v240, s31, v240\n"
        "v_add_u32 v241, s31, v241\n"
        "v_mfma_f32_16x16x32_bf16 v[100:103], a[220:223], a[188:191], v[100:103]\n"
        "v_mfma_f32_16x16x32_bf16 v[112:115], a[236:239], a[192:195], 0\n"
        "s_waitcnt lgkmcnt(2)\n"
        "v_pk_min_u16 v182, v182, s26\n"
        "v_pk_min_u16 v183, v183, s26\n"
        "v_mfma_f32_16x16x32_bf16 v[112:115], a[240:243], a[196:199], v[112:115]\n"
        "v_pk_min_u16 v184, v184, s26\n"
        "v_pk_min_u16 v185, v185, s26\n"
        "v_mfma_f32_16x16x32_bf16 v[112:115], a[244:247], a[200:203], v[112:115]\n"
        "v_mfma_f32_16x16x32_bf16 v[112:115], a[248:251], a[204:207], v[112:115]\n"
        "v_cvt_pk_bf16_f32 v140, v100, v101\n"
        "v_cvt_pk_bf16_f32 v141, v102, v103\n"
        "v_mfma_f32_16x16x32_bf16 v[112:115], a[216:219], a[208:211], v[112:115]\n"
        "s_waitcnt lgkmcnt(1)\n"
        "v_pk_min_u16 v202, v202, s26\n"
        "v_pk_min_u16 v203, v203, s26\n"
        "v_mfma_f32_16x16x32_bf16 v[112:115], a[220:223], a[212:215], v[112:115]\n"
        "v_pk_min_u16 v204, v204, s26\n"
        "v_pk_min_u16 v205, v205, s26\n"
        "v_mfma_f32_16x16x32_bf16 v[64:67], v[152:155], v[156:159], v[0:3]\n"
        "v_mfma_f32_16x16x32_bf16 v[64:67], v[168:171], v[160:163], v[64:67]\n"
        "v_mfma_f32_16x16x32_bf16 v[68:71], v[172:175], v[156:159], v[4:7]\n"
        "s_waitcnt lgkmcnt(0)\n"
        "v_pk_min_u16 v222, v222, s26\n"
        "v_pk_min_u16 v223, v223, s26\n"
        "v_mfma_f32_16x16x32_bf16 v[68:71], v[188:191], v[164:167], v[68:71]\n"
        "ds_read_b128 v[162:165], v252 offset:0\n"
        "v_cvt_pk_bf16_f32 v148, v112, v113\n"
        "v_mfma_f32_16x16x32_bf16 v[80:83], v[152:155], v[176:179], v[16:19]\n"
        "v_cvt_pk_bf16_f32 v149, v114, v115\n"
        "v_pk_min_u16 v224, v224, s26\n"
        "v_mfma_f32_16x16x32_bf16 v[80:83], v[168:171], v[180:183], v[80:83]\n"
        "v_pk_min_u16 v225, v225, s26\n"
        "v_max3_f32 v232, v64, v65, v66\n"
        "v_mfma_f32_16x16x32_bf16 v[84:87], v[172:175], v[176:179], v[20:23]\n"
        "v_max3_f32 v232, v232, v67, v68\n"
        "v_max3_f32 v232, v232, v69, v70\n"
        "v_mfma_f32_16x16x32_bf16 v[84:87], v[188:191], v[184:187], v[84:87]\n"
        "ds_read_b128 v[182:185], v252 offset:512\n"
        "v_mfma_f32_16x16x32_bf16 v[96:99], v[152:155], v[196:199], v[32:35]\n"
        "v_max3_f32 v233, v80, v81, v82\n"
        "v_mfma_f32_16x16x32_bf16 v[96:99], v[168:171], v[200:203], v[96:99]\n"
        "v_mfma_f32_16x16x32_bf16 v[100:103], v[172:175], v[196:199], v[36:39]\n"
        "v_mfma_f32_16x16x32_bf16 v[100:103], v[188:191], v[204:207], v[100:103]\n"
        "ds_read_b128 v[202:205], v252 offset:4096\n"
        "v_mfma_f32_16x16x32_bf16 v[112:115], v[152:155], v[216:219], v[48:51]\n"
        "s_waitcnt lgkmcnt(2)\n"
        "v_pk_min_u16 v162, v162, s26\n"
        "v_pk_min_u16 v163, v163, s26\n"
        "v_mfma_f32_16x16x32_bf16 v[112:115], v[168:171], v[220:223], v[112:115]\n"
        "v_pk_min_u16 v164, v164, s26\n"
        "v_pk_min_u16 v165, v165, s26\n"
        "v_mfma_f32_16x16x32_bf16 v[116:119], v[172:175], v[216:219], v[52:55]\n"
        "v_max3_f32 v233, v233, v83, v84\n"
        "v_max3_f32 v233, v233, v85, v86\n"
        "v_mfma_f32_16x16x32_bf16 v[116:119], v[188:191], v[224:227], v[116:119]\n"
        "ds_read_b128 v[222:225], v252 offset:4608\n"
        "v_max3_f32 v234, v96, v97, v98\n"
        "v_mfma_f32_16x16x32_bf16 v[72:75], v[192:195], v[156:159], v[8:11]\n"
        "s_waitcnt lgkmcnt(2)\n"
        "v_pk_min_u16 v182, v182, s26\n"
        "v_pk_min_u16 v183, v183, s26\n"
        "v_mfma_f32_16x16x32_bf16 v[72:75], v[208:211], v[160:163], v[72:75]\n"
        "v_pk_min_u16 v184, v184, s26\n"
        "v_pk_min_u16 v185, v185, s26\n"
        "v_mfma_f32_16x16x32_bf16 v[76:79], v[212:215], v[156:159], v[12:15]\n"
        "v_max3_f32 v234, v234, v99, v100\n"
        "v_max3_f32 v234, v234, v101, v102\n"
        "v_mfma_f32_16x16x32_bf16 v[76:79], v[228:231], v[164:167], v[76:79]\n"
        "v_max3_f32 v235, v112, v113, v114\n"
        "v_max3_f32 v235, v235, v115, v116\n"
        "v_mfma_f32_16x16x32_bf16 v[88:91], v[192:195], v[176:179], v[24:27]\n"
        "s_barrier\n"
        "ds_read_b128 v[152:155], v247 offset:0\n"
        "ds_read_b128 v[160:163], v247 offset:4096\n"
        "v_mfma_f32_16x16x32_bf16 v[88:91], v[208:211], v[180:183], v[88:91]\n"
        "ds_read_b128 v[156:159], v247 offset:256\n"
        "ds_read_b128 v[164:167], v247 offset:4352\n"
        "v_mfma_f32_16x16x32_bf16 v[92:95], v[212:215], v[176:179], v[28:31]\n"
        "ds_read_b128 a[216:219], v248 offset:0\n"
        "ds_read_b128 a[220:223], v248 offset:6144\n"
        "v_mfma_f32_16x16x32_bf16 v[92:95], v[228:231], v[184:187], v[92:95]\n"
        "s_waitcnt lgkmcnt(7)\n"
        "v_pk_min_u16 v202, v202, s26\n"
        "v_pk_min_u16 v203, v203, s26\n"
        "v_mfma_f32_16x16x32_bf16 v[104:107], v[192:195], v[196:199], v[40:43]\n"
        "ds_read_b128 a[224:227], v254 offset:16\n"
        "ds_read_b128 a[228:231], v254 offset:6160\n"
        "v_mfma_f32_16x16x32_bf16 v[104:107], v[208:211], v[200:203], v[104:107]\n"
        "v_pk_min_u16 v204, v204, s26\n"
        "v_pk_min_u16 v205, v205, s26\n"
        "v_mfma_f32_16x16x32_bf16 v[108:111], v[212:215], v[196:199], v[44:47]\n"
        "s_waitcnt lgkmcnt(8)\n"
        "v_pk_min_u16 v222, v222, s26\n"
        "v_pk_min_u16 v223, v223, s26\n"
        "v_mfma_f32_16x16x32_bf16 v[108:111], v[228:231], v[204:207], v[108:111]\n"
        "v_pk_min_u16 v224, v224, s26\n"
        "v_pk_min_u16 v225, v225, s26\n"
        "v_mfma_f32_16x16x32_bf16 v[120:123], v[192:195], v[216:219], v[56:59]\n"
        "ds_read_b128 a[232:235], v248 offset:32\n"
        "v_max3_f32 v232, v232, v71, v72\n"
        "v_mfma_f32_16x16x32_bf16 v[120:123], v[208:211], v[220:223], v[120:123]\n"
        "v_max3_f32 v232, v232, v73, v74\n"
        "v_max3_f32 v232, v232, v75, v76\n"
        "v_mfma_f32_16x16x32_bf16 v[124:127], v[212:215], v[216:219], v[60:63]\n"
        "v_max3_f32 v232, v232, v77, v78\n"
        "v_max_f32 v232, v232, v79\n"
        "v_mfma_f32_16x16x32_bf16 v[124:127], v[228:231], v[224:227], v[124:127]\n"
        "s_waitcnt lgkmcnt(4)\n"
        "v_max3_f32 v233, v233, v87, v88\n"
        "v_max3_f32 v233, v233, v89, v90\n"
        "v_mfma_f32_16x16x32_bf16 v[172:175], v[152:155], a[216:219], 0\n"
        "s_waitcnt lgkmcnt(3)\n"
        "v_max3_f32 v233, v233, v91, v92\n"
        "v_max3_f32 v233, v233, v93, v94\n"
        "v_mfma_f32_16x16x32_bf16 v[172:175], v[160:163], a[220:223], v[172:175]\n"
        "v_max_f32 v233, v233, v95\n"
        "v_max3_f32 v234, v234, v103, v104\n"
        "v_mfma_f32_16x16x32_bf16 v[180:183], v[164:167], a[220:223], 0\n"
        "ds_read_b128 a[220:223], v248 offset:6176\n"
        "ds_read_b128 a[236:239], v247 offset:8192\n"
        "v_mfma_f32_16x16x32_bf16 v[180:183], v[156:159], a[216:219], v[180:183]\n"
        "s_waitcnt lgkmcnt(4)\n"
        "ds_read_b128 a[216:219], v254 offset:48\n"
        "ds_read_b128 a[244:247], v247 offset:12288\n"
        "v_mfma_f32_16x16x32_bf16 v[184:187], v[156:159], a[224:227], 0\n"
        "s_waitcnt lgkmcnt(5)\n"
        "ds_read_b128 a[240:243], v247 offset:8448\n"
        "ds_read_b128 a[248:251], v247 offset:12544\n"
        "v_mfma_f32_16x16x32_bf16 v[184:187], v[164:167], a[228:231], v[184:187]\n"
        "v_permlane32_swap_b32 v232, v233\n"
        "v_mfma_f32_16x16x32_bf16 v[176:179], v[160:163], a[228:231], 0\n"
        "ds_read_b128 a[228:231], v254 offset:6192\n"
        "v_max_f32 v232, v232, v233\n"
        "v_mfma_f32_16x16x32_bf16 v[176:179], v[152:155], a[224:227], v[176:179]\n"
        "s_waitcnt lgkmcnt(7)\n"
        "ds_read_b128 a[224:227], v248 offset:64\n"
        "v_mov_b32 v233, v232\n"
        "v_mfma_f32_16x16x32_bf16 v[212:215], v[152:155], a[232:235], 0\n"
        "s_waitcnt lgkmcnt(7)\n"
        "v_max3_f32 v234, v234, v105, v106\n"
        "v_max3_f32 v234, v234, v107, v108\n"
        "v_mfma_f32_16x16x32_bf16 v[212:215], v[160:163], a[220:223], v[212:215]\n"
        "v_permlane16_swap_b32 v232, v233\n"
        "v_mfma_f32_16x16x32_bf16 v[192:195], v[164:167], a[220:223], 0\n"
        "ds_read_b128 a[220:223], v248 offset:6208\n"
        "v_max_f32 v232, v232, v233\n"
        "v_mfma_f32_16x16x32_bf16 v[192:195], v[156:159], a[232:235], v[192:195]\n"
        "s_waitcnt lgkmcnt(6)\n"
        "ds_read_b128 a[232:235], v254 offset:80\n"
        "v_mul_f32 v236, s25, v232\n"
        "v_mfma_f32_16x16x32_bf16 v[196:199], v[156:159], a[216:219], 0\n"
        "s_waitcnt lgkmcnt(3)\n"
        "v_mul_f32 v237, s25, v232\n"
        "v_max3_f32 v234, v234, v109, v110\n"
        "v_mfma_f32_16x16x32_bf16 v[196:199], v[164:167], a[228:231], v[196:199]\n"
        "v_permlane32_swap_b32 v236, v237\n"
        "v_mfma_f32_16x16x32_bf16 v[216:219], v[160:163], a[228:231], 0\n"
        "ds_read_b128 a[228:231], v254 offset:6224\n"
        "v_max_f32 v234, v234, v111\n"
        "v_mfma_f32_16x16x32_bf16 v[216:219], v[152:155], a[216:219], v[216:219]\n"
        "s_waitcnt lgkmcnt(3)\n"
        "ds_read_b128 a[216:219], v248 offset:36864\n"
        "v_max3_f32 v235, v235, v117, v118\n"
        "v_mfma_f32_16x16x32_bf16 v[220:223], v[152:155], a[224:227], 0\n"
        "s_waitcnt lgkmcnt(3)\n"
        "v_max3_f32 v235, v235, v119, v120\n"
        "v_max3_f32 v235, v235, v121, v122\n"
        "v_mfma_f32_16x16x32_bf16 v[220:223], v[160:163], a[220:223], v[220:223]\n"
        "v_max3_f32 v235, v235, v123, v124\n"
        "v_max3_f32 v235, v235, v125, v126\n"
        "v_mfma_f32_16x16x32_bf16 v[200:203], v[164:167], a[220:223], 0\n"
        "ds_read_b128 a[220:223], v248 offset:43008\n"
        "v_max_f32 v235, v235, v127\n"
        "v_mfma_f32_16x16x32_bf16 v[200:203], v[156:159], a[224:227], v[200:203]\n"
        "s_waitcnt lgkmcnt(3)\n"
        "ds_read_b128 a[224:227], v254 offset:36880\n"
        "v_fma_f32 v64, v64, s24, v236\n"
        "v_mfma_f32_16x16x32_bf16 v[204:207], v[156:159], a[232:235], 0\n"
        "s_waitcnt lgkmcnt(3)\n"
        "v_permlane32_swap_b32 v234, v235\n"
        "v_mfma_f32_16x16x32_bf16 v[204:207], v[164:167], a[228:231], v[204:207]\n"
        "v_max_f32 v234, v234, v235\n"
        "v_mov_b32 v235, v234\n"
        "v_mfma_f32_16x16x32_bf16 v[224:227], v[160:163], a[228:231], 0\n"
        "ds_read_b128 a[228:231], v254 offset:43024\n"
        "v_fma_f32 v65, v65, s24, v236\n"
        "v_mfma_f32_16x16x32_bf16 v[224:227], v[152:155], a[232:235], v[224:227]\n"
        "s_waitcnt lgkmcnt(3)\n"
        "ds_read_b128 a[232:235], v248 offset:36896\n"
        "v_fma_f32 v66, v66, s24, v236\n"
        "v_mfma_f32_16x16x32_bf16 v[172:175], a[236:239], a[216:219], v[172:175]\n"
        "s_waitcnt lgkmcnt(3)\n"
        "v_permlane16_swap_b32 v234, v235\n"
        "v_mfma_f32_16x16x32_bf16 v[172:175], a[244:247], a[220:223], v[172:175]\n"
        "v_max_f32 v234, v234, v235\n"
        "v_mul_f32 v238, s25, v234\n"
        "v_mfma_f32_16x16x32_bf16 v[180:183], a[248:251], a[220:223], v[180:183]\n"
        "ds_read_b128 a[220:223], v248 offset:43040\n"
        "ds_read_b128 v[152:155], v247 offset:16384\n"
        "v_mfma_f32_16x16x32_bf16 v[180:183], a[240:243], a[216:219], v[180:183]\n"
        "s_waitcnt lgkmcnt(4)\n"
        "ds_read_b128 a[216:219], v254 offset:36912\n"
        "ds_read_b128 v[160:163], v247 offset:20480\n"
        "v_mfma_f32_16x16x32_bf16 v[184:187], a[240:243], a[224:227], v[184:187]\n"
        "s_waitcnt lgkmcnt(5)\n"
        "ds_read_b128 v[156:159], v247 offset:16640\n"
        "ds_read_b128 v[164:167], v247 offset:20736\n"
        "v_mfma_f32_16x16x32_bf16 v[184:187], a[248:251], a[228:231], v[184:187]\n"
        "v_mul_f32 v239, s25, v234\n"
        "v_fma_f32 v67, v67, s24, v236\n"
        "v_mfma_f32_16x16x32_bf16 v[176:179], a[244:247], a[228:231], v[176:179]\n"
        "ds_read_b128 a[228:231], v254 offset:43056\n"
        "v_fma_f32 v68, v68, s24, v236\n"
        "v_mfma_f32_16x16x32_bf16 v[176:179], a[236:239], a[224:227], v[176:179]\n"
        "s_waitcnt lgkmcnt(7)\n"
        "ds_read_b128 a[224:227], v248 offset:36928\n"
        "v_fma_f32 v69, v69, s24, v236\n"
        "v_mfma_f32_16x16x32_bf16 v[212:215], a[236:239], a[232:235], v[212:215]\n"
        "s_waitcnt lgkmcnt(7)\n"
        "v_permlane32_swap_b32 v238, v239\n"
        "v_mfma_f32_16x16x32_bf16 v[212:215], a[244:247], a[220:223], v[212:215]\n"
        "v_exp_f32 v64, v64\n"
        "v_mfma_f32_16x16x32_bf16 v[192:195], a[248:251], a[220:223], v[192:195]\n"
        "ds_read_b128 a[220:223], v248 offset:43072\n"
        "v_fma_f32 v70, v70, s24, v236\n"
        "v_mfma_f32_16x16x32_bf16 v[192:195], a[240:243], a[232:235], v[192:195]\n"
        "s_waitcnt lgkmcnt(6)\n"
        "ds_read_b128 a[232:235], v254 offset:36944\n"
        "v_fma_f32 v71, v71, s24, v236\n"
        "v_mfma_f32_16x16x32_bf16 v[196:199], a[240:243], a[216:219], v[196:199]\n"
        "s_waitcnt lgkmcnt(3)\n"
        "v_exp_f32 v65, v65\n"
        "v_mfma_f32_16x16x32_bf16 v[196:199], a[248:251], a[228:231], v[196:199]\n"
        "v_exp_f32 v66, v66\n"
        "v_mfma_f32_16x16x32_bf16 v[216:219], a[244:247], a[228:231], v[216:219]\n"
        "ds_read_b128 a[228:231], v254 offset:43088\n"
        "v_cvt_pk_bf16_f32 v64, v64, v65\n"
        "v_mfma_f32_16x16x32_bf16 v[216:219], a[236:239], a[216:219], v[216:219]\n"
        "s_waitcnt lgkmcnt(3)\n"
        "ds_read_b128 a[216:219], v248 offset:12288\n"
        "v_fma_f32 v72, v72, s24, v236\n"
        "v_mfma_f32_16x16x32_bf16 v[220:223], a[236:239], a[224:227], v[220:223]\n"
        "s_waitcnt lgkmcnt(3)\n"
        "v_exp_f32 v67, v67\n"
        "v_mfma_f32_16x16x32_bf16 v[220:223], a[244:247], a[220:223], v[220:223]\n"
        "v_exp_f32 v68, v68\n"
        "v_mfma_f32_16x16x32_bf16 v[200:203], a[248:251], a[220:223], v[200:203]\n"
        "ds_read_b128 a[220:223], v248 offset:49152\n"
        "v_cvt_pk_bf16_f32 v65, v66, v67\n"
        "v_mfma_f32_16x16x32_bf16 v[200:203], a[240:243], a[224:227], v[200:203]\n"
        "s_waitcnt lgkmcnt(3)\n"
        "ds_read_b128 a[224:227], v254 offset:12304\n"
        "v_fma_f32 v73, v73, s24, v236\n"
        "v_mfma_f32_16x16x32_bf16 v[204:207], a[240:243], a[232:235], v[204:207]\n"
        "s_waitcnt lgkmcnt(3)\n"
        "v_exp_f32 v69, v69\n"
        "v_mfma_f32_16x16x32_bf16 v[204:207], a[248:251], a[228:231], v[204:207]\n"
        "v_exp_f32 v70, v70\n"
        "v_mfma_f32_16x16x32_bf16 v[224:227], a[244:247], a[228:231], v[224:227]\n"
        "ds_read_b128 a[228:231], v254 offset:49168\n"
        "v_cvt_pk_bf16_f32 v66, v68, v69\n"
        "v_mfma_f32_16x16x32_bf16 v[224:227], a[236:239], a[232:235], v[224:227]\n"
        "s_waitcnt lgkmcnt(3)\n"
        "ds_read_b128 a[232:235], v248 offset:12320\n"
        "v_fma_f32 v74, v74, s24, v236\n"
        "v_mfma_f32_16x16x32_bf16 v[172:175], v[152:155], a[216:219], v[172:175]\n"
        "s_waitcnt lgkmcnt(3)\n"
        "v_exp_f32 v71, v71\n"
        "v_mfma_f32_16x16x32_bf16 v[172:175], v[160:163], a[220:223], v[172:175]\n"
        "v_exp_f32 v72, v72\n"
        "v_mfma_f32_16x16x32_bf16 v[180:183], v[164:167], a[220:223], v[180:183]\n"
        "ds_read_b128 a[220:223], v248 offset:49184\n"
        "v_cvt_pk_bf16_f32 v67, v70, v71\n"
        "v_mfma_f32_16x16x32_bf16 v[180:183], v[156:159], a[216:219], v[180:183]\n"
        "s_waitcnt lgkmcnt(3)\n"
        "ds_read_b128 a[216:219], v254 offset:12336\n"
        "v_fma_f32 v75, v75, s24, v236\n"
        "v_mfma_f32_16x16x32_bf16 v[184:187], v[156:159], a[224:227], v[184:187]\n"
        "s_waitcnt lgkmcnt(3)\n"
        "v_exp_f32 v73, v73\n"
        "v_mfma_f32_16x16x32_bf16 v[184:187], v[164:167], a[228:231], v[184:187]\n"
        "v_cvt_pk_bf16_f32 v68, v72, v73\n"
        "v_xor_b32 v251, 0x2000, v251\n"
        "v_mfma_f32_16x16x32_bf16 v[176:179], v[160:163], a[228:231], v[176:179]\n"
        "ds_read_b128 a[228:231], v254 offset:49200\n"
        "v_xor_b32 v252, 0x2000, v252\n"
        "v_mfma_f32_16x16x32_bf16 v[176:179], v[152:155], a[224:227], v[176:179]\n"
        "s_waitcnt lgkmcnt(3)\n"
        "ds_read_b128 a[224:227], v248 offset:12352\n"
        "v_add_u32 v245, s45, v245\n"
        "v_mfma_f32_16x16x32_bf16 v[212:215], v[152:155], a[232:235], v[212:215]\n"
        "s_waitcnt lgkmcnt(3)\n"
        "s_waitcnt vmcnt(0)\n"
        "s_barrier\n"
        "v_exp_f32 v74, v74\n"
        "v_mfma_f32_16x16x32_bf16 v[212:215], v[160:163], a[220:223], v[212:215]\n"
        "v_exp_f32 v75, v75\n"
        "v_mfma_f32_16x16x32_bf16 v[192:195], v[164:167], a[220:223], v[192:195]\n"
        "ds_read_b128 a[220:223], v248 offset:49216\n"
        "ds_read_b128 a[236:239], v240 offset:0\n"
        "v_mfma_f32_16x16x32_bf16 v[192:195], v[156:159], a[232:235], v[192:195]\n"
        "s_waitcnt lgkmcnt(4)\n"
        "ds_read_b128 a[232:235], v254 offset:12368\n"
        "ds_read_b128 a[240:243], v241 offset:0\n"
        "v_mfma_f32_16x16x32_bf16 v[196:199], v[156:159], a[216:219], v[196:199]\n"
        "s_waitcnt lgkmcnt(5)\n"
        "ds_read_b128 a[244:247], v240 offset:128\n"
        "v_cvt_pk_bf16_f32 v69, v74, v75\n"
        "v_mfma_f32_16x16x32_bf16 v[196:199], v[164:167], a[228:231], v[196:199]\n"
        "ds_read_b128 a[248:251], v241 offset:128\n"
        "v_add_u32 v246, s45, v246\n"
        "v_mfma_f32_16x16x32_bf16 v[216:219], v[160:163], a[228:231], v[216:219]\n"
        "ds_read_b128 a[228:231], v254 offset:49232\n"
        "v_add_u32 v247, s45, v247\n"
        "v_mfma_f32_16x16x32_bf16 v[216:219], v[152:155], a[216:219], v[216:219]\n"
        "s_waitcnt lgkmcnt(7)\n"
        "ds_read_b128 a[216:219], v240 offset:256\n"
        "v_mfma_f32_16x16x32_bf16 v[220:223], v[152:155], a[224:227], v[220:223]\n"
        "s_waitcnt lgkmcnt(7)\n"
        "v_mfma_f32_16x16x32_bf16 v[220:223], v[160:163], a[220:223], v[220:223]\n"
        "v_mfma_f32_16x16x32_bf16 v[200:203], v[164:167], a[220:223], v[200:203]\n"
        "ds_read_b128 a[220:223], v241 offset:256\n"
        "v_mfma_f32_16x16x32_bf16 v[200:203], v[156:159], a[224:227], v[200:203]\n"
        "s_waitcnt lgkmcnt(6)\n"
        "v_mfma_f32_16x16x32_bf16 v[204:207], v[156:159], a[232:235], v[204:207]\n"
        "s_waitcnt lgkmcnt(2)\n"
        "v_mfma_f32_16x16x32_bf16 v[204:207], v[164:167], a[228:231], v[204:207]\n"
        "v_mfma_f32_16x16x32_bf16 v[224:227], v[160:163], a[228:231], v[224:227]\n"
        "v_mfma_f32_16x16x32_bf16 v[224:227], v[152:155], a[232:235], v[224:227]\n"
        "s_waitcnt lgkmcnt(0)\n"
        ".p2align 6\n"
        "10:\n"
        "v_mfma_f32_16x16x32_bf16 v[162:165], a[72:75], a[236:239], 0\n"
        "v_fma_f32 v76, v76, s24, v236\n"
        "v_fma_f32 v77, v77, s24, v236\n"
        "v_mfma_f32_16x16x32_bf16 v[162:165], a[76:79], a[240:243], v[162:165]\n"
        "s_waitcnt lgkmcnt(3)\n"
        "s_add_u32 s41, s30, s40\n"
        "v_cvt_pk_bf16_f32 v232, v172, v176\n"
        "v_cvt_pk_bf16_f32 v233, v212, v216\n"
        "v_mfma_f32_16x16x32_bf16 v[162:165], a[80:83], a[244:247], v[162:165]\n"
        "s_waitcnt lgkmcnt(2)\n"
        "v_cvt_pk_bf16_f32 v234, v220, v224\n"
        "v_fma_f32 v78, v78, s24, v236\n"
        "v_mfma_f32_16x16x32_bf16 v[162:165], a[84:87], a[248:251], v[162:165]\n"
        "s_waitcnt lgkmcnt(1)\n"
        "buffer_store_dwordx3 v[232:234], v250, s[16:19], s38 offen offset:0 sc1\n"
        "v_mfma_f32_16x16x32_bf16 v[162:165], a[88:91], a[216:219], v[162:165]\n"
        "s_waitcnt lgkmcnt(0)\n"
        "v_exp_f32 v76, v76\n"
        "v_mfma_f32_16x16x32_bf16 v[162:165], a[92:95], a[220:223], v[162:165]\n"
        "v_exp_f32 v77, v77\n"
        "v_mfma_f32_16x16x32_bf16 v[72:75], a[116:119], a[220:223], 0\n"
        "v_fma_f32 v79, v79, s24, v236\n"
        "v_cvt_pk_bf16_f32 v70, v76, v77\n"
        "v_mfma_f32_16x16x32_bf16 v[72:75], a[112:115], a[216:219], v[72:75]\n"
        "v_exp_f32 v78, v78\n"
        "v_mfma_f32_16x16x32_bf16 v[72:75], a[108:111], a[248:251], v[72:75]\n"
        "v_exp_f32 v79, v79\n"
        "v_mfma_f32_16x16x32_bf16 v[72:75], a[104:107], a[244:247], v[72:75]\n"
        "v_cvt_pk_bf16_f32 v71, v78, v79\n"
        "v_cvt_pk_bf16_f32 v232, v173, v177\n"
        "v_mfma_f32_16x16x32_bf16 v[72:75], a[100:103], a[240:243], v[72:75]\n"
        "v_cvt_pk_bf16_f32 v233, v213, v217\n"
        "v_cvt_pk_bf16_f32 v234, v221, v225\n"
        "v_mfma_f32_16x16x32_bf16 v[72:75], a[96:99], a[236:239], v[72:75]\n"
        "buffer_store_dwordx3 v[232:234], v250, s[16:19], s38 offen offset:384 sc1\n"
        "v_mfma_f32_16x16x32_bf16 v[76:79], a[120:123], a[236:239], 0\n"
        "s_add_u32 s35, s47, 786432\n"
        "v_cvt_pk_bf16_f32 v152, v162, v163\n"
        "v_cvt_pk_bf16_f32 v153, v164, v165\n"
        "v_mfma_f32_16x16x32_bf16 v[76:79], a[124:127], a[240:243], v[76:79]\n"
        "s_sub_u32 s46, s35, 100663296\n"
        "v_fma_f32 v80, v80, s24, v237\n"
        "v_cvt_pk_bf16_f32 v154, v72, v73\n"
        "v_mfma_f32_16x16x32_bf16 v[76:79], a[128:131], a[244:247], v[76:79]\n"
        "s_cmp_lt_u32 s46, 786432\n"
        "v_exp_f32 v80, v80\n"
        "v_mfma_f32_16x16x32_bf16 v[76:79], a[132:135], a[248:251], v[76:79]\n"
        "s_cselect_b32 s35, s46, s35\n"
        "v_cvt_pk_bf16_f32 v155, v74, v75\n"
        "v_cvt_pk_bf16_f32 v232, v174, v178\n"
        "v_mfma_f32_16x16x32_bf16 v[76:79], a[136:139], a[216:219], v[76:79]\n"
        "v_cvt_pk_bf16_f32 v233, v214, v218\n"
        "v_cvt_pk_bf16_f32 v234, v222, v226\n"
        "v_mfma_f32_16x16x32_bf16 v[76:79], a[140:143], a[220:223], v[76:79]\n"
        "v_fma_f32 v81, v81, s24, v237\n"
        "v_fma_f32 v82, v82, s24, v237\n"
        "v_mfma_f32_16x16x32_bf16 v[162:165], a[20:23], a[220:223], 0\n"
        "v_exp_f32 v81, v81\n"
        "v_mfma_f32_16x16x32_bf16 v[162:165], a[16:19], a[216:219], v[162:165]\n"
        "v_exp_f32 v82, v82\n"
        "v_mfma_f32_16x16x32_bf16 v[162:165], a[12:15], a[248:251], v[162:165]\n"
        "buffer_store_dwordx3 v[232:234], v250, s[16:19], s38 offen offset:768 sc1\n"
        "v_mfma_f32_16x16x32_bf16 v[162:165], a[8:11], a[244:247], v[162:165]\n"
        "v_cvt_pk_bf16_f32 v168, v76, v77\n"
        "v_cvt_pk_bf16_f32 v169, v78, v79\n"
        "v_mfma_f32_16x16x32_bf16 v[162:165], a[4:7], a[240:243], v[162:165]\n"
        "v_cvt_pk_bf16_f32 v80, v80, v81\n"
        "v_fma_f32 v83, v83, s24, v237\n"
        "v_mfma_f32_16x16x32_bf16 v[162:165], a[0:3], a[236:239], v[162:165]\n"
        "v_exp_f32 v83, v83\n"
        "v_mfma_f32_16x16x32_bf16 v[72:75], a[24:27], a[236:239], 0\n"
        "v_cvt_pk_bf16_f32 v232, v175, v179\n"
        "v_cvt_pk_bf16_f32 v233, v215, v219\n"
        "v_mfma_f32_16x16x32_bf16 v[72:75], a[28:31], a[240:243], v[72:75]\n"
        "v_cvt_pk_bf16_f32 v234, v223, v227\n"
        "v_cvt_pk_bf16_f32 v81, v82, v83\n"
        "v_mfma_f32_16x16x32_bf16 v[72:75], a[32:35], a[244:247], v[72:75]\n"
        "v_cvt_pk_bf16_f32 v156, v162, v163\n"
        "v_cvt_pk_bf16_f32 v157, v164, v165\n"
        "v_mfma_f32_16x16x32_bf16 v[72:75], a[36:39], a[248:251], v[72:75]\n"
        "v_fma_f32 v84, v84, s24, v237\n"
        "v_fma_f32 v85, v85, s24, v237\n"
        "v_mfma_f32_16x16x32_bf16 v[72:75], a[40:43], a[216:219], v[72:75]\n"
        "v_exp_f32 v84, v84\n"
        "v_mfma_f32_16x16x32_bf16 v[72:75], a[44:47], a[220:223], v[72:75]\n"
        "buffer_store_dwordx3 v[232:234], v250, s[16:19], s38 offen offset:1152 sc1\n"
        "v_mfma_f32_16x16x32_bf16 v[76:79], a[68:71], a[220:223], 0\n"
        "v_exp_f32 v85, v85\n"
        "v_mfma_f32_16x16x32_bf16 v[76:79], a[64:67], a[216:219], v[76:79]\n"
        "v_cvt_pk_bf16_f32 v82, v84, v85\n"
        "v_fma_f32 v86, v86, s24, v237\n"
        "v_mfma_f32_16x16x32_bf16 v[76:79], a[60:63], a[248:251], v[76:79]\n"
        "ds_read_b128 a[224:227], v240 offset:1536\n"
        "v_cvt_pk_bf16_f32 v158, v72, v73\n"
        "v_mfma_f32_16x16x32_bf16 v[76:79], a[56:59], a[244:247], v[76:79]\n"
        "ds_read_b128 a[228:231], v241 offset:1536\n"
        "v_cvt_pk_bf16_f32 v159, v74, v75\n"
        "v_mfma_f32_16x16x32_bf16 v[76:79], a[52:55], a[240:243], v[76:79]\n"
        "ds_read_b128 a[232:235], v240 offset:1664\n"
        "v_cvt_pk_bf16_f32 v232, v180, v184\n"
        "v_mfma_f32_16x16x32_bf16 v[76:79], a[48:51], a[236:239], v[76:79]\n"
        "v_cvt_pk_bf16_f32 v233, v192, v196\n"
        "v_cvt_pk_bf16_f32 v234, v200, v204\n"
        "v_mfma_f32_16x16x32_bf16 v[162:165], a[236:239], a[144:147], 0\n"
        "v_exp_f32 v86, v86\n"
        "v_mfma_f32_16x16x32_bf16 v[162:165], a[240:243], a[148:151], v[162:165]\n"
        "v_fma_f32 v87, v87, s24, v237\n"
        "v_fma_f32 v88, v88, s24, v237\n"
        "v_mfma_f32_16x16x32_bf16 v[162:165], a[244:247], a[152:155], v[162:165]\n"
        "buffer_store_dwordx3 v[232:234], v250, s[16:19], s39 offen offset:0 sc1\n"
        "v_mfma_f32_16x16x32_bf16 v[162:165], a[248:251], a[156:159], v[162:165]\n"
        "v_exp_f32 v87, v87\n"
        "v_mfma_f32_16x16x32_bf16 v[162:165], a[216:219], a[160:163], v[162:165]\n"
        "v_cvt_pk_bf16_f32 v160, v76, v77\n"
        "v_cvt_pk_bf16_f32 v161, v78, v79\n"
        "v_mfma_f32_16x16x32_bf16 v[162:165], a[220:223], a[164:167], v[162:165]\n"
        "v_cvt_pk_bf16_f32 v166, v76, v77\n"
        "v_cvt_pk_bf16_f32 v167, v78, v79\n"
        "v_mfma_f32_16x16x32_bf16 v[72:75], a[220:223], a[188:191], 0\n"
        "v_exp_f32 v88, v88\n"
        "v_mfma_f32_16x16x32_bf16 v[72:75], a[216:219], a[184:187], v[72:75]\n"
        "v_cvt_pk_bf16_f32 v232, v181, v185\n"
        "v_cvt_pk_bf16_f32 v233, v193, v197\n"
        "v_mfma_f32_16x16x32_bf16 v[72:75], a[248:251], a[180:183], v[72:75]\n"
        "v_cvt_pk_bf16_f32 v234, v201, v205\n"
        "v_cvt_pk_bf16_f32 v212, v162, v163\n"
        "v_mfma_f32_16x16x32_bf16 v[72:75], a[244:247], a[176:179], v[72:75]\n"
        "v_cvt_pk_bf16_f32 v213, v164, v165\n"
        "v_cvt_pk_bf16_f32 v83, v86, v87\n"
        "v_mfma_f32_16x16x32_bf16 v[72:75], a[240:243], a[172:175], v[72:75]\n"
        "v_fma_f32 v89, v89, s24, v237\n"
        "v_fma_f32 v90, v90, s24, v237\n"
        "v_mfma_f32_16x16x32_bf16 v[72:75], a[236:239], a[168:171], v[72:75]\n"
        "buffer_store_dwordx3 v[232:234], v250, s[16:19], s39 offen offset:384 sc1\n"
        "v_mfma_f32_16x16x32_bf16 v[76:79], a[236:239], a[192:195], 0\n"
        "ds_read_b128 a[236:239], v241 offset:1664\n"
        "v_fma_f32 v91, v91, s24, v237\n"
        "v_mfma_f32_16x16x32_bf16 v[76:79], a[240:243], a[196:199], v[76:79]\n"
        "ds_read_b128 a[240:243], v240 offset:1792\n"
        "v_fma_f32 v92, v92, s24, v237\n"
        "v_mfma_f32_16x16x32_bf16 v[76:79], a[244:247], a[200:203], v[76:79]\n"
        "ds_read_b128 a[244:247], v241 offset:1792\n"
        "v_cvt_pk_bf16_f32 v214, v72, v73\n"
        "v_mfma_f32_16x16x32_bf16 v[76:79], a[248:251], a[204:207], v[76:79]\n"
        "v_exp_f32 v89, v89\n"
        "v_mfma_f32_16x16x32_bf16 v[76:79], a[216:219], a[208:211], v[76:79]\n"
        "v_cvt_pk_bf16_f32 v215, v74, v75\n"
        "v_cvt_pk_bf16_f32 v232, v182, v186\n"
        "v_mfma_f32_16x16x32_bf16 v[76:79], a[220:223], a[212:215], v[76:79]\n"
        "s_waitcnt lgkmcnt(5)\n"
        "v_cvt_pk_bf16_f32 v233, v194, v198\n"
        "v_cvt_pk_bf16_f32 v234, v202, v206\n"
        "v_mfma_f32_16x16x32_bf16 v[162:165], a[72:75], a[224:227], 0\n"
        "s_waitcnt lgkmcnt(4)\n"
        "v_exp_f32 v90, v90\n"
        "v_mfma_f32_16x16x32_bf16 v[162:165], a[76:79], a[228:231], v[162:165]\n"
        "s_waitcnt lgkmcnt(3)\n"
        "v_exp_f32 v91, v91\n"
        "v_mfma_f32_16x16x32_bf16 v[162:165], a[80:83], a[232:235], v[162:165]\n"
        "s_waitcnt lgkmcnt(2)\n"
        "buffer_store_dwordx3 v[232:234], v250, s[16:19], s39 offen offset:768 sc1\n"
        "v_mfma_f32_16x16x32_bf16 v[162:165], a[84:87], a[236:239], v[162:165]\n"
        "s_waitcnt lgkmcnt(1)\n"
        "v_exp_f32 v92, v92\n"
        "v_mfma_f32_16x16x32_bf16 v[162:165], a[88:91], a[240:243], v[162:165]\n"
        "s_waitcnt lgkmcnt(0)\n"
        "v_cvt_pk_bf16_f32 v216, v76, v77\n"
        "v_cvt_pk_bf16_f32 v217, v78, v79\n"
        "v_mfma_f32_16x16x32_bf16 v[162:165], a[92:95], a[244:247], v[162:165]\n"
        "v_cvt_pk_bf16_f32 v232, v183, v187\n"
        "v_cvt_pk_bf16_f32 v233, v195, v199\n"
        "v_mfma_f32_16x16x32_bf16 v[72:75], a[116:119], a[244:247], 0\n"
        "v_cvt_pk_bf16_f32 v234, v203, v207\n"
        "v_cvt_pk_bf16_f32 v84, v88, v89\n"
        "v_mfma_f32_16x16x32_bf16 v[72:75], a[112:115], a[240:243], v[72:75]\n"
        "v_cvt_pk_bf16_f32 v85, v90, v91\n"
        "v_fma_f32 v93, v93, s24, v237\n"
        "v_mfma_f32_16x16x32_bf16 v[72:75], a[108:111], a[236:239], v[72:75]\n"
        "v_exp_f32 v93, v93\n"
        "v_mfma_f32_16x16x32_bf16 v[72:75], a[104:107], a[232:235], v[72:75]\n"
        "v_cvt_pk_bf16_f32 v172, v162, v163\n"
        "v_cvt_pk_bf16_f32 v173, v164, v165\n"
        "v_mfma_f32_16x16x32_bf16 v[72:75], a[100:103], a[228:231], v[72:75]\n"
        "v_cvt_pk_bf16_f32 v86, v92, v93\n"
        "v_fma_f32 v94, v94, s24, v237\n"
        "v_mfma_f32_16x16x32_bf16 v[72:75], a[96:99], a[224:227], v[72:75]\n"
        "buffer_store_dwordx3 v[232:234], v250, s[16:19], s39 offen offset:1152 sc1\n"
        "v_mfma_f32_16x16x32_bf16 v[76:79], a[120:123], a[224:227], 0\n"
        "v_exp_f32 v94, v94\n"
        "v_mfma_f32_16x16x32_bf16 v[76:79], a[124:127], a[228:231], v[76:79]\n"
        "v_fma_f32 v95, v95, s24, v237\n"
        "v_fma_f32 v96, v96, s24, v238\n"
        "v_mfma_f32_16x16x32_bf16 v[76:79], a[128:131], a[232:235], v[76:79]\n"
        "v_exp_f32 v95, v95\n"
        "v_mfma_f32_16x16x32_bf16 v[76:79], a[132:135], a[236:239], v[76:79]\n"
        "v_exp_f32 v96, v96\n"
        "v_mfma_f32_16x16x32_bf16 v[76:79], a[136:139], a[240:243], v[76:79]\n"
        "v_cvt_pk_bf16_f32 v174, v72, v73\n"
        "v_cvt_pk_bf16_f32 v175, v74, v75\n"
        "v_mfma_f32_16x16x32_bf16 v[76:79], a[140:143], a[244:247], v[76:79]\n"
        "v_cvt_pk_bf16_f32 v87, v94, v95\n"
        "v_fma_f32 v97, v97, s24, v238\n"
        "v_mfma_f32_16x16x32_bf16 v[162:165], a[20:23], a[244:247], 0\n"
        "s_mov_b32 m0, s48\n"
        "v_exp_f32 v97, v97\n"
        "v_mfma_f32_16x16x32_bf16 v[162:165], a[16:19], a[240:243], v[162:165]\n"
        "v_cvt_pk_bf16_f32 v96, v96, v97\n"
        "v_fma_f32 v98, v98, s24, v238\n"
        "v_mfma_f32_16x16x32_bf16 v[162:165], a[12:15], a[236:239], v[162:165]\n"
        "buffer_load_dwordx4 v249, s[12:15], s36 offen offset:0 lds\n"
        "v_mfma_f32_16x16x32_bf16 v[162:165], a[8:11], a[232:235], v[162:165]\n"
        "v_exp_f32 v98, v98\n"
        "v_mfma_f32_16x16x32_bf16 v[162:165], a[4:7], a[228:231], v[162:165]\n"
        "v_cvt_pk_bf16_f32 v190, v76, v77\n"
        "v_cvt_pk_bf16_f32 v191, v78, v79\n"
        "v_mfma_f32_16x16x32_bf16 v[162:165], a[0:3], a[224:227], v[162:165]\n"
        "v_fma_f32 v99, v99, s24, v238\n"
        "v_fma_f32 v100, v100, s24, v238\n"
        "v_mfma_f32_16x16x32_bf16 v[72:75], a[24:27], a[224:227], 0\n"
        "v_exp_f32 v99, v99\n"
        "v_mfma_f32_16x16x32_bf16 v[72:75], a[28:31], a[228:231], v[72:75]\n"
        "v_exp_f32 v100, v100\n"
        "v_mfma_f32_16x16x32_bf16 v[72:75], a[32:35], a[232:235], v[72:75]\n"
        "v_cvt_pk_bf16_f32 v97, v98, v99\n"
        "v_cvt_pk_bf16_f32 v176, v162, v163\n"
        "v_mfma_f32_16x16x32_bf16 v[72:75], a[36:39], a[236:239], v[72:75]\n"
        "v_cvt_pk_bf16_f32 v177, v164, v165\n"
        "v_fma_f32 v101, v101, s24, v238\n"
        "v_mfma_f32_16x16x32_bf16 v[72:75], a[40:43], a[240:243], v[72:75]\n"
        "v_exp_f32 v101, v101\n"
        "v_mfma_f32_16x16x32_bf16 v[72:75], a[44:47], a[244:247], v[72:75]\n"
        "v_cvt_pk_bf16_f32 v98, v100, v101\n"
        "v_fma_f32 v102, v102, s24, v238\n"
        "v_mfma_f32_16x16x32_bf16 v[76:79], a[68:71], a[244:247], 0\n"
        "v_xor_b32 v253, 32, v249\n"
        "v_fma_f32 v103, v103, s24, v238\n"
        "v_mfma_f32_16x16x32_bf16 v[76:79], a[64:67], a[240:243], v[76:79]\n"
        "v_exp_f32 v102, v102\n"
        "v_mfma_f32_16x16x32_bf16 v[76:79], a[60:63], a[236:239], v[76:79]\n"
        "ds_read_b128 a[248:251], v240 offset:12288\n"
        "v_cvt_pk_bf16_f32 v178, v72, v73\n"
        "v_mfma_f32_16x16x32_bf16 v[76:79], a[56:59], a[232:235], v[76:79]\n"
        "ds_read_b128 a[216:219], v241 offset:12288\n"
        "v_cvt_pk_bf16_f32 v179, v74, v75\n"
        "v_mfma_f32_16x16x32_bf16 v[76:79], a[52:55], a[228:231], v[76:79]\n"
        "buffer_load_dwordx4 v253, s[12:15], s36 offen offset:1024 lds\n"
        "v_mfma_f32_16x16x32_bf16 v[76:79], a[48:51], a[224:227], v[76:79]\n"
        "ds_read_b128 a[220:223], v240 offset:12416\n"
        "v_fma_f32 v104, v104, s24, v238\n"
        "v_mfma_f32_16x16x32_bf16 v[162:165], a[224:227], a[144:147], 0\n"
        "v_exp_f32 v103, v103\n"
        "v_mfma_f32_16x16x32_bf16 v[162:165], a[228:231], a[148:151], v[162:165]\n"
        "v_exp_f32 v104, v104\n"
        "v_mfma_f32_16x16x32_bf16 v[162:165], a[232:235], a[152:155], v[162:165]\n"
        "v_cvt_pk_bf16_f32 v99, v102, v103\n"
        "v_cvt_pk_bf16_f32 v180, v76, v77\n"
        "v_mfma_f32_16x16x32_bf16 v[162:165], a[236:239], a[156:159], v[162:165]\n"
        "v_cvt_pk_bf16_f32 v181, v78, v79\n"
        "v_cvt_pk_bf16_f32 v186, v76, v77\n"
        "v_mfma_f32_16x16x32_bf16 v[162:165], a[240:243], a[160:163], v[162:165]\n"
        "v_cvt_pk_bf16_f32 v187, v78, v79\n"
        "v_fma_f32 v105, v105, s24, v238\n"
        "v_mfma_f32_16x16x32_bf16 v[162:165], a[244:247], a[164:167], v[162:165]\n"
        "v_exp_f32 v105, v105\n"
        "v_mfma_f32_16x16x32_bf16 v[72:75], a[244:247], a[188:191], 0\n"
        "v_cvt_pk_bf16_f32 v100, v104, v105\n"
        "v_fma_f32 v106, v106, s24, v238\n"
        "v_mfma_f32_16x16x32_bf16 v[72:75], a[240:243], a[184:187], v[72:75]\n"
        "v_exp_f32 v106, v106\n"
        "v_mfma_f32_16x16x32_bf16 v[72:75], a[236:239], a[180:183], v[72:75]\n"
        "v_fma_f32 v107, v107, s24, v238\n"
        "v_cvt_pk_bf16_f32 v218, v162, v163\n"
        "v_mfma_f32_16x16x32_bf16 v[72:75], a[232:235], a[176:179], v[72:75]\n"
        "v_exp_f32 v107, v107\n"
        "v_mfma_f32_16x16x32_bf16 v[72:75], a[228:231], a[172:175], v[72:75]\n"
        "s_add_u32 m0, s41, 0\n"
        "v_cvt_pk_bf16_f32 v219, v164, v165\n"
        "v_cvt_pk_bf16_f32 v101, v106, v107\n"
        "v_mfma_f32_16x16x32_bf16 v[72:75], a[224:227], a[168:171], v[72:75]\n"
        "v_fma_f32 v108, v108, s24, v238\n"
        "v_fma_f32 v109, v109, s24, v238\n"
        "v_mfma_f32_16x16x32_bf16 v[76:79], a[224:227], a[192:195], 0\n"
        "ds_read_b128 a[224:227], v241 offset:12416\n"
        "v_fma_f32 v110, v110, s24, v238\n"
        "v_mfma_f32_16x16x32_bf16 v[76:79], a[228:231], a[196:199], v[76:79]\n"
        "ds_read_b128 a[228:231], v240 offset:12544\n"
        "v_fma_f32 v111, v111, s24, v238\n"
        "v_mfma_f32_16x16x32_bf16 v[76:79], a[232:235], a[200:203], v[76:79]\n"
        "buffer_load_dwordx4 v242, s[8:11], s47 offen lds\n"
        "s_sub_u32 s31, s30, s28\n"
        "v_mfma_f32_16x16x32_bf16 v[76:79], a[236:239], a[204:207], v[76:79]\n"
        "ds_read_b128 a[232:235], v241 offset:12544\n"
        "s_sub_u32 s45, s28, s29\n"
        "v_cvt_pk_bf16_f32 v220, v72, v73\n"
        "v_mfma_f32_16x16x32_bf16 v[76:79], a[240:243], a[208:211], v[76:79]\n"
        "s_add_u32 s28, s28, s31\n"
        "v_cvt_pk_bf16_f32 v221, v74, v75\n"
        "v_fma_f32 v112, v112, s24, v239\n"
        "v_mfma_f32_16x16x32_bf16 v[76:79], a[244:247], a[212:215], v[76:79]\n"
        "s_waitcnt lgkmcnt(5)\n"
        "v_exp_f32 v108, v108\n"
        "v_mfma_f32_16x16x32_bf16 v[162:165], a[72:75], a[248:251], 0\n"
        "s_waitcnt lgkmcnt(4)\n"
        "v_exp_f32 v109, v109\n"
        "v_mfma_f32_16x16x32_bf16 v[162:165], a[76:79], a[216:219], v[162:165]\n"
        "s_waitcnt lgkmcnt(3)\n"
        "v_exp_f32 v110, v110\n"
        "v_mfma_f32_16x16x32_bf16 v[162:165], a[80:83], a[220:223], v[162:165]\n"
        "s_waitcnt lgkmcnt(2)\n"
        "v_exp_f32 v111, v111\n"
        "v_mfma_f32_16x16x32_bf16 v[162:165], a[84:87], a[224:227], v[162:165]\n"
        "s_waitcnt lgkmcnt(1)\n"
        "v_exp_f32 v112, v112\n"
        "v_mfma_f32_16x16x32_bf16 v[162:165], a[88:91], a[228:231], v[162:165]\n"
        "s_waitcnt lgkmcnt(0)\n"
        "v_cvt_pk_bf16_f32 v222, v76, v77\n"
        "v_cvt_pk_bf16_f32 v223, v78, v79\n"
        "v_mfma_f32_16x16x32_bf16 v[162:165], a[92:95], a[232:235], v[162:165]\n"
        "s_mov_b32 s30, s29\n"
        "v_cvt_pk_bf16_f32 v102, v108, v109\n"
        "v_cvt_pk_bf16_f32 v103, v110, v111\n"
        "v_mfma_f32_16x16x32_bf16 v[72:75], a[116:119], a[232:235], 0\n"
        "s_add_u32 m0, s41, 1024\n"
        "v_fma_f32 v113, v113, s24, v239\n"
        "v_fma_f32 v114, v114, s24, v239\n"
        "v_mfma_f32_16x16x32_bf16 v[72:75], a[112:115], a[228:231], v[72:75]\n"
        "s_add_u32 s29, s29, s45\n"
        "v_exp_f32 v113, v113\n"
        "v_mfma_f32_16x16x32_bf16 v[72:75], a[108:111], a[224:227], v[72:75]\n"
        "buffer_load_dwordx4 v243, s[8:11], s47 offen lds\n"
        "v_mfma_f32_16x16x32_bf16 v[72:75], a[104:107], a[220:223], v[72:75]\n"
        "v_exp_f32 v114, v114\n"
        "v_mfma_f32_16x16x32_bf16 v[72:75], a[100:103], a[216:219], v[72:75]\n"
        "v_cvt_pk_bf16_f32 v192, v162, v163\n"
        "v_cvt_pk_bf16_f32 v193, v164, v165\n"
        "v_mfma_f32_16x16x32_bf16 v[72:75], a[96:99], a[248:251], v[72:75]\n"
        "v_cvt_pk_bf16_f32 v112, v112, v113\n"
        "v_fma_f32 v115, v115, s24, v239\n"
        "v_mfma_f32_16x16x32_bf16 v[76:79], a[120:123], a[248:251], 0\n"
        "v_exp_f32 v115, v115\n"
        "v_mfma_f32_16x16x32_bf16 v[76:79], a[124:127], a[216:219], v[76:79]\n"
        "v_cvt_pk_bf16_f32 v113, v114, v115\n"
        "v_fma_f32 v116, v116, s24, v239\n"
        "v_mfma_f32_16x16x32_bf16 v[76:79], a[128:131], a[220:223], v[76:79]\n"
        "v_exp_f32 v116, v116\n"
        "v_mfma_f32_16x16x32_bf16 v[76:79], a[132:135], a[224:227], v[76:79]\n"
        "v_cvt_pk_bf16_f32 v194, v72, v73\n"
        "v_cvt_pk_bf16_f32 v195, v74, v75\n"
        "v_mfma_f32_16x16x32_bf16 v[76:79], a[136:139], a[228:231], v[76:79]\n"
        "v_fma_f32 v117, v117, s24, v239\n"
        "v_fma_f32 v118, v118, s24, v239\n"
        "v_mfma_f32_16x16x32_bf16 v[76:79], a[140:143], a[232:235], v[76:79]\n"
        "v_exp_f32 v117, v117\n"
        "v_mfma_f32_16x16x32_bf16 v[162:165], a[20:23], a[232:235], 0\n"
        "v_exp_f32 v118, v118\n"
        "v_mfma_f32_16x16x32_bf16 v[162:165], a[16:19], a[228:231], v[162:165]\n"
        "v_cvt_pk_bf16_f32 v114, v116, v117\n"
        "v_fma_f32 v119, v119, s24, v239\n"
        "v_mfma_f32_16x16x32_bf16 v[162:165], a[12:15], a[224:227], v[162:165]\n"
        "s_add_u32 m0, s41, 2048\n"
        "v_exp_f32 v119, v119\n"
        "v_mfma_f32_16x16x32_bf16 v[162:165], a[8:11], a[220:223], v[162:165]\n"
        "v_cvt_pk_bf16_f32 v208, v76, v77\n"
        "v_cvt_pk_bf16_f32 v209, v78, v79\n"
        "v_mfma_f32_16x16x32_bf16 v[162:165], a[4:7], a[216:219], v[162:165]\n"
        "buffer_load_dwordx4 v244, s[8:11], s47 offen lds\n"
        "v_mfma_f32_16x16x32_bf16 v[162:165], a[0:3], a[248:251], v[162:165]\n"
        "v_cvt_pk_bf16_f32 v115, v118, v119\n"
        "v_fma_f32 v120, v120, s24, v239\n"
        "v_mfma_f32_16x16x32_bf16 v[72:75], a[24:27], a[248:251], 0\n"
        "v_exp_f32 v120, v120\n"
        "v_mfma_f32_16x16x32_bf16 v[72:75], a[28:31], a[216:219], v[72:75]\n"
        "v_fma_f32 v121, v121, s24, v239\n"
        "v_fma_f32 v122, v122, s24, v239\n"
        "v_mfma_f32_16x16x32_bf16 v[72:75], a[32:35], a[220:223], v[72:75]\n"
        "v_exp_f32 v121, v121\n"
        "v_mfma_f32_16x16x32_bf16 v[72:75], a[36:39], a[224:227], v[72:75]\n"
        "v_exp_f32 v122, v122\n"
        "v_mfma_f32_16x16x32_bf16 v[72:75], a[40:43], a[228:231], v[72:75]\n"
        "v_cvt_pk_bf16_f32 v196, v162, v163\n"
        "v_cvt_pk_bf16_f32 v197, v164, v165\n"
        "v_mfma_f32_16x16x32_bf16 v[72:75], a[44:47], a[232:235], v[72:75]\n"
        "v_cvt_pk_bf16_f32 v116, v120, v121\n"
        "v_fma_f32 v123, v123, s24, v239\n"
        "v_mfma_f32_16x16x32_bf16 v[76:79], a[68:71], a[232:235], 0\n"
        "v_exp_f32 v123, v123\n"
        "v_mfma_f32_16x16x32_bf16 v[76:79], a[64:67], a[228:231], v[76:79]\n"
        "v_cvt_pk_bf16_f32 v117, v122, v123\n"
        "v_fma_f32 v124, v124, s24, v239\n"
        "v_mfma_f32_16x16x32_bf16 v[76:79], a[60:63], a[224:227], v[76:79]\n"
        "v_exp_f32 v124, v124\n"
        "v_mfma_f32_16x16x32_bf16 v[76:79], a[56:59], a[220:223], v[76:79]\n"
        "v_cvt_pk_bf16_f32 v198, v72, v73\n"
        "v_cvt_pk_bf16_f32 v199, v74, v75\n"
        "v_mfma_f32_16x16x32_bf16 v[76:79], a[52:55], a[216:219], v[76:79]\n"
        "s_add_u32 m0, s41, 12288\n"
        "v_fma_f32 v125, v125, s24, v239\n"
        "v_fma_f32 v126, v126, s24, v239\n"
        "v_mfma_f32_16x16x32_bf16 v[76:79], a[48:51], a[248:251], v[76:79]\n"
        "v_exp_f32 v125, v125\n"
        "v_mfma_f32_16x16x32_bf16 v[182:185], a[252:255], v[64:67], 0\n"
        "buffer_load_dwordx4 v242, s[8:11], s35 offen lds\n"
        "v_mfma_f32_16x16x32_bf16 v[182:185], a[252:255], v[68:71], v[182:185]\n"
        "v_exp_f32 v126, v126\n"
        "v_mfma_f32_16x16x32_bf16 v[202:205], v[132:135], v[68:71], 0\n"
        "v_cvt_pk_bf16_f32 v118, v124, v125\n"
        "v_fma_f32 v127, v127, s24, v239\n"
        "v_mfma_f32_16x16x32_bf16 v[202:205], v[128:131], v[64:67], v[202:205]\n"
        "v_exp_f32 v127, v127\n"
        "v_mfma_f32_16x16x32_bf16 v[88:91], v[136:139], v[64:67], 0\n"
        "v_cvt_pk_bf16_f32 v200, v76, v77\n"
        "v_cvt_pk_bf16_f32 v201, v78, v79\n"
        "v_mfma_f32_16x16x32_bf16 v[88:91], v[140:143], v[68:71], v[88:91]\n"
        "v_cvt_pk_bf16_f32 v206, v76, v77\n"
        "v_cvt_pk_bf16_f32 v207, v78, v79\n"
        "v_mfma_f32_16x16x32_bf16 v[92:95], v[148:151], v[68:71], 0\n"
        "s_cmp_eq_u32 s37, 100663296\n"
        "v_rcp_f32 v182, v182\n"
        "v_mfma_f32_16x16x32_bf16 v[92:95], v[144:147], v[64:67], v[92:95]\n"
        "s_cselect_b32 s46, 0, s37\n"
        "v_cvt_pk_bf16_f32 v119, v126, v127\n"
        "v_mul_f32 v202, v182, v202\n"
        "v_mfma_f32_16x16x32_bf16 v[64:67], a[252:255], v[80:83], 0\n"
        "s_add_u32 s38, s46, s43\n"
        "v_mul_f32 v203, v182, v203\n"
        "v_mul_f32 v204, v182, v204\n"
        "v_mfma_f32_16x16x32_bf16 v[64:67], a[252:255], v[84:87], v[64:67]\n"
        "s_add_u32 s39, s46, s44\n"
        "v_mul_f32 v205, v182, v205\n"
        "v_cvt_pk_bf16_f32 v202, v202, v203\n"
        "v_mfma_f32_16x16x32_bf16 v[68:71], v[132:135], v[84:87], 0\n"
        "s_add_u32 s37, s37, 6291456\n"
        "v_cvt_pk_bf16_f32 v203, v204, v205\n"
        "v_mul_f32 v88, v182, v88\n"
        "v_mfma_f32_16x16x32_bf16 v[68:71], v[128:131], v[80:83], v[68:71]\n"
        "v_rcp_f32 v64, v64\n"
        "v_mfma_f32_16x16x32_bf16 v[72:75], v[136:139], v[80:83], 0\n"
        "s_add_u32 m0, s41, 13312\n"
        "v_mul_f32 v89, v182, v89\n"
        "v_mul_f32 v90, v182, v90\n"
        "v_mfma_f32_16x16x32_bf16 v[72:75], v[140:143], v[84:87], v[72:75]\n"
        "v_mul_f32 v91, v182, v91\n"
        "v_cvt_pk_bf16_f32 v204, v88, v89\n"
        "v_mfma_f32_16x16x32_bf16 v[162:165], v[148:151], v[84:87], 0\n"
        "buffer_load_dwordx4 v243, s[8:11], s35 offen lds\n"
        "v_mfma_f32_16x16x32_bf16 v[162:165], v[144:147], v[80:83], v[162:165]\n"
        "v_cvt_pk_bf16_f32 v205, v90, v91\n"
        "v_mul_f32 v92, v182, v92\n"
        "v_mfma_f32_16x16x32_bf16 v[80:83], a[252:255], v[96:99], 0\n"
        "ds_write_b128 v245, v[202:205] offset:0\n"
        "v_mfma_f32_16x16x32_bf16 v[80:83], a[252:255], v[100:103], v[80:83]\n"
        "v_mul_f32 v93, v182, v93\n"
        "v_mul_f32 v94, v182, v94\n"
        "v_mfma_f32_16x16x32_bf16 v[84:87], v[132:135], v[100:103], 0\n"
        "v_mul_f32 v95, v182, v95\n"
        "v_cvt_pk_bf16_f32 v92, v92, v93\n"
        "v_mfma_f32_16x16x32_bf16 v[84:87], v[128:131], v[96:99], v[84:87]\n"
        "v_cvt_pk_bf16_f32 v93, v94, v95\n"
        "v_mul_f32 v68, v64, v68\n"
        "v_mfma_f32_16x16x32_bf16 v[76:79], v[136:139], v[96:99], 0\n"
        "v_rcp_f32 v80, v80\n"
        "v_mfma_f32_16x16x32_bf16 v[76:79], v[140:143], v[100:103], v[76:79]\n"
        "ds_write_b64 v246, v[92:93] offset:0\n"
        "v_mfma_f32_16x16x32_bf16 v[104:107], v[148:151], v[100:103], 0\n"
        "v_mul_f32 v69, v64, v69\n"
        "v_mul_f32 v70, v64, v70\n"
        "v_mfma_f32_16x16x32_bf16 v[104:107], v[144:147], v[96:99], v[104:107]\n"
        "v_mul_f32 v71, v64, v71\n"
        "v_cvt_pk_bf16_f32 v68, v68, v69\n"
        "v_mfma_f32_16x16x32_bf16 v[96:99], a[252:255], v[112:115], 0\n"
        "v_cvt_pk_bf16_f32 v69, v70, v71\n"
        "v_mul_f32 v72, v64, v72\n"
        "v_mfma_f32_16x16x32_bf16 v[96:99], a[252:255], v[116:119], v[96:99]\n"
        "v_mul_f32 v73, v64, v73\n"
        "v_mul_f32 v74, v64, v74\n"
        "v_mfma_f32_16x16x32_bf16 v[108:111], v[132:135], v[116:119], 0\n"
        "s_add_u32 m0, s41, 14336\n"
        "v_mul_f32 v75, v64, v75\n"
        "v_cvt_pk_bf16_f32 v70, v72, v73\n"
        "v_mfma_f32_16x16x32_bf16 v[108:111], v[128:131], v[112:115], v[108:111]\n"
        "v_mov_b32 v128, v212\n"
        "v_mov_b32 v129, v213\n"
        "v_mfma_f32_16x16x32_bf16 v[120:123], v[136:139], v[112:115], 0\n"
        "buffer_load_dwordx4 v244, s[8:11], s35 offen lds\n"
        "v_mfma_f32_16x16x32_bf16 v[120:123], v[140:143], v[116:119], v[120:123]\n"
        "ds_read_b128 a[236:239], v240 offset:13824\n"
        "v_mov_b32 v136, v214\n"
        "v_mfma_f32_16x16x32_bf16 v[124:127], v[148:151], v[116:119], 0\n"
        "ds_read_b128 a[240:243], v241 offset:13824\n"
        "v_mov_b32 v137, v215\n"
        "v_mfma_f32_16x16x32_bf16 v[124:127], v[144:147], v[112:115], v[124:127]\n"
        "ds_read_b128 a[244:247], v240 offset:13952\n"
        "v_mov_b32 v144, v216\n"
        "v_mfma_f32_16x16x32_bf16 v[100:103], a[248:251], a[144:147], 0\n"
        "v_mov_b32 v145, v217\n"
        "v_cvt_pk_bf16_f32 v71, v74, v75\n"
        "v_mfma_f32_16x16x32_bf16 v[100:103], a[216:219], a[148:151], v[100:103]\n"
        "ds_write_b128 v245, v[68:71] offset:256\n"
        "v_mfma_f32_16x16x32_bf16 v[100:103], a[220:223], a[152:155], v[100:103]\n"
        "v_mov_b32 v130, v218\n"
        "v_mov_b32 v131, v219\n"
        "v_mfma_f32_16x16x32_bf16 v[100:103], a[224:227], a[156:159], v[100:103]\n"
        "v_mov_b32 v138, v220\n"
        "v_mov_b32 v139, v221\n"
        "v_mfma_f32_16x16x32_bf16 v[100:103], a[228:231], a[160:163], v[100:103]\n"
        "v_mul_f32 v162, v64, v162\n"
        "v_mul_f32 v163, v64, v163\n"
        "v_mfma_f32_16x16x32_bf16 v[100:103], a[232:235], a[164:167], v[100:103]\n"
        "v_mul_f32 v164, v64, v164\n"
        "v_mul_f32 v165, v64, v165\n"
        "v_mfma_f32_16x16x32_bf16 v[112:115], a[232:235], a[188:191], 0\n"
        "v_mov_b32 v146, v222\n"
        "v_mov_b32 v147, v223\n"
        "v_mfma_f32_16x16x32_bf16 v[112:115], a[228:231], a[184:187], v[112:115]\n"
        "v_cvt_pk_bf16_f32 v162, v162, v163\n"
        "v_cvt_pk_bf16_f32 v163, v164, v165\n"
        "v_mfma_f32_16x16x32_bf16 v[112:115], a[224:227], a[180:183], v[112:115]\n"
        "ds_write_b64 v246, v[162:163] offset:256\n"
        "v_mfma_f32_16x16x32_bf16 v[112:115], a[220:223], a[176:179], v[112:115]\n"
        "v_cvt_pk_bf16_f32 v132, v100, v101\n"
        "v_cvt_pk_bf16_f32 v133, v102, v103\n"
        "v_mfma_f32_16x16x32_bf16 v[112:115], a[216:219], a[172:175], v[112:115]\n"
        "v_mul_f32 v84, v80, v84\n"
        "v_mul_f32 v85, v80, v85\n"
        "v_mfma_f32_16x16x32_bf16 v[112:115], a[248:251], a[168:171], v[112:115]\n"
        "v_rcp_f32 v96, v96\n"
        "v_mfma_f32_16x16x32_bf16 v[116:119], a[248:251], a[192:195], 0\n"
        "ds_read_b128 a[248:251], v241 offset:13952\n"
        "v_mul_f32 v86, v80, v86\n"
        "v_mfma_f32_16x16x32_bf16 v[116:119], a[216:219], a[196:199], v[116:119]\n"
        "ds_read_b128 a[216:219], v240 offset:14080\n"
        "v_mul_f32 v87, v80, v87\n"
        "v_mfma_f32_16x16x32_bf16 v[116:119], a[220:223], a[200:203], v[116:119]\n"
        "ds_read_b128 a[220:223], v241 offset:14080\n"
        "v_cvt_pk_bf16_f32 v140, v112, v113\n"
        "v_mfma_f32_16x16x32_bf16 v[116:119], a[224:227], a[204:207], v[116:119]\n"
        "v_cvt_pk_bf16_f32 v141, v114, v115\n"
        "v_cvt_pk_bf16_f32 v84, v84, v85\n"
        "v_mfma_f32_16x16x32_bf16 v[116:119], a[228:231], a[208:211], v[116:119]\n"
        "v_cvt_pk_bf16_f32 v85, v86, v87\n"
        "v_mul_f32 v76, v80, v76\n"
        "v_mfma_f32_16x16x32_bf16 v[116:119], a[232:235], a[212:215], v[116:119]\n"
        "s_waitcnt lgkmcnt(7)\n"
        "v_mul_f32 v77, v80, v77\n"
        "v_mul_f32 v78, v80, v78\n"
        "v_mfma_f32_16x16x32_bf16 v[100:103], a[236:239], a[144:147], 0\n"
        "s_waitcnt lgkmcnt(6)\n"
        "v_mul_f32 v79, v80, v79\n"
        "v_cvt_pk_bf16_f32 v86, v76, v77\n"
        "v_mfma_f32_16x16x32_bf16 v[100:103], a[240:243], a[148:151], v[100:103]\n"
        "s_waitcnt lgkmcnt(5)\n"
        "v_cvt_pk_bf16_f32 v87, v78, v79\n"
        "v_mul_f32 v104, v80, v104\n"
        "v_mfma_f32_16x16x32_bf16 v[100:103], a[244:247], a[152:155], v[100:103]\n"
        "s_waitcnt lgkmcnt(2)\n"
        "ds_write_b128 v245, v[84:87] offset:512\n"
        "v_mfma_f32_16x16x32_bf16 v[100:103], a[248:251], a[156:159], v[100:103]\n"
        "s_waitcnt lgkmcnt(2)\n"
        "v_cvt_pk_bf16_f32 v148, v116, v117\n"
        "v_cvt_pk_bf16_f32 v149, v118, v119\n"
        "v_mfma_f32_16x16x32_bf16 v[100:103], a[216:219], a[160:163], v[100:103]\n"
        "s_waitcnt lgkmcnt(1)\n"
        "v_mul_f32 v105, v80, v105\n"
        "v_mul_f32 v106, v80, v106\n"
        "v_mfma_f32_16x16x32_bf16 v[100:103], a[220:223], a[164:167], v[100:103]\n"
        "v_mul_f32 v107, v80, v107\n"
        "v_cvt_pk_bf16_f32 v104, v104, v105\n"
        "v_mfma_f32_16x16x32_bf16 v[112:115], a[220:223], a[188:191], 0\n"
        "v_cvt_pk_bf16_f32 v105, v106, v107\n"
        "v_mul_f32 v108, v96, v108\n"
        "v_mfma_f32_16x16x32_bf16 v[112:115], a[216:219], a[184:187], v[112:115]\n"
        "ds_write_b64 v246, v[104:105] offset:512\n"
        "v_mfma_f32_16x16x32_bf16 v[112:115], a[248:251], a[180:183], v[112:115]\n"
        "v_cvt_pk_bf16_f32 v134, v100, v101\n"
        "v_cvt_pk_bf16_f32 v135, v102, v103\n"
        "v_mfma_f32_16x16x32_bf16 v[112:115], a[244:247], a[176:179], v[112:115]\n"
        "v_mul_f32 v109, v96, v109\n"
        "v_mul_f32 v110, v96, v110\n"
        "v_mfma_f32_16x16x32_bf16 v[112:115], a[240:243], a[172:175], v[112:115]\n"
        "v_mul_f32 v111, v96, v111\n"
        "v_cvt_pk_bf16_f32 v108, v108, v109\n"
        "v_mfma_f32_16x16x32_bf16 v[112:115], a[236:239], a[168:171], v[112:115]\n"
        "v_cvt_pk_bf16_f32 v109, v110, v111\n"
        "v_mul_f32 v120, v96, v120\n"
        "v_mfma_f32_16x16x32_bf16 v[116:119], a[236:239], a[192:195], 0\n"
        "v_mul_f32 v121, v96, v121\n"
        "v_mul_f32 v122, v96, v122\n"
        "v_mfma_f32_16x16x32_bf16 v[116:119], a[240:243], a[196:199], v[116:119]\n"
        "v_mul_f32 v123, v96, v123\n"
        "v_cvt_pk_bf16_f32 v110, v120, v121\n"
        "v_mfma_f32_16x16x32_bf16 v[116:119], a[244:247], a[200:203], v[116:119]\n"
        "v_cvt_pk_bf16_f32 v142, v112, v113\n"
        "v_cvt_pk_bf16_f32 v143, v114, v115\n"
        "v_mfma_f32_16x16x32_bf16 v[116:119], a[248:251], a[204:207], v[116:119]\n"
        "v_cvt_pk_bf16_f32 v111, v122, v123\n"
        "v_mul_f32 v124, v96, v124\n"
        "v_mfma_f32_16x16x32_bf16 v[116:119], a[216:219], a[208:211], v[116:119]\n"
        "ds_write_b128 v245, v[108:111] offset:768\n"
        "v_mfma_f32_16x16x32_bf16 v[116:119], a[220:223], a[212:215], v[116:119]\n"
        "v_mul_f32 v125, v96, v125\n"
        "v_mul_f32 v126, v96, v126\n"
        "v_mfma_f32_16x16x32_bf16 v[100:103], a[20:23], a[220:223], 0\n"
        "v_mul_f32 v127, v96, v127\n"
        "v_cvt_pk_bf16_f32 v124, v124, v125\n"
        "v_mfma_f32_16x16x32_bf16 v[100:103], a[16:19], a[216:219], v[100:103]\n"
        "v_cvt_pk_bf16_f32 v125, v126, v127\n"
        "v_mfma_f32_16x16x32_bf16 v[100:103], a[12:15], a[248:251], v[100:103]\n"
        "v_cvt_pk_bf16_f32 v150, v116, v117\n"
        "v_cvt_pk_bf16_f32 v151, v118, v119\n"
        "v_mfma_f32_16x16x32_bf16 v[100:103], a[8:11], a[244:247], v[100:103]\n"
        "ds_write_b64 v246, v[124:125] offset:768\n"
        "v_mfma_f32_16x16x32_bf16 v[100:103], a[4:7], a[240:243], v[100:103]\n"
        "v_mfma_f32_16x16x32_bf16 v[100:103], a[0:3], a[236:239], v[100:103]\n"
        "v_mfma_f32_16x16x32_bf16 v[112:115], a[24:27], a[236:239], 0\n"
        "v_mfma_f32_16x16x32_bf16 v[112:115], a[28:31], a[240:243], v[112:115]\n"
        "v_mfma_f32_16x16x32_bf16 v[112:115], a[32:35], a[244:247], v[112:115]\n"
        "v_mfma_f32_16x16x32_bf16 v[112:115], a[36:39], a[248:251], v[112:115]\n"
        "v_mfma_f32_16x16x32_bf16 v[112:115], a[40:43], a[216:219], v[112:115]\n"
        "v_mfma_f32_16x16x32_bf16 v[112:115], a[44:47], a[220:223], v[112:115]\n"
        "v_mfma_f32_16x16x32_bf16 v[116:119], a[68:71], a[220:223], 0\n"
        "ds_read_b128 v[162:165], v251 offset:0\n"
        "v_cvt_pk_bf16_f32 v216, v100, v101\n"
        "v_mfma_f32_16x16x32_bf16 v[116:119], a[64:67], a[216:219], v[116:119]\n"
        "v_cvt_pk_bf16_f32 v217, v102, v103\n"
        "v_mfma_f32_16x16x32_bf16 v[116:119], a[60:63], a[248:251], v[116:119]\n"
        "v_mfma_f32_16x16x32_bf16 v[116:119], a[56:59], a[244:247], v[116:119]\n"
        "v_mfma_f32_16x16x32_bf16 v[116:119], a[52:55], a[240:243], v[116:119]\n"
        "ds_read_b128 v[182:185], v251 offset:512\n"
        "v_cvt_pk_bf16_f32 v218, v112, v113\n"
        "v_mfma_f32_16x16x32_bf16 v[116:119], a[48:51], a[236:239], v[116:119]\n"
        "v_cvt_pk_bf16_f32 v219, v114, v115\n"
        "v_mfma_f32_16x16x32_bf16 v[100:103], a[72:75], a[236:239], 0\n"
        "s_add_u32 s47, s47, 6291456\n"
        "v_mfma_f32_16x16x32_bf16 v[100:103], a[76:79], a[240:243], v[100:103]\n"
        "s_add_u32 s36, s36, 2097152\n"
        "v_mfma_f32_16x16x32_bf16 v[100:103], a[80:83], a[244:247], v[100:103]\n"
        "ds_read_b128 v[202:205], v251 offset:4096\n"
        "s_xor_b32 s48, s48, 8192\n"
        "v_cvt_pk_bf16_f32 v220, v116, v117\n"
        "v_mfma_f32_16x16x32_bf16 v[100:103], a[84:87], a[248:251], v[100:103]\n"
        "v_cvt_pk_bf16_f32 v221, v118, v119\n"
        "v_cvt_pk_bf16_f32 v226, v116, v117\n"
        "v_mfma_f32_16x16x32_bf16 v[100:103], a[88:91], a[216:219], v[100:103]\n"
        "v_cvt_pk_bf16_f32 v227, v118, v119\n"
        "v_mfma_f32_16x16x32_bf16 v[100:103], a[92:95], a[220:223], v[100:103]\n"
        "v_mfma_f32_16x16x32_bf16 v[112:115], a[116:119], a[220:223], 0\n"
        "ds_read_b128 v[222:225], v251 offset:4608\n"
        "v_mfma_f32_16x16x32_bf16 v[112:115], a[112:115], a[216:219], v[112:115]\n"
        "v_mfma_f32_16x16x32_bf16 v[112:115], a[108:111], a[248:251], v[112:115]\n"
        "s_waitcnt lgkmcnt(3)\n"
        "v_pk_min_u16 v162, v162, s26\n"
        "v_pk_min_u16 v163, v163, s26\n"
        "v_mfma_f32_16x16x32_bf16 v[112:115], a[104:107], a[244:247], v[112:115]\n"
        "v_pk_min_u16 v164, v164, s26\n"
        "v_cvt_pk_bf16_f32 v212, v100, v101\n"
        "v_mfma_f32_16x16x32_bf16 v[112:115], a[100:103], a[240:243], v[112:115]\n"
        "v_cvt_pk_bf16_f32 v213, v102, v103\n"
        "v_pk_min_u16 v165, v165, s26\n"
        "v_mfma_f32_16x16x32_bf16 v[112:115], a[96:99], a[236:239], v[112:115]\n"
        "v_add_u32 v240, s31, v240\n"
        "v_add_u32 v241, s31, v241\n"
        "v_mfma_f32_16x16x32_bf16 v[116:119], a[120:123], a[236:239], 0\n"
        "s_waitcnt lgkmcnt(2)\n"
        "v_pk_min_u16 v182, v182, s26\n"
        "v_pk_min_u16 v183, v183, s26\n"
        "v_mfma_f32_16x16x32_bf16 v[116:119], a[124:127], a[240:243], v[116:119]\n"
        "v_pk_min_u16 v184, v184, s26\n"
        "v_pk_min_u16 v185, v185, s26\n"
        "v_mfma_f32_16x16x32_bf16 v[116:119], a[128:131], a[244:247], v[116:119]\n"
        "v_cvt_pk_bf16_f32 v214, v112, v113\n"
        "v_cvt_pk_bf16_f32 v215, v114, v115\n"
        "v_mfma_f32_16x16x32_bf16 v[116:119], a[132:135], a[248:251], v[116:119]\n"
        "v_mfma_f32_16x16x32_bf16 v[116:119], a[136:139], a[216:219], v[116:119]\n"
        "s_waitcnt lgkmcnt(1)\n"
        "v_pk_min_u16 v202, v202, s26\n"
        "v_pk_min_u16 v203, v203, s26\n"
        "v_mfma_f32_16x16x32_bf16 v[116:119], a[140:143], a[220:223], v[116:119]\n"
        "v_pk_min_u16 v204, v204, s26\n"
        "v_pk_min_u16 v205, v205, s26\n"
        "v_mfma_f32_16x16x32_bf16 v[64:67], v[152:155], v[156:159], v[0:3]\n"
        "v_mfma_f32_16x16x32_bf16 v[64:67], v[168:171], v[160:163], v[64:67]\n"
        "v_mfma_f32_16x16x32_bf16 v[68:71], v[172:175], v[156:159], v[4:7]\n"
        "s_waitcnt lgkmcnt(0)\n"
        "v_pk_min_u16 v222, v222, s26\n"
        "v_pk_min_u16 v223, v223, s26\n"
        "v_mfma_f32_16x16x32_bf16 v[68:71], v[188:191], v[164:167], v[68:71]\n"
        "ds_read_b128 v[162:165], v252 offset:0\n"
        "v_cvt_pk_bf16_f32 v230, v116, v117\n"
        "v_mfma_f32_16x16x32_bf16 v[80:83], v[152:155], v[176:179], v[16:19]\n"
        "v_cvt_pk_bf16_f32 v231, v118, v119\n"
        "v_pk_min_u16 v224, v224, s26\n"
        "v_mfma_f32_16x16x32_bf16 v[80:83], v[168:171], v[180:183], v[80:83]\n"
        "v_pk_min_u16 v225, v225, s26\n"
        "v_max3_f32 v232, v64, v65, v66\n"
        "v_mfma_f32_16x16x32_bf16 v[84:87], v[172:175], v[176:179], v[20:23]\n"
        "v_max3_f32 v232, v232, v67, v68\n"
        "v_max3_f32 v232, v232, v69, v70\n"
        "v_mfma_f32_16x16x32_bf16 v[84:87], v[188:191], v[184:187], v[84:87]\n"
        "ds_read_b128 v[182:185], v252 offset:512\n"
        "v_mfma_f32_16x16x32_bf16 v[96:99], v[152:155], v[196:199], v[32:35]\n"
        "v_max3_f32 v233, v80, v81, v82\n"
        "v_mfma_f32_16x16x32_bf16 v[96:99], v[168:171], v[200:203], v[96:99]\n"
        "v_mfma_f32_16x16x32_bf16 v[100:103], v[172:175], v[196:199], v[36:39]\n"
        "v_mfma_f32_16x16x32_bf16 v[100:103], v[188:191], v[204:207], v[100:103]\n"
        "ds_read_b128 v[202:205], v252 offset:4096\n"
        "v_mfma_f32_16x16x32_bf16 v[112:115], v[152:155], v[216:219], v[48:51]\n"
        "s_waitcnt lgkmcnt(2)\n"
        "v_pk_min_u16 v162, v162, s26\n"
        "v_pk_min_u16 v163, v163, s26\n"
        "v_mfma_f32_16x16x32_bf16 v[112:115], v[168:171], v[220:223], v[112:115]\n"
        "v_pk_min_u16 v164, v164, s26\n"
        "v_pk_min_u16 v165, v165, s26\n"
        "v_mfma_f32_16x16x32_bf16 v[116:119], v[172:175], v[216:219], v[52:55]\n"
        "v_max3_f32 v233, v233, v83, v84\n"
        "v_max3_f32 v233, v233, v85, v86\n"
        "v_mfma_f32_16x16x32_bf16 v[116:119], v[188:191], v[224:227], v[116:119]\n"
        "ds_read_b128 v[222:225], v252 offset:4608\n"
        "v_max3_f32 v234, v96, v97, v98\n"
        "v_mfma_f32_16x16x32_bf16 v[72:75], v[192:195], v[156:159], v[8:11]\n"
        "s_waitcnt lgkmcnt(2)\n"
        "v_pk_min_u16 v182, v182, s26\n"
        "v_pk_min_u16 v183, v183, s26\n"
        "v_mfma_f32_16x16x32_bf16 v[72:75], v[208:211], v[160:163], v[72:75]\n"
        "v_pk_min_u16 v184, v184, s26\n"
        "v_pk_min_u16 v185, v185, s26\n"
        "v_mfma_f32_16x16x32_bf16 v[76:79], v[212:215], v[156:159], v[12:15]\n"
        "v_max3_f32 v234, v234, v99, v100\n"
        "v_max3_f32 v234, v234, v101, v102\n"
        "v_mfma_f32_16x16x32_bf16 v[76:79], v[228:231], v[164:167], v[76:79]\n"
        "v_max3_f32 v235, v112, v113, v114\n"
        "v_max3_f32 v235, v235, v115, v116\n"
        "v_mfma_f32_16x16x32_bf16 v[88:91], v[192:195], v[176:179], v[24:27]\n"
        "s_barrier\n"
        "ds_read_b128 v[152:155], v247 offset:0\n"
        "s_waitcnt lgkmcnt(2)\n"
        "v_pk_min_u16 v202, v202, s26\n"
        "v_mfma_f32_16x16x32_bf16 v[88:91], v[208:211], v[180:183], v[88:91]\n"
        "ds_read_b128 v[160:163], v247 offset:4096\n"
        "v_pk_min_u16 v203, v203, s26\n"
        "v_mfma_f32_16x16x32_bf16 v[92:95], v[212:215], v[176:179], v[28:31]\n"
        "ds_read_b128 v[156:159], v247 offset:256\n"
        "v_pk_min_u16 v204, v204, s26\n"
        "v_mfma_f32_16x16x32_bf16 v[92:95], v[228:231], v[184:187], v[92:95]\n"
        "ds_read_b128 v[164:167], v247 offset:4352\n"
        "v_pk_min_u16 v205, v205, s26\n"
        "v_mfma_f32_16x16x32_bf16 v[104:107], v[192:195], v[196:199], v[40:43]\n"
        "ds_read_b128 a[216:219], v248 offset:0\n"
        "s_waitcnt lgkmcnt(5)\n"
        "v_pk_min_u16 v222, v222, s26\n"
        "v_mfma_f32_16x16x32_bf16 v[104:107], v[208:211], v[200:203], v[104:107]\n"
        "ds_read_b128 a[220:223], v248 offset:6144\n"
        "v_pk_min_u16 v223, v223, s26\n"
        "v_mfma_f32_16x16x32_bf16 v[108:111], v[212:215], v[196:199], v[44:47]\n"
        "ds_read_b128 a[224:227], v254 offset:16\n"
        "v_pk_min_u16 v224, v224, s26\n"
        "v_mfma_f32_16x16x32_bf16 v[108:111], v[228:231], v[204:207], v[108:111]\n"
        "ds_read_b128 a[228:231], v254 offset:6160\n"
        "v_pk_min_u16 v225, v225, s26\n"
        "v_mfma_f32_16x16x32_bf16 v[120:123], v[192:195], v[216:219], v[56:59]\n"
        "ds_read_b128 a[232:235], v248 offset:32\n"
        "v_max3_f32 v232, v232, v71, v72\n"
        "v_mfma_f32_16x16x32_bf16 v[120:123], v[208:211], v[220:223], v[120:123]\n"
        "v_max3_f32 v232, v232, v73, v74\n"
        "v_max3_f32 v232, v232, v75, v76\n"
        "v_mfma_f32_16x16x32_bf16 v[124:127], v[212:215], v[216:219], v[60:63]\n"
        "v_max3_f32 v232, v232, v77, v78\n"
        "v_max_f32 v232, v232, v79\n"
        "v_mfma_f32_16x16x32_bf16 v[124:127], v[228:231], v[224:227], v[124:127]\n"
        "s_waitcnt lgkmcnt(4)\n"
        "v_max3_f32 v233, v233, v87, v88\n"
        "v_max3_f32 v233, v233, v89, v90\n"
        "v_mfma_f32_16x16x32_bf16 v[172:175], v[152:155], a[216:219], 0\n"
        "s_waitcnt lgkmcnt(3)\n"
        "v_max3_f32 v233, v233, v91, v92\n"
        "v_max3_f32 v233, v233, v93, v94\n"
        "v_mfma_f32_16x16x32_bf16 v[172:175], v[160:163], a[220:223], v[172:175]\n"
        "v_max_f32 v233, v233, v95\n"
        "v_max3_f32 v234, v234, v103, v104\n"
        "v_mfma_f32_16x16x32_bf16 v[180:183], v[164:167], a[220:223], 0\n"
        "ds_read_b128 a[220:223], v248 offset:6176\n"
        "v_max3_f32 v234, v234, v105, v106\n"
        "v_mfma_f32_16x16x32_bf16 v[180:183], v[156:159], a[216:219], v[180:183]\n"
        "s_waitcnt lgkmcnt(3)\n"
        "ds_read_b128 a[216:219], v254 offset:48\n"
        "v_max3_f32 v234, v234, v107, v108\n"
        "v_mfma_f32_16x16x32_bf16 v[184:187], v[156:159], a[224:227], 0\n"
        "s_waitcnt lgkmcnt(3)\n"
        "ds_read_b128 a[236:239], v247 offset:8192\n"
        "v_max3_f32 v234, v234, v109, v110\n"
        "v_mfma_f32_16x16x32_bf16 v[184:187], v[164:167], a[228:231], v[184:187]\n"
        "ds_read_b128 a[244:247], v247 offset:12288\n"
        "v_max_f32 v234, v234, v111\n"
        "v_mfma_f32_16x16x32_bf16 v[176:179], v[160:163], a[228:231], 0\n"
        "ds_read_b128 a[228:231], v254 offset:6192\n"
        "v_max3_f32 v235, v235, v117, v118\n"
        "v_mfma_f32_16x16x32_bf16 v[176:179], v[152:155], a[224:227], v[176:179]\n"
        "s_waitcnt lgkmcnt(5)\n"
        "ds_read_b128 a[224:227], v248 offset:64\n"
        "v_max3_f32 v235, v235, v119, v120\n"
        "v_mfma_f32_16x16x32_bf16 v[212:215], v[152:155], a[232:235], 0\n"
        "s_waitcnt lgkmcnt(5)\n"
        "ds_read_b128 a[240:243], v247 offset:8448\n"
        "v_max3_f32 v235, v235, v121, v122\n"
        "v_mfma_f32_16x16x32_bf16 v[212:215], v[160:163], a[220:223], v[212:215]\n"
        "ds_read_b128 a[248:251], v247 offset:12544\n"
        "v_max3_f32 v235, v235, v123, v124\n"
        "v_mfma_f32_16x16x32_bf16 v[192:195], v[164:167], a[220:223], 0\n"
        "ds_read_b128 a[220:223], v248 offset:6208\n"
        "v_max3_f32 v235, v235, v125, v126\n"
        "v_mfma_f32_16x16x32_bf16 v[192:195], v[156:159], a[232:235], v[192:195]\n"
        "s_waitcnt lgkmcnt(7)\n"
        "ds_read_b128 a[232:235], v254 offset:80\n"
        "v_max_f32 v235, v235, v127\n"
        "v_mfma_f32_16x16x32_bf16 v[196:199], v[156:159], a[216:219], 0\n"
        "s_waitcnt lgkmcnt(5)\n"
        "v_permlane32_swap_b32 v232, v233\n"
        "v_mfma_f32_16x16x32_bf16 v[196:199], v[164:167], a[228:231], v[196:199]\n"
        "v_max_f32 v232, v232, v233\n"
        "v_mov_b32 v233, v232\n"
        "v_mfma_f32_16x16x32_bf16 v[216:219], v[160:163], a[228:231], 0\n"
        "ds_read_b128 a[228:231], v254 offset:6224\n"
        "v_xor_b32 v251, 0x2000, v251\n"
        "v_mfma_f32_16x16x32_bf16 v[216:219], v[152:155], a[216:219], v[216:219]\n"
        "s_waitcnt lgkmcnt(5)\n"
        "ds_read_b128 a[216:219], v248 offset:36864\n"
        "v_xor_b32 v252, 0x2000, v252\n"
        "v_mfma_f32_16x16x32_bf16 v[220:223], v[152:155], a[224:227], 0\n"
        "s_waitcnt lgkmcnt(3)\n"
        "v_permlane16_swap_b32 v232, v233\n"
        "v_mfma_f32_16x16x32_bf16 v[220:223], v[160:163], a[220:223], v[220:223]\n"
        "v_max_f32 v232, v232, v233\n"
        "v_mul_f32 v236, s25, v232\n"
        "v_mfma_f32_16x16x32_bf16 v[200:203], v[164:167], a[220:223], 0\n"
        "ds_read_b128 a[220:223], v248 offset:43008\n"
        "v_mul_f32 v237, s25, v232\n"
        "v_mfma_f32_16x16x32_bf16 v[200:203], v[156:159], a[224:227], v[200:203]\n"
        "s_waitcnt lgkmcnt(3)\n"
        "ds_read_b128 a[224:227], v254 offset:36880\n"
        "v_add_u32 v245, s45, v245\n"
        "v_mfma_f32_16x16x32_bf16 v[204:207], v[156:159], a[232:235], 0\n"
        "s_waitcnt lgkmcnt(3)\n"
        "v_permlane32_swap_b32 v236, v237\n"
        "v_mfma_f32_16x16x32_bf16 v[204:207], v[164:167], a[228:231], v[204:207]\n"
        "v_permlane32_swap_b32 v234, v235\n"
        "v_mfma_f32_16x16x32_bf16 v[224:227], v[160:163], a[228:231], 0\n"
        "ds_read_b128 a[228:231], v254 offset:43024\n"
        "v_max_f32 v234, v234, v235\n"
        "v_mfma_f32_16x16x32_bf16 v[224:227], v[152:155], a[232:235], v[224:227]\n"
        "s_waitcnt lgkmcnt(3)\n"
        "ds_read_b128 a[232:235], v248 offset:36896\n"
        "v_mov_b32 v235, v234\n"
        "v_mfma_f32_16x16x32_bf16 v[172:175], a[236:239], a[216:219], v[172:175]\n"
        "s_waitcnt lgkmcnt(3)\n"
        "v_fma_f32 v64, v64, s24, v236\n"
        "v_fma_f32 v65, v65, s24, v236\n"
        "v_mfma_f32_16x16x32_bf16 v[172:175], a[244:247], a[220:223], v[172:175]\n"
        "v_permlane16_swap_b32 v234, v235\n"
        "v_mfma_f32_16x16x32_bf16 v[180:183], a[248:251], a[220:223], v[180:183]\n"
        "ds_read_b128 a[220:223], v248 offset:43040\n"
        "v_max_f32 v234, v234, v235\n"
        "v_mfma_f32_16x16x32_bf16 v[180:183], a[240:243], a[216:219], v[180:183]\n"
        "s_waitcnt lgkmcnt(3)\n"
        "ds_read_b128 a[216:219], v254 offset:36912\n"
        "v_mul_f32 v238, s25, v234\n"
        "v_mfma_f32_16x16x32_bf16 v[184:187], a[240:243], a[224:227], v[184:187]\n"
        "s_waitcnt lgkmcnt(3)\n"
        "ds_read_b128 v[152:155], v247 offset:16384\n"
        "v_mul_f32 v239, s25, v234\n"
        "v_mfma_f32_16x16x32_bf16 v[184:187], a[248:251], a[228:231], v[184:187]\n"
        "ds_read_b128 v[160:163], v247 offset:20480\n"
        "v_fma_f32 v66, v66, s24, v236\n"
        "v_mfma_f32_16x16x32_bf16 v[176:179], a[244:247], a[228:231], v[176:179]\n"
        "ds_read_b128 a[228:231], v254 offset:43056\n"
        "v_fma_f32 v67, v67, s24, v236\n"
        "v_mfma_f32_16x16x32_bf16 v[176:179], a[236:239], a[224:227], v[176:179]\n"
        "s_waitcnt lgkmcnt(5)\n"
        "ds_read_b128 a[224:227], v248 offset:36928\n"
        "v_fma_f32 v68, v68, s24, v236\n"
        "v_mfma_f32_16x16x32_bf16 v[212:215], a[236:239], a[232:235], v[212:215]\n"
        "s_waitcnt lgkmcnt(5)\n"
        "ds_read_b128 v[156:159], v247 offset:16640\n"
        "v_fma_f32 v69, v69, s24, v236\n"
        "v_mfma_f32_16x16x32_bf16 v[212:215], a[244:247], a[220:223], v[212:215]\n"
        "ds_read_b128 v[164:167], v247 offset:20736\n"
        "v_fma_f32 v70, v70, s24, v236\n"
        "v_mfma_f32_16x16x32_bf16 v[192:195], a[248:251], a[220:223], v[192:195]\n"
        "ds_read_b128 a[220:223], v248 offset:43072\n"
        "v_fma_f32 v71, v71, s24, v236\n"
        "v_mfma_f32_16x16x32_bf16 v[192:195], a[240:243], a[232:235], v[192:195]\n"
        "s_waitcnt lgkmcnt(7)\n"
        "ds_read_b128 a[232:235], v254 offset:36944\n"
        "v_fma_f32 v72, v72, s24, v236\n"
        "v_mfma_f32_16x16x32_bf16 v[196:199], a[240:243], a[216:219], v[196:199]\n"
        "s_waitcnt lgkmcnt(5)\n"
        "v_permlane32_swap_b32 v238, v239\n"
        "v_mfma_f32_16x16x32_bf16 v[196:199], a[248:251], a[228:231], v[196:199]\n"
        "v_exp_f32 v64, v64\n"
        "v_mfma_f32_16x16x32_bf16 v[216:219], a[244:247], a[228:231], v[216:219]\n"
        "ds_read_b128 a[228:231], v254 offset:43088\n"
        "v_fma_f32 v73, v73, s24, v236\n"
        "v_mfma_f32_16x16x32_bf16 v[216:219], a[236:239], a[216:219], v[216:219]\n"
        "s_waitcnt lgkmcnt(5)\n"
        "ds_read_b128 a[216:219], v248 offset:12288\n"
        "v_fma_f32 v74, v74, s24, v236\n"
        "v_mfma_f32_16x16x32_bf16 v[220:223], a[236:239], a[224:227], v[220:223]\n"
        "s_waitcnt lgkmcnt(3)\n"
        "v_exp_f32 v65, v65\n"
        "v_mfma_f32_16x16x32_bf16 v[220:223], a[244:247], a[220:223], v[220:223]\n"
        "v_exp_f32 v66, v66\n"
        "v_mfma_f32_16x16x32_bf16 v[200:203], a[248:251], a[220:223], v[200:203]\n"
        "ds_read_b128 a[220:223], v248 offset:49152\n"
        "v_cvt_pk_bf16_f32 v64, v64, v65\n"
        "v_mfma_f32_16x16x32_bf16 v[200:203], a[240:243], a[224:227], v[200:203]\n"
        "s_waitcnt lgkmcnt(3)\n"
        "ds_read_b128 a[224:227], v254 offset:12304\n"
        "v_fma_f32 v75, v75, s24, v236\n"
        "v_mfma_f32_16x16x32_bf16 v[204:207], a[240:243], a[232:235], v[204:207]\n"
        "s_waitcnt lgkmcnt(3)\n"
        "v_exp_f32 v67, v67\n"
        "v_mfma_f32_16x16x32_bf16 v[204:207], a[248:251], a[228:231], v[204:207]\n"
        "v_exp_f32 v68, v68\n"
        "v_mfma_f32_16x16x32_bf16 v[224:227], a[244:247], a[228:231], v[224:227]\n"
        "ds_read_b128 a[228:231], v254 offset:49168\n"
        "v_cvt_pk_bf16_f32 v65, v66, v67\n"
        "v_mfma_f32_16x16x32_bf16 v[224:227], a[236:239], a[232:235], v[224:227]\n"
        "s_waitcnt lgkmcnt(3)\n"
        "ds_read_b128 a[232:235], v248 offset:12320\n"
        "v_add_u32 v246, s45, v246\n"
        "v_mfma_f32_16x16x32_bf16 v[172:175], v[152:155], a[216:219], v[172:175]\n"
        "s_waitcnt lgkmcnt(3)\n"
        "v_exp_f32 v69, v69\n"
        "v_mfma_f32_16x16x32_bf16 v[172:175], v[160:163], a[220:223], v[172:175]\n"
        "v_cvt_pk_bf16_f32 v66, v68, v69\n"
        "v_add_u32 v247, s45, v247\n"
        "v_mfma_f32_16x16x32_bf16 v[180:183], v[164:167], a[220:223], v[180:183]\n"
        "ds_read_b128 a[220:223], v248 offset:49184\n"
        "v_mfma_f32_16x16x32_bf16 v[180:183], v[156:159], a[216:219], v[180:183]\n"
        "s_waitcnt lgkmcnt(3)\n"
        "ds_read_b128 a[216:219], v254 offset:12336\n"
        "v_mfma_f32_16x16x32_bf16 v[184:187], v[156:159], a[224:227], v[184:187]\n"
        "s_waitcnt lgkmcnt(3)\n"
        "v_exp_f32 v70, v70\n"
        "v_mfma_f32_16x16x32_bf16 v[184:187], v[164:167], a[228:231], v[184:187]\n"
        "v_exp_f32 v71, v71\n"
        "v_mfma_f32_16x16x32_bf16 v[176:179], v[160:163], a[228:231], v[176:179]\n"
        "ds_read_b128 a[228:231], v254 offset:49200\n"
        "v_cvt_pk_bf16_f32 v67, v70, v71\n"
        "v_mfma_f32_16x16x32_bf16 v[176:179], v[152:155], a[224:227], v[176:179]\n"
        "s_waitcnt lgkmcnt(3)\n"
        "ds_read_b128 a[224:227], v248 offset:12352\n"
        "v_mfma_f32_16x16x32_bf16 v[212:215], v[152:155], a[232:235], v[212:215]\n"
        "s_waitcnt lgkmcnt(3)\n"
        "s_waitcnt vmcnt(0)\n"
        "s_barrier\n"
        "v_exp_f32 v72, v72\n"
        "v_mfma_f32_16x16x32_bf16 v[212:215], v[160:163], a[220:223], v[212:215]\n"
        "v_exp_f32 v73, v73\n"
        "v_mfma_f32_16x16x32_bf16 v[192:195], v[164:167], a[220:223], v[192:195]\n"
        "ds_read_b128 a[220:223], v248 offset:49216\n"
        "v_cvt_pk_bf16_f32 v68, v72, v73\n"
        "v_mfma_f32_16x16x32_bf16 v[192:195], v[156:159], a[232:235], v[192:195]\n"
        "s_waitcnt lgkmcnt(3)\n"
        "ds_read_b128 a[232:235], v254 offset:12368\n"
        "v_mfma_f32_16x16x32_bf16 v[196:199], v[156:159], a[216:219], v[196:199]\n"
        "s_waitcnt lgkmcnt(3)\n"
        "ds_read_b128 a[236:239], v240 offset:0\n"
        "v_mfma_f32_16x16x32_bf16 v[196:199], v[164:167], a[228:231], v[196:199]\n"
        "ds_read_b128 a[240:243], v241 offset:0\n"
        "v_mfma_f32_16x16x32_bf16 v[216:219], v[160:163], a[228:231], v[216:219]\n"
        "ds_read_b128 a[228:231], v254 offset:49232\n"
        "v_mfma_f32_16x16x32_bf16 v[216:219], v[152:155], a[216:219], v[216:219]\n"
        "s_waitcnt lgkmcnt(5)\n"
        "ds_read_b128 a[244:247], v240 offset:128\n"
        "v_mfma_f32_16x16x32_bf16 v[220:223], v[152:155], a[224:227], v[220:223]\n"
        "s_waitcnt lgkmcnt(5)\n"
        "ds_read_b128 a[248:251], v241 offset:128\n"
        "v_mfma_f32_16x16x32_bf16 v[220:223], v[160:163], a[220:223], v[220:223]\n"
        "ds_read_b128 a[216:219], v240 offset:256\n"
        "v_mfma_f32_16x16x32_bf16 v[200:203], v[164:167], a[220:223], v[200:203]\n"
        "ds_read_b128 a[220:223], v241 offset:256\n"
        "v_mfma_f32_16x16x32_bf16 v[200:203], v[156:159], a[224:227], v[200:203]\n"
        "s_waitcnt lgkmcnt(7)\n"
        "v_exp_f32 v74, v74\n"
        "v_mfma_f32_16x16x32_bf16 v[204:207], v[156:159], a[232:235], v[204:207]\n"
        "s_waitcnt lgkmcnt(4)\n"
        "v_exp_f32 v75, v75\n"
        "v_mfma_f32_16x16x32_bf16 v[204:207], v[164:167], a[228:231], v[204:207]\n"
        "s_sub_u32 s27, s27, 1\n"
        "v_cvt_pk_bf16_f32 v69, v74, v75\n"
        "v_mfma_f32_16x16x32_bf16 v[224:227], v[160:163], a[228:231], v[224:227]\n"
        "s_cmp_lg_u32 s27, 0\n"
        "v_mfma_f32_16x16x32_bf16 v[224:227], v[152:155], a[232:235], v[224:227]\n"
        "s_cbranch_scc1 10b\n"
        "20:\n"
        "s_waitcnt lgkmcnt(0)\n"
        "s_nop 7\n"
        "v_mfma_f32_16x16x32_bf16 v[48:51], a[252:255], v[64:67], 0\n"
        "s_and_b32 s46, s42, 1\n"
        "s_lshl_b32 s31, s46, 8\n"
        "s_mul_i32 s45, s46, 786432\n"
        "s_sub_u32 s37, s37, s45\n"
        "s_cmp_eq_u32 s46, 0\n"
        "s_cselect_b32 s43, s43, s44\n"
        "s_add_u32 s28, s37, 786432\n"
        "s_cmp_eq_u32 s28, 100663296\n"
        "s_cselect_b32 s28, 0, s28\n"
        "v_fma_f32 v76, v76, s24, v236\n"
        "v_exp_f32 v76, v76\n"
        "v_fma_f32 v77, v77, s24, v236\n"
        "v_exp_f32 v77, v77\n"
        "v_fma_f32 v78, v78, s24, v236\n"
        "v_cvt_pk_bf16_f32 v70, v76, v77\n"
        "v_exp_f32 v78, v78\n"
        "v_fma_f32 v79, v79, s24, v236\n"
        "v_exp_f32 v79, v79\n"
        "v_subrev_u32 v247, s31, v247\n"
        "v_cvt_pk_bf16_f32 v71, v78, v79\n"
        "s_nop 1\n"
        "v_mfma_f32_16x16x32_bf16 v[48:51], a[252:255], v[68:71], v[48:51]\n"
        "v_cvt_pk_bf16_f32 v232, v172, v176\n"
        "v_cvt_pk_bf16_f32 v233, v212, v216\n"
        "v_cvt_pk_bf16_f32 v234, v220, v224\n"
        "v_fma_f32 v80, v80, s24, v237\n"
        "v_exp_f32 v80, v80\n"
        "v_mfma_f32_16x16x32_bf16 v[52:55], v[132:135], v[68:71], 0\n"
        "buffer_store_dwordx3 v[232:234], v250, s[16:19], s38 offen offset:0 sc1\n"
        "ds_read_b128 a[216:219], v248 offset:0\n"
        "v_mfma_f32_16x16x32_bf16 v[52:55], v[128:131], v[64:67], v[52:55]\n"
        "ds_read_b128 a[220:223], v248 offset:6144\n"
        "v_cvt_pk_bf16_f32 v232, v173, v177\n"
        "v_cvt_pk_bf16_f32 v233, v213, v217\n"
        "v_cvt_pk_bf16_f32 v234, v221, v225\n"
        "v_fma_f32 v81, v81, s24, v237\n"
        "v_fma_f32 v82, v82, s24, v237\n"
        "v_mfma_f32_16x16x32_bf16 v[56:59], v[136:139], v[64:67], 0\n"
        "buffer_store_dwordx3 v[232:234], v250, s[16:19], s38 offen offset:384 sc1\n"
        "ds_read_b128 a[224:227], v248 offset:36864\n"
        "v_mfma_f32_16x16x32_bf16 v[56:59], v[140:143], v[68:71], v[56:59]\n"
        "ds_read_b128 a[228:231], v248 offset:43008\n"
        "v_cvt_pk_bf16_f32 v232, v174, v178\n"
        "v_cvt_pk_bf16_f32 v233, v214, v218\n"
        "v_cvt_pk_bf16_f32 v234, v222, v226\n"
        "v_exp_f32 v81, v81\n"
        "v_mfma_f32_16x16x32_bf16 v[60:63], v[148:151], v[68:71], 0\n"
        "buffer_store_dwordx3 v[232:234], v250, s[16:19], s38 offen offset:768 sc1\n"
        "ds_read_b128 a[232:235], v248 offset:12288\n"
        "v_mfma_f32_16x16x32_bf16 v[60:63], v[144:147], v[64:67], v[60:63]\n"
        "ds_read_b128 a[236:239], v248 offset:49152\n"
        "v_cvt_pk_bf16_f32 v232, v175, v179\n"
        "v_cvt_pk_bf16_f32 v233, v215, v219\n"
        "v_cvt_pk_bf16_f32 v234, v223, v227\n"
        "v_cvt_pk_bf16_f32 v80, v80, v81\n"
        "v_fma_f32 v83, v83, s24, v237\n"
        "v_exp_f32 v82, v82\n"
        "v_exp_f32 v83, v83\n"
        "v_fma_f32 v84, v84, s24, v237\n"
        "v_cvt_pk_bf16_f32 v81, v82, v83\n"
        "v_exp_f32 v84, v84\n"
        "v_fma_f32 v85, v85, s24, v237\n"
        "v_exp_f32 v85, v85\n"
        "v_fma_f32 v86, v86, s24, v237\n"
        "v_cvt_pk_bf16_f32 v82, v84, v85\n"
        "v_exp_f32 v86, v86\n"
        "v_fma_f32 v87, v87, s24, v237\n"
        "v_exp_f32 v87, v87\n"
        "v_fma_f32 v88, v88, s24, v237\n"
        "v_cvt_pk_bf16_f32 v83, v86, v87\n"
        "s_nop 1\n"
        "v_mfma_f32_16x16x32_bf16 v[168:171], a[252:255], v[80:83], 0\n"
        "buffer_store_dwordx3 v[232:234], v250, s[16:19], s38 offen offset:1152 sc1\n"
        "ds_read_b128 a[240:243], v254 offset:16\n"
        "v_exp_f32 v88, v88\n"
        "v_fma_f32 v89, v89, s24, v237\n"
        "v_exp_f32 v89, v89\n"
        "v_fma_f32 v90, v90, s24, v237\n"
        "v_cvt_pk_bf16_f32 v84, v88, v89\n"
        "v_exp_f32 v90, v90\n"
        "v_fma_f32 v91, v91, s24, v237\n"
        "v_exp_f32 v91, v91\n"
        "v_fma_f32 v92, v92, s24, v237\n"
        "v_cvt_pk_bf16_f32 v85, v90, v91\n"
        "v_exp_f32 v92, v92\n"
        "v_fma_f32 v93, v93, s24, v237\n"
        "v_exp_f32 v93, v93\n"
        "v_fma_f32 v94, v94, s24, v237\n"
        "v_cvt_pk_bf16_f32 v86, v92, v93\n"
        "v_exp_f32 v94, v94\n"
        "v_fma_f32 v95, v95, s24, v237\n"
        "v_exp_f32 v95, v95\n"
        "v_cvt_pk_bf16_f32 v232, v180, v184\n"
        "v_cvt_pk_bf16_f32 v87, v94, v95\n"
        "s_nop 1\n"
        "v_mfma_f32_16x16x32_bf16 v[168:171], a[252:255], v[84:87], v[168:171]\n"
        "ds_read_b128 a[244:247], v254 offset:6160\n"
        "v_cvt_pk_bf16_f32 v233, v192, v196\n"
        "v_cvt_pk_bf16_f32 v234, v200, v204\n"
        "v_rcp_f32 v48, v48\n"
        "v_fma_f32 v96, v96, s24, v238\n"
        "v_mfma_f32_16x16x32_bf16 v[188:191], v[132:135], v[84:87], 0\n"
        "buffer_store_dwordx3 v[232:234], v250, s[16:19], s39 offen offset:0 sc1\n"
        "ds_read_b128 a[248:251], v254 offset:36880\n"
        "v_mfma_f32_16x16x32_bf16 v[188:191], v[128:131], v[80:83], v[188:191]\n"
        "v_cvt_pk_bf16_f32 v232, v181, v185\n"
        "v_cvt_pk_bf16_f32 v233, v193, v197\n"
        "v_cvt_pk_bf16_f32 v234, v201, v205\n"
        "v_mul_f32 v52, v48, v52\n"
        "v_mul_f32 v53, v48, v53\n"
        "v_mul_f32 v54, v48, v54\n"
        "v_mfma_f32_16x16x32_bf16 v[208:211], v[136:139], v[80:83], 0\n"
        "buffer_store_dwordx3 v[232:234], v250, s[16:19], s39 offen offset:384 sc1\n"
        "v_mul_f32 v55, v48, v55\n"
        "v_mfma_f32_16x16x32_bf16 v[208:211], v[140:143], v[84:87], v[208:211]\n"
        "v_cvt_pk_bf16_f32 v232, v182, v186\n"
        "v_cvt_pk_bf16_f32 v233, v194, v198\n"
        "v_cvt_pk_bf16_f32 v234, v202, v206\n"
        "v_cvt_pk_bf16_f32 v52, v52, v53\n"
        "v_cvt_pk_bf16_f32 v53, v54, v55\n"
        "v_mul_f32 v56, v48, v56\n"
        "v_mfma_f32_16x16x32_bf16 v[228:231], v[148:151], v[84:87], 0\n"
        "buffer_store_dwordx3 v[232:234], v250, s[16:19], s39 offen offset:768 sc1\n"
        "v_mul_f32 v57, v48, v57\n"
        "v_mfma_f32_16x16x32_bf16 v[228:231], v[144:147], v[80:83], v[228:231]\n"
        "v_cvt_pk_bf16_f32 v232, v183, v187\n"
        "v_cvt_pk_bf16_f32 v233, v195, v199\n"
        "v_cvt_pk_bf16_f32 v234, v203, v207\n"
        "v_mul_f32 v58, v48, v58\n"
        "v_mul_f32 v59, v48, v59\n"
        "v_cvt_pk_bf16_f32 v54, v56, v57\n"
        "v_cvt_pk_bf16_f32 v55, v58, v59\n"
        "v_mul_f32 v60, v48, v60\n"
        "v_mul_f32 v61, v48, v61\n"
        "v_mul_f32 v62, v48, v62\n"
        "v_mul_f32 v63, v48, v63\n"
        "v_cvt_pk_bf16_f32 v60, v60, v61\n"
        "v_cvt_pk_bf16_f32 v61, v62, v63\n"
        "ds_write_b128 v245, v[52:55] offset:0\n"
        "ds_write_b64 v246, v[60:61] offset:0\n"
        "v_rcp_f32 v168, v168\n"
        "v_exp_f32 v96, v96\n"
        "v_mul_f32 v188, v168, v188\n"
        "v_mul_f32 v189, v168, v189\n"
        "v_mul_f32 v190, v168, v190\n"
        "v_mul_f32 v191, v168, v191\n"
        "v_cvt_pk_bf16_f32 v188, v188, v189\n"
        "v_cvt_pk_bf16_f32 v189, v190, v191\n"
        "v_mul_f32 v208, v168, v208\n"
        "v_mul_f32 v209, v168, v209\n"
        "v_mul_f32 v210, v168, v210\n"
        "v_mul_f32 v211, v168, v211\n"
        "v_cvt_pk_bf16_f32 v190, v208, v209\n"
        "v_cvt_pk_bf16_f32 v191, v210, v211\n"
        "v_mul_f32 v228, v168, v228\n"
        "v_mul_f32 v229, v168, v229\n"
        "v_mul_f32 v230, v168, v230\n"
        "v_mul_f32 v231, v168, v231\n"
        "v_cvt_pk_bf16_f32 v228, v228, v229\n"
        "v_cvt_pk_bf16_f32 v229, v230, v231\n"
        "ds_write_b128 v245, v[188:191] offset:256\n"
        "ds_write_b64 v246, v[228:229] offset:256\n"
        "s_waitcnt lgkmcnt(0)\n"
        "s_barrier\n"
        "ds_read_b128 v[152:155], v247 offset:0\n"
        "s_waitcnt lgkmcnt(0)\n"
        "v_mfma_f32_16x16x32_bf16 v[0:3], v[152:155], a[216:219], 0\n"
        "buffer_store_dwordx3 v[232:234], v250, s[16:19], s39 offen offset:1152 sc1\n"
        "ds_read_b128 v[156:159], v247 offset:4096\n"
        "s_waitcnt lgkmcnt(0)\n"
        "v_mfma_f32_16x16x32_bf16 v[0:3], v[156:159], a[220:223], v[0:3]\n"
        "ds_read_b128 v[172:175], v247 offset:8192\n"
        "ds_read_b128 v[176:179], v247 offset:12288\n"
        "ds_read_b128 v[212:215], v247 offset:16384\n"
        "ds_read_b128 v[216:219], v247 offset:20480\n"
        "s_waitcnt lgkmcnt(3)\n"
        "v_mfma_f32_16x16x32_bf16 v[0:3], v[172:175], a[224:227], v[0:3]\n"
        "s_waitcnt lgkmcnt(2)\n"
        "ds_read_b128 a[216:219], v254 offset:43024\n"
        "ds_read_b128 a[220:223], v254 offset:12304\n"
        "v_fma_f32 v97, v97, s24, v238\n"
        "v_fma_f32 v98, v98, s24, v238\n"
        "v_mfma_f32_16x16x32_bf16 v[0:3], v[176:179], a[228:231], v[0:3]\n"
        "s_waitcnt lgkmcnt(3)\n"
        "ds_read_b128 a[224:227], v254 offset:49168\n"
        "ds_read_b128 a[228:231], v248 offset:32\n"
        "v_exp_f32 v97, v97\n"
        "v_mfma_f32_16x16x32_bf16 v[0:3], v[212:215], a[232:235], v[0:3]\n"
        "s_waitcnt lgkmcnt(4)\n"
        "ds_read_b128 a[232:235], v248 offset:6176\n"
        "v_exp_f32 v98, v98\n"
        "v_cvt_pk_bf16_f32 v96, v96, v97\n"
        "v_mfma_f32_16x16x32_bf16 v[0:3], v[216:219], a[236:239], v[0:3]\n"
        "ds_read_b128 a[236:239], v248 offset:36896\n"
        "v_fma_f32 v99, v99, s24, v238\n"
        "v_exp_f32 v99, v99\n"
        "v_mfma_f32_16x16x32_bf16 v[4:7], v[152:155], a[240:243], 0\n"
        "ds_read_b128 a[240:243], v248 offset:43040\n"
        "v_cvt_pk_bf16_f32 v97, v98, v99\n"
        "v_fma_f32 v100, v100, s24, v238\n"
        "v_fma_f32 v101, v101, s24, v238\n"
        "v_mfma_f32_16x16x32_bf16 v[4:7], v[156:159], a[244:247], v[4:7]\n"
        "ds_read_b128 a[244:247], v248 offset:12320\n"
        "v_exp_f32 v100, v100\n"
        "v_fma_f32 v102, v102, s24, v238\n"
        "v_mfma_f32_16x16x32_bf16 v[4:7], v[172:175], a[248:251], v[4:7]\n"
        "s_waitcnt lgkmcnt(7)\n"
        "ds_read_b128 a[248:251], v248 offset:49184\n"
        "v_exp_f32 v101, v101\n"
        "v_fma_f32 v103, v103, s24, v238\n"
        "v_mfma_f32_16x16x32_bf16 v[4:7], v[176:179], a[216:219], v[4:7]\n"
        "s_waitcnt lgkmcnt(7)\n"
        "v_cvt_pk_bf16_f32 v98, v100, v101\n"
        "v_exp_f32 v102, v102\n"
        "v_fma_f32 v104, v104, s24, v238\n"
        "v_mfma_f32_16x16x32_bf16 v[4:7], v[212:215], a[220:223], v[4:7]\n"
        "s_waitcnt lgkmcnt(6)\n"
        "v_exp_f32 v103, v103\n"
        "v_exp_f32 v104, v104\n"
        "v_mfma_f32_16x16x32_bf16 v[4:7], v[216:219], a[224:227], v[4:7]\n"
        "s_waitcnt lgkmcnt(5)\n"
        "v_cvt_pk_bf16_f32 v99, v102, v103\n"
        "v_fma_f32 v105, v105, s24, v238\n"
        "v_exp_f32 v105, v105\n"
        "v_mfma_f32_16x16x32_bf16 v[8:11], v[152:155], a[228:231], 0\n"
        "s_waitcnt lgkmcnt(4)\n"
        "ds_read_b128 a[216:219], v254 offset:48\n"
        "v_cvt_pk_bf16_f32 v100, v104, v105\n"
        "v_fma_f32 v106, v106, s24, v238\n"
        "v_fma_f32 v107, v107, s24, v238\n"
        "v_mfma_f32_16x16x32_bf16 v[8:11], v[156:159], a[232:235], v[8:11]\n"
        "s_waitcnt lgkmcnt(4)\n"
        "ds_read_b128 a[220:223], v254 offset:6192\n"
        "v_exp_f32 v106, v106\n"
        "v_fma_f32 v108, v108, s24, v238\n"
        "v_mfma_f32_16x16x32_bf16 v[8:11], v[172:175], a[236:239], v[8:11]\n"
        "s_waitcnt lgkmcnt(4)\n"
        "ds_read_b128 a[224:227], v254 offset:36912\n"
        "v_exp_f32 v107, v107\n"
        "v_fma_f32 v109, v109, s24, v238\n"
        "v_mfma_f32_16x16x32_bf16 v[8:11], v[176:179], a[240:243], v[8:11]\n"
        "s_waitcnt lgkmcnt(4)\n"
        "ds_read_b128 a[228:231], v254 offset:43056\n"
        "v_cvt_pk_bf16_f32 v101, v106, v107\n"
        "v_exp_f32 v108, v108\n"
        "v_mfma_f32_16x16x32_bf16 v[8:11], v[212:215], a[244:247], v[8:11]\n"
        "s_waitcnt lgkmcnt(4)\n"
        "ds_read_b128 a[232:235], v254 offset:12336\n"
        "v_exp_f32 v109, v109\n"
        "v_fma_f32 v110, v110, s24, v238\n"
        "v_mfma_f32_16x16x32_bf16 v[8:11], v[216:219], a[248:251], v[8:11]\n"
        "ds_read_b128 a[236:239], v254 offset:49200\n"
        "s_add_u32 s38, s37, s43\n"
        "v_cvt_pk_bf16_f32 v102, v108, v109\n"
        "v_exp_f32 v110, v110\n"
        "v_mfma_f32_16x16x32_bf16 v[64:67], a[252:255], v[96:99], 0\n"
        "ds_read_b128 a[240:243], v248 offset:64\n"
        "s_add_u32 s39, s28, s43\n"
        "v_fma_f32 v111, v111, s24, v238\n"
        "v_exp_f32 v111, v111\n"
        "v_fma_f32 v112, v112, s24, v239\n"
        "v_cvt_pk_bf16_f32 v103, v110, v111\n"
        "s_nop 1\n"
        "v_mfma_f32_16x16x32_bf16 v[64:67], a[252:255], v[100:103], v[64:67]\n"
        "ds_read_b128 a[244:247], v248 offset:6208\n"
        "v_exp_f32 v112, v112\n"
        "v_fma_f32 v113, v113, s24, v239\n"
        "v_mfma_f32_16x16x32_bf16 v[68:71], v[132:135], v[100:103], 0\n"
        "ds_read_b128 a[248:251], v248 offset:36928\n"
        "v_exp_f32 v113, v113\n"
        "v_fma_f32 v114, v114, s24, v239\n"
        "v_mfma_f32_16x16x32_bf16 v[68:71], v[128:131], v[96:99], v[68:71]\n"
        "v_rcp_f32 v64, v64\n"
        "v_exp_f32 v114, v114\n"
        "v_mfma_f32_16x16x32_bf16 v[72:75], v[136:139], v[96:99], 0\n"
        "v_cvt_pk_bf16_f32 v112, v112, v113\n"
        "v_fma_f32 v115, v115, s24, v239\n"
        "v_exp_f32 v115, v115\n"
        "v_mfma_f32_16x16x32_bf16 v[72:75], v[140:143], v[100:103], v[72:75]\n"
        "v_cvt_pk_bf16_f32 v113, v114, v115\n"
        "v_mul_f32 v68, v64, v68\n"
        "v_mul_f32 v69, v64, v69\n"
        "v_mul_f32 v70, v64, v70\n"
        "v_mfma_f32_16x16x32_bf16 v[76:79], v[148:151], v[100:103], 0\n"
        "v_mul_f32 v71, v64, v71\n"
        "v_cvt_pk_bf16_f32 v68, v68, v69\n"
        "v_cvt_pk_bf16_f32 v69, v70, v71\n"
        "v_mul_f32 v72, v64, v72\n"
        "v_mfma_f32_16x16x32_bf16 v[76:79], v[144:147], v[96:99], v[76:79]\n"
        "s_waitcnt lgkmcnt(8)\n"
        "v_mul_f32 v73, v64, v73\n"
        "v_mul_f32 v74, v64, v74\n"
        "v_mul_f32 v75, v64, v75\n"
        "v_cvt_pk_bf16_f32 v70, v72, v73\n"
        "v_mfma_f32_16x16x32_bf16 v[12:15], v[152:155], a[216:219], 0\n"
        "s_waitcnt lgkmcnt(7)\n"
        "ds_read_b128 a[216:219], v248 offset:43072\n"
        "v_cvt_pk_bf16_f32 v71, v74, v75\n"
        "v_fma_f32 v116, v116, s24, v239\n"
        "v_mul_f32 v76, v64, v76\n"
        "v_mfma_f32_16x16x32_bf16 v[12:15], v[156:159], a[220:223], v[12:15]\n"
        "s_waitcnt lgkmcnt(7)\n"
        "ds_read_b128 a[220:223], v248 offset:12352\n"
        "v_mul_f32 v77, v64, v77\n"
        "v_mul_f32 v78, v64, v78\n"
        "v_mul_f32 v79, v64, v79\n"
        "v_mfma_f32_16x16x32_bf16 v[12:15], v[172:175], a[224:227], v[12:15]\n"
        "s_waitcnt lgkmcnt(7)\n"
        "ds_write_b128 v245, v[68:71] offset:512\n"
        "v_mfma_f32_16x16x32_bf16 v[12:15], v[176:179], a[228:231], v[12:15]\n"
        "s_waitcnt lgkmcnt(7)\n"
        "ds_read_b128 a[224:227], v248 offset:49216\n"
        "ds_read_b128 a[228:231], v254 offset:80\n"
        "v_cvt_pk_bf16_f32 v76, v76, v77\n"
        "v_cvt_pk_bf16_f32 v77, v78, v79\n"
        "v_mfma_f32_16x16x32_bf16 v[12:15], v[212:215], a[232:235], v[12:15]\n"
        "s_waitcnt lgkmcnt(8)\n"
        "ds_read_b128 a[232:235], v254 offset:6224\n"
        "v_exp_f32 v116, v116\n"
        "v_fma_f32 v117, v117, s24, v239\n"
        "v_mfma_f32_16x16x32_bf16 v[12:15], v[216:219], a[236:239], v[12:15]\n"
        "s_waitcnt lgkmcnt(8)\n"
        "ds_write_b64 v246, v[76:77] offset:512\n"
        "ds_read_b128 a[236:239], v254 offset:36944\n"
        "v_mfma_f32_16x16x32_bf16 v[16:19], v[152:155], a[240:243], 0\n"
        "s_waitcnt lgkmcnt(9)\n"
        "ds_read_b128 a[240:243], v254 offset:43088\n"
        "v_exp_f32 v117, v117\n"
        "v_fma_f32 v118, v118, s24, v239\n"
        "v_mfma_f32_16x16x32_bf16 v[16:19], v[156:159], a[244:247], v[16:19]\n"
        "s_waitcnt lgkmcnt(9)\n"
        "ds_read_b128 a[244:247], v254 offset:12368\n"
        "v_exp_f32 v118, v118\n"
        "v_cvt_pk_bf16_f32 v114, v116, v117\n"
        "v_mfma_f32_16x16x32_bf16 v[16:19], v[172:175], a[248:251], v[16:19]\n"
        "s_waitcnt lgkmcnt(9)\n"
        "ds_read_b128 a[248:251], v254 offset:49232\n"
        "v_fma_f32 v119, v119, s24, v239\n"
        "v_exp_f32 v119, v119\n"
        "v_mfma_f32_16x16x32_bf16 v[16:19], v[176:179], a[216:219], v[16:19]\n"
        "s_waitcnt lgkmcnt(9)\n"
        "v_cvt_pk_bf16_f32 v115, v118, v119\n"
        "v_fma_f32 v120, v120, s24, v239\n"
        "v_exp_f32 v120, v120\n"
        "v_mfma_f32_16x16x32_bf16 v[16:19], v[212:215], a[220:223], v[16:19]\n"
        "s_waitcnt lgkmcnt(7)\n"
        "v_fma_f32 v121, v121, s24, v239\n"
        "v_exp_f32 v121, v121\n"
        "v_fma_f32 v122, v122, s24, v239\n"
        "v_mfma_f32_16x16x32_bf16 v[16:19], v[216:219], a[224:227], v[16:19]\n"
        "s_waitcnt lgkmcnt(6)\n"
        "v_cvt_pk_bf16_f32 v116, v120, v121\n"
        "v_exp_f32 v122, v122\n"
        "v_fma_f32 v123, v123, s24, v239\n"
        "v_mfma_f32_16x16x32_bf16 v[20:23], v[152:155], a[228:231], 0\n"
        "s_waitcnt lgkmcnt(5)\n"
        "ds_read_b128 a[216:219], v248 offset:0\n"
        "v_exp_f32 v123, v123\n"
        "v_fma_f32 v124, v124, s24, v239\n"
        "v_mfma_f32_16x16x32_bf16 v[20:23], v[156:159], a[232:235], v[20:23]\n"
        "s_waitcnt lgkmcnt(4)\n"
        "ds_read_b128 a[220:223], v248 offset:6144\n"
        "v_cvt_pk_bf16_f32 v117, v122, v123\n"
        "v_exp_f32 v124, v124\n"
        "v_mfma_f32_16x16x32_bf16 v[20:23], v[172:175], a[236:239], v[20:23]\n"
        "s_waitcnt lgkmcnt(4)\n"
        "ds_read_b128 a[224:227], v248 offset:36864\n"
        "v_fma_f32 v125, v125, s24, v239\n"
        "v_exp_f32 v125, v125\n"
        "v_mfma_f32_16x16x32_bf16 v[20:23], v[176:179], a[240:243], v[20:23]\n"
        "s_waitcnt lgkmcnt(4)\n"
        "ds_read_b128 a[228:231], v248 offset:43008\n"
        "v_cvt_pk_bf16_f32 v118, v124, v125\n"
        "v_fma_f32 v126, v126, s24, v239\n"
        "v_fma_f32 v127, v127, s24, v239\n"
        "v_mfma_f32_16x16x32_bf16 v[20:23], v[212:215], a[244:247], v[20:23]\n"
        "s_waitcnt lgkmcnt(4)\n"
        "ds_read_b128 a[232:235], v248 offset:12288\n"
        "v_exp_f32 v126, v126\n"
        "v_cvt_pk_bf16_f32 v192, v0, v4\n"
        "v_mfma_f32_16x16x32_bf16 v[20:23], v[216:219], a[248:251], v[20:23]\n"
        "ds_read_b128 a[236:239], v248 offset:49152\n"
        "v_exp_f32 v127, v127\n"
        "v_cvt_pk_bf16_f32 v193, v8, v12\n"
        "v_mfma_f32_16x16x32_bf16 v[80:83], a[252:255], v[112:115], 0\n"
        "ds_read_b128 a[240:243], v254 offset:16\n"
        "v_cvt_pk_bf16_f32 v119, v126, v127\n"
        "v_cvt_pk_bf16_f32 v196, v1, v5\n"
        "v_cvt_pk_bf16_f32 v197, v9, v13\n"
        "v_mfma_f32_16x16x32_bf16 v[80:83], a[252:255], v[116:119], v[80:83]\n"
        "ds_read_b128 a[244:247], v254 offset:6160\n"
        "v_cvt_pk_bf16_f32 v194, v16, v20\n"
        "v_cvt_pk_bf16_f32 v198, v17, v21\n"
        "v_cvt_pk_bf16_f32 v200, v2, v6\n"
        "v_mfma_f32_16x16x32_bf16 v[84:87], v[132:135], v[116:119], 0\n"
        "buffer_store_dwordx3 v[192:194], v250, s[16:19], s38 offen offset:0 sc1\n"
        "v_mfma_f32_16x16x32_bf16 v[84:87], v[128:131], v[112:115], v[84:87]\n"
        "ds_read_b128 a[248:251], v254 offset:36880\n"
        "v_rcp_f32 v80, v80\n"
        "v_cvt_pk_bf16_f32 v201, v10, v14\n"
        "v_mfma_f32_16x16x32_bf16 v[88:91], v[136:139], v[112:115], 0\n"
        "v_cvt_pk_bf16_f32 v202, v18, v22\n"
        "v_cvt_pk_bf16_f32 v204, v3, v7\n"
        "v_cvt_pk_bf16_f32 v205, v11, v15\n"
        "v_cvt_pk_bf16_f32 v206, v19, v23\n"
        "v_mfma_f32_16x16x32_bf16 v[88:91], v[140:143], v[116:119], v[88:91]\n"
        "buffer_store_dwordx3 v[196:198], v250, s[16:19], s38 offen offset:384 sc1\n"
        "v_mfma_f32_16x16x32_bf16 v[92:95], v[148:151], v[116:119], 0\n"
        "v_mul_f32 v84, v80, v84\n"
        "v_mul_f32 v85, v80, v85\n"
        "v_mul_f32 v86, v80, v86\n"
        "v_mul_f32 v87, v80, v87\n"
        "v_mfma_f32_16x16x32_bf16 v[92:95], v[144:147], v[112:115], v[92:95]\n"
        "v_cvt_pk_bf16_f32 v84, v84, v85\n"
        "v_cvt_pk_bf16_f32 v85, v86, v87\n"
        "v_mul_f32 v88, v80, v88\n"
        "v_mul_f32 v89, v80, v89\n"
        "v_mul_f32 v90, v80, v90\n"
        "v_mul_f32 v91, v80, v91\n"
        "v_cvt_pk_bf16_f32 v86, v88, v89\n"
        "v_cvt_pk_bf16_f32 v87, v90, v91\n"
        "v_mul_f32 v92, v80, v92\n"
        "v_mul_f32 v93, v80, v93\n"
        "v_mul_f32 v94, v80, v94\n"
        "v_mul_f32 v95, v80, v95\n"
        "v_cvt_pk_bf16_f32 v92, v92, v93\n"
        "v_cvt_pk_bf16_f32 v93, v94, v95\n"
        "ds_write_b128 v245, v[84:87] offset:768\n"
        "ds_write_b64 v246, v[92:93] offset:768\n"
        "s_waitcnt lgkmcnt(0)\n"
        "s_barrier\n"
        "ds_read_b128 v[128:131], v247 offset:512\n"
        "s_waitcnt lgkmcnt(0)\n"
        "v_mfma_f32_16x16x32_bf16 v[24:27], v[128:131], a[216:219], 0\n"
        "ds_read_b128 v[132:135], v247 offset:4608\n"
        "ds_read_b128 v[136:139], v247 offset:8704\n"
        "s_waitcnt lgkmcnt(1)\n"
        "v_mfma_f32_16x16x32_bf16 v[24:27], v[132:135], a[220:223], v[24:27]\n"
        "s_waitcnt lgkmcnt(0)\n"
        "ds_read_b128 v[140:143], v247 offset:12800\n"
        "ds_read_b128 v[144:147], v247 offset:16896\n"
        "v_mfma_f32_16x16x32_bf16 v[24:27], v[136:139], a[224:227], v[24:27]\n"
        "s_waitcnt lgkmcnt(1)\n"
        "ds_read_b128 v[148:151], v247 offset:20992\n"
        "ds_read_b128 a[216:219], v254 offset:43024\n"
        "v_mfma_f32_16x16x32_bf16 v[24:27], v[140:143], a[228:231], v[24:27]\n"
        "s_waitcnt lgkmcnt(2)\n"
        "buffer_store_dwordx3 v[200:202], v250, s[16:19], s38 offen offset:768 sc1\n"
        "v_mfma_f32_16x16x32_bf16 v[24:27], v[144:147], a[232:235], v[24:27]\n"
        "s_waitcnt lgkmcnt(1)\n"
        "ds_read_b128 a[220:223], v254 offset:12304\n"
        "ds_read_b128 a[224:227], v254 offset:49168\n"
        "v_mfma_f32_16x16x32_bf16 v[24:27], v[148:151], a[236:239], v[24:27]\n"
        "ds_read_b128 a[228:231], v248 offset:32\n"
        "ds_read_b128 a[232:235], v248 offset:6176\n"
        "v_mfma_f32_16x16x32_bf16 v[28:31], v[128:131], a[240:243], 0\n"
        "buffer_store_dwordx3 v[204:206], v250, s[16:19], s38 offen offset:1152 sc1\n"
        "v_mfma_f32_16x16x32_bf16 v[28:31], v[132:135], a[244:247], v[28:31]\n"
        "ds_read_b128 a[236:239], v248 offset:36896\n"
        "ds_read_b128 a[240:243], v248 offset:43040\n"
        "v_mfma_f32_16x16x32_bf16 v[28:31], v[136:139], a[248:251], v[28:31]\n"
        "s_waitcnt lgkmcnt(6)\n"
        "ds_read_b128 a[244:247], v248 offset:12320\n"
        "ds_read_b128 a[248:251], v248 offset:49184\n"
        "v_mfma_f32_16x16x32_bf16 v[28:31], v[140:143], a[216:219], v[28:31]\n"
        "s_waitcnt lgkmcnt(7)\n"
        "ds_read_b128 a[216:219], v254 offset:48\n"
        "v_mfma_f32_16x16x32_bf16 v[28:31], v[144:147], a[220:223], v[28:31]\n"
        "s_waitcnt lgkmcnt(7)\n"
        "ds_read_b128 a[220:223], v254 offset:6192\n"
        "v_mfma_f32_16x16x32_bf16 v[28:31], v[148:151], a[224:227], v[28:31]\n"
        "s_waitcnt lgkmcnt(7)\n"
        "ds_read_b128 a[224:227], v254 offset:36912\n"
        "v_mfma_f32_16x16x32_bf16 v[32:35], v[128:131], a[228:231], 0\n"
        "s_waitcnt lgkmcnt(7)\n"
        "ds_read_b128 a[228:231], v254 offset:43056\n"
        "v_mfma_f32_16x16x32_bf16 v[32:35], v[132:135], a[232:235], v[32:35]\n"
        "s_waitcnt lgkmcnt(7)\n"
        "ds_read_b128 a[232:235], v254 offset:12336\n"
        "v_mfma_f32_16x16x32_bf16 v[32:35], v[136:139], a[236:239], v[32:35]\n"
        "s_waitcnt lgkmcnt(7)\n"
        "ds_read_b128 a[236:239], v254 offset:49200\n"
        "v_mfma_f32_16x16x32_bf16 v[32:35], v[140:143], a[240:243], v[32:35]\n"
        "s_waitcnt lgkmcnt(7)\n"
        "ds_read_b128 a[240:243], v248 offset:64\n"
        "v_cvt_pk_bf16_f32 v180, v24, v28\n"
        "v_mfma_f32_16x16x32_bf16 v[32:35], v[144:147], a[244:247], v[32:35]\n"
        "s_waitcnt lgkmcnt(7)\n"
        "ds_read_b128 a[244:247], v248 offset:6208\n"
        "v_cvt_pk_bf16_f32 v184, v25, v29\n"
        "v_mfma_f32_16x16x32_bf16 v[32:35], v[148:151], a[248:251], v[32:35]\n"
        "s_waitcnt lgkmcnt(7)\n"
        "ds_read_b128 a[248:251], v248 offset:36928\n"
        "v_cvt_pk_bf16_f32 v220, v26, v30\n"
        "v_mfma_f32_16x16x32_bf16 v[36:39], v[128:131], a[216:219], 0\n"
        "s_waitcnt lgkmcnt(7)\n"
        "ds_read_b128 a[216:219], v248 offset:43072\n"
        "v_cvt_pk_bf16_f32 v224, v27, v31\n"
        "v_mfma_f32_16x16x32_bf16 v[36:39], v[132:135], a[220:223], v[36:39]\n"
        "s_waitcnt lgkmcnt(7)\n"
        "ds_read_b128 a[220:223], v248 offset:12352\n"
        "v_mfma_f32_16x16x32_bf16 v[36:39], v[136:139], a[224:227], v[36:39]\n"
        "s_waitcnt lgkmcnt(7)\n"
        "ds_read_b128 a[224:227], v248 offset:49216\n"
        "v_mfma_f32_16x16x32_bf16 v[36:39], v[140:143], a[228:231], v[36:39]\n"
        "s_waitcnt lgkmcnt(7)\n"
        "ds_read_b128 a[228:231], v254 offset:80\n"
        "v_mfma_f32_16x16x32_bf16 v[36:39], v[144:147], a[232:235], v[36:39]\n"
        "s_waitcnt lgkmcnt(7)\n"
        "ds_read_b128 a[232:235], v254 offset:6224\n"
        "v_mfma_f32_16x16x32_bf16 v[36:39], v[148:151], a[236:239], v[36:39]\n"
        "s_waitcnt lgkmcnt(7)\n"
        "ds_read_b128 a[236:239], v254 offset:36944\n"
        "v_mfma_f32_16x16x32_bf16 v[40:43], v[128:131], a[240:243], 0\n"
        "s_waitcnt lgkmcnt(7)\n"
        "ds_read_b128 a[240:243], v254 offset:43088\n"
        "v_mfma_f32_16x16x32_bf16 v[40:43], v[132:135], a[244:247], v[40:43]\n"
        "s_waitcnt lgkmcnt(7)\n"
        "ds_read_b128 a[244:247], v254 offset:12368\n"
        "v_mfma_f32_16x16x32_bf16 v[40:43], v[136:139], a[248:251], v[40:43]\n"
        "s_waitcnt lgkmcnt(7)\n"
        "ds_read_b128 a[248:251], v254 offset:49232\n"
        "v_mfma_f32_16x16x32_bf16 v[40:43], v[140:143], a[216:219], v[40:43]\n"
        "s_waitcnt lgkmcnt(7)\n"
        "v_cvt_pk_bf16_f32 v181, v32, v36\n"
        "v_cvt_pk_bf16_f32 v185, v33, v37\n"
        "v_mfma_f32_16x16x32_bf16 v[40:43], v[144:147], a[220:223], v[40:43]\n"
        "s_waitcnt lgkmcnt(6)\n"
        "v_cvt_pk_bf16_f32 v221, v34, v38\n"
        "v_cvt_pk_bf16_f32 v225, v35, v39\n"
        "v_mfma_f32_16x16x32_bf16 v[40:43], v[148:151], a[224:227], v[40:43]\n"
        "s_waitcnt lgkmcnt(5)\n"
        "v_mfma_f32_16x16x32_bf16 v[44:47], v[128:131], a[228:231], 0\n"
        "s_waitcnt lgkmcnt(4)\n"
        "v_mfma_f32_16x16x32_bf16 v[44:47], v[132:135], a[232:235], v[44:47]\n"
        "s_waitcnt lgkmcnt(3)\n"
        "v_mfma_f32_16x16x32_bf16 v[44:47], v[136:139], a[236:239], v[44:47]\n"
        "s_waitcnt lgkmcnt(2)\n"
        "v_mfma_f32_16x16x32_bf16 v[44:47], v[140:143], a[240:243], v[44:47]\n"
        "s_waitcnt lgkmcnt(1)\n"
        "v_mfma_f32_16x16x32_bf16 v[44:47], v[144:147], a[244:247], v[44:47]\n"
        "s_waitcnt lgkmcnt(0)\n"
        "v_mfma_f32_16x16x32_bf16 v[44:47], v[148:151], a[248:251], v[44:47]\n"
        "s_nop 7\n"
        "v_cvt_pk_bf16_f32 v182, v40, v44\n"
        "buffer_store_dwordx3 v[180:182], v250, s[16:19], s39 offen offset:0 sc1\n"
        "v_cvt_pk_bf16_f32 v186, v41, v45\n"
        "buffer_store_dwordx3 v[184:186], v250, s[16:19], s39 offen offset:384 sc1\n"
        "v_cvt_pk_bf16_f32 v222, v42, v46\n"
        "buffer_store_dwordx3 v[220:222], v250, s[16:19], s39 offen offset:768 sc1\n"
        "v_cvt_pk_bf16_f32 v226, v43, v47\n"
        "buffer_store_dwordx3 v[224:226], v250, s[16:19], s39 offen offset:1152 sc1\n"
        : "+{v240}"(xr[0]), "+{v241}"(xr[1]), "+{v251}"(mr[0]), "+{v252}"(mr[1]), "+{v242}"(dm[0]), "+{v243}"(dm[1]), "+{v244}"(dm[2]), "+{v245}"(ow), "+{v246}"(ow2), "+{v247}"(ord), "+{v248}"(wor), "+{v249}"(ms), "+{v250}"(st), "+{v142}"(l16), "+{v170}"(id0), "+{v171}"(id1), "+{v139}"(rs), "+{v144}"(rr), "+{v140}"(oa), "+{v141}"(ob), "+{v147}"(od), "+{v145}"(rbs), "+{v146}"(rbw), "+{v254}"(wor2), "+{v255}"(ml), "+{v148}"(pf0), "+{v149}"(pf1), "+{v128}"(wsrc[0]), "+{v129}"(wsrc[1]), "+{v130}"(wsrc[2]), "+{v131}"(wrd[0]), "+{v132}"(wrd[1]), "+{v133}"(wrd[2]), "+{s47}"(ya), "+{s36}"(mk), "+{s37}"(raw), "+{s43}"(x0), "+{s44}"(x1)
        : "{s[8:11]}"(srdx), "{s[12:15]}"(srdm), "{s[16:19]}"(srdo), "{s[20:23]}"(srdw), "{s[52:55]}"(srdwo), "{s[56:59]}"(srdrp), "{s[60:63]}"(srdrb), "{s42}"(wv), "s"(ldsb)
        : "v0", "v1", "v2", "v3", "v4", "v5", "v6", "v7", "v8", "v9", "v10", "v11", "v12", "v13", "v14", "v15", "v16", "v17", "v18", "v19", "v20", "v21", "v22", "v23", "v24", "v25", "v26", "v27", "v28", "v29", "v30", "v31", "v32", "v33", "v34", "v35", "v36", "v37", "v38", "v39", "v40", "v41", "v42", "v43", "v44", "v45", "v46", "v47", "v48", "v49", "v50", "v51", "v52", "v53", "v54", "v55", "v56", "v57", "v58", "v59", "v60", "v61", "v62", "v63", "v64", "v65", "v66", "v67", "v68", "v69", "v70", "v71", "v72", "v73", "v74", "v75", "v76", "v77", "v78", "v79", "v80", "v81", "v82", "v83", "v84", "v85", "v86", "v87", "v88", "v89", "v90", "v91", "v92", "v93", "v94", "v95", "v96", "v97", "v98", "v99", "v100", "v101", "v102", "v103", "v104", "v105", "v106", "v107", "v108", "v109", "v110", "v111", "v112", "v113", "v114", "v115", "v116", "v117", "v118", "v119", "v120", "v121", "v122", "v123", "v124", "v125", "v126", "v127", "v134", "v135", "v136", "v137", "v138", "v143", "v150", "v151", "v152", "v153", "v154", "v155", "v156", "v157", "v158", "v159", "v160", "v161", "v162", "v163", "v164", "v165", "v166", "v167", "v168", "v169", "v172", "v173", "v174", "v175", "v176", "v177", "v178", "v179", "v180", "v181", "v182", "v183", "v184", "v185", "v186", "v187", "v188", "v189", "v190", "v191", "v192", "v193", "v194", "v195", "v196", "v197", "v198", "v199", "v200", "v201", "v202", "v203", "v204", "v205", "v206", "v207", "v208", "v209", "v210", "v211", "v212", "v213", "v214", "v215", "v216", "v217", "v218", "v219", "v220", "v221", "v222", "v223", "v224", "v225", "v226", "v227", "v228", "v229", "v230", "v231", "v232", "v233", "v234", "v235", "v236", "v237", "v238", "v239", "v253", "a0", "a1", "a2", "a3", "a4", "a5", "a6", "a7", "a8", "a9", "a10", "a11", "a12", "a13", "a14", "a15", "a16", "a17", "a18", "a19", "a20", "a21", "a22", "a23", "a24", "a25", "a26", "a27", "a28", "a29", "a30", "a31", "a32", "a33", "a34", "a35", "a36", "a37", "a38", "a39", "a40", "a41", "a42", "a43", "a44", "a45", "a46", "a47", "a48", "a49", "a50", "a51", "a52", "a53", "a54", "a55", "a56", "a57", "a58", "a59", "a60", "a61", "a62", "a63", "a64", "a65", "a66", "a67", "a68", "a69", "a70", "a71", "a72", "a73", "a74", "a75", "a76", "a77", "a78", "a79", "a80", "a81", "a82", "a83", "a84", "a85", "a86", "a87", "a88", "a89", "a90", "a91", "a92", "a93", "a94", "a95", "a96", "a97", "a98", "a99", "a100", "a101", "a102", "a103", "a104", "a105", "a106", "a107", "a108", "a109", "a110", "a111", "a112", "a113", "a114", "a115", "a116", "a117", "a118", "a119", "a120", "a121", "a122", "a123", "a124", "a125", "a126", "a127", "a128", "a129", "a130", "a131", "a132", "a133", "a134", "a135", "a136", "a137", "a138", "a139", "a140", "a141", "a142", "a143", "a144", "a145", "a146", "a147", "a148", "a149", "a150", "a151", "a152", "a153", "a154", "a155", "a156", "a157", "a158", "a159", "a160", "a161", "a162", "a163", "a164", "a165", "a166", "a167", "a168", "a169", "a170", "a171", "a172", "a173", "a174", "a175", "a176", "a177", "a178", "a179", "a180", "a181", "a182", "a183", "a184", "a185", "a186", "a187", "a188", "a189", "a190", "a191", "a192", "a193", "a194", "a195", "a196", "a197", "a198", "a199", "a200", "a201", "a202", "a203", "a204", "a205", "a206", "a207", "a208", "a209", "a210", "a211", "a212", "a213", "a214", "a215", "a216", "a217", "a218", "a219", "a220", "a221", "a222", "a223", "a224", "a225", "a226", "a227", "a228", "a229", "a230", "a231", "a232", "a233", "a234", "a235", "a236", "a237", "a238", "a239", "a240", "a241", "a242", "a243", "a244", "a245", "a246", "a247", "a248", "a249", "a250", "a251", "a252", "a253", "a254", "a255", "s24", "s25", "s26", "s27", "s28", "s29", "s30", "s31", "s34", "s35", "s38", "s39", "s40", "s41", "s45", "s46", "s48", "s49", "s50", "s51", "s64", "s65", "s66", "s67", "vcc", "scc", "memory");
}


__global__ __launch_bounds__(256, 1) __attribute__((aligned(4096))) void win_attn_kernel(const char* __restrict__ x, const char* __restrict__ wqkv,
                                                          const char* __restrict__ wo, const char* __restrict__ rpb,
                                                          const char* __restrict__ rpi, const char* __restrict__ mask,
                                                          char* __restrict__ out, char* ws) {
    __shared__ __attribute__((aligned(16))) char smem[163840];
    unsigned tid = threadIdx.x, w, nit;
    unsigned long long hist;
    const unsigned ldsb = (unsigned)(size_t)smem;
    asm volatile(
        "s_load_dwordx2 s[8:9], s[0:1], 0\n"
        "s_load_dwordx2 s[20:21], s[0:1], 8\n"
        "s_load_dwordx2 s[52:53], s[0:1], 16\n"
        "s_load_dwordx2 s[60:61], s[0:1], 24\n"
        "s_load_dwordx2 s[56:57], s[0:1], 32\n"
        "s_load_dwordx2 s[12:13], s[0:1], 40\n"
        "s_load_dwordx2 s[16:17], s[0:1], 48\n"
        "v_readfirstlane_b32 s42, v0\n"
        "v_and_b32 v1, 63, v0\n"
        "v_lshlrev_b32 v145, 3, v0\n"
        "s_lshr_b32 s42, s42, 6\n"
        "s_bfe_u32 s41, s2, 0x50003\n"
        "s_lshl2_add_u32 s41, s41, s42\n"
        "s_lshl_b32 s49, s41, 8\n"
        "v_and_b32 v10, 1, v0\n"
        "v_lshl_add_u32 v10, v10, 7, s49\n"
        "v_min_u32 v10, 32256, v10\n"
        "s_getpc_b64 s[38:39]\n"
        "global_load_dword v66, v10, s[38:39]\n"
        "v_bfe_u32 v2, v0, 4, 2\n"
        "s_and_b32 s51, s42, 1\n"
        "s_lshl_b32 s66, s51, 3\n"
        "v_xor_b32 v11, v1, v2\n"
        "v_xor_b32 v11, s66, v11\n"
        "v_lshlrev_b32 v139, 4, v11\n"
        "v_xor_b32 v144, 64, v139\n"
        "s_lshl_b32 s34, s42, 12\n"
        "s_mul_i32 s40, s42, 3072\n"
        "s_and_b32 s31, s2, 63\n"
        "s_lshl_b32 s31, s31, 3\n"
        "s_add_u32 s31, s31, 4\n"
        "s_lshr_b32 s45, s2, 6\n"
        "s_lshl_b32 s45, s45, 3\n"
        "s_add_u32 s47, s45, s42\n"
        "s_add_u32 s47, s47, 68\n"
        "s_mul_i32 s47, s47, 196608\n"
        "s_lshl_b32 s46, s42, 1\n"
        "s_lshl_b32 s49, s41, 11\n"
        "v_lshl_add_u32 v148, v1, 7, s49\n"
        "v_lshl_or_b32 v148, v2, 28, v148\n"
        "s_mov_b32 s62, 1800\n"
        "s_mov_b32 s63, 0x20000\n"
        "s_mov_b32 s58, 16384\n"
        "s_mov_b32 s59, 0x20000\n"
        "s_mov_b32 s22, 221184\n"
        "s_mov_b32 s23, 0x20000\n"
        "s_mov_b32 s10, 100663296\n"
        "s_mov_b32 s11, 0x20000\n"
        "v_mul_u32_u24 v15, 171, v1\n"
        "v_lshrrev_b32 v15, 12, v15\n"
        "v_mul_u32_u24 v13, 24, v15\n"
        "v_sub_u32 v18, v1, v13\n"
        "v_bfe_u32 v21, v15, 1, 1\n"
        "v_add_u32 v13, s31, v15\n"
        "v_and_b32 v13, 0x1ff, v13\n"
        "v_mul_u32_u24 v13, 384, v13\n"
        "v_bitop3_b32 v14, v21, v18, s46 bitop3:0x36\n"
        "v_lshl_add_u32 v242, v14, 4, v13\n"
        "v_add_u32 v12, 64, v1\n"
        "v_mul_u32_u24 v16, 171, v12\n"
        "v_lshrrev_b32 v16, 12, v16\n"
        "v_mul_u32_u24 v13, 24, v16\n"
        "v_sub_u32 v19, v12, v13\n"
        "v_bfe_u32 v22, v16, 1, 1\n"
        "v_add_u32 v13, s31, v16\n"
        "v_and_b32 v13, 0x1ff, v13\n"
        "v_mul_u32_u24 v13, 384, v13\n"
        "v_bitop3_b32 v14, v22, v19, s46 bitop3:0x36\n"
        "v_lshl_add_u32 v243, v14, 4, v13\n"
        "v_add_u32 v12, 128, v1\n"
        "v_mul_u32_u24 v17, 171, v12\n"
        "v_lshrrev_b32 v17, 12, v17\n"
        "v_mul_u32_u24 v13, 24, v17\n"
        "v_sub_u32 v20, v12, v13\n"
        "v_bfe_u32 v23, v17, 1, 1\n"
        "v_add_u32 v13, s31, v17\n"
        "v_and_b32 v13, 0x1ff, v13\n"
        "v_mul_u32_u24 v13, 384, v13\n"
        "v_bitop3_b32 v14, v23, v20, s46 bitop3:0x36\n"
        "v_lshl_add_u32 v244, v14, 4, v13\n"
        "s_mov_b32 s14, 33554432\n"
        "s_mov_b32 s15, 0x20000\n"
        "v_bfe_u32 v4, v0, 4, 1\n"
        "s_lshl_b32 s66, s51, 2\n"
        "v_bitop3_b32 v11, v4, v1, s66 bitop3:0x36\n"
        "v_lshlrev_b32 v249, 4, v11\n"
        "s_waitcnt lgkmcnt(0)\n"
        "buffer_load_dword v64, v148, s[20:23], 0 offen\n"
        "buffer_load_dwordx2 v[28:29], v145, s[60:63], 0 offen\n"
        "s_add_u32 m0, s34, 122880\n"
        "s_mov_b32 s64, s34\n"
        "buffer_load_dwordx4 v139, s[56:59], s64 offen lds\n"
        "v_xor_b32 v149, 32, v249\n"
        "s_lshl_b32 s48, s42, 11\n"
        "s_lshl_b32 s36, s2, 13\n"
        "s_add_u32 s36, s36, s48\n"
        "s_add_u32 s36, s36, 4194304\n"
        "s_add_u32 s48, s48, 147456\n"
        "s_mov_b32 s54, 73728\n"
        "s_mov_b32 s55, 0x20000\n"
        "s_mov_b32 s18, 100663296\n"
        "s_mov_b32 s19, 0x20000\n"
        "s_mul_i32 s65, s42, 9216\n"
        "v_lshl_or_b32 v13, v21, 1, s66\n"
        "buffer_load_dwordx4 v139, s[56:59], s64 offen offset:1024 lds\n"
        "v_xor_b32 v13, v13, v18\n"
        "v_mul_u32_u24 v14, 1152, v15\n"
        "v_lshl_add_u32 v128, v13, 4, v14\n"
        "v_lshl_or_b32 v13, v22, 1, s66\n"
        "v_xor_b32 v13, v13, v19\n"
        "v_mul_u32_u24 v14, 1152, v16\n"
        "v_lshl_add_u32 v129, v13, 4, v14\n"
        "v_subrev_u32 v129, 1024, v129\n"
        "v_lshl_or_b32 v13, v23, 1, s66\n"
        "v_xor_b32 v13, v13, v20\n"
        "v_mul_u32_u24 v14, 1152, v17\n"
        "v_lshl_add_u32 v130, v13, 4, v14\n"
        "buffer_load_dwordx4 v144, s[56:59], s64 offen offset:2048 lds\n"
        "v_subrev_u32 v130, 2048, v130\n"
        "v_bfe_u32 v6, v0, 2, 2\n"
        "v_and_b32 v7, 3, v0\n"
        "v_lshlrev_b32 v150, 4, v1\n"
        "v_lshl_add_u32 v8, v6, 3, v7\n"
        "v_and_b32 v9, 0xf0, v150\n"
        "v_lshlrev_b32 v146, 2, v0\n"
        "v_add_u32 v146, 139264, v146\n"
        "v_lshlrev_b32 v10, 5, v2\n"
        "v_xor_b32 v10, v10, v9\n"
        "v_lshl_add_u32 v10, v8, 8, v10\n"
        "v_add_u32 v140, 122880, v10\n"
        "buffer_load_dwordx4 v144, s[56:59], s64 offen offset:3072 lds\n"
        "v_xor_b32 v141, 16, v140\n"
        "v_xor_b32 v142, 128, v140\n"
        "v_xor_b32 v143, 144, v140\n"
        "s_lshl_b32 s67, s42, 10\n"
        "s_add_u32 s67, s67, 139264\n"
        "v_bfe_u32 v10, v0, 1, 3\n"
        "v_xor_b32 v10, v10, v2\n"
        "v_lshlrev_b32 v10, 4, v10\n"
        "v_mul_u32_u24 v11, 384, v8\n"
        "v_add_u32 v11, 73728, v11\n"
        "v_add_u32 v240, v10, v11\n"
        "v_xor_b32 v241, 64, v240\n"
        "s_add_u32 m0, s40, 73728\n"
        "s_sub_u32 s64, s47, 12582912\n"
        "buffer_load_dwordx4 v242, s[8:11], s64 offen nt lds\n"
        "v_lshl_add_u32 v10, v8, 7, v10\n"
        "v_add_u32 v251, 147456, v10\n"
        "v_xor_b32 v252, 64, v251\n"
        "v_lshl_add_u32 v10, v2, 3, v6\n"
        "v_mul_u32_u24 v10, 384, v10\n"
        "v_and_b32 v11, 1, v0\n"
        "v_lshl_add_u32 v10, v11, 3, v10\n"
        "v_and_b32 v12, 2, v6\n"
        "v_lshl_or_b32 v12, v4, 2, v12\n"
        "s_mul_i32 s35, s42, 6\n"
        "v_bfe_u32 v13, v0, 1, 1\n"
        "v_add_u32 v13, s35, v13\n"
        "s_add_u32 m0, s40, 74752\n"
        "s_sub_u32 s64, s47, 12582912\n"
        "buffer_load_dwordx4 v243, s[8:11], s64 offen nt lds\n"
        "v_xor_b32 v14, v13, v12\n"
        "v_lshl_add_u32 v131, v14, 4, v10\n"
        "v_add_u32 v136, 98304, v131\n"
        "v_add_u32 v13, 2, v13\n"
        "v_xor_b32 v14, v13, v12\n"
        "v_lshl_add_u32 v132, v14, 4, v10\n"
        "v_add_u32 v137, 98304, v132\n"
        "v_add_u32 v13, 2, v13\n"
        "v_xor_b32 v14, v13, v12\n"
        "v_lshl_add_u32 v133, v14, 4, v10\n"
        "v_add_u32 v138, 98304, v133\n"
        "v_lshlrev_b32 v10, 4, v11\n"
        "s_add_u32 m0, s40, 75776\n"
        "s_sub_u32 s64, s47, 12582912\n"
        "buffer_load_dwordx4 v244, s[8:11], s64 offen nt lds\n"
        "v_mov_b32 v12, 0x3fb9\n"
        "v_cmp_eq_u32 vcc, v6, v2\n"
        "v_lshlrev_b32 v10, v10, v12\n"
        "v_bfe_i32 v13, v0, 0, 1\n"
        "v_cndmask_b32 v10, 0, v10, vcc\n"
        "v_cmp_gt_u32 vcc, 2, v7\n"
        "v_and_b32 v13, 0xaaaaaaaa, v13\n"
        "v_or_b32 v255, 0x44444444, v13\n"
        "v_cndmask_b32 v170, 0, v10, vcc\n"
        "v_xor_b32 v171, v170, v10\n"
        "v_mov_b64 v[188:189], v[170:171]\n"
        "v_mov_b64 v[210:211], v[170:171]\n"
        "s_add_u32 m0, s40, 86016\n"
        "s_sub_u32 s64, s47, 11796480\n"
        "buffer_load_dwordx4 v242, s[8:11], s64 offen nt lds\n"
        "v_mov_b64 v[228:229], v[170:171]\n"
        "v_bfe_u32 v5, v0, 5, 1\n"
        "v_and_b32 v3, 15, v0\n"
        "v_lshl_add_u32 v10, v2, 10, v9\n"
        "s_add_u32 s49, s34, 73728\n"
        "v_add_u32 v245, s49, v10\n"
        "s_lshl_b32 s49, s51, 9\n"
        "s_add_u32 s49, s49, 73728\n"
        "v_add_u32 v247, s49, v10\n"
        "v_lshl_add_u32 v10, v5, 10, v9\n"
        "v_lshl_add_u32 v10, v4, 3, v10\n"
        "s_lshl_b32 s49, s42, 11\n"
        "s_add_u32 m0, s40, 87040\n"
        "s_sub_u32 s64, s47, 11796480\n"
        "buffer_load_dwordx4 v243, s[8:11], s64 offen nt lds\n"
        "s_add_u32 s49, s49, 90112\n"
        "v_add_u32 v246, s49, v10\n"
        "s_lshr_b32 s35, s42, 1\n"
        "s_mul_i32 s49, s35, 18432\n"
        "v_mul_u32_u24 v12, 1536, v2\n"
        "v_add_u32 v12, s49, v12\n"
        "v_mad_u32_u24 v13, v9, 6, v12\n"
        "v_lshl_add_u32 v248, v4, 4, v13\n"
        "v_mad_i32_i24 v254, v4, -16, v13\n"
        "v_mul_u32_u24 v10, 196608, v2\n"
        "v_lshl_add_u32 v10, v3, 2, v10\n"
        "s_lshl_b32 s49, s35, 6\n"
        "s_add_u32 m0, s40, 88064\n"
        "s_sub_u32 s64, s47, 11796480\n"
        "buffer_load_dwordx4 v244, s[8:11], s64 offen nt lds\n"
        "s_add_u32 s49, s49, 128\n"
        "v_add_u32 v250, s49, v10\n"
        "s_mul_i32 s26, s35, 192\n"
        "s_sub_u32 s26, s26, 128\n"
        "s_mul_i32 s43, s31, 384\n"
        "s_add_u32 s44, s31, 4\n"
        "s_and_b32 s44, s44, 511\n"
        "s_mul_i32 s44, s44, 384\n"
        "s_add_u32 s37, s45, s66\n"
        "s_add_u32 s37, s37, 4\n"
        "s_mul_i32 s37, s37, 196608\n"
        "s_mul_i32 s51, s51, 36864\n"
        "s_mov_b32 m0, s48\n"
        "s_sub_u32 s64, s36, 4194304\n"
        "buffer_load_dwordx4 v249, s[12:15], s64 offen nt lds\n"
        "s_mul_i32 s66, s35, 18432\n"
        "s_add_u32 s66, s66, s51\n"
        "v_add_u32 v12, s51, v12\n"
        "v_bfrev_b32 v14, v0\n"
        "v_bfe_u32 v10, v14, 28, 2\n"
        "v_mad_u32_u24 v10, v10, 6, v7\n"
        "v_bfe_u32 v13, v0, 1, 3\n"
        "v_mad_u32_u24 v13, v13, 6, v11\n"
        "v_add_u32 v13, 4, v13\n"
        "v_xor_b32 v10, v10, v4\n"
        "v_xor_b32 v13, v13, v4\n"
        "v_lshl_add_u32 v236, v10, 4, v12\n"
        "buffer_load_dwordx4 v149, s[12:15], s64 offen offset:1024 nt lds\n"
        "v_lshl_add_u32 v237, v13, 4, v12\n"
        "v_lshrrev_b32 v10, 2, v1\n"
        "v_mul_u32_u24 v10, 192, v10\n"
        "v_add_u32 v10, s66, v10\n"
        "v_lshlrev_b32 v13, 5, v4\n"
        "v_add_u32 v12, v10, v13\n"
        "v_sub_u32 v13, v10, v13\n"
        "v_lshrrev_b32 v14, 30, v14\n"
        "v_lshl_add_u32 v234, v14, 3, v12\n"
        "v_lshl_add_u32 v235, v14, 3, v13\n"
        "v_lshl_add_u32 v238, v7, 3, v12\n"
        "v_lshl_add_u32 v239, v7, 3, v13\n"
        "s_load_dwordx2 s[4:5], s[0:1], 56\n"
        "s_lshl_b32 s46, s35, 8\n"
        "s_mul_i32 s35, s35, -192\n"
        "s_movk_i32 s49, 1536\n"
        "v_mul_u32_u24 v10, 171, v1\n"
        "v_lshrrev_b32 v10, 11, v10\n"
        "v_mad_i32_i24 v13, v10, -12, v1\n"
        "v_lshrrev_b32 v13, 3, v13\n"
        "v_mul_u32_u24 v14, 192, v10\n"
        "v_add_u32 v14, s46, v14\n"
        "v_mad_i32_i24 v14, v13, s35, v14\n"
        "v_and_b32 v13, 4, v10\n"
        "v_lshlrev_b32 v13, 3, v13\n"
        "v_xad_u32 v124, v150, v13, v14\n"
        "v_lshrrev_b32 v13, 2, v10\n"
        "v_and_or_b32 v13, v10, 8, v13\n"
        "v_mad_u32_u24 v127, v13, s49, v124\n"
        "v_add_u32 v12, 64, v1\n"
        "v_mul_u32_u24 v10, 171, v12\n"
        "v_lshrrev_b32 v10, 11, v10\n"
        "v_mad_i32_i24 v13, v10, -12, v12\n"
        "v_lshrrev_b32 v13, 3, v13\n"
        "v_mul_u32_u24 v14, 192, v10\n"
        "v_add_u32 v14, s46, v14\n"
        "v_mad_i32_i24 v14, v13, s35, v14\n"
        "v_and_b32 v13, 4, v10\n"
        "v_lshlrev_b32 v13, 3, v13\n"
        "v_xad_u32 v125, v150, v13, v14\n"
        "v_lshrrev_b32 v13, 2, v10\n"
        "v_and_or_b32 v13, v10, 8, v13\n"
        "v_mad_u32_u24 v232, v13, s49, v125\n"
        "v_add_u32 v12, 128, v1\n"
        "v_mul_u32_u24 v10, 171, v12\n"
        "v_lshrrev_b32 v10, 11, v10\n"
        "v_mad_i32_i24 v13, v10, -12, v12\n"
        "v_lshrrev_b32 v13, 3, v13\n"
        "v_mul_u32_u24 v14, 192, v10\n"
        "v_add_u32 v14, s46, v14\n"
        "v_mad_i32_i24 v14, v13, s35, v14\n"
        "v_and_b32 v13, 4, v10\n"
        "v_lshlrev_b32 v13, 3, v13\n"
        "v_xad_u32 v126, v150, v13, v14\n"
        "v_lshrrev_b32 v13, 2, v10\n"
        "v_and_or_b32 v13, v10, 8, v13\n"
        "v_mad_u32_u24 v233, v13, s49, v126\n"
        "s_mov_b32 s24, 0x3fb8aa3b\n"
        "s_mov_b32 s25, 0x4f800000\n"
        "s_mov_b32 s50, 0x3e553b95\n"
        "s_mov_b32 s27, 0\n"
        "s_mov_b32 s28, 98304\n"
        "s_mov_b32 s30, 73728\n"
        "s_mov_b32 s29, 122880\n"
        "v_mov_b32 v253, 0x3f803f80\n"
        "s_nop 0\n"
        "v_accvgpr_write_b32 a252, v253\n"
        "v_accvgpr_write_b32 a253, v253\n"
        "v_accvgpr_write_b32 a254, v253\n"
        "v_accvgpr_write_b32 a255, v253\n"
        "s_waitcnt vmcnt(12)\n"
        "v_lshlrev_b32 v32, 16, v28\n"
        "v_and_b32 v33, 0xffff0000, v28\n"
        "v_lshlrev_b32 v34, 16, v29\n"
        "v_and_b32 v35, 0xffff0000, v29\n"
        "v_mul_f32 v32, s24, v32\n"
        "v_mul_f32 v33, s24, v33\n"
        "v_mul_f32 v34, s24, v34\n"
        "v_mul_f32 v35, s24, v35\n"
        "ds_write_b32 v146, v32 offset:0\n"
        "ds_write_b32 v146, v33 offset:1024\n"
        "ds_write_b32 v146, v34 offset:2048\n"
        "ds_write_b32 v146, v35 offset:3072\n"
        "s_cmp_eq_u32 s42, 0\n"
        "s_cselect_b32 s3, 4, 0\n"
        "s_cmp_eq_u32 s2, 0\n"
        "s_cselect_b32 s6, s3, 0\n"
        "s_mov_b32 s7, 0x20000\n"
        "v_mov_b32 v67, 0\n"
        "s_add_u32 s24, s42, 4\n"
        "s_mul_i32 s24, s24, 196608\n"
        "s_lshl_b32 s34, s42, 11\n"
        "s_and_b32 s37, s42, 1\n"
        "s_add_u32 s37, s37, 1\n"
        "s_mul_i32 s37, s37, 786432\n"
        "s_waitcnt vmcnt(8) lgkmcnt(0)\n"
        "s_barrier\n"
        "s_mov_b32 s56, s2\n"
        "s_add_u32 s57, s2, 256\n"
        "s_add_u32 s58, s2, 512\n"
        "s_mov_b32 s59, -1\n"
        "s_mul_i32 s60, s2, 0x01010101\n"
        "s_mov_b32 s61, s60\n"
        "ds_read_b128 v[0:3], v140 offset:0\n"
        "ds_read_b128 v[4:7], v141 offset:0\n"
        "ds_read_b128 v[8:11], v142 offset:0\n"
        "ds_read_b128 v[12:15], v143 offset:0\n"
        "ds_read_b128 v[16:19], v140 offset:1024\n"
        "ds_read_b128 v[20:23], v141 offset:1024\n"
        "ds_read_b128 v[24:27], v142 offset:1024\n"
        "ds_read_b128 v[28:31], v143 offset:1024\n"
        "ds_read_b128 v[32:35], v140 offset:8192\n"
        "ds_read_b128 v[36:39], v141 offset:8192\n"
        "ds_read_b128 v[40:43], v142 offset:8192\n"
        "ds_read_b128 v[44:47], v143 offset:8192\n"
        "ds_read_b128 v[48:51], v140 offset:9216\n"
        "ds_read_b128 v[52:55], v141 offset:9216\n"
        "ds_read_b128 v[56:59], v142 offset:9216\n"
        "ds_read_b128 v[60:63], v143 offset:9216\n"
        "s_add_u32 m0, s40, 0\n"
        "s_add_u32 s64, s65, 0\n"
        "buffer_load_dwordx4 v128, s[20:23], s64 offen lds\n"
        "s_waitcnt lgkmcnt(15)\n"
        "v_lshl_add_u32 v0, v0, 2, s67\n"
        "v_lshl_add_u32 v1, v1, 2, s67\n"
        "v_lshl_add_u32 v2, v2, 2, s67\n"
        "v_lshl_add_u32 v3, v3, 2, s67\n"
        "s_waitcnt lgkmcnt(14)\n"
        "v_lshl_add_u32 v4, v4, 2, s67\n"
        "v_lshl_add_u32 v5, v5, 2, s67\n"
        "v_lshl_add_u32 v6, v6, 2, s67\n"
        "v_lshl_add_u32 v7, v7, 2, s67\n"
        "ds_read_b32 v0, v0\n"
        "ds_read_b32 v1, v1\n"
        "ds_read_b32 v2, v2\n"
        "ds_read_b32 v3, v3\n"
        "ds_read_b32 v4, v4\n"
        "ds_read_b32 v5, v5\n"
        "ds_read_b32 v6, v6\n"
        "ds_read_b32 v7, v7\n"
        "buffer_load_dwordx4 v129, s[20:23], s64 offen offset:1024 lds\n"
        "s_waitcnt lgkmcnt(15)\n"
        "v_lshl_add_u32 v8, v8, 2, s67\n"
        "v_lshl_add_u32 v9, v9, 2, s67\n"
        "v_lshl_add_u32 v10, v10, 2, s67\n"
        "v_lshl_add_u32 v11, v11, 2, s67\n"
        "v_lshl_add_u32 v12, v12, 2, s67\n"
        "v_lshl_add_u32 v13, v13, 2, s67\n"
        "v_lshl_add_u32 v14, v14, 2, s67\n"
        "v_lshl_add_u32 v15, v15, 2, s67\n"
        "ds_read_b32 v8, v8\n"
        "ds_read_b32 v9, v9\n"
        "ds_read_b32 v10, v10\n"
        "ds_read_b32 v11, v11\n"
        "ds_read_b32 v12, v12\n"
        "ds_read_b32 v13, v13\n"
        "ds_read_b32 v14, v14\n"
        "ds_read_b32 v15, v15\n"
        "buffer_load_dwordx4 v130, s[20:23], s64 offen offset:2048 lds\n"
        "v_lshl_add_u32 v16, v16, 2, s67\n"
        "v_lshl_add_u32 v17, v17, 2, s67\n"
        "v_lshl_add_u32 v18, v18, 2, s67\n"
        "v_lshl_add_u32 v19, v19, 2, s67\n"
        "v_lshl_add_u32 v20, v20, 2, s67\n"
        "v_lshl_add_u32 v21, v21, 2, s67\n"
        "v_lshl_add_u32 v22, v22, 2, s67\n"
        "v_lshl_add_u32 v23, v23, 2, s67\n"
        "ds_read_b32 v16, v16\n"
        "ds_read_b32 v17, v17\n"
        "ds_read_b32 v18, v18\n"
        "ds_read_b32 v19, v19\n"
        "ds_read_b32 v20, v20\n"
        "ds_read_b32 v21, v21\n"
        "ds_read_b32 v22, v22\n"
        "ds_read_b32 v23, v23\n"
        "s_add_u32 m0, s40, 12288\n"
        "s_add_u32 s64, s65, 36864\n"
        "buffer_load_dwordx4 v128, s[20:23], s64 offen lds\n"
        "v_lshl_add_u32 v24, v24, 2, s67\n"
        "v_lshl_add_u32 v25, v25, 2, s67\n"
        "v_lshl_add_u32 v26, v26, 2, s67\n"
        "v_lshl_add_u32 v27, v27, 2, s67\n"
        "v_lshl_add_u32 v28, v28, 2, s67\n"
        "v_lshl_add_u32 v29, v29, 2, s67\n"
        "v_lshl_add_u32 v30, v30, 2, s67\n"
        "v_lshl_add_u32 v31, v31, 2, s67\n"
        "ds_read_b32 v24, v24\n"
        "ds_read_b32 v25, v25\n"
        "ds_read_b32 v26, v26\n"
        "ds_read_b32 v27, v27\n"
        "ds_read_b32 v28, v28\n"
        "ds_read_b32 v29, v29\n"
        "ds_read_b32 v30, v30\n"
        "ds_read_b32 v31, v31\n"
        "buffer_load_dwordx4 v129, s[20:23], s64 offen offset:1024 lds\n"
        "buffer_load_dwordx4 v130, s[20:23], s64 offen offset:2048 lds\n"
        "s_mov_b64 exec, 0\n"
        "s_cmp_eq_u32 s42, 0\n"
        "s_cbranch_scc1 41f\n"
        "s_cmp_eq_u32 s42, 1\n"
        "s_cbranch_scc1 42f\n"
        "s_cmp_eq_u32 s42, 2\n"
        "s_cbranch_scc1 43f\n"
        "s_branch 40f\n"
        ".fill 268, 4, 0xbf800000\n"
        "40:\n"
        "s_mov_b64 exec, -1\n"
        "s_add_u32 m0, s40, 24576\n"
        "s_add_u32 s64, s65, 73728\n"
        "buffer_load_dwordx4 v128, s[20:23], s64 offen lds\n"
        "v_lshl_add_u32 v32, v32, 2, s67\n"
        "v_lshl_add_u32 v33, v33, 2, s67\n"
        "v_lshl_add_u32 v34, v34, 2, s67\n"
        "v_lshl_add_u32 v35, v35, 2, s67\n"
        "s_waitcnt lgkmcnt(15)\n"
        "v_lshl_add_u32 v36, v36, 2, s67\n"
        "v_lshl_add_u32 v37, v37, 2, s67\n"
        "v_lshl_add_u32 v38, v38, 2, s67\n"
        "v_lshl_add_u32 v39, v39, 2, s67\n"
        "ds_read_b32 v32, v32\n"
        "ds_read_b32 v33, v33\n"
        "ds_read_b32 v34, v34\n"
        "ds_read_b32 v35, v35\n"
        "ds_read_b32 v36, v36\n"
        "ds_read_b32 v37, v37\n"
        "ds_read_b32 v38, v38\n"
        "ds_read_b32 v39, v39\n"
        "buffer_load_dwordx4 v129, s[20:23], s64 offen offset:1024 lds\n"
        "v_lshl_add_u32 v40, v40, 2, s67\n"
        "v_lshl_add_u32 v41, v41, 2, s67\n"
        "v_lshl_add_u32 v42, v42, 2, s67\n"
        "v_lshl_add_u32 v43, v43, 2, s67\n"
        "v_lshl_add_u32 v44, v44, 2, s67\n"
        "v_lshl_add_u32 v45, v45, 2, s67\n"
        "v_lshl_add_u32 v46, v46, 2, s67\n"
        "v_lshl_add_u32 v47, v47, 2, s67\n"
        "ds_read_b32 v40, v40\n"
        "ds_read_b32 v41, v41\n"
        "ds_read_b32 v42, v42\n"
        "ds_read_b32 v43, v43\n"
        "ds_read_b32 v44, v44\n"
        "ds_read_b32 v45, v45\n"
        "ds_read_b32 v46, v46\n"
        "ds_read_b32 v47, v47\n"
        "buffer_load_dwordx4 v130, s[20:23], s64 offen offset:2048 lds\n"
        "v_lshl_add_u32 v48, v48, 2, s67\n"
        "v_lshl_add_u32 v49, v49, 2, s67\n"
        "v_lshl_add_u32 v50, v50, 2, s67\n"
        "v_lshl_add_u32 v51, v51, 2, s67\n"
        "v_lshl_add_u32 v52, v52, 2, s67\n"
        "v_lshl_add_u32 v53, v53, 2, s67\n"
        "v_lshl_add_u32 v54, v54, 2, s67\n"
        "v_lshl_add_u32 v55, v55, 2, s67\n"
        "ds_read_b32 v48, v48\n"
        "ds_read_b32 v49, v49\n"
        "ds_read_b32 v50, v50\n"
        "ds_read_b32 v51, v51\n"
        "ds_read_b32 v52, v52\n"
        "ds_read_b32 v53, v53\n"
        "ds_read_b32 v54, v54\n"
        "ds_read_b32 v55, v55\n"
        "s_add_u32 m0, s40, 36864\n"
        "s_add_u32 s64, s65, 110592\n"
        "buffer_load_dwordx4 v128, s[20:23], s64 offen lds\n"
        "v_lshl_add_u32 v56, v56, 2, s67\n"
        "v_lshl_add_u32 v57, v57, 2, s67\n"
        "v_lshl_add_u32 v58, v58, 2, s67\n"
        "v_lshl_add_u32 v59, v59, 2, s67\n"
        "v_lshl_add_u32 v60, v60, 2, s67\n"
        "v_lshl_add_u32 v61, v61, 2, s67\n"
        "v_lshl_add_u32 v62, v62, 2, s67\n"
        "v_lshl_add_u32 v63, v63, 2, s67\n"
        "ds_read_b32 v56, v56\n"
        "ds_read_b32 v57, v57\n"
        "ds_read_b32 v58, v58\n"
        "ds_read_b32 v59, v59\n"
        "ds_read_b32 v60, v60\n"
        "ds_read_b32 v61, v61\n"
        "ds_read_b32 v62, v62\n"
        "ds_read_b32 v63, v63\n"
        "buffer_load_dwordx4 v129, s[20:23], s64 offen offset:1024 lds\n"
        "buffer_load_dwordx4 v130, s[20:23], s64 offen offset:2048 lds\n"
        "s_waitcnt vmcnt(14)\n"
        "s_barrier\n"
        "s_add_u32 m0, s40, 49152\n"
        "s_add_u32 s64, s65, 147456\n"
        "buffer_load_dwordx4 v128, s[20:23], s64 offen lds\n"
        "ds_read_b128 a[144:147], v240 offset:0\n"
        "ds_read_b128 a[148:151], v241 offset:0\n"
        "ds_read_b128 a[160:163], v240 offset:1536\n"
        "ds_read_b128 a[164:167], v241 offset:1536\n"
        "buffer_load_dwordx4 v129, s[20:23], s64 offen offset:1024 lds\n"
        "ds_read_b128 a[176:179], v240 offset:12288\n"
        "ds_read_b128 a[180:183], v241 offset:12288\n"
        "ds_read_b128 a[192:195], v240 offset:13824\n"
        "ds_read_b128 a[196:199], v241 offset:13824\n"
        "buffer_load_dwordx4 v130, s[20:23], s64 offen offset:2048 lds\n"
        "ds_read_b128 a[152:155], v240 offset:128\n"
        "ds_read_b128 a[156:159], v241 offset:128\n"
        "ds_read_b128 a[168:171], v240 offset:1664\n"
        "ds_read_b128 a[172:175], v241 offset:1664\n"
        "s_add_u32 m0, s40, 61440\n"
        "s_add_u32 s64, s65, 184320\n"
        "buffer_load_dwordx4 v128, s[20:23], s64 offen lds\n"
        "ds_read_b128 a[184:187], v240 offset:12416\n"
        "ds_read_b128 a[188:191], v241 offset:12416\n"
        "ds_read_b128 a[200:203], v240 offset:13952\n"
        "ds_read_b128 a[204:207], v241 offset:13952\n"
        "buffer_load_dwordx4 v129, s[20:23], s64 offen offset:1024 lds\n"
        "ds_read_b128 a[216:219], v240 offset:256\n"
        "ds_read_b128 a[220:223], v241 offset:256\n"
        "ds_read_b128 a[224:227], v240 offset:1792\n"
        "ds_read_b128 a[228:231], v241 offset:1792\n"
        "buffer_load_dwordx4 v130, s[20:23], s64 offen offset:2048 lds\n"
        "ds_read_b128 a[232:235], v240 offset:12544\n"
        "ds_read_b128 a[236:239], v241 offset:12544\n"
        "ds_read_b128 a[240:243], v240 offset:14080\n"
        "ds_read_b128 a[244:247], v241 offset:14080\n"
        "s_lshr_b32 s67, s42, 1\n"
        "s_add_u32 s67, s67, s40\n"
        "s_sub_u32 s67, -2, s67\n"
        "s_waitcnt vmcnt(12)\n"
        "s_barrier\n"
        "ds_read_b64_tr_b16 a[0:1], v131 offset:0\n"
        "ds_read_b64_tr_b16 a[2:3], v131 offset:1536\n"
        "ds_read_b64_tr_b16 a[4:5], v131 offset:12288\n"
        "ds_read_b64_tr_b16 a[6:7], v131 offset:13824\n"
        "ds_read_b64_tr_b16 a[24:25], v132 offset:0\n"
        "ds_read_b64_tr_b16 a[26:27], v132 offset:1536\n"
        "ds_read_b64_tr_b16 a[28:29], v132 offset:12288\n"
        "ds_read_b64_tr_b16 a[30:31], v132 offset:13824\n"
        "ds_read_b64_tr_b16 a[48:49], v133 offset:0\n"
        "ds_read_b64_tr_b16 a[50:51], v133 offset:1536\n"
        "ds_read_b64_tr_b16 a[52:53], v133 offset:12288\n"
        "ds_read_b64_tr_b16 a[54:55], v133 offset:13824\n"
        "s_waitcnt lgkmcnt(10)\n"
        "v_mfma_f32_16x16x32_bf16 v[96:99], a[0:3], a[144:147], 0\n"
        "s_waitcnt lgkmcnt(8)\n"
        "s_add_u32 m0, s40, 98304\n"
        "s_add_u32 s64, s65, 384\n"
        "v_mfma_f32_16x16x32_bf16 v[96:99], a[4:7], a[148:151], v[96:99]\n"
        "s_waitcnt lgkmcnt(4)\n"
        "buffer_load_dwordx4 v128, s[20:23], s64 offen lds\n"
        "v_mfma_f32_16x16x32_bf16 v[100:103], a[28:31], a[148:151], 0\n"
        "v_mfma_f32_16x16x32_bf16 v[100:103], a[24:27], a[144:147], v[100:103]\n"
        "s_waitcnt lgkmcnt(2)\n"
        "v_mfma_f32_16x16x32_bf16 v[104:107], a[48:51], a[144:147], 0\n"
        "s_waitcnt lgkmcnt(0)\n"
        "v_mfma_f32_16x16x32_bf16 v[104:107], a[52:55], a[148:151], v[104:107]\n"
        "buffer_load_dwordx4 v129, s[20:23], s64 offen offset:1024 lds\n"
        "v_mfma_f32_16x16x32_bf16 v[108:111], a[4:7], a[164:167], 0\n"
        "v_mfma_f32_16x16x32_bf16 v[108:111], a[0:3], a[160:163], v[108:111]\n"
        "v_mfma_f32_16x16x32_bf16 v[112:115], a[24:27], a[160:163], 0\n"
        "v_mfma_f32_16x16x32_bf16 v[112:115], a[28:31], a[164:167], v[112:115]\n"
        "buffer_load_dwordx4 v130, s[20:23], s64 offen offset:2048 lds\n"
        "v_mfma_f32_16x16x32_bf16 v[116:119], a[52:55], a[164:167], 0\n"
        "s_add_u32 m0, s40, 110592\n"
        "v_mfma_f32_16x16x32_bf16 v[116:119], a[48:51], a[160:163], v[116:119]\n"
        "s_add_u32 s64, s65, 37248\n"
        "v_mfma_f32_16x16x32_bf16 v[120:123], a[0:3], a[176:179], 0\n"
        "v_mfma_f32_16x16x32_bf16 v[120:123], a[4:7], a[180:183], v[120:123]\n"
        "s_waitcnt vmcnt(9)\n"
        "buffer_load_dwordx4 v128, s[20:23], s64 offen lds\n"
        "v_mfma_f32_16x16x32_bf16 v[92:95], a[28:31], a[180:183], 0\n"
        "s_barrier\n"
        "ds_read_b64_tr_b16 a[8:9], v131 offset:24576\n"
        "v_mfma_f32_16x16x32_bf16 v[92:95], a[24:27], a[176:179], v[92:95]\n"
        "ds_read_b64_tr_b16 a[10:11], v131 offset:26112\n"
        "v_mfma_f32_16x16x32_bf16 v[152:155], a[48:51], a[176:179], 0\n"
        "ds_read_b64_tr_b16 a[12:13], v131 offset:36864\n"
        "v_mfma_f32_16x16x32_bf16 v[152:155], a[52:55], a[180:183], v[152:155]\n"
        "buffer_load_dwordx4 v129, s[20:23], s64 offen offset:1024 lds\n"
        "v_mfma_f32_16x16x32_bf16 v[172:175], a[4:7], a[196:199], 0\n"
        "ds_read_b64_tr_b16 a[14:15], v131 offset:38400\n"
        "ds_read_b64_tr_b16 a[32:33], v132 offset:24576\n"
        "v_mfma_f32_16x16x32_bf16 v[172:175], a[0:3], a[192:195], v[172:175]\n"
        "ds_read_b64_tr_b16 a[34:35], v132 offset:26112\n"
        "v_mfma_f32_16x16x32_bf16 v[192:195], a[24:27], a[192:195], 0\n"
        "ds_read_b64_tr_b16 a[36:37], v132 offset:36864\n"
        "v_mfma_f32_16x16x32_bf16 v[192:195], a[28:31], a[196:199], v[192:195]\n"
        "buffer_load_dwordx4 v130, s[20:23], s64 offen offset:2048 lds\n"
        "v_mfma_f32_16x16x32_bf16 v[212:215], a[52:55], a[196:199], 0\n"
        "s_add_u32 m0, s40, 0\n"
        "ds_read_b64_tr_b16 a[38:39], v132 offset:38400\n"
        "ds_read_b64_tr_b16 a[56:57], v133 offset:24576\n"
        "v_mfma_f32_16x16x32_bf16 v[212:215], a[48:51], a[192:195], v[212:215]\n"
        "s_waitcnt lgkmcnt(7)\n"
        "s_add_u32 s64, s65, 74112\n"
        "ds_read_b64_tr_b16 a[58:59], v133 offset:26112\n"
        "v_mfma_f32_16x16x32_bf16 v[96:99], a[8:11], a[152:155], v[96:99]\n"
        "s_waitcnt lgkmcnt(6)\n"
        "ds_read_b64_tr_b16 a[60:61], v133 offset:36864\n"
        "v_mfma_f32_16x16x32_bf16 v[96:99], a[12:15], a[156:159], v[96:99]\n"
        "s_waitcnt lgkmcnt(3)\n"
        "ds_read_b64_tr_b16 a[62:63], v133 offset:38400\n"
        "v_mfma_f32_16x16x32_bf16 v[100:103], a[36:39], a[156:159], v[100:103]\n"
        "buffer_load_dwordx4 v128, s[20:23], s64 offen lds\n"
        "v_mfma_f32_16x16x32_bf16 v[100:103], a[32:35], a[152:155], v[100:103]\n"
        "s_waitcnt lgkmcnt(2)\n"
        "v_mfma_f32_16x16x32_bf16 v[104:107], a[56:59], a[152:155], v[104:107]\n"
        "s_waitcnt lgkmcnt(0)\n"
        "v_mfma_f32_16x16x32_bf16 v[104:107], a[60:63], a[156:159], v[104:107]\n"
        "buffer_load_dwordx4 v129, s[20:23], s64 offen offset:1024 lds\n"
        "v_mfma_f32_16x16x32_bf16 v[108:111], a[12:15], a[172:175], v[108:111]\n"
        "v_mfma_f32_16x16x32_bf16 v[108:111], a[8:11], a[168:171], v[108:111]\n"
        "buffer_atomic_swap v67, v249, s[4:7], 0 offen sc1\n"
        "v_mfma_f32_16x16x32_bf16 v[112:115], a[32:35], a[168:171], v[112:115]\n"
        "s_mov_b32 s6, 0\n"
        "v_mfma_f32_16x16x32_bf16 v[112:115], a[36:39], a[172:175], v[112:115]\n"
        "buffer_load_dwordx4 v130, s[20:23], s64 offen offset:2048 lds\n"
        "v_mfma_f32_16x16x32_bf16 v[116:119], a[60:63], a[172:175], v[116:119]\n"
        "s_add_u32 m0, s40, 12288\n"
        "v_mfma_f32_16x16x32_bf16 v[116:119], a[56:59], a[168:171], v[116:119]\n"
        "s_add_u32 s64, s65, 110976\n"
        "v_mfma_f32_16x16x32_bf16 v[120:123], a[8:11], a[184:187], v[120:123]\n"
        "s_waitcnt vmcnt(10)\n"
        "v_mfma_f32_16x16x32_bf16 v[120:123], a[12:15], a[188:191], v[120:123]\n"
        "s_barrier\n"
        "buffer_load_dwordx4 v128, s[20:23], s64 offen lds\n"
        "v_mfma_f32_16x16x32_bf16 v[92:95], a[36:39], a[188:191], v[92:95]\n"
        "ds_read_b64_tr_b16 a[16:17], v131 offset:49152\n"
        "ds_read_b64_tr_b16 a[18:19], v131 offset:50688\n"
        "v_mfma_f32_16x16x32_bf16 v[92:95], a[32:35], a[184:187], v[92:95]\n"
        "ds_read_b64_tr_b16 a[20:21], v131 offset:61440\n"
        "v_mfma_f32_16x16x32_bf16 v[152:155], a[56:59], a[184:187], v[152:155]\n"
        "ds_read_b64_tr_b16 a[22:23], v131 offset:62976\n"
        "v_mfma_f32_16x16x32_bf16 v[152:155], a[60:63], a[188:191], v[152:155]\n"
        "buffer_load_dwordx4 v129, s[20:23], s64 offen offset:1024 lds\n"
        "v_mfma_f32_16x16x32_bf16 v[172:175], a[12:15], a[204:207], v[172:175]\n"
        "ds_read_b64_tr_b16 a[40:41], v132 offset:49152\n"
        "ds_read_b64_tr_b16 a[42:43], v132 offset:50688\n"
        "v_mfma_f32_16x16x32_bf16 v[172:175], a[8:11], a[200:203], v[172:175]\n"
        "ds_read_b64_tr_b16 a[44:45], v132 offset:61440\n"
        "v_mfma_f32_16x16x32_bf16 v[192:195], a[32:35], a[200:203], v[192:195]\n"
        "ds_read_b64_tr_b16 a[46:47], v132 offset:62976\n"
        "v_mfma_f32_16x16x32_bf16 v[192:195], a[36:39], a[204:207], v[192:195]\n"
        "buffer_load_dwordx4 v130, s[20:23], s64 offen offset:2048 lds\n"
        "v_mfma_f32_16x16x32_bf16 v[212:215], a[60:63], a[204:207], v[212:215]\n"
        "s_add_u32 m0, s40, 24576\n"
        "ds_read_b64_tr_b16 a[64:65], v133 offset:49152\n"
        "ds_read_b64_tr_b16 a[66:67], v133 offset:50688\n"
        "v_mfma_f32_16x16x32_bf16 v[212:215], a[56:59], a[200:203], v[212:215]\n"
        "s_waitcnt lgkmcnt(8)\n"
        "s_add_u32 s64, s65, 147840\n"
        "ds_read_b64_tr_b16 a[68:69], v133 offset:61440\n"
        "v_mfma_f32_16x16x32_bf16 v[96:99], a[16:19], a[216:219], v[96:99]\n"
        "s_waitcnt lgkmcnt(7)\n"
        "ds_read_b64_tr_b16 a[70:71], v133 offset:62976\n"
        "v_mfma_f32_16x16x32_bf16 v[96:99], a[20:23], a[220:223], v[96:99]\n"
        "s_waitcnt lgkmcnt(4)\n"
        "buffer_load_dwordx4 v128, s[20:23], s64 offen lds\n"
        "v_mfma_f32_16x16x32_bf16 v[100:103], a[44:47], a[220:223], v[100:103]\n"
        "v_mfma_f32_16x16x32_bf16 v[100:103], a[40:43], a[216:219], v[100:103]\n"
        "s_waitcnt lgkmcnt(2)\n"
        "v_mfma_f32_16x16x32_bf16 v[104:107], a[64:67], a[216:219], v[104:107]\n"
        "s_waitcnt lgkmcnt(0)\n"
        "v_mfma_f32_16x16x32_bf16 v[104:107], a[68:71], a[220:223], v[104:107]\n"
        "buffer_load_dwordx4 v129, s[20:23], s64 offen offset:1024 lds\n"
        "v_mfma_f32_16x16x32_bf16 v[108:111], a[20:23], a[228:231], v[108:111]\n"
        "v_mfma_f32_16x16x32_bf16 v[108:111], a[16:19], a[224:227], v[108:111]\n"
        "v_mul_f32 v96, s50, v96\n"
        "v_mul_f32 v97, s50, v97\n"
        "v_mfma_f32_16x16x32_bf16 v[112:115], a[40:43], a[224:227], v[112:115]\n"
        "v_mul_f32 v98, s50, v98\n"
        "v_mul_f32 v99, s50, v99\n"
        "v_mfma_f32_16x16x32_bf16 v[112:115], a[44:47], a[228:231], v[112:115]\n"
        "buffer_load_dwordx4 v130, s[20:23], s64 offen offset:2048 lds\n"
        "v_mfma_f32_16x16x32_bf16 v[116:119], a[68:71], a[228:231], v[116:119]\n"
        "s_add_u32 m0, s40, 36864\n"
        "v_cvt_pk_bf16_f32 v156, v96, v97\n"
        "v_cvt_pk_bf16_f32 v157, v98, v99\n"
        "v_mfma_f32_16x16x32_bf16 v[116:119], a[64:67], a[224:227], v[116:119]\n"
        "s_add_u32 s64, s65, 184704\n"
        "v_mul_f32 v100, s50, v100\n"
        "v_mul_f32 v101, s50, v101\n"
        "v_mfma_f32_16x16x32_bf16 v[120:123], a[16:19], a[232:235], v[120:123]\n"
        "s_waitcnt vmcnt(10)\n"
        "s_barrier\n"
        "ds_read_b64_tr_b16 a[72:73], v136 offset:0\n"
        "v_mul_f32 v102, s50, v102\n"
        "v_mfma_f32_16x16x32_bf16 v[120:123], a[20:23], a[236:239], v[120:123]\n"
        "buffer_load_dwordx4 v128, s[20:23], s64 offen lds\n"
        "v_mfma_f32_16x16x32_bf16 v[92:95], a[44:47], a[236:239], v[92:95]\n"
        "ds_read_b64_tr_b16 a[74:75], v136 offset:1536\n"
        "ds_read_b64_tr_b16 a[76:77], v136 offset:12288\n"
        "v_mfma_f32_16x16x32_bf16 v[92:95], a[40:43], a[232:235], v[92:95]\n"
        "ds_read_b64_tr_b16 a[78:79], v136 offset:13824\n"
        "v_mul_f32 v103, s50, v103\n"
        "v_mfma_f32_16x16x32_bf16 v[152:155], a[64:67], a[232:235], v[152:155]\n"
        "ds_read_b64_tr_b16 a[96:97], v137 offset:0\n"
        "v_cvt_pk_bf16_f32 v158, v100, v101\n"
        "v_mfma_f32_16x16x32_bf16 v[152:155], a[68:71], a[236:239], v[152:155]\n"
        "buffer_load_dwordx4 v129, s[20:23], s64 offen offset:1024 lds\n"
        "v_mfma_f32_16x16x32_bf16 v[172:175], a[20:23], a[244:247], v[172:175]\n"
        "ds_read_b64_tr_b16 a[98:99], v137 offset:1536\n"
        "ds_read_b64_tr_b16 a[100:101], v137 offset:12288\n"
        "v_mfma_f32_16x16x32_bf16 v[172:175], a[16:19], a[240:243], v[172:175]\n"
        "ds_read_b64_tr_b16 a[102:103], v137 offset:13824\n"
        "v_cvt_pk_bf16_f32 v159, v102, v103\n"
        "v_mfma_f32_16x16x32_bf16 v[192:195], a[40:43], a[240:243], v[192:195]\n"
        "ds_read_b64_tr_b16 a[120:121], v138 offset:0\n"
        "v_mul_f32 v104, s50, v104\n"
        "v_mfma_f32_16x16x32_bf16 v[192:195], a[44:47], a[244:247], v[192:195]\n"
        "buffer_load_dwordx4 v130, s[20:23], s64 offen offset:2048 lds\n"
        "v_mfma_f32_16x16x32_bf16 v[212:215], a[68:71], a[244:247], v[212:215]\n"
        "s_add_u32 m0, s40, 49152\n"
        "ds_read_b64_tr_b16 a[122:123], v138 offset:1536\n"
        "ds_read_b64_tr_b16 a[124:125], v138 offset:12288\n"
        "v_mfma_f32_16x16x32_bf16 v[212:215], a[64:67], a[240:243], v[212:215]\n"
        "s_waitcnt lgkmcnt(9)\n"
        "s_add_u32 s64, s65, 768\n"
        "ds_read_b64_tr_b16 a[126:127], v138 offset:13824\n"
        "v_mul_f32 v105, s50, v105\n"
        "v_mfma_f32_16x16x32_bf16 v[64:67], a[72:75], a[144:147], 0\n"
        "s_waitcnt lgkmcnt(8)\n"
        "v_mul_f32 v106, s50, v106\n"
        "v_mul_f32 v107, s50, v107\n"
        "v_mfma_f32_16x16x32_bf16 v[64:67], a[76:79], a[148:151], v[64:67]\n"
        "s_waitcnt lgkmcnt(4)\n"
        "buffer_load_dwordx4 v128, s[20:23], s64 offen lds\n"
        "v_mfma_f32_16x16x32_bf16 v[68:71], a[100:103], a[148:151], 0\n"
        "v_cvt_pk_bf16_f32 v160, v104, v105\n"
        "v_cvt_pk_bf16_f32 v161, v106, v107\n"
        "v_mfma_f32_16x16x32_bf16 v[68:71], a[96:99], a[144:147], v[68:71]\n"
        "s_waitcnt lgkmcnt(2)\n"
        "v_mul_f32 v108, s50, v108\n"
        "v_mul_f32 v109, s50, v109\n"
        "v_mfma_f32_16x16x32_bf16 v[72:75], a[120:123], a[144:147], 0\n"
        "s_waitcnt lgkmcnt(0)\n"
        "v_mov_b64 v[166:167], v[160:161]\n"
        "v_mul_f32 v110, s50, v110\n"
        "v_mfma_f32_16x16x32_bf16 v[72:75], a[124:127], a[148:151], v[72:75]\n"
        "buffer_load_dwordx4 v129, s[20:23], s64 offen offset:1024 lds\n"
        "v_mfma_f32_16x16x32_bf16 v[76:79], a[76:79], a[164:167], 0\n"
        "v_mul_f32 v111, s50, v111\n"
        "v_cvt_pk_bf16_f32 v176, v108, v109\n"
        "v_mfma_f32_16x16x32_bf16 v[76:79], a[72:75], a[160:163], v[76:79]\n"
        "v_cvt_pk_bf16_f32 v177, v110, v111\n"
        "v_mul_f32 v112, s50, v112\n"
        "v_mfma_f32_16x16x32_bf16 v[80:83], a[96:99], a[160:163], 0\n"
        "v_mul_f32 v113, s50, v113\n"
        "v_mul_f32 v114, s50, v114\n"
        "v_mfma_f32_16x16x32_bf16 v[80:83], a[100:103], a[164:167], v[80:83]\n"
        "s_waitcnt vmcnt(8)\n"
        "buffer_load_dwordx4 v130, s[20:23], s64 offen offset:2048 lds\n"
        "v_mfma_f32_16x16x32_bf16 v[84:87], a[124:127], a[164:167], 0\n"
        "s_barrier\n"
        "ds_read_b64_tr_b16 a[80:81], v131 offset:0\n"
        "v_mul_f32 v115, s50, v115\n"
        "v_mfma_f32_16x16x32_bf16 v[84:87], a[120:123], a[160:163], v[84:87]\n"
        "s_add_u32 m0, s40, 61440\n"
        "ds_read_b64_tr_b16 a[82:83], v131 offset:1536\n"
        "v_cvt_pk_bf16_f32 v178, v112, v113\n"
        "v_mfma_f32_16x16x32_bf16 v[88:91], a[72:75], a[176:179], 0\n"
        "s_add_u32 s64, s65, 37632\n"
        "ds_read_b64_tr_b16 a[84:85], v131 offset:12288\n"
        "v_cvt_pk_bf16_f32 v179, v114, v115\n"
        "v_mfma_f32_16x16x32_bf16 v[88:91], a[76:79], a[180:183], v[88:91]\n"
        "buffer_load_dwordx4 v128, s[20:23], s64 offen lds\n"
        "v_mfma_f32_16x16x32_bf16 v[108:111], a[100:103], a[180:183], 0\n"
        "ds_read_b64_tr_b16 a[86:87], v131 offset:13824\n"
        "ds_read_b64_tr_b16 a[104:105], v132 offset:0\n"
        "v_mfma_f32_16x16x32_bf16 v[108:111], a[96:99], a[176:179], v[108:111]\n"
        "ds_read_b64_tr_b16 a[106:107], v132 offset:1536\n"
        "v_mul_f32 v116, s50, v116\n"
        "v_mfma_f32_16x16x32_bf16 v[112:115], a[120:123], a[176:179], 0\n"
        "ds_read_b64_tr_b16 a[108:109], v132 offset:12288\n"
        "v_mul_f32 v117, s50, v117\n"
        "v_mfma_f32_16x16x32_bf16 v[112:115], a[124:127], a[180:183], v[112:115]\n"
        "buffer_load_dwordx4 v129, s[20:23], s64 offen offset:1024 lds\n"
        "v_mfma_f32_16x16x32_bf16 v[96:99], a[76:79], a[196:199], 0\n"
        "ds_read_b64_tr_b16 a[110:111], v132 offset:13824\n"
        "ds_read_b64_tr_b16 a[128:129], v133 offset:0\n"
        "v_mfma_f32_16x16x32_bf16 v[96:99], a[72:75], a[192:195], v[96:99]\n"
        "ds_read_b64_tr_b16 a[130:131], v133 offset:1536\n"
        "v_mul_f32 v118, s50, v118\n"
        "v_mfma_f32_16x16x32_bf16 v[100:103], a[96:99], a[192:195], 0\n"
        "ds_read_b64_tr_b16 a[132:133], v133 offset:12288\n"
        "v_mul_f32 v119, s50, v119\n"
        "v_mfma_f32_16x16x32_bf16 v[100:103], a[100:103], a[196:199], v[100:103]\n"
        "buffer_load_dwordx4 v130, s[20:23], s64 offen offset:2048 lds\n"
        "v_mfma_f32_16x16x32_bf16 v[104:107], a[124:127], a[196:199], 0\n"
        "ds_read_b64_tr_b16 a[134:135], v133 offset:13824\n"
        "v_cvt_pk_bf16_f32 v180, v116, v117\n"
        "v_mfma_f32_16x16x32_bf16 v[104:107], a[120:123], a[192:195], v[104:107]\n"
        "s_waitcnt lgkmcnt(10)\n"
        "v_cvt_pk_bf16_f32 v181, v118, v119\n"
        "v_mul_f32 v120, s50, v120\n"
        "v_mfma_f32_16x16x32_bf16 v[64:67], a[80:83], a[152:155], v[64:67]\n"
        "s_waitcnt lgkmcnt(8)\n"
        "v_mul_f32 v121, s50, v121\n"
        "v_mov_b64 v[186:187], v[180:181]\n"
        "v_mfma_f32_16x16x32_bf16 v[64:67], a[84:87], a[156:159], v[64:67]\n"
        "s_waitcnt lgkmcnt(4)\n"
        "v_mul_f32 v122, s50, v122\n"
        "v_mul_f32 v123, s50, v123\n"
        "v_mfma_f32_16x16x32_bf16 v[68:71], a[108:111], a[156:159], v[68:71]\n"
        "v_cvt_pk_bf16_f32 v196, v120, v121\n"
        "v_cvt_pk_bf16_f32 v197, v122, v123\n"
        "v_mfma_f32_16x16x32_bf16 v[68:71], a[104:107], a[152:155], v[68:71]\n"
        "s_waitcnt lgkmcnt(2)\n"
        "v_mul_f32 v92, s50, v92\n"
        "v_mul_f32 v93, s50, v93\n"
        "v_mfma_f32_16x16x32_bf16 v[72:75], a[128:131], a[152:155], v[72:75]\n"
        "s_waitcnt lgkmcnt(0)\n"
        "v_mul_f32 v94, s50, v94\n"
        "v_mul_f32 v95, s50, v95\n"
        "v_mfma_f32_16x16x32_bf16 v[72:75], a[132:135], a[156:159], v[72:75]\n"
        "v_cvt_pk_bf16_f32 v198, v92, v93\n"
        "v_cvt_pk_bf16_f32 v199, v94, v95\n"
        "v_mfma_f32_16x16x32_bf16 v[76:79], a[84:87], a[172:175], v[76:79]\n"
        "v_mul_f32 v152, s50, v152\n"
        "v_mul_f32 v153, s50, v153\n"
        "v_mfma_f32_16x16x32_bf16 v[76:79], a[80:83], a[168:171], v[76:79]\n"
        "v_mul_f32 v154, s50, v154\n"
        "v_mul_f32 v155, s50, v155\n"
        "v_mfma_f32_16x16x32_bf16 v[80:83], a[104:107], a[168:171], v[80:83]\n"
        "s_waitcnt vmcnt(6)\n"
        "v_cvt_pk_bf16_f32 v200, v152, v153\n"
        "v_cvt_pk_bf16_f32 v201, v154, v155\n"
        "v_mfma_f32_16x16x32_bf16 v[80:83], a[108:111], a[172:175], v[80:83]\n"
        "s_barrier\n"
        "s_add_u32 m0, s40, 0\n"
        "ds_read_b64_tr_b16 a[88:89], v131 offset:24576\n"
        "v_mfma_f32_16x16x32_bf16 v[84:87], a[132:135], a[172:175], v[84:87]\n"
        "s_add_u32 s64, s65, 74496\n"
        "ds_read_b64_tr_b16 a[90:91], v131 offset:26112\n"
        "v_mul_f32 v172, s50, v172\n"
        "v_mfma_f32_16x16x32_bf16 v[84:87], a[128:131], a[168:171], v[84:87]\n"
        "ds_read_b64_tr_b16 a[92:93], v131 offset:36864\n"
        "v_mul_f32 v173, s50, v173\n"
        "v_mfma_f32_16x16x32_bf16 v[88:91], a[80:83], a[184:187], v[88:91]\n"
        "buffer_load_dwordx4 v128, s[20:23], s64 offen lds\n"
        "v_mfma_f32_16x16x32_bf16 v[88:91], a[84:87], a[188:191], v[88:91]\n"
        "ds_read_b64_tr_b16 a[94:95], v131 offset:38400\n"
        "ds_read_b64_tr_b16 a[112:113], v132 offset:24576\n"
        "v_mfma_f32_16x16x32_bf16 v[108:111], a[108:111], a[188:191], v[108:111]\n"
        "ds_read_b64_tr_b16 a[114:115], v132 offset:26112\n"
        "v_mov_b64 v[206:207], v[200:201]\n"
        "v_mfma_f32_16x16x32_bf16 v[108:111], a[104:107], a[184:187], v[108:111]\n"
        "ds_read_b64_tr_b16 a[116:117], v132 offset:36864\n"
        "v_mul_f32 v174, s50, v174\n"
        "v_mfma_f32_16x16x32_bf16 v[112:115], a[128:131], a[184:187], v[112:115]\n"
        "buffer_load_dwordx4 v129, s[20:23], s64 offen offset:1024 lds\n"
        "v_mfma_f32_16x16x32_bf16 v[112:115], a[132:135], a[188:191], v[112:115]\n"
        "ds_read_b64_tr_b16 a[118:119], v132 offset:38400\n"
        "ds_read_b64_tr_b16 a[136:137], v133 offset:24576\n"
        "v_mfma_f32_16x16x32_bf16 v[96:99], a[84:87], a[204:207], v[96:99]\n"
        "ds_read_b64_tr_b16 a[138:139], v133 offset:26112\n"
        "v_mul_f32 v175, s50, v175\n"
        "v_mfma_f32_16x16x32_bf16 v[96:99], a[80:83], a[200:203], v[96:99]\n"
        "ds_read_b64_tr_b16 a[140:141], v133 offset:36864\n"
        "v_cvt_pk_bf16_f32 v216, v172, v173\n"
        "v_mfma_f32_16x16x32_bf16 v[100:103], a[104:107], a[200:203], v[100:103]\n"
        "buffer_load_dwordx4 v130, s[20:23], s64 offen offset:2048 lds\n"
        "v_mfma_f32_16x16x32_bf16 v[100:103], a[108:111], a[204:207], v[100:103]\n"
        "s_add_u32 m0, s40, 12288\n"
        "ds_read_b64_tr_b16 a[142:143], v133 offset:38400\n"
        "v_cvt_pk_bf16_f32 v217, v174, v175\n"
        "v_mfma_f32_16x16x32_bf16 v[104:107], a[132:135], a[204:207], v[104:107]\n"
        "s_add_u32 s64, s65, 111360\n"
        "v_mul_f32 v192, s50, v192\n"
        "v_mul_f32 v193, s50, v193\n"
        "v_mfma_f32_16x16x32_bf16 v[104:107], a[128:131], a[200:203], v[104:107]\n"
        "s_waitcnt lgkmcnt(10)\n"
        "buffer_load_dwordx4 v128, s[20:23], s64 offen lds\n"
        "v_mfma_f32_16x16x32_bf16 v[64:67], a[88:91], a[216:219], v[64:67]\n"
        "s_waitcnt lgkmcnt(8)\n"
        "v_mul_f32 v194, s50, v194\n"
        "v_mul_f32 v195, s50, v195\n"
        "v_mfma_f32_16x16x32_bf16 v[64:67], a[92:95], a[220:223], v[64:67]\n"
        "s_waitcnt lgkmcnt(4)\n"
        "v_cvt_pk_bf16_f32 v218, v192, v193\n"
        "v_cvt_pk_bf16_f32 v219, v194, v195\n"
        "v_mfma_f32_16x16x32_bf16 v[68:71], a[116:119], a[220:223], v[68:71]\n"
        "v_mul_f32 v212, s50, v212\n"
        "v_mul_f32 v213, s50, v213\n"
        "v_mfma_f32_16x16x32_bf16 v[68:71], a[112:115], a[216:219], v[68:71]\n"
        "s_waitcnt lgkmcnt(2)\n"
        "buffer_load_dwordx4 v129, s[20:23], s64 offen offset:1024 lds\n"
        "v_mfma_f32_16x16x32_bf16 v[72:75], a[136:139], a[216:219], v[72:75]\n"
        "s_waitcnt lgkmcnt(0)\n"
        "v_mul_f32 v214, s50, v214\n"
        "v_mul_f32 v215, s50, v215\n"
        "v_mfma_f32_16x16x32_bf16 v[72:75], a[140:143], a[220:223], v[72:75]\n"
        "v_cvt_pk_bf16_f32 v220, v212, v213\n"
        "v_cvt_pk_bf16_f32 v221, v214, v215\n"
        "v_mfma_f32_16x16x32_bf16 v[76:79], a[92:95], a[228:231], v[76:79]\n"
        "v_mov_b64 v[226:227], v[220:221]\n"
        "v_cvt_pk_bf16_f32 v152, v64, v65\n"
        "v_mfma_f32_16x16x32_bf16 v[76:79], a[88:91], a[224:227], v[76:79]\n"
        "buffer_load_dwordx4 v130, s[20:23], s64 offen offset:2048 lds\n"
        "v_mfma_f32_16x16x32_bf16 v[80:83], a[112:115], a[224:227], v[80:83]\n"
        "v_cvt_pk_bf16_f32 v153, v66, v67\n"
        "v_cvt_pk_bf16_f32 v154, v68, v69\n"
        "v_mfma_f32_16x16x32_bf16 v[80:83], a[116:119], a[228:231], v[80:83]\n"
        "v_cvt_pk_bf16_f32 v155, v70, v71\n"
        "v_cvt_pk_bf16_f32 v168, v72, v73\n"
        "v_mfma_f32_16x16x32_bf16 v[84:87], a[140:143], a[228:231], v[84:87]\n"
        "v_cvt_pk_bf16_f32 v169, v74, v75\n"
        "v_cvt_pk_bf16_f32 v172, v76, v77\n"
        "v_mfma_f32_16x16x32_bf16 v[84:87], a[136:139], a[224:227], v[84:87]\n"
        "s_waitcnt vmcnt(6)\n"
        "ds_read_b128 v[162:165], v251 offset:0\n"
        "v_cvt_pk_bf16_f32 v173, v78, v79\n"
        "v_mfma_f32_16x16x32_bf16 v[88:91], a[88:91], a[232:235], v[88:91]\n"
        "s_barrier\n"
        "s_add_u32 m0, s40, 24576\n"
        "ds_read_b64_tr_b16 a[144:145], v131 offset:49152\n"
        "v_mfma_f32_16x16x32_bf16 v[88:91], a[92:95], a[236:239], v[88:91]\n"
        "s_add_u32 s64, s65, 148224\n"
        "ds_read_b64_tr_b16 a[146:147], v131 offset:50688\n"
        "v_cvt_pk_bf16_f32 v174, v80, v81\n"
        "v_mfma_f32_16x16x32_bf16 v[108:111], a[116:119], a[236:239], v[108:111]\n"
        "ds_read_b64_tr_b16 a[148:149], v131 offset:61440\n"
        "v_cvt_pk_bf16_f32 v175, v82, v83\n"
        "v_mfma_f32_16x16x32_bf16 v[108:111], a[112:115], a[232:235], v[108:111]\n"
        "buffer_load_dwordx4 v128, s[20:23], s64 offen lds\n"
        "v_mfma_f32_16x16x32_bf16 v[112:115], a[136:139], a[232:235], v[112:115]\n"
        "ds_read_b64_tr_b16 a[150:151], v131 offset:62976\n"
        "ds_read_b64_tr_b16 a[168:169], v132 offset:49152\n"
        "v_mfma_f32_16x16x32_bf16 v[112:115], a[140:143], a[236:239], v[112:115]\n"
        "ds_read_b64_tr_b16 a[170:171], v132 offset:50688\n"
        "v_cvt_pk_bf16_f32 v190, v84, v85\n"
        "v_mfma_f32_16x16x32_bf16 v[96:99], a[92:95], a[244:247], v[96:99]\n"
        "ds_read_b64_tr_b16 a[172:173], v132 offset:61440\n"
        "ds_read_b128 v[182:185], v251 offset:512\n"
        "v_mfma_f32_16x16x32_bf16 v[96:99], a[88:91], a[240:243], v[96:99]\n"
        "buffer_load_dwordx4 v129, s[20:23], s64 offen offset:1024 lds\n"
        "v_mfma_f32_16x16x32_bf16 v[100:103], a[112:115], a[240:243], v[100:103]\n"
        "ds_read_b64_tr_b16 a[174:175], v132 offset:62976\n"
        "ds_read_b64_tr_b16 a[192:193], v133 offset:49152\n"
        "v_mfma_f32_16x16x32_bf16 v[100:103], a[116:119], a[244:247], v[100:103]\n"
        "ds_read_b64_tr_b16 a[194:195], v133 offset:50688\n"
        "v_cvt_pk_bf16_f32 v191, v86, v87\n"
        "v_mfma_f32_16x16x32_bf16 v[104:107], a[140:143], a[244:247], v[104:107]\n"
        "ds_read_b64_tr_b16 a[196:197], v133 offset:61440\n"
        "ds_read_b128 v[202:205], v251 offset:4096\n"
        "v_mfma_f32_16x16x32_bf16 v[104:107], a[136:139], a[240:243], v[104:107]\n"
        "buffer_load_dwordx4 v130, s[20:23], s64 offen offset:2048 lds\n"
        "v_mfma_f32_16x16x32_bf16 v[64:67], v[152:155], v[156:159], v[0:3]\n"
        "s_waitcnt lgkmcnt(13)\n"
        "s_add_u32 m0, s40, 36864\n"
        "ds_read_b64_tr_b16 a[198:199], v133 offset:62976\n"
        "v_cvt_pk_bf16_f32 v192, v88, v89\n"
        "v_mfma_f32_16x16x32_bf16 v[64:67], v[168:171], v[160:163], v[64:67]\n"
        "s_add_u32 s64, s65, 185088\n"
        "v_cvt_pk_bf16_f32 v193, v90, v91\n"
        "v_cvt_pk_bf16_f32 v194, v108, v109\n"
        "v_mfma_f32_16x16x32_bf16 v[68:71], v[172:175], v[156:159], v[4:7]\n"
        "buffer_load_dwordx4 v128, s[20:23], s64 offen lds\n"
        "v_mfma_f32_16x16x32_bf16 v[68:71], v[188:191], v[164:167], v[68:71]\n"
        "ds_read_b128 v[222:225], v251 offset:4608\n"
        "v_cvt_pk_bf16_f32 v195, v110, v111\n"
        "v_mfma_f32_16x16x32_bf16 v[72:75], v[152:155], v[176:179], v[16:19]\n"
        "s_waitcnt lgkmcnt(7)\n"
        "v_cvt_pk_bf16_f32 v208, v112, v113\n"
        "v_cvt_pk_bf16_f32 v209, v114, v115\n"
        "v_mfma_f32_16x16x32_bf16 v[72:75], v[168:171], v[180:183], v[72:75]\n"
        "v_cvt_pk_bf16_f32 v212, v96, v97\n"
        "v_cvt_pk_bf16_f32 v213, v98, v99\n"
        "v_mfma_f32_16x16x32_bf16 v[76:79], v[172:175], v[176:179], v[20:23]\n"
        "buffer_load_dwordx4 v129, s[20:23], s64 offen offset:1024 lds\n"
        "v_mfma_f32_16x16x32_bf16 v[76:79], v[188:191], v[184:187], v[76:79]\n"
        "ds_read_b128 v[162:165], v252 offset:0\n"
        "v_cvt_pk_bf16_f32 v214, v100, v101\n"
        "v_mfma_f32_16x16x32_bf16 v[80:83], v[152:155], v[196:199], v[32:35]\n"
        "s_waitcnt lgkmcnt(3)\n"
        "ds_read_b128 v[182:185], v252 offset:512\n"
        "v_cvt_pk_bf16_f32 v215, v102, v103\n"
        "v_mfma_f32_16x16x32_bf16 v[80:83], v[168:171], v[200:203], v[80:83]\n"
        "v_cvt_pk_bf16_f32 v230, v104, v105\n"
        "v_cvt_pk_bf16_f32 v231, v106, v107\n"
        "v_mfma_f32_16x16x32_bf16 v[84:87], v[172:175], v[196:199], v[36:39]\n"
        "buffer_load_dwordx4 v130, s[20:23], s64 offen offset:2048 lds\n"
        "v_mfma_f32_16x16x32_bf16 v[84:87], v[188:191], v[204:207], v[84:87]\n"
        "41:\n"
        "s_cbranch_execz 44f\n"
        "s_add_u32 m0, s40, 98304\n"
        "ds_read_b128 v[202:205], v252 offset:4096\n"
        "v_mfma_f32_16x16x32_bf16 v[88:91], v[152:155], v[216:219], v[48:51]\n"
        "s_waitcnt vmcnt(6) lgkmcnt(3)\n"
        "s_barrier\n"
        "ds_read_b64_tr_b16 a[152:153], v131 offset:0\n"
        "ds_read_b128 a[248:251], v240 offset:0\n"
        "v_mfma_f32_16x16x32_bf16 v[88:91], v[168:171], v[220:223], v[88:91]\n"
        "ds_read_b64_tr_b16 a[154:155], v131 offset:1536\n"
        "ds_read_b128 a[216:219], v241 offset:0\n"
        "v_mfma_f32_16x16x32_bf16 v[92:95], v[172:175], v[216:219], v[52:55]\n"
        "ds_read_b64_tr_b16 a[156:157], v131 offset:12288\n"
        "ds_read_b128 a[220:223], v240 offset:128\n"
        "v_mfma_f32_16x16x32_bf16 v[92:95], v[188:191], v[224:227], v[92:95]\n"
        "buffer_load_dword v148, s[52:55], 0 offen lds\n"
        "v_mfma_f32_16x16x32_bf16 v[108:111], v[212:215], v[156:159], v[12:15]\n"
        "s_waitcnt lgkmcnt(8)\n"
        "s_add_u32 m0, s40, 98304\n"
        "s_sub_u32 s64, s47, 6291456\n"
        "ds_read_b64_tr_b16 a[158:159], v131 offset:13824\n"
        "v_mfma_f32_16x16x32_bf16 v[108:111], v[228:231], v[164:167], v[108:111]\n"
        "ds_read_b128 a[224:227], v241 offset:128\n"
        "ds_read_b64_tr_b16 a[176:177], v132 offset:0\n"
        "v_mfma_f32_16x16x32_bf16 v[112:115], v[212:215], v[176:179], v[28:31]\n"
        "s_waitcnt lgkmcnt(10)\n"
        "buffer_load_dwordx4 v242, s[8:11], s64 offen lds\n"
        "v_mfma_f32_16x16x32_bf16 v[112:115], v[228:231], v[184:187], v[112:115]\n"
        "s_add_u32 m0, s40, 99328\n"
        "s_sub_u32 s64, s47, 6291456\n"
        "ds_read_b64_tr_b16 a[178:179], v132 offset:1536\n"
        "v_mfma_f32_16x16x32_bf16 v[116:119], v[212:215], v[196:199], v[44:47]\n"
        "s_waitcnt lgkmcnt(10)\n"
        "ds_read_b64_tr_b16 a[180:181], v132 offset:12288\n"
        "ds_read_b64_tr_b16 a[182:183], v132 offset:13824\n"
        "v_mfma_f32_16x16x32_bf16 v[116:119], v[228:231], v[204:207], v[116:119]\n"
        "s_waitcnt lgkmcnt(10)\n"
        "ds_read_b64_tr_b16 a[200:201], v133 offset:0\n"
        "ds_read_b64_tr_b16 a[202:203], v133 offset:1536\n"
        "v_mfma_f32_16x16x32_bf16 v[120:123], a[248:251], a[144:147], 0\n"
        "s_waitcnt lgkmcnt(10)\n"
        "buffer_load_dwordx4 v243, s[8:11], s64 offen lds\n"
        "v_mfma_f32_16x16x32_bf16 v[120:123], a[216:219], a[148:151], v[120:123]\n"
        "s_waitcnt lgkmcnt(8)\n"
        "s_add_u32 m0, s40, 100352\n"
        "s_sub_u32 s64, s47, 6291456\n"
        "ds_read_b64_tr_b16 a[204:205], v133 offset:12288\n"
        "v_mfma_f32_16x16x32_bf16 v[120:123], a[220:223], a[152:155], v[120:123]\n"
        "s_waitcnt lgkmcnt(7)\n"
        "ds_read_b64_tr_b16 a[206:207], v133 offset:13824\n"
        "v_mfma_f32_16x16x32_bf16 v[120:123], a[224:227], a[156:159], v[120:123]\n"
        "s_waitcnt lgkmcnt(4)\n"
        "buffer_load_dwordx4 v244, s[8:11], s64 offen lds\n"
        "v_mfma_f32_16x16x32_bf16 v[96:99], a[224:227], a[180:183], 0\n"
        "s_add_u32 m0, s40, 110592\n"
        "ds_read_b128 a[228:231], v240 offset:1536\n"
        "v_mfma_f32_16x16x32_bf16 v[96:99], a[220:223], a[176:179], v[96:99]\n"
        "s_sub_u32 s64, s47, 5505024\n"
        "ds_read_b128 a[232:235], v241 offset:1536\n"
        "v_mfma_f32_16x16x32_bf16 v[96:99], a[216:219], a[172:175], v[96:99]\n"
        "buffer_load_dwordx4 v242, s[8:11], s64 offen lds\n"
        "v_mfma_f32_16x16x32_bf16 v[96:99], a[248:251], a[168:171], v[96:99]\n"
        "s_add_u32 m0, s40, 111616\n"
        "ds_read_b128 a[236:239], v240 offset:1664\n"
        "ds_read_b128 a[240:243], v241 offset:1664\n"
        "v_mfma_f32_16x16x32_bf16 v[100:103], a[248:251], a[192:195], 0\n"
        "s_sub_u32 s64, s47, 5505024\n"
        "v_exp_f32 v64, v64\n"
        "v_mfma_f32_16x16x32_bf16 v[100:103], a[216:219], a[196:199], v[100:103]\n"
        "s_waitcnt lgkmcnt(6)\n"
        "v_exp_f32 v65, v65\n"
        "v_mfma_f32_16x16x32_bf16 v[100:103], a[220:223], a[200:203], v[100:103]\n"
        "s_waitcnt lgkmcnt(4)\n"
        "buffer_load_dwordx4 v243, s[8:11], s64 offen lds\n"
        "v_mfma_f32_16x16x32_bf16 v[100:103], a[224:227], a[204:207], v[100:103]\n"
        "s_add_u32 m0, s40, 112640\n"
        "v_exp_f32 v66, v66\n"
        "v_exp_f32 v68, v68\n"
        "v_cvt_pk_bf16_f32 v64, v64, v65\n"
        "v_exp_f32 v67, v67\n"
        "v_exp_f32 v69, v69\n"
        "v_cvt_pk_bf16_f32 v65, v66, v67\n"
        "v_exp_f32 v70, v70\n"
        "v_cvt_pk_bf16_f32 v66, v68, v69\n"
        "v_exp_f32 v71, v71\n"
        "v_exp_f32 v72, v72\n"
        "v_cvt_pk_bf16_f32 v67, v70, v71\n"
        "v_mfma_f32_16x16x32_bf16 v[68:71], v[192:195], v[156:159], v[8:11]\n"
        "s_sub_u32 s64, s47, 5505024\n"
        "v_exp_f32 v73, v73\n"
        "v_mfma_f32_16x16x32_bf16 v[68:71], v[208:211], v[160:163], v[68:71]\n"
        "s_waitcnt lgkmcnt(0)\n"
        "v_exp_f32 v74, v74\n"
        "v_mfma_f32_16x16x32_bf16 v[104:107], a[240:243], a[156:159], 0\n"
        "buffer_load_dwordx4 v244, s[8:11], s64 offen lds\n"
        "v_mfma_f32_16x16x32_bf16 v[104:107], a[236:239], a[152:155], v[104:107]\n"
        "s_xor_b32 m0, s48, 8192\n"
        "ds_read_b128 a[244:247], v240 offset:12288\n"
        "v_cvt_pk_bf16_f32 v72, v72, v73\n"
        "v_mfma_f32_16x16x32_bf16 v[104:107], a[232:235], a[148:151], v[104:107]\n"
        "s_sub_u32 s64, s36, 2097152\n"
        "v_exp_f32 v75, v75\n"
        "v_mfma_f32_16x16x32_bf16 v[104:107], a[228:231], a[144:147], v[104:107]\n"
        "s_waitcnt vmcnt(7)\n"
        "ds_read_b128 a[248:251], v241 offset:12288\n"
        "v_cvt_pk_bf16_f32 v73, v74, v75\n"
        "v_mfma_f32_16x16x32_bf16 v[156:159], a[228:231], a[168:171], 0\n"
        "s_barrier\n"
        "ds_read_b64_tr_b16 a[160:161], v131 offset:24576\n"
        "v_mfma_f32_16x16x32_bf16 v[156:159], a[232:235], a[172:175], v[156:159]\n"
        "buffer_load_dwordx4 v249, s[12:15], s64 offen lds\n"
        "v_mfma_f32_16x16x32_bf16 v[156:159], a[236:239], a[176:179], v[156:159]\n"
        "ds_read_b64_tr_b16 a[162:163], v131 offset:26112\n"
        "ds_read_b64_tr_b16 a[164:165], v131 offset:36864\n"
        "v_mfma_f32_16x16x32_bf16 v[156:159], a[240:243], a[180:183], v[156:159]\n"
        "ds_read_b128 a[216:219], v240 offset:12416\n"
        "ds_read_b64_tr_b16 a[166:167], v131 offset:38400\n"
        "v_mfma_f32_16x16x32_bf16 v[160:163], a[240:243], a[204:207], 0\n"
        "ds_read_b128 a[220:223], v241 offset:12416\n"
        "ds_read_b64_tr_b16 a[184:185], v132 offset:24576\n"
        "v_mfma_f32_16x16x32_bf16 v[160:163], a[236:239], a[200:203], v[160:163]\n"
        "buffer_load_dwordx4 v149, s[12:15], s64 offen offset:1024 lds\n"
        "v_mfma_f32_16x16x32_bf16 v[160:163], a[232:235], a[196:199], v[160:163]\n"
        "ds_read_b64_tr_b16 a[186:187], v132 offset:26112\n"
        "ds_read_b64_tr_b16 a[188:189], v132 offset:36864\n"
        "v_mfma_f32_16x16x32_bf16 v[160:163], a[228:231], a[192:195], v[160:163]\n"
        "ds_read_b64_tr_b16 a[190:191], v132 offset:38400\n"
        "v_exp_f32 v76, v76\n"
        "v_exp_f32 v77, v77\n"
        "v_exp_f32 v78, v78\n"
        "v_cvt_pk_bf16_f32 v74, v76, v77\n"
        "v_exp_f32 v79, v79\n"
        "v_exp_f32 v80, v80\n"
        "v_cvt_pk_bf16_f32 v75, v78, v79\n"
        "v_mfma_f32_16x16x32_bf16 v[76:79], v[192:195], v[176:179], v[24:27]\n"
        "ds_read_b64_tr_b16 a[208:209], v133 offset:24576\n"
        "v_mfma_f32_16x16x32_bf16 v[76:79], v[208:211], v[180:183], v[76:79]\n"
        "s_waitcnt lgkmcnt(12)\n"
        "ds_read_b64_tr_b16 a[210:211], v133 offset:26112\n"
        "v_mfma_f32_16x16x32_bf16 v[164:167], a[244:247], a[144:147], 0\n"
        "s_waitcnt lgkmcnt(12)\n"
        "ds_read_b64_tr_b16 a[212:213], v133 offset:36864\n"
        "v_mfma_f32_16x16x32_bf16 v[164:167], a[248:251], a[148:151], v[164:167]\n"
        "s_waitcnt lgkmcnt(9)\n"
        "ds_read_b64_tr_b16 a[214:215], v133 offset:38400\n"
        "v_mfma_f32_16x16x32_bf16 v[164:167], a[216:219], a[152:155], v[164:167]\n"
        "s_waitcnt lgkmcnt(8)\n"
        "v_exp_f32 v81, v81\n"
        "v_mfma_f32_16x16x32_bf16 v[164:167], a[220:223], a[156:159], v[164:167]\n"
        "v_exp_f32 v82, v82\n"
        "v_mfma_f32_16x16x32_bf16 v[176:179], a[220:223], a[180:183], 0\n"
        "ds_read_b128 a[224:227], v240 offset:13824\n"
        "v_cvt_pk_bf16_f32 v80, v80, v81\n"
        "v_mfma_f32_16x16x32_bf16 v[176:179], a[216:219], a[176:179], v[176:179]\n"
        "ds_read_b128 a[228:231], v241 offset:13824\n"
        "v_mfma_f32_16x16x32_bf16 v[176:179], a[248:251], a[172:175], v[176:179]\n"
        "ds_read_b128 a[232:235], v240 offset:13952\n"
        "v_mfma_f32_16x16x32_bf16 v[176:179], a[244:247], a[168:171], v[176:179]\n"
        "ds_read_b128 a[236:239], v241 offset:13952\n"
        "v_mfma_f32_16x16x32_bf16 v[180:183], a[244:247], a[192:195], 0\n"
        "v_exp_f32 v83, v83\n"
        "v_mfma_f32_16x16x32_bf16 v[180:183], a[248:251], a[196:199], v[180:183]\n"
        "ds_read_b128 v[222:225], v252 offset:4608\n"
        "v_cvt_pk_bf16_f32 v81, v82, v83\n"
        "v_mfma_f32_16x16x32_bf16 v[180:183], a[216:219], a[200:203], v[180:183]\n"
        "v_exp_f32 v84, v84\n"
        "v_mfma_f32_16x16x32_bf16 v[180:183], a[220:223], a[204:207], v[180:183]\n"
        "v_exp_f32 v85, v85\n"
        "v_exp_f32 v86, v86\n"
        "v_cvt_pk_bf16_f32 v82, v84, v85\n"
        "v_exp_f32 v87, v87\n"
        "v_exp_f32 v88, v88\n"
        "v_cvt_pk_bf16_f32 v83, v86, v87\n"
        "v_mfma_f32_16x16x32_bf16 v[84:87], v[192:195], v[196:199], v[40:43]\n"
        "v_exp_f32 v89, v89\n"
        "v_mfma_f32_16x16x32_bf16 v[84:87], v[208:211], v[200:203], v[84:87]\n"
        "s_waitcnt lgkmcnt(1)\n"
        "v_exp_f32 v90, v90\n"
        "v_mfma_f32_16x16x32_bf16 v[184:187], a[236:239], a[156:159], 0\n"
        "ds_read_b128 a[240:243], v240 offset:256\n"
        "v_cvt_pk_bf16_f32 v88, v88, v89\n"
        "v_mfma_f32_16x16x32_bf16 v[184:187], a[232:235], a[152:155], v[184:187]\n"
        "v_exp_f32 v91, v91\n"
        "v_mfma_f32_16x16x32_bf16 v[184:187], a[228:231], a[148:151], v[184:187]\n"
        "ds_read_b128 a[244:247], v241 offset:256\n"
        "v_cvt_pk_bf16_f32 v89, v90, v91\n"
        "v_mfma_f32_16x16x32_bf16 v[184:187], a[224:227], a[144:147], v[184:187]\n"
        "v_exp_f32 v92, v92\n"
        "v_mfma_f32_16x16x32_bf16 v[196:199], a[224:227], a[168:171], 0\n"
        "v_exp_f32 v93, v93\n"
        "v_mfma_f32_16x16x32_bf16 v[196:199], a[228:231], a[172:175], v[196:199]\n"
        "v_exp_f32 v94, v94\n"
        "v_mfma_f32_16x16x32_bf16 v[196:199], a[232:235], a[176:179], v[196:199]\n"
        "ds_read_b128 a[248:251], v240 offset:1792\n"
        "v_cvt_pk_bf16_f32 v90, v92, v93\n"
        "v_mfma_f32_16x16x32_bf16 v[196:199], a[236:239], a[180:183], v[196:199]\n"
        "v_exp_f32 v95, v95\n"
        "v_mfma_f32_16x16x32_bf16 v[200:203], a[236:239], a[204:207], 0\n"
        "ds_read_b128 a[216:219], v241 offset:1792\n"
        "v_cvt_pk_bf16_f32 v91, v94, v95\n"
        "v_mfma_f32_16x16x32_bf16 v[200:203], a[232:235], a[200:203], v[200:203]\n"
        "v_exp_f32 v108, v108\n"
        "v_mfma_f32_16x16x32_bf16 v[200:203], a[228:231], a[196:199], v[200:203]\n"
        "v_exp_f32 v109, v109\n"
        "v_mfma_f32_16x16x32_bf16 v[200:203], a[224:227], a[192:195], v[200:203]\n"
        "v_exp_f32 v110, v110\n"
        "v_exp_f32 v111, v111\n"
        "v_exp_f32 v70, v70\n"
        "v_exp_f32 v69, v69\n"
        "v_exp_f32 v68, v68\n"
        "v_exp_f32 v71, v71\n"
        "v_cvt_pk_bf16_f32 v68, v68, v69\n"
        "v_cvt_pk_bf16_f32 v69, v70, v71\n"
        "v_cvt_pk_bf16_f32 v70, v108, v109\n"
        "v_cvt_pk_bf16_f32 v71, v110, v111\n"
        "v_mfma_f32_16x16x32_bf16 v[108:111], v[192:195], v[216:219], v[56:59]\n"
        "s_waitcnt lgkmcnt(4)\n"
        "v_exp_f32 v112, v112\n"
        "v_mfma_f32_16x16x32_bf16 v[108:111], v[208:211], v[220:223], v[108:111]\n"
        "v_exp_f32 v113, v113\n"
        "v_exp_f32 v114, v114\n"
        "v_exp_f32 v115, v115\n"
        "v_exp_f32 v78, v78\n"
        "v_exp_f32 v77, v77\n"
        "v_exp_f32 v76, v76\n"
        "v_exp_f32 v79, v79\n"
        "v_cvt_pk_bf16_f32 v76, v76, v77\n"
        "v_cvt_pk_bf16_f32 v77, v78, v79\n"
        "v_cvt_pk_bf16_f32 v78, v112, v113\n"
        "v_cvt_pk_bf16_f32 v79, v114, v115\n"
        "v_mfma_f32_16x16x32_bf16 v[112:115], v[212:215], v[216:219], v[60:63]\n"
        "v_exp_f32 v116, v116\n"
        "v_mfma_f32_16x16x32_bf16 v[112:115], v[228:231], v[224:227], v[112:115]\n"
        "s_waitcnt lgkmcnt(3)\n"
        "v_exp_f32 v117, v117\n"
        "v_mfma_f32_16x16x32_bf16 v[120:123], a[240:243], a[160:163], v[120:123]\n"
        "s_waitcnt lgkmcnt(2)\n"
        "v_exp_f32 v118, v118\n"
        "v_mfma_f32_16x16x32_bf16 v[120:123], a[244:247], a[164:167], v[120:123]\n"
        "v_exp_f32 v119, v119\n"
        "v_mfma_f32_16x16x32_bf16 v[96:99], a[244:247], a[188:191], v[96:99]\n"
        "ds_read_b128 a[220:223], v240 offset:12544\n"
        "v_mfma_f32_16x16x32_bf16 v[96:99], a[240:243], a[184:187], v[96:99]\n"
        "ds_read_b128 a[224:227], v241 offset:12544\n"
        "v_mfma_f32_16x16x32_bf16 v[100:103], a[240:243], a[208:211], v[100:103]\n"
        "v_exp_f32 v84, v84\n"
        "v_mfma_f32_16x16x32_bf16 v[100:103], a[244:247], a[212:215], v[100:103]\n"
        "s_waitcnt lgkmcnt(2)\n"
        "v_exp_f32 v85, v85\n"
        "v_mfma_f32_16x16x32_bf16 v[104:107], a[216:219], a[164:167], v[104:107]\n"
        "v_exp_f32 v86, v86\n"
        "v_mfma_f32_16x16x32_bf16 v[104:107], a[248:251], a[160:163], v[104:107]\n"
        "v_cvt_pk_bf16_f32 v84, v84, v85\n"
        "v_cvt_pk_bf16_f32 v128, v120, v121\n"
        "v_mfma_f32_16x16x32_bf16 v[156:159], a[248:251], a[184:187], v[156:159]\n"
        "ds_read_b128 a[228:231], v240 offset:14080\n"
        "v_cvt_pk_bf16_f32 v129, v122, v123\n"
        "v_mfma_f32_16x16x32_bf16 v[156:159], a[216:219], a[188:191], v[156:159]\n"
        "ds_read_b128 a[232:235], v241 offset:14080\n"
        "v_cvt_pk_bf16_f32 v136, v96, v97\n"
        "v_mfma_f32_16x16x32_bf16 v[160:163], a[216:219], a[212:215], v[160:163]\n"
        "v_exp_f32 v87, v87\n"
        "v_mfma_f32_16x16x32_bf16 v[160:163], a[248:251], a[208:211], v[160:163]\n"
        "s_waitcnt lgkmcnt(3)\n"
        "v_cvt_pk_bf16_f32 v85, v86, v87\n"
        "v_cvt_pk_bf16_f32 v86, v116, v117\n"
        "v_mfma_f32_16x16x32_bf16 v[164:167], a[220:223], a[160:163], v[164:167]\n"
        "s_waitcnt lgkmcnt(2)\n"
        "v_cvt_pk_bf16_f32 v87, v118, v119\n"
        "v_cvt_pk_bf16_f32 v137, v98, v99\n"
        "v_mfma_f32_16x16x32_bf16 v[164:167], a[224:227], a[164:167], v[164:167]\n"
        "v_exp_f32 v108, v108\n"
        "v_mfma_f32_16x16x32_bf16 v[176:179], a[224:227], a[188:191], v[176:179]\n"
        "v_exp_f32 v109, v109\n"
        "v_mfma_f32_16x16x32_bf16 v[176:179], a[220:223], a[184:187], v[176:179]\n"
        "v_exp_f32 v110, v110\n"
        "v_mfma_f32_16x16x32_bf16 v[180:183], a[220:223], a[208:211], v[180:183]\n"
        "v_cvt_pk_bf16_f32 v92, v108, v109\n"
        "v_cvt_pk_bf16_f32 v144, v100, v101\n"
        "v_mfma_f32_16x16x32_bf16 v[180:183], a[224:227], a[212:215], v[180:183]\n"
        "s_waitcnt lgkmcnt(0)\n"
        "v_exp_f32 v111, v111\n"
        "v_mfma_f32_16x16x32_bf16 v[184:187], a[232:235], a[164:167], v[184:187]\n"
        "v_cvt_pk_bf16_f32 v93, v110, v111\n"
        "v_cvt_pk_bf16_f32 v145, v102, v103\n"
        "v_mfma_f32_16x16x32_bf16 v[184:187], a[228:231], a[160:163], v[184:187]\n"
        "v_exp_f32 v112, v112\n"
        "v_mfma_f32_16x16x32_bf16 v[196:199], a[228:231], a[184:187], v[196:199]\n"
        "v_exp_f32 v113, v113\n"
        "v_mfma_f32_16x16x32_bf16 v[196:199], a[232:235], a[188:191], v[196:199]\n"
        "v_exp_f32 v114, v114\n"
        "v_mfma_f32_16x16x32_bf16 v[200:203], a[232:235], a[212:215], v[200:203]\n"
        "v_cvt_pk_bf16_f32 v94, v112, v113\n"
        "v_cvt_pk_bf16_f32 v130, v104, v105\n"
        "v_mfma_f32_16x16x32_bf16 v[200:203], a[228:231], a[208:211], v[200:203]\n"
        "v_exp_f32 v115, v115\n"
        "v_mov_b64 v[120:121], 0\n"
        "s_nop 1\n"
        "v_smfmac_f32_16x16x64_bf16 v[120:123], a[252:255], v[64:71], v255\n"
        "v_cvt_pk_bf16_f32 v95, v114, v115\n"
        "v_cvt_pk_bf16_f32 v131, v106, v107\n"
        "s_nop 1\n"
        "v_mfma_f32_16x16x32_bf16 v[116:119], v[128:131], v[64:67], 0\n"
        "v_cvt_pk_bf16_f32 v138, v156, v157\n"
        "v_cvt_pk_bf16_f32 v139, v158, v159\n"
        "v_cvt_pk_bf16_f32 v132, v164, v165\n"
        "v_cvt_pk_bf16_f32 v133, v166, v167\n"
        "v_cvt_pk_bf16_f32 v134, v184, v185\n"
        "v_cvt_pk_bf16_f32 v135, v186, v187\n"
        "s_nop 1\n"
        "v_mfma_f32_16x16x32_bf16 v[116:119], v[132:135], v[68:71], v[116:119]\n"
        "v_cvt_pk_bf16_f32 v146, v160, v161\n"
        "v_cvt_pk_bf16_f32 v147, v162, v163\n"
        "v_cvt_pk_bf16_f32 v140, v176, v177\n"
        "v_cvt_pk_bf16_f32 v141, v178, v179\n"
        "v_cvt_pk_bf16_f32 v142, v196, v197\n"
        "v_cvt_pk_bf16_f32 v143, v198, v199\n"
        "s_nop 1\n"
        "v_mfma_f32_16x16x32_bf16 v[152:155], v[140:143], v[68:71], 0\n"
        "v_mov_b64 v[172:173], 0\n"
        "v_cvt_pk_bf16_f32 v148, v180, v181\n"
        "v_mfma_f32_16x16x32_bf16 v[152:155], v[136:139], v[64:67], v[152:155]\n"
        "v_cvt_pk_bf16_f32 v149, v182, v183\n"
        "v_cvt_pk_bf16_f32 v150, v200, v201\n"
        "v_mfma_f32_16x16x32_bf16 v[160:163], v[144:147], v[64:67], 0\n"
        "v_cvt_pk_bf16_f32 v151, v202, v203\n"
        "v_mov_b64 v[192:193], 0\n"
        "s_nop 0\n"
        "v_mfma_f32_16x16x32_bf16 v[160:163], v[148:151], v[68:71], v[160:163]\n"
        "v_add_f32 v120, v120, v121\n"
        "v_add_u32 v240, 24576, v240\n"
        "v_smfmac_f32_16x16x64_bf16 v[172:175], a[252:255], v[72:79], v255\n"
        "v_add_u32 v241, 24576, v241\n"
        "v_mfma_f32_16x16x32_bf16 v[176:179], v[128:131], v[72:75], 0\n"
        "v_rcp_f32 v121, v120\n"
        "v_mfma_f32_16x16x32_bf16 v[176:179], v[132:135], v[76:79], v[176:179]\n"
        "v_mov_b64 v[212:213], 0\n"
        "v_mul_f32 v116, v121, v116\n"
        "v_mfma_f32_16x16x32_bf16 v[180:183], v[140:143], v[76:79], 0\n"
        "v_mul_f32 v117, v121, v117\n"
        "v_mul_f32 v118, v121, v118\n"
        "v_mfma_f32_16x16x32_bf16 v[180:183], v[136:139], v[72:75], v[180:183]\n"
        "v_mul_f32 v119, v121, v119\n"
        "v_cvt_pk_bf16_f32 v116, v116, v117\n"
        "v_mfma_f32_16x16x32_bf16 v[184:187], v[144:147], v[72:75], 0\n"
        "v_cvt_pk_bf16_f32 v117, v118, v119\n"
        "v_mul_f32 v152, v121, v152\n"
        "v_mfma_f32_16x16x32_bf16 v[184:187], v[148:151], v[76:79], v[184:187]\n"
        "s_waitcnt vmcnt(5)\n"
        "v_mul_f32 v153, v121, v153\n"
        "v_maximum3_f32 v253, v253, v120, v121\n"
        "v_smfmac_f32_16x16x64_bf16 v[192:195], a[252:255], v[80:87], v255\n"
        "s_barrier\n"
        "ds_read_b128 a[236:239], v240 offset:0\n"
        "v_mul_f32 v154, v121, v154\n"
        "v_mfma_f32_16x16x32_bf16 v[196:199], v[128:131], v[80:83], 0\n"
        "s_add_u32 m0, s66, 0\n"
        "ds_read_b128 a[240:243], v241 offset:0\n"
        "v_mul_f32 v155, v121, v155\n"
        "v_mfma_f32_16x16x32_bf16 v[196:199], v[132:135], v[84:87], v[196:199]\n"
        "s_add_u32 s64, s51, 0\n"
        "v_cvt_pk_bf16_f32 v118, v152, v153\n"
        "v_cvt_pk_bf16_f32 v119, v154, v155\n"
        "v_mfma_f32_16x16x32_bf16 v[200:203], v[140:143], v[84:87], 0\n"
        "ds_read_b128 a[244:247], v240 offset:128\n"
        "v_mul_f32 v160, v121, v160\n"
        "v_mfma_f32_16x16x32_bf16 v[200:203], v[136:139], v[80:83], v[200:203]\n"
        "buffer_load_dwordx4 v124, s[52:55], s64 offen lds\n"
        "v_mfma_f32_16x16x32_bf16 v[204:207], v[144:147], v[80:83], 0\n"
        "ds_read_b128 a[248:251], v241 offset:128\n"
        "v_mul_f32 v161, v121, v161\n"
        "v_mfma_f32_16x16x32_bf16 v[204:207], v[148:151], v[84:87], v[204:207]\n"
        "ds_read_b128 a[216:219], v240 offset:256\n"
        "v_mul_f32 v162, v121, v162\n"
        "v_smfmac_f32_16x16x64_bf16 v[212:215], a[252:255], v[88:95], v255\n"
        "ds_read_b128 a[220:223], v241 offset:256\n"
        "v_mul_f32 v163, v121, v163\n"
        "v_mfma_f32_16x16x32_bf16 v[216:219], v[128:131], v[88:91], 0\n"
        "ds_write_b128 v245, v[116:119] offset:0\n"
        "v_mfma_f32_16x16x32_bf16 v[216:219], v[132:135], v[92:95], v[216:219]\n"
        "v_cvt_pk_bf16_f32 v160, v160, v161\n"
        "v_cvt_pk_bf16_f32 v161, v162, v163\n"
        "v_mfma_f32_16x16x32_bf16 v[220:223], v[140:143], v[92:95], 0\n"
        "v_add_f32 v172, v172, v173\n"
        "v_add_f32 v192, v192, v193\n"
        "v_mfma_f32_16x16x32_bf16 v[220:223], v[136:139], v[88:91], v[220:223]\n"
        "v_rcp_f32 v173, v172\n"
        "v_mfma_f32_16x16x32_bf16 v[224:227], v[144:147], v[88:91], 0\n"
        "ds_write_b64 v246, v[160:161] offset:0\n"
        "v_mfma_f32_16x16x32_bf16 v[224:227], v[148:151], v[92:95], v[224:227]\n"
        "s_waitcnt lgkmcnt(7)\n"
        "v_mul_f32 v176, v173, v176\n"
        "v_mul_f32 v177, v173, v177\n"
        "v_mfma_f32_16x16x32_bf16 v[96:99], a[72:75], a[236:239], 0\n"
        "s_waitcnt lgkmcnt(6)\n"
        "v_mul_f32 v178, v173, v178\n"
        "v_mul_f32 v179, v173, v179\n"
        "v_mfma_f32_16x16x32_bf16 v[96:99], a[76:79], a[240:243], v[96:99]\n"
        "s_waitcnt lgkmcnt(5)\n"
        "buffer_load_dwordx4 v125, s[52:55], s64 offen offset:1024 lds\n"
        "v_mfma_f32_16x16x32_bf16 v[96:99], a[80:83], a[244:247], v[96:99]\n"
        "s_waitcnt lgkmcnt(4)\n"
        "v_cvt_pk_bf16_f32 v176, v176, v177\n"
        "v_cvt_pk_bf16_f32 v177, v178, v179\n"
        "v_mfma_f32_16x16x32_bf16 v[96:99], a[84:87], a[248:251], v[96:99]\n"
        "s_waitcnt lgkmcnt(3)\n"
        "v_rcp_f32 v193, v192\n"
        "v_mfma_f32_16x16x32_bf16 v[96:99], a[88:91], a[216:219], v[96:99]\n"
        "s_waitcnt lgkmcnt(2)\n"
        "v_mul_f32 v180, v173, v180\n"
        "v_mul_f32 v181, v173, v181\n"
        "v_mfma_f32_16x16x32_bf16 v[96:99], a[92:95], a[220:223], v[96:99]\n"
        "s_add_u32 s41, s29, s40\n"
        "v_maximum3_f32 v253, v253, v172, v173\n"
        "v_mul_f32 v182, v173, v182\n"
        "v_mfma_f32_16x16x32_bf16 v[100:103], a[116:119], a[220:223], 0\n"
        "s_lshr_b32 s47, s58, 6\n"
        "v_mul_f32 v183, v173, v183\n"
        "v_cvt_pk_bf16_f32 v178, v180, v181\n"
        "v_mfma_f32_16x16x32_bf16 v[100:103], a[112:115], a[216:219], v[100:103]\n"
        "s_mul_i32 s47, s47, 1572864\n"
        "v_cvt_pk_bf16_f32 v179, v182, v183\n"
        "v_cvt_pk_bf16_f32 v152, v96, v97\n"
        "v_mfma_f32_16x16x32_bf16 v[100:103], a[108:111], a[248:251], v[100:103]\n"
        "buffer_load_dwordx4 v126, s[52:55], s64 offen offset:2048 lds\n"
        "v_mfma_f32_16x16x32_bf16 v[100:103], a[104:107], a[244:247], v[100:103]\n"
        "s_sub_u32 s31, s28, s30\n"
        "v_cvt_pk_bf16_f32 v153, v98, v99\n"
        "v_mul_f32 v184, v173, v184\n"
        "v_mfma_f32_16x16x32_bf16 v[100:103], a[100:103], a[240:243], v[100:103]\n"
        "s_sub_u32 s45, s29, s28\n"
        "ds_write_b128 v245, v[176:179] offset:256\n"
        "v_mfma_f32_16x16x32_bf16 v[100:103], a[96:99], a[236:239], v[100:103]\n"
        "s_mov_b32 s29, s30\n"
        "v_mul_f32 v185, v173, v185\n"
        "v_mul_f32 v186, v173, v186\n"
        "v_mfma_f32_16x16x32_bf16 v[104:107], a[120:123], a[236:239], 0\n"
        "s_mov_b32 s30, s28\n"
        "v_mul_f32 v187, v173, v187\n"
        "v_cvt_pk_bf16_f32 v184, v184, v185\n"
        "v_mfma_f32_16x16x32_bf16 v[104:107], a[124:127], a[240:243], v[104:107]\n"
        "s_add_u32 s28, s28, s45\n"
        "v_cvt_pk_bf16_f32 v185, v186, v187\n"
        "v_cvt_pk_bf16_f32 v154, v100, v101\n"
        "v_mfma_f32_16x16x32_bf16 v[104:107], a[128:131], a[244:247], v[104:107]\n"
        "s_add_u32 m0, s66, 3072\n"
        "v_cvt_pk_bf16_f32 v155, v102, v103\n"
        "v_mul_f32 v196, v193, v196\n"
        "v_mfma_f32_16x16x32_bf16 v[104:107], a[132:135], a[248:251], v[104:107]\n"
        "s_add_u32 s47, s47, s24\n"
        "ds_write_b64 v246, v[184:185] offset:256\n"
        "v_mfma_f32_16x16x32_bf16 v[104:107], a[136:139], a[216:219], v[104:107]\n"
        "s_add_u32 s64, s51, 6144\n"
        "v_mul_f32 v197, v193, v197\n"
        "v_mul_f32 v198, v193, v198\n"
        "v_mfma_f32_16x16x32_bf16 v[104:107], a[140:143], a[220:223], v[104:107]\n"
        "s_lshl_b32 s36, s58, 13\n"
        "v_mul_f32 v199, v193, v199\n"
        "v_cvt_pk_bf16_f32 v196, v196, v197\n"
        "v_mfma_f32_16x16x32_bf16 v[96:99], a[20:23], a[220:223], 0\n"
        "s_add_u32 s36, s36, s34\n"
        "v_cvt_pk_bf16_f32 v197, v198, v199\n"
        "v_mul_f32 v200, v193, v200\n"
        "v_mfma_f32_16x16x32_bf16 v[96:99], a[16:19], a[216:219], v[96:99]\n"
        "buffer_load_dwordx4 v124, s[52:55], s64 offen lds\n"
        "v_mfma_f32_16x16x32_bf16 v[96:99], a[12:15], a[248:251], v[96:99]\n"
        "s_sub_u32 s62, s58, s57\n"
        "v_cvt_pk_bf16_f32 v168, v104, v105\n"
        "v_cvt_pk_bf16_f32 v169, v106, v107\n"
        "v_mfma_f32_16x16x32_bf16 v[96:99], a[8:11], a[244:247], v[96:99]\n"
        "s_and_b32 s62, s62, 63\n"
        "v_mul_f32 v201, v193, v201\n"
        "v_maximum3_f32 v253, v253, v192, v193\n"
        "v_mfma_f32_16x16x32_bf16 v[96:99], a[4:7], a[240:243], v[96:99]\n"
        "s_mul_i32 s62, s62, 3072\n"
        "v_mul_f32 v202, v193, v202\n"
        "v_mul_f32 v203, v193, v203\n"
        "v_mfma_f32_16x16x32_bf16 v[96:99], a[0:3], a[236:239], v[96:99]\n"
        "s_add_u32 s35, s47, 786432\n"
        "v_cvt_pk_bf16_f32 v198, v200, v201\n"
        "v_cvt_pk_bf16_f32 v199, v202, v203\n"
        "v_mfma_f32_16x16x32_bf16 v[100:103], a[24:27], a[236:239], 0\n"
        "s_sub_u32 s46, s35, 100663296\n"
        "ds_write_b128 v245, v[196:199] offset:512\n"
        "v_mfma_f32_16x16x32_bf16 v[100:103], a[28:31], a[240:243], v[100:103]\n"
        "s_cmp_lt_u32 s46, 786432\n"
        "v_mul_f32 v204, v193, v204\n"
        "v_mul_f32 v96, s50, v96\n"
        "v_mfma_f32_16x16x32_bf16 v[100:103], a[32:35], a[244:247], v[100:103]\n"
        "s_cselect_b32 s35, s46, s35\n"
        "v_mul_f32 v97, s50, v97\n"
        "v_mul_f32 v98, s50, v98\n"
        "v_mfma_f32_16x16x32_bf16 v[100:103], a[36:39], a[248:251], v[100:103]\n"
        "v_mul_f32 v99, s50, v99\n"
        "v_cvt_pk_bf16_f32 v156, v96, v97\n"
        "v_mfma_f32_16x16x32_bf16 v[100:103], a[40:43], a[216:219], v[100:103]\n"
        "buffer_load_dwordx4 v125, s[52:55], s64 offen offset:1024 lds\n"
        "v_mfma_f32_16x16x32_bf16 v[100:103], a[44:47], a[220:223], v[100:103]\n"
        "v_cvt_pk_bf16_f32 v157, v98, v99\n"
        "v_mul_f32 v205, v193, v205\n"
        "v_mfma_f32_16x16x32_bf16 v[104:107], a[68:71], a[220:223], 0\n"
        "v_mul_f32 v206, v193, v206\n"
        "v_mul_f32 v207, v193, v207\n"
        "v_mfma_f32_16x16x32_bf16 v[104:107], a[64:67], a[216:219], v[104:107]\n"
        "v_cvt_pk_bf16_f32 v204, v204, v205\n"
        "v_cvt_pk_bf16_f32 v205, v206, v207\n"
        "v_mfma_f32_16x16x32_bf16 v[104:107], a[60:63], a[248:251], v[104:107]\n"
        "v_mul_f32 v100, s50, v100\n"
        "v_mul_f32 v101, s50, v101\n"
        "v_mfma_f32_16x16x32_bf16 v[104:107], a[56:59], a[244:247], v[104:107]\n"
        "v_mul_f32 v102, s50, v102\n"
        "v_mul_f32 v103, s50, v103\n"
        "v_mfma_f32_16x16x32_bf16 v[104:107], a[52:55], a[240:243], v[104:107]\n"
        "v_cvt_pk_bf16_f32 v158, v100, v101\n"
        "v_cvt_pk_bf16_f32 v159, v102, v103\n"
        "v_mfma_f32_16x16x32_bf16 v[104:107], a[48:51], a[236:239], v[104:107]\n"
        "buffer_load_dwordx4 v126, s[52:55], s64 offen offset:2048 lds\n"
        "v_mfma_f32_16x16x32_bf16 v[96:99], a[236:239], a[144:147], 0\n"
        "ds_write_b64 v246, v[204:205] offset:512\n"
        "v_mfma_f32_16x16x32_bf16 v[96:99], a[240:243], a[148:151], v[96:99]\n"
        "v_add_f32 v212, v212, v213\n"
        "v_xor_b32 v251, 0x2000, v251\n"
        "v_mfma_f32_16x16x32_bf16 v[96:99], a[244:247], a[152:155], v[96:99]\n"
        "v_rcp_f32 v213, v212\n"
        "v_mfma_f32_16x16x32_bf16 v[96:99], a[248:251], a[156:159], v[96:99]\n"
        "v_mul_f32 v104, s50, v104\n"
        "v_mul_f32 v105, s50, v105\n"
        "v_mfma_f32_16x16x32_bf16 v[96:99], a[216:219], a[160:163], v[96:99]\n"
        "s_lshl_b64 s[60:61], s[60:61], 8\n"
        "v_mul_f32 v106, s50, v106\n"
        "v_mul_f32 v107, s50, v107\n"
        "v_mfma_f32_16x16x32_bf16 v[96:99], a[220:223], a[164:167], v[96:99]\n"
        "s_add_u32 m0, s66, 6144\n"
        "v_cvt_pk_bf16_f32 v160, v104, v105\n"
        "v_cvt_pk_bf16_f32 v161, v106, v107\n"
        "v_mfma_f32_16x16x32_bf16 v[100:103], a[220:223], a[188:191], 0\n"
        "s_add_u32 s64, s51, 18432\n"
        "v_mov_b64 v[166:167], v[160:161]\n"
        "v_mul_f32 v216, v213, v216\n"
        "v_mfma_f32_16x16x32_bf16 v[100:103], a[216:219], a[184:187], v[100:103]\n"
        "s_and_b32 s63, s58, 255\n"
        "v_mul_f32 v217, v213, v217\n"
        "v_mul_f32 v218, v213, v218\n"
        "v_mfma_f32_16x16x32_bf16 v[100:103], a[248:251], a[180:183], v[100:103]\n"
        "buffer_load_dwordx4 v124, s[52:55], s64 offen lds\n"
        "v_mfma_f32_16x16x32_bf16 v[100:103], a[244:247], a[176:179], v[100:103]\n"
        "s_or_b32 s60, s60, s63\n"
        "ds_read_b128 a[224:227], v240 offset:1536\n"
        "ds_read_b128 a[228:231], v241 offset:1536\n"
        "v_mfma_f32_16x16x32_bf16 v[100:103], a[240:243], a[172:175], v[100:103]\n"
        "s_add_u32 s27, s27, 1\n"
        "ds_read_b128 a[232:235], v240 offset:1664\n"
        "v_mul_f32 v219, v213, v219\n"
        "v_mfma_f32_16x16x32_bf16 v[100:103], a[236:239], a[168:171], v[100:103]\n"
        "s_add_u32 s63, s58, 256\n"
        "v_cvt_pk_bf16_f32 v216, v216, v217\n"
        "v_cvt_pk_bf16_f32 v217, v218, v219\n"
        "v_mfma_f32_16x16x32_bf16 v[104:107], a[236:239], a[192:195], 0\n"
        "s_cmp_lt_u32 s63, 3328\n"
        "ds_read_b128 a[236:239], v241 offset:1664\n"
        "v_cvt_pk_bf16_f32 v128, v96, v97\n"
        "v_mfma_f32_16x16x32_bf16 v[104:107], a[240:243], a[196:199], v[104:107]\n"
        "s_cselect_b32 s63, s63, -1\n"
        "ds_read_b128 a[240:243], v240 offset:1792\n"
        "v_cvt_pk_bf16_f32 v129, v98, v99\n"
        "v_mfma_f32_16x16x32_bf16 v[104:107], a[244:247], a[200:203], v[104:107]\n"
        "s_cmp_lt_u32 s27, 3072\n"
        "ds_read_b128 a[244:247], v241 offset:1792\n"
        "v_mul_f32 v220, v213, v220\n"
        "v_mfma_f32_16x16x32_bf16 v[104:107], a[248:251], a[204:207], v[104:107]\n"
        "s_cselect_b32 s49, 4096, 0\n"
        "v_mul_f32 v221, v213, v221\n"
        "v_maximum3_f32 v253, v253, v212, v213\n"
        "v_mfma_f32_16x16x32_bf16 v[104:107], a[216:219], a[208:211], v[104:107]\n"
        "v_mul_f32 v222, v213, v222\n"
        "v_mul_f32 v223, v213, v223\n"
        "v_mfma_f32_16x16x32_bf16 v[104:107], a[220:223], a[212:215], v[104:107]\n"
        "s_waitcnt lgkmcnt(5)\n"
        "buffer_load_dwordx4 v125, s[52:55], s64 offen offset:1024 lds\n"
        "v_mfma_f32_16x16x32_bf16 v[96:99], a[72:75], a[224:227], 0\n"
        "s_waitcnt lgkmcnt(4)\n"
        "v_cvt_pk_bf16_f32 v218, v220, v221\n"
        "v_cvt_pk_bf16_f32 v219, v222, v223\n"
        "v_mfma_f32_16x16x32_bf16 v[96:99], a[76:79], a[228:231], v[96:99]\n"
        "s_waitcnt lgkmcnt(3)\n"
        "v_xor_b32 v252, 0x2000, v252\n"
        "v_mul_f32 v224, v213, v224\n"
        "v_mfma_f32_16x16x32_bf16 v[96:99], a[80:83], a[232:235], v[96:99]\n"
        "s_waitcnt lgkmcnt(2)\n"
        "ds_write_b128 v245, v[216:219] offset:768\n"
        "v_mfma_f32_16x16x32_bf16 v[96:99], a[84:87], a[236:239], v[96:99]\n"
        "s_waitcnt lgkmcnt(2)\n"
        "v_mul_f32 v225, v213, v225\n"
        "v_cvt_pk_bf16_f32 v136, v100, v101\n"
        "v_mfma_f32_16x16x32_bf16 v[96:99], a[88:91], a[240:243], v[96:99]\n"
        "s_waitcnt lgkmcnt(1)\n"
        "v_cvt_pk_bf16_f32 v137, v102, v103\n"
        "v_mul_f32 v226, v213, v226\n"
        "v_mfma_f32_16x16x32_bf16 v[96:99], a[92:95], a[244:247], v[96:99]\n"
        "s_waitcnt vmcnt(8)\n"
        "v_mul_f32 v227, v213, v227\n"
        "v_cvt_pk_bf16_f32 v224, v224, v225\n"
        "v_mfma_f32_16x16x32_bf16 v[100:103], a[116:119], a[244:247], 0\n"
        "s_barrier\n"
        "v_cvt_pk_bf16_f32 v225, v226, v227\n"
        "v_cvt_pk_bf16_f32 v144, v104, v105\n"
        "v_mfma_f32_16x16x32_bf16 v[100:103], a[112:115], a[240:243], v[100:103]\n"
        "ds_write_b64 v246, v[224:225] offset:768\n"
        "v_mfma_f32_16x16x32_bf16 v[100:103], a[108:111], a[236:239], v[100:103]\n"
        "v_cvt_pk_bf16_f32 v145, v106, v107\n"
        "v_cvt_pk_bf16_f32 v172, v96, v97\n"
        "v_mfma_f32_16x16x32_bf16 v[100:103], a[104:107], a[232:235], v[100:103]\n"
        "v_cvt_pk_bf16_f32 v173, v98, v99\n"
        "v_add_u32 v245, s31, v245\n"
        "v_mfma_f32_16x16x32_bf16 v[100:103], a[100:103], a[228:231], v[100:103]\n"
        "42:\n"
        "s_cbranch_execz 45f\n"
        "buffer_load_dwordx4 v126, s[52:55], s64 offen offset:2048 lds\n"
        "v_mfma_f32_16x16x32_bf16 v[100:103], a[96:99], a[224:227], v[100:103]\n"
        "s_waitcnt vmcnt(3)\n"
        "ds_read_b64_tr_b16 v[88:89], v234 offset:0\n"
        "ds_read_b64_tr_b16 v[90:91], v234 offset:3072\n"
        "v_mfma_f32_16x16x32_bf16 v[104:107], a[120:123], a[224:227], 0\n"
        "ds_read_b64_tr_b16 v[92:93], v235 offset:32\n"
        "v_add_u32 v246, s31, v246\n"
        "v_mfma_f32_16x16x32_bf16 v[104:107], a[124:127], a[228:231], v[104:107]\n"
        "ds_read_b64_tr_b16 v[94:95], v235 offset:3104\n"
        "v_mfma_f32_16x16x32_bf16 v[104:107], a[128:131], a[232:235], v[104:107]\n"
        "s_add_u32 m0, s66, 9216\n"
        "ds_read_b64_tr_b16 v[120:121], v234 offset:64\n"
        "ds_read_b128 v[162:165], v251 offset:0\n"
        "v_mfma_f32_16x16x32_bf16 v[104:107], a[132:135], a[236:239], v[104:107]\n"
        "s_add_u32 s64, s51, 24576\n"
        "ds_read_b64_tr_b16 v[122:123], v234 offset:3136\n"
        "v_cvt_pk_bf16_f32 v174, v100, v101\n"
        "v_mfma_f32_16x16x32_bf16 v[104:107], a[136:139], a[240:243], v[104:107]\n"
        "ds_read_b64_tr_b16 v[216:217], v235 offset:96\n"
        "v_cvt_pk_bf16_f32 v175, v102, v103\n"
        "v_mfma_f32_16x16x32_bf16 v[104:107], a[140:143], a[244:247], v[104:107]\n"
        "ds_read_b64_tr_b16 v[218:219], v235 offset:3168\n"
        "v_mfma_f32_16x16x32_bf16 v[96:99], a[20:23], a[244:247], 0\n"
        "buffer_load_dwordx4 v124, s[52:55], s64 offen lds\n"
        "v_mfma_f32_16x16x32_bf16 v[96:99], a[16:19], a[240:243], v[96:99]\n"
        "ds_read_b64_tr_b16 v[220:221], v238 offset:128\n"
        "ds_read_b64_tr_b16 v[222:223], v238 offset:3200\n"
        "v_mfma_f32_16x16x32_bf16 v[96:99], a[12:15], a[236:239], v[96:99]\n"
        "ds_read_b64_tr_b16 v[224:225], v239 offset:160\n"
        "v_cvt_pk_bf16_f32 v190, v104, v105\n"
        "v_mfma_f32_16x16x32_bf16 v[96:99], a[8:11], a[232:235], v[96:99]\n"
        "ds_read_b64_tr_b16 v[226:227], v239 offset:3232\n"
        "v_cvt_pk_bf16_f32 v191, v106, v107\n"
        "v_mfma_f32_16x16x32_bf16 v[96:99], a[4:7], a[228:231], v[96:99]\n"
        "s_waitcnt lgkmcnt(11)\n"
        "ds_write_b128 v236, v[88:91] offset:0\n"
        "v_mfma_f32_16x16x32_bf16 v[96:99], a[0:3], a[224:227], v[96:99]\n"
        "v_mfma_f32_16x16x32_bf16 v[64:67], v[152:155], v[156:159], v[0:3]\n"
        "s_waitcnt lgkmcnt(8)\n"
        "v_mfma_f32_16x16x32_bf16 v[64:67], v[168:171], v[160:163], v[64:67]\n"
        "v_mfma_f32_16x16x32_bf16 v[68:71], v[172:175], v[156:159], v[4:7]\n"
        "buffer_load_dwordx4 v125, s[52:55], s64 offen offset:1024 lds\n"
        "v_mfma_f32_16x16x32_bf16 v[68:71], v[188:191], v[164:167], v[68:71]\n"
        "ds_write_b128 v236, v[92:95] offset:384\n"
        "v_mfma_f32_16x16x32_bf16 v[100:103], a[24:27], a[224:227], 0\n"
        "v_mfma_f32_16x16x32_bf16 v[100:103], a[28:31], a[228:231], v[100:103]\n"
        "v_mul_f32 v96, s50, v96\n"
        "v_mul_f32 v97, s50, v97\n"
        "v_mfma_f32_16x16x32_bf16 v[100:103], a[32:35], a[232:235], v[100:103]\n"
        "v_mul_f32 v98, s50, v98\n"
        "v_mul_f32 v99, s50, v99\n"
        "v_mfma_f32_16x16x32_bf16 v[100:103], a[36:39], a[236:239], v[100:103]\n"
        "v_cvt_pk_bf16_f32 v176, v96, v97\n"
        "v_cvt_pk_bf16_f32 v177, v98, v99\n"
        "v_mfma_f32_16x16x32_bf16 v[100:103], a[40:43], a[240:243], v[100:103]\n"
        "s_waitcnt lgkmcnt(8)\n"
        "ds_write_b128 v236, v[120:123] offset:768\n"
        "v_mfma_f32_16x16x32_bf16 v[100:103], a[44:47], a[244:247], v[100:103]\n"
        "v_exp_f32 v64, v64\n"
        "v_mfma_f32_16x16x32_bf16 v[104:107], a[68:71], a[244:247], 0\n"
        "v_exp_f32 v65, v65\n"
        "v_mfma_f32_16x16x32_bf16 v[104:107], a[64:67], a[240:243], v[104:107]\n"
        "v_exp_f32 v66, v66\n"
        "v_mfma_f32_16x16x32_bf16 v[104:107], a[60:63], a[236:239], v[104:107]\n"
        "buffer_load_dwordx4 v126, s[52:55], s64 offen offset:2048 lds\n"
        "v_mfma_f32_16x16x32_bf16 v[104:107], a[56:59], a[232:235], v[104:107]\n"
        "ds_read_b128 v[182:185], v251 offset:512\n"
        "v_mul_f32 v100, s50, v100\n"
        "v_mfma_f32_16x16x32_bf16 v[104:107], a[52:55], a[228:231], v[104:107]\n"
        "v_mul_f32 v101, s50, v101\n"
        "v_mul_f32 v102, s50, v102\n"
        "v_mfma_f32_16x16x32_bf16 v[104:107], a[48:51], a[224:227], v[104:107]\n"
        "v_mul_f32 v103, s50, v103\n"
        "v_cvt_pk_bf16_f32 v178, v100, v101\n"
        "v_mfma_f32_16x16x32_bf16 v[96:99], a[224:227], a[144:147], 0\n"
        "ds_read_b128 a[248:251], v240 offset:12288\n"
        "v_cvt_pk_bf16_f32 v179, v102, v103\n"
        "v_mfma_f32_16x16x32_bf16 v[96:99], a[228:231], a[148:151], v[96:99]\n"
        "s_add_u32 m0, s66, 12288\n"
        "s_waitcnt lgkmcnt(9)\n"
        "ds_write_b128 v236, v[216:219] offset:1152\n"
        "v_mfma_f32_16x16x32_bf16 v[96:99], a[232:235], a[152:155], v[96:99]\n"
        "s_add_u32 s64, s51, 12288\n"
        "v_mul_f32 v104, s50, v104\n"
        "v_mul_f32 v105, s50, v105\n"
        "v_mfma_f32_16x16x32_bf16 v[96:99], a[236:239], a[156:159], v[96:99]\n"
        "v_mul_f32 v106, s50, v106\n"
        "v_mul_f32 v107, s50, v107\n"
        "v_mfma_f32_16x16x32_bf16 v[96:99], a[240:243], a[160:163], v[96:99]\n"
        "v_cvt_pk_bf16_f32 v180, v104, v105\n"
        "v_cvt_pk_bf16_f32 v181, v106, v107\n"
        "v_mfma_f32_16x16x32_bf16 v[96:99], a[244:247], a[164:167], v[96:99]\n"
        "buffer_load_dwordx4 v127, s[52:55], s64 offen lds\n"
        "v_mfma_f32_16x16x32_bf16 v[72:75], v[152:155], v[176:179], v[16:19]\n"
        "s_waitcnt lgkmcnt(2)\n"
        "ds_write_b128 v237, v[220:223] offset:0\n"
        "v_mfma_f32_16x16x32_bf16 v[72:75], v[168:171], v[180:183], v[72:75]\n"
        "v_mov_b64 v[186:187], v[180:181]\n"
        "v_cvt_pk_bf16_f32 v64, v64, v65\n"
        "v_mfma_f32_16x16x32_bf16 v[76:79], v[172:175], v[176:179], v[20:23]\n"
        "v_exp_f32 v67, v67\n"
        "v_mfma_f32_16x16x32_bf16 v[76:79], v[188:191], v[184:187], v[76:79]\n"
        "v_cvt_pk_bf16_f32 v130, v96, v97\n"
        "v_cvt_pk_bf16_f32 v131, v98, v99\n"
        "v_mfma_f32_16x16x32_bf16 v[100:103], a[244:247], a[188:191], 0\n"
        "ds_write_b128 v237, v[224:227] offset:768\n"
        "v_mfma_f32_16x16x32_bf16 v[100:103], a[240:243], a[184:187], v[100:103]\n"
        "v_exp_f32 v68, v68\n"
        "v_mfma_f32_16x16x32_bf16 v[100:103], a[236:239], a[180:183], v[100:103]\n"
        "v_exp_f32 v69, v69\n"
        "v_mfma_f32_16x16x32_bf16 v[100:103], a[232:235], a[176:179], v[100:103]\n"
        "v_exp_f32 v70, v70\n"
        "v_mfma_f32_16x16x32_bf16 v[100:103], a[228:231], a[172:175], v[100:103]\n"
        "buffer_load_dwordx4 v232, s[52:55], s64 offen offset:1024 lds\n"
        "v_mfma_f32_16x16x32_bf16 v[100:103], a[224:227], a[168:171], v[100:103]\n"
        "ds_read_b128 a[216:219], v241 offset:12288\n"
        "ds_read_b128 a[220:223], v240 offset:12416\n"
        "v_mfma_f32_16x16x32_bf16 v[104:107], a[224:227], a[192:195], 0\n"
        "ds_read_b128 a[224:227], v241 offset:12416\n"
        "v_cvt_pk_bf16_f32 v65, v66, v67\n"
        "v_mfma_f32_16x16x32_bf16 v[104:107], a[228:231], a[196:199], v[104:107]\n"
        "ds_read_b128 a[228:231], v240 offset:12544\n"
        "v_cvt_pk_bf16_f32 v66, v68, v69\n"
        "v_mfma_f32_16x16x32_bf16 v[104:107], a[232:235], a[200:203], v[104:107]\n"
        "ds_read_b128 a[232:235], v241 offset:12544\n"
        "v_cvt_pk_bf16_f32 v138, v100, v101\n"
        "v_mfma_f32_16x16x32_bf16 v[104:107], a[236:239], a[204:207], v[104:107]\n"
        "v_cvt_pk_bf16_f32 v139, v102, v103\n"
        "v_mfma_f32_16x16x32_bf16 v[104:107], a[240:243], a[208:211], v[104:107]\n"
        "v_exp_f32 v71, v71\n"
        "v_mfma_f32_16x16x32_bf16 v[104:107], a[244:247], a[212:215], v[104:107]\n"
        "s_waitcnt lgkmcnt(8)\n"
        "v_exp_f32 v72, v72\n"
        "v_mfma_f32_16x16x32_bf16 v[96:99], a[72:75], a[248:251], 0\n"
        "s_waitcnt lgkmcnt(4)\n"
        "buffer_load_dwordx4 v233, s[52:55], s64 offen offset:2048 lds\n"
        "v_mfma_f32_16x16x32_bf16 v[96:99], a[76:79], a[216:219], v[96:99]\n"
        "s_waitcnt lgkmcnt(3)\n"
        "v_exp_f32 v73, v73\n"
        "v_mfma_f32_16x16x32_bf16 v[96:99], a[80:83], a[220:223], v[96:99]\n"
        "s_waitcnt lgkmcnt(2)\n"
        "v_exp_f32 v74, v74\n"
        "v_mfma_f32_16x16x32_bf16 v[96:99], a[84:87], a[224:227], v[96:99]\n"
        "s_waitcnt lgkmcnt(1)\n"
        "v_cvt_pk_bf16_f32 v146, v104, v105\n"
        "v_cvt_pk_bf16_f32 v147, v106, v107\n"
        "v_mfma_f32_16x16x32_bf16 v[96:99], a[88:91], a[228:231], v[96:99]\n"
        "s_waitcnt vmcnt(3) lgkmcnt(0)\n"
        "ds_read_b64_tr_b16 v[88:89], v234 offset:6144\n"
        "v_cvt_pk_bf16_f32 v67, v70, v71\n"
        "v_mfma_f32_16x16x32_bf16 v[96:99], a[92:95], a[232:235], v[96:99]\n"
        "ds_read_b64_tr_b16 v[90:91], v234 offset:9216\n"
        "v_cvt_pk_bf16_f32 v72, v72, v73\n"
        "v_mfma_f32_16x16x32_bf16 v[100:103], a[116:119], a[232:235], 0\n"
        "s_add_u32 m0, s66, 15360\n"
        "ds_read_b64_tr_b16 v[92:93], v235 offset:6176\n"
        "v_mfma_f32_16x16x32_bf16 v[100:103], a[112:115], a[228:231], v[100:103]\n"
        "s_add_u32 s64, s51, 13824\n"
        "ds_read_b64_tr_b16 v[94:95], v235 offset:9248\n"
        "v_cvt_pk_bf16_f32 v192, v96, v97\n"
        "v_mfma_f32_16x16x32_bf16 v[100:103], a[108:111], a[224:227], v[100:103]\n"
        "ds_read_b64_tr_b16 v[120:121], v234 offset:6208\n"
        "v_cvt_pk_bf16_f32 v193, v98, v99\n"
        "v_mfma_f32_16x16x32_bf16 v[100:103], a[104:107], a[220:223], v[100:103]\n"
        "ds_read_b64_tr_b16 v[122:123], v234 offset:9280\n"
        "v_mfma_f32_16x16x32_bf16 v[100:103], a[100:103], a[216:219], v[100:103]\n"
        "buffer_load_dwordx4 v127, s[52:55], s64 offen lds\n"
        "v_mfma_f32_16x16x32_bf16 v[100:103], a[96:99], a[248:251], v[100:103]\n"
        "ds_read_b64_tr_b16 v[216:217], v235 offset:6240\n"
        "ds_read_b64_tr_b16 v[218:219], v235 offset:9312\n"
        "v_mfma_f32_16x16x32_bf16 v[104:107], a[120:123], a[248:251], 0\n"
        "ds_read_b64_tr_b16 v[220:221], v238 offset:6272\n"
        "v_mfma_f32_16x16x32_bf16 v[104:107], a[124:127], a[216:219], v[104:107]\n"
        "ds_read_b64_tr_b16 v[222:223], v238 offset:9344\n"
        "v_mfma_f32_16x16x32_bf16 v[104:107], a[128:131], a[220:223], v[104:107]\n"
        "s_cmp_ge_u32 s58, 2816\n"
        "ds_read_b64_tr_b16 v[224:225], v239 offset:6304\n"
        "v_cvt_pk_bf16_f32 v194, v100, v101\n"
        "v_mfma_f32_16x16x32_bf16 v[104:107], a[132:135], a[224:227], v[104:107]\n"
        "s_cselect_b32 s20, s3, 0\n"
        "ds_read_b64_tr_b16 v[226:227], v239 offset:9376\n"
        "v_cvt_pk_bf16_f32 v195, v102, v103\n"
        "v_mfma_f32_16x16x32_bf16 v[104:107], a[136:139], a[228:231], v[104:107]\n"
        "s_lshl_b32 s21, s27, 8\n"
        "s_waitcnt lgkmcnt(10)\n"
        "ds_write_b128 v236, v[88:91] offset:6144\n"
        "v_mfma_f32_16x16x32_bf16 v[104:107], a[140:143], a[232:235], v[104:107]\n"
        "s_add_u32 s21, s21, 704\n"
        "v_exp_f32 v75, v75\n"
        "v_mfma_f32_16x16x32_bf16 v[96:99], a[20:23], a[232:235], 0\n"
        "v_exp_f32 v76, v76\n"
        "v_mfma_f32_16x16x32_bf16 v[96:99], a[16:19], a[228:231], v[96:99]\n"
        "v_exp_f32 v77, v77\n"
        "v_mfma_f32_16x16x32_bf16 v[96:99], a[12:15], a[224:227], v[96:99]\n"
        "buffer_load_dwordx4 v232, s[52:55], s64 offen offset:1024 lds\n"
        "v_mfma_f32_16x16x32_bf16 v[96:99], a[8:11], a[220:223], v[96:99]\n"
        "s_waitcnt lgkmcnt(9)\n"
        "ds_write_b128 v236, v[92:95] offset:6528\n"
        "v_mfma_f32_16x16x32_bf16 v[96:99], a[4:7], a[216:219], v[96:99]\n"
        "v_cvt_pk_bf16_f32 v208, v104, v105\n"
        "v_cvt_pk_bf16_f32 v209, v106, v107\n"
        "v_mfma_f32_16x16x32_bf16 v[96:99], a[0:3], a[248:251], v[96:99]\n"
        "v_exp_f32 v78, v78\n"
        "v_mfma_f32_16x16x32_bf16 v[100:103], a[24:27], a[248:251], 0\n"
        "v_exp_f32 v79, v79\n"
        "v_mfma_f32_16x16x32_bf16 v[100:103], a[28:31], a[216:219], v[100:103]\n"
        "ds_read_b128 v[162:165], v252 offset:0\n"
        "v_cvt_pk_bf16_f32 v73, v74, v75\n"
        "v_mfma_f32_16x16x32_bf16 v[100:103], a[32:35], a[220:223], v[100:103]\n"
        "ds_read_b128 v[182:185], v252 offset:512\n"
        "v_mul_f32 v96, s50, v96\n"
        "v_mfma_f32_16x16x32_bf16 v[100:103], a[36:39], a[224:227], v[100:103]\n"
        "buffer_load_dwordx4 v233, s[52:55], s64 offen offset:2048 lds\n"
        "v_mfma_f32_16x16x32_bf16 v[100:103], a[40:43], a[228:231], v[100:103]\n"
        "s_waitcnt lgkmcnt(10)\n"
        "ds_write_b128 v236, v[120:123] offset:6912\n"
        "v_mfma_f32_16x16x32_bf16 v[100:103], a[44:47], a[232:235], v[100:103]\n"
        "v_mul_f32 v97, s50, v97\n"
        "v_mul_f32 v98, s50, v98\n"
        "v_mfma_f32_16x16x32_bf16 v[104:107], a[68:71], a[232:235], 0\n"
        "v_mul_f32 v99, s50, v99\n"
        "v_cvt_pk_bf16_f32 v196, v96, v97\n"
        "v_mfma_f32_16x16x32_bf16 v[104:107], a[64:67], a[228:231], v[104:107]\n"
        "v_cvt_pk_bf16_f32 v197, v98, v99\n"
        "v_cvt_pk_bf16_f32 v74, v76, v77\n"
        "v_mfma_f32_16x16x32_bf16 v[104:107], a[60:63], a[224:227], v[104:107]\n"
        "s_waitcnt lgkmcnt(9)\n"
        "ds_write_b128 v236, v[216:219] offset:7296\n"
        "v_mfma_f32_16x16x32_bf16 v[104:107], a[56:59], a[220:223], v[104:107]\n"
        "v_mul_f32 v100, s50, v100\n"
        "v_mul_f32 v101, s50, v101\n"
        "v_mfma_f32_16x16x32_bf16 v[104:107], a[52:55], a[216:219], v[104:107]\n"
        "s_add_u32 m0, s41, 0\n"
        "v_mul_f32 v102, s50, v102\n"
        "v_mul_f32 v103, s50, v103\n"
        "v_mfma_f32_16x16x32_bf16 v[104:107], a[48:51], a[248:251], v[104:107]\n"
        "v_cvt_pk_bf16_f32 v198, v100, v101\n"
        "v_cvt_pk_bf16_f32 v199, v102, v103\n"
        "v_mfma_f32_16x16x32_bf16 v[68:71], v[192:195], v[156:159], v[8:11]\n"
        "s_waitcnt lgkmcnt(3)\n"
        "buffer_load_dwordx4 v242, s[8:11], s47 offen lds\n"
        "v_mfma_f32_16x16x32_bf16 v[68:71], v[208:211], v[160:163], v[68:71]\n"
        "ds_write_b128 v237, v[220:223] offset:6144\n"
        "v_mfma_f32_16x16x32_bf16 v[96:99], a[248:251], a[144:147], 0\n"
        "v_cvt_pk_bf16_f32 v75, v78, v79\n"
        "v_mul_f32 v104, s50, v104\n"
        "v_mfma_f32_16x16x32_bf16 v[96:99], a[216:219], a[148:151], v[96:99]\n"
        "s_add_u32 m0, s41, 1024\n"
        "v_mul_f32 v105, s50, v105\n"
        "v_mul_f32 v106, s50, v106\n"
        "v_mfma_f32_16x16x32_bf16 v[96:99], a[220:223], a[152:155], v[96:99]\n"
        "v_mul_f32 v107, s50, v107\n"
        "v_cvt_pk_bf16_f32 v200, v104, v105\n"
        "v_mfma_f32_16x16x32_bf16 v[96:99], a[224:227], a[156:159], v[96:99]\n"
        "buffer_load_dwordx4 v243, s[8:11], s47 offen lds\n"
        "v_mfma_f32_16x16x32_bf16 v[96:99], a[228:231], a[160:163], v[96:99]\n"
        "ds_read_b128 v[202:205], v251 offset:4096\n"
        "v_cvt_pk_bf16_f32 v201, v106, v107\n"
        "v_mfma_f32_16x16x32_bf16 v[96:99], a[232:235], a[164:167], v[96:99]\n"
        "ds_write_b128 v237, v[224:227] offset:6912\n"
        "v_mfma_f32_16x16x32_bf16 v[76:79], v[192:195], v[176:179], v[24:27]\n"
        "s_waitcnt lgkmcnt(5)\n"
        "s_add_u32 m0, s41, 2048\n"
        "v_mov_b64 v[206:207], v[200:201]\n"
        "v_mfma_f32_16x16x32_bf16 v[76:79], v[208:211], v[180:183], v[76:79]\n"
        "v_exp_f32 v68, v68\n"
        "v_mfma_f32_16x16x32_bf16 v[100:103], a[232:235], a[188:191], 0\n"
        "v_exp_f32 v69, v69\n"
        "v_mfma_f32_16x16x32_bf16 v[100:103], a[228:231], a[184:187], v[100:103]\n"
        "buffer_load_dwordx4 v244, s[8:11], s47 offen lds\n"
        "v_mfma_f32_16x16x32_bf16 v[100:103], a[224:227], a[180:183], v[100:103]\n"
        "v_cvt_pk_bf16_f32 v132, v96, v97\n"
        "v_cvt_pk_bf16_f32 v133, v98, v99\n"
        "v_mfma_f32_16x16x32_bf16 v[100:103], a[220:223], a[176:179], v[100:103]\n"
        "s_add_u32 m0, s41, 12288\n"
        "ds_read_b128 a[236:239], v240 offset:13824\n"
        "v_cvt_pk_bf16_f32 v68, v68, v69\n"
        "v_mfma_f32_16x16x32_bf16 v[100:103], a[216:219], a[172:175], v[100:103]\n"
        "s_waitcnt vmcnt(3)\n"
        "ds_read_b64_tr_b16 v[220:221], v234 offset:12288\n"
        "v_mfma_f32_16x16x32_bf16 v[100:103], a[248:251], a[168:171], v[100:103]\n"
        "ds_read_b64_tr_b16 v[222:223], v234 offset:15360\n"
        "v_mfma_f32_16x16x32_bf16 v[80:83], v[152:155], v[196:199], v[32:35]\n"
        "s_waitcnt lgkmcnt(4)\n"
        "buffer_load_dwordx4 v242, s[8:11], s35 offen lds\n"
        "v_mfma_f32_16x16x32_bf16 v[80:83], v[168:171], v[200:203], v[80:83]\n"
        "ds_read_b64_tr_b16 v[224:225], v235 offset:12320\n"
        "ds_read_b64_tr_b16 v[226:227], v235 offset:15392\n"
        "v_mfma_f32_16x16x32_bf16 v[104:107], a[248:251], a[192:195], 0\n"
        "s_add_u32 m0, s41, 13312\n"
        "ds_read_b128 a[240:243], v241 offset:13824\n"
        "ds_read_b64_tr_b16 v[88:89], v234 offset:12352\n"
        "v_mfma_f32_16x16x32_bf16 v[104:107], a[216:219], a[196:199], v[104:107]\n"
        "ds_read_b128 a[244:247], v240 offset:13952\n"
        "ds_read_b64_tr_b16 v[90:91], v234 offset:15424\n"
        "v_mfma_f32_16x16x32_bf16 v[104:107], a[220:223], a[200:203], v[104:107]\n"
        "ds_read_b128 a[248:251], v241 offset:13952\n"
        "ds_read_b64_tr_b16 v[92:93], v235 offset:12384\n"
        "v_mfma_f32_16x16x32_bf16 v[104:107], a[224:227], a[204:207], v[104:107]\n"
        "ds_read_b128 a[216:219], v240 offset:14080\n"
        "ds_read_b64_tr_b16 v[94:95], v235 offset:15456\n"
        "v_mfma_f32_16x16x32_bf16 v[104:107], a[228:231], a[208:211], v[104:107]\n"
        "buffer_load_dwordx4 v243, s[8:11], s35 offen lds\n"
        "v_mfma_f32_16x16x32_bf16 v[104:107], a[232:235], a[212:215], v[104:107]\n"
        "s_add_u32 m0, s41, 14336\n"
        "ds_read_b128 a[220:223], v241 offset:14080\n"
        "ds_read_b64_tr_b16 v[120:121], v238 offset:12416\n"
        "v_mfma_f32_16x16x32_bf16 v[84:87], v[172:175], v[196:199], v[36:39]\n"
        "ds_read_b64_tr_b16 v[122:123], v238 offset:15488\n"
        "ds_read_b64_tr_b16 v[124:125], v239 offset:12448\n"
        "v_mfma_f32_16x16x32_bf16 v[84:87], v[188:191], v[204:207], v[84:87]\n"
        "s_waitcnt lgkmcnt(15)\n"
        "ds_read_b64_tr_b16 v[126:127], v239 offset:15520\n"
        "v_cvt_pk_bf16_f32 v140, v100, v101\n"
        "v_mfma_f32_16x16x32_bf16 v[96:99], a[72:75], a[236:239], 0\n"
        "s_waitcnt lgkmcnt(12)\n"
        "buffer_load_dwordx4 v244, s[8:11], s35 offen lds\n"
        "v_mfma_f32_16x16x32_bf16 v[96:99], a[76:79], a[240:243], v[96:99]\n"
        "s_waitcnt lgkmcnt(10)\n"
        "v_cvt_pk_bf16_f32 v141, v102, v103\n"
        "v_cvt_pk_bf16_f32 v148, v104, v105\n"
        "v_mfma_f32_16x16x32_bf16 v[96:99], a[80:83], a[244:247], v[96:99]\n"
        "s_waitcnt lgkmcnt(8)\n"
        "s_mov_b32 m0, s48\n"
        "v_cvt_pk_bf16_f32 v149, v106, v107\n"
        "v_mfma_f32_16x16x32_bf16 v[96:99], a[84:87], a[248:251], v[96:99]\n"
        "s_waitcnt lgkmcnt(6)\n"
        "v_exp_f32 v70, v70\n"
        "v_mfma_f32_16x16x32_bf16 v[96:99], a[88:91], a[216:219], v[96:99]\n"
        "s_waitcnt lgkmcnt(4)\n"
        "buffer_load_dwordx4 v249, s[12:15], s36 offen offset:0 lds\n"
        "v_mfma_f32_16x16x32_bf16 v[96:99], a[92:95], a[220:223], v[96:99]\n"
        "ds_read_b128 v[202:205], v252 offset:4096\n"
        "v_xor_b32 v116, 32, v249\n"
        "v_mfma_f32_16x16x32_bf16 v[100:103], a[116:119], a[220:223], 0\n"
        "ds_write_b128 v236, v[220:223] offset:12288\n"
        "v_mfma_f32_16x16x32_bf16 v[100:103], a[112:115], a[216:219], v[100:103]\n"
        "v_exp_f32 v71, v71\n"
        "v_mfma_f32_16x16x32_bf16 v[100:103], a[108:111], a[248:251], v[100:103]\n"
        "v_exp_f32 v76, v76\n"
        "v_mfma_f32_16x16x32_bf16 v[100:103], a[104:107], a[244:247], v[100:103]\n"
        "v_cvt_pk_bf16_f32 v212, v96, v97\n"
        "v_cvt_pk_bf16_f32 v213, v98, v99\n"
        "v_mfma_f32_16x16x32_bf16 v[100:103], a[100:103], a[240:243], v[100:103]\n"
        "buffer_load_dwordx4 v116, s[12:15], s36 offen offset:1024 lds\n"
        "v_mfma_f32_16x16x32_bf16 v[100:103], a[96:99], a[236:239], v[100:103]\n"
        "ds_write_b128 v236, v[224:227] offset:12672\n"
        "v_mfma_f32_16x16x32_bf16 v[104:107], a[120:123], a[236:239], 0\n"
        "v_exp_f32 v77, v77\n"
        "v_mfma_f32_16x16x32_bf16 v[104:107], a[124:127], a[240:243], v[104:107]\n"
        "v_exp_f32 v78, v78\n"
        "v_mfma_f32_16x16x32_bf16 v[104:107], a[128:131], a[244:247], v[104:107]\n"
        "v_exp_f32 v79, v79\n"
        "v_mfma_f32_16x16x32_bf16 v[104:107], a[132:135], a[248:251], v[104:107]\n"
        "v_cvt_pk_bf16_f32 v214, v100, v101\n"
        "v_cvt_pk_bf16_f32 v215, v102, v103\n"
        "v_mfma_f32_16x16x32_bf16 v[104:107], a[136:139], a[216:219], v[104:107]\n"
        "ds_write_b128 v236, v[88:91] offset:13056\n"
        "v_mfma_f32_16x16x32_bf16 v[104:107], a[140:143], a[220:223], v[104:107]\n"
        "v_cvt_pk_bf16_f32 v69, v70, v71\n"
        "v_cvt_pk_bf16_f32 v76, v76, v77\n"
        "v_mfma_f32_16x16x32_bf16 v[96:99], a[20:23], a[220:223], 0\n"
        "v_exp_f32 v80, v80\n"
        "v_mfma_f32_16x16x32_bf16 v[96:99], a[16:19], a[216:219], v[96:99]\n"
        "v_exp_f32 v81, v81\n"
        "v_mfma_f32_16x16x32_bf16 v[96:99], a[12:15], a[248:251], v[96:99]\n"
        "s_lshr_b32 s46, s56, 6\n"
        "v_cvt_pk_bf16_f32 v230, v104, v105\n"
        "v_cvt_pk_bf16_f32 v231, v106, v107\n"
        "v_mfma_f32_16x16x32_bf16 v[96:99], a[8:11], a[244:247], v[96:99]\n"
        "s_mul_i32 s46, s46, 1572864\n"
        "ds_write_b128 v236, v[92:95] offset:13440\n"
        "v_mfma_f32_16x16x32_bf16 v[96:99], a[4:7], a[240:243], v[96:99]\n"
        "s_add_u32 s46, s46, s37\n"
        "v_cvt_pk_bf16_f32 v77, v78, v79\n"
        "v_cvt_pk_bf16_f32 v80, v80, v81\n"
        "v_mfma_f32_16x16x32_bf16 v[96:99], a[0:3], a[236:239], v[96:99]\n"
        "s_cmp_eq_u32 s46, 100663296\n"
        "v_exp_f32 v82, v82\n"
        "v_mfma_f32_16x16x32_bf16 v[108:111], v[212:215], v[156:159], v[12:15]\n"
        "s_cselect_b32 s46, 0, s46\n"
        "v_exp_f32 v83, v83\n"
        "v_mfma_f32_16x16x32_bf16 v[108:111], v[228:231], v[164:167], v[108:111]\n"
        "s_and_b32 s43, s56, 63\n"
        "v_exp_f32 v84, v84\n"
        "v_mfma_f32_16x16x32_bf16 v[112:115], v[212:215], v[176:179], v[28:31]\n"
        "s_mul_i32 s43, s43, 3072\n"
        "v_mul_f32 v96, s50, v96\n"
        "v_mul_f32 v97, s50, v97\n"
        "v_mfma_f32_16x16x32_bf16 v[112:115], v[228:231], v[184:187], v[112:115]\n"
        "s_add_u32 s44, s43, 3072\n"
        "v_mul_f32 v98, s50, v98\n"
        "v_mul_f32 v99, s50, v99\n"
        "v_mfma_f32_16x16x32_bf16 v[116:119], v[212:215], v[196:199], v[44:47]\n"
        "s_waitcnt lgkmcnt(4)\n"
        "ds_write_b128 v237, v[120:123] offset:12288\n"
        "v_mfma_f32_16x16x32_bf16 v[116:119], v[228:231], v[204:207], v[116:119]\n"
        "s_cmp_eq_u32 s44, 196608\n"
        "v_cvt_pk_bf16_f32 v216, v96, v97\n"
        "v_cvt_pk_bf16_f32 v217, v98, v99\n"
        "v_mfma_f32_16x16x32_bf16 v[100:103], a[24:27], a[236:239], 0\n"
        "s_cselect_b32 s44, 0, s44\n"
        "v_cvt_pk_bf16_f32 v81, v82, v83\n"
        "v_add_u32 v240, s45, v240\n"
        "v_mfma_f32_16x16x32_bf16 v[100:103], a[28:31], a[240:243], v[100:103]\n"
        "s_add_u32 s39, s46, s44\n"
        "v_exp_f32 v85, v85\n"
        "v_mfma_f32_16x16x32_bf16 v[100:103], a[32:35], a[244:247], v[100:103]\n"
        "s_add_u32 s43, s43, s46\n"
        "v_exp_f32 v86, v86\n"
        "v_mfma_f32_16x16x32_bf16 v[100:103], a[36:39], a[248:251], v[100:103]\n"
        "s_add_u32 s38, s43, 1536\n"
        "ds_read_b128 v[222:225], v251 offset:4608\n"
        "v_cvt_pk_bf16_f32 v82, v84, v85\n"
        "v_mfma_f32_16x16x32_bf16 v[100:103], a[40:43], a[216:219], v[100:103]\n"
        "ds_write_b128 v237, v[124:127] offset:13056\n"
        "v_mfma_f32_16x16x32_bf16 v[100:103], a[44:47], a[220:223], v[100:103]\n"
        "v_exp_f32 v87, v87\n"
        "v_add_u32 v241, s45, v241\n"
        "v_cvt_pk_bf16_f32 v83, v86, v87\n"
        "v_mfma_f32_16x16x32_bf16 v[84:87], v[192:195], v[196:199], v[40:43]\n"
        "v_exp_f32 v108, v108\n"
        "v_mfma_f32_16x16x32_bf16 v[84:87], v[208:211], v[200:203], v[84:87]\n"
        "v_exp_f32 v109, v109\n"
        "v_mfma_f32_16x16x32_bf16 v[104:107], a[68:71], a[220:223], 0\n"
        "v_mul_f32 v100, s50, v100\n"
        "v_mul_f32 v101, s50, v101\n"
        "v_mfma_f32_16x16x32_bf16 v[104:107], a[64:67], a[216:219], v[104:107]\n"
        "v_mul_f32 v102, s50, v102\n"
        "v_mul_f32 v103, s50, v103\n"
        "v_mfma_f32_16x16x32_bf16 v[104:107], a[60:63], a[248:251], v[104:107]\n"
        "v_cvt_pk_bf16_f32 v218, v100, v101\n"
        "v_cvt_pk_bf16_f32 v219, v102, v103\n"
        "v_mfma_f32_16x16x32_bf16 v[104:107], a[56:59], a[244:247], v[104:107]\n"
        "s_waitcnt lgkmcnt(0)\n"
        "v_exp_f32 v110, v110\n"
        "v_mfma_f32_16x16x32_bf16 v[104:107], a[52:55], a[240:243], v[104:107]\n"
        "s_barrier\n"
        "v_exp_f32 v111, v111\n"
        "v_mfma_f32_16x16x32_bf16 v[104:107], a[48:51], a[236:239], v[104:107]\n"
        "v_exp_f32 v112, v112\n"
        "v_mfma_f32_16x16x32_bf16 v[96:99], a[236:239], a[144:147], 0\n"
        "v_exp_f32 v113, v113\n"
        "v_mfma_f32_16x16x32_bf16 v[96:99], a[240:243], a[148:151], v[96:99]\n"
        "v_exp_f32 v114, v114\n"
        "v_mfma_f32_16x16x32_bf16 v[96:99], a[244:247], a[152:155], v[96:99]\n"
        "v_exp_f32 v115, v115\n"
        "v_mfma_f32_16x16x32_bf16 v[96:99], a[248:251], a[156:159], v[96:99]\n"
        "v_mul_f32 v104, s50, v104\n"
        "v_mul_f32 v105, s50, v105\n"
        "v_mfma_f32_16x16x32_bf16 v[96:99], a[216:219], a[160:163], v[96:99]\n"
        "v_mul_f32 v106, s50, v106\n"
        "v_mul_f32 v107, s50, v107\n"
        "v_mfma_f32_16x16x32_bf16 v[96:99], a[220:223], a[164:167], v[96:99]\n"
        "v_cvt_pk_bf16_f32 v220, v104, v105\n"
        "v_cvt_pk_bf16_f32 v221, v106, v107\n"
        "v_mfma_f32_16x16x32_bf16 v[88:91], v[152:155], v[216:219], v[48:51]\n"
        "ds_read_b128 v[156:159], v247 offset:0\n"
        "v_mov_b64 v[226:227], v[220:221]\n"
        "v_mfma_f32_16x16x32_bf16 v[88:91], v[168:171], v[220:223], v[88:91]\n"
        "ds_read_b128 v[164:167], v247 offset:4096\n"
        "v_cvt_pk_bf16_f32 v70, v108, v109\n"
        "v_mfma_f32_16x16x32_bf16 v[92:95], v[172:175], v[216:219], v[52:55]\n"
        "s_mov_b32 s56, s57\n"
        "ds_read_b128 v[160:163], v247 offset:256\n"
        "v_cvt_pk_bf16_f32 v134, v96, v97\n"
        "v_mfma_f32_16x16x32_bf16 v[92:95], v[188:191], v[224:227], v[92:95]\n"
        "s_mov_b32 s57, s58\n"
        "ds_read_b128 v[222:225], v252 offset:4608\n"
        "v_cvt_pk_bf16_f32 v135, v98, v99\n"
        "v_mfma_f32_16x16x32_bf16 v[100:103], a[220:223], a[188:191], 0\n"
        "s_xor_b32 s48, s48, 8192\n"
        "ds_read_b128 v[152:155], v247 offset:4352\n"
        "v_cvt_pk_bf16_f32 v71, v110, v111\n"
        "v_mfma_f32_16x16x32_bf16 v[100:103], a[216:219], a[184:187], v[100:103]\n"
        "v_exp_f32 v116, v116\n"
        "v_mfma_f32_16x16x32_bf16 v[100:103], a[248:251], a[180:183], v[100:103]\n"
        "v_exp_f32 v117, v117\n"
        "v_mfma_f32_16x16x32_bf16 v[100:103], a[244:247], a[176:179], v[100:103]\n"
        "v_exp_f32 v118, v118\n"
        "v_mfma_f32_16x16x32_bf16 v[100:103], a[240:243], a[172:175], v[100:103]\n"
        "v_exp_f32 v119, v119\n"
        "v_mfma_f32_16x16x32_bf16 v[100:103], a[236:239], a[168:171], v[100:103]\n"
        "v_exp_f32 v84, v84\n"
        "v_mfma_f32_16x16x32_bf16 v[104:107], a[236:239], a[192:195], 0\n"
        "ds_read_b128 a[236:239], v248 offset:0\n"
        "v_cvt_pk_bf16_f32 v78, v112, v113\n"
        "v_mfma_f32_16x16x32_bf16 v[104:107], a[240:243], a[196:199], v[104:107]\n"
        "ds_read_b128 a[240:243], v248 offset:6144\n"
        "v_cvt_pk_bf16_f32 v79, v114, v115\n"
        "v_mfma_f32_16x16x32_bf16 v[104:107], a[244:247], a[200:203], v[104:107]\n"
        "ds_read_b128 a[244:247], v254 offset:16\n"
        "v_cvt_pk_bf16_f32 v142, v100, v101\n"
        "v_mfma_f32_16x16x32_bf16 v[104:107], a[248:251], a[204:207], v[104:107]\n"
        "ds_read_b128 a[248:251], v254 offset:6160\n"
        "v_cvt_pk_bf16_f32 v143, v102, v103\n"
        "v_mfma_f32_16x16x32_bf16 v[104:107], a[216:219], a[208:211], v[104:107]\n"
        "v_exp_f32 v85, v85\n"
        "v_mfma_f32_16x16x32_bf16 v[104:107], a[220:223], a[212:215], v[104:107]\n"
        "v_exp_f32 v86, v86\n"
        "v_mfma_f32_16x16x32_bf16 v[108:111], v[192:195], v[216:219], v[56:59]\n"
        "s_waitcnt lgkmcnt(5)\n"
        "ds_read_b128 a[216:219], v248 offset:32\n"
        "v_cvt_pk_bf16_f32 v84, v84, v85\n"
        "v_mfma_f32_16x16x32_bf16 v[108:111], v[208:211], v[220:223], v[108:111]\n"
        "v_exp_f32 v87, v87\n"
        "v_mfma_f32_16x16x32_bf16 v[112:115], v[212:215], v[216:219], v[60:63]\n"
        "ds_read_b128 a[220:223], v248 offset:6176\n"
        "v_cvt_pk_bf16_f32 v150, v104, v105\n"
        "v_mfma_f32_16x16x32_bf16 v[112:115], v[228:231], v[224:227], v[112:115]\n"
        "s_waitcnt lgkmcnt(5)\n"
        "ds_read_b128 a[224:227], v254 offset:48\n"
        "v_cvt_pk_bf16_f32 v151, v106, v107\n"
        "v_mfma_f32_16x16x32_bf16 v[172:175], v[156:159], a[236:239], 0\n"
        "s_waitcnt lgkmcnt(5)\n"
        "ds_read_b128 a[228:231], v254 offset:6192\n"
        "v_cvt_pk_bf16_f32 v85, v86, v87\n"
        "v_mfma_f32_16x16x32_bf16 v[172:175], v[164:167], a[240:243], v[172:175]\n"
        "v_cvt_pk_bf16_f32 v86, v116, v117\n"
        "v_mov_b64 v[116:117], 0\n"
        "v_mfma_f32_16x16x32_bf16 v[200:203], v[152:155], a[240:243], 0\n"
        "ds_read_b128 a[232:235], v248 offset:64\n"
        "v_cvt_pk_bf16_f32 v87, v118, v119\n"
        "v_mfma_f32_16x16x32_bf16 v[200:203], v[160:163], a[236:239], v[200:203]\n"
        "v_exp_f32 v88, v88\n"
        "v_smfmac_f32_16x16x64_bf16 v[116:119], a[252:255], v[64:71], v255\n"
        "v_exp_f32 v89, v89\n"
        "v_mfma_f32_16x16x32_bf16 v[120:123], v[128:131], v[64:67], 0\n"
        "v_exp_f32 v90, v90\n"
        "v_mfma_f32_16x16x32_bf16 v[120:123], v[132:135], v[68:71], v[120:123]\n"
        "ds_read_b128 a[236:239], v248 offset:6208\n"
        "v_cvt_pk_bf16_f32 v88, v88, v89\n"
        "v_mfma_f32_16x16x32_bf16 v[124:127], v[140:143], v[68:71], 0\n"
        "v_exp_f32 v91, v91\n"
        "v_mfma_f32_16x16x32_bf16 v[124:127], v[136:139], v[64:67], v[124:127]\n"
        "v_cvt_pk_bf16_f32 v89, v90, v91\n"
        "v_add_f32 v116, v116, v117\n"
        "v_mfma_f32_16x16x32_bf16 v[96:99], v[144:147], v[64:67], 0\n"
        "v_exp_f32 v92, v92\n"
        "v_mfma_f32_16x16x32_bf16 v[96:99], v[148:151], v[68:71], v[96:99]\n"
        "s_waitcnt lgkmcnt(7)\n"
        "v_exp_f32 v93, v93\n"
        "v_mfma_f32_16x16x32_bf16 v[204:207], v[160:163], a[244:247], 0\n"
        "s_waitcnt lgkmcnt(6)\n"
        "v_exp_f32 v94, v94\n"
        "v_mfma_f32_16x16x32_bf16 v[204:207], v[152:155], a[248:251], v[204:207]\n"
        "ds_read_b128 a[240:243], v254 offset:80\n"
        "v_cvt_pk_bf16_f32 v90, v92, v93\n"
        "v_mfma_f32_16x16x32_bf16 v[176:179], v[164:167], a[248:251], 0\n"
        "v_exp_f32 v95, v95\n"
        "v_mfma_f32_16x16x32_bf16 v[176:179], v[156:159], a[244:247], v[176:179]\n"
        "s_waitcnt lgkmcnt(6)\n"
        "ds_read_b128 a[244:247], v254 offset:6224\n"
        "v_cvt_pk_bf16_f32 v91, v94, v95\n"
        "v_mfma_f32_16x16x32_bf16 v[180:183], v[156:159], a[216:219], 0\n"
        "s_waitcnt lgkmcnt(6)\n"
        "ds_read_b128 a[248:251], v248 offset:36864\n"
        "v_mov_b64 v[100:101], 0\n"
        "v_mfma_f32_16x16x32_bf16 v[180:183], v[164:167], a[220:223], v[180:183]\n"
        "v_exp_f32 v108, v108\n"
        "v_mfma_f32_16x16x32_bf16 v[212:215], v[152:155], a[220:223], 0\n"
        "v_exp_f32 v109, v109\n"
        "v_mfma_f32_16x16x32_bf16 v[212:215], v[160:163], a[216:219], v[212:215]\n"
        "s_waitcnt lgkmcnt(6)\n"
        "v_exp_f32 v110, v110\n"
        "v_mfma_f32_16x16x32_bf16 v[216:219], v[160:163], a[224:227], 0\n"
        "s_waitcnt lgkmcnt(5)\n"
        "v_rcp_f32 v117, v116\n"
        "v_mfma_f32_16x16x32_bf16 v[216:219], v[152:155], a[228:231], v[216:219]\n"
        "v_cvt_pk_bf16_f32 v92, v108, v109\n"
        "v_mul_f32 v120, v117, v120\n"
        "v_mfma_f32_16x16x32_bf16 v[184:187], v[164:167], a[228:231], 0\n"
        "v_exp_f32 v111, v111\n"
        "v_mfma_f32_16x16x32_bf16 v[184:187], v[156:159], a[224:227], v[184:187]\n"
        "s_waitcnt lgkmcnt(4)\n"
        "v_mul_f32 v121, v117, v121\n"
        "v_mul_f32 v122, v117, v122\n"
        "v_mfma_f32_16x16x32_bf16 v[192:195], v[156:159], a[232:235], 0\n"
        "s_waitcnt lgkmcnt(3)\n"
        "v_mul_f32 v123, v117, v123\n"
        "v_cvt_pk_bf16_f32 v93, v110, v111\n"
        "v_mfma_f32_16x16x32_bf16 v[192:195], v[164:167], a[236:239], v[192:195]\n"
        "v_exp_f32 v112, v112\n"
        "v_mfma_f32_16x16x32_bf16 v[220:223], v[152:155], a[236:239], 0\n"
        "v_cvt_pk_bf16_f32 v120, v120, v121\n"
        "v_cvt_pk_bf16_f32 v121, v122, v123\n"
        "v_mfma_f32_16x16x32_bf16 v[220:223], v[160:163], a[232:235], v[220:223]\n"
        "ds_read_b128 v[232:235], v247 offset:8192\n"
        "v_mul_f32 v124, v117, v124\n"
        "v_smfmac_f32_16x16x64_bf16 v[100:103], a[252:255], v[72:79], v255\n"
        "ds_read_b128 v[64:67], v247 offset:12288\n"
        "v_mul_f32 v125, v117, v125\n"
        "v_mfma_f32_16x16x32_bf16 v[104:107], v[128:131], v[72:75], 0\n"
        "43:\n"
        "s_cbranch_execz 46f\n"
        "ds_read_b128 v[236:239], v247 offset:8448\n"
        "v_maximum3_f32 v253, v253, v116, v117\n"
        "v_mfma_f32_16x16x32_bf16 v[104:107], v[132:135], v[76:79], v[104:107]\n"
        "ds_read_b128 v[68:71], v247 offset:12544\n"
        "ds_read_b128 a[216:219], v248 offset:43008\n"
        "v_mfma_f32_16x16x32_bf16 v[108:111], v[140:143], v[76:79], 0\n"
        "v_exp_f32 v113, v113\n"
        "v_mfma_f32_16x16x32_bf16 v[108:111], v[136:139], v[72:75], v[108:111]\n"
        "ds_write_b32 v245, v120 offset:0\n"
        "v_exp_f32 v114, v114\n"
        "v_cvt_pk_bf16_f32 v94, v112, v113\n"
        "v_exp_f32 v115, v115\n"
        "v_mul_f32 v126, v117, v126\n"
        "v_cvt_pk_bf16_f32 v95, v114, v115\n"
        "v_mfma_f32_16x16x32_bf16 v[112:115], v[144:147], v[72:75], 0\n"
        "ds_read_b128 a[220:223], v254 offset:36880\n"
        "v_mul_f32 v127, v117, v127\n"
        "v_mfma_f32_16x16x32_bf16 v[112:115], v[148:151], v[76:79], v[112:115]\n"
        "s_waitcnt lgkmcnt(9)\n"
        "ds_read_b128 a[224:227], v254 offset:43024\n"
        "v_cvt_pk_bf16_f32 v124, v124, v125\n"
        "v_mfma_f32_16x16x32_bf16 v[224:227], v[160:163], a[240:243], 0\n"
        "s_waitcnt lgkmcnt(9)\n"
        "ds_write_b32 v245, v121 offset:4\n"
        "v_mfma_f32_16x16x32_bf16 v[224:227], v[152:155], a[244:247], v[224:227]\n"
        "v_cvt_pk_bf16_f32 v125, v126, v127\n"
        "v_mul_f32 v96, v117, v96\n"
        "v_mfma_f32_16x16x32_bf16 v[196:199], v[164:167], a[244:247], 0\n"
        "ds_read_b128 a[228:231], v248 offset:36896\n"
        "v_mul_f32 v97, v117, v97\n"
        "v_mfma_f32_16x16x32_bf16 v[196:199], v[156:159], a[240:243], v[196:199]\n"
        "s_waitcnt lgkmcnt(9)\n"
        "ds_read_b128 a[232:235], v248 offset:43040\n"
        "v_mul_f32 v98, v117, v98\n"
        "v_mfma_f32_16x16x32_bf16 v[172:175], v[232:235], a[248:251], v[172:175]\n"
        "s_waitcnt lgkmcnt(6)\n"
        "ds_write_b32 v245, v124 offset:8\n"
        "v_mfma_f32_16x16x32_bf16 v[172:175], v[64:67], a[216:219], v[172:175]\n"
        "ds_write_b32 v245, v125 offset:12\n"
        "v_mfma_f32_16x16x32_bf16 v[200:203], v[68:71], a[216:219], v[200:203]\n"
        "ds_read_b128 a[236:239], v254 offset:36912\n"
        "v_mul_f32 v99, v117, v99\n"
        "v_mfma_f32_16x16x32_bf16 v[200:203], v[236:239], a[248:251], v[200:203]\n"
        "s_waitcnt lgkmcnt(7)\n"
        "ds_read_b128 a[240:243], v254 offset:43056\n"
        "v_cvt_pk_bf16_f32 v96, v96, v97\n"
        "v_mfma_f32_16x16x32_bf16 v[204:207], v[236:239], a[220:223], v[204:207]\n"
        "s_waitcnt lgkmcnt(7)\n"
        "v_cvt_pk_bf16_f32 v97, v98, v99\n"
        "v_add_f32 v100, v100, v101\n"
        "v_mfma_f32_16x16x32_bf16 v[204:207], v[68:71], a[224:227], v[204:207]\n"
        "ds_write_b32 v246, v96 offset:0\n"
        "v_mfma_f32_16x16x32_bf16 v[176:179], v[64:67], a[224:227], v[176:179]\n"
        "ds_write_b32 v246, v97 offset:4\n"
        "v_mfma_f32_16x16x32_bf16 v[176:179], v[232:235], a[220:223], v[176:179]\n"
        "s_waitcnt lgkmcnt(7)\n"
        "v_rcp_f32 v101, v100\n"
        "v_mfma_f32_16x16x32_bf16 v[180:183], v[232:235], a[228:231], v[180:183]\n"
        "s_waitcnt lgkmcnt(6)\n"
        "v_mul_f32 v104, v101, v104\n"
        "v_mul_f32 v105, v101, v105\n"
        "v_mfma_f32_16x16x32_bf16 v[180:183], v[64:67], a[232:235], v[180:183]\n"
        "v_mov_b64 v[116:117], 0\n"
        "v_mul_f32 v106, v101, v106\n"
        "v_mfma_f32_16x16x32_bf16 v[212:215], v[68:71], a[232:235], v[212:215]\n"
        "v_mul_f32 v107, v101, v107\n"
        "v_cvt_pk_bf16_f32 v104, v104, v105\n"
        "v_mfma_f32_16x16x32_bf16 v[212:215], v[236:239], a[228:231], v[212:215]\n"
        "s_waitcnt lgkmcnt(3)\n"
        "v_cvt_pk_bf16_f32 v105, v106, v107\n"
        "v_mul_f32 v108, v101, v108\n"
        "v_mfma_f32_16x16x32_bf16 v[216:219], v[236:239], a[236:239], v[216:219]\n"
        "s_waitcnt lgkmcnt(2)\n"
        "ds_write_b32 v245, v104 offset:256\n"
        "v_mfma_f32_16x16x32_bf16 v[216:219], v[68:71], a[240:243], v[216:219]\n"
        "ds_read_b128 a[244:247], v248 offset:36928\n"
        "v_mul_f32 v109, v101, v109\n"
        "v_mfma_f32_16x16x32_bf16 v[184:187], v[64:67], a[240:243], v[184:187]\n"
        "ds_read_b128 a[248:251], v248 offset:43072\n"
        "v_maximum3_f32 v253, v253, v100, v101\n"
        "v_mfma_f32_16x16x32_bf16 v[184:187], v[232:235], a[236:239], v[184:187]\n"
        "ds_write_b32 v245, v105 offset:260\n"
        "v_smfmac_f32_16x16x64_bf16 v[116:119], a[252:255], v[80:87], v255\n"
        "v_mul_f32 v110, v101, v110\n"
        "v_mul_f32 v111, v101, v111\n"
        "v_mfma_f32_16x16x32_bf16 v[120:123], v[128:131], v[80:83], 0\n"
        "ds_read_b128 a[216:219], v254 offset:36944\n"
        "v_cvt_pk_bf16_f32 v108, v108, v109\n"
        "v_mfma_f32_16x16x32_bf16 v[120:123], v[132:135], v[84:87], v[120:123]\n"
        "ds_read_b128 a[220:223], v254 offset:43088\n"
        "v_cvt_pk_bf16_f32 v109, v110, v111\n"
        "v_mfma_f32_16x16x32_bf16 v[124:127], v[140:143], v[84:87], 0\n"
        "ds_read_b128 v[156:159], v247 offset:16384\n"
        "v_mul_f32 v112, v101, v112\n"
        "v_mfma_f32_16x16x32_bf16 v[124:127], v[136:139], v[80:83], v[124:127]\n"
        "ds_read_b128 v[164:167], v247 offset:20480\n"
        "v_mul_f32 v113, v101, v113\n"
        "v_mfma_f32_16x16x32_bf16 v[96:99], v[144:147], v[80:83], 0\n"
        "ds_read_b128 v[160:163], v247 offset:16640\n"
        "ds_read_b128 a[224:227], v248 offset:12288\n"
        "v_mfma_f32_16x16x32_bf16 v[96:99], v[148:151], v[84:87], v[96:99]\n"
        "s_waitcnt lgkmcnt(8)\n"
        "ds_read_b128 v[152:155], v247 offset:20736\n"
        "ds_read_b128 a[228:231], v248 offset:49152\n"
        "v_mfma_f32_16x16x32_bf16 v[192:195], v[232:235], a[244:247], v[192:195]\n"
        "s_waitcnt lgkmcnt(9)\n"
        "ds_write_b32 v245, v108 offset:264\n"
        "v_mfma_f32_16x16x32_bf16 v[192:195], v[64:67], a[248:251], v[192:195]\n"
        "ds_write_b32 v245, v109 offset:268\n"
        "v_mfma_f32_16x16x32_bf16 v[220:223], v[68:71], a[248:251], v[220:223]\n"
        "ds_read_b128 a[232:235], v254 offset:12304\n"
        "v_mul_f32 v114, v101, v114\n"
        "v_mfma_f32_16x16x32_bf16 v[220:223], v[236:239], a[244:247], v[220:223]\n"
        "s_waitcnt lgkmcnt(10)\n"
        "ds_read_b128 a[236:239], v254 offset:49168\n"
        "v_mul_f32 v115, v101, v115\n"
        "v_mfma_f32_16x16x32_bf16 v[224:227], v[236:239], a[216:219], v[224:227]\n"
        "s_waitcnt lgkmcnt(10)\n"
        "ds_read_b128 a[240:243], v248 offset:12320\n"
        "v_cvt_pk_bf16_f32 v112, v112, v113\n"
        "v_mfma_f32_16x16x32_bf16 v[224:227], v[68:71], a[220:223], v[224:227]\n"
        "v_cvt_pk_bf16_f32 v113, v114, v115\n"
        "v_add_f32 v116, v116, v117\n"
        "v_mfma_f32_16x16x32_bf16 v[196:199], v[64:67], a[220:223], v[196:199]\n"
        "ds_write_b32 v246, v112 offset:256\n"
        "v_mfma_f32_16x16x32_bf16 v[196:199], v[232:235], a[216:219], v[196:199]\n"
        "s_waitcnt lgkmcnt(8)\n"
        "ds_read_b128 a[244:247], v248 offset:49184\n"
        "v_add_u32 v247, s31, v247\n"
        "v_mfma_f32_16x16x32_bf16 v[172:175], v[156:159], a[224:227], v[172:175]\n"
        "s_waitcnt lgkmcnt(7)\n"
        "ds_write_b32 v246, v113 offset:260\n"
        "v_mfma_f32_16x16x32_bf16 v[172:175], v[164:167], a[228:231], v[172:175]\n"
        "v_rcp_f32 v117, v116\n"
        "v_mfma_f32_16x16x32_bf16 v[200:203], v[152:155], a[228:231], v[200:203]\n"
        "ds_read_b128 a[248:251], v254 offset:12336\n"
        "v_mul_f32 v120, v117, v120\n"
        "v_mfma_f32_16x16x32_bf16 v[200:203], v[160:163], a[224:227], v[200:203]\n"
        "s_waitcnt lgkmcnt(6)\n"
        "ds_read_b128 a[216:219], v254 offset:49200\n"
        "v_mul_f32 v121, v117, v121\n"
        "v_mfma_f32_16x16x32_bf16 v[204:207], v[160:163], a[232:235], v[204:207]\n"
        "s_waitcnt lgkmcnt(6)\n"
        "v_mul_f32 v122, v117, v122\n"
        "v_mul_f32 v123, v117, v123\n"
        "v_mfma_f32_16x16x32_bf16 v[204:207], v[152:155], a[236:239], v[204:207]\n"
        "v_cvt_pk_bf16_f32 v120, v120, v121\n"
        "v_cvt_pk_bf16_f32 v121, v122, v123\n"
        "v_mfma_f32_16x16x32_bf16 v[176:179], v[164:167], a[236:239], v[176:179]\n"
        "ds_read_b128 a[220:223], v248 offset:12352\n"
        "v_mul_f32 v124, v117, v124\n"
        "v_mfma_f32_16x16x32_bf16 v[176:179], v[156:159], a[232:235], v[176:179]\n"
        "s_waitcnt lgkmcnt(6)\n"
        "ds_read_b128 a[224:227], v248 offset:49216\n"
        "v_mul_f32 v125, v117, v125\n"
        "v_mfma_f32_16x16x32_bf16 v[180:183], v[156:159], a[240:243], v[180:183]\n"
        "s_waitcnt lgkmcnt(5)\n"
        "ds_write_b32 v245, v120 offset:512\n"
        "v_mfma_f32_16x16x32_bf16 v[180:183], v[164:167], a[244:247], v[180:183]\n"
        "ds_write_b32 v245, v121 offset:516\n"
        "v_mfma_f32_16x16x32_bf16 v[212:215], v[152:155], a[244:247], v[212:215]\n"
        "ds_read_b128 a[228:231], v254 offset:12368\n"
        "v_maximum3_f32 v253, v253, v116, v117\n"
        "v_mfma_f32_16x16x32_bf16 v[212:215], v[160:163], a[240:243], v[212:215]\n"
        "s_waitcnt lgkmcnt(6)\n"
        "ds_read_b128 a[232:235], v254 offset:49232\n"
        "v_mul_f32 v126, v117, v126\n"
        "v_mfma_f32_16x16x32_bf16 v[216:219], v[160:163], a[248:251], v[216:219]\n"
        "s_waitcnt lgkmcnt(6)\n"
        "v_mul_f32 v127, v117, v127\n"
        "v_cvt_pk_bf16_f32 v124, v124, v125\n"
        "v_mfma_f32_16x16x32_bf16 v[216:219], v[152:155], a[216:219], v[216:219]\n"
        "v_cvt_pk_bf16_f32 v125, v126, v127\n"
        "v_mul_f32 v96, v117, v96\n"
        "v_mfma_f32_16x16x32_bf16 v[184:187], v[164:167], a[216:219], v[184:187]\n"
        "s_waitcnt vmcnt(0) lgkmcnt(5)\n"
        "ds_write_b32 v245, v124 offset:520\n"
        "v_mfma_f32_16x16x32_bf16 v[184:187], v[156:159], a[248:251], v[184:187]\n"
        "s_barrier\n"
        "ds_write_b32 v245, v125 offset:524\n"
        "v_mfma_f32_16x16x32_bf16 v[192:195], v[156:159], a[220:223], v[192:195]\n"
        "s_waitcnt lgkmcnt(6)\n"
        "v_mul_f32 v97, v117, v97\n"
        "v_mul_f32 v98, v117, v98\n"
        "v_mfma_f32_16x16x32_bf16 v[192:195], v[164:167], a[224:227], v[192:195]\n"
        "v_mul_f32 v99, v117, v99\n"
        "v_cvt_pk_bf16_f32 v96, v96, v97\n"
        "v_mfma_f32_16x16x32_bf16 v[220:223], v[152:155], a[224:227], v[220:223]\n"
        "v_cvt_pk_bf16_f32 v97, v98, v99\n"
        "v_mov_b64 v[80:81], 0\n"
        "v_mfma_f32_16x16x32_bf16 v[220:223], v[160:163], a[220:223], v[220:223]\n"
        "s_waitcnt lgkmcnt(3)\n"
        "ds_write_b32 v246, v96 offset:512\n"
        "v_mfma_f32_16x16x32_bf16 v[224:227], v[160:163], a[228:231], v[224:227]\n"
        "s_waitcnt lgkmcnt(3)\n"
        "ds_write_b32 v246, v97 offset:516\n"
        "v_mfma_f32_16x16x32_bf16 v[224:227], v[152:155], a[232:235], v[224:227]\n"
        "ds_read_b128 a[236:239], v240 offset:0\n"
        "v_mfma_f32_16x16x32_bf16 v[196:199], v[164:167], a[232:235], v[196:199]\n"
        "ds_read_b128 a[240:243], v241 offset:0\n"
        "v_mfma_f32_16x16x32_bf16 v[196:199], v[156:159], a[228:231], v[196:199]\n"
        "ds_read_b128 a[244:247], v240 offset:128\n"
        "v_smfmac_f32_16x16x64_bf16 v[80:83], a[252:255], v[88:95], v255\n"
        "ds_read_b128 a[248:251], v241 offset:128\n"
        "v_mfma_f32_16x16x32_bf16 v[84:87], v[128:131], v[88:91], 0\n"
        "s_min_u32 s58, s63, s59\n"
        "ds_read_b128 a[216:219], v240 offset:256\n"
        "v_mfma_f32_16x16x32_bf16 v[84:87], v[132:135], v[92:95], v[84:87]\n"
        "ds_read_b128 a[220:223], v241 offset:256\n"
        "v_mfma_f32_16x16x32_bf16 v[232:235], v[140:143], v[92:95], 0\n"
        "v_mfma_f32_16x16x32_bf16 v[232:235], v[136:139], v[88:91], v[232:235]\n"
        "v_mfma_f32_16x16x32_bf16 v[236:239], v[144:147], v[88:91], 0\n"
        "v_mfma_f32_16x16x32_bf16 v[236:239], v[148:151], v[92:95], v[236:239]\n"
        "s_waitcnt lgkmcnt(0)\n"
        "s_branch 11f\n"
        "12:\n"
        "s_waitcnt vmcnt(16)\n"
        "s_mov_b32 s6, s20\n"
        "v_mov_b32 v120, 1\n"
        "buffer_atomic_add v120, v249, s[4:7], 0 offen sc0 sc1\n"
        "s_branch 13f\n"
        ".p2align 6\n"
        "10:\n"
        "v_mfma_f32_16x16x32_bf16 v[232:235], v[136:139], v[88:91], v[232:235]\n"
        "s_cmp_lt_u32 s58, s21\n"
        "v_mov_b32 v120, 1\n"
        "v_mfma_f32_16x16x32_bf16 v[236:239], v[144:147], v[88:91], 0\n"
        "s_cselect_b32 s6, s20, 0\n"
        "v_mfma_f32_16x16x32_bf16 v[236:239], v[148:151], v[92:95], v[236:239]\n"
        "s_waitcnt lgkmcnt(5)\n"
        "buffer_atomic_add v120, v249, s[4:7], 0 offen sc0 sc1\n"
        "11:\n"
        "v_mfma_f32_16x16x32_bf16 v[96:99], a[72:75], a[236:239], 0\n"
        "s_waitcnt lgkmcnt(4)\n"
        "v_and_b32 v117, 60, v250\n"
        "v_add3_u32 v117, v250, v117, s26\n"
        "v_mfma_f32_16x16x32_bf16 v[96:99], a[76:79], a[240:243], v[96:99]\n"
        "s_waitcnt lgkmcnt(3)\n"
        "v_cvt_pk_bf16_f32 v108, v172, v176\n"
        "v_cvt_pk_bf16_f32 v109, v180, v184\n"
        "v_mfma_f32_16x16x32_bf16 v[96:99], a[80:83], a[244:247], v[96:99]\n"
        "s_waitcnt lgkmcnt(2)\n"
        "v_cvt_pk_bf16_f32 v110, v192, v196\n"
        "v_add_f32 v80, v80, v81\n"
        "v_mfma_f32_16x16x32_bf16 v[96:99], a[84:87], a[248:251], v[96:99]\n"
        "s_waitcnt lgkmcnt(1)\n"
        "buffer_store_dwordx2 v[108:109], v117, s[16:19], s38 offen offset:0 sc1\n"
        "v_mfma_f32_16x16x32_bf16 v[96:99], a[88:91], a[216:219], v[96:99]\n"
        "s_waitcnt lgkmcnt(0)\n"
        "v_rcp_f32 v81, v80\n"
        "v_mfma_f32_16x16x32_bf16 v[96:99], a[92:95], a[220:223], v[96:99]\n"
        "s_add_u32 s41, s29, s40\n"
        "v_cvt_pk_bf16_f32 v112, v173, v177\n"
        "v_cvt_pk_bf16_f32 v113, v181, v185\n"
        "v_mfma_f32_16x16x32_bf16 v[100:103], a[116:119], a[220:223], 0\n"
        "s_lshr_b32 s47, s58, 6\n"
        "v_mul_f32 v84, v81, v84\n"
        "v_mul_f32 v85, v81, v85\n"
        "v_mfma_f32_16x16x32_bf16 v[100:103], a[112:115], a[216:219], v[100:103]\n"
        "s_mul_i32 s47, s47, 1572864\n"
        "v_cvt_pk_bf16_f32 v152, v96, v97\n"
        "v_cvt_pk_bf16_f32 v153, v98, v99\n"
        "v_mfma_f32_16x16x32_bf16 v[100:103], a[108:111], a[248:251], v[100:103]\n"
        "buffer_store_dword v110, v250, s[16:19], s38 offen offset:0 sc1\n"
        "v_mfma_f32_16x16x32_bf16 v[100:103], a[104:107], a[244:247], v[100:103]\n"
        "s_add_u32 s47, s47, s24\n"
        "v_cvt_pk_bf16_f32 v114, v193, v197\n"
        "v_mul_f32 v86, v81, v86\n"
        "v_mfma_f32_16x16x32_bf16 v[100:103], a[100:103], a[240:243], v[100:103]\n"
        "s_sub_u32 s31, s28, s30\n"
        "v_mul_f32 v87, v81, v87\n"
        "v_cvt_pk_bf16_f32 v84, v84, v85\n"
        "v_mfma_f32_16x16x32_bf16 v[100:103], a[96:99], a[236:239], v[100:103]\n"
        "s_sub_u32 s45, s29, s28\n"
        "ds_write_b32 v245, v84 offset:768\n"
        "v_mfma_f32_16x16x32_bf16 v[104:107], a[120:123], a[236:239], 0\n"
        "s_mov_b32 s29, s30\n"
        "v_cvt_pk_bf16_f32 v85, v86, v87\n"
        "v_mul_f32 v232, v81, v232\n"
        "v_mfma_f32_16x16x32_bf16 v[104:107], a[124:127], a[240:243], v[104:107]\n"
        "buffer_store_dwordx2 v[112:113], v117, s[16:19], s38 offen offset:384 sc1\n"
        "v_mfma_f32_16x16x32_bf16 v[104:107], a[128:131], a[244:247], v[104:107]\n"
        "s_mov_b32 s30, s28\n"
        "v_cvt_pk_bf16_f32 v154, v100, v101\n"
        "v_cvt_pk_bf16_f32 v155, v102, v103\n"
        "v_mfma_f32_16x16x32_bf16 v[104:107], a[132:135], a[248:251], v[104:107]\n"
        "s_add_u32 s28, s28, s45\n"
        "ds_write_b32 v245, v85 offset:772\n"
        "v_mfma_f32_16x16x32_bf16 v[104:107], a[136:139], a[216:219], v[104:107]\n"
        "s_lshl_b32 s36, s58, 13\n"
        "v_cvt_pk_bf16_f32 v108, v174, v178\n"
        "v_cvt_pk_bf16_f32 v109, v182, v186\n"
        "v_mfma_f32_16x16x32_bf16 v[104:107], a[140:143], a[220:223], v[104:107]\n"
        "s_add_u32 s36, s36, s34\n"
        "v_mul_f32 v233, v81, v233\n"
        "v_maximum3_f32 v253, v253, v80, v81\n"
        "v_mfma_f32_16x16x32_bf16 v[96:99], a[20:23], a[220:223], 0\n"
        "buffer_store_dword v114, v250, s[16:19], s38 offen offset:384 sc1\n"
        "v_mfma_f32_16x16x32_bf16 v[96:99], a[16:19], a[216:219], v[96:99]\n"
        "s_sub_u32 s62, s58, s57\n"
        "v_cvt_pk_bf16_f32 v110, v194, v198\n"
        "v_mul_f32 v234, v81, v234\n"
        "v_mfma_f32_16x16x32_bf16 v[96:99], a[12:15], a[248:251], v[96:99]\n"
        "s_and_b32 s62, s62, 63\n"
        "v_cvt_pk_bf16_f32 v168, v104, v105\n"
        "v_cvt_pk_bf16_f32 v169, v106, v107\n"
        "v_mfma_f32_16x16x32_bf16 v[96:99], a[8:11], a[244:247], v[96:99]\n"
        "s_mul_i32 s62, s62, 3072\n"
        "v_mul_f32 v235, v81, v235\n"
        "v_cvt_pk_bf16_f32 v232, v232, v233\n"
        "v_mfma_f32_16x16x32_bf16 v[96:99], a[4:7], a[240:243], v[96:99]\n"
        "s_add_u32 s35, s47, 786432\n"
        "ds_write_b32 v245, v232 offset:776\n"
        "v_mfma_f32_16x16x32_bf16 v[96:99], a[0:3], a[236:239], v[96:99]\n"
        "buffer_store_dwordx2 v[108:109], v117, s[16:19], s38 offen offset:768 sc1\n"
        "v_mfma_f32_16x16x32_bf16 v[100:103], a[24:27], a[236:239], 0\n"
        "s_sub_u32 s46, s35, 100663296\n"
        "v_cvt_pk_bf16_f32 v112, v175, v179\n"
        "v_cvt_pk_bf16_f32 v113, v183, v187\n"
        "v_mfma_f32_16x16x32_bf16 v[100:103], a[28:31], a[240:243], v[100:103]\n"
        "s_cmp_lt_u32 s46, 786432\n"
        "v_cvt_pk_bf16_f32 v233, v234, v235\n"
        "v_xor_b32 v251, 0x2000, v251\n"
        "v_mfma_f32_16x16x32_bf16 v[100:103], a[32:35], a[244:247], v[100:103]\n"
        "s_cselect_b32 s35, s46, s35\n"
        "v_mul_f32 v96, s50, v96\n"
        "v_mul_f32 v97, s50, v97\n"
        "v_mfma_f32_16x16x32_bf16 v[100:103], a[36:39], a[248:251], v[100:103]\n"
        "v_mul_f32 v98, s50, v98\n"
        "v_mul_f32 v99, s50, v99\n"
        "v_mfma_f32_16x16x32_bf16 v[100:103], a[40:43], a[216:219], v[100:103]\n"
        "v_cvt_pk_bf16_f32 v156, v96, v97\n"
        "v_cvt_pk_bf16_f32 v157, v98, v99\n"
        "v_mfma_f32_16x16x32_bf16 v[100:103], a[44:47], a[220:223], v[100:103]\n"
        "buffer_store_dword v110, v250, s[16:19], s38 offen offset:768 sc1\n"
        "v_mfma_f32_16x16x32_bf16 v[104:107], a[68:71], a[220:223], 0\n"
        "ds_write_b32 v245, v233 offset:780\n"
        "v_mfma_f32_16x16x32_bf16 v[104:107], a[64:67], a[216:219], v[104:107]\n"
        "v_cvt_pk_bf16_f32 v114, v195, v199\n"
        "v_xor_b32 v252, 0x2000, v252\n"
        "v_mfma_f32_16x16x32_bf16 v[104:107], a[60:63], a[248:251], v[104:107]\n"
        "ds_read_b128 a[224:227], v240 offset:1536\n"
        "v_mul_f32 v100, s50, v100\n"
        "v_mfma_f32_16x16x32_bf16 v[104:107], a[56:59], a[244:247], v[104:107]\n"
        "v_mul_f32 v101, s50, v101\n"
        "v_mul_f32 v102, s50, v102\n"
        "v_mfma_f32_16x16x32_bf16 v[104:107], a[52:55], a[240:243], v[104:107]\n"
        "v_mul_f32 v103, s50, v103\n"
        "v_cvt_pk_bf16_f32 v158, v100, v101\n"
        "v_mfma_f32_16x16x32_bf16 v[104:107], a[48:51], a[236:239], v[104:107]\n"
        "buffer_store_dwordx2 v[112:113], v117, s[16:19], s38 offen offset:1152 sc1\n"
        "v_mfma_f32_16x16x32_bf16 v[96:99], a[236:239], a[144:147], 0\n"
        "ds_read_b128 a[228:231], v241 offset:1536\n"
        "v_cvt_pk_bf16_f32 v159, v102, v103\n"
        "v_mfma_f32_16x16x32_bf16 v[96:99], a[240:243], a[148:151], v[96:99]\n"
        "ds_read_b128 a[232:235], v240 offset:1664\n"
        "v_cvt_pk_bf16_f32 v108, v200, v204\n"
        "v_mfma_f32_16x16x32_bf16 v[96:99], a[244:247], a[152:155], v[96:99]\n"
        "v_mul_f32 v104, s50, v104\n"
        "v_mul_f32 v105, s50, v105\n"
        "v_mfma_f32_16x16x32_bf16 v[96:99], a[248:251], a[156:159], v[96:99]\n"
        "buffer_store_dword v114, v250, s[16:19], s38 offen offset:1152 sc1\n"
        "v_mfma_f32_16x16x32_bf16 v[96:99], a[216:219], a[160:163], v[96:99]\n"
        "s_lshl_b64 s[60:61], s[60:61], 8\n"
        "v_mul_f32 v106, s50, v106\n"
        "v_mul_f32 v107, s50, v107\n"
        "v_mfma_f32_16x16x32_bf16 v[96:99], a[220:223], a[164:167], v[96:99]\n"
        "s_and_b32 s63, s58, 255\n"
        "v_cvt_pk_bf16_f32 v160, v104, v105\n"
        "v_cvt_pk_bf16_f32 v161, v106, v107\n"
        "v_mfma_f32_16x16x32_bf16 v[100:103], a[220:223], a[188:191], 0\n"
        "s_or_b32 s60, s60, s63\n"
        "v_mov_b64 v[166:167], v[160:161]\n"
        "v_cvt_pk_bf16_f32 v109, v212, v216\n"
        "v_mfma_f32_16x16x32_bf16 v[100:103], a[216:219], a[184:187], v[100:103]\n"
        "buffer_store_dwordx2 v[108:109], v117, s[16:19], s39 offen offset:0 sc1\n"
        "v_mfma_f32_16x16x32_bf16 v[100:103], a[248:251], a[180:183], v[100:103]\n"
        "s_add_u32 s27, s27, 1\n"
        "v_cvt_pk_bf16_f32 v128, v96, v97\n"
        "v_cvt_pk_bf16_f32 v129, v98, v99\n"
        "v_mfma_f32_16x16x32_bf16 v[100:103], a[244:247], a[176:179], v[100:103]\n"
        "s_add_u32 s63, s58, 256\n"
        "v_cvt_pk_bf16_f32 v110, v220, v224\n"
        "v_cvt_pk_bf16_f32 v112, v201, v205\n"
        "v_mfma_f32_16x16x32_bf16 v[100:103], a[240:243], a[172:175], v[100:103]\n"
        "s_cmp_lt_u32 s27, 3072\n"
        "v_cvt_pk_bf16_f32 v113, v213, v217\n"
        "v_mul_f32 v236, v81, v236\n"
        "v_mfma_f32_16x16x32_bf16 v[100:103], a[236:239], a[168:171], v[100:103]\n"
        "s_cselect_b32 s49, 4096, 0\n"
        "v_mul_f32 v237, v81, v237\n"
        "v_mul_f32 v238, v81, v238\n"
        "v_mfma_f32_16x16x32_bf16 v[104:107], a[236:239], a[192:195], 0\n"
        "s_cmp_lt_u32 s63, 3328\n"
        "ds_read_b128 a[236:239], v241 offset:1664\n"
        "v_mul_f32 v239, v81, v239\n"
        "v_mfma_f32_16x16x32_bf16 v[104:107], a[240:243], a[196:199], v[104:107]\n"
        "s_cselect_b32 s63, s63, -1\n"
        "ds_read_b128 a[240:243], v240 offset:1792\n"
        "v_cvt_pk_bf16_f32 v236, v236, v237\n"
        "v_mfma_f32_16x16x32_bf16 v[104:107], a[244:247], a[200:203], v[104:107]\n"
        "ds_read_b128 a[244:247], v241 offset:1792\n"
        "v_cvt_pk_bf16_f32 v136, v100, v101\n"
        "v_mfma_f32_16x16x32_bf16 v[104:107], a[248:251], a[204:207], v[104:107]\n"
        "buffer_store_dword v110, v250, s[16:19], s39 offen offset:0 sc1\n"
        "v_mfma_f32_16x16x32_bf16 v[104:107], a[216:219], a[208:211], v[104:107]\n"
        "v_cvt_pk_bf16_f32 v137, v102, v103\n"
        "v_cvt_pk_bf16_f32 v237, v238, v239\n"
        "v_mfma_f32_16x16x32_bf16 v[104:107], a[220:223], a[212:215], v[104:107]\n"
        "s_waitcnt lgkmcnt(5)\n"
        "buffer_store_dwordx2 v[112:113], v117, s[16:19], s39 offen offset:384 sc1\n"
        "v_mfma_f32_16x16x32_bf16 v[96:99], a[72:75], a[224:227], 0\n"
        "s_waitcnt lgkmcnt(4)\n"
        "ds_write_b32 v246, v236 offset:768\n"
        "v_mfma_f32_16x16x32_bf16 v[96:99], a[76:79], a[228:231], v[96:99]\n"
        "s_waitcnt lgkmcnt(4)\n"
        "ds_write_b32 v246, v237 offset:772\n"
        "v_mfma_f32_16x16x32_bf16 v[96:99], a[80:83], a[232:235], v[96:99]\n"
        "s_waitcnt lgkmcnt(4)\n"
        "ds_read_b128 v[162:165], v251 offset:0\n"
        "v_cvt_pk_bf16_f32 v114, v221, v225\n"
        "v_mfma_f32_16x16x32_bf16 v[96:99], a[84:87], a[236:239], v[96:99]\n"
        "s_waitcnt lgkmcnt(4)\n"
        "v_cvt_pk_bf16_f32 v144, v104, v105\n"
        "v_cvt_pk_bf16_f32 v145, v106, v107\n"
        "v_mfma_f32_16x16x32_bf16 v[96:99], a[88:91], a[240:243], v[96:99]\n"
        "s_waitcnt lgkmcnt(3)\n"
        "buffer_store_dword v114, v250, s[16:19], s39 offen offset:384 sc1\n"
        "v_mfma_f32_16x16x32_bf16 v[96:99], a[92:95], a[244:247], v[96:99]\n"
        "v_cvt_pk_bf16_f32 v108, v202, v206\n"
        "v_cvt_pk_bf16_f32 v109, v214, v218\n"
        "v_mfma_f32_16x16x32_bf16 v[100:103], a[116:119], a[244:247], 0\n"
        "v_cvt_pk_bf16_f32 v110, v222, v226\n"
        "v_add_u32 v245, s31, v245\n"
        "v_mfma_f32_16x16x32_bf16 v[100:103], a[112:115], a[240:243], v[100:103]\n"
        "v_add_u32 v246, s31, v246\n"
        "v_add_u32 v242, s62, v242\n"
        "v_mfma_f32_16x16x32_bf16 v[100:103], a[108:111], a[236:239], v[100:103]\n"
        "v_cvt_pk_bf16_f32 v172, v96, v97\n"
        "v_cvt_pk_bf16_f32 v173, v98, v99\n"
        "v_mfma_f32_16x16x32_bf16 v[100:103], a[104:107], a[232:235], v[100:103]\n"
        "buffer_store_dwordx2 v[108:109], v117, s[16:19], s39 offen offset:768 sc1\n"
        "v_mfma_f32_16x16x32_bf16 v[100:103], a[100:103], a[228:231], v[100:103]\n"
        "v_cvt_pk_bf16_f32 v112, v203, v207\n"
        "v_cvt_pk_bf16_f32 v113, v215, v219\n"
        "v_mfma_f32_16x16x32_bf16 v[100:103], a[96:99], a[224:227], v[100:103]\n"
        "s_nop 0\n"
        "v_subrev_u32 v118, 196608, v242\n"
        "v_add_u32 v243, s62, v243\n"
        "v_mfma_f32_16x16x32_bf16 v[104:107], a[120:123], a[224:227], 0\n"
        "s_nop 0\n"
        "v_min_u32 v242, v242, v118\n"
        "v_subrev_u32 v119, 196608, v243\n"
        "v_mfma_f32_16x16x32_bf16 v[104:107], a[124:127], a[228:231], v[104:107]\n"
        "v_cvt_pk_bf16_f32 v174, v100, v101\n"
        "v_cvt_pk_bf16_f32 v175, v102, v103\n"
        "v_mfma_f32_16x16x32_bf16 v[104:107], a[128:131], a[232:235], v[104:107]\n"
        "buffer_store_dword v110, v250, s[16:19], s39 offen offset:768 sc1\n"
        "v_mfma_f32_16x16x32_bf16 v[104:107], a[132:135], a[236:239], v[104:107]\n"
        "v_cvt_pk_bf16_f32 v114, v223, v227\n"
        "v_min_u32 v243, v243, v119\n"
        "v_mfma_f32_16x16x32_bf16 v[104:107], a[136:139], a[240:243], v[104:107]\n"
        "v_add_u32 v244, s62, v244\n"
        "v_mfma_f32_16x16x32_bf16 v[104:107], a[140:143], a[244:247], v[104:107]\n"
        "s_nop 1\n"
        "v_subrev_u32 v118, 196608, v244\n"
        "v_mfma_f32_16x16x32_bf16 v[96:99], a[20:23], a[244:247], 0\n"
        "s_nop 1\n"
        "v_min_u32 v244, v244, v118\n"
        "v_mfma_f32_16x16x32_bf16 v[96:99], a[16:19], a[240:243], v[96:99]\n"
        "buffer_store_dwordx2 v[112:113], v117, s[16:19], s39 offen offset:1152 sc1\n"
        "v_mfma_f32_16x16x32_bf16 v[96:99], a[12:15], a[236:239], v[96:99]\n"
        "v_cvt_pk_bf16_f32 v190, v104, v105\n"
        "v_cvt_pk_bf16_f32 v191, v106, v107\n"
        "v_mfma_f32_16x16x32_bf16 v[96:99], a[8:11], a[232:235], v[96:99]\n"
        "v_mfma_f32_16x16x32_bf16 v[96:99], a[4:7], a[228:231], v[96:99]\n"
        "ds_read_b128 v[182:185], v251 offset:512\n"
        "v_mfma_f32_16x16x32_bf16 v[96:99], a[0:3], a[224:227], v[96:99]\n"
        "s_cmp_lg_u32 s20, s6\n"
        "s_nop 1\n"
        "v_mfma_f32_16x16x32_bf16 v[64:67], v[152:155], v[156:159], v[0:3]\n"
        "s_waitcnt lgkmcnt(1)\n"
        "buffer_store_dword v114, v250, s[16:19], s39 offen offset:1152 sc1\n"
        "v_mfma_f32_16x16x32_bf16 v[64:67], v[168:171], v[160:163], v[64:67]\n"
        "s_cbranch_scc1 12b\n"
        "13:\n"
        "v_mfma_f32_16x16x32_bf16 v[68:71], v[172:175], v[156:159], v[4:7]\n"
        "v_mul_f32 v96, s50, v96\n"
        "v_mul_f32 v97, s50, v97\n"
        "v_mfma_f32_16x16x32_bf16 v[68:71], v[188:191], v[164:167], v[68:71]\n"
        "v_mul_f32 v98, s50, v98\n"
        "v_mul_f32 v99, s50, v99\n"
        "v_mfma_f32_16x16x32_bf16 v[100:103], a[24:27], a[224:227], 0\n"
        "v_cvt_pk_bf16_f32 v176, v96, v97\n"
        "v_cvt_pk_bf16_f32 v177, v98, v99\n"
        "v_mfma_f32_16x16x32_bf16 v[100:103], a[28:31], a[228:231], v[100:103]\n"
        "v_exp_f32 v64, v64\n"
        "v_mfma_f32_16x16x32_bf16 v[100:103], a[32:35], a[232:235], v[100:103]\n"
        "s_add_u32 m0, s41, 0\n"
        "v_exp_f32 v65, v65\n"
        "v_mfma_f32_16x16x32_bf16 v[100:103], a[36:39], a[236:239], v[100:103]\n"
        "v_exp_f32 v66, v66\n"
        "v_mfma_f32_16x16x32_bf16 v[100:103], a[40:43], a[240:243], v[100:103]\n"
        "buffer_load_dwordx4 v242, s[8:11], s47 offen lds\n"
        "v_mfma_f32_16x16x32_bf16 v[100:103], a[44:47], a[244:247], v[100:103]\n"
        "v_exp_f32 v67, v67\n"
        "v_mfma_f32_16x16x32_bf16 v[104:107], a[68:71], a[244:247], 0\n"
        "v_exp_f32 v68, v68\n"
        "v_mfma_f32_16x16x32_bf16 v[104:107], a[64:67], a[240:243], v[104:107]\n"
        "s_add_u32 m0, s41, 1024\n"
        "v_exp_f32 v69, v69\n"
        "v_mfma_f32_16x16x32_bf16 v[104:107], a[60:63], a[236:239], v[104:107]\n"
        "v_exp_f32 v70, v70\n"
        "v_mfma_f32_16x16x32_bf16 v[104:107], a[56:59], a[232:235], v[104:107]\n"
        "buffer_load_dwordx4 v243, s[8:11], s47 offen lds\n"
        "v_mfma_f32_16x16x32_bf16 v[104:107], a[52:55], a[228:231], v[104:107]\n"
        "v_mul_f32 v100, s50, v100\n"
        "v_mul_f32 v101, s50, v101\n"
        "v_mfma_f32_16x16x32_bf16 v[104:107], a[48:51], a[224:227], v[104:107]\n"
        "v_mul_f32 v102, s50, v102\n"
        "v_mul_f32 v103, s50, v103\n"
        "v_mfma_f32_16x16x32_bf16 v[96:99], a[224:227], a[144:147], 0\n"
        "s_add_u32 m0, s41, 2048\n"
        "v_cvt_pk_bf16_f32 v178, v100, v101\n"
        "v_cvt_pk_bf16_f32 v179, v102, v103\n"
        "v_mfma_f32_16x16x32_bf16 v[96:99], a[228:231], a[148:151], v[96:99]\n"
        "ds_read_b128 a[248:251], v240 offset:12288\n"
        "v_mul_f32 v104, s50, v104\n"
        "v_mfma_f32_16x16x32_bf16 v[96:99], a[232:235], a[152:155], v[96:99]\n"
        "buffer_load_dwordx4 v244, s[8:11], s47 offen lds\n"
        "v_mfma_f32_16x16x32_bf16 v[96:99], a[236:239], a[156:159], v[96:99]\n"
        "v_mul_f32 v105, s50, v105\n"
        "v_mul_f32 v106, s50, v106\n"
        "v_mfma_f32_16x16x32_bf16 v[96:99], a[240:243], a[160:163], v[96:99]\n"
        "v_mul_f32 v107, s50, v107\n"
        "v_cvt_pk_bf16_f32 v180, v104, v105\n"
        "v_mfma_f32_16x16x32_bf16 v[96:99], a[244:247], a[164:167], v[96:99]\n"
        "s_add_u32 m0, s41, 12288\n"
        "ds_read_b128 a[216:219], v241 offset:12288\n"
        "v_cvt_pk_bf16_f32 v181, v106, v107\n"
        "v_mfma_f32_16x16x32_bf16 v[72:75], v[152:155], v[176:179], v[16:19]\n"
        "s_waitcnt lgkmcnt(2)\n"
        "ds_read_b128 a[220:223], v240 offset:12416\n"
        "v_mov_b64 v[186:187], v[180:181]\n"
        "v_mfma_f32_16x16x32_bf16 v[72:75], v[168:171], v[180:183], v[72:75]\n"
        "buffer_load_dwordx4 v242, s[8:11], s35 offen lds\n"
        "v_mfma_f32_16x16x32_bf16 v[76:79], v[172:175], v[176:179], v[20:23]\n"
        "v_cvt_pk_bf16_f32 v130, v96, v97\n"
        "v_cvt_pk_bf16_f32 v131, v98, v99\n"
        "v_mfma_f32_16x16x32_bf16 v[76:79], v[188:191], v[184:187], v[76:79]\n"
        "v_exp_f32 v71, v71\n"
        "v_mfma_f32_16x16x32_bf16 v[100:103], a[244:247], a[188:191], 0\n"
        "s_add_u32 m0, s41, 13312\n"
        "v_exp_f32 v72, v72\n"
        "v_mfma_f32_16x16x32_bf16 v[100:103], a[240:243], a[184:187], v[100:103]\n"
        "v_exp_f32 v73, v73\n"
        "v_mfma_f32_16x16x32_bf16 v[100:103], a[236:239], a[180:183], v[100:103]\n"
        "buffer_load_dwordx4 v243, s[8:11], s35 offen lds\n"
        "v_mfma_f32_16x16x32_bf16 v[100:103], a[232:235], a[176:179], v[100:103]\n"
        "v_exp_f32 v74, v74\n"
        "v_mfma_f32_16x16x32_bf16 v[100:103], a[228:231], a[172:175], v[100:103]\n"
        "v_exp_f32 v75, v75\n"
        "v_mfma_f32_16x16x32_bf16 v[100:103], a[224:227], a[168:171], v[100:103]\n"
        "s_add_u32 m0, s41, 14336\n"
        "v_exp_f32 v76, v76\n"
        "v_mfma_f32_16x16x32_bf16 v[104:107], a[224:227], a[192:195], 0\n"
        "ds_read_b128 a[224:227], v241 offset:12416\n"
        "v_cvt_pk_bf16_f32 v64, v64, v65\n"
        "v_mfma_f32_16x16x32_bf16 v[104:107], a[228:231], a[196:199], v[104:107]\n"
        "ds_read_b128 a[228:231], v240 offset:12544\n"
        "v_cvt_pk_bf16_f32 v65, v66, v67\n"
        "v_mfma_f32_16x16x32_bf16 v[104:107], a[232:235], a[200:203], v[104:107]\n"
        "ds_read_b128 a[232:235], v241 offset:12544\n"
        "v_cvt_pk_bf16_f32 v138, v100, v101\n"
        "v_mfma_f32_16x16x32_bf16 v[104:107], a[236:239], a[204:207], v[104:107]\n"
        "buffer_load_dwordx4 v244, s[8:11], s35 offen lds\n"
        "v_mfma_f32_16x16x32_bf16 v[104:107], a[240:243], a[208:211], v[104:107]\n"
        "s_mov_b32 m0, s48\n"
        "v_cvt_pk_bf16_f32 v139, v102, v103\n"
        "v_cvt_pk_bf16_f32 v66, v68, v69\n"
        "v_mfma_f32_16x16x32_bf16 v[104:107], a[244:247], a[212:215], v[104:107]\n"
        "s_waitcnt lgkmcnt(5)\n"
        "v_exp_f32 v77, v77\n"
        "v_mfma_f32_16x16x32_bf16 v[96:99], a[72:75], a[248:251], 0\n"
        "s_waitcnt lgkmcnt(4)\n"
        "buffer_load_dwordx4 v249, s[12:15], s36 offen offset:0 lds\n"
        "v_mfma_f32_16x16x32_bf16 v[96:99], a[76:79], a[216:219], v[96:99]\n"
        "s_waitcnt lgkmcnt(3)\n"
        "ds_read_b128 v[162:165], v252 offset:0\n"
        "v_xor_b32 v116, 32, v249\n"
        "v_mfma_f32_16x16x32_bf16 v[96:99], a[80:83], a[220:223], v[96:99]\n"
        "s_waitcnt lgkmcnt(3)\n"
        "ds_read_b128 v[182:185], v252 offset:512\n"
        "v_cvt_pk_bf16_f32 v146, v104, v105\n"
        "v_mfma_f32_16x16x32_bf16 v[96:99], a[84:87], a[224:227], v[96:99]\n"
        "s_waitcnt lgkmcnt(3)\n"
        "v_cvt_pk_bf16_f32 v147, v106, v107\n"
        "v_cvt_pk_bf16_f32 v67, v70, v71\n"
        "v_mfma_f32_16x16x32_bf16 v[96:99], a[88:91], a[228:231], v[96:99]\n"
        "s_waitcnt lgkmcnt(2)\n"
        "v_exp_f32 v78, v78\n"
        "v_mfma_f32_16x16x32_bf16 v[96:99], a[92:95], a[232:235], v[96:99]\n"
        "buffer_load_dwordx4 v116, s[12:15], s36 offen offset:1024 lds\n"
        "v_mfma_f32_16x16x32_bf16 v[100:103], a[116:119], a[232:235], 0\n"
        "v_exp_f32 v79, v79\n"
        "v_mfma_f32_16x16x32_bf16 v[100:103], a[112:115], a[228:231], v[100:103]\n"
        "v_cvt_pk_bf16_f32 v72, v72, v73\n"
        "v_cvt_pk_bf16_f32 v73, v74, v75\n"
        "v_mfma_f32_16x16x32_bf16 v[100:103], a[108:111], a[224:227], v[100:103]\n"
        "v_cvt_pk_bf16_f32 v74, v76, v77\n"
        "v_cvt_pk_bf16_f32 v192, v96, v97\n"
        "v_mfma_f32_16x16x32_bf16 v[100:103], a[104:107], a[220:223], v[100:103]\n"
        "v_cvt_pk_bf16_f32 v193, v98, v99\n"
        "v_cvt_pk_bf16_f32 v75, v78, v79\n"
        "v_mfma_f32_16x16x32_bf16 v[100:103], a[100:103], a[216:219], v[100:103]\n"
        "44:\n"
        "s_cbranch_execz 40b\n"
        "v_mfma_f32_16x16x32_bf16 v[100:103], a[96:99], a[248:251], v[100:103]\n"
        "s_nop 2\n"
        "v_mfma_f32_16x16x32_bf16 v[104:107], a[120:123], a[248:251], 0\n"
        "s_nop 2\n"
        "v_mfma_f32_16x16x32_bf16 v[104:107], a[124:127], a[216:219], v[104:107]\n"
        "v_cvt_pk_bf16_f32 v194, v100, v101\n"
        "v_cvt_pk_bf16_f32 v195, v102, v103\n"
        "v_mfma_f32_16x16x32_bf16 v[104:107], a[128:131], a[220:223], v[104:107]\n"
        "s_cmp_ge_u32 s58, 2816\n"
        "v_mfma_f32_16x16x32_bf16 v[104:107], a[132:135], a[224:227], v[104:107]\n"
        "s_cselect_b32 s20, s3, 0\n"
        "v_mfma_f32_16x16x32_bf16 v[104:107], a[136:139], a[228:231], v[104:107]\n"
        "s_lshl_b32 s21, s27, 8\n"
        "v_mfma_f32_16x16x32_bf16 v[104:107], a[140:143], a[232:235], v[104:107]\n"
        "s_add_u32 s21, s21, 704\n"
        "s_nop 1\n"
        "v_mfma_f32_16x16x32_bf16 v[96:99], a[20:23], a[232:235], 0\n"
        "s_nop 2\n"
        "v_mfma_f32_16x16x32_bf16 v[96:99], a[16:19], a[228:231], v[96:99]\n"
        "v_cvt_pk_bf16_f32 v208, v104, v105\n"
        "v_cvt_pk_bf16_f32 v209, v106, v107\n"
        "v_mfma_f32_16x16x32_bf16 v[96:99], a[12:15], a[224:227], v[96:99]\n"
        "v_mfma_f32_16x16x32_bf16 v[96:99], a[8:11], a[220:223], v[96:99]\n"
        "v_mfma_f32_16x16x32_bf16 v[96:99], a[4:7], a[216:219], v[96:99]\n"
        "ds_read_b128 v[202:205], v251 offset:4096\n"
        "v_mfma_f32_16x16x32_bf16 v[96:99], a[0:3], a[248:251], v[96:99]\n"
        "s_nop 2\n"
        "v_mfma_f32_16x16x32_bf16 v[68:71], v[192:195], v[156:159], v[8:11]\n"
        "s_waitcnt lgkmcnt(2)\n"
        "s_nop 1\n"
        "v_mfma_f32_16x16x32_bf16 v[68:71], v[208:211], v[160:163], v[68:71]\n"
        "v_mfma_f32_16x16x32_bf16 v[76:79], v[192:195], v[176:179], v[24:27]\n"
        "s_waitcnt lgkmcnt(1)\n"
        "v_mul_f32 v96, s50, v96\n"
        "v_mul_f32 v97, s50, v97\n"
        "v_mfma_f32_16x16x32_bf16 v[76:79], v[208:211], v[180:183], v[76:79]\n"
        "v_mul_f32 v98, s50, v98\n"
        "v_mul_f32 v99, s50, v99\n"
        "v_mfma_f32_16x16x32_bf16 v[100:103], a[24:27], a[248:251], 0\n"
        "v_cvt_pk_bf16_f32 v196, v96, v97\n"
        "v_cvt_pk_bf16_f32 v197, v98, v99\n"
        "v_mfma_f32_16x16x32_bf16 v[100:103], a[28:31], a[216:219], v[100:103]\n"
        "v_exp_f32 v68, v68\n"
        "v_mfma_f32_16x16x32_bf16 v[100:103], a[32:35], a[220:223], v[100:103]\n"
        "v_exp_f32 v69, v69\n"
        "v_mfma_f32_16x16x32_bf16 v[100:103], a[36:39], a[224:227], v[100:103]\n"
        "v_exp_f32 v70, v70\n"
        "v_mfma_f32_16x16x32_bf16 v[100:103], a[40:43], a[228:231], v[100:103]\n"
        "v_exp_f32 v71, v71\n"
        "v_mfma_f32_16x16x32_bf16 v[100:103], a[44:47], a[232:235], v[100:103]\n"
        "v_exp_f32 v76, v76\n"
        "v_mfma_f32_16x16x32_bf16 v[104:107], a[68:71], a[232:235], 0\n"
        "v_exp_f32 v77, v77\n"
        "v_mfma_f32_16x16x32_bf16 v[104:107], a[64:67], a[228:231], v[104:107]\n"
        "v_exp_f32 v78, v78\n"
        "v_mfma_f32_16x16x32_bf16 v[104:107], a[60:63], a[224:227], v[104:107]\n"
        "v_exp_f32 v79, v79\n"
        "v_mfma_f32_16x16x32_bf16 v[104:107], a[56:59], a[220:223], v[104:107]\n"
        "v_mul_f32 v100, s50, v100\n"
        "v_mul_f32 v101, s50, v101\n"
        "v_mfma_f32_16x16x32_bf16 v[104:107], a[52:55], a[216:219], v[104:107]\n"
        "v_mul_f32 v102, s50, v102\n"
        "v_mul_f32 v103, s50, v103\n"
        "v_mfma_f32_16x16x32_bf16 v[104:107], a[48:51], a[248:251], v[104:107]\n"
        "v_cvt_pk_bf16_f32 v198, v100, v101\n"
        "v_cvt_pk_bf16_f32 v199, v102, v103\n"
        "v_mfma_f32_16x16x32_bf16 v[96:99], a[248:251], a[144:147], 0\n"
        "ds_read_b128 a[236:239], v240 offset:13824\n"
        "v_cvt_pk_bf16_f32 v68, v68, v69\n"
        "v_mfma_f32_16x16x32_bf16 v[96:99], a[216:219], a[148:151], v[96:99]\n"
        "ds_read_b128 a[240:243], v241 offset:13824\n"
        "v_cvt_pk_bf16_f32 v69, v70, v71\n"
        "v_mfma_f32_16x16x32_bf16 v[96:99], a[220:223], a[152:155], v[96:99]\n"
        "v_mul_f32 v104, s50, v104\n"
        "v_mul_f32 v105, s50, v105\n"
        "v_mfma_f32_16x16x32_bf16 v[96:99], a[224:227], a[156:159], v[96:99]\n"
        "v_mul_f32 v106, s50, v106\n"
        "v_mul_f32 v107, s50, v107\n"
        "v_mfma_f32_16x16x32_bf16 v[96:99], a[228:231], a[160:163], v[96:99]\n"
        "v_cvt_pk_bf16_f32 v200, v104, v105\n"
        "v_cvt_pk_bf16_f32 v201, v106, v107\n"
        "v_mfma_f32_16x16x32_bf16 v[96:99], a[232:235], a[164:167], v[96:99]\n"
        "ds_read_b128 a[244:247], v240 offset:13952\n"
        "v_mov_b64 v[206:207], v[200:201]\n"
        "v_mfma_f32_16x16x32_bf16 v[80:83], v[152:155], v[196:199], v[32:35]\n"
        "s_waitcnt lgkmcnt(3)\n"
        "v_cvt_pk_bf16_f32 v76, v76, v77\n"
        "v_cvt_pk_bf16_f32 v77, v78, v79\n"
        "v_mfma_f32_16x16x32_bf16 v[80:83], v[168:171], v[200:203], v[80:83]\n"
        "s_nop 0\n"
        "v_mfma_f32_16x16x32_bf16 v[84:87], v[172:175], v[196:199], v[36:39]\n"
        "v_cvt_pk_bf16_f32 v132, v96, v97\n"
        "v_cvt_pk_bf16_f32 v133, v98, v99\n"
        "v_mfma_f32_16x16x32_bf16 v[84:87], v[188:191], v[204:207], v[84:87]\n"
        "ds_read_b128 v[202:205], v252 offset:4096\n"
        "v_lshrrev_b32 v121, 6, v250\n"
        "v_mfma_f32_16x16x32_bf16 v[100:103], a[232:235], a[188:191], 0\n"
        "v_add3_u32 v121, v121, v245, s67\n"
        "v_mfma_f32_16x16x32_bf16 v[100:103], a[228:231], a[184:187], v[100:103]\n"
        "v_exp_f32 v80, v80\n"
        "v_mfma_f32_16x16x32_bf16 v[100:103], a[224:227], a[180:183], v[100:103]\n"
        "v_exp_f32 v81, v81\n"
        "v_mfma_f32_16x16x32_bf16 v[100:103], a[220:223], a[176:179], v[100:103]\n"
        "v_exp_f32 v82, v82\n"
        "v_mfma_f32_16x16x32_bf16 v[100:103], a[216:219], a[172:175], v[100:103]\n"
        "v_exp_f32 v83, v83\n"
        "v_mfma_f32_16x16x32_bf16 v[100:103], a[248:251], a[168:171], v[100:103]\n"
        "v_exp_f32 v84, v84\n"
        "v_mfma_f32_16x16x32_bf16 v[104:107], a[248:251], a[192:195], 0\n"
        "ds_read_b128 a[248:251], v241 offset:13952\n"
        "v_cvt_pk_bf16_f32 v80, v80, v81\n"
        "v_mfma_f32_16x16x32_bf16 v[104:107], a[216:219], a[196:199], v[104:107]\n"
        "ds_read_b128 a[216:219], v240 offset:14080\n"
        "v_cvt_pk_bf16_f32 v81, v82, v83\n"
        "v_mfma_f32_16x16x32_bf16 v[104:107], a[220:223], a[200:203], v[104:107]\n"
        "ds_read_b128 a[220:223], v241 offset:14080\n"
        "v_cvt_pk_bf16_f32 v140, v100, v101\n"
        "v_mfma_f32_16x16x32_bf16 v[104:107], a[224:227], a[204:207], v[104:107]\n"
        "v_cvt_pk_bf16_f32 v141, v102, v103\n"
        "v_mfma_f32_16x16x32_bf16 v[104:107], a[228:231], a[208:211], v[104:107]\n"
        "v_exp_f32 v85, v85\n"
        "v_mfma_f32_16x16x32_bf16 v[104:107], a[232:235], a[212:215], v[104:107]\n"
        "s_waitcnt lgkmcnt(6)\n"
        "v_exp_f32 v86, v86\n"
        "v_mfma_f32_16x16x32_bf16 v[96:99], a[72:75], a[236:239], 0\n"
        "s_waitcnt lgkmcnt(5)\n"
        "v_cvt_pk_bf16_f32 v82, v84, v85\n"
        "v_mfma_f32_16x16x32_bf16 v[96:99], a[76:79], a[240:243], v[96:99]\n"
        "s_waitcnt lgkmcnt(4)\n"
        "v_exp_f32 v87, v87\n"
        "v_mfma_f32_16x16x32_bf16 v[96:99], a[80:83], a[244:247], v[96:99]\n"
        "s_waitcnt lgkmcnt(2)\n"
        "v_cvt_pk_bf16_f32 v83, v86, v87\n"
        "v_mfma_f32_16x16x32_bf16 v[96:99], a[84:87], a[248:251], v[96:99]\n"
        "s_waitcnt lgkmcnt(1)\n"
        "v_cvt_pk_bf16_f32 v148, v104, v105\n"
        "v_cvt_pk_bf16_f32 v149, v106, v107\n"
        "v_mfma_f32_16x16x32_bf16 v[96:99], a[88:91], a[216:219], v[96:99]\n"
        "s_waitcnt lgkmcnt(0)\n"
        "v_mfma_f32_16x16x32_bf16 v[96:99], a[92:95], a[220:223], v[96:99]\n"
        "s_nop 2\n"
        "v_mfma_f32_16x16x32_bf16 v[84:87], v[192:195], v[196:199], v[40:43]\n"
        "s_nop 1\n"
        "v_mfma_f32_16x16x32_bf16 v[84:87], v[208:211], v[200:203], v[84:87]\n"
        "v_mfma_f32_16x16x32_bf16 v[100:103], a[116:119], a[220:223], 0\n"
        "v_cvt_pk_bf16_f32 v212, v96, v97\n"
        "v_cvt_pk_bf16_f32 v213, v98, v99\n"
        "v_mfma_f32_16x16x32_bf16 v[100:103], a[112:115], a[216:219], v[100:103]\n"
        "v_add_u32 v240, s45, v240\n"
        "v_add_u32 v241, s45, v241\n"
        "v_mfma_f32_16x16x32_bf16 v[100:103], a[108:111], a[248:251], v[100:103]\n"
        "v_mfma_f32_16x16x32_bf16 v[100:103], a[104:107], a[244:247], v[100:103]\n"
        "v_exp_f32 v84, v84\n"
        "v_mfma_f32_16x16x32_bf16 v[100:103], a[100:103], a[240:243], v[100:103]\n"
        "v_exp_f32 v85, v85\n"
        "v_mfma_f32_16x16x32_bf16 v[100:103], a[96:99], a[236:239], v[100:103]\n"
        "v_exp_f32 v86, v86\n"
        "v_mfma_f32_16x16x32_bf16 v[104:107], a[120:123], a[236:239], 0\n"
        "v_exp_f32 v87, v87\n"
        "v_mfma_f32_16x16x32_bf16 v[104:107], a[124:127], a[240:243], v[104:107]\n"
        "v_cvt_pk_bf16_f32 v84, v84, v85\n"
        "v_cvt_pk_bf16_f32 v85, v86, v87\n"
        "v_mfma_f32_16x16x32_bf16 v[104:107], a[128:131], a[244:247], v[104:107]\n"
        "s_waitcnt vmcnt(8)\n"
        "v_readfirstlane_b32 s59, v120\n"
        "v_mfma_f32_16x16x32_bf16 v[104:107], a[132:135], a[248:251], v[104:107]\n"
        "v_cvt_pk_bf16_f32 v214, v100, v101\n"
        "v_cvt_pk_bf16_f32 v215, v102, v103\n"
        "v_mfma_f32_16x16x32_bf16 v[104:107], a[136:139], a[216:219], v[104:107]\n"
        "s_min_u32 s59, s59, 768\n"
        "v_mfma_f32_16x16x32_bf16 v[104:107], a[140:143], a[220:223], v[104:107]\n"
        "s_add_u32 s59, s59, 3328\n"
        "s_nop 0\n"
        "v_mov_b32 v120, s59\n"
        "v_mfma_f32_16x16x32_bf16 v[96:99], a[20:23], a[220:223], 0\n"
        "s_lshr_b32 s46, s56, 6\n"
        "s_nop 1\n"
        "v_mfma_f32_16x16x32_bf16 v[96:99], a[16:19], a[216:219], v[96:99]\n"
        "s_mul_i32 s46, s46, 1572864\n"
        "v_cvt_pk_bf16_f32 v230, v104, v105\n"
        "v_cvt_pk_bf16_f32 v231, v106, v107\n"
        "v_mfma_f32_16x16x32_bf16 v[96:99], a[12:15], a[248:251], v[96:99]\n"
        "s_barrier\n"
        "v_mfma_f32_16x16x32_bf16 v[96:99], a[8:11], a[244:247], v[96:99]\n"
        "s_add_u32 s46, s46, s37\n"
        "ds_write_b32 v121, v120 offset:768\n"
        "v_mfma_f32_16x16x32_bf16 v[96:99], a[4:7], a[240:243], v[96:99]\n"
        "s_cmp_eq_u32 s46, 100663296\n"
        "v_mfma_f32_16x16x32_bf16 v[96:99], a[0:3], a[236:239], v[96:99]\n"
        "s_cselect_b32 s46, 0, s46\n"
        "s_nop 1\n"
        "v_mfma_f32_16x16x32_bf16 v[108:111], v[212:215], v[156:159], v[12:15]\n"
        "s_and_b32 s43, s56, 63\n"
        "ds_read_b128 v[222:225], v251 offset:4608\n"
        "v_mfma_f32_16x16x32_bf16 v[108:111], v[228:231], v[164:167], v[108:111]\n"
        "s_mul_i32 s43, s43, 3072\n"
        "v_mul_f32 v96, s50, v96\n"
        "v_mul_f32 v97, s50, v97\n"
        "v_mfma_f32_16x16x32_bf16 v[112:115], v[212:215], v[176:179], v[28:31]\n"
        "s_add_u32 s44, s43, 3072\n"
        "v_mul_f32 v98, s50, v98\n"
        "v_mul_f32 v99, s50, v99\n"
        "v_mfma_f32_16x16x32_bf16 v[112:115], v[228:231], v[184:187], v[112:115]\n"
        "s_cmp_eq_u32 s44, 196608\n"
        "v_cvt_pk_bf16_f32 v216, v96, v97\n"
        "v_cvt_pk_bf16_f32 v217, v98, v99\n"
        "v_mfma_f32_16x16x32_bf16 v[116:119], v[212:215], v[196:199], v[44:47]\n"
        "s_cselect_b32 s44, 0, s44\n"
        "v_exp_f32 v108, v108\n"
        "v_mfma_f32_16x16x32_bf16 v[116:119], v[228:231], v[204:207], v[116:119]\n"
        "s_add_u32 s39, s46, s44\n"
        "v_exp_f32 v109, v109\n"
        "v_mfma_f32_16x16x32_bf16 v[100:103], a[24:27], a[236:239], 0\n"
        "s_add_u32 s43, s43, s46\n"
        "v_exp_f32 v110, v110\n"
        "v_mfma_f32_16x16x32_bf16 v[100:103], a[28:31], a[240:243], v[100:103]\n"
        "s_add_u32 s38, s43, 1536\n"
        "v_exp_f32 v111, v111\n"
        "v_mfma_f32_16x16x32_bf16 v[100:103], a[32:35], a[244:247], v[100:103]\n"
        "v_exp_f32 v112, v112\n"
        "v_mfma_f32_16x16x32_bf16 v[100:103], a[36:39], a[248:251], v[100:103]\n"
        "v_exp_f32 v113, v113\n"
        "v_mfma_f32_16x16x32_bf16 v[100:103], a[40:43], a[216:219], v[100:103]\n"
        "v_exp_f32 v114, v114\n"
        "v_mfma_f32_16x16x32_bf16 v[100:103], a[44:47], a[220:223], v[100:103]\n"
        "v_exp_f32 v115, v115\n"
        "v_mfma_f32_16x16x32_bf16 v[104:107], a[68:71], a[220:223], 0\n"
        "v_exp_f32 v116, v116\n"
        "v_mfma_f32_16x16x32_bf16 v[104:107], a[64:67], a[216:219], v[104:107]\n"
        "v_exp_f32 v117, v117\n"
        "v_mfma_f32_16x16x32_bf16 v[104:107], a[60:63], a[248:251], v[104:107]\n"
        "v_exp_f32 v118, v118\n"
        "v_mfma_f32_16x16x32_bf16 v[104:107], a[56:59], a[244:247], v[104:107]\n"
        "v_mul_f32 v100, s50, v100\n"
        "v_mul_f32 v101, s50, v101\n"
        "v_mfma_f32_16x16x32_bf16 v[104:107], a[52:55], a[240:243], v[104:107]\n"
        "v_mul_f32 v102, s50, v102\n"
        "v_mul_f32 v103, s50, v103\n"
        "v_mfma_f32_16x16x32_bf16 v[104:107], a[48:51], a[236:239], v[104:107]\n"
        "v_cvt_pk_bf16_f32 v218, v100, v101\n"
        "v_cvt_pk_bf16_f32 v219, v102, v103\n"
        "v_mfma_f32_16x16x32_bf16 v[96:99], a[236:239], a[144:147], 0\n"
        "v_exp_f32 v119, v119\n"
        "v_mfma_f32_16x16x32_bf16 v[96:99], a[240:243], a[148:151], v[96:99]\n"
        "v_cvt_pk_bf16_f32 v70, v108, v109\n"
        "v_cvt_pk_bf16_f32 v71, v110, v111\n"
        "v_mfma_f32_16x16x32_bf16 v[96:99], a[244:247], a[152:155], v[96:99]\n"
        "v_mul_f32 v104, s50, v104\n"
        "v_mul_f32 v105, s50, v105\n"
        "v_mfma_f32_16x16x32_bf16 v[96:99], a[248:251], a[156:159], v[96:99]\n"
        "v_mul_f32 v106, s50, v106\n"
        "v_mul_f32 v107, s50, v107\n"
        "v_mfma_f32_16x16x32_bf16 v[96:99], a[216:219], a[160:163], v[96:99]\n"
        "v_cvt_pk_bf16_f32 v220, v104, v105\n"
        "v_cvt_pk_bf16_f32 v221, v106, v107\n"
        "v_mfma_f32_16x16x32_bf16 v[96:99], a[220:223], a[164:167], v[96:99]\n"
        "ds_read_b128 v[156:159], v247 offset:0\n"
        "v_mov_b64 v[226:227], v[220:221]\n"
        "v_mfma_f32_16x16x32_bf16 v[88:91], v[152:155], v[216:219], v[48:51]\n"
        "s_waitcnt lgkmcnt(1)\n"
        "ds_read_b128 v[164:167], v247 offset:4096\n"
        "v_cvt_pk_bf16_f32 v78, v112, v113\n"
        "v_mfma_f32_16x16x32_bf16 v[88:91], v[168:171], v[220:223], v[88:91]\n"
        "ds_read_b128 v[160:163], v247 offset:256\n"
        "v_cvt_pk_bf16_f32 v79, v114, v115\n"
        "v_mfma_f32_16x16x32_bf16 v[92:95], v[172:175], v[216:219], v[52:55]\n"
        "s_mov_b32 s56, s57\n"
        "ds_read_b128 v[152:155], v247 offset:4352\n"
        "v_cvt_pk_bf16_f32 v134, v96, v97\n"
        "v_mfma_f32_16x16x32_bf16 v[92:95], v[188:191], v[224:227], v[92:95]\n"
        "s_mov_b32 s57, s58\n"
        "ds_read_b128 v[222:225], v252 offset:4608\n"
        "v_cvt_pk_bf16_f32 v135, v98, v99\n"
        "v_mfma_f32_16x16x32_bf16 v[100:103], a[220:223], a[188:191], 0\n"
        "s_xor_b32 s48, s48, 8192\n"
        "v_exp_f32 v88, v88\n"
        "v_mfma_f32_16x16x32_bf16 v[100:103], a[216:219], a[184:187], v[100:103]\n"
        "v_exp_f32 v89, v89\n"
        "v_mfma_f32_16x16x32_bf16 v[100:103], a[248:251], a[180:183], v[100:103]\n"
        "v_exp_f32 v90, v90\n"
        "v_mfma_f32_16x16x32_bf16 v[100:103], a[244:247], a[176:179], v[100:103]\n"
        "v_exp_f32 v91, v91\n"
        "v_mfma_f32_16x16x32_bf16 v[100:103], a[240:243], a[172:175], v[100:103]\n"
        "v_exp_f32 v92, v92\n"
        "v_mfma_f32_16x16x32_bf16 v[100:103], a[236:239], a[168:171], v[100:103]\n"
        "v_exp_f32 v93, v93\n"
        "v_mfma_f32_16x16x32_bf16 v[104:107], a[236:239], a[192:195], 0\n"
        "ds_read_b128 a[236:239], v248 offset:0\n"
        "v_cvt_pk_bf16_f32 v86, v116, v117\n"
        "v_mfma_f32_16x16x32_bf16 v[104:107], a[240:243], a[196:199], v[104:107]\n"
        "ds_read_b128 a[240:243], v248 offset:6144\n"
        "v_cvt_pk_bf16_f32 v87, v118, v119\n"
        "v_mfma_f32_16x16x32_bf16 v[104:107], a[244:247], a[200:203], v[104:107]\n"
        "ds_read_b128 a[244:247], v254 offset:16\n"
        "v_mov_b64 v[116:117], 0\n"
        "v_mfma_f32_16x16x32_bf16 v[104:107], a[248:251], a[204:207], v[104:107]\n"
        "v_cvt_pk_bf16_f32 v142, v100, v101\n"
        "v_cvt_pk_bf16_f32 v143, v102, v103\n"
        "v_mfma_f32_16x16x32_bf16 v[104:107], a[216:219], a[208:211], v[104:107]\n"
        "ds_read_b128 a[248:251], v254 offset:6160\n"
        "v_cvt_pk_bf16_f32 v88, v88, v89\n"
        "v_mfma_f32_16x16x32_bf16 v[104:107], a[220:223], a[212:215], v[104:107]\n"
        "ds_read_b128 a[216:219], v248 offset:32\n"
        "v_cvt_pk_bf16_f32 v89, v90, v91\n"
        "v_mfma_f32_16x16x32_bf16 v[108:111], v[192:195], v[216:219], v[56:59]\n"
        "s_waitcnt lgkmcnt(5)\n"
        "ds_read_b128 a[220:223], v248 offset:6176\n"
        "v_cvt_pk_bf16_f32 v90, v92, v93\n"
        "v_mfma_f32_16x16x32_bf16 v[108:111], v[208:211], v[220:223], v[108:111]\n"
        "v_exp_f32 v94, v94\n"
        "v_mfma_f32_16x16x32_bf16 v[112:115], v[212:215], v[216:219], v[60:63]\n"
        "ds_read_b128 a[224:227], v254 offset:48\n"
        "v_cvt_pk_bf16_f32 v150, v104, v105\n"
        "v_mfma_f32_16x16x32_bf16 v[112:115], v[228:231], v[224:227], v[112:115]\n"
        "s_waitcnt lgkmcnt(6)\n"
        "ds_read_b128 a[228:231], v254 offset:6192\n"
        "v_cvt_pk_bf16_f32 v151, v106, v107\n"
        "v_mfma_f32_16x16x32_bf16 v[172:175], v[156:159], a[236:239], 0\n"
        "s_waitcnt lgkmcnt(6)\n"
        "v_exp_f32 v95, v95\n"
        "v_mfma_f32_16x16x32_bf16 v[172:175], v[164:167], a[240:243], v[172:175]\n"
        "ds_read_b128 a[232:235], v248 offset:64\n"
        "v_cvt_pk_bf16_f32 v91, v94, v95\n"
        "v_mfma_f32_16x16x32_bf16 v[200:203], v[152:155], a[240:243], 0\n"
        "v_exp_f32 v108, v108\n"
        "v_mfma_f32_16x16x32_bf16 v[200:203], v[160:163], a[236:239], v[200:203]\n"
        "v_exp_f32 v109, v109\n"
        "v_smfmac_f32_16x16x64_bf16 v[116:119], a[252:255], v[64:71], v255\n"
        "ds_read_b128 a[236:239], v248 offset:6208\n"
        "v_cvt_pk_bf16_f32 v92, v108, v109\n"
        "v_mfma_f32_16x16x32_bf16 v[120:123], v[128:131], v[64:67], 0\n"
        "v_exp_f32 v110, v110\n"
        "v_mfma_f32_16x16x32_bf16 v[120:123], v[132:135], v[68:71], v[120:123]\n"
        "v_exp_f32 v111, v111\n"
        "v_mfma_f32_16x16x32_bf16 v[124:127], v[140:143], v[68:71], 0\n"
        "v_exp_f32 v112, v112\n"
        "v_mfma_f32_16x16x32_bf16 v[124:127], v[136:139], v[64:67], v[124:127]\n"
        "v_exp_f32 v113, v113\n"
        "v_mfma_f32_16x16x32_bf16 v[96:99], v[144:147], v[64:67], 0\n"
        "v_exp_f32 v114, v114\n"
        "v_mfma_f32_16x16x32_bf16 v[96:99], v[148:151], v[68:71], v[96:99]\n"
        "s_waitcnt lgkmcnt(7)\n"
        "ds_read_b128 a[240:243], v254 offset:80\n"
        "v_add_f32 v116, v116, v117\n"
        "v_mfma_f32_16x16x32_bf16 v[204:207], v[160:163], a[244:247], 0\n"
        "s_waitcnt lgkmcnt(7)\n"
        "v_rcp_f32 v117, v116\n"
        "v_mfma_f32_16x16x32_bf16 v[204:207], v[152:155], a[248:251], v[204:207]\n"
        "v_exp_f32 v115, v115\n"
        "v_mfma_f32_16x16x32_bf16 v[176:179], v[164:167], a[248:251], 0\n"
        "v_mul_f32 v120, v117, v120\n"
        "v_mul_f32 v121, v117, v121\n"
        "v_mfma_f32_16x16x32_bf16 v[176:179], v[156:159], a[244:247], v[176:179]\n"
        "s_waitcnt lgkmcnt(6)\n"
        "ds_read_b128 a[244:247], v254 offset:6224\n"
        "v_mul_f32 v122, v117, v122\n"
        "v_mfma_f32_16x16x32_bf16 v[180:183], v[156:159], a[216:219], 0\n"
        "s_waitcnt lgkmcnt(6)\n"
        "ds_read_b128 a[248:251], v248 offset:36864\n"
        "v_mov_b64 v[100:101], 0\n"
        "v_mfma_f32_16x16x32_bf16 v[180:183], v[164:167], a[220:223], v[180:183]\n"
        "ds_read_b128 v[232:235], v247 offset:8192\n"
        "v_mul_f32 v123, v117, v123\n"
        "v_mfma_f32_16x16x32_bf16 v[212:215], v[152:155], a[220:223], 0\n"
        "ds_read_b128 v[64:67], v247 offset:12288\n"
        "v_cvt_pk_bf16_f32 v93, v110, v111\n"
        "v_mfma_f32_16x16x32_bf16 v[212:215], v[160:163], a[216:219], v[212:215]\n"
        "s_waitcnt lgkmcnt(8)\n"
        "ds_read_b128 v[236:239], v247 offset:8448\n"
        "v_cvt_pk_bf16_f32 v120, v120, v121\n"
        "v_mfma_f32_16x16x32_bf16 v[216:219], v[160:163], a[224:227], 0\n"
        "s_waitcnt lgkmcnt(8)\n"
        "ds_read_b128 v[68:71], v247 offset:12544\n"
        "v_cvt_pk_bf16_f32 v121, v122, v123\n"
        "v_mfma_f32_16x16x32_bf16 v[216:219], v[152:155], a[228:231], v[216:219]\n"
        "ds_write_b32 v245, v120 offset:0\n"
        "v_mfma_f32_16x16x32_bf16 v[184:187], v[164:167], a[228:231], 0\n"
        "ds_write_b32 v245, v121 offset:4\n"
        "v_mfma_f32_16x16x32_bf16 v[184:187], v[156:159], a[224:227], v[184:187]\n"
        "s_waitcnt lgkmcnt(10)\n"
        "ds_read_b128 a[216:219], v248 offset:43008\n"
        "v_cvt_pk_bf16_f32 v94, v112, v113\n"
        "v_mfma_f32_16x16x32_bf16 v[192:195], v[156:159], a[232:235], 0\n"
        "s_waitcnt lgkmcnt(10)\n"
        "v_mul_f32 v124, v117, v124\n"
        "v_mul_f32 v125, v117, v125\n"
        "v_mfma_f32_16x16x32_bf16 v[192:195], v[164:167], a[236:239], v[192:195]\n"
        "v_maximum3_f32 v253, v253, v116, v117\n"
        "v_mul_f32 v126, v117, v126\n"
        "v_mfma_f32_16x16x32_bf16 v[220:223], v[152:155], a[236:239], 0\n"
        "v_mul_f32 v127, v117, v127\n"
        "v_cvt_pk_bf16_f32 v95, v114, v115\n"
        "v_mfma_f32_16x16x32_bf16 v[220:223], v[160:163], a[232:235], v[220:223]\n"
        "ds_read_b128 a[220:223], v254 offset:36880\n"
        "v_cvt_pk_bf16_f32 v124, v124, v125\n"
        "v_smfmac_f32_16x16x64_bf16 v[100:103], a[252:255], v[72:79], v255\n"
        "ds_read_b128 a[224:227], v254 offset:43024\n"
        "v_cvt_pk_bf16_f32 v125, v126, v127\n"
        "v_mfma_f32_16x16x32_bf16 v[104:107], v[128:131], v[72:75], 0\n"
        "ds_write_b32 v245, v124 offset:8\n"
        "v_mfma_f32_16x16x32_bf16 v[104:107], v[132:135], v[76:79], v[104:107]\n"
        "ds_write_b32 v245, v125 offset:12\n"
        "v_mfma_f32_16x16x32_bf16 v[108:111], v[140:143], v[76:79], 0\n"
        "v_mul_f32 v96, v117, v96\n"
        "v_mul_f32 v97, v117, v97\n"
        "v_mfma_f32_16x16x32_bf16 v[108:111], v[136:139], v[72:75], v[108:111]\n"
        "v_mul_f32 v98, v117, v98\n"
        "v_mul_f32 v99, v117, v99\n"
        "v_mfma_f32_16x16x32_bf16 v[112:115], v[144:147], v[72:75], 0\n"
        "ds_read_b128 a[228:231], v248 offset:36896\n"
        "v_cvt_pk_bf16_f32 v96, v96, v97\n"
        "v_mfma_f32_16x16x32_bf16 v[112:115], v[148:151], v[76:79], v[112:115]\n"
        "s_waitcnt lgkmcnt(14)\n"
        "ds_read_b128 a[232:235], v248 offset:43040\n"
        "v_cvt_pk_bf16_f32 v97, v98, v99\n"
        "v_mfma_f32_16x16x32_bf16 v[224:227], v[160:163], a[240:243], 0\n"
        "s_waitcnt lgkmcnt(14)\n"
        "ds_write_b32 v246, v96 offset:0\n"
        "v_mfma_f32_16x16x32_bf16 v[224:227], v[152:155], a[244:247], v[224:227]\n"
        "ds_write_b32 v246, v97 offset:4\n"
        "v_mfma_f32_16x16x32_bf16 v[196:199], v[164:167], a[244:247], 0\n"
        "ds_read_b128 a[236:239], v254 offset:36912\n"
        "v_add_f32 v100, v100, v101\n"
        "v_mfma_f32_16x16x32_bf16 v[196:199], v[156:159], a[240:243], v[196:199]\n"
        "s_waitcnt lgkmcnt(15)\n"
        "v_rcp_f32 v101, v100\n"
        "v_mfma_f32_16x16x32_bf16 v[172:175], v[232:235], a[248:251], v[172:175]\n"
        "s_waitcnt lgkmcnt(9)\n"
        "ds_read_b128 a[240:243], v254 offset:43056\n"
        "v_mul_f32 v104, v101, v104\n"
        "v_mfma_f32_16x16x32_bf16 v[172:175], v[64:67], a[216:219], v[172:175]\n"
        "ds_read_b128 a[244:247], v248 offset:36928\n"
        "v_mul_f32 v105, v101, v105\n"
        "v_mfma_f32_16x16x32_bf16 v[200:203], v[68:71], a[216:219], v[200:203]\n"
        "v_mul_f32 v106, v101, v106\n"
        "v_mul_f32 v107, v101, v107\n"
        "v_mfma_f32_16x16x32_bf16 v[200:203], v[236:239], a[248:251], v[200:203]\n"
        "s_waitcnt lgkmcnt(10)\n"
        "ds_read_b128 a[248:251], v248 offset:43072\n"
        "v_cvt_pk_bf16_f32 v104, v104, v105\n"
        "v_mfma_f32_16x16x32_bf16 v[204:207], v[236:239], a[220:223], v[204:207]\n"
        "s_waitcnt lgkmcnt(10)\n"
        "ds_read_b128 a[216:219], v254 offset:36944\n"
        "v_mov_b64 v[116:117], 0\n"
        "v_mfma_f32_16x16x32_bf16 v[204:207], v[68:71], a[224:227], v[204:207]\n"
        "v_cvt_pk_bf16_f32 v105, v106, v107\n"
        "v_mul_f32 v108, v101, v108\n"
        "v_mfma_f32_16x16x32_bf16 v[176:179], v[64:67], a[224:227], v[176:179]\n"
        "ds_write_b32 v245, v104 offset:256\n"
        "v_mfma_f32_16x16x32_bf16 v[176:179], v[232:235], a[220:223], v[176:179]\n"
        "s_waitcnt lgkmcnt(9)\n"
        "ds_write_b32 v245, v105 offset:260\n"
        "v_mfma_f32_16x16x32_bf16 v[180:183], v[232:235], a[228:231], v[180:183]\n"
        "s_waitcnt lgkmcnt(9)\n"
        "ds_read_b128 a[220:223], v254 offset:43088\n"
        "v_mul_f32 v109, v101, v109\n"
        "v_mfma_f32_16x16x32_bf16 v[180:183], v[64:67], a[232:235], v[180:183]\n"
        "v_maximum3_f32 v253, v253, v100, v101\n"
        "v_mul_f32 v110, v101, v110\n"
        "v_mfma_f32_16x16x32_bf16 v[212:215], v[68:71], a[232:235], v[212:215]\n"
        "v_mul_f32 v111, v101, v111\n"
        "v_cvt_pk_bf16_f32 v108, v108, v109\n"
        "v_mfma_f32_16x16x32_bf16 v[212:215], v[236:239], a[228:231], v[212:215]\n"
        "s_waitcnt lgkmcnt(7)\n"
        "ds_read_b128 v[156:159], v247 offset:16384\n"
        "v_cvt_pk_bf16_f32 v109, v110, v111\n"
        "v_mfma_f32_16x16x32_bf16 v[216:219], v[236:239], a[236:239], v[216:219]\n"
        "s_waitcnt lgkmcnt(7)\n"
        "ds_write_b32 v245, v108 offset:264\n"
        "v_mfma_f32_16x16x32_bf16 v[216:219], v[68:71], a[240:243], v[216:219]\n"
        "ds_write_b32 v245, v109 offset:268\n"
        "v_mfma_f32_16x16x32_bf16 v[184:187], v[64:67], a[240:243], v[184:187]\n"
        "v_mul_f32 v112, v101, v112\n"
        "v_mul_f32 v113, v101, v113\n"
        "v_mfma_f32_16x16x32_bf16 v[184:187], v[232:235], a[236:239], v[184:187]\n"
        "v_mul_f32 v114, v101, v114\n"
        "v_mul_f32 v115, v101, v115\n"
        "v_smfmac_f32_16x16x64_bf16 v[116:119], a[252:255], v[80:87], v255\n"
        "ds_read_b128 v[164:167], v247 offset:20480\n"
        "v_cvt_pk_bf16_f32 v112, v112, v113\n"
        "v_mfma_f32_16x16x32_bf16 v[120:123], v[128:131], v[80:83], 0\n"
        "ds_read_b128 v[160:163], v247 offset:16640\n"
        "v_cvt_pk_bf16_f32 v113, v114, v115\n"
        "v_mfma_f32_16x16x32_bf16 v[120:123], v[132:135], v[84:87], v[120:123]\n"
        "ds_write_b32 v246, v112 offset:256\n"
        "v_mfma_f32_16x16x32_bf16 v[124:127], v[140:143], v[84:87], 0\n"
        "ds_write_b32 v246, v113 offset:260\n"
        "v_mfma_f32_16x16x32_bf16 v[124:127], v[136:139], v[80:83], v[124:127]\n"
        "ds_read_b128 a[224:227], v248 offset:12288\n"
        "v_add_f32 v116, v116, v117\n"
        "v_mfma_f32_16x16x32_bf16 v[96:99], v[144:147], v[80:83], 0\n"
        "ds_read_b128 v[152:155], v247 offset:20736\n"
        "v_mfma_f32_16x16x32_bf16 v[96:99], v[148:151], v[84:87], v[96:99]\n"
        "s_waitcnt lgkmcnt(14)\n"
        "ds_read_b128 a[228:231], v248 offset:49152\n"
        "v_mfma_f32_16x16x32_bf16 v[192:195], v[232:235], a[244:247], v[192:195]\n"
        "s_waitcnt lgkmcnt(14)\n"
        "v_rcp_f32 v117, v116\n"
        "v_mfma_f32_16x16x32_bf16 v[192:195], v[64:67], a[248:251], v[192:195]\n"
        "ds_read_b128 a[232:235], v254 offset:12304\n"
        "v_mul_f32 v120, v117, v120\n"
        "v_mfma_f32_16x16x32_bf16 v[220:223], v[68:71], a[248:251], v[220:223]\n"
        "ds_read_b128 a[236:239], v254 offset:49168\n"
        "v_mul_f32 v121, v117, v121\n"
        "v_mfma_f32_16x16x32_bf16 v[220:223], v[236:239], a[244:247], v[220:223]\n"
        "s_waitcnt lgkmcnt(15)\n"
        "ds_read_b128 a[240:243], v248 offset:12320\n"
        "v_mul_f32 v122, v117, v122\n"
        "v_mfma_f32_16x16x32_bf16 v[224:227], v[236:239], a[216:219], v[224:227]\n"
        "s_waitcnt lgkmcnt(13)\n"
        "ds_read_b128 a[244:247], v248 offset:49184\n"
        "v_mul_f32 v123, v117, v123\n"
        "v_mfma_f32_16x16x32_bf16 v[224:227], v[68:71], a[220:223], v[224:227]\n"
        "ds_read_b128 a[248:251], v254 offset:12336\n"
        "v_cvt_pk_bf16_f32 v120, v120, v121\n"
        "v_mfma_f32_16x16x32_bf16 v[196:199], v[64:67], a[220:223], v[196:199]\n"
        "ds_read_b128 a[220:223], v248 offset:12352\n"
        "v_cvt_pk_bf16_f32 v121, v122, v123\n"
        "v_mfma_f32_16x16x32_bf16 v[196:199], v[232:235], a[216:219], v[196:199]\n"
        "s_waitcnt lgkmcnt(8)\n"
        "ds_write_b32 v245, v120 offset:512\n"
        "v_mfma_f32_16x16x32_bf16 v[172:175], v[156:159], a[224:227], v[172:175]\n"
        "s_waitcnt lgkmcnt(7)\n"
        "ds_read_b128 a[216:219], v254 offset:49200\n"
        "v_mul_f32 v124, v117, v124\n"
        "v_mfma_f32_16x16x32_bf16 v[172:175], v[164:167], a[228:231], v[172:175]\n"
        "ds_write_b32 v245, v121 offset:516\n"
        "v_mfma_f32_16x16x32_bf16 v[200:203], v[152:155], a[228:231], v[200:203]\n"
        "ds_read_b128 a[228:231], v254 offset:12368\n"
        "v_mul_f32 v125, v117, v125\n"
        "v_mfma_f32_16x16x32_bf16 v[200:203], v[160:163], a[224:227], v[200:203]\n"
        "s_waitcnt lgkmcnt(9)\n"
        "ds_read_b128 a[224:227], v248 offset:49216\n"
        "v_maximum3_f32 v253, v253, v116, v117\n"
        "v_mfma_f32_16x16x32_bf16 v[204:207], v[160:163], a[232:235], v[204:207]\n"
        "s_waitcnt lgkmcnt(9)\n"
        "v_mul_f32 v126, v117, v126\n"
        "v_mul_f32 v127, v117, v127\n"
        "v_mfma_f32_16x16x32_bf16 v[204:207], v[152:155], a[236:239], v[204:207]\n"
        "v_cvt_pk_bf16_f32 v124, v124, v125\n"
        "v_cvt_pk_bf16_f32 v125, v126, v127\n"
        "v_mfma_f32_16x16x32_bf16 v[176:179], v[164:167], a[236:239], v[176:179]\n"
        "ds_write_b32 v245, v124 offset:520\n"
        "v_mfma_f32_16x16x32_bf16 v[176:179], v[156:159], a[232:235], v[176:179]\n"
        "s_waitcnt lgkmcnt(9)\n"
        "ds_write_b32 v245, v125 offset:524\n"
        "v_mfma_f32_16x16x32_bf16 v[180:183], v[156:159], a[240:243], v[180:183]\n"
        "s_waitcnt lgkmcnt(9)\n"
        "ds_read_b128 a[232:235], v254 offset:49232\n"
        "v_mul_f32 v96, v117, v96\n"
        "v_mfma_f32_16x16x32_bf16 v[180:183], v[164:167], a[244:247], v[180:183]\n"
        "v_mul_f32 v97, v117, v97\n"
        "v_mul_f32 v98, v117, v98\n"
        "v_mfma_f32_16x16x32_bf16 v[212:215], v[152:155], a[244:247], v[212:215]\n"
        "v_mul_f32 v99, v117, v99\n"
        "v_cvt_pk_bf16_f32 v96, v96, v97\n"
        "v_mfma_f32_16x16x32_bf16 v[212:215], v[160:163], a[240:243], v[212:215]\n"
        "s_waitcnt lgkmcnt(9)\n"
        "v_cvt_pk_bf16_f32 v97, v98, v99\n"
        "v_add_u32 v247, s31, v247\n"
        "v_mfma_f32_16x16x32_bf16 v[216:219], v[160:163], a[248:251], v[216:219]\n"
        "s_waitcnt lgkmcnt(6)\n"
        "v_mov_b64 v[80:81], 0\n"
        "v_mfma_f32_16x16x32_bf16 v[216:219], v[152:155], a[216:219], v[216:219]\n"
        "ds_write_b32 v246, v96 offset:512\n"
        "v_mfma_f32_16x16x32_bf16 v[184:187], v[164:167], a[216:219], v[184:187]\n"
        "s_waitcnt vmcnt(0)\n"
        "ds_write_b32 v246, v97 offset:516\n"
        "v_mfma_f32_16x16x32_bf16 v[184:187], v[156:159], a[248:251], v[184:187]\n"
        "s_barrier\n"
        "v_mfma_f32_16x16x32_bf16 v[192:195], v[156:159], a[220:223], v[192:195]\n"
        "s_waitcnt lgkmcnt(5)\n"
        "ds_read_b32 v108, v245 offset:768\n"
        "v_mfma_f32_16x16x32_bf16 v[192:195], v[164:167], a[224:227], v[192:195]\n"
        "ds_read_b128 a[236:239], v240 offset:0\n"
        "v_mfma_f32_16x16x32_bf16 v[220:223], v[152:155], a[224:227], v[220:223]\n"
        "ds_read_b128 a[240:243], v241 offset:0\n"
        "v_mfma_f32_16x16x32_bf16 v[220:223], v[160:163], a[220:223], v[220:223]\n"
        "ds_read_b128 a[244:247], v240 offset:128\n"
        "v_mfma_f32_16x16x32_bf16 v[224:227], v[160:163], a[228:231], v[224:227]\n"
        "s_waitcnt lgkmcnt(6)\n"
        "ds_read_b128 a[248:251], v241 offset:128\n"
        "v_mfma_f32_16x16x32_bf16 v[224:227], v[152:155], a[232:235], v[224:227]\n"
        "ds_read_b128 a[216:219], v240 offset:256\n"
        "v_mfma_f32_16x16x32_bf16 v[196:199], v[164:167], a[232:235], v[196:199]\n"
        "ds_read_b128 a[220:223], v241 offset:256\n"
        "v_mfma_f32_16x16x32_bf16 v[196:199], v[156:159], a[228:231], v[196:199]\n"
        "v_smfmac_f32_16x16x64_bf16 v[80:83], a[252:255], v[88:95], v255\n"
        "s_waitcnt lgkmcnt(6)\n"
        "v_readfirstlane_b32 s59, v108\n"
        "v_mfma_f32_16x16x32_bf16 v[84:87], v[128:131], v[88:91], 0\n"
        "v_mfma_f32_16x16x32_bf16 v[84:87], v[132:135], v[92:95], v[84:87]\n"
        "s_min_u32 s58, s63, s59\n"
        "s_cmp_lt_u32 s58, s49\n"
        "v_mfma_f32_16x16x32_bf16 v[232:235], v[140:143], v[92:95], 0\n"
        "s_cbranch_scc1 10b\n"
        "v_mfma_f32_16x16x32_bf16 v[232:235], v[136:139], v[88:91], v[232:235]\n"
        "v_mfma_f32_16x16x32_bf16 v[236:239], v[144:147], v[88:91], 0\n"
        "v_mfma_f32_16x16x32_bf16 v[236:239], v[148:151], v[92:95], v[236:239]\n"
        "20:\n"
        "s_waitcnt lgkmcnt(0)\n"
        "v_mfma_f32_16x16x32_bf16 v[96:99], a[72:75], a[236:239], 0\n"
        "v_and_b32 v242, 60, v250\n"
        "v_add3_u32 v242, v250, v242, s26\n"
        "v_mfma_f32_16x16x32_bf16 v[96:99], a[76:79], a[240:243], v[96:99]\n"
        "v_cvt_pk_bf16_f32 v108, v172, v176\n"
        "v_cvt_pk_bf16_f32 v109, v180, v184\n"
        "v_mfma_f32_16x16x32_bf16 v[96:99], a[80:83], a[244:247], v[96:99]\n"
        "v_cvt_pk_bf16_f32 v110, v192, v196\n"
        "v_add_f32 v80, v80, v81\n"
        "v_mfma_f32_16x16x32_bf16 v[96:99], a[84:87], a[248:251], v[96:99]\n"
        "45:\n"
        "s_cbranch_execz 40b\n"
        "buffer_store_dwordx2 v[108:109], v242, s[16:19], s38 offen offset:0 sc1\n"
        "v_mfma_f32_16x16x32_bf16 v[96:99], a[88:91], a[216:219], v[96:99]\n"
        "v_rcp_f32 v81, v80\n"
        "v_mfma_f32_16x16x32_bf16 v[96:99], a[92:95], a[220:223], v[96:99]\n"
        "v_cvt_pk_bf16_f32 v112, v173, v177\n"
        "v_cvt_pk_bf16_f32 v113, v181, v185\n"
        "v_mfma_f32_16x16x32_bf16 v[100:103], a[116:119], a[220:223], 0\n"
        "v_mul_f32 v84, v81, v84\n"
        "v_mul_f32 v85, v81, v85\n"
        "v_mfma_f32_16x16x32_bf16 v[100:103], a[112:115], a[216:219], v[100:103]\n"
        "v_mul_f32 v86, v81, v86\n"
        "v_mul_f32 v87, v81, v87\n"
        "v_mfma_f32_16x16x32_bf16 v[100:103], a[108:111], a[248:251], v[100:103]\n"
        "buffer_store_dword v110, v250, s[16:19], s38 offen offset:0 sc1\n"
        "v_mfma_f32_16x16x32_bf16 v[100:103], a[104:107], a[244:247], v[100:103]\n"
        "s_sub_u32 s31, s29, s30\n"
        "v_cvt_pk_bf16_f32 v152, v96, v97\n"
        "v_cvt_pk_bf16_f32 v153, v98, v99\n"
        "v_mfma_f32_16x16x32_bf16 v[100:103], a[100:103], a[240:243], v[100:103]\n"
        "v_cvt_pk_bf16_f32 v114, v193, v197\n"
        "v_cvt_pk_bf16_f32 v84, v84, v85\n"
        "v_mfma_f32_16x16x32_bf16 v[100:103], a[96:99], a[236:239], v[100:103]\n"
        "ds_write_b32 v245, v84 offset:768\n"
        "v_mfma_f32_16x16x32_bf16 v[104:107], a[120:123], a[236:239], 0\n"
        "v_cvt_pk_bf16_f32 v85, v86, v87\n"
        "v_mul_f32 v232, v81, v232\n"
        "v_mfma_f32_16x16x32_bf16 v[104:107], a[124:127], a[240:243], v[104:107]\n"
        "buffer_store_dwordx2 v[112:113], v242, s[16:19], s38 offen offset:384 sc1\n"
        "v_mfma_f32_16x16x32_bf16 v[104:107], a[128:131], a[244:247], v[104:107]\n"
        "ds_write_b32 v245, v85 offset:772\n"
        "v_mfma_f32_16x16x32_bf16 v[104:107], a[132:135], a[248:251], v[104:107]\n"
        "v_cvt_pk_bf16_f32 v154, v100, v101\n"
        "v_cvt_pk_bf16_f32 v155, v102, v103\n"
        "v_mfma_f32_16x16x32_bf16 v[104:107], a[136:139], a[216:219], v[104:107]\n"
        "v_cvt_pk_bf16_f32 v108, v174, v178\n"
        "v_cvt_pk_bf16_f32 v109, v182, v186\n"
        "v_mfma_f32_16x16x32_bf16 v[104:107], a[140:143], a[220:223], v[104:107]\n"
        "v_mul_f32 v233, v81, v233\n"
        "v_maximum3_f32 v253, v253, v80, v81\n"
        "v_mfma_f32_16x16x32_bf16 v[96:99], a[20:23], a[220:223], 0\n"
        "buffer_store_dword v114, v250, s[16:19], s38 offen offset:384 sc1\n"
        "v_mfma_f32_16x16x32_bf16 v[96:99], a[16:19], a[216:219], v[96:99]\n"
        "v_cvt_pk_bf16_f32 v110, v194, v198\n"
        "v_mul_f32 v234, v81, v234\n"
        "v_mfma_f32_16x16x32_bf16 v[96:99], a[12:15], a[248:251], v[96:99]\n"
        "v_cvt_pk_bf16_f32 v168, v104, v105\n"
        "v_cvt_pk_bf16_f32 v169, v106, v107\n"
        "v_mfma_f32_16x16x32_bf16 v[96:99], a[8:11], a[244:247], v[96:99]\n"
        "v_mul_f32 v235, v81, v235\n"
        "v_cvt_pk_bf16_f32 v232, v232, v233\n"
        "v_mfma_f32_16x16x32_bf16 v[96:99], a[4:7], a[240:243], v[96:99]\n"
        "ds_write_b32 v245, v232 offset:776\n"
        "v_mfma_f32_16x16x32_bf16 v[96:99], a[0:3], a[236:239], v[96:99]\n"
        "buffer_store_dwordx2 v[108:109], v242, s[16:19], s38 offen offset:768 sc1\n"
        "v_mfma_f32_16x16x32_bf16 v[100:103], a[24:27], a[236:239], 0\n"
        "v_cvt_pk_bf16_f32 v112, v175, v179\n"
        "v_cvt_pk_bf16_f32 v113, v183, v187\n"
        "v_mfma_f32_16x16x32_bf16 v[100:103], a[28:31], a[240:243], v[100:103]\n"
        "v_cvt_pk_bf16_f32 v233, v234, v235\n"
        "v_xor_b32 v251, 0x2000, v251\n"
        "v_mfma_f32_16x16x32_bf16 v[100:103], a[32:35], a[244:247], v[100:103]\n"
        "v_mul_f32 v96, s50, v96\n"
        "v_mul_f32 v97, s50, v97\n"
        "v_mfma_f32_16x16x32_bf16 v[100:103], a[36:39], a[248:251], v[100:103]\n"
        "v_mul_f32 v98, s50, v98\n"
        "v_mul_f32 v99, s50, v99\n"
        "v_mfma_f32_16x16x32_bf16 v[100:103], a[40:43], a[216:219], v[100:103]\n"
        "buffer_store_dword v110, v250, s[16:19], s38 offen offset:768 sc1\n"
        "v_mfma_f32_16x16x32_bf16 v[100:103], a[44:47], a[220:223], v[100:103]\n"
        "v_cvt_pk_bf16_f32 v156, v96, v97\n"
        "v_cvt_pk_bf16_f32 v157, v98, v99\n"
        "v_mfma_f32_16x16x32_bf16 v[104:107], a[68:71], a[220:223], 0\n"
        "ds_write_b32 v245, v233 offset:780\n"
        "v_mfma_f32_16x16x32_bf16 v[104:107], a[64:67], a[216:219], v[104:107]\n"
        "v_cvt_pk_bf16_f32 v114, v195, v199\n"
        "v_xor_b32 v252, 0x2000, v252\n"
        "v_mfma_f32_16x16x32_bf16 v[104:107], a[60:63], a[248:251], v[104:107]\n"
        "v_mul_f32 v100, s50, v100\n"
        "v_mul_f32 v101, s50, v101\n"
        "v_mfma_f32_16x16x32_bf16 v[104:107], a[56:59], a[244:247], v[104:107]\n"
        "buffer_store_dwordx2 v[112:113], v242, s[16:19], s38 offen offset:1152 sc1\n"
        "v_mfma_f32_16x16x32_bf16 v[104:107], a[52:55], a[240:243], v[104:107]\n"
        "v_mul_f32 v102, s50, v102\n"
        "v_mul_f32 v103, s50, v103\n"
        "v_mfma_f32_16x16x32_bf16 v[104:107], a[48:51], a[236:239], v[104:107]\n"
        "v_cvt_pk_bf16_f32 v158, v100, v101\n"
        "v_cvt_pk_bf16_f32 v159, v102, v103\n"
        "v_mfma_f32_16x16x32_bf16 v[96:99], a[236:239], a[144:147], 0\n"
        "ds_read_b128 a[224:227], v240 offset:1536\n"
        "v_cvt_pk_bf16_f32 v108, v200, v204\n"
        "v_mfma_f32_16x16x32_bf16 v[96:99], a[240:243], a[148:151], v[96:99]\n"
        "ds_read_b128 a[228:231], v241 offset:1536\n"
        "v_cvt_pk_bf16_f32 v109, v212, v216\n"
        "v_mfma_f32_16x16x32_bf16 v[96:99], a[244:247], a[152:155], v[96:99]\n"
        "buffer_store_dword v114, v250, s[16:19], s38 offen offset:1152 sc1\n"
        "v_mfma_f32_16x16x32_bf16 v[96:99], a[248:251], a[156:159], v[96:99]\n"
        "v_mul_f32 v104, s50, v104\n"
        "v_mul_f32 v105, s50, v105\n"
        "v_mfma_f32_16x16x32_bf16 v[96:99], a[216:219], a[160:163], v[96:99]\n"
        "v_mul_f32 v106, s50, v106\n"
        "v_mul_f32 v107, s50, v107\n"
        "v_mfma_f32_16x16x32_bf16 v[96:99], a[220:223], a[164:167], v[96:99]\n"
        "v_cvt_pk_bf16_f32 v160, v104, v105\n"
        "v_cvt_pk_bf16_f32 v161, v106, v107\n"
        "v_mfma_f32_16x16x32_bf16 v[100:103], a[220:223], a[188:191], 0\n"
        "ds_read_b128 a[232:235], v240 offset:1664\n"
        "v_mov_b64 v[166:167], v[160:161]\n"
        "v_mfma_f32_16x16x32_bf16 v[100:103], a[216:219], a[184:187], v[100:103]\n"
        "buffer_store_dwordx2 v[108:109], v242, s[16:19], s39 offen offset:0 sc1\n"
        "v_mfma_f32_16x16x32_bf16 v[100:103], a[248:251], a[180:183], v[100:103]\n"
        "v_cvt_pk_bf16_f32 v128, v96, v97\n"
        "v_cvt_pk_bf16_f32 v129, v98, v99\n"
        "v_mfma_f32_16x16x32_bf16 v[100:103], a[244:247], a[176:179], v[100:103]\n"
        "v_cvt_pk_bf16_f32 v110, v220, v224\n"
        "v_cvt_pk_bf16_f32 v112, v201, v205\n"
        "v_mfma_f32_16x16x32_bf16 v[100:103], a[240:243], a[172:175], v[100:103]\n"
        "v_cvt_pk_bf16_f32 v113, v213, v217\n"
        "v_mul_f32 v236, v81, v236\n"
        "v_mfma_f32_16x16x32_bf16 v[100:103], a[236:239], a[168:171], v[100:103]\n"
        "v_mul_f32 v237, v81, v237\n"
        "v_mul_f32 v238, v81, v238\n"
        "v_mfma_f32_16x16x32_bf16 v[104:107], a[236:239], a[192:195], 0\n"
        "buffer_store_dword v110, v250, s[16:19], s39 offen offset:0 sc1\n"
        "v_mfma_f32_16x16x32_bf16 v[104:107], a[240:243], a[196:199], v[104:107]\n"
        "ds_read_b128 a[236:239], v241 offset:1664\n"
        "ds_read_b128 a[240:243], v240 offset:1792\n"
        "v_mfma_f32_16x16x32_bf16 v[104:107], a[244:247], a[200:203], v[104:107]\n"
        "ds_read_b128 a[244:247], v241 offset:1792\n"
        "v_cvt_pk_bf16_f32 v136, v100, v101\n"
        "v_mfma_f32_16x16x32_bf16 v[104:107], a[248:251], a[204:207], v[104:107]\n"
        "v_cvt_pk_bf16_f32 v137, v102, v103\n"
        "v_mul_f32 v239, v81, v239\n"
        "v_mfma_f32_16x16x32_bf16 v[104:107], a[216:219], a[208:211], v[104:107]\n"
        "v_cvt_pk_bf16_f32 v236, v236, v237\n"
        "v_cvt_pk_bf16_f32 v237, v238, v239\n"
        "v_mfma_f32_16x16x32_bf16 v[104:107], a[220:223], a[212:215], v[104:107]\n"
        "s_waitcnt lgkmcnt(5)\n"
        "buffer_store_dwordx2 v[112:113], v242, s[16:19], s39 offen offset:384 sc1\n"
        "v_mfma_f32_16x16x32_bf16 v[96:99], a[72:75], a[224:227], 0\n"
        "s_waitcnt lgkmcnt(4)\n"
        "ds_write_b32 v246, v236 offset:768\n"
        "v_mfma_f32_16x16x32_bf16 v[96:99], a[76:79], a[228:231], v[96:99]\n"
        "s_waitcnt lgkmcnt(4)\n"
        "ds_write_b32 v246, v237 offset:772\n"
        "v_mfma_f32_16x16x32_bf16 v[96:99], a[80:83], a[232:235], v[96:99]\n"
        "s_waitcnt lgkmcnt(4)\n"
        "ds_read_b128 v[162:165], v251 offset:0\n"
        "v_cvt_pk_bf16_f32 v114, v221, v225\n"
        "v_mfma_f32_16x16x32_bf16 v[96:99], a[84:87], a[236:239], v[96:99]\n"
        "s_waitcnt lgkmcnt(4)\n"
        "v_cvt_pk_bf16_f32 v144, v104, v105\n"
        "v_cvt_pk_bf16_f32 v145, v106, v107\n"
        "v_mfma_f32_16x16x32_bf16 v[96:99], a[88:91], a[240:243], v[96:99]\n"
        "s_waitcnt lgkmcnt(3)\n"
        "buffer_store_dword v114, v250, s[16:19], s39 offen offset:384 sc1\n"
        "v_mfma_f32_16x16x32_bf16 v[96:99], a[92:95], a[244:247], v[96:99]\n"
        "v_cvt_pk_bf16_f32 v108, v202, v206\n"
        "v_cvt_pk_bf16_f32 v109, v214, v218\n"
        "v_mfma_f32_16x16x32_bf16 v[100:103], a[116:119], a[244:247], 0\n"
        "v_cvt_pk_bf16_f32 v110, v222, v226\n"
        "v_add_u32 v245, s31, v245\n"
        "v_mfma_f32_16x16x32_bf16 v[100:103], a[112:115], a[240:243], v[100:103]\n"
        "v_add_u32 v246, s31, v246\n"
        "v_mfma_f32_16x16x32_bf16 v[100:103], a[108:111], a[236:239], v[100:103]\n"
        "v_cvt_pk_bf16_f32 v172, v96, v97\n"
        "v_cvt_pk_bf16_f32 v173, v98, v99\n"
        "v_mfma_f32_16x16x32_bf16 v[100:103], a[104:107], a[232:235], v[100:103]\n"
        "buffer_store_dwordx2 v[108:109], v242, s[16:19], s39 offen offset:768 sc1\n"
        "v_mfma_f32_16x16x32_bf16 v[100:103], a[100:103], a[228:231], v[100:103]\n"
        "v_cvt_pk_bf16_f32 v112, v203, v207\n"
        "v_cvt_pk_bf16_f32 v113, v215, v219\n"
        "v_mfma_f32_16x16x32_bf16 v[100:103], a[96:99], a[224:227], v[100:103]\n"
        "s_nop 2\n"
        "v_mfma_f32_16x16x32_bf16 v[104:107], a[120:123], a[224:227], 0\n"
        "s_nop 2\n"
        "v_mfma_f32_16x16x32_bf16 v[104:107], a[124:127], a[228:231], v[104:107]\n"
        "v_cvt_pk_bf16_f32 v174, v100, v101\n"
        "v_cvt_pk_bf16_f32 v175, v102, v103\n"
        "v_mfma_f32_16x16x32_bf16 v[104:107], a[128:131], a[232:235], v[104:107]\n"
        "buffer_store_dword v110, v250, s[16:19], s39 offen offset:768 sc1\n"
        "v_mfma_f32_16x16x32_bf16 v[104:107], a[132:135], a[236:239], v[104:107]\n"
        "v_cvt_pk_bf16_f32 v114, v223, v227\n"
        "v_mfma_f32_16x16x32_bf16 v[104:107], a[136:139], a[240:243], v[104:107]\n"
        "v_mfma_f32_16x16x32_bf16 v[104:107], a[140:143], a[244:247], v[104:107]\n"
        "s_nop 2\n"
        "v_mfma_f32_16x16x32_bf16 v[96:99], a[20:23], a[244:247], 0\n"
        "ds_read_b128 a[248:251], v240 offset:12288\n"
        "s_nop 0\n"
        "v_mfma_f32_16x16x32_bf16 v[96:99], a[16:19], a[240:243], v[96:99]\n"
        "buffer_store_dwordx2 v[112:113], v242, s[16:19], s39 offen offset:1152 sc1\n"
        "v_mfma_f32_16x16x32_bf16 v[96:99], a[12:15], a[236:239], v[96:99]\n"
        "ds_read_b128 a[216:219], v241 offset:12288\n"
        "v_cvt_pk_bf16_f32 v190, v104, v105\n"
        "v_mfma_f32_16x16x32_bf16 v[96:99], a[8:11], a[232:235], v[96:99]\n"
        "ds_read_b128 a[220:223], v240 offset:12416\n"
        "v_cvt_pk_bf16_f32 v191, v106, v107\n"
        "v_mfma_f32_16x16x32_bf16 v[96:99], a[4:7], a[228:231], v[96:99]\n"
        "ds_read_b128 v[182:185], v251 offset:512\n"
        "v_mfma_f32_16x16x32_bf16 v[96:99], a[0:3], a[224:227], v[96:99]\n"
        "s_nop 2\n"
        "v_mfma_f32_16x16x32_bf16 v[0:3], v[152:155], v[156:159], v[0:3]\n"
        "s_waitcnt lgkmcnt(4)\n"
        "buffer_store_dword v114, v250, s[16:19], s39 offen offset:1152 sc1\n"
        "v_mfma_f32_16x16x32_bf16 v[0:3], v[168:171], v[160:163], v[0:3]\n"
        "s_nop 0\n"
        "v_mfma_f32_16x16x32_bf16 v[4:7], v[172:175], v[156:159], v[4:7]\n"
        "v_mul_f32 v96, s50, v96\n"
        "v_mul_f32 v97, s50, v97\n"
        "v_mfma_f32_16x16x32_bf16 v[4:7], v[188:191], v[164:167], v[4:7]\n"
        "v_mul_f32 v98, s50, v98\n"
        "v_mul_f32 v99, s50, v99\n"
        "v_mfma_f32_16x16x32_bf16 v[100:103], a[24:27], a[224:227], 0\n"
        "v_cvt_pk_bf16_f32 v176, v96, v97\n"
        "v_cvt_pk_bf16_f32 v177, v98, v99\n"
        "v_mfma_f32_16x16x32_bf16 v[100:103], a[28:31], a[228:231], v[100:103]\n"
        "v_exp_f32 v0, v0\n"
        "v_mfma_f32_16x16x32_bf16 v[100:103], a[32:35], a[232:235], v[100:103]\n"
        "v_exp_f32 v1, v1\n"
        "v_mfma_f32_16x16x32_bf16 v[100:103], a[36:39], a[236:239], v[100:103]\n"
        "v_exp_f32 v2, v2\n"
        "v_mfma_f32_16x16x32_bf16 v[100:103], a[40:43], a[240:243], v[100:103]\n"
        "v_exp_f32 v3, v3\n"
        "v_mfma_f32_16x16x32_bf16 v[100:103], a[44:47], a[244:247], v[100:103]\n"
        "v_exp_f32 v4, v4\n"
        "v_mfma_f32_16x16x32_bf16 v[104:107], a[68:71], a[244:247], 0\n"
        "ds_read_b128 a[244:247], v241 offset:12416\n"
        "v_cvt_pk_bf16_f32 v64, v0, v1\n"
        "v_mfma_f32_16x16x32_bf16 v[104:107], a[64:67], a[240:243], v[104:107]\n"
        "ds_read_b128 a[240:243], v240 offset:12544\n"
        "v_cvt_pk_bf16_f32 v65, v2, v3\n"
        "v_mfma_f32_16x16x32_bf16 v[104:107], a[60:63], a[236:239], v[104:107]\n"
        "s_barrier\n"
        "ds_read_b128 a[236:239], v241 offset:12544\n"
        "v_mul_f32 v100, s50, v100\n"
        "v_mfma_f32_16x16x32_bf16 v[104:107], a[56:59], a[232:235], v[104:107]\n"
        "v_mul_f32 v101, s50, v101\n"
        "v_mul_f32 v102, s50, v102\n"
        "v_mfma_f32_16x16x32_bf16 v[104:107], a[52:55], a[228:231], v[104:107]\n"
        "v_mul_f32 v103, s50, v103\n"
        "v_cvt_pk_bf16_f32 v178, v100, v101\n"
        "v_mfma_f32_16x16x32_bf16 v[104:107], a[48:51], a[224:227], v[104:107]\n"
        "s_waitcnt lgkmcnt(6)\n"
        "v_cvt_pk_bf16_f32 v179, v102, v103\n"
        "v_mfma_f32_16x16x32_bf16 v[96:99], a[72:75], a[248:251], 0\n"
        "s_waitcnt lgkmcnt(5)\n"
        "v_exp_f32 v5, v5\n"
        "v_mfma_f32_16x16x32_bf16 v[96:99], a[76:79], a[216:219], v[96:99]\n"
        "s_waitcnt lgkmcnt(4)\n"
        "v_exp_f32 v6, v6\n"
        "v_mfma_f32_16x16x32_bf16 v[96:99], a[80:83], a[220:223], v[96:99]\n"
        "s_waitcnt lgkmcnt(2)\n"
        "v_exp_f32 v7, v7\n"
        "v_mfma_f32_16x16x32_bf16 v[96:99], a[84:87], a[244:247], v[96:99]\n"
        "s_waitcnt lgkmcnt(1)\n"
        "v_mul_f32 v104, s50, v104\n"
        "v_mul_f32 v105, s50, v105\n"
        "v_mfma_f32_16x16x32_bf16 v[96:99], a[88:91], a[240:243], v[96:99]\n"
        "s_waitcnt lgkmcnt(0)\n"
        "v_mul_f32 v106, s50, v106\n"
        "v_mul_f32 v107, s50, v107\n"
        "v_mfma_f32_16x16x32_bf16 v[96:99], a[92:95], a[236:239], v[96:99]\n"
        "v_cvt_pk_bf16_f32 v180, v104, v105\n"
        "v_cvt_pk_bf16_f32 v181, v106, v107\n"
        "v_mfma_f32_16x16x32_bf16 v[16:19], v[152:155], v[176:179], v[16:19]\n"
        "ds_read_b128 v[162:165], v252 offset:0\n"
        "v_mov_b64 v[186:187], v[180:181]\n"
        "v_mfma_f32_16x16x32_bf16 v[16:19], v[168:171], v[180:183], v[16:19]\n"
        "s_lshr_b32 s46, s56, 6\n"
        "v_cvt_pk_bf16_f32 v66, v4, v5\n"
        "v_cvt_pk_bf16_f32 v192, v96, v97\n"
        "v_mfma_f32_16x16x32_bf16 v[20:23], v[172:175], v[176:179], v[20:23]\n"
        "s_mul_i32 s46, s46, 1572864\n"
        "v_cvt_pk_bf16_f32 v193, v98, v99\n"
        "v_cvt_pk_bf16_f32 v67, v6, v7\n"
        "v_mfma_f32_16x16x32_bf16 v[20:23], v[188:191], v[184:187], v[20:23]\n"
        "s_add_u32 s46, s46, s37\n"
        "v_exp_f32 v16, v16\n"
        "v_mfma_f32_16x16x32_bf16 v[100:103], a[116:119], a[236:239], 0\n"
        "s_cmp_eq_u32 s46, 100663296\n"
        "ds_read_b128 v[182:185], v252 offset:512\n"
        "v_add_u32 v243, s31, v247\n"
        "v_mfma_f32_16x16x32_bf16 v[100:103], a[112:115], a[240:243], v[100:103]\n"
        "s_cselect_b32 s46, 0, s46\n"
        "v_exp_f32 v17, v17\n"
        "v_mfma_f32_16x16x32_bf16 v[100:103], a[108:111], a[244:247], v[100:103]\n"
        "s_and_b32 s43, s56, 63\n"
        "v_exp_f32 v18, v18\n"
        "v_mfma_f32_16x16x32_bf16 v[100:103], a[104:107], a[220:223], v[100:103]\n"
        "s_mul_i32 s43, s43, 3072\n"
        "v_exp_f32 v19, v19\n"
        "v_mfma_f32_16x16x32_bf16 v[100:103], a[100:103], a[216:219], v[100:103]\n"
        "s_add_u32 s44, s43, 3072\n"
        "v_exp_f32 v20, v20\n"
        "v_mfma_f32_16x16x32_bf16 v[100:103], a[96:99], a[248:251], v[100:103]\n"
        "s_cmp_eq_u32 s44, 196608\n"
        "v_exp_f32 v21, v21\n"
        "v_mfma_f32_16x16x32_bf16 v[104:107], a[120:123], a[248:251], 0\n"
        "s_cselect_b32 s44, 0, s44\n"
        "v_exp_f32 v22, v22\n"
        "v_mfma_f32_16x16x32_bf16 v[104:107], a[124:127], a[216:219], v[104:107]\n"
        "s_add_u32 s39, s46, s44\n"
        "v_exp_f32 v23, v23\n"
        "v_mfma_f32_16x16x32_bf16 v[104:107], a[128:131], a[220:223], v[104:107]\n"
        "s_add_u32 s43, s43, s46\n"
        "v_cvt_pk_bf16_f32 v194, v100, v101\n"
        "v_cvt_pk_bf16_f32 v195, v102, v103\n"
        "v_mfma_f32_16x16x32_bf16 v[104:107], a[132:135], a[244:247], v[104:107]\n"
        "s_add_u32 s38, s43, 1536\n"
        "v_cvt_pk_bf16_f32 v72, v16, v17\n"
        "v_cvt_pk_bf16_f32 v73, v18, v19\n"
        "v_mfma_f32_16x16x32_bf16 v[104:107], a[136:139], a[240:243], v[104:107]\n"
        "s_lshl_b32 s29, s42, 8\n"
        "v_cvt_pk_bf16_f32 v74, v20, v21\n"
        "v_cvt_pk_bf16_f32 v75, v22, v23\n"
        "v_mfma_f32_16x16x32_bf16 v[104:107], a[140:143], a[236:239], v[104:107]\n"
        "s_add_u32 s29, s29, 147456\n"
        "v_mbcnt_lo_u32_b32 v244, -1, 0\n"
        "v_mbcnt_hi_u32_b32 v244, -1, v244\n"
        "v_mfma_f32_16x16x32_bf16 v[96:99], a[20:23], a[236:239], 0\n"
        "s_mov_b32 s47, 147456\n"
        "ds_read_b128 a[232:235], v240 offset:13824\n"
        "v_lshl_add_u32 v249, v244, 4, s47\n"
        "v_mfma_f32_16x16x32_bf16 v[96:99], a[16:19], a[240:243], v[96:99]\n"
        "ds_read_b128 a[228:231], v241 offset:13824\n"
        "v_cvt_pk_bf16_f32 v208, v104, v105\n"
        "v_mfma_f32_16x16x32_bf16 v[96:99], a[12:15], a[244:247], v[96:99]\n"
        "ds_read_b128 a[224:227], v240 offset:13952\n"
        "v_cvt_pk_bf16_f32 v209, v106, v107\n"
        "v_mfma_f32_16x16x32_bf16 v[96:99], a[8:11], a[220:223], v[96:99]\n"
        "v_lshl_add_u32 v244, v244, 2, s29\n"
        "v_mfma_f32_16x16x32_bf16 v[96:99], a[4:7], a[216:219], v[96:99]\n"
        "ds_read_b128 v[202:205], v251 offset:4096\n"
        "v_mfma_f32_16x16x32_bf16 v[96:99], a[0:3], a[248:251], v[96:99]\n"
        "s_nop 2\n"
        "v_mfma_f32_16x16x32_bf16 v[8:11], v[192:195], v[156:159], v[8:11]\n"
        "s_waitcnt lgkmcnt(5)\n"
        "s_nop 1\n"
        "v_mfma_f32_16x16x32_bf16 v[8:11], v[208:211], v[160:163], v[8:11]\n"
        "v_mfma_f32_16x16x32_bf16 v[24:27], v[192:195], v[176:179], v[24:27]\n"
        "s_waitcnt lgkmcnt(4)\n"
        "v_mul_f32 v96, s50, v96\n"
        "v_mul_f32 v97, s50, v97\n"
        "v_mfma_f32_16x16x32_bf16 v[24:27], v[208:211], v[180:183], v[24:27]\n"
        "v_mul_f32 v98, s50, v98\n"
        "v_mul_f32 v99, s50, v99\n"
        "v_mfma_f32_16x16x32_bf16 v[100:103], a[24:27], a[248:251], 0\n"
        "v_cvt_pk_bf16_f32 v196, v96, v97\n"
        "v_cvt_pk_bf16_f32 v197, v98, v99\n"
        "v_mfma_f32_16x16x32_bf16 v[100:103], a[28:31], a[216:219], v[100:103]\n"
        "v_exp_f32 v8, v8\n"
        "v_mfma_f32_16x16x32_bf16 v[100:103], a[32:35], a[220:223], v[100:103]\n"
        "v_exp_f32 v9, v9\n"
        "v_mfma_f32_16x16x32_bf16 v[100:103], a[36:39], a[244:247], v[100:103]\n"
        "v_exp_f32 v10, v10\n"
        "v_mfma_f32_16x16x32_bf16 v[100:103], a[40:43], a[240:243], v[100:103]\n"
        "s_lshr_b32 s46, s57, 6\n"
        "v_exp_f32 v11, v11\n"
        "v_mfma_f32_16x16x32_bf16 v[100:103], a[44:47], a[236:239], v[100:103]\n"
        "s_mul_i32 s46, s46, 1572864\n"
        "v_exp_f32 v24, v24\n"
        "v_mfma_f32_16x16x32_bf16 v[104:107], a[68:71], a[236:239], 0\n"
        "s_add_u32 s46, s46, s37\n"
        "ds_read_b128 a[236:239], v241 offset:13952\n"
        "v_cvt_pk_bf16_f32 v68, v8, v9\n"
        "v_mfma_f32_16x16x32_bf16 v[104:107], a[64:67], a[240:243], v[104:107]\n"
        "s_cmp_eq_u32 s46, 100663296\n"
        "ds_read_b128 a[240:243], v240 offset:14080\n"
        "v_mul_f32 v100, s50, v100\n"
        "v_mfma_f32_16x16x32_bf16 v[104:107], a[60:63], a[244:247], v[104:107]\n"
        "s_cselect_b32 s46, 0, s46\n"
        "ds_read_b128 a[244:247], v241 offset:14080\n"
        "v_mul_f32 v101, s50, v101\n"
        "v_mfma_f32_16x16x32_bf16 v[104:107], a[56:59], a[220:223], v[104:107]\n"
        "s_and_b32 s43, s57, 63\n"
        "v_mul_f32 v102, s50, v102\n"
        "v_mul_f32 v103, s50, v103\n"
        "v_mfma_f32_16x16x32_bf16 v[104:107], a[52:55], a[216:219], v[104:107]\n"
        "s_mul_i32 s43, s43, 3072\n"
        "v_cvt_pk_bf16_f32 v198, v100, v101\n"
        "v_cvt_pk_bf16_f32 v199, v102, v103\n"
        "v_mfma_f32_16x16x32_bf16 v[104:107], a[48:51], a[248:251], v[104:107]\n"
        "s_waitcnt lgkmcnt(6)\n"
        "v_exp_f32 v25, v25\n"
        "v_mfma_f32_16x16x32_bf16 v[96:99], a[72:75], a[232:235], 0\n"
        "s_waitcnt lgkmcnt(5)\n"
        "v_cvt_pk_bf16_f32 v69, v10, v11\n"
        "v_cvt_pk_bf16_f32 v76, v24, v25\n"
        "v_mfma_f32_16x16x32_bf16 v[96:99], a[76:79], a[228:231], v[96:99]\n"
        "s_waitcnt lgkmcnt(4)\n"
        "v_exp_f32 v26, v26\n"
        "v_mfma_f32_16x16x32_bf16 v[96:99], a[80:83], a[224:227], v[96:99]\n"
        "s_waitcnt lgkmcnt(2)\n"
        "v_exp_f32 v27, v27\n"
        "v_mfma_f32_16x16x32_bf16 v[96:99], a[84:87], a[236:239], v[96:99]\n"
        "s_waitcnt lgkmcnt(1)\n"
        "v_mul_f32 v104, s50, v104\n"
        "v_mul_f32 v105, s50, v105\n"
        "v_mfma_f32_16x16x32_bf16 v[96:99], a[88:91], a[240:243], v[96:99]\n"
        "s_waitcnt lgkmcnt(0)\n"
        "v_mul_f32 v106, s50, v106\n"
        "v_mul_f32 v107, s50, v107\n"
        "v_mfma_f32_16x16x32_bf16 v[96:99], a[92:95], a[244:247], v[96:99]\n"
        "s_add_u32 s44, s43, 3072\n"
        "v_cvt_pk_bf16_f32 v200, v104, v105\n"
        "v_cvt_pk_bf16_f32 v201, v106, v107\n"
        "v_mfma_f32_16x16x32_bf16 v[32:35], v[152:155], v[196:199], v[32:35]\n"
        "v_mov_b64 v[206:207], v[200:201]\n"
        "v_cvt_pk_bf16_f32 v77, v26, v27\n"
        "v_mfma_f32_16x16x32_bf16 v[32:35], v[168:171], v[200:203], v[32:35]\n"
        "s_cmp_eq_u32 s44, 196608\n"
        "v_cvt_pk_bf16_f32 v212, v96, v97\n"
        "v_cvt_pk_bf16_f32 v213, v98, v99\n"
        "v_mfma_f32_16x16x32_bf16 v[36:39], v[172:175], v[196:199], v[36:39]\n"
        "s_cselect_b32 s44, 0, s44\n"
        "v_mfma_f32_16x16x32_bf16 v[36:39], v[188:191], v[204:207], v[36:39]\n"
        "s_add_u32 s36, s46, s44\n"
        "ds_read_b128 v[202:205], v252 offset:4096\n"
        "v_mfma_f32_16x16x32_bf16 v[100:103], a[116:119], a[244:247], 0\n"
        "s_add_u32 s43, s43, s46\n"
        "v_exp_f32 v32, v32\n"
        "v_mfma_f32_16x16x32_bf16 v[100:103], a[112:115], a[240:243], v[100:103]\n"
        "s_add_u32 s35, s43, 1536\n"
        "v_exp_f32 v33, v33\n"
        "v_mfma_f32_16x16x32_bf16 v[100:103], a[108:111], a[236:239], v[100:103]\n"
        "v_exp_f32 v34, v34\n"
        "v_mfma_f32_16x16x32_bf16 v[100:103], a[104:107], a[224:227], v[100:103]\n"
        "ds_read_b128 a[72:75], v248 offset:0\n"
        "v_cvt_pk_bf16_f32 v80, v32, v33\n"
        "v_mfma_f32_16x16x32_bf16 v[100:103], a[100:103], a[228:231], v[100:103]\n"
        "v_exp_f32 v35, v35\n"
        "v_mfma_f32_16x16x32_bf16 v[100:103], a[96:99], a[232:235], v[100:103]\n"
        "ds_read_b128 a[76:79], v248 offset:6144\n"
        "v_cvt_pk_bf16_f32 v81, v34, v35\n"
        "v_mfma_f32_16x16x32_bf16 v[40:43], v[192:195], v[196:199], v[40:43]\n"
        "s_waitcnt lgkmcnt(2)\n"
        "v_exp_f32 v36, v36\n"
        "v_mfma_f32_16x16x32_bf16 v[40:43], v[208:211], v[200:203], v[40:43]\n"
        "v_exp_f32 v37, v37\n"
        "v_mfma_f32_16x16x32_bf16 v[104:107], a[120:123], a[232:235], 0\n"
        "v_exp_f32 v38, v38\n"
        "v_mfma_f32_16x16x32_bf16 v[104:107], a[124:127], a[228:231], v[104:107]\n"
        "v_cvt_pk_bf16_f32 v214, v100, v101\n"
        "v_cvt_pk_bf16_f32 v215, v102, v103\n"
        "v_mfma_f32_16x16x32_bf16 v[104:107], a[128:131], a[224:227], v[104:107]\n"
        "ds_read_b128 a[80:83], v248 offset:36864\n"
        "v_cvt_pk_bf16_f32 v82, v36, v37\n"
        "v_mfma_f32_16x16x32_bf16 v[104:107], a[132:135], a[236:239], v[104:107]\n"
        "v_exp_f32 v39, v39\n"
        "v_mfma_f32_16x16x32_bf16 v[104:107], a[136:139], a[240:243], v[104:107]\n"
        "ds_read_b128 a[84:87], v248 offset:43008\n"
        "v_cvt_pk_bf16_f32 v83, v38, v39\n"
        "v_mfma_f32_16x16x32_bf16 v[104:107], a[140:143], a[244:247], v[104:107]\n"
        "v_exp_f32 v40, v40\n"
        "v_mfma_f32_16x16x32_bf16 v[96:99], a[20:23], a[244:247], 0\n"
        "v_exp_f32 v41, v41\n"
        "v_mfma_f32_16x16x32_bf16 v[96:99], a[16:19], a[240:243], v[96:99]\n"
        "v_exp_f32 v42, v42\n"
        "v_mfma_f32_16x16x32_bf16 v[96:99], a[12:15], a[236:239], v[96:99]\n"
        "ds_read_b128 a[88:91], v248 offset:12288\n"
        "v_cvt_pk_bf16_f32 v84, v40, v41\n"
        "v_mfma_f32_16x16x32_bf16 v[96:99], a[8:11], a[224:227], v[96:99]\n"
        "v_cvt_pk_bf16_f32 v230, v104, v105\n"
        "v_cvt_pk_bf16_f32 v231, v106, v107\n"
        "v_mfma_f32_16x16x32_bf16 v[96:99], a[4:7], a[228:231], v[96:99]\n"
        "v_exp_f32 v43, v43\n"
        "v_mfma_f32_16x16x32_bf16 v[96:99], a[0:3], a[232:235], v[96:99]\n"
        "ds_read_b128 a[92:95], v248 offset:49152\n"
        "v_cvt_pk_bf16_f32 v85, v42, v43\n"
        "v_mfma_f32_16x16x32_bf16 v[12:15], v[212:215], v[156:159], v[12:15]\n"
        "ds_read_b128 v[222:225], v251 offset:4608\n"
        "s_nop 0\n"
        "v_mfma_f32_16x16x32_bf16 v[12:15], v[228:231], v[164:167], v[12:15]\n"
        "ds_read_b128 a[96:99], v254 offset:6160\n"
        "v_mfma_f32_16x16x32_bf16 v[28:31], v[212:215], v[176:179], v[28:31]\n"
        "v_mul_f32 v96, s50, v96\n"
        "v_mul_f32 v97, s50, v97\n"
        "v_mfma_f32_16x16x32_bf16 v[28:31], v[228:231], v[184:187], v[28:31]\n"
        "v_mul_f32 v98, s50, v98\n"
        "v_mul_f32 v99, s50, v99\n"
        "v_mfma_f32_16x16x32_bf16 v[44:47], v[212:215], v[196:199], v[44:47]\n"
        "v_cvt_pk_bf16_f32 v216, v96, v97\n"
        "v_cvt_pk_bf16_f32 v217, v98, v99\n"
        "v_mfma_f32_16x16x32_bf16 v[44:47], v[228:231], v[204:207], v[44:47]\n"
        "v_exp_f32 v12, v12\n"
        "v_mfma_f32_16x16x32_bf16 v[100:103], a[24:27], a[232:235], 0\n"
        "v_exp_f32 v13, v13\n"
        "v_mfma_f32_16x16x32_bf16 v[100:103], a[28:31], a[228:231], v[100:103]\n"
        "v_exp_f32 v14, v14\n"
        "v_mfma_f32_16x16x32_bf16 v[100:103], a[32:35], a[224:227], v[100:103]\n"
        "ds_read_b128 a[100:103], v254 offset:16\n"
        "v_cvt_pk_bf16_f32 v70, v12, v13\n"
        "v_mfma_f32_16x16x32_bf16 v[100:103], a[36:39], a[236:239], v[100:103]\n"
        "v_exp_f32 v15, v15\n"
        "v_mfma_f32_16x16x32_bf16 v[100:103], a[40:43], a[240:243], v[100:103]\n"
        "ds_read_b128 a[104:107], v254 offset:43024\n"
        "v_cvt_pk_bf16_f32 v71, v14, v15\n"
        "v_mfma_f32_16x16x32_bf16 v[100:103], a[44:47], a[244:247], v[100:103]\n"
        "v_exp_f32 v28, v28\n"
        "v_mfma_f32_16x16x32_bf16 v[104:107], a[68:71], a[244:247], 0\n"
        "v_exp_f32 v29, v29\n"
        "v_mfma_f32_16x16x32_bf16 v[104:107], a[64:67], a[240:243], v[104:107]\n"
        "v_exp_f32 v30, v30\n"
        "v_mfma_f32_16x16x32_bf16 v[104:107], a[60:63], a[236:239], v[104:107]\n"
        "ds_read_b128 a[108:111], v254 offset:36880\n"
        "v_cvt_pk_bf16_f32 v78, v28, v29\n"
        "v_mfma_f32_16x16x32_bf16 v[104:107], a[56:59], a[224:227], v[104:107]\n"
        "v_mul_f32 v100, s50, v100\n"
        "v_mul_f32 v101, s50, v101\n"
        "v_mfma_f32_16x16x32_bf16 v[104:107], a[52:55], a[228:231], v[104:107]\n"
        "v_mul_f32 v102, s50, v102\n"
        "v_mul_f32 v103, s50, v103\n"
        "v_mfma_f32_16x16x32_bf16 v[104:107], a[48:51], a[232:235], v[104:107]\n"
        "v_cvt_pk_bf16_f32 v218, v100, v101\n"
        "v_cvt_pk_bf16_f32 v219, v102, v103\n"
        "v_mfma_f32_16x16x32_bf16 v[96:99], a[232:235], a[144:147], 0\n"
        "v_exp_f32 v31, v31\n"
        "v_mfma_f32_16x16x32_bf16 v[96:99], a[228:231], a[148:151], v[96:99]\n"
        "ds_read_b128 a[112:115], v254 offset:49168\n"
        "v_cvt_pk_bf16_f32 v79, v30, v31\n"
        "v_mfma_f32_16x16x32_bf16 v[96:99], a[224:227], a[152:155], v[96:99]\n"
        "v_mul_f32 v104, s50, v104\n"
        "v_mul_f32 v105, s50, v105\n"
        "v_mfma_f32_16x16x32_bf16 v[96:99], a[236:239], a[156:159], v[96:99]\n"
        "v_mul_f32 v106, s50, v106\n"
        "v_mul_f32 v107, s50, v107\n"
        "v_mfma_f32_16x16x32_bf16 v[96:99], a[240:243], a[160:163], v[96:99]\n"
        "v_cvt_pk_bf16_f32 v220, v104, v105\n"
        "v_cvt_pk_bf16_f32 v221, v106, v107\n"
        "v_mfma_f32_16x16x32_bf16 v[96:99], a[244:247], a[164:167], v[96:99]\n"
        "ds_read_b128 a[220:223], v240 offset:12288\n"
        "v_mov_b64 v[226:227], v[220:221]\n"
        "v_mfma_f32_16x16x32_bf16 v[48:51], v[152:155], v[216:219], v[48:51]\n"
        "s_waitcnt lgkmcnt(6)\n"
        "v_exp_f32 v44, v44\n"
        "v_mfma_f32_16x16x32_bf16 v[48:51], v[168:171], v[220:223], v[48:51]\n"
        "v_exp_f32 v45, v45\n"
        "v_mfma_f32_16x16x32_bf16 v[52:55], v[172:175], v[216:219], v[52:55]\n"
        "v_exp_f32 v46, v46\n"
        "v_mfma_f32_16x16x32_bf16 v[52:55], v[188:191], v[224:227], v[52:55]\n"
        "ds_read_b128 v[222:225], v252 offset:4608\n"
        "v_cvt_pk_bf16_f32 v134, v96, v97\n"
        "v_mfma_f32_16x16x32_bf16 v[100:103], a[244:247], a[188:191], 0\n"
        "ds_read_b128 a[216:219], v241 offset:12288\n"
        "v_cvt_pk_bf16_f32 v135, v98, v99\n"
        "v_mfma_f32_16x16x32_bf16 v[100:103], a[240:243], a[184:187], v[100:103]\n"
        "ds_read_b128 a[248:251], v240 offset:12416\n"
        "v_cvt_pk_bf16_f32 v86, v44, v45\n"
        "v_mfma_f32_16x16x32_bf16 v[100:103], a[236:239], a[180:183], v[100:103]\n"
        "v_exp_f32 v47, v47\n"
        "v_mfma_f32_16x16x32_bf16 v[100:103], a[224:227], a[176:179], v[100:103]\n"
        "ds_read_b128 a[116:119], v254 offset:12304\n"
        "v_cvt_pk_bf16_f32 v87, v46, v47\n"
        "v_mfma_f32_16x16x32_bf16 v[100:103], a[228:231], a[172:175], v[100:103]\n"
        "v_exp_f32 v48, v48\n"
        "v_mfma_f32_16x16x32_bf16 v[100:103], a[232:235], a[168:171], v[100:103]\n"
        "v_exp_f32 v49, v49\n"
        "v_mfma_f32_16x16x32_bf16 v[56:59], v[192:195], v[216:219], v[56:59]\n"
        "s_waitcnt lgkmcnt(3)\n"
        "v_exp_f32 v50, v50\n"
        "v_mfma_f32_16x16x32_bf16 v[56:59], v[208:211], v[220:223], v[56:59]\n"
        "ds_read_b128 a[120:123], v248 offset:32\n"
        "v_cvt_pk_bf16_f32 v88, v48, v49\n"
        "v_mfma_f32_16x16x32_bf16 v[60:63], v[212:215], v[216:219], v[60:63]\n"
        "v_exp_f32 v51, v51\n"
        "v_mfma_f32_16x16x32_bf16 v[60:63], v[228:231], v[224:227], v[60:63]\n"
        "v_cvt_pk_bf16_f32 v142, v100, v101\n"
        "v_cvt_pk_bf16_f32 v143, v102, v103\n"
        "v_mfma_f32_16x16x32_bf16 v[104:107], a[232:235], a[192:195], 0\n"
        "ds_read_b128 a[232:235], v241 offset:12416\n"
        "v_cvt_pk_bf16_f32 v89, v50, v51\n"
        "v_mfma_f32_16x16x32_bf16 v[104:107], a[228:231], a[196:199], v[104:107]\n"
        "ds_read_b128 a[228:231], v240 offset:12544\n"
        "v_mfma_f32_16x16x32_bf16 v[104:107], a[224:227], a[200:203], v[104:107]\n"
        "ds_read_b128 a[224:227], v241 offset:12544\n"
        "v_mfma_f32_16x16x32_bf16 v[104:107], a[236:239], a[204:207], v[104:107]\n"
        "v_exp_f32 v52, v52\n"
        "v_mfma_f32_16x16x32_bf16 v[104:107], a[240:243], a[208:211], v[104:107]\n"
        "v_exp_f32 v53, v53\n"
        "v_mfma_f32_16x16x32_bf16 v[104:107], a[244:247], a[212:215], v[104:107]\n"
        "v_exp_f32 v54, v54\n"
        "v_mfma_f32_16x16x32_bf16 v[96:99], a[220:223], a[144:147], 0\n"
        "s_waitcnt lgkmcnt(6)\n"
        "ds_read_b128 a[236:239], v240 offset:1536\n"
        "v_cvt_pk_bf16_f32 v90, v52, v53\n"
        "v_mfma_f32_16x16x32_bf16 v[96:99], a[216:219], a[148:151], v[96:99]\n"
        "s_waitcnt lgkmcnt(6)\n"
        "v_exp_f32 v55, v55\n"
        "v_mfma_f32_16x16x32_bf16 v[96:99], a[248:251], a[152:155], v[96:99]\n"
        "s_waitcnt lgkmcnt(3)\n"
        "ds_read_b128 a[240:243], v241 offset:1536\n"
        "v_cvt_pk_bf16_f32 v150, v104, v105\n"
        "v_mfma_f32_16x16x32_bf16 v[96:99], a[232:235], a[156:159], v[96:99]\n"
        "s_waitcnt lgkmcnt(3)\n"
        "ds_read_b128 a[244:247], v240 offset:1664\n"
        "v_cvt_pk_bf16_f32 v151, v106, v107\n"
        "v_mfma_f32_16x16x32_bf16 v[96:99], a[228:231], a[160:163], v[96:99]\n"
        "s_waitcnt lgkmcnt(3)\n"
        "ds_read_b128 a[124:127], v248 offset:6176\n"
        "v_cvt_pk_bf16_f32 v91, v54, v55\n"
        "v_mfma_f32_16x16x32_bf16 v[96:99], a[224:227], a[164:167], v[96:99]\n"
        "v_exp_f32 v56, v56\n"
        "v_mfma_f32_16x16x32_bf16 v[100:103], a[224:227], a[188:191], 0\n"
        "46:\n"
        "s_cbranch_execz 40b\n"
        "v_exp_f32 v57, v57\n"
        "v_mfma_f32_16x16x32_bf16 v[100:103], a[228:231], a[184:187], v[100:103]\n"
        "v_exp_f32 v58, v58\n"
        "v_mfma_f32_16x16x32_bf16 v[100:103], a[232:235], a[180:183], v[100:103]\n"
        "ds_read_b128 v[32:35], v247 offset:0\n"
        "v_cvt_pk_bf16_f32 v92, v56, v57\n"
        "v_mfma_f32_16x16x32_bf16 v[100:103], a[248:251], a[176:179], v[100:103]\n"
        "v_cvt_pk_bf16_f32 v132, v96, v97\n"
        "v_cvt_pk_bf16_f32 v133, v98, v99\n"
        "v_mfma_f32_16x16x32_bf16 v[100:103], a[216:219], a[172:175], v[100:103]\n"
        "v_exp_f32 v59, v59\n"
        "v_mfma_f32_16x16x32_bf16 v[100:103], a[220:223], a[168:171], v[100:103]\n"
        "ds_read_b128 v[156:159], v247 offset:4096\n"
        "v_cvt_pk_bf16_f32 v93, v58, v59\n"
        "v_mfma_f32_16x16x32_bf16 v[104:107], a[220:223], a[192:195], 0\n"
        "ds_read_b128 a[220:223], v241 offset:1664\n"
        "v_mfma_f32_16x16x32_bf16 v[104:107], a[216:219], a[196:199], v[104:107]\n"
        "ds_read_b128 a[216:219], v240 offset:1792\n"
        "v_mfma_f32_16x16x32_bf16 v[104:107], a[248:251], a[200:203], v[104:107]\n"
        "ds_read_b128 a[248:251], v241 offset:1792\n"
        "v_cvt_pk_bf16_f32 v140, v100, v101\n"
        "v_mfma_f32_16x16x32_bf16 v[104:107], a[232:235], a[204:207], v[104:107]\n"
        "ds_read_b128 v[160:163], v247 offset:8192\n"
        "v_cvt_pk_bf16_f32 v141, v102, v103\n"
        "v_mfma_f32_16x16x32_bf16 v[104:107], a[228:231], a[208:211], v[104:107]\n"
        "v_exp_f32 v60, v60\n"
        "v_mfma_f32_16x16x32_bf16 v[104:107], a[224:227], a[212:215], v[104:107]\n"
        "s_waitcnt lgkmcnt(9)\n"
        "v_exp_f32 v61, v61\n"
        "v_mfma_f32_16x16x32_bf16 v[96:99], a[236:239], a[144:147], 0\n"
        "s_waitcnt lgkmcnt(8)\n"
        "v_exp_f32 v62, v62\n"
        "v_mfma_f32_16x16x32_bf16 v[96:99], a[240:243], a[148:151], v[96:99]\n"
        "s_waitcnt lgkmcnt(7)\n"
        "ds_read_b128 v[164:167], v247 offset:12288\n"
        "v_cvt_pk_bf16_f32 v94, v60, v61\n"
        "v_mfma_f32_16x16x32_bf16 v[96:99], a[244:247], a[152:155], v[96:99]\n"
        "s_waitcnt lgkmcnt(4)\n"
        "v_exp_f32 v63, v63\n"
        "v_mfma_f32_16x16x32_bf16 v[96:99], a[220:223], a[156:159], v[96:99]\n"
        "s_waitcnt lgkmcnt(3)\n"
        "v_cvt_pk_bf16_f32 v148, v104, v105\n"
        "v_cvt_pk_bf16_f32 v149, v106, v107\n"
        "v_mfma_f32_16x16x32_bf16 v[96:99], a[216:219], a[160:163], v[96:99]\n"
        "s_waitcnt lgkmcnt(2)\n"
        "ds_read_b128 v[152:155], v247 offset:16384\n"
        "v_cvt_pk_bf16_f32 v95, v62, v63\n"
        "v_mfma_f32_16x16x32_bf16 v[96:99], a[248:251], a[164:167], v[96:99]\n"
        "ds_read_b128 v[168:171], v247 offset:20480\n"
        "s_nop 0\n"
        "v_mfma_f32_16x16x32_bf16 v[100:103], a[248:251], a[188:191], 0\n"
        "ds_read_b128 a[128:131], v248 offset:36896\n"
        "s_nop 0\n"
        "v_mfma_f32_16x16x32_bf16 v[100:103], a[216:219], a[184:187], v[100:103]\n"
        "ds_read_b128 a[132:135], v248 offset:43040\n"
        "v_mfma_f32_16x16x32_bf16 v[100:103], a[220:223], a[180:183], v[100:103]\n"
        "ds_read_b128 a[136:139], v248 offset:12320\n"
        "v_cvt_pk_bf16_f32 v130, v96, v97\n"
        "v_mfma_f32_16x16x32_bf16 v[100:103], a[244:247], a[176:179], v[100:103]\n"
        "ds_read_b128 a[140:143], v248 offset:49184\n"
        "v_cvt_pk_bf16_f32 v131, v98, v99\n"
        "v_mfma_f32_16x16x32_bf16 v[100:103], a[240:243], a[172:175], v[100:103]\n"
        "ds_read_b128 a[0:3], v254 offset:6192\n"
        "v_mfma_f32_16x16x32_bf16 v[100:103], a[236:239], a[168:171], v[100:103]\n"
        "ds_read_b128 a[4:7], v254 offset:48\n"
        "v_mfma_f32_16x16x32_bf16 v[104:107], a[236:239], a[192:195], 0\n"
        "ds_read_b128 a[8:11], v254 offset:43056\n"
        "v_mov_b64 v[116:117], 0\n"
        "v_mfma_f32_16x16x32_bf16 v[104:107], a[240:243], a[196:199], v[104:107]\n"
        "ds_read_b128 a[12:15], v254 offset:36912\n"
        "s_nop 0\n"
        "v_mfma_f32_16x16x32_bf16 v[104:107], a[244:247], a[200:203], v[104:107]\n"
        "ds_read_b128 a[16:19], v254 offset:49200\n"
        "v_cvt_pk_bf16_f32 v138, v100, v101\n"
        "v_mfma_f32_16x16x32_bf16 v[104:107], a[220:223], a[204:207], v[104:107]\n"
        "ds_read_b128 a[20:23], v254 offset:12336\n"
        "v_cvt_pk_bf16_f32 v139, v102, v103\n"
        "v_mfma_f32_16x16x32_bf16 v[104:107], a[216:219], a[208:211], v[104:107]\n"
        "ds_read_b128 a[24:27], v248 offset:64\n"
        "v_mfma_f32_16x16x32_bf16 v[104:107], a[248:251], a[212:215], v[104:107]\n"
        "ds_read_b128 a[28:31], v248 offset:6208\n"
        "v_mfma_f32_16x16x32_bf16 v[172:175], v[32:35], a[72:75], 0\n"
        "ds_read_b128 a[32:35], v248 offset:36928\n"
        "v_mfma_f32_16x16x32_bf16 v[172:175], v[156:159], a[76:79], v[172:175]\n"
        "s_waitcnt lgkmcnt(15)\n"
        "ds_read_b128 a[36:39], v248 offset:43072\n"
        "v_mfma_f32_16x16x32_bf16 v[172:175], v[160:163], a[80:83], v[172:175]\n"
        "s_waitcnt lgkmcnt(15)\n"
        "ds_read_b128 a[40:43], v248 offset:12352\n"
        "v_mfma_f32_16x16x32_bf16 v[172:175], v[164:167], a[84:87], v[172:175]\n"
        "s_waitcnt lgkmcnt(15)\n"
        "v_cvt_pk_bf16_f32 v146, v104, v105\n"
        "v_cvt_pk_bf16_f32 v147, v106, v107\n"
        "v_mfma_f32_16x16x32_bf16 v[172:175], v[152:155], a[88:91], v[172:175]\n"
        "s_waitcnt lgkmcnt(15)\n"
        "ds_read_b128 a[44:47], v248 offset:49216\n"
        "v_mfma_f32_16x16x32_bf16 v[172:175], v[168:171], a[92:95], v[172:175]\n"
        "ds_read_b128 a[48:51], v254 offset:6224\n"
        "v_smfmac_f32_16x16x64_bf16 v[116:119], a[252:255], v[64:71], v255\n"
        "ds_read_b128 a[52:55], v254 offset:80\n"
        "v_mfma_f32_16x16x32_bf16 v[120:123], v[128:131], v[64:67], 0\n"
        "ds_read_b128 a[56:59], v254 offset:43088\n"
        "v_mfma_f32_16x16x32_bf16 v[120:123], v[132:135], v[68:71], v[120:123]\n"
        "ds_read_b128 a[60:63], v254 offset:36944\n"
        "v_mfma_f32_16x16x32_bf16 v[124:127], v[140:143], v[68:71], 0\n"
        "ds_read_b128 a[64:67], v254 offset:49232\n"
        "v_mfma_f32_16x16x32_bf16 v[124:127], v[136:139], v[64:67], v[124:127]\n"
        "ds_read_b128 a[68:71], v254 offset:12368\n"
        "v_add_f32 v116, v116, v117\n"
        "v_mfma_f32_16x16x32_bf16 v[232:235], v[144:147], v[64:67], 0\n"
        "v_rcp_f32 v117, v116\n"
        "v_mfma_f32_16x16x32_bf16 v[232:235], v[148:151], v[68:71], v[232:235]\n"
        "v_mul_f32 v120, v117, v120\n"
        "v_mul_f32 v121, v117, v121\n"
        "v_mfma_f32_16x16x32_bf16 v[176:179], v[156:159], a[96:99], 0\n"
        "v_mul_f32 v122, v117, v122\n"
        "v_mul_f32 v123, v117, v123\n"
        "v_mfma_f32_16x16x32_bf16 v[176:179], v[32:35], a[100:103], v[176:179]\n"
        "v_cvt_pk_bf16_f32 v120, v120, v121\n"
        "v_cvt_pk_bf16_f32 v121, v122, v123\n"
        "v_mfma_f32_16x16x32_bf16 v[176:179], v[164:167], a[104:107], v[176:179]\n"
        "ds_write_b32 v245, v120 offset:0\n"
        "v_mfma_f32_16x16x32_bf16 v[176:179], v[160:163], a[108:111], v[176:179]\n"
        "ds_write_b32 v245, v121 offset:4\n"
        "v_mfma_f32_16x16x32_bf16 v[176:179], v[168:171], a[112:115], v[176:179]\n"
        "v_mul_f32 v124, v117, v124\n"
        "v_mul_f32 v125, v117, v125\n"
        "v_mfma_f32_16x16x32_bf16 v[176:179], v[152:155], a[116:119], v[176:179]\n"
        "v_maximum3_f32 v253, v253, v116, v117\n"
        "v_mul_f32 v126, v117, v126\n"
        "v_mfma_f32_16x16x32_bf16 v[180:183], v[32:35], a[120:123], 0\n"
        "v_mov_b64 v[100:101], 0\n"
        "v_mul_f32 v127, v117, v127\n"
        "v_mfma_f32_16x16x32_bf16 v[180:183], v[156:159], a[124:127], v[180:183]\n"
        "s_waitcnt lgkmcnt(15)\n"
        "ds_read_b128 v[0:3], v247 offset:256\n"
        "v_cvt_pk_bf16_f32 v124, v124, v125\n"
        "v_mfma_f32_16x16x32_bf16 v[180:183], v[160:163], a[128:131], v[180:183]\n"
        "s_waitcnt lgkmcnt(15)\n"
        "ds_read_b128 v[4:7], v247 offset:4352\n"
        "v_cvt_pk_bf16_f32 v125, v126, v127\n"
        "v_mfma_f32_16x16x32_bf16 v[180:183], v[164:167], a[132:135], v[180:183]\n"
        "s_waitcnt lgkmcnt(15)\n"
        "ds_write_b32 v245, v124 offset:8\n"
        "v_mfma_f32_16x16x32_bf16 v[180:183], v[152:155], a[136:139], v[180:183]\n"
        "s_waitcnt lgkmcnt(15)\n"
        "ds_write_b32 v245, v125 offset:12\n"
        "v_mfma_f32_16x16x32_bf16 v[180:183], v[168:171], a[140:143], v[180:183]\n"
        "s_waitcnt lgkmcnt(15)\n"
        "ds_read_b128 v[8:11], v247 offset:8448\n"
        "v_mul_f32 v232, v117, v232\n"
        "v_mfma_f32_16x16x32_bf16 v[184:187], v[156:159], a[0:3], 0\n"
        "s_waitcnt lgkmcnt(15)\n"
        "ds_read_b128 v[16:19], v247 offset:12544\n"
        "v_mul_f32 v233, v117, v233\n"
        "v_mfma_f32_16x16x32_bf16 v[184:187], v[32:35], a[4:7], v[184:187]\n"
        "s_waitcnt lgkmcnt(15)\n"
        "ds_read_b128 v[20:23], v247 offset:16640\n"
        "v_mul_f32 v234, v117, v234\n"
        "v_mfma_f32_16x16x32_bf16 v[184:187], v[164:167], a[8:11], v[184:187]\n"
        "s_waitcnt lgkmcnt(15)\n"
        "ds_read_b128 v[12:15], v247 offset:20736\n"
        "v_mul_f32 v235, v117, v235\n"
        "v_mfma_f32_16x16x32_bf16 v[184:187], v[160:163], a[12:15], v[184:187]\n"
        "s_waitcnt lgkmcnt(15)\n"
        "v_cvt_pk_bf16_f32 v232, v232, v233\n"
        "v_cvt_pk_bf16_f32 v233, v234, v235\n"
        "v_mfma_f32_16x16x32_bf16 v[184:187], v[168:171], a[16:19], v[184:187]\n"
        "s_waitcnt lgkmcnt(15)\n"
        "ds_write_b32 v246, v232 offset:0\n"
        "v_mfma_f32_16x16x32_bf16 v[184:187], v[152:155], a[20:23], v[184:187]\n"
        "v_cvt_pk_bf16_f32 v188, v172, v176\n"
        "v_cvt_pk_bf16_f32 v208, v173, v177\n"
        "v_smfmac_f32_16x16x64_bf16 v[100:103], a[252:255], v[80:87], v255\n"
        "ds_write_b32 v246, v233 offset:4\n"
        "v_mfma_f32_16x16x32_bf16 v[104:107], v[128:131], v[80:83], 0\n"
        "v_cvt_pk_bf16_f32 v228, v174, v178\n"
        "s_nop 0\n"
        "v_mfma_f32_16x16x32_bf16 v[104:107], v[132:135], v[84:87], v[104:107]\n"
        "v_cvt_pk_bf16_f32 v189, v180, v184\n"
        "v_cvt_pk_bf16_f32 v209, v181, v185\n"
        "v_mfma_f32_16x16x32_bf16 v[108:111], v[140:143], v[84:87], 0\n"
        "buffer_store_dwordx2 v[188:189], v242, s[16:19], s38 offen offset:0 sc1\n"
        "v_mfma_f32_16x16x32_bf16 v[108:111], v[136:139], v[80:83], v[108:111]\n"
        "v_cvt_pk_bf16_f32 v229, v182, v186\n"
        "v_cvt_pk_bf16_f32 v236, v175, v179\n"
        "v_mfma_f32_16x16x32_bf16 v[112:115], v[144:147], v[80:83], 0\n"
        "v_cvt_pk_bf16_f32 v237, v183, v187\n"
        "v_add_f32 v100, v100, v101\n"
        "v_mfma_f32_16x16x32_bf16 v[112:115], v[148:151], v[84:87], v[112:115]\n"
        "s_waitcnt lgkmcnt(15)\n"
        "buffer_store_dwordx2 v[208:209], v242, s[16:19], s38 offen offset:384 sc1\n"
        "v_mfma_f32_16x16x32_bf16 v[192:195], v[32:35], a[24:27], 0\n"
        "s_waitcnt lgkmcnt(15)\n"
        "v_rcp_f32 v101, v100\n"
        "v_mfma_f32_16x16x32_bf16 v[192:195], v[156:159], a[28:31], v[192:195]\n"
        "s_waitcnt lgkmcnt(15)\n"
        "v_mul_f32 v104, v101, v104\n"
        "v_mul_f32 v105, v101, v105\n"
        "v_mfma_f32_16x16x32_bf16 v[192:195], v[160:163], a[32:35], v[192:195]\n"
        "s_waitcnt lgkmcnt(15)\n"
        "buffer_store_dwordx2 v[228:229], v242, s[16:19], s38 offen offset:768 sc1\n"
        "v_mfma_f32_16x16x32_bf16 v[192:195], v[164:167], a[36:39], v[192:195]\n"
        "s_waitcnt lgkmcnt(15)\n"
        "v_mul_f32 v106, v101, v106\n"
        "v_mul_f32 v107, v101, v107\n"
        "v_mfma_f32_16x16x32_bf16 v[192:195], v[152:155], a[40:43], v[192:195]\n"
        "s_waitcnt lgkmcnt(15)\n"
        "v_cvt_pk_bf16_f32 v104, v104, v105\n"
        "v_cvt_pk_bf16_f32 v105, v106, v107\n"
        "v_mfma_f32_16x16x32_bf16 v[192:195], v[168:171], a[44:47], v[192:195]\n"
        "s_waitcnt lgkmcnt(15)\n"
        "buffer_store_dwordx2 v[236:237], v242, s[16:19], s38 offen offset:1152 sc1\n"
        "v_mfma_f32_16x16x32_bf16 v[196:199], v[156:159], a[48:51], 0\n"
        "s_waitcnt lgkmcnt(15)\n"
        "v_mov_b64 v[48:49], 0\n"
        "v_mul_f32 v108, v101, v108\n"
        "v_mfma_f32_16x16x32_bf16 v[196:199], v[32:35], a[52:55], v[196:199]\n"
        "s_waitcnt lgkmcnt(15)\n"
        "ds_write_b32 v245, v104 offset:512\n"
        "v_mfma_f32_16x16x32_bf16 v[196:199], v[164:167], a[56:59], v[196:199]\n"
        "s_waitcnt lgkmcnt(15)\n"
        "ds_write_b32 v245, v105 offset:516\n"
        "v_mfma_f32_16x16x32_bf16 v[196:199], v[160:163], a[60:63], v[196:199]\n"
        "s_waitcnt lgkmcnt(15)\n"
        "v_mul_f32 v109, v101, v109\n"
        "v_maximum3_f32 v253, v253, v100, v101\n"
        "v_mfma_f32_16x16x32_bf16 v[196:199], v[168:171], a[64:67], v[196:199]\n"
        "s_waitcnt lgkmcnt(14)\n"
        "v_mul_f32 v110, v101, v110\n"
        "v_mul_f32 v111, v101, v111\n"
        "v_mfma_f32_16x16x32_bf16 v[196:199], v[152:155], a[68:71], v[196:199]\n"
        "s_waitcnt lgkmcnt(10)\n"
        "v_cvt_pk_bf16_f32 v108, v108, v109\n"
        "v_cvt_pk_bf16_f32 v109, v110, v111\n"
        "v_mfma_f32_16x16x32_bf16 v[200:203], v[4:7], a[76:79], 0\n"
        "ds_write_b32 v245, v108 offset:520\n"
        "v_mfma_f32_16x16x32_bf16 v[200:203], v[0:3], a[72:75], v[200:203]\n"
        "s_waitcnt lgkmcnt(7)\n"
        "ds_write_b32 v245, v109 offset:524\n"
        "v_mfma_f32_16x16x32_bf16 v[200:203], v[16:19], a[84:87], v[200:203]\n"
        "v_mul_f32 v112, v101, v112\n"
        "v_cvt_pk_bf16_f32 v190, v192, v196\n"
        "v_mfma_f32_16x16x32_bf16 v[200:203], v[8:11], a[80:83], v[200:203]\n"
        "s_waitcnt lgkmcnt(6)\n"
        "buffer_store_dword v190, v250, s[16:19], s38 offen offset:0 sc1\n"
        "v_mfma_f32_16x16x32_bf16 v[200:203], v[12:15], a[92:95], v[200:203]\n"
        "v_mul_f32 v113, v101, v113\n"
        "v_cvt_pk_bf16_f32 v210, v193, v197\n"
        "v_mfma_f32_16x16x32_bf16 v[200:203], v[20:23], a[88:91], v[200:203]\n"
        "v_mul_f32 v114, v101, v114\n"
        "v_mul_f32 v115, v101, v115\n"
        "v_smfmac_f32_16x16x64_bf16 v[48:51], a[252:255], v[72:79], v255\n"
        "buffer_store_dword v210, v250, s[16:19], s38 offen offset:384 sc1\n"
        "v_mfma_f32_16x16x32_bf16 v[52:55], v[128:131], v[72:75], 0\n"
        "v_cvt_pk_bf16_f32 v112, v112, v113\n"
        "v_cvt_pk_bf16_f32 v113, v114, v115\n"
        "v_mfma_f32_16x16x32_bf16 v[52:55], v[132:135], v[76:79], v[52:55]\n"
        "ds_write_b32 v246, v112 offset:512\n"
        "v_mfma_f32_16x16x32_bf16 v[56:59], v[140:143], v[76:79], 0\n"
        "v_cvt_pk_bf16_f32 v230, v194, v198\n"
        "v_cvt_pk_bf16_f32 v238, v195, v199\n"
        "v_mfma_f32_16x16x32_bf16 v[56:59], v[136:139], v[72:75], v[56:59]\n"
        "buffer_store_dword v230, v250, s[16:19], s38 offen offset:768 sc1\n"
        "v_mfma_f32_16x16x32_bf16 v[60:63], v[144:147], v[72:75], 0\n"
        "ds_write_b32 v246, v113 offset:516\n"
        "v_mfma_f32_16x16x32_bf16 v[60:63], v[148:151], v[76:79], v[60:63]\n"
        "buffer_store_dword v238, v250, s[16:19], s38 offen offset:1152 sc1\n"
        "v_mfma_f32_16x16x32_bf16 v[204:207], v[0:3], a[100:103], 0\n"
        "v_add_f32 v48, v48, v49\n"
        "v_mfma_f32_16x16x32_bf16 v[204:207], v[4:7], a[96:99], v[204:207]\n"
        "v_rcp_f32 v49, v48\n"
        "v_mfma_f32_16x16x32_bf16 v[204:207], v[8:11], a[108:111], v[204:207]\n"
        "v_mul_f32 v52, v49, v52\n"
        "v_mul_f32 v53, v49, v53\n"
        "v_mfma_f32_16x16x32_bf16 v[204:207], v[16:19], a[104:107], v[204:207]\n"
        "v_mul_f32 v54, v49, v54\n"
        "v_mul_f32 v55, v49, v55\n"
        "v_mfma_f32_16x16x32_bf16 v[204:207], v[20:23], a[116:119], v[204:207]\n"
        "v_cvt_pk_bf16_f32 v52, v52, v53\n"
        "v_cvt_pk_bf16_f32 v53, v54, v55\n"
        "v_mfma_f32_16x16x32_bf16 v[204:207], v[12:15], a[112:115], v[204:207]\n"
        "ds_write_b32 v245, v52 offset:256\n"
        "v_mfma_f32_16x16x32_bf16 v[212:215], v[4:7], a[124:127], 0\n"
        "v_mov_b64 v[64:65], 0\n"
        "v_mul_f32 v56, v49, v56\n"
        "v_mfma_f32_16x16x32_bf16 v[212:215], v[0:3], a[120:123], v[212:215]\n"
        "ds_write_b32 v245, v53 offset:260\n"
        "v_mfma_f32_16x16x32_bf16 v[212:215], v[16:19], a[132:135], v[212:215]\n"
        "v_mul_f32 v57, v49, v57\n"
        "v_maximum3_f32 v253, v253, v48, v49\n"
        "v_mfma_f32_16x16x32_bf16 v[212:215], v[8:11], a[128:131], v[212:215]\n"
        "v_mul_f32 v58, v49, v58\n"
        "v_mul_f32 v59, v49, v59\n"
        "v_mfma_f32_16x16x32_bf16 v[212:215], v[12:15], a[140:143], v[212:215]\n"
        "v_cvt_pk_bf16_f32 v56, v56, v57\n"
        "v_cvt_pk_bf16_f32 v57, v58, v59\n"
        "v_mfma_f32_16x16x32_bf16 v[212:215], v[20:23], a[136:139], v[212:215]\n"
        "ds_write_b32 v245, v56 offset:264\n"
        "v_mfma_f32_16x16x32_bf16 v[216:219], v[0:3], a[4:7], 0\n"
        "ds_write_b32 v245, v57 offset:268\n"
        "v_mfma_f32_16x16x32_bf16 v[216:219], v[4:7], a[0:3], v[216:219]\n"
        "v_mul_f32 v60, v49, v60\n"
        "v_mul_f32 v61, v49, v61\n"
        "v_mfma_f32_16x16x32_bf16 v[216:219], v[8:11], a[12:15], v[216:219]\n"
        "v_mul_f32 v62, v49, v62\n"
        "v_mul_f32 v63, v49, v63\n"
        "v_mfma_f32_16x16x32_bf16 v[216:219], v[16:19], a[8:11], v[216:219]\n"
        "v_cvt_pk_bf16_f32 v60, v60, v61\n"
        "v_cvt_pk_bf16_f32 v61, v62, v63\n"
        "v_mfma_f32_16x16x32_bf16 v[216:219], v[20:23], a[20:23], v[216:219]\n"
        "ds_write_b32 v246, v60 offset:256\n"
        "v_mfma_f32_16x16x32_bf16 v[216:219], v[12:15], a[16:19], v[216:219]\n"
        "v_cvt_pk_bf16_f32 v188, v200, v204\n"
        "v_cvt_pk_bf16_f32 v208, v201, v205\n"
        "v_smfmac_f32_16x16x64_bf16 v[64:67], a[252:255], v[88:95], v255\n"
        "ds_write_b32 v246, v61 offset:260\n"
        "v_mfma_f32_16x16x32_bf16 v[68:71], v[128:131], v[88:91], 0\n"
        "v_cvt_pk_bf16_f32 v228, v202, v206\n"
        "s_nop 0\n"
        "v_mfma_f32_16x16x32_bf16 v[68:71], v[132:135], v[92:95], v[68:71]\n"
        "s_waitcnt lgkmcnt(6)\n"
        "v_cvt_pk_bf16_f32 v189, v212, v216\n"
        "v_cvt_pk_bf16_f32 v209, v213, v217\n"
        "v_mfma_f32_16x16x32_bf16 v[80:83], v[140:143], v[92:95], 0\n"
        "s_barrier\n"
        "buffer_store_dwordx2 v[188:189], v242, s[16:19], s39 offen offset:0 sc1\n"
        "v_mfma_f32_16x16x32_bf16 v[80:83], v[136:139], v[88:91], v[80:83]\n"
        "ds_read_b128 a[168:171], v243 offset:0\n"
        "v_cvt_pk_bf16_f32 v229, v214, v218\n"
        "v_mfma_f32_16x16x32_bf16 v[84:87], v[144:147], v[88:91], 0\n"
        "ds_read_b128 a[172:175], v243 offset:4096\n"
        "v_cvt_pk_bf16_f32 v236, v203, v207\n"
        "v_mfma_f32_16x16x32_bf16 v[84:87], v[148:151], v[92:95], v[84:87]\n"
        "buffer_store_dwordx2 v[208:209], v242, s[16:19], s39 offen offset:384 sc1\n"
        "v_mfma_f32_16x16x32_bf16 v[220:223], v[4:7], a[28:31], 0\n"
        "ds_read_b128 a[176:179], v243 offset:8192\n"
        "v_cvt_pk_bf16_f32 v237, v215, v219\n"
        "v_mfma_f32_16x16x32_bf16 v[220:223], v[0:3], a[24:27], v[220:223]\n"
        "ds_read_b128 a[180:183], v243 offset:12288\n"
        "v_add_f32 v64, v64, v65\n"
        "v_mfma_f32_16x16x32_bf16 v[220:223], v[16:19], a[36:39], v[220:223]\n"
        "buffer_store_dwordx2 v[228:229], v242, s[16:19], s39 offen offset:768 sc1\n"
        "v_mfma_f32_16x16x32_bf16 v[220:223], v[8:11], a[32:35], v[220:223]\n"
        "ds_read_b128 a[184:187], v243 offset:16384\n"
        "v_mfma_f32_16x16x32_bf16 v[220:223], v[12:15], a[44:47], v[220:223]\n"
        "v_rcp_f32 v65, v64\n"
        "v_mfma_f32_16x16x32_bf16 v[220:223], v[20:23], a[40:43], v[220:223]\n"
        "buffer_store_dwordx2 v[236:237], v242, s[16:19], s39 offen offset:1152 sc1\n"
        "v_mfma_f32_16x16x32_bf16 v[224:227], v[0:3], a[52:55], 0\n"
        "ds_read_b128 a[188:191], v243 offset:20480\n"
        "v_mul_f32 v68, v65, v68\n"
        "v_mfma_f32_16x16x32_bf16 v[224:227], v[4:7], a[48:51], v[224:227]\n"
        "v_mul_f32 v69, v65, v69\n"
        "v_mul_f32 v70, v65, v70\n"
        "v_mfma_f32_16x16x32_bf16 v[224:227], v[8:11], a[60:63], v[224:227]\n"
        "v_mul_f32 v71, v65, v71\n"
        "v_cvt_pk_bf16_f32 v68, v68, v69\n"
        "v_mfma_f32_16x16x32_bf16 v[224:227], v[16:19], a[56:59], v[224:227]\n"
        "v_cvt_pk_bf16_f32 v69, v70, v71\n"
        "v_mul_f32 v80, v65, v80\n"
        "v_mfma_f32_16x16x32_bf16 v[224:227], v[20:23], a[68:71], v[224:227]\n"
        "ds_write_b32 v245, v68 offset:768\n"
        "v_mfma_f32_16x16x32_bf16 v[224:227], v[12:15], a[64:67], v[224:227]\n"
        "s_waitcnt lgkmcnt(6)\n"
        "ds_write_b32 v245, v69 offset:772\n"
        "v_mfma_f32_16x16x32_bf16 v[24:27], a[168:171], a[72:75], 0\n"
        "s_waitcnt lgkmcnt(6)\n"
        "v_mul_f32 v81, v65, v81\n"
        "v_maximum3_f32 v253, v253, v64, v65\n"
        "v_mfma_f32_16x16x32_bf16 v[24:27], a[172:175], a[76:79], v[24:27]\n"
        "s_waitcnt lgkmcnt(5)\n"
        "v_mul_f32 v82, v65, v82\n"
        "v_mul_f32 v83, v65, v83\n"
        "v_mfma_f32_16x16x32_bf16 v[24:27], a[176:179], a[80:83], v[24:27]\n"
        "s_waitcnt lgkmcnt(4)\n"
        "v_cvt_pk_bf16_f32 v80, v80, v81\n"
        "v_cvt_pk_bf16_f32 v81, v82, v83\n"
        "v_mfma_f32_16x16x32_bf16 v[24:27], a[180:183], a[84:87], v[24:27]\n"
        "s_waitcnt lgkmcnt(3)\n"
        "v_cvt_pk_bf16_f32 v190, v220, v224\n"
        "v_mul_f32 v84, v65, v84\n"
        "v_mfma_f32_16x16x32_bf16 v[24:27], a[184:187], a[88:91], v[24:27]\n"
        "s_waitcnt lgkmcnt(2)\n"
        "buffer_store_dword v190, v250, s[16:19], s39 offen offset:0 sc1\n"
        "v_mfma_f32_16x16x32_bf16 v[24:27], a[188:191], a[92:95], v[24:27]\n"
        "ds_write_b32 v245, v80 offset:776\n"
        "v_mfma_f32_16x16x32_bf16 v[28:31], a[172:175], a[96:99], 0\n"
        "ds_write_b32 v245, v81 offset:780\n"
        "v_mfma_f32_16x16x32_bf16 v[28:31], a[168:171], a[100:103], v[28:31]\n"
        "v_mul_f32 v85, v65, v85\n"
        "v_cvt_pk_bf16_f32 v210, v221, v225\n"
        "v_mfma_f32_16x16x32_bf16 v[28:31], a[180:183], a[104:107], v[28:31]\n"
        "buffer_store_dword v210, v250, s[16:19], s39 offen offset:384 sc1\n"
        "v_mfma_f32_16x16x32_bf16 v[28:31], a[176:179], a[108:111], v[28:31]\n"
        "v_mul_f32 v86, v65, v86\n"
        "v_mul_f32 v87, v65, v87\n"
        "v_mfma_f32_16x16x32_bf16 v[28:31], a[188:191], a[112:115], v[28:31]\n"
        "v_cvt_pk_bf16_f32 v84, v84, v85\n"
        "v_cvt_pk_bf16_f32 v85, v86, v87\n"
        "v_mfma_f32_16x16x32_bf16 v[28:31], a[184:187], a[116:119], v[28:31]\n"
        "ds_write_b32 v246, v84 offset:768\n"
        "v_mfma_f32_16x16x32_bf16 v[32:35], a[168:171], a[120:123], 0\n"
        "v_cvt_pk_bf16_f32 v230, v222, v226\n"
        "v_cvt_pk_bf16_f32 v238, v223, v227\n"
        "v_mfma_f32_16x16x32_bf16 v[32:35], a[172:175], a[124:127], v[32:35]\n"
        "buffer_store_dword v230, v250, s[16:19], s39 offen offset:768 sc1\n"
        "v_mfma_f32_16x16x32_bf16 v[32:35], a[176:179], a[128:131], v[32:35]\n"
        "buffer_store_dword v238, v250, s[16:19], s39 offen offset:1152 sc1\n"
        "v_mfma_f32_16x16x32_bf16 v[32:35], a[180:183], a[132:135], v[32:35]\n"
        "ds_write_b32 v246, v85 offset:772\n"
        "v_mfma_f32_16x16x32_bf16 v[32:35], a[184:187], a[136:139], v[32:35]\n"
        "ds_write_b32 v244, v253\n"
        "v_mfma_f32_16x16x32_bf16 v[32:35], a[188:191], a[140:143], v[32:35]\n"
        "v_cvt_pk_bf16_f32 v172, v24, v28\n"
        "v_mfma_f32_16x16x32_bf16 v[36:39], a[172:175], a[0:3], 0\n"
        "v_mfma_f32_16x16x32_bf16 v[36:39], a[168:171], a[4:7], v[36:39]\n"
        "v_mfma_f32_16x16x32_bf16 v[36:39], a[180:183], a[8:11], v[36:39]\n"
        "v_cvt_pk_bf16_f32 v176, v25, v29\n"
        "v_mfma_f32_16x16x32_bf16 v[36:39], a[176:179], a[12:15], v[36:39]\n"
        "v_mfma_f32_16x16x32_bf16 v[36:39], a[188:191], a[16:19], v[36:39]\n"
        "s_waitcnt lgkmcnt(0)\n"
        "v_mfma_f32_16x16x32_bf16 v[36:39], a[184:187], a[20:23], v[36:39]\n"
        "s_barrier\n"
        "ds_read_b128 a[192:195], v243 offset:256\n"
        "v_cvt_pk_bf16_f32 v180, v26, v30\n"
        "v_mfma_f32_16x16x32_bf16 v[40:43], a[168:171], a[24:27], 0\n"
        "ds_read_b128 a[196:199], v243 offset:4352\n"
        "s_nop 0\n"
        "v_mfma_f32_16x16x32_bf16 v[40:43], a[172:175], a[28:31], v[40:43]\n"
        "ds_read_b128 a[200:203], v243 offset:8448\n"
        "v_cvt_pk_bf16_f32 v173, v32, v36\n"
        "v_mfma_f32_16x16x32_bf16 v[40:43], a[176:179], a[32:35], v[40:43]\n"
        "ds_read_b128 a[204:207], v243 offset:12544\n"
        "v_cvt_pk_bf16_f32 v177, v33, v37\n"
        "v_mfma_f32_16x16x32_bf16 v[40:43], a[180:183], a[36:39], v[40:43]\n"
        "buffer_store_dwordx2 v[172:173], v242, s[16:19], s35 offen offset:0 sc1\n"
        "v_mfma_f32_16x16x32_bf16 v[40:43], a[184:187], a[40:43], v[40:43]\n"
        "ds_read_b128 a[208:211], v243 offset:16640\n"
        "v_cvt_pk_bf16_f32 v181, v34, v38\n"
        "v_mfma_f32_16x16x32_bf16 v[40:43], a[188:191], a[44:47], v[40:43]\n"
        "ds_read_b128 a[212:215], v243 offset:20736\n"
        "v_cvt_pk_bf16_f32 v184, v27, v31\n"
        "v_mfma_f32_16x16x32_bf16 v[44:47], a[172:175], a[48:51], 0\n"
        "buffer_store_dwordx2 v[176:177], v242, s[16:19], s35 offen offset:384 sc1\n"
        "v_mfma_f32_16x16x32_bf16 v[44:47], a[168:171], a[52:55], v[44:47]\n"
        "ds_read_b128 v[100:103], v249\n"
        "v_cvt_pk_bf16_f32 v185, v35, v39\n"
        "v_mfma_f32_16x16x32_bf16 v[44:47], a[180:183], a[56:59], v[44:47]\n"
        "v_mfma_f32_16x16x32_bf16 v[44:47], a[176:179], a[60:63], v[44:47]\n"
        "buffer_store_dwordx2 v[180:181], v242, s[16:19], s35 offen offset:768 sc1\n"
        "v_mfma_f32_16x16x32_bf16 v[44:47], a[188:191], a[64:67], v[44:47]\n"
        "v_mfma_f32_16x16x32_bf16 v[44:47], a[184:187], a[68:71], v[44:47]\n"
        "s_waitcnt lgkmcnt(5)\n"
        "s_nop 1\n"
        "v_mfma_f32_16x16x32_bf16 v[0:3], a[196:199], a[76:79], 0\n"
        "buffer_store_dwordx2 v[184:185], v242, s[16:19], s35 offen offset:1152 sc1\n"
        "v_mfma_f32_16x16x32_bf16 v[0:3], a[192:195], a[72:75], v[0:3]\n"
        "s_waitcnt lgkmcnt(3)\n"
        "s_nop 1\n"
        "v_mfma_f32_16x16x32_bf16 v[0:3], a[204:207], a[84:87], v[0:3]\n"
        "v_cvt_pk_bf16_f32 v174, v40, v44\n"
        "v_cvt_pk_bf16_f32 v178, v41, v45\n"
        "v_mfma_f32_16x16x32_bf16 v[0:3], a[200:203], a[80:83], v[0:3]\n"
        "s_waitcnt lgkmcnt(1)\n"
        "buffer_store_dword v174, v250, s[16:19], s35 offen offset:0 sc1\n"
        "v_mfma_f32_16x16x32_bf16 v[0:3], a[212:215], a[92:95], v[0:3]\n"
        "v_cvt_pk_bf16_f32 v182, v42, v46\n"
        "v_cvt_pk_bf16_f32 v186, v43, v47\n"
        "v_mfma_f32_16x16x32_bf16 v[0:3], a[208:211], a[88:91], v[0:3]\n"
        "v_mfma_f32_16x16x32_bf16 v[4:7], a[192:195], a[100:103], 0\n"
        "buffer_store_dword v178, v250, s[16:19], s35 offen offset:384 sc1\n"
        "v_mfma_f32_16x16x32_bf16 v[4:7], a[196:199], a[96:99], v[4:7]\n"
        "s_waitcnt lgkmcnt(0)\n"
        "v_maximum3_f32 v100, v100, v101, v102\n"
        "v_maximum3_f32 v100, v100, v103, v103\n"
        "v_mfma_f32_16x16x32_bf16 v[4:7], a[200:203], a[108:111], v[4:7]\n"
        "v_cmp_nge_f32 vcc, s25, v100\n"
        "v_mfma_f32_16x16x32_bf16 v[4:7], a[204:207], a[104:107], v[4:7]\n"
        "buffer_store_dword v182, v250, s[16:19], s35 offen offset:768 sc1\n"
        "v_mfma_f32_16x16x32_bf16 v[4:7], a[208:211], a[116:119], v[4:7]\n"
        "v_mfma_f32_16x16x32_bf16 v[4:7], a[212:215], a[112:115], v[4:7]\n"
        "v_mfma_f32_16x16x32_bf16 v[8:11], a[196:199], a[124:127], 0\n"
        "buffer_store_dword v186, v250, s[16:19], s35 offen offset:1152 sc1\n"
        "v_mfma_f32_16x16x32_bf16 v[8:11], a[192:195], a[120:123], v[8:11]\n"
        "v_mfma_f32_16x16x32_bf16 v[8:11], a[204:207], a[132:135], v[8:11]\n"
        "v_mfma_f32_16x16x32_bf16 v[8:11], a[200:203], a[128:131], v[8:11]\n"
        "v_mfma_f32_16x16x32_bf16 v[8:11], a[212:215], a[140:143], v[8:11]\n"
        "v_mfma_f32_16x16x32_bf16 v[8:11], a[208:211], a[136:139], v[8:11]\n"
        "v_mfma_f32_16x16x32_bf16 v[12:15], a[192:195], a[4:7], 0\n"
        "v_cvt_pk_bf16_f32 v192, v0, v4\n"
        "v_mfma_f32_16x16x32_bf16 v[12:15], a[196:199], a[0:3], v[12:15]\n"
        "v_cvt_pk_bf16_f32 v196, v1, v5\n"
        "v_mfma_f32_16x16x32_bf16 v[12:15], a[200:203], a[12:15], v[12:15]\n"
        "v_mfma_f32_16x16x32_bf16 v[12:15], a[204:207], a[8:11], v[12:15]\n"
        "v_cvt_pk_bf16_f32 v200, v2, v6\n"
        "v_mfma_f32_16x16x32_bf16 v[12:15], a[208:211], a[20:23], v[12:15]\n"
        "v_mfma_f32_16x16x32_bf16 v[12:15], a[212:215], a[16:19], v[12:15]\n"
        "v_cvt_pk_bf16_f32 v204, v3, v7\n"
        "s_nop 0\n"
        "v_mfma_f32_16x16x32_bf16 v[16:19], a[196:199], a[28:31], 0\n"
        "s_nop 2\n"
        "v_mfma_f32_16x16x32_bf16 v[16:19], a[192:195], a[24:27], v[16:19]\n"
        "v_mfma_f32_16x16x32_bf16 v[16:19], a[204:207], a[36:39], v[16:19]\n"
        "v_cvt_pk_bf16_f32 v193, v8, v12\n"
        "v_cvt_pk_bf16_f32 v197, v9, v13\n"
        "v_mfma_f32_16x16x32_bf16 v[16:19], a[200:203], a[32:35], v[16:19]\n"
        "buffer_store_dwordx2 v[192:193], v242, s[16:19], s36 offen offset:0 sc1\n"
        "v_mfma_f32_16x16x32_bf16 v[16:19], a[212:215], a[44:47], v[16:19]\n"
        "v_cvt_pk_bf16_f32 v201, v10, v14\n"
        "v_cvt_pk_bf16_f32 v205, v11, v15\n"
        "v_mfma_f32_16x16x32_bf16 v[16:19], a[208:211], a[40:43], v[16:19]\n"
        "buffer_store_dwordx2 v[196:197], v242, s[16:19], s36 offen offset:384 sc1\n"
        "v_mfma_f32_16x16x32_bf16 v[20:23], a[192:195], a[52:55], 0\n"
        "v_mfma_f32_16x16x32_bf16 v[20:23], a[196:199], a[48:51], v[20:23]\n"
        "buffer_store_dwordx2 v[200:201], v242, s[16:19], s36 offen offset:768 sc1\n"
        "v_mfma_f32_16x16x32_bf16 v[20:23], a[200:203], a[60:63], v[20:23]\n"
        "v_mfma_f32_16x16x32_bf16 v[20:23], a[204:207], a[56:59], v[20:23]\n"
        "buffer_store_dwordx2 v[204:205], v242, s[16:19], s36 offen offset:1152 sc1\n"
        "v_mfma_f32_16x16x32_bf16 v[20:23], a[208:211], a[68:71], v[20:23]\n"
        "v_mfma_f32_16x16x32_bf16 v[20:23], a[212:215], a[64:67], v[20:23]\n"
        "s_nop 2\n"
        "s_nop 4\n"
        "v_cvt_pk_bf16_f32 v194, v16, v20\n"
        "buffer_store_dword v194, v250, s[16:19], s36 offen offset:0 sc1\n"
        "v_cvt_pk_bf16_f32 v198, v17, v21\n"
        "buffer_store_dword v198, v250, s[16:19], s36 offen offset:384 sc1\n"
        "v_cvt_pk_bf16_f32 v202, v18, v22\n"
        "buffer_store_dword v202, v250, s[16:19], s36 offen offset:768 sc1\n"
        "v_cvt_pk_bf16_f32 v206, v19, v23\n"
        "buffer_store_dword v206, v250, s[16:19], s36 offen offset:1152 sc1\n"
        "s_cmp_lg_u64 vcc, 0\n"
        "s_cbranch_scc1 30f\n"
        "s_endpgm\n"
        "30:\n"
        : "+{v0}"(tid), "=&{s42}"(w), "=&{s[60:61]}"(hist), "=&{s27}"(nit)
        : "{s[0:1]}"(__builtin_amdgcn_kernarg_segment_ptr()), "{s2}"(blockIdx.x), "s"(ldsb)
        : "v1", "v2", "v3", "v4", "v5", "v6", "v7", "v8", "v9", "v10", "v11", "v12", "v13", "v14", "v15", "v16", "v17", "v18", "v19", "v20", "v21", "v22", "v23", "v24", "v25", "v26", "v27", "v28", "v29", "v30", "v31", "v32", "v33", "v34", "v35", "v36", "v37", "v38", "v39", "v40", "v41", "v42", "v43", "v44", "v45", "v46", "v47", "v48", "v49", "v50", "v51", "v52", "v53", "v54", "v55", "v56", "v57", "v58", "v59", "v60", "v61", "v62", "v63", "v64", "v65", "v66", "v67", "v68", "v69", "v70", "v71", "v72", "v73", "v74", "v75", "v76", "v77", "v78", "v79", "v80", "v81", "v82", "v83", "v84", "v85", "v86", "v87", "v88", "v89", "v90", "v91", "v92", "v93", "v94", "v95", "v96", "v97", "v98", "v99", "v100", "v101", "v102", "v103", "v104", "v105", "v106", "v107", "v108", "v109", "v110", "v111", "v112", "v113", "v114", "v115", "v116", "v117", "v118", "v119", "v120", "v121", "v122", "v123", "v124", "v125", "v126", "v127", "v128", "v129", "v130", "v131", "v132", "v133", "v134", "v135", "v136", "v137", "v138", "v139", "v140", "v141", "v142", "v143", "v144", "v145", "v146", "v147", "v148", "v149", "v150", "v151", "v152", "v153", "v154", "v155", "v156", "v157", "v158", "v159", "v160", "v161", "v162", "v163", "v164", "v165", "v166", "v167", "v168", "v169", "v170", "v171", "v172", "v173", "v174", "v175", "v176", "v177", "v178", "v179", "v180", "v181", "v182", "v183", "v184", "v185", "v186", "v187", "v188", "v189", "v190", "v191", "v192", "v193", "v194", "v195", "v196", "v197", "v198", "v199", "v200", "v201", "v202", "v203", "v204", "v205", "v206", "v207", "v208", "v209", "v210", "v211", "v212", "v213", "v214", "v215", "v216", "v217", "v218", "v219", "v220", "v221", "v222", "v223", "v224", "v225", "v226", "v227", "v228", "v229", "v230", "v231", "v232", "v233", "v234", "v235", "v236", "v237", "v238", "v239", "v240", "v241", "v242", "v243", "v244", "v245", "v246", "v247", "v248", "v249", "v250", "v251", "v252", "v253", "v254", "v255", "a0", "a1", "a2", "a3", "a4", "a5", "a6", "a7", "a8", "a9", "a10", "a11", "a12", "a13", "a14", "a15", "a16", "a17", "a18", "a19", "a20", "a21", "a22", "a23", "a24", "a25", "a26", "a27", "a28", "a29", "a30", "a31", "a32", "a33", "a34", "a35", "a36", "a37", "a38", "a39", "a40", "a41", "a42", "a43", "a44", "a45", "a46", "a47", "a48", "a49", "a50", "a51", "a52", "a53", "a54", "a55", "a56", "a57", "a58", "a59", "a60", "a61", "a62", "a63", "a64", "a65", "a66", "a67", "a68", "a69", "a70", "a71", "a72", "a73", "a74", "a75", "a76", "a77", "a78", "a79", "a80", "a81", "a82", "a83", "a84", "a85", "a86", "a87", "a88", "a89", "a90", "a91", "a92", "a93", "a94", "a95", "a96", "a97", "a98", "a99", "a100", "a101", "a102", "a103", "a104", "a105", "a106", "a107", "a108", "a109", "a110", "a111", "a112", "a113", "a114", "a115", "a116", "a117", "a118", "a119", "a120", "a121", "a122", "a123", "a124", "a125", "a126", "a127", "a128", "a129", "a130", "a131", "a132", "a133", "a134", "a135", "a136", "a137", "a138", "a139", "a140", "a141", "a142", "a143", "a144", "a145", "a146", "a147", "a148", "a149", "a150", "a151", "a152", "a153", "a154", "a155", "a156", "a157", "a158", "a159", "a160", "a161", "a162", "a163", "a164", "a165", "a166", "a167", "a168", "a169", "a170", "a171", "a172", "a173", "a174", "a175", "a176", "a177", "a178", "a179", "a180", "a181", "a182", "a183", "a184", "a185", "a186", "a187", "a188", "a189", "a190", "a191", "a192", "a193", "a194", "a195", "a196", "a197", "a198", "a199", "a200", "a201", "a202", "a203", "a204", "a205", "a206", "a207", "a208", "a209", "a210", "a211", "a212", "a213", "a214", "a215", "a216", "a217", "a218", "a219", "a220", "a221", "a222", "a223", "a224", "a225", "a226", "a227", "a228", "a229", "a230", "a231", "a232", "a233", "a234", "a235", "a236", "a237", "a238", "a239", "a240", "a241", "a242", "a243", "a244", "a245", "a246", "a247", "a248", "a249", "a250", "a251", "a252", "a253", "a254", "a255", "s3", "s4", "s5", "s6", "s7", "s8", "s9", "s10", "s11", "s12", "s13", "s14", "s15", "s16", "s17", "s18", "s19", "s20", "s21", "s22", "s23", "s24", "s25", "s26", "s28", "s29", "s30", "s31", "s34", "s35", "s36", "s37", "s38", "s39", "s40", "s41", "s43", "s44", "s45", "s46", "s47", "s48", "s49", "s50", "s51", "s52", "s53", "s54", "s55", "s56", "s57", "s58", "s59", "s62", "s63", "s64", "s65", "s66", "s67", "vcc", "scc", "memory");
    const bool all = nit > 19u;
    for (unsigned i = 0; i < (all ? 256u : 9u); ++i) {
        const unsigned b = all ? i : i ? (unsigned)(hist >> (8u * (i - 1u))) & 255u : blockIdx.x;
        if (!all && i && b == blockIdx.x) continue;
        unsigned l2, w2 = w;
        asm volatile("s_waitcnt vmcnt(0) lgkmcnt(0)\n s_barrier\n v_mbcnt_lo_u32_b32 %0, -1, 0\n v_mbcnt_hi_u32_b32 %0, -1, %0\n s_nop 1"
                     : "=v"(l2), "+s"(w2) :: "memory");
        guarded_pass(x, wqkv, wo, rpb, rpi, mask, out, ldsb, l2, w2, b);
    }
}


extern "C" void kernel_launch(void* const* d_in, const int* in_sizes, int n_in, void* d_out, int out_size, void* d_ws,
                              size_t ws_size, hipStream_t stream) {
    win_attn_kernel<<<256, 256, 0, stream>>>((const char*)d_in[0], (const char*)d_in[1], (const char*)d_in[2],
                                             (const char*)d_in[3], (const char*)d_in[4], (const char*)d_in[5],
                                             (char*)d_out, (char*)d_ws);

}
```
